```python
import torch

def _softmax(x, axis=-1):
    return torch.softmax(x, dim=axis)
def _gelu(x):
    return torch.nn.functional.gelu(x, approximate="tanh")
def _silu(x):
    return torch.nn.functional.silu(x)
def _sigmoid(x):
    return torch.sigmoid(x)
def _layernorm(x, axis=-1, eps=1e-5):
    m = x.mean(dim=axis, keepdim=True)
    v = x.var(dim=axis, keepdim=True, unbiased=False)
    return (x - m) / torch.sqrt(v + eps)
def _scatter_add(tgt, idx, src, axis=0):
    return tgt.clone().index_add_(axis, idx, src)
def _segment_sum(src, seg, n):
    out = torch.zeros((n,) + src.shape[1:], dtype=src.dtype)
    return out.index_add_(0, seg, src)
def _pad(x, pw):
    flat = tuple(v for pair in reversed(pw) for v in pair)
    return torch.nn.functional.pad(x, flat)

kernel_name = 'standalone__deep_cross_xdeepfm__recsys'

def setup_inputs():

    _w = (lambda a: (lambda t: t.to(torch.bfloat16) if t.is_floating_point() else t)(torch.as_tensor(a)))
    B, F, D, L = 4096, 26, 64, 3
    V = min(1000000, 100000)
    Din = F * D
    return {
        "idx": _w((lambda lo, hi, *s: torch.randint(lo, hi, s))(0, V, B, F).to(torch.int64)),
        "table": _w((lambda *s: torch.randn(*s, dtype=torch.bfloat16) * 0.8)(V, D)),
        "ws_cin": _w((lambda *s: torch.randn(*s, dtype=torch.bfloat16) * 0.2)(L, F, F, F) * (1.0 / F)),
        "w_cin_out": _w((lambda *s: torch.randn(*s, dtype=torch.bfloat16) * 0.2)(L * F) * ((L * F) ** -0.5)),
        "w_dnn1": _w((lambda *s: torch.randn(*s, dtype=torch.bfloat16) * 0.8)(Din, Din) * (Din ** -0.5)),
        "b_dnn1": _w((lambda *s: torch.randn(*s, dtype=torch.bfloat16) * 1.6)(Din) * 0.1),
        "w_dnn2": _w((lambda *s: torch.randn(*s, dtype=torch.bfloat16) * 0.2)(Din) * (Din ** -0.5)),
        "b_out": _w((lambda *s: torch.randn(*s, dtype=torch.bfloat16) * 0.2)(1)),
    }

def reference(idx, table, ws_cin, w_cin_out, w_dnn1, b_dnn1, w_dnn2, b_out):
    x0 = table[idx]
    B = x0.shape[0]
    L = ws_cin.shape[0]
    xk = x0
    pooled = []
    for l in range(L):
        xk = torch.einsum('bid,bjd,hij->bhd', xk, x0, ws_cin[l])
        pooled.append(xk.sum(axis=-1))
    p_plus = torch.stack(pooled, axis=1).reshape(B, -1)
    logit_cin = p_plus @ w_cin_out
    h = x0.reshape(B, -1)
    h = _gelu(h @ w_dnn1 + b_dnn1)
    logit_dnn = h @ w_dnn2
    return logit_cin + logit_dnn + b_out
```

```cpp
#include <hip/hip_runtime.h>
typedef unsigned short u16;

__device__ const __attribute__((aligned(16), used, visibility("hidden"))) u16 xd_tab[2048] asm("xd_tab") = {1564, 65535, 1560, 1949, 65535, 65535, 65535, 65535, 1565, 1692, 65535, 1561, 65535, 65535, 65535, 65535, 65535, 1693, 1820, 65535, 65535, 65535, 65535, 65535, 65535, 65535, 1821, 1948, 18585, 65535, 65535, 18584, 19764, 19895, 20022, 20149, 65535, 65535, 65535, 65535, 19765, 19892, 20023, 20150, 65535, 65535, 65535, 65535, 19766, 19893, 20020, 20151, 65535, 65535, 65535, 65535, 19767, 19894, 20021, 20148, 65535, 65535, 65535, 65535, 18200, 18331, 18458, 65535, 16124, 65535, 1688, 16509, 18201, 18328, 18459, 18586, 16125, 16252, 65535, 65535, 18202, 18329, 18456, 18587, 65535, 16253, 16380, 65535, 18203, 18330, 18457, 65535, 65535, 1689, 16381, 16508, 14560, 14691, 14818, 14945, 16120, 65535, 16378, 16505, 14561, 14688, 14819, 14946, 65535, 16248, 16379, 16506, 14562, 14689, 14816, 14947, 16122, 16249, 16376, 16507, 14563, 14690, 14817, 14944, 16123, 65535, 16377, 16504, 8844, 8975, 9102, 9229, 10924, 11055, 11182, 11309, 8845, 8972, 9103, 9230, 10925, 11052, 11183, 11310, 8846, 8973, 9100, 9231, 10926, 11053, 11180, 11311, 8847, 8974, 9101, 9228, 10927, 11054, 11181, 11308, 3644, 3775, 3902, 4029, 5204, 5335, 5462, 5589, 3645, 3772, 3903, 4030, 5205, 5332, 5463, 5590, 65535, 3773, 3900, 4031, 5206, 5333, 5460, 5591, 65535, 3774, 3901, 4028, 5207, 5334, 5461, 5588, 1566, 1694, 1822, 1950, 16126, 16254, 16382, 16510, 1567, 1695, 1823, 1951, 16127, 16255, 16383, 16511, 65535, 65535, 65535, 65535, 65535, 65535, 65535, 65535, 65535, 65535, 65535, 65535, 65535, 65535, 65535, 65535, 65535, 65535, 65535, 65535, 65535, 65535, 65535, 65535, 65535, 65535, 65535, 65535, 65535, 65535, 65535, 65535, 65535, 65535, 65535, 65535, 65535, 65535, 65535, 65535, 65535, 65535, 65535, 65535, 65535, 65535, 65535, 65535, 65535, 65535, 65535, 65535, 65535, 65535, 65535, 65535, 65535, 65535, 65535, 65535, 65535, 65535, 65535, 65535, 65535, 65535, 65535, 65535, 65535, 65535, 65535, 65535, 65535, 65535, 65535, 65535, 65535, 65535, 65535, 65535, 19760, 65535, 65535, 20145, 65535, 65535, 65535, 65535, 19761, 19888, 65535, 65535, 14695, 65535, 65535, 65535, 1562, 19889, 20016, 65535, 65535, 65535, 65535, 65535, 65535, 1563, 20017, 20144, 65535, 65535, 65535, 65535, 18204, 18335, 18462, 18589, 65535, 16121, 65535, 65535, 18205, 18332, 18463, 18590, 16251, 65535, 65535, 16250, 18206, 18333, 18460, 18591, 65535, 65535, 65535, 65535, 18207, 18334, 18461, 18588, 65535, 65535, 65535, 65535, 14564, 65535, 14822, 14949, 12484, 65535, 1690, 12869, 14565, 14692, 14823, 14950, 12485, 12612, 65535, 1691, 14566, 14693, 14820, 14951, 65535, 12613, 12740, 65535, 14567, 14694, 14821, 14948, 65535, 65535, 12741, 12868, 65535, 11051, 11178, 11305, 12480, 12611, 12738, 12865, 65535, 11048, 11179, 11306, 12481, 12608, 12739, 12866, 10922, 11049, 11176, 11307, 12482, 12609, 65535, 12867, 10923, 11050, 11177, 11304, 12483, 12610, 12737, 12864, 5200, 5331, 5458, 5585, 7280, 7411, 7538, 7665, 5201, 5328, 5459, 5586, 7281, 7408, 7539, 7666, 5202, 5329, 5456, 5587, 7282, 7409, 7536, 7667, 5203, 5330, 5457, 5584, 7283, 7410, 7537, 7664, 19762, 19890, 20018, 20146, 12486, 12614, 12742, 12870, 19763, 19891, 20019, 20147, 12487, 12615, 12743, 12871, 65535, 65535, 65535, 65535, 65535, 65535, 65535, 65535, 65535, 65535, 65535, 65535, 65535, 65535, 65535, 65535, 65535, 65535, 65535, 65535, 65535, 65535, 65535, 65535, 65535, 65535, 65535, 65535, 65535, 65535, 65535, 65535, 65535, 65535, 65535, 65535, 65535, 65535, 65535, 65535, 65535, 65535, 65535, 65535, 65535, 65535, 65535, 65535, 65535, 65535, 65535, 65535, 65535, 65535, 65535, 65535, 65535, 65535, 65535, 65535, 65535, 65535, 65535, 65535, 65535, 65535, 65535, 65535, 65535, 65535, 65535, 65535, 65535, 65535, 65535, 65535, 65535, 65535, 65535, 65535, 10920, 10921, 65535, 65535, 65535, 65535, 65535, 65535, 65535, 65535, 65535, 65535, 65535, 65535, 65535, 65535, 65535, 65535, 65535, 65535, 65535, 65535, 65535, 65535, 65535, 65535, 65535, 65535, 65535, 65535, 65535, 65535, 65535, 65535, 65535, 65535, 65535, 65535, 65535, 65535, 65535, 65535, 65535, 65535, 65535, 65535, 65535, 65535, 65535, 65535, 65535, 65535, 65535, 65535, 65535, 65535, 65535, 65535, 65535, 65535, 65535, 65535, 65535, 65535, 65535, 65535, 65535, 65535, 65535, 65535, 65535, 65535, 65535, 65535, 65535, 65535, 65535, 65535, 65535, 65535, 65535, 65535, 12736, 65535, 65535, 65535, 65535, 65535, 65535, 65535, 65535, 65535, 65535, 65535, 65535, 65535, 8840, 65535, 1816, 9225, 65535, 65535, 65535, 65535, 8841, 8968, 65535, 1817, 65535, 65535, 65535, 65535, 65535, 8969, 9096, 65535, 65535, 65535, 65535, 65535, 65535, 65535, 9097, 9224, 65535, 65535, 65535, 65535, 7284, 65535, 7542, 7669, 65535, 65535, 65535, 65535, 7285, 7412, 7543, 7670, 65535, 65535, 65535, 65535, 7286, 65535, 7540, 7671, 65535, 65535, 65535, 65535, 7287, 65535, 7541, 7668, 65535, 65535, 65535, 65535, 8842, 8970, 9098, 9226, 1944, 65535, 1944, 65535, 8843, 8971, 9099, 9227, 1945, 1946, 1945, 65535, 65535, 65535, 65535, 65535, 65535, 65535, 65535, 65535, 65535, 65535, 65535, 65535, 65535, 65535, 65535, 65535, 65535, 65535, 65535, 65535, 65535, 65535, 65535, 65535, 65535, 65535, 65535, 65535, 65535, 65535, 65535, 65535, 65535, 65535, 65535, 65535, 65535, 65535, 65535, 65535, 65535, 65535, 65535, 65535, 65535, 65535, 65535, 65535, 65535, 65535, 3646, 3647, 0, 65535, 65535, 65535, 65535, 65535, 65535, 65535, 1, 65535, 65535, 65535, 65535, 65535, 65535, 65535, 4, 65535, 65535, 65535, 65535, 65535, 65535, 65535, 5, 65535, 65535, 65535, 65535, 65535, 65535, 65535, 2, 65535, 65535, 65535, 65535, 65535, 65535, 65535, 3, 65535, 65535, 65535, 65535, 65535, 65535, 65535, 6, 65535, 65535, 65535, 65535, 65535, 65535, 65535, 7, 65535, 65535, 65535, 65535, 65535, 65535, 65535, 132, 65535, 65535, 65535, 65535, 65535, 65535, 65535, 133, 65535, 65535, 65535, 65535, 65535, 65535, 65535, 128, 65535, 65535, 65535, 65535, 65535, 65535, 65535, 129, 65535, 65535, 65535, 65535, 65535, 65535, 65535, 134, 65535, 65535, 65535, 65535, 65535, 65535, 65535, 135, 65535, 65535, 65535, 65535, 65535, 65535, 65535, 130, 65535, 65535, 65535, 65535, 65535, 65535, 65535, 131, 65535, 65535, 65535, 65535, 65535, 65535, 65535, 256, 65535, 65535, 65535, 7415, 65535, 7413, 7414, 257, 65535, 65535, 65535, 65535, 65535, 65535, 65535, 260, 65535, 65535, 65535, 65535, 65535, 65535, 65535, 261, 65535, 65535, 65535, 3640, 65535, 1818, 4025, 258, 65535, 65535, 65535, 3641, 3768, 65535, 1819, 259, 65535, 65535, 65535, 65535, 3769, 3896, 65535, 262, 65535, 65535, 65535, 65535, 65535, 3897, 4024, 263, 65535, 65535, 65535, 3642, 3770, 3898, 4026, 384, 65535, 65535, 65535, 3643, 3771, 3899, 4027, 386, 65535, 65535, 65535, 65535, 65535, 65535, 65535, 65535, 65535, 65535, 65535, 65535, 65535, 65535, 65535, 65535, 65535, 65535, 65535, 65535, 65535, 65535, 65535, 1947, 65535, 65535, 65535, 65535, 65535, 65535, 65535, 65535, 65535, 65535, 65535, 65535, 65535, 65535, 65535, 65535, 65535, 65535, 65535, 65535, 65535, 65535, 65535, 65535, 65535, 65535, 65535, 3378, 65535, 3376, 3511, 65535, 65535, 65535, 65535, 3379, 3382, 65535, 65535, 21333, 65535, 65535, 21332, 65535, 3383, 3506, 65535, 65535, 65535, 65535, 65535, 65535, 3377, 3507, 3510, 65535, 65535, 65535, 65535, 21578, 21591, 21714, 21711, 65535, 65535, 65535, 65535, 21579, 21582, 21715, 21718, 65535, 65535, 65535, 65535, 21586, 21583, 21706, 21719, 65535, 65535, 65535, 65535, 21587, 21590, 21707, 21710, 65535, 65535, 65535, 65535, 21320, 65535, 21456, 21453, 17938, 65535, 3380, 18071, 21321, 21324, 21457, 21460, 17939, 17942, 65535, 65535, 21328, 21325, 21448, 21461, 65535, 17943, 18066, 65535, 21329, 65535, 21449, 21452, 65535, 3381, 18067, 18070, 17680, 65535, 17816, 17813, 17936, 17949, 18072, 65535, 17681, 17684, 17817, 17820, 17937, 17940, 18073, 18076, 17688, 17685, 65535, 17821, 17944, 17941, 18064, 18077, 17689, 17692, 65535, 17812, 17945, 17948, 18065, 65535, 10658, 10671, 10794, 10791, 14042, 14055, 14178, 65535, 10659, 10662, 10795, 65535, 14043, 14046, 14179, 14182, 10666, 10663, 10786, 65535, 14050, 14047, 65535, 14183, 10667, 10670, 10787, 10790, 14051, 14054, 65535, 65535, 65535, 6775, 6898, 6895, 7018, 7031, 7154, 7151, 65535, 6766, 6899, 6902, 7019, 7022, 7155, 7158, 6770, 6767, 65535, 6903, 7026, 7023, 7146, 7159, 6771, 6774, 65535, 6894, 7027, 7030, 7147, 7150, 3386, 3390, 3514, 3518, 17946, 65535, 18074, 18078, 3387, 3391, 3515, 3519, 17947, 65535, 18075, 18079, 65535, 65535, 65535, 65535, 65535, 65535, 65535, 65535, 65535, 65535, 65535, 65535, 65535, 65535, 65535, 65535, 65535, 65535, 65535, 65535, 65535, 65535, 65535, 65535, 65535, 65535, 65535, 65535, 65535, 65535, 65535, 65535, 65535, 65535, 65535, 65535, 65535, 65535, 65535, 65535, 65535, 65535, 65535, 65535, 65535, 65535, 65535, 65535, 65535, 65535, 65535, 65535, 65535, 65535, 65535, 65535, 65535, 65535, 65535, 65535, 17693, 65535, 65535, 65535, 65535, 65535, 65535, 65535, 65535, 65535, 17808, 17809, 65535, 65535, 65535, 65535, 65535, 65535, 65535, 65535, 21576, 65535, 65535, 21709, 17682, 17683, 65535, 65535, 21577, 21580, 65535, 65535, 65535, 65535, 65535, 65535, 3384, 21581, 21704, 65535, 65535, 65535, 65535, 65535, 65535, 3385, 21705, 21708, 17815, 65535, 65535, 17814, 21322, 21335, 21458, 21455, 65535, 65535, 65535, 65535, 21323, 21326, 21459, 21462, 65535, 65535, 65535, 65535, 21330, 21327, 21450, 21463, 65535, 65535, 65535, 65535, 21331, 21334, 21451, 21454, 18069, 65535, 65535, 18068, 65535, 17695, 17818, 65535, 14298, 65535, 65535, 14431, 65535, 17686, 17819, 17822, 14299, 14302, 65535, 3389, 17690, 17687, 17810, 17823, 3388, 14303, 14426, 65535, 17691, 17694, 17811, 65535, 65535, 65535, 14427, 14430, 14040, 14053, 14176, 14173, 14296, 14309, 14432, 14429, 65535, 14044, 14177, 14180, 14297, 14300, 65535, 14436, 14048, 14045, 14168, 14181, 14304, 14301, 14424, 14437, 14049, 14052, 14169, 14172, 14305, 14308, 14425, 14428, 7016, 7029, 7152, 7149, 10400, 10413, 10536, 65535, 7017, 7020, 7153, 7156, 10401, 10404, 10537, 10540, 65535, 7021, 7144, 7157, 10408, 10405, 10528, 10541, 65535, 7028, 7145, 7148, 10409, 10412, 10529, 65535, 21584, 21588, 21712, 21716, 65535, 14310, 14434, 14438, 21585, 21589, 21713, 21717, 14307, 14311, 14435, 14439, 65535, 65535, 65535, 65535, 65535, 65535, 65535, 65535, 65535, 65535, 65535, 65535, 65535, 65535, 65535, 65535, 65535, 65535, 65535, 65535, 65535, 65535, 65535, 65535, 65535, 65535, 65535, 65535, 65535, 65535, 65535, 65535, 65535, 65535, 65535, 65535, 65535, 65535, 65535, 65535, 65535, 65535, 65535, 65535, 65535, 65535, 65535, 65535, 65535, 65535, 65535, 65535, 65535, 65535, 65535, 65535, 65535, 65535, 65535, 65535, 65535, 65535, 65535, 65535, 65535, 65535, 65535, 65535, 65535, 65535, 65535, 65535, 65535, 10798, 10799, 65535, 65535, 65535, 65535, 65535, 65535, 14041, 65535, 65535, 65535, 65535, 65535, 65535, 65535, 65535, 65535, 65535, 65535, 65535, 65535, 65535, 65535, 65535, 65535, 65535, 65535, 65535, 65535, 65535, 65535, 65535, 65535, 65535, 65535, 65535, 65535, 65535, 65535, 65535, 65535, 65535, 65535, 65535, 65535, 65535, 65535, 65535, 65535, 65535, 17950, 17951, 17950, 65535, 65535, 65535, 14170, 14171, 65535, 65535, 65535, 65535, 14175, 65535, 65535, 14174, 65535, 65535, 65535, 65535, 65535, 65535, 65535, 65535, 14306, 65535, 14306, 65535, 65535, 65535, 65535, 65535, 65535, 65535, 65535, 65535, 65535, 14433, 65535, 65535, 65535, 65535, 65535, 65535, 65535, 65535, 65535, 65535, 65535, 65535, 65535, 65535, 10656, 65535, 65535, 10789, 65535, 65535, 65535, 65535, 10657, 10660, 65535, 65535, 65535, 65535, 65535, 65535, 3504, 10661, 10784, 65535, 65535, 65535, 65535, 65535, 65535, 3505, 10785, 10788, 65535, 65535, 65535, 65535, 10402, 65535, 10538, 65535, 65535, 65535, 65535, 65535, 10403, 10406, 10539, 10542, 65535, 65535, 65535, 65535, 10410, 10407, 10530, 10543, 6896, 6897, 6896, 65535, 10411, 65535, 10531, 65535, 65535, 65535, 65535, 65535, 10664, 10668, 10792, 10796, 3508, 65535, 3508, 65535, 10665, 10669, 10793, 10797, 3509, 3516, 3509, 65535, 65535, 65535, 65535, 65535, 65535, 65535, 65535, 65535, 65535, 65535, 65535, 65535, 65535, 65535, 65535, 65535, 65535, 65535, 65535, 65535, 65535, 65535, 65535, 65535, 65535, 65535, 65535, 65535, 65535, 65535, 65535, 65535, 65535, 65535, 65535, 65535, 65535, 65535, 65535, 65535, 65535, 65535, 65535, 65535, 65535, 65535, 65535, 65535, 6762, 6763, 65535, 65535, 3120, 65535, 65535, 65535, 65535, 65535, 65535, 65535, 3121, 65535, 65535, 65535, 65535, 65535, 6890, 6891, 3122, 65535, 65535, 65535, 65535, 65535, 65535, 65535, 3123, 65535, 65535, 65535, 65535, 65535, 7024, 7025, 3128, 65535, 65535, 65535, 65535, 65535, 65535, 65535, 3129, 65535, 65535, 65535, 65535, 65535, 65535, 65535, 3130, 65535, 65535, 65535, 65535, 65535, 65535, 65535, 3131, 65535, 65535, 65535, 65535, 65535, 65535, 65535, 3126, 65535, 65535, 65535, 65535, 65535, 65535, 65535, 3127, 65535, 65535, 65535, 65535, 65535, 65535, 65535, 3124, 65535, 65535, 65535, 65535, 65535, 65535, 65535, 3125, 65535, 65535, 65535, 65535, 65535, 65535, 65535, 3134, 65535, 65535, 65535, 65535, 65535, 65535, 65535, 3135, 65535, 65535, 65535, 65535, 65535, 65535, 65535, 3132, 65535, 65535, 65535, 10533, 65535, 65535, 10532, 3133, 65535, 65535, 65535, 65535, 65535, 65535, 65535, 3248, 65535, 65535, 65535, 10415, 65535, 65535, 10414, 3249, 65535, 65535, 65535, 65535, 65535, 65535, 65535, 3250, 65535, 65535, 65535, 10535, 65535, 65535, 10534, 3251, 65535, 65535, 65535, 6760, 65535, 65535, 6893, 3256, 65535, 65535, 65535, 6761, 6764, 65535, 3513, 3257, 65535, 65535, 65535, 3512, 6765, 6888, 65535, 3258, 65535, 65535, 65535, 65535, 65535, 6889, 6892, 3259, 65535, 65535, 65535, 6768, 6772, 65535, 6900, 3252, 65535, 65535, 65535, 6769, 6773, 65535, 6901, 3260, 65535, 65535, 65535, 65535, 65535, 65535, 65535, 65535, 65535, 65535, 65535, 65535, 65535, 65535, 65535, 65535, 65535, 65535, 65535, 65535, 65535, 65535, 65535, 3517, 65535, 65535, 65535, 65535, 65535, 65535, 65535, 65535, 65535, 65535, 65535, 65535, 65535, 65535, 65535, 65535, 65535, 65535, 65535, 65535, 65535, 65535, 65535, 65535, 65535, 65535, 65535};

__global__ __launch_bounds__(512) void xd_kernel(const int* idx, const u16* w1, const u16* table, char* ws, const u16* b1, const u16* w2, const u16* b_out, u16* out,
                                                 const u16* ws_cin, const u16* wout) {
  __shared__ __attribute__((aligned(1024))) char shm[163840];
  asm volatile("" :: "v"((__attribute__((address_space(3))) char*)shm));
  unsigned tid = threadIdx.x;
  asm volatile(
    "s_load_dwordx16 s[4:19], s[0:1], 0\n"
    "s_setprio 3\n"
    "v_readfirstlane_b32 s20, v0\n"
    "v_and_b32 v1, 63, v0\n"
    "s_sub_u32 s3, s2, 48\n"
    "s_lshr_b32 s20, s20, 6\n"
    "s_cmp_lt_u32 s2, 48\n"
    "s_cbranch_scc1 .Lxd_cin\n"
    "s_and_b32 s40, s2, 7\n"
    "s_lshr_b32 s23, s3, 4\n"
    "s_bfe_u32 s41, s3, 0x10003\n"
    "s_lshl1_add_u32 s24, s40, s41\n"
    "s_lshl_b32 s24, s24, 8\n"
    "s_mul_i32 s40, s40, 3\n"
    "s_and_b32 s41, s23, 3\n"
    "s_mul_i32 s41, s41, 6\n"
    "s_add_u32 s43, s40, s41\n"
    "s_lshr_b32 s43, s43, 1\n"
    "s_sub_u32 s40, s43, 12\n"
    "s_min_u32 s43, s43, s40\n"
    "s_cmp_eq_u32 s43, 10\n"
    "s_cselect_b32 s42, 11, s43\n"
    "s_lshl_b32 s42, s42, 3\n"
    "v_and_b32 v84, 3, v0\n"
    "v_lshrrev_b32 v44, 3, v1\n"
    "v_lshl_add_u32 v85, v84, 3, v44\n"
    "s_lshl_b32 s40, s20, 5\n"
    "s_add_u32 s40, s40, s24\n"
    "s_mul_i32 s40, s40, 104\n"
    "s_movk_i32 s41, 104\n"
    "v_mov_b32 v86, s42\n"
    "v_mad_u32_u24 v86, v85, s41, v86\n"
    "v_lshrrev_b32 v85, 4, v1\n"
    "v_bfe_u32 v87, v0, 4, 3\n"
    "v_lshlrev_b32 v87, 5, v87\n"
    "v_and_b32 v88, 15, v0\n"
    "v_lshlrev_b32 v47, 4, v88\n"
    "v_xor_b32 v87, v87, v47\n"
    "s_movk_i32 s41, 3328\n"
    "v_mad_u32_u24 v46, v85, s41, v87\n"
    "v_add_u32 v47, 12288, v46\n"
    "s_mul_i32 s41, s20, 26624\n"
    "s_lshl_b32 s66, s23, 8\n"
    "s_add_u32 s41, s41, s66\n"
    "s_mul_i32 s42, s42, 53248\n"
    "s_lshl_b32 s35, s20, 11\n"
    "s_add_u32 s35, s35, 32768\n"
    "s_waitcnt lgkmcnt(0)\n"
    "s_add_u32 s38, s4, s40\n"
    "s_addc_u32 s39, s5, 0\n"
    "global_load_dwordx4 v[92:95], v86, s[38:39]\n"
    "s_add_u32 s6, s6, s41\n"
    "s_addc_u32 s7, s7, 0\n"
    "s_add_u32 s30, s6, s42\n"
    "s_addc_u32 s31, s7, 0\n"
    "s_add_u32 m0, s35, 0\n"
    "s_nop 0\n"
    "global_load_lds_dwordx4 v46, s[30:31] offset:0\n"
    "global_load_lds_dwordx4 v47, s[30:31] offset:1024\n"
    "s_add_u32 s30, s30, 212992\n"
    "s_addc_u32 s31, s31, 0\n"
    "s_setprio 1\n"
    "s_and_b32 s40, s20, 2\n"
    "s_lshl_b32 s40, s40, 6\n"
    "s_add_u32 s40, s40, s66\n"
    "v_and_b32 v43, 31, v0\n"
    "s_lshl_b32 s64, s20, 8\n"
    "s_add_u32 s64, s64, 147456\n"
    "s_mov_b32 m0, s64\n"
    "v_lshl_add_u32 v43, v43, 2, s40\n"
    "global_load_lds_dword v43, s[12:13]\n"
    "s_sub_u32 s36, s8, 3072\n"
    "s_subb_u32 s37, s9, 0\n"
    "s_lshl_b32 s34, s20, 12\n"
    "v_and_b32 v86, 7, v0\n"
    "v_xor_b32 v86, v86, v85\n"
    "v_lshlrev_b32 v86, 4, v86\n"
    "v_xor_b32 v51, 64, v86\n"
    "v_add_u32 v48, 3072, v86\n"
    "v_add_u32 v49, 2048, v51\n"
    "v_add_u32 v50, 1024, v86\n"
    "s_setprio 0\n"
    "s_cmp_eq_u32 s20, 7\n"
    "s_cbranch_scc1 .Lxd_on1\n"
    ".Lxd_on1r:\n"
    "v_lshrrev_b32 v86, 1, v88\n"
    "v_xor_b32 v86, v86, v85\n"
    "v_lshlrev_b32 v86, 4, v86\n"
    "v_lshl_add_u32 v86, v88, 7, v86\n"
    "s_lshr_b32 s21, s20, 2\n"
    "s_and_b32 s41, s20, 1\n"
    "s_lshl1_add_u32 s40, s21, s41\n"
    "s_lshl_b32 s40, s40, 13\n"
    "v_add_u32 v56, s40, v86\n"
    "v_xor_b32 v57, 64, v56\n"
    "v_lshrrev_b32 v86, 2, v88\n"
    "v_lshl_add_u32 v88, v85, 3, v86\n"
    "v_bfe_u32 v86, v0, 2, 3\n"
    "s_and_b32 s40, s20, 2\n"
    "s_lshl1_add_u32 s40, s40, 1024\n"
    "v_xor_b32 v86, s40, v86\n"
    "v_lshlrev_b32 v86, 5, v86\n"
    "v_lshl_add_u32 v86, v88, 8, v86\n"
    "v_lshl_add_u32 v76, v84, 3, v86\n"
    "v_xor_b32 v77, 32, v76\n"
    "v_xor_b32 v78, 64, v76\n"
    "v_xor_b32 v79, 96, v76\n"
    "v_lshl_add_u32 v42, v85, 3, s64\n"
    "s_setprio 3\n"
    "s_waitcnt vmcnt(3)\n"
    "v_lshlrev_b32 v92, 7, v92\n"
    "v_lshlrev_b32 v93, 7, v93\n"
    "v_lshlrev_b32 v94, 7, v94\n"
    "v_add_u32_dpp v52, v92, v48 quad_perm:[0,0,0,0] row_mask:0xf bank_mask:0xf\n"
    "v_add_u32_dpp v53, v92, v49 quad_perm:[1,1,1,1] row_mask:0xf bank_mask:0xf\n"
    "v_add_u32_dpp v54, v92, v50 quad_perm:[2,2,2,2] row_mask:0xf bank_mask:0xf\n"
    "v_add_u32_dpp v55, v92, v51 quad_perm:[3,3,3,3] row_mask:0xf bank_mask:0xf\n"
    "s_add_u32 m0, s34, 0\n"
    "s_nop 0\n"
    "global_load_lds_dwordx4 v52, s[36:37] offset:0\n"
    "global_load_lds_dwordx4 v53, s[36:37] offset:1024\n"
    "global_load_lds_dwordx4 v54, s[36:37] offset:2048\n"
    "global_load_lds_dwordx4 v55, s[36:37] offset:3072\n"
    "s_setprio 1\n"
    "s_add_u32 m0, s35, 49152\n"
    "s_nop 0\n"
    "global_load_lds_dwordx4 v46, s[30:31] offset:0\n"
    "global_load_lds_dwordx4 v47, s[30:31] offset:1024\n"
    "s_add_u32 s30, s30, 212992\n"
    "s_addc_u32 s31, s31, 0\n"
    "v_add_u32_dpp v52, v93, v48 quad_perm:[0,0,0,0] row_mask:0xf bank_mask:0xf\n"
    "v_add_u32_dpp v53, v93, v49 quad_perm:[1,1,1,1] row_mask:0xf bank_mask:0xf\n"
    "v_add_u32_dpp v54, v93, v50 quad_perm:[2,2,2,2] row_mask:0xf bank_mask:0xf\n"
    "v_add_u32_dpp v55, v93, v51 quad_perm:[3,3,3,3] row_mask:0xf bank_mask:0xf\n"
    "s_add_u32 m0, s34, 49152\n"
    "s_nop 0\n"
    "global_load_lds_dwordx4 v52, s[36:37] offset:0\n"
    "global_load_lds_dwordx4 v53, s[36:37] offset:1024\n"
    "global_load_lds_dwordx4 v54, s[36:37] offset:2048\n"
    "global_load_lds_dwordx4 v55, s[36:37] offset:3072\n"
    "s_setprio 0\n"
    "s_lshr_b32 s41, s43, 1\n"
    "s_lshl3_add_u32 s41, s41, 8\n"
    "s_bitcmp1_b32 s43, 0\n"
    "s_cselect_b32 s28, 0x38281808, 0\n"
    "s_cselect_b32 s29, 0x5848, 0\n"
    "s_cselect_b32 s40, 0, 0x50505000\n"
    "s_cselect_b32 s28, s28, 0x30201000\n"
    "s_cselect_b32 s29, s29, 0x5840\n"
    "s_lshr_b64 s[42:43], s[28:29], s41\n"
    "s_sub_u32 s41, 48, s41\n"
    "s_lshl_b64 s[28:29], s[28:29], s41\n"
    "s_or_b64 s[28:29], s[28:29], s[42:43]\n"
    "s_and_b32 s29, s29, 0xff\n"
    "s_or_b32 s29, s29, s40\n"
    "v_lshlrev_b32 v12, 3, v0\n"
    "v_mul_u32_u24 v13, 104, v44\n"
    "v_bfe_u32 v44, s28, v12, 8\n"
    "v_bfe_u32 v45, s29, v12, 8\n"
    "v_add_u32 v44, v44, v13\n"
    "v_add_u32 v45, v45, v13\n"
    "s_waitcnt vmcnt(10)\n"
    "ds_read_b64 v[84:85], v42 offset:0\n"
    "ds_read_b64 v[86:87], v42 offset:32\n"
    "ds_read_b64 v[88:89], v42 offset:64\n"
    "ds_read_b64 v[90:91], v42 offset:96\n"
    "v_add_u32_dpp v52, v94, v48 quad_perm:[0,0,0,0] row_mask:0xf bank_mask:0xf\n"
    "v_add_u32_dpp v53, v94, v49 quad_perm:[1,1,1,1] row_mask:0xf bank_mask:0xf\n"
    "v_add_u32_dpp v54, v94, v50 quad_perm:[2,2,2,2] row_mask:0xf bank_mask:0xf\n"
    "v_add_u32_dpp v55, v94, v51 quad_perm:[3,3,3,3] row_mask:0xf bank_mask:0xf\n"
    "v_lshlrev_b32 v95, 7, v95\n"
    "s_waitcnt lgkmcnt(0)\n"
    "v_lshlrev_b32 v60, 16, v84\n"
    "v_and_b32 v61, 0xffff0000, v84\n"
    "v_lshlrev_b32 v62, 16, v85\n"
    "v_and_b32 v63, 0xffff0000, v85\n"
    "v_lshlrev_b32 v64, 16, v86\n"
    "v_and_b32 v65, 0xffff0000, v86\n"
    "v_lshlrev_b32 v66, 16, v87\n"
    "v_and_b32 v67, 0xffff0000, v87\n"
    "v_lshlrev_b32 v68, 16, v88\n"
    "v_and_b32 v69, 0xffff0000, v88\n"
    "v_lshlrev_b32 v70, 16, v89\n"
    "v_and_b32 v71, 0xffff0000, v89\n"
    "v_lshlrev_b32 v72, 16, v90\n"
    "v_and_b32 v73, 0xffff0000, v90\n"
    "v_lshlrev_b32 v74, 16, v91\n"
    "v_and_b32 v75, 0xffff0000, v91\n"
    "global_load_dwordx4 v[96:99], v44, s[38:39] offset:0\n"
    "global_load_dwordx4 v[104:107], v44, s[38:39] offset:832\n"
    "global_load_dwordx4 v[112:115], v44, s[38:39] offset:1664\n"
    "global_load_dwordx4 v[120:123], v44, s[38:39] offset:2496\n"
    "s_cmp_eq_u32 s21, 0\n"
    "s_cbranch_scc1 .Lxd_g0\n"
    "s_barrier\n"
    ".Lxd_g0:\n"
    "s_waitcnt vmcnt(10)\n"
    "s_barrier\n"
    "s_add_u32 m0, s35, 98304\n"
    "s_nop 0\n"
    "global_load_lds_dwordx4 v46, s[30:31] offset:0\n"
    "ds_read_b128 v[128:131], v56 offset:0\n"
    "ds_read_b128 v[132:135], v56 offset:2048\n"
    "ds_read_b128 v[136:139], v56 offset:4096\n"
    "ds_read_b128 v[140:143], v56 offset:6144\n"
    "global_load_lds_dwordx4 v47, s[30:31] offset:1024\n"
    "s_add_u32 s30, s30, 212992\n"
    "s_addc_u32 s31, s31, 0\n"
    "ds_read_b64_tr_b16 v[144:145], v76 offset:0\n"
    "ds_read_b64_tr_b16 v[146:147], v76 offset:1024\n"
    "ds_read_b64_tr_b16 v[148:149], v77 offset:0\n"
    "ds_read_b64_tr_b16 v[150:151], v77 offset:1024\n"
    "s_add_u32 m0, s34, 98304\n"
    "s_nop 0\n"
    "global_load_lds_dwordx4 v52, s[36:37] offset:0\n"
    "ds_read_b64_tr_b16 v[152:153], v78 offset:0\n"
    "ds_read_b64_tr_b16 v[154:155], v78 offset:1024\n"
    "ds_read_b64_tr_b16 v[156:157], v79 offset:0\n"
    "ds_read_b64_tr_b16 v[158:159], v79 offset:1024\n"
    "global_load_lds_dwordx4 v53, s[36:37] offset:1024\n"
    "ds_read_b128 v[160:163], v57 offset:0\n"
    "ds_read_b128 v[164:167], v57 offset:2048\n"
    "ds_read_b128 v[168:171], v57 offset:4096\n"
    "ds_read_b128 v[172:175], v57 offset:6144\n"
    "global_load_lds_dwordx4 v54, s[36:37] offset:2048\n"
    "ds_read_b64_tr_b16 v[176:177], v76 offset:8192\n"
    "ds_read_b64_tr_b16 v[178:179], v76 offset:9216\n"
    "ds_read_b64_tr_b16 v[180:181], v77 offset:8192\n"
    "ds_read_b64_tr_b16 v[182:183], v77 offset:9216\n"
    "global_load_lds_dwordx4 v55, s[36:37] offset:3072\n"
    "ds_read_b64_tr_b16 v[184:185], v78 offset:8192\n"
    "ds_read_b64_tr_b16 v[186:187], v78 offset:9216\n"
    "ds_read_b64_tr_b16 v[188:189], v79 offset:8192\n"
    "ds_read_b64_tr_b16 v[190:191], v79 offset:9216\n"
    "s_waitcnt vmcnt(14) lgkmcnt(0)\n"
    "s_barrier\n"
    "v_mfma_f32_16x16x32_bf16 v[192:195], v[144:147], v[128:131], v[60:63]\n"
    "v_mfma_f32_16x16x32_bf16 v[192:195], v[176:179], v[160:163], v[192:195]\n"
    "v_add_u32_dpp v52, v95, v48 quad_perm:[0,0,0,0] row_mask:0xf bank_mask:0xf\n"
    "v_mfma_f32_16x16x32_bf16 v[196:199], v[176:179], v[164:167], v[60:63]\n"
    "v_mfma_f32_16x16x32_bf16 v[196:199], v[144:147], v[132:135], v[196:199]\n"
    "v_add_u32_dpp v53, v95, v49 quad_perm:[1,1,1,1] row_mask:0xf bank_mask:0xf\n"
    "v_mfma_f32_16x16x32_bf16 v[200:203], v[144:147], v[136:139], v[60:63]\n"
    "v_mfma_f32_16x16x32_bf16 v[200:203], v[176:179], v[168:171], v[200:203]\n"
    "v_mfma_f32_16x16x32_bf16 v[204:207], v[176:179], v[172:175], v[60:63]\n"
    "v_add_u32_dpp v54, v95, v50 quad_perm:[2,2,2,2] row_mask:0xf bank_mask:0xf\n"
    "v_mfma_f32_16x16x32_bf16 v[204:207], v[144:147], v[140:143], v[204:207]\n"
    "v_mfma_f32_16x16x32_bf16 v[220:223], v[148:151], v[140:143], v[64:67]\n"
    "v_mfma_f32_16x16x32_bf16 v[220:223], v[180:183], v[172:175], v[220:223]\n"
    "v_add_u32_dpp v55, v95, v51 quad_perm:[3,3,3,3] row_mask:0xf bank_mask:0xf\n"
    "v_mfma_f32_16x16x32_bf16 v[216:219], v[180:183], v[168:171], v[64:67]\n"
    "v_mfma_f32_16x16x32_bf16 v[216:219], v[148:151], v[136:139], v[216:219]\n"
    "v_mfma_f32_16x16x32_bf16 v[212:215], v[148:151], v[132:135], v[64:67]\n"
    "v_add_u32 v58, 98304, v56\n"
    "v_mfma_f32_16x16x32_bf16 v[212:215], v[180:183], v[164:167], v[212:215]\n"
    "v_mfma_f32_16x16x32_bf16 v[208:211], v[180:183], v[160:163], v[64:67]\n"
    "v_mfma_f32_16x16x32_bf16 v[208:211], v[148:151], v[128:131], v[208:211]\n"
    "v_add_u32 v59, 98304, v57\n"
    "v_mfma_f32_16x16x32_bf16 v[224:227], v[152:155], v[128:131], v[68:71]\n"
    "v_mfma_f32_16x16x32_bf16 v[224:227], v[184:187], v[160:163], v[224:227]\n"
    "v_mfma_f32_16x16x32_bf16 v[228:231], v[184:187], v[164:167], v[68:71]\n"
    "v_add_u32 v80, 98304, v76\n"
    "v_mfma_f32_16x16x32_bf16 v[228:231], v[152:155], v[132:135], v[228:231]\n"
    "v_mfma_f32_16x16x32_bf16 v[232:235], v[152:155], v[136:139], v[68:71]\n"
    "v_mfma_f32_16x16x32_bf16 v[232:235], v[184:187], v[168:171], v[232:235]\n"
    "v_add_u32 v81, 98304, v77\n"
    "v_mfma_f32_16x16x32_bf16 v[236:239], v[184:187], v[172:175], v[68:71]\n"
    "v_mfma_f32_16x16x32_bf16 v[236:239], v[152:155], v[140:143], v[236:239]\n"
    "v_mfma_f32_16x16x32_bf16 v[252:255], v[156:159], v[140:143], v[72:75]\n"
    "v_add_u32 v82, 98304, v78\n"
    "v_mfma_f32_16x16x32_bf16 v[252:255], v[188:191], v[172:175], v[252:255]\n"
    "v_mfma_f32_16x16x32_bf16 v[248:251], v[188:191], v[168:171], v[72:75]\n"
    "v_mfma_f32_16x16x32_bf16 v[248:251], v[156:159], v[136:139], v[248:251]\n"
    "v_add_u32 v83, 98304, v79\n"
    "v_mfma_f32_16x16x32_bf16 v[244:247], v[156:159], v[132:135], v[72:75]\n"
    "v_mfma_f32_16x16x32_bf16 v[244:247], v[188:191], v[164:167], v[244:247]\n"
    "v_mfma_f32_16x16x32_bf16 v[240:243], v[188:191], v[160:163], v[72:75]\n"
    "v_mfma_f32_16x16x32_bf16 v[240:243], v[156:159], v[128:131], v[240:243]\n"
    "s_waitcnt vmcnt(10)\n"
    "s_barrier\n"
    "s_add_u32 m0, s35, 0\n"
    "s_nop 0\n"
    "global_load_lds_dwordx4 v46, s[30:31] offset:0\n"
    "ds_read_b128 v[128:131], v56 offset:49152\n"
    "ds_read_b128 v[132:135], v56 offset:51200\n"
    "ds_read_b128 v[136:139], v56 offset:53248\n"
    "ds_read_b128 v[140:143], v56 offset:55296\n"
    "global_load_lds_dwordx4 v47, s[30:31] offset:1024\n"
    "s_bfe_u32 s66, s28, 0x80000\n"
    "s_mul_i32 s66, s66, 53248\n"
    "s_add_u32 s30, s6, s66\n"
    "s_addc_u32 s31, s7, 0\n"
    "ds_read_b64_tr_b16 v[144:145], v76 offset:49152\n"
    "ds_read_b64_tr_b16 v[146:147], v76 offset:50176\n"
    "ds_read_b64_tr_b16 v[148:149], v77 offset:49152\n"
    "ds_read_b64_tr_b16 v[150:151], v77 offset:50176\n"
    "s_add_u32 m0, s34, 0\n"
    "s_nop 0\n"
    "global_load_lds_dwordx4 v52, s[36:37] offset:0\n"
    "ds_read_b64_tr_b16 v[152:153], v78 offset:49152\n"
    "ds_read_b64_tr_b16 v[154:155], v78 offset:50176\n"
    "ds_read_b64_tr_b16 v[156:157], v79 offset:49152\n"
    "ds_read_b64_tr_b16 v[158:159], v79 offset:50176\n"
    "global_load_lds_dwordx4 v53, s[36:37] offset:1024\n"
    "ds_read_b128 v[160:163], v57 offset:49152\n"
    "ds_read_b128 v[164:167], v57 offset:51200\n"
    "ds_read_b128 v[168:171], v57 offset:53248\n"
    "ds_read_b128 v[172:175], v57 offset:55296\n"
    "global_load_lds_dwordx4 v54, s[36:37] offset:2048\n"
    "ds_read_b64_tr_b16 v[176:177], v76 offset:57344\n"
    "ds_read_b64_tr_b16 v[178:179], v76 offset:58368\n"
    "ds_read_b64_tr_b16 v[180:181], v77 offset:57344\n"
    "ds_read_b64_tr_b16 v[182:183], v77 offset:58368\n"
    "global_load_lds_dwordx4 v55, s[36:37] offset:3072\n"
    "ds_read_b64_tr_b16 v[184:185], v78 offset:57344\n"
    "ds_read_b64_tr_b16 v[186:187], v78 offset:58368\n"
    "ds_read_b64_tr_b16 v[188:189], v79 offset:57344\n"
    "ds_read_b64_tr_b16 v[190:191], v79 offset:58368\n"
    "s_waitcnt vmcnt(10) lgkmcnt(0)\n"
    "s_barrier\n"
    "v_mfma_f32_16x16x32_bf16 v[192:195], v[144:147], v[128:131], v[192:195]\n"
    "v_mfma_f32_16x16x32_bf16 v[192:195], v[176:179], v[160:163], v[192:195]\n"
    "v_lshlrev_b32 v96, 7, v96\n"
    "v_mfma_f32_16x16x32_bf16 v[196:199], v[176:179], v[164:167], v[196:199]\n"
    "v_mfma_f32_16x16x32_bf16 v[196:199], v[144:147], v[132:135], v[196:199]\n"
    "v_mfma_f32_16x16x32_bf16 v[200:203], v[144:147], v[136:139], v[200:203]\n"
    "v_lshlrev_b32 v104, 7, v104\n"
    "v_mfma_f32_16x16x32_bf16 v[200:203], v[176:179], v[168:171], v[200:203]\n"
    "v_mfma_f32_16x16x32_bf16 v[204:207], v[176:179], v[172:175], v[204:207]\n"
    "v_mfma_f32_16x16x32_bf16 v[204:207], v[144:147], v[140:143], v[204:207]\n"
    "v_mfma_f32_16x16x32_bf16 v[220:223], v[148:151], v[140:143], v[220:223]\n"
    "v_lshlrev_b32 v112, 7, v112\n"
    "v_mfma_f32_16x16x32_bf16 v[220:223], v[180:183], v[172:175], v[220:223]\n"
    "v_mfma_f32_16x16x32_bf16 v[216:219], v[180:183], v[168:171], v[216:219]\n"
    "v_mfma_f32_16x16x32_bf16 v[216:219], v[148:151], v[136:139], v[216:219]\n"
    "v_lshlrev_b32 v120, 7, v120\n"
    "v_mfma_f32_16x16x32_bf16 v[212:215], v[148:151], v[132:135], v[212:215]\n"
    "v_mfma_f32_16x16x32_bf16 v[212:215], v[180:183], v[164:167], v[212:215]\n"
    "v_mfma_f32_16x16x32_bf16 v[208:211], v[180:183], v[160:163], v[208:211]\n"
    "v_mfma_f32_16x16x32_bf16 v[208:211], v[148:151], v[128:131], v[208:211]\n"
    "v_add_u32_dpp v52, v96, v48 quad_perm:[0,0,0,0] row_mask:0xf bank_mask:0xf\n"
    "v_mfma_f32_16x16x32_bf16 v[224:227], v[152:155], v[128:131], v[224:227]\n"
    "v_mfma_f32_16x16x32_bf16 v[224:227], v[184:187], v[160:163], v[224:227]\n"
    "v_mfma_f32_16x16x32_bf16 v[228:231], v[184:187], v[164:167], v[228:231]\n"
    "v_mfma_f32_16x16x32_bf16 v[228:231], v[152:155], v[132:135], v[228:231]\n"
    "v_add_u32_dpp v53, v104, v49 quad_perm:[0,0,0,0] row_mask:0xf bank_mask:0xf\n"
    "v_mfma_f32_16x16x32_bf16 v[232:235], v[152:155], v[136:139], v[232:235]\n"
    "v_mfma_f32_16x16x32_bf16 v[232:235], v[184:187], v[168:171], v[232:235]\n"
    "v_mfma_f32_16x16x32_bf16 v[236:239], v[184:187], v[172:175], v[236:239]\n"
    "v_add_u32_dpp v54, v112, v50 quad_perm:[0,0,0,0] row_mask:0xf bank_mask:0xf\n"
    "v_mfma_f32_16x16x32_bf16 v[236:239], v[152:155], v[140:143], v[236:239]\n"
    "v_mfma_f32_16x16x32_bf16 v[252:255], v[156:159], v[140:143], v[252:255]\n"
    "v_mfma_f32_16x16x32_bf16 v[252:255], v[188:191], v[172:175], v[252:255]\n"
    "v_mfma_f32_16x16x32_bf16 v[248:251], v[188:191], v[168:171], v[248:251]\n"
    "v_add_u32_dpp v55, v120, v51 quad_perm:[0,0,0,0] row_mask:0xf bank_mask:0xf\n"
    "v_mfma_f32_16x16x32_bf16 v[248:251], v[156:159], v[136:139], v[248:251]\n"
    "v_mfma_f32_16x16x32_bf16 v[244:247], v[156:159], v[132:135], v[244:247]\n"
    "v_mfma_f32_16x16x32_bf16 v[244:247], v[188:191], v[164:167], v[244:247]\n"
    "v_mfma_f32_16x16x32_bf16 v[240:243], v[188:191], v[160:163], v[240:243]\n"
    "v_mfma_f32_16x16x32_bf16 v[240:243], v[156:159], v[128:131], v[240:243]\n"
    "s_waitcnt vmcnt(6)\n"
    "s_barrier\n"
    "s_add_u32 m0, s35, 49152\n"
    "s_nop 0\n"
    "global_load_lds_dwordx4 v46, s[30:31] offset:0\n"
    "ds_read_b128 v[128:131], v58 offset:0\n"
    "ds_read_b128 v[132:135], v58 offset:2048\n"
    "ds_read_b128 v[136:139], v58 offset:4096\n"
    "global_load_lds_dwordx4 v47, s[30:31] offset:1024\n"
    "s_add_u32 s30, s30, 212992\n"
    "s_addc_u32 s31, s31, 0\n"
    "ds_read_b128 v[140:143], v58 offset:6144\n"
    "ds_read_b64_tr_b16 v[144:145], v80 offset:0\n"
    "ds_read_b64_tr_b16 v[146:147], v80 offset:1024\n"
    "s_add_u32 m0, s34, 49152\n"
    "s_nop 0\n"
    "global_load_lds_dwordx4 v52, s[36:37] offset:0\n"
    "ds_read_b64_tr_b16 v[148:149], v81 offset:0\n"
    "ds_read_b64_tr_b16 v[150:151], v81 offset:1024\n"
    "ds_read_b64_tr_b16 v[152:153], v82 offset:0\n"
    "global_load_lds_dwordx4 v53, s[36:37] offset:1024\n"
    "ds_read_b64_tr_b16 v[154:155], v82 offset:1024\n"
    "ds_read_b64_tr_b16 v[156:157], v83 offset:0\n"
    "ds_read_b64_tr_b16 v[158:159], v83 offset:1024\n"
    "global_load_lds_dwordx4 v54, s[36:37] offset:2048\n"
    "ds_read_b128 v[160:163], v59 offset:0\n"
    "ds_read_b128 v[164:167], v59 offset:2048\n"
    "ds_read_b128 v[168:171], v59 offset:4096\n"
    "global_load_lds_dwordx4 v55, s[36:37] offset:3072\n"
    "ds_read_b128 v[172:175], v59 offset:6144\n"
    "ds_read_b64_tr_b16 v[176:177], v80 offset:8192\n"
    "ds_read_b64_tr_b16 v[178:179], v80 offset:9216\n"
    "global_load_dwordx4 v[100:103], v45, s[38:39] offset:0\n"
    "ds_read_b64_tr_b16 v[180:181], v81 offset:8192\n"
    "ds_read_b64_tr_b16 v[182:183], v81 offset:9216\n"
    "ds_read_b64_tr_b16 v[184:185], v82 offset:8192\n"
    "ds_read_b64_tr_b16 v[186:187], v82 offset:9216\n"
    "ds_read_b64_tr_b16 v[188:189], v83 offset:8192\n"
    "ds_read_b64_tr_b16 v[190:191], v83 offset:9216\n"
    "s_waitcnt vmcnt(11) lgkmcnt(0)\n"
    "s_barrier\n"
    "v_mfma_f32_16x16x32_bf16 v[192:195], v[144:147], v[128:131], v[192:195]\n"
    "v_mfma_f32_16x16x32_bf16 v[192:195], v[176:179], v[160:163], v[192:195]\n"
    "v_lshlrev_b32 v97, 7, v97\n"
    "v_mfma_f32_16x16x32_bf16 v[196:199], v[176:179], v[164:167], v[196:199]\n"
    "v_mfma_f32_16x16x32_bf16 v[196:199], v[144:147], v[132:135], v[196:199]\n"
    "v_mfma_f32_16x16x32_bf16 v[200:203], v[144:147], v[136:139], v[200:203]\n"
    "v_lshlrev_b32 v105, 7, v105\n"
    "v_mfma_f32_16x16x32_bf16 v[200:203], v[176:179], v[168:171], v[200:203]\n"
    "v_mfma_f32_16x16x32_bf16 v[204:207], v[176:179], v[172:175], v[204:207]\n"
    "v_mfma_f32_16x16x32_bf16 v[204:207], v[144:147], v[140:143], v[204:207]\n"
    "v_mfma_f32_16x16x32_bf16 v[220:223], v[148:151], v[140:143], v[220:223]\n"
    "v_lshlrev_b32 v113, 7, v113\n"
    "v_mfma_f32_16x16x32_bf16 v[220:223], v[180:183], v[172:175], v[220:223]\n"
    "v_mfma_f32_16x16x32_bf16 v[216:219], v[180:183], v[168:171], v[216:219]\n"
    "v_mfma_f32_16x16x32_bf16 v[216:219], v[148:151], v[136:139], v[216:219]\n"
    "v_lshlrev_b32 v121, 7, v121\n"
    "v_mfma_f32_16x16x32_bf16 v[212:215], v[148:151], v[132:135], v[212:215]\n"
    "v_mfma_f32_16x16x32_bf16 v[212:215], v[180:183], v[164:167], v[212:215]\n"
    "v_mfma_f32_16x16x32_bf16 v[208:211], v[180:183], v[160:163], v[208:211]\n"
    "v_mfma_f32_16x16x32_bf16 v[208:211], v[148:151], v[128:131], v[208:211]\n"
    "v_add_u32_dpp v52, v97, v48 quad_perm:[0,0,0,0] row_mask:0xf bank_mask:0xf\n"
    "v_mfma_f32_16x16x32_bf16 v[224:227], v[152:155], v[128:131], v[224:227]\n"
    "v_mfma_f32_16x16x32_bf16 v[224:227], v[184:187], v[160:163], v[224:227]\n"
    "v_mfma_f32_16x16x32_bf16 v[228:231], v[184:187], v[164:167], v[228:231]\n"
    "v_mfma_f32_16x16x32_bf16 v[228:231], v[152:155], v[132:135], v[228:231]\n"
    "v_add_u32_dpp v53, v105, v49 quad_perm:[0,0,0,0] row_mask:0xf bank_mask:0xf\n"
    "v_mfma_f32_16x16x32_bf16 v[232:235], v[152:155], v[136:139], v[232:235]\n"
    "v_mfma_f32_16x16x32_bf16 v[232:235], v[184:187], v[168:171], v[232:235]\n"
    "v_mfma_f32_16x16x32_bf16 v[236:239], v[184:187], v[172:175], v[236:239]\n"
    "v_add_u32_dpp v54, v113, v50 quad_perm:[0,0,0,0] row_mask:0xf bank_mask:0xf\n"
    "v_mfma_f32_16x16x32_bf16 v[236:239], v[152:155], v[140:143], v[236:239]\n"
    "v_mfma_f32_16x16x32_bf16 v[252:255], v[156:159], v[140:143], v[252:255]\n"
    "v_mfma_f32_16x16x32_bf16 v[252:255], v[188:191], v[172:175], v[252:255]\n"
    "v_mfma_f32_16x16x32_bf16 v[248:251], v[188:191], v[168:171], v[248:251]\n"
    "v_add_u32_dpp v55, v121, v51 quad_perm:[0,0,0,0] row_mask:0xf bank_mask:0xf\n"
    "v_mfma_f32_16x16x32_bf16 v[248:251], v[156:159], v[136:139], v[248:251]\n"
    "v_mfma_f32_16x16x32_bf16 v[244:247], v[156:159], v[132:135], v[244:247]\n"
    "v_mfma_f32_16x16x32_bf16 v[244:247], v[188:191], v[164:167], v[244:247]\n"
    "v_mfma_f32_16x16x32_bf16 v[240:243], v[188:191], v[160:163], v[240:243]\n"
    "v_mfma_f32_16x16x32_bf16 v[240:243], v[156:159], v[128:131], v[240:243]\n"
    "s_waitcnt vmcnt(7)\n"
    "s_barrier\n"
    "s_add_u32 m0, s35, 98304\n"
    "s_nop 0\n"
    "global_load_lds_dwordx4 v46, s[30:31] offset:0\n"
    "ds_read_b128 v[128:131], v56 offset:0\n"
    "ds_read_b128 v[132:135], v56 offset:2048\n"
    "ds_read_b128 v[136:139], v56 offset:4096\n"
    "global_load_lds_dwordx4 v47, s[30:31] offset:1024\n"
    "s_add_u32 s30, s30, 212992\n"
    "s_addc_u32 s31, s31, 0\n"
    "ds_read_b128 v[140:143], v56 offset:6144\n"
    "ds_read_b64_tr_b16 v[144:145], v76 offset:0\n"
    "ds_read_b64_tr_b16 v[146:147], v76 offset:1024\n"
    "s_add_u32 m0, s34, 98304\n"
    "s_nop 0\n"
    "global_load_lds_dwordx4 v52, s[36:37] offset:0\n"
    "ds_read_b64_tr_b16 v[148:149], v77 offset:0\n"
    "ds_read_b64_tr_b16 v[150:151], v77 offset:1024\n"
    "ds_read_b64_tr_b16 v[152:153], v78 offset:0\n"
    "global_load_lds_dwordx4 v53, s[36:37] offset:1024\n"
    "ds_read_b64_tr_b16 v[154:155], v78 offset:1024\n"
    "ds_read_b64_tr_b16 v[156:157], v79 offset:0\n"
    "ds_read_b64_tr_b16 v[158:159], v79 offset:1024\n"
    "global_load_lds_dwordx4 v54, s[36:37] offset:2048\n"
    "ds_read_b128 v[160:163], v57 offset:0\n"
    "ds_read_b128 v[164:167], v57 offset:2048\n"
    "ds_read_b128 v[168:171], v57 offset:4096\n"
    "global_load_lds_dwordx4 v55, s[36:37] offset:3072\n"
    "ds_read_b128 v[172:175], v57 offset:6144\n"
    "ds_read_b64_tr_b16 v[176:177], v76 offset:8192\n"
    "ds_read_b64_tr_b16 v[178:179], v76 offset:9216\n"
    "global_load_dwordx4 v[108:111], v45, s[38:39] offset:832\n"
    "ds_read_b64_tr_b16 v[180:181], v77 offset:8192\n"
    "ds_read_b64_tr_b16 v[182:183], v77 offset:9216\n"
    "ds_read_b64_tr_b16 v[184:185], v78 offset:8192\n"
    "ds_read_b64_tr_b16 v[186:187], v78 offset:9216\n"
    "ds_read_b64_tr_b16 v[188:189], v79 offset:8192\n"
    "ds_read_b64_tr_b16 v[190:191], v79 offset:9216\n"
    "s_waitcnt vmcnt(12) lgkmcnt(0)\n"
    "s_barrier\n"
    "v_mfma_f32_16x16x32_bf16 v[192:195], v[144:147], v[128:131], v[192:195]\n"
    "v_mfma_f32_16x16x32_bf16 v[192:195], v[176:179], v[160:163], v[192:195]\n"
    "v_lshlrev_b32 v98, 7, v98\n"
    "v_mfma_f32_16x16x32_bf16 v[196:199], v[176:179], v[164:167], v[196:199]\n"
    "v_mfma_f32_16x16x32_bf16 v[196:199], v[144:147], v[132:135], v[196:199]\n"
    "v_mfma_f32_16x16x32_bf16 v[200:203], v[144:147], v[136:139], v[200:203]\n"
    "v_lshlrev_b32 v106, 7, v106\n"
    "v_mfma_f32_16x16x32_bf16 v[200:203], v[176:179], v[168:171], v[200:203]\n"
    "v_mfma_f32_16x16x32_bf16 v[204:207], v[176:179], v[172:175], v[204:207]\n"
    "v_mfma_f32_16x16x32_bf16 v[204:207], v[144:147], v[140:143], v[204:207]\n"
    "v_mfma_f32_16x16x32_bf16 v[220:223], v[148:151], v[140:143], v[220:223]\n"
    "v_lshlrev_b32 v114, 7, v114\n"
    "v_mfma_f32_16x16x32_bf16 v[220:223], v[180:183], v[172:175], v[220:223]\n"
    "v_mfma_f32_16x16x32_bf16 v[216:219], v[180:183], v[168:171], v[216:219]\n"
    "v_mfma_f32_16x16x32_bf16 v[216:219], v[148:151], v[136:139], v[216:219]\n"
    "v_lshlrev_b32 v122, 7, v122\n"
    "v_mfma_f32_16x16x32_bf16 v[212:215], v[148:151], v[132:135], v[212:215]\n"
    "v_mfma_f32_16x16x32_bf16 v[212:215], v[180:183], v[164:167], v[212:215]\n"
    "v_mfma_f32_16x16x32_bf16 v[208:211], v[180:183], v[160:163], v[208:211]\n"
    "v_mfma_f32_16x16x32_bf16 v[208:211], v[148:151], v[128:131], v[208:211]\n"
    "v_add_u32_dpp v52, v98, v48 quad_perm:[0,0,0,0] row_mask:0xf bank_mask:0xf\n"
    "v_mfma_f32_16x16x32_bf16 v[224:227], v[152:155], v[128:131], v[224:227]\n"
    "v_mfma_f32_16x16x32_bf16 v[224:227], v[184:187], v[160:163], v[224:227]\n"
    "v_mfma_f32_16x16x32_bf16 v[228:231], v[184:187], v[164:167], v[228:231]\n"
    "v_mfma_f32_16x16x32_bf16 v[228:231], v[152:155], v[132:135], v[228:231]\n"
    "v_add_u32_dpp v53, v106, v49 quad_perm:[0,0,0,0] row_mask:0xf bank_mask:0xf\n"
    "v_mfma_f32_16x16x32_bf16 v[232:235], v[152:155], v[136:139], v[232:235]\n"
    "v_mfma_f32_16x16x32_bf16 v[232:235], v[184:187], v[168:171], v[232:235]\n"
    "v_mfma_f32_16x16x32_bf16 v[236:239], v[184:187], v[172:175], v[236:239]\n"
    "v_add_u32_dpp v54, v114, v50 quad_perm:[0,0,0,0] row_mask:0xf bank_mask:0xf\n"
    "v_mfma_f32_16x16x32_bf16 v[236:239], v[152:155], v[140:143], v[236:239]\n"
    "v_mfma_f32_16x16x32_bf16 v[252:255], v[156:159], v[140:143], v[252:255]\n"
    "v_mfma_f32_16x16x32_bf16 v[252:255], v[188:191], v[172:175], v[252:255]\n"
    "v_mfma_f32_16x16x32_bf16 v[248:251], v[188:191], v[168:171], v[248:251]\n"
    "v_add_u32_dpp v55, v122, v51 quad_perm:[0,0,0,0] row_mask:0xf bank_mask:0xf\n"
    "v_mfma_f32_16x16x32_bf16 v[248:251], v[156:159], v[136:139], v[248:251]\n"
    "v_mfma_f32_16x16x32_bf16 v[244:247], v[156:159], v[132:135], v[244:247]\n"
    "v_mfma_f32_16x16x32_bf16 v[244:247], v[188:191], v[164:167], v[244:247]\n"
    "v_mfma_f32_16x16x32_bf16 v[240:243], v[188:191], v[160:163], v[240:243]\n"
    "v_mfma_f32_16x16x32_bf16 v[240:243], v[156:159], v[128:131], v[240:243]\n"
    "s_waitcnt vmcnt(8)\n"
    "s_barrier\n"
    "s_add_u32 m0, s35, 0\n"
    "s_nop 0\n"
    "global_load_lds_dwordx4 v46, s[30:31] offset:0\n"
    "ds_read_b128 v[128:131], v56 offset:49152\n"
    "ds_read_b128 v[132:135], v56 offset:51200\n"
    "ds_read_b128 v[136:139], v56 offset:53248\n"
    "global_load_lds_dwordx4 v47, s[30:31] offset:1024\n"
    "s_add_u32 s30, s30, 212992\n"
    "s_addc_u32 s31, s31, 0\n"
    "ds_read_b128 v[140:143], v56 offset:55296\n"
    "ds_read_b64_tr_b16 v[144:145], v76 offset:49152\n"
    "ds_read_b64_tr_b16 v[146:147], v76 offset:50176\n"
    "s_add_u32 m0, s34, 0\n"
    "s_nop 0\n"
    "global_load_lds_dwordx4 v52, s[36:37] offset:0\n"
    "ds_read_b64_tr_b16 v[148:149], v77 offset:49152\n"
    "ds_read_b64_tr_b16 v[150:151], v77 offset:50176\n"
    "ds_read_b64_tr_b16 v[152:153], v78 offset:49152\n"
    "global_load_lds_dwordx4 v53, s[36:37] offset:1024\n"
    "ds_read_b64_tr_b16 v[154:155], v78 offset:50176\n"
    "ds_read_b64_tr_b16 v[156:157], v79 offset:49152\n"
    "ds_read_b64_tr_b16 v[158:159], v79 offset:50176\n"
    "global_load_lds_dwordx4 v54, s[36:37] offset:2048\n"
    "ds_read_b128 v[160:163], v57 offset:49152\n"
    "ds_read_b128 v[164:167], v57 offset:51200\n"
    "ds_read_b128 v[168:171], v57 offset:53248\n"
    "global_load_lds_dwordx4 v55, s[36:37] offset:3072\n"
    "ds_read_b128 v[172:175], v57 offset:55296\n"
    "ds_read_b64_tr_b16 v[176:177], v76 offset:57344\n"
    "ds_read_b64_tr_b16 v[178:179], v76 offset:58368\n"
    "global_load_dwordx4 v[116:119], v45, s[38:39] offset:1664\n"
    "ds_read_b64_tr_b16 v[180:181], v77 offset:57344\n"
    "ds_read_b64_tr_b16 v[182:183], v77 offset:58368\n"
    "ds_read_b64_tr_b16 v[184:185], v78 offset:57344\n"
    "ds_read_b64_tr_b16 v[186:187], v78 offset:58368\n"
    "ds_read_b64_tr_b16 v[188:189], v79 offset:57344\n"
    "ds_read_b64_tr_b16 v[190:191], v79 offset:58368\n"
    "s_waitcnt vmcnt(12) lgkmcnt(0)\n"
    "s_barrier\n"
    "v_mfma_f32_16x16x32_bf16 v[192:195], v[144:147], v[128:131], v[192:195]\n"
    "v_mfma_f32_16x16x32_bf16 v[192:195], v[176:179], v[160:163], v[192:195]\n"
    "v_lshlrev_b32 v99, 7, v99\n"
    "v_mfma_f32_16x16x32_bf16 v[196:199], v[176:179], v[164:167], v[196:199]\n"
    "v_mfma_f32_16x16x32_bf16 v[196:199], v[144:147], v[132:135], v[196:199]\n"
    "v_mfma_f32_16x16x32_bf16 v[200:203], v[144:147], v[136:139], v[200:203]\n"
    "v_lshlrev_b32 v107, 7, v107\n"
    "v_mfma_f32_16x16x32_bf16 v[200:203], v[176:179], v[168:171], v[200:203]\n"
    "v_mfma_f32_16x16x32_bf16 v[204:207], v[176:179], v[172:175], v[204:207]\n"
    "v_mfma_f32_16x16x32_bf16 v[204:207], v[144:147], v[140:143], v[204:207]\n"
    "v_lshlrev_b32 v115, 7, v115\n"
    "v_mfma_f32_16x16x32_bf16 v[220:223], v[148:151], v[140:143], v[220:223]\n"
    "v_mfma_f32_16x16x32_bf16 v[220:223], v[180:183], v[172:175], v[220:223]\n"
    "v_mfma_f32_16x16x32_bf16 v[216:219], v[180:183], v[168:171], v[216:219]\n"
    "v_lshlrev_b32 v123, 7, v123\n"
    "v_mfma_f32_16x16x32_bf16 v[216:219], v[148:151], v[136:139], v[216:219]\n"
    "v_mfma_f32_16x16x32_bf16 v[212:215], v[148:151], v[132:135], v[212:215]\n"
    "v_mfma_f32_16x16x32_bf16 v[212:215], v[180:183], v[164:167], v[212:215]\n"
    "s_lshl_b32 s40, s2, 2\n"
    "s_load_dword s25, s[10:11], s40\n"
    "s_load_dword s26, s[16:17], 0\n"
    "v_mfma_f32_16x16x32_bf16 v[208:211], v[180:183], v[160:163], v[208:211]\n"
    "v_mfma_f32_16x16x32_bf16 v[208:211], v[148:151], v[128:131], v[208:211]\n"
    "v_mfma_f32_16x16x32_bf16 v[224:227], v[152:155], v[128:131], v[224:227]\n"
    "v_mfma_f32_16x16x32_bf16 v[224:227], v[184:187], v[160:163], v[224:227]\n"
    "v_add_u32_dpp v52, v99, v48 quad_perm:[0,0,0,0] row_mask:0xf bank_mask:0xf\n"
    "v_mfma_f32_16x16x32_bf16 v[228:231], v[184:187], v[164:167], v[228:231]\n"
    "v_mfma_f32_16x16x32_bf16 v[228:231], v[152:155], v[132:135], v[228:231]\n"
    "v_mfma_f32_16x16x32_bf16 v[232:235], v[152:155], v[136:139], v[232:235]\n"
    "v_add_u32_dpp v53, v107, v49 quad_perm:[0,0,0,0] row_mask:0xf bank_mask:0xf\n"
    "v_mfma_f32_16x16x32_bf16 v[232:235], v[184:187], v[168:171], v[232:235]\n"
    "v_mfma_f32_16x16x32_bf16 v[236:239], v[184:187], v[172:175], v[236:239]\n"
    "v_mfma_f32_16x16x32_bf16 v[236:239], v[152:155], v[140:143], v[236:239]\n"
    "v_add_u32_dpp v54, v115, v50 quad_perm:[0,0,0,0] row_mask:0xf bank_mask:0xf\n"
    "v_mfma_f32_16x16x32_bf16 v[252:255], v[156:159], v[140:143], v[252:255]\n"
    "v_mfma_f32_16x16x32_bf16 v[252:255], v[188:191], v[172:175], v[252:255]\n"
    "v_mfma_f32_16x16x32_bf16 v[248:251], v[188:191], v[168:171], v[248:251]\n"
    "v_add_u32_dpp v55, v123, v51 quad_perm:[0,0,0,0] row_mask:0xf bank_mask:0xf\n"
    "v_mfma_f32_16x16x32_bf16 v[248:251], v[156:159], v[136:139], v[248:251]\n"
    "v_mfma_f32_16x16x32_bf16 v[244:247], v[156:159], v[132:135], v[244:247]\n"
    "v_mfma_f32_16x16x32_bf16 v[244:247], v[188:191], v[164:167], v[244:247]\n"
    "v_mfma_f32_16x16x32_bf16 v[240:243], v[188:191], v[160:163], v[240:243]\n"
    "v_mfma_f32_16x16x32_bf16 v[240:243], v[156:159], v[128:131], v[240:243]\n"
    "s_waitcnt vmcnt(8)\n"
    "s_barrier\n"
    "s_add_u32 m0, s35, 49152\n"
    "s_nop 0\n"
    "global_load_lds_dwordx4 v46, s[30:31] offset:0\n"
    "ds_read_b128 v[128:131], v58 offset:0\n"
    "ds_read_b128 v[132:135], v58 offset:2048\n"
    "ds_read_b128 v[136:139], v58 offset:4096\n"
    "global_load_lds_dwordx4 v47, s[30:31] offset:1024\n"
    "s_bfe_u32 s66, s28, 0x80008\n"
    "s_mul_i32 s66, s66, 53248\n"
    "s_add_u32 s30, s6, s66\n"
    "s_addc_u32 s31, s7, 0\n"
    "ds_read_b128 v[140:143], v58 offset:6144\n"
    "ds_read_b64_tr_b16 v[144:145], v80 offset:0\n"
    "ds_read_b64_tr_b16 v[146:147], v80 offset:1024\n"
    "s_add_u32 m0, s34, 49152\n"
    "s_nop 0\n"
    "global_load_lds_dwordx4 v52, s[36:37] offset:0\n"
    "ds_read_b64_tr_b16 v[148:149], v81 offset:0\n"
    "ds_read_b64_tr_b16 v[150:151], v81 offset:1024\n"
    "ds_read_b64_tr_b16 v[152:153], v82 offset:0\n"
    "global_load_lds_dwordx4 v53, s[36:37] offset:1024\n"
    "ds_read_b64_tr_b16 v[154:155], v82 offset:1024\n"
    "ds_read_b64_tr_b16 v[156:157], v83 offset:0\n"
    "ds_read_b64_tr_b16 v[158:159], v83 offset:1024\n"
    "global_load_lds_dwordx4 v54, s[36:37] offset:2048\n"
    "ds_read_b128 v[160:163], v59 offset:0\n"
    "ds_read_b128 v[164:167], v59 offset:2048\n"
    "ds_read_b128 v[168:171], v59 offset:4096\n"
    "global_load_lds_dwordx4 v55, s[36:37] offset:3072\n"
    "ds_read_b128 v[172:175], v59 offset:6144\n"
    "ds_read_b64_tr_b16 v[176:177], v80 offset:8192\n"
    "ds_read_b64_tr_b16 v[178:179], v80 offset:9216\n"
    "global_load_dwordx4 v[124:127], v45, s[38:39] offset:2496\n"
    "ds_read_b64_tr_b16 v[180:181], v81 offset:8192\n"
    "ds_read_b64_tr_b16 v[182:183], v81 offset:9216\n"
    "ds_read_b64_tr_b16 v[184:185], v82 offset:8192\n"
    "ds_read_b64_tr_b16 v[186:187], v82 offset:9216\n"
    "ds_read_b64_tr_b16 v[188:189], v83 offset:8192\n"
    "ds_read_b64_tr_b16 v[190:191], v83 offset:9216\n"
    "s_waitcnt vmcnt(12) lgkmcnt(0)\n"
    "s_barrier\n"
    "v_mfma_f32_16x16x32_bf16 v[192:195], v[144:147], v[128:131], v[192:195]\n"
    "v_mfma_f32_16x16x32_bf16 v[192:195], v[176:179], v[160:163], v[192:195]\n"
    "v_add_u32_dpp v52, v96, v48 quad_perm:[1,1,1,1] row_mask:0xf bank_mask:0xf\n"
    "v_mfma_f32_16x16x32_bf16 v[196:199], v[176:179], v[164:167], v[196:199]\n"
    "v_add_u32_dpp v53, v104, v49 quad_perm:[1,1,1,1] row_mask:0xf bank_mask:0xf\n"
    "v_mfma_f32_16x16x32_bf16 v[196:199], v[144:147], v[132:135], v[196:199]\n"
    "v_mfma_f32_16x16x32_bf16 v[200:203], v[144:147], v[136:139], v[200:203]\n"
    "v_add_u32_dpp v54, v112, v50 quad_perm:[1,1,1,1] row_mask:0xf bank_mask:0xf\n"
    "v_mfma_f32_16x16x32_bf16 v[200:203], v[176:179], v[168:171], v[200:203]\n"
    "v_add_u32_dpp v55, v120, v51 quad_perm:[1,1,1,1] row_mask:0xf bank_mask:0xf\n"
    "v_mfma_f32_16x16x32_bf16 v[204:207], v[176:179], v[172:175], v[204:207]\n"
    "v_mfma_f32_16x16x32_bf16 v[204:207], v[144:147], v[140:143], v[204:207]\n"
    "s_cmp_lt_u32 s20, 4\n"
    "s_cselect_b32 s40, -1, 0\n"
    "v_mfma_f32_16x16x32_bf16 v[220:223], v[148:151], v[140:143], v[220:223]\n"
    "v_mfma_f32_16x16x32_bf16 v[220:223], v[180:183], v[172:175], v[220:223]\n"
    "v_lshlrev_b32 v13, 2, v0\n"
    "v_mfma_f32_16x16x32_bf16 v[216:219], v[180:183], v[168:171], v[216:219]\n"
    "v_mov_b32 v12, s40\n"
    "v_mfma_f32_16x16x32_bf16 v[216:219], v[148:151], v[136:139], v[216:219]\n"
    "v_mfma_f32_16x16x32_bf16 v[212:215], v[148:151], v[132:135], v[212:215]\n"
    "v_add_u32 v13, 149504, v13\n"
    "v_mfma_f32_16x16x32_bf16 v[212:215], v[180:183], v[164:167], v[212:215]\n"
    "ds_write_b32 v13, v12\n"
    "v_mfma_f32_16x16x32_bf16 v[208:211], v[180:183], v[160:163], v[208:211]\n"
    "v_mfma_f32_16x16x32_bf16 v[208:211], v[148:151], v[128:131], v[208:211]\n"
    "s_mul_i32 s47, s3, 5042\n"
    "v_mfma_f32_16x16x32_bf16 v[224:227], v[152:155], v[128:131], v[224:227]\n"
    "v_mfma_f32_16x16x32_bf16 v[224:227], v[184:187], v[160:163], v[224:227]\n"
    "s_lshr_b32 s47, s47, 16\n"
    "v_mfma_f32_16x16x32_bf16 v[228:231], v[184:187], v[164:167], v[228:231]\n"
    "s_lshr_b32 s21, s20, 2\n"
    "v_mfma_f32_16x16x32_bf16 v[228:231], v[152:155], v[132:135], v[228:231]\n"
    "v_mfma_f32_16x16x32_bf16 v[232:235], v[152:155], v[136:139], v[232:235]\n"
    "s_lshl1_add_u32 s22, s21, s20\n"
    "v_mfma_f32_16x16x32_bf16 v[232:235], v[184:187], v[168:171], v[232:235]\n"
    "s_and_b32 s22, s22, 3\n"
    "v_mfma_f32_16x16x32_bf16 v[236:239], v[184:187], v[172:175], v[236:239]\n"
    "v_mfma_f32_16x16x32_bf16 v[236:239], v[152:155], v[140:143], v[236:239]\n"
    "s_mul_i32 s43, s21, 49152\n"
    "v_mfma_f32_16x16x32_bf16 v[252:255], v[156:159], v[140:143], v[252:255]\n"
    "v_mfma_f32_16x16x32_bf16 v[252:255], v[188:191], v[172:175], v[252:255]\n"
    "s_lshl_b32 s58, s21, 8\n"
    "v_mfma_f32_16x16x32_bf16 v[248:251], v[188:191], v[168:171], v[248:251]\n"
    "s_add_u32 s58, s58, 150528\n"
    "v_mfma_f32_16x16x32_bf16 v[248:251], v[156:159], v[136:139], v[248:251]\n"
    "v_mfma_f32_16x16x32_bf16 v[244:247], v[156:159], v[132:135], v[244:247]\n"
    "s_lshl_b32 s54, s47, 9\n"
    "v_mfma_f32_16x16x32_bf16 v[244:247], v[188:191], v[164:167], v[244:247]\n"
    "v_mfma_f32_16x16x32_bf16 v[240:243], v[188:191], v[160:163], v[240:243]\n"
    "v_mfma_f32_16x16x32_bf16 v[240:243], v[156:159], v[128:131], v[240:243]\n"
    "s_waitcnt vmcnt(8)\n"
    "s_barrier\n"
    "s_add_u32 m0, s35, 98304\n"
    "s_nop 0\n"
    "global_load_lds_dwordx4 v46, s[30:31] offset:0\n"
    "ds_read_b128 v[128:131], v56 offset:0\n"
    "ds_read_b128 v[132:135], v56 offset:2048\n"
    "ds_read_b128 v[136:139], v56 offset:4096\n"
    "global_load_lds_dwordx4 v47, s[30:31] offset:1024\n"
    "s_add_u32 s30, s30, 212992\n"
    "s_addc_u32 s31, s31, 0\n"
    "ds_read_b128 v[140:143], v56 offset:6144\n"
    "ds_read_b64_tr_b16 v[144:145], v76 offset:0\n"
    "ds_read_b64_tr_b16 v[146:147], v76 offset:1024\n"
    "s_add_u32 m0, s34, 98304\n"
    "s_nop 0\n"
    "global_load_lds_dwordx4 v52, s[36:37] offset:0\n"
    "ds_read_b64_tr_b16 v[148:149], v77 offset:0\n"
    "ds_read_b64_tr_b16 v[150:151], v77 offset:1024\n"
    "ds_read_b64_tr_b16 v[152:153], v78 offset:0\n"
    "global_load_lds_dwordx4 v53, s[36:37] offset:1024\n"
    "ds_read_b64_tr_b16 v[154:155], v78 offset:1024\n"
    "ds_read_b64_tr_b16 v[156:157], v79 offset:0\n"
    "ds_read_b64_tr_b16 v[158:159], v79 offset:1024\n"
    "global_load_lds_dwordx4 v54, s[36:37] offset:2048\n"
    "ds_read_b128 v[160:163], v57 offset:0\n"
    "ds_read_b128 v[164:167], v57 offset:2048\n"
    "ds_read_b128 v[168:171], v57 offset:4096\n"
    "global_load_lds_dwordx4 v55, s[36:37] offset:3072\n"
    "ds_read_b128 v[172:175], v57 offset:6144\n"
    "ds_read_b64_tr_b16 v[176:177], v76 offset:8192\n"
    "ds_read_b64_tr_b16 v[178:179], v76 offset:9216\n"
    "s_mov_b32 m0, s64\n"
    "s_nop 0\n"
    "global_load_lds_dword v43, s[14:15]\n"
    "ds_read_b64_tr_b16 v[180:181], v77 offset:8192\n"
    "ds_read_b64_tr_b16 v[182:183], v77 offset:9216\n"
    "ds_read_b64_tr_b16 v[184:185], v78 offset:8192\n"
    "ds_read_b64_tr_b16 v[186:187], v78 offset:9216\n"
    "ds_read_b64_tr_b16 v[188:189], v79 offset:8192\n"
    "ds_read_b64_tr_b16 v[190:191], v79 offset:9216\n"
    "s_waitcnt vmcnt(12) lgkmcnt(0)\n"
    "s_barrier\n"
    "v_mfma_f32_16x16x32_bf16 v[192:195], v[144:147], v[128:131], v[192:195]\n"
    "v_mfma_f32_16x16x32_bf16 v[192:195], v[176:179], v[160:163], v[192:195]\n"
    "v_add_u32_dpp v52, v97, v48 quad_perm:[1,1,1,1] row_mask:0xf bank_mask:0xf\n"
    "v_mfma_f32_16x16x32_bf16 v[196:199], v[176:179], v[164:167], v[196:199]\n"
    "v_add_u32_dpp v53, v105, v49 quad_perm:[1,1,1,1] row_mask:0xf bank_mask:0xf\n"
    "v_mfma_f32_16x16x32_bf16 v[196:199], v[144:147], v[132:135], v[196:199]\n"
    "v_mfma_f32_16x16x32_bf16 v[200:203], v[144:147], v[136:139], v[200:203]\n"
    "v_add_u32_dpp v54, v113, v50 quad_perm:[1,1,1,1] row_mask:0xf bank_mask:0xf\n"
    "v_mfma_f32_16x16x32_bf16 v[200:203], v[176:179], v[168:171], v[200:203]\n"
    "v_add_u32_dpp v55, v121, v51 quad_perm:[1,1,1,1] row_mask:0xf bank_mask:0xf\n"
    "v_mfma_f32_16x16x32_bf16 v[204:207], v[176:179], v[172:175], v[204:207]\n"
    "v_mfma_f32_16x16x32_bf16 v[204:207], v[144:147], v[140:143], v[204:207]\n"
    "s_add_u32 s54, s54, 2\n"
    "v_mfma_f32_16x16x32_bf16 v[220:223], v[148:151], v[140:143], v[220:223]\n"
    "v_mfma_f32_16x16x32_bf16 v[220:223], v[180:183], v[172:175], v[220:223]\n"
    "s_lshl_b32 s40, s47, 7\n"
    "v_mfma_f32_16x16x32_bf16 v[216:219], v[180:183], v[168:171], v[216:219]\n"
    "s_add_u32 s40, s40, 32768\n"
    "v_mfma_f32_16x16x32_bf16 v[216:219], v[148:151], v[136:139], v[216:219]\n"
    "v_mfma_f32_16x16x32_bf16 v[212:215], v[148:151], v[132:135], v[212:215]\n"
    "s_add_u32 s52, s10, s40\n"
    "s_addc_u32 s53, s11, 0\n"
    "v_mfma_f32_16x16x32_bf16 v[212:215], v[180:183], v[164:167], v[212:215]\n"
    "s_mov_b32 s57, 0x7fff7fff\n"
    "v_mfma_f32_16x16x32_bf16 v[208:211], v[180:183], v[160:163], v[208:211]\n"
    "v_mfma_f32_16x16x32_bf16 v[208:211], v[148:151], v[128:131], v[208:211]\n"
    "s_mov_b32 s56, 0\n"
    "v_mfma_f32_16x16x32_bf16 v[224:227], v[152:155], v[128:131], v[224:227]\n"
    "v_mfma_f32_16x16x32_bf16 v[224:227], v[184:187], v[160:163], v[224:227]\n"
    "s_mov_b32 s27, 0\n"
    "v_mfma_f32_16x16x32_bf16 v[228:231], v[184:187], v[164:167], v[228:231]\n"
    "s_mov_b32 s55, 9\n"
    "v_mfma_f32_16x16x32_bf16 v[228:231], v[152:155], v[132:135], v[228:231]\n"
    "v_mfma_f32_16x16x32_bf16 v[232:235], v[152:155], v[136:139], v[232:235]\n"
    "s_mov_b32 s44, 0\n"
    "v_mfma_f32_16x16x32_bf16 v[232:235], v[184:187], v[168:171], v[232:235]\n"
    "s_movk_i32 s46, 17920\n"
    "v_mfma_f32_16x16x32_bf16 v[236:239], v[184:187], v[172:175], v[236:239]\n"
    "v_mfma_f32_16x16x32_bf16 v[236:239], v[152:155], v[140:143], v[236:239]\n"
    "v_lshrrev_b32 v12, 4, v1\n"
    "v_mfma_f32_16x16x32_bf16 v[252:255], v[156:159], v[140:143], v[252:255]\n"
    "v_mfma_f32_16x16x32_bf16 v[252:255], v[188:191], v[172:175], v[252:255]\n"
    "v_bfe_u32 v13, v1, 2, 2\n"
    "v_mfma_f32_16x16x32_bf16 v[248:251], v[188:191], v[168:171], v[248:251]\n"
    "v_and_b32 v21, 3, v1\n"
    "v_mfma_f32_16x16x32_bf16 v[248:251], v[156:159], v[136:139], v[248:251]\n"
    "v_mfma_f32_16x16x32_bf16 v[244:247], v[156:159], v[132:135], v[244:247]\n"
    "s_lshl_b32 s40, s22, 5\n"
    "v_mfma_f32_16x16x32_bf16 v[244:247], v[188:191], v[164:167], v[244:247]\n"
    "v_mfma_f32_16x16x32_bf16 v[240:243], v[188:191], v[160:163], v[240:243]\n"
    "v_mfma_f32_16x16x32_bf16 v[240:243], v[156:159], v[128:131], v[240:243]\n"
    "s_waitcnt vmcnt(8)\n"
    "s_barrier\n"
    "s_add_u32 m0, s35, 0\n"
    "s_nop 0\n"
    "global_load_lds_dwordx4 v46, s[30:31] offset:0\n"
    "ds_read_b128 v[128:131], v56 offset:49152\n"
    "ds_read_b128 v[132:135], v56 offset:51200\n"
    "ds_read_b128 v[136:139], v56 offset:53248\n"
    "ds_read_b128 v[140:143], v56 offset:55296\n"
    "global_load_lds_dwordx4 v47, s[30:31] offset:1024\n"
    "s_add_u32 s30, s30, 212992\n"
    "s_addc_u32 s31, s31, 0\n"
    "ds_read_b64_tr_b16 v[144:145], v76 offset:49152\n"
    "ds_read_b64_tr_b16 v[146:147], v76 offset:50176\n"
    "ds_read_b64_tr_b16 v[148:149], v77 offset:49152\n"
    "ds_read_b64_tr_b16 v[150:151], v77 offset:50176\n"
    "s_add_u32 m0, s34, 0\n"
    "s_nop 0\n"
    "global_load_lds_dwordx4 v52, s[36:37] offset:0\n"
    "ds_read_b64_tr_b16 v[152:153], v78 offset:49152\n"
    "ds_read_b64_tr_b16 v[154:155], v78 offset:50176\n"
    "ds_read_b64_tr_b16 v[156:157], v79 offset:49152\n"
    "ds_read_b64_tr_b16 v[158:159], v79 offset:50176\n"
    "global_load_lds_dwordx4 v53, s[36:37] offset:1024\n"
    "ds_read_b128 v[160:163], v57 offset:49152\n"
    "ds_read_b128 v[164:167], v57 offset:51200\n"
    "ds_read_b128 v[168:171], v57 offset:53248\n"
    "ds_read_b128 v[172:175], v57 offset:55296\n"
    "global_load_lds_dwordx4 v54, s[36:37] offset:2048\n"
    "ds_read_b64_tr_b16 v[176:177], v76 offset:57344\n"
    "ds_read_b64_tr_b16 v[178:179], v76 offset:58368\n"
    "ds_read_b64_tr_b16 v[180:181], v77 offset:57344\n"
    "ds_read_b64_tr_b16 v[182:183], v77 offset:58368\n"
    "global_load_lds_dwordx4 v55, s[36:37] offset:3072\n"
    "ds_read_b64_tr_b16 v[184:185], v78 offset:57344\n"
    "ds_read_b64_tr_b16 v[186:187], v78 offset:58368\n"
    "ds_read_b64_tr_b16 v[188:189], v79 offset:57344\n"
    "ds_read_b64_tr_b16 v[190:191], v79 offset:58368\n"
    "s_waitcnt vmcnt(11) lgkmcnt(0)\n"
    "s_barrier\n"
    "v_mfma_f32_16x16x32_bf16 v[192:195], v[144:147], v[128:131], v[192:195]\n"
    "v_mfma_f32_16x16x32_bf16 v[192:195], v[176:179], v[160:163], v[192:195]\n"
    "v_add_u32_dpp v52, v98, v48 quad_perm:[1,1,1,1] row_mask:0xf bank_mask:0xf\n"
    "v_mfma_f32_16x16x32_bf16 v[196:199], v[176:179], v[164:167], v[196:199]\n"
    "v_add_u32_dpp v53, v106, v49 quad_perm:[1,1,1,1] row_mask:0xf bank_mask:0xf\n"
    "v_mfma_f32_16x16x32_bf16 v[196:199], v[144:147], v[132:135], v[196:199]\n"
    "v_mfma_f32_16x16x32_bf16 v[200:203], v[144:147], v[136:139], v[200:203]\n"
    "v_add_u32_dpp v54, v114, v50 quad_perm:[1,1,1,1] row_mask:0xf bank_mask:0xf\n"
    "v_mfma_f32_16x16x32_bf16 v[200:203], v[176:179], v[168:171], v[200:203]\n"
    "v_add_u32_dpp v55, v122, v51 quad_perm:[1,1,1,1] row_mask:0xf bank_mask:0xf\n"
    "v_mfma_f32_16x16x32_bf16 v[204:207], v[176:179], v[172:175], v[204:207]\n"
    "v_mfma_f32_16x16x32_bf16 v[204:207], v[144:147], v[140:143], v[204:207]\n"
    "s_add_u32 s40, s40, s43\n"
    "v_mfma_f32_16x16x32_bf16 v[220:223], v[148:151], v[140:143], v[220:223]\n"
    "v_mfma_f32_16x16x32_bf16 v[220:223], v[180:183], v[172:175], v[220:223]\n"
    "v_lshl_add_u32 v21, v21, 3, s40\n"
    "v_mfma_f32_16x16x32_bf16 v[216:219], v[180:183], v[168:171], v[216:219]\n"
    "s_movk_i32 s41, 160\n"
    "v_mfma_f32_16x16x32_bf16 v[216:219], v[148:151], v[136:139], v[216:219]\n"
    "v_mfma_f32_16x16x32_bf16 v[212:215], v[148:151], v[132:135], v[212:215]\n"
    "v_add_u32 v22, v12, v13\n"
    "v_mfma_f32_16x16x32_bf16 v[212:215], v[180:183], v[164:167], v[212:215]\n"
    "v_and_b32 v22, 3, v22\n"
    "v_mfma_f32_16x16x32_bf16 v[208:211], v[180:183], v[160:163], v[208:211]\n"
    "v_mfma_f32_16x16x32_bf16 v[208:211], v[148:151], v[128:131], v[208:211]\n"
    "v_mad_u32_u24 v4, v22, s41, v21\n"
    "v_mfma_f32_16x16x32_bf16 v[224:227], v[152:155], v[128:131], v[224:227]\n"
    "v_mfma_f32_16x16x32_bf16 v[224:227], v[184:187], v[160:163], v[224:227]\n"
    "v_cmp_gt_u32 vcc, 2, v13\n"
    "v_mfma_f32_16x16x32_bf16 v[228:231], v[184:187], v[164:167], v[228:231]\n"
    "v_add_u32 v24, 22, v13\n"
    "v_mfma_f32_16x16x32_bf16 v[228:231], v[152:155], v[132:135], v[228:231]\n"
    "v_mfma_f32_16x16x32_bf16 v[232:235], v[152:155], v[136:139], v[232:235]\n"
    "v_lshlrev_b32 v13, 3, v13\n"
    "v_mfma_f32_16x16x32_bf16 v[232:235], v[184:187], v[168:171], v[232:235]\n"
    "v_cndmask_b32 v22, v24, v22, vcc\n"
    "v_mfma_f32_16x16x32_bf16 v[236:239], v[184:187], v[172:175], v[236:239]\n"
    "v_mfma_f32_16x16x32_bf16 v[236:239], v[152:155], v[140:143], v[236:239]\n"
    "v_mad_u32_u24 v3, v22, s41, v21\n"
    "v_mfma_f32_16x16x32_bf16 v[252:255], v[156:159], v[140:143], v[252:255]\n"
    "v_mfma_f32_16x16x32_bf16 v[252:255], v[188:191], v[172:175], v[252:255]\n"
    "v_cmp_eq_u32 vcc, 3, v12\n"
    "v_mfma_f32_16x16x32_bf16 v[248:251], v[188:191], v[168:171], v[248:251]\n"
    "v_xor_b32 v22, 0, v12\n"
    "v_mfma_f32_16x16x32_bf16 v[248:251], v[156:159], v[136:139], v[248:251]\n"
    "v_mfma_f32_16x16x32_bf16 v[244:247], v[156:159], v[132:135], v[244:247]\n"
    "v_and_b32 v22, 1, v22\n"
    "v_mfma_f32_16x16x32_bf16 v[244:247], v[188:191], v[164:167], v[244:247]\n"
    "v_mfma_f32_16x16x32_bf16 v[240:243], v[188:191], v[160:163], v[240:243]\n"
    "v_mfma_f32_16x16x32_bf16 v[240:243], v[156:159], v[128:131], v[240:243]\n"
    "s_waitcnt vmcnt(7)\n"
    "s_barrier\n"
    "s_add_u32 m0, s35, 49152\n"
    "s_nop 0\n"
    "global_load_lds_dwordx4 v46, s[30:31] offset:0\n"
    "ds_read_b128 v[128:131], v58 offset:0\n"
    "ds_read_b128 v[132:135], v58 offset:2048\n"
    "ds_read_b128 v[136:139], v58 offset:4096\n"
    "ds_read_b128 v[140:143], v58 offset:6144\n"
    "global_load_lds_dwordx4 v47, s[30:31] offset:1024\n"
    "s_add_u32 s30, s30, 212992\n"
    "s_addc_u32 s31, s31, 0\n"
    "ds_read_b64_tr_b16 v[144:145], v80 offset:0\n"
    "ds_read_b64_tr_b16 v[146:147], v80 offset:1024\n"
    "ds_read_b64_tr_b16 v[148:149], v81 offset:0\n"
    "ds_read_b64_tr_b16 v[150:151], v81 offset:1024\n"
    "s_add_u32 m0, s34, 49152\n"
    "s_nop 0\n"
    "global_load_lds_dwordx4 v52, s[36:37] offset:0\n"
    "ds_read_b64_tr_b16 v[152:153], v82 offset:0\n"
    "ds_read_b64_tr_b16 v[154:155], v82 offset:1024\n"
    "ds_read_b64_tr_b16 v[156:157], v83 offset:0\n"
    "ds_read_b64_tr_b16 v[158:159], v83 offset:1024\n"
    "global_load_lds_dwordx4 v53, s[36:37] offset:1024\n"
    "ds_read_b128 v[160:163], v59 offset:0\n"
    "ds_read_b128 v[164:167], v59 offset:2048\n"
    "ds_read_b128 v[168:171], v59 offset:4096\n"
    "ds_read_b128 v[172:175], v59 offset:6144\n"
    "global_load_lds_dwordx4 v54, s[36:37] offset:2048\n"
    "ds_read_b64_tr_b16 v[176:177], v80 offset:8192\n"
    "ds_read_b64_tr_b16 v[178:179], v80 offset:9216\n"
    "ds_read_b64_tr_b16 v[180:181], v81 offset:8192\n"
    "ds_read_b64_tr_b16 v[182:183], v81 offset:9216\n"
    "global_load_lds_dwordx4 v55, s[36:37] offset:3072\n"
    "ds_read_b64_tr_b16 v[184:185], v82 offset:8192\n"
    "ds_read_b64_tr_b16 v[186:187], v82 offset:9216\n"
    "ds_read_b64_tr_b16 v[188:189], v83 offset:8192\n"
    "ds_read_b64_tr_b16 v[190:191], v83 offset:9216\n"
    "s_waitcnt vmcnt(10) lgkmcnt(0)\n"
    "s_barrier\n"
    "v_mfma_f32_16x16x32_bf16 v[192:195], v[144:147], v[128:131], v[192:195]\n"
    "v_mfma_f32_16x16x32_bf16 v[192:195], v[176:179], v[160:163], v[192:195]\n"
    "v_add_u32_dpp v52, v99, v48 quad_perm:[1,1,1,1] row_mask:0xf bank_mask:0xf\n"
    "v_mfma_f32_16x16x32_bf16 v[196:199], v[176:179], v[164:167], v[196:199]\n"
    "v_add_u32_dpp v53, v107, v49 quad_perm:[1,1,1,1] row_mask:0xf bank_mask:0xf\n"
    "v_mfma_f32_16x16x32_bf16 v[196:199], v[144:147], v[132:135], v[196:199]\n"
    "v_mfma_f32_16x16x32_bf16 v[200:203], v[144:147], v[136:139], v[200:203]\n"
    "v_add_u32_dpp v54, v115, v50 quad_perm:[1,1,1,1] row_mask:0xf bank_mask:0xf\n"
    "v_mfma_f32_16x16x32_bf16 v[200:203], v[176:179], v[168:171], v[200:203]\n"
    "v_add_u32_dpp v55, v123, v51 quad_perm:[1,1,1,1] row_mask:0xf bank_mask:0xf\n"
    "v_mfma_f32_16x16x32_bf16 v[204:207], v[176:179], v[172:175], v[204:207]\n"
    "v_mfma_f32_16x16x32_bf16 v[204:207], v[144:147], v[140:143], v[204:207]\n"
    "v_lshlrev_b32 v22, 1, v22\n"
    "v_mfma_f32_16x16x32_bf16 v[220:223], v[148:151], v[140:143], v[220:223]\n"
    "v_mfma_f32_16x16x32_bf16 v[220:223], v[180:183], v[172:175], v[220:223]\n"
    "v_lshl_add_u32 v22, v12, 3, v22\n"
    "v_mfma_f32_16x16x32_bf16 v[216:219], v[180:183], v[168:171], v[216:219]\n"
    "s_mov_b32 s40, 0x05040100\n"
    "v_mfma_f32_16x16x32_bf16 v[216:219], v[148:151], v[136:139], v[216:219]\n"
    "v_mfma_f32_16x16x32_bf16 v[212:215], v[148:151], v[132:135], v[212:215]\n"
    "v_bfe_u32 v24, s40, v13, 8\n"
    "v_mfma_f32_16x16x32_bf16 v[212:215], v[180:183], v[164:167], v[212:215]\n"
    "v_add_u32 v22, v22, v24\n"
    "v_mfma_f32_16x16x32_bf16 v[208:211], v[180:183], v[160:163], v[208:211]\n"
    "v_mfma_f32_16x16x32_bf16 v[208:211], v[148:151], v[128:131], v[208:211]\n"
    "s_mov_b32 s40, 0x1a191818\n"
    "v_mfma_f32_16x16x32_bf16 v[224:227], v[152:155], v[128:131], v[224:227]\n"
    "v_mfma_f32_16x16x32_bf16 v[224:227], v[184:187], v[160:163], v[224:227]\n"
    "v_bfe_u32 v24, s40, v13, 8\n"
    "v_mfma_f32_16x16x32_bf16 v[228:231], v[184:187], v[164:167], v[228:231]\n"
    "v_cndmask_b32 v22, v22, v24, vcc\n"
    "v_mfma_f32_16x16x32_bf16 v[228:231], v[152:155], v[132:135], v[228:231]\n"
    "v_mfma_f32_16x16x32_bf16 v[232:235], v[152:155], v[136:139], v[232:235]\n"
    "v_mad_u32_u24 v5, v22, s41, v21\n"
    "v_mfma_f32_16x16x32_bf16 v[232:235], v[184:187], v[168:171], v[232:235]\n"
    "v_xor_b32 v22, 1, v12\n"
    "v_mfma_f32_16x16x32_bf16 v[236:239], v[184:187], v[172:175], v[236:239]\n"
    "v_mfma_f32_16x16x32_bf16 v[236:239], v[152:155], v[140:143], v[236:239]\n"
    "v_and_b32 v22, 1, v22\n"
    "v_mfma_f32_16x16x32_bf16 v[252:255], v[156:159], v[140:143], v[252:255]\n"
    "v_mfma_f32_16x16x32_bf16 v[252:255], v[188:191], v[172:175], v[252:255]\n"
    "v_lshlrev_b32 v22, 1, v22\n"
    "v_mfma_f32_16x16x32_bf16 v[248:251], v[188:191], v[168:171], v[248:251]\n"
    "v_lshl_add_u32 v22, v12, 3, v22\n"
    "v_mfma_f32_16x16x32_bf16 v[248:251], v[156:159], v[136:139], v[248:251]\n"
    "v_mfma_f32_16x16x32_bf16 v[244:247], v[156:159], v[132:135], v[244:247]\n"
    "s_mov_b32 s40, 0x05040100\n"
    "v_mfma_f32_16x16x32_bf16 v[244:247], v[188:191], v[164:167], v[244:247]\n"
    "v_mfma_f32_16x16x32_bf16 v[240:243], v[188:191], v[160:163], v[240:243]\n"
    "v_mfma_f32_16x16x32_bf16 v[240:243], v[156:159], v[128:131], v[240:243]\n"
    "s_waitcnt vmcnt(6)\n"
    "s_barrier\n"
    "s_add_u32 m0, s35, 98304\n"
    "s_nop 0\n"
    "global_load_lds_dwordx4 v46, s[30:31] offset:0\n"
    "ds_read_b128 v[128:131], v56 offset:0\n"
    "ds_read_b128 v[132:135], v56 offset:2048\n"
    "ds_read_b128 v[136:139], v56 offset:4096\n"
    "ds_read_b128 v[140:143], v56 offset:6144\n"
    "global_load_lds_dwordx4 v47, s[30:31] offset:1024\n"
    "s_bfe_u32 s66, s28, 0x80010\n"
    "s_mul_i32 s66, s66, 53248\n"
    "s_add_u32 s30, s6, s66\n"
    "s_addc_u32 s31, s7, 0\n"
    "ds_read_b64_tr_b16 v[144:145], v76 offset:0\n"
    "ds_read_b64_tr_b16 v[146:147], v76 offset:1024\n"
    "ds_read_b64_tr_b16 v[148:149], v77 offset:0\n"
    "ds_read_b64_tr_b16 v[150:151], v77 offset:1024\n"
    "s_add_u32 m0, s34, 98304\n"
    "s_nop 0\n"
    "global_load_lds_dwordx4 v52, s[36:37] offset:0\n"
    "ds_read_b64_tr_b16 v[152:153], v78 offset:0\n"
    "ds_read_b64_tr_b16 v[154:155], v78 offset:1024\n"
    "ds_read_b64_tr_b16 v[156:157], v79 offset:0\n"
    "ds_read_b64_tr_b16 v[158:159], v79 offset:1024\n"
    "global_load_lds_dwordx4 v53, s[36:37] offset:1024\n"
    "ds_read_b128 v[160:163], v57 offset:0\n"
    "ds_read_b128 v[164:167], v57 offset:2048\n"
    "ds_read_b128 v[168:171], v57 offset:4096\n"
    "ds_read_b128 v[172:175], v57 offset:6144\n"
    "global_load_lds_dwordx4 v54, s[36:37] offset:2048\n"
    "ds_read_b64_tr_b16 v[176:177], v76 offset:8192\n"
    "ds_read_b64_tr_b16 v[178:179], v76 offset:9216\n"
    "ds_read_b64_tr_b16 v[180:181], v77 offset:8192\n"
    "ds_read_b64_tr_b16 v[182:183], v77 offset:9216\n"
    "global_load_lds_dwordx4 v55, s[36:37] offset:3072\n"
    "ds_read_b64_tr_b16 v[184:185], v78 offset:8192\n"
    "ds_read_b64_tr_b16 v[186:187], v78 offset:9216\n"
    "ds_read_b64_tr_b16 v[188:189], v79 offset:8192\n"
    "ds_read_b64_tr_b16 v[190:191], v79 offset:9216\n"
    "s_waitcnt vmcnt(10) lgkmcnt(0)\n"
    "s_barrier\n"
    "v_mfma_f32_16x16x32_bf16 v[192:195], v[144:147], v[128:131], v[192:195]\n"
    "v_mfma_f32_16x16x32_bf16 v[192:195], v[176:179], v[160:163], v[192:195]\n"
    "v_add_u32_dpp v52, v96, v48 quad_perm:[2,2,2,2] row_mask:0xf bank_mask:0xf\n"
    "v_mfma_f32_16x16x32_bf16 v[196:199], v[176:179], v[164:167], v[196:199]\n"
    "v_add_u32_dpp v53, v104, v49 quad_perm:[2,2,2,2] row_mask:0xf bank_mask:0xf\n"
    "v_mfma_f32_16x16x32_bf16 v[196:199], v[144:147], v[132:135], v[196:199]\n"
    "v_mfma_f32_16x16x32_bf16 v[200:203], v[144:147], v[136:139], v[200:203]\n"
    "v_add_u32_dpp v54, v112, v50 quad_perm:[2,2,2,2] row_mask:0xf bank_mask:0xf\n"
    "v_mfma_f32_16x16x32_bf16 v[200:203], v[176:179], v[168:171], v[200:203]\n"
    "v_add_u32_dpp v55, v120, v51 quad_perm:[2,2,2,2] row_mask:0xf bank_mask:0xf\n"
    "v_mfma_f32_16x16x32_bf16 v[204:207], v[176:179], v[172:175], v[204:207]\n"
    "v_mfma_f32_16x16x32_bf16 v[204:207], v[144:147], v[140:143], v[204:207]\n"
    "v_bfe_u32 v24, s40, v13, 8\n"
    "v_mfma_f32_16x16x32_bf16 v[220:223], v[148:151], v[140:143], v[220:223]\n"
    "v_mfma_f32_16x16x32_bf16 v[220:223], v[180:183], v[172:175], v[220:223]\n"
    "v_add_u32 v22, v22, v24\n"
    "v_mfma_f32_16x16x32_bf16 v[216:219], v[180:183], v[168:171], v[216:219]\n"
    "s_mov_b32 s40, 0x1a191918\n"
    "v_mfma_f32_16x16x32_bf16 v[216:219], v[148:151], v[136:139], v[216:219]\n"
    "v_mfma_f32_16x16x32_bf16 v[212:215], v[148:151], v[132:135], v[212:215]\n"
    "v_bfe_u32 v24, s40, v13, 8\n"
    "v_mfma_f32_16x16x32_bf16 v[212:215], v[180:183], v[164:167], v[212:215]\n"
    "v_cndmask_b32 v22, v22, v24, vcc\n"
    "v_mfma_f32_16x16x32_bf16 v[208:211], v[180:183], v[160:163], v[208:211]\n"
    "v_mfma_f32_16x16x32_bf16 v[208:211], v[148:151], v[128:131], v[208:211]\n"
    "v_mad_u32_u24 v6, v22, s41, v21\n"
    "v_mfma_f32_16x16x32_bf16 v[224:227], v[152:155], v[128:131], v[224:227]\n"
    "v_mfma_f32_16x16x32_bf16 v[224:227], v[184:187], v[160:163], v[224:227]\n"
    "v_bfrev_b32 v14, v12\n"
    "v_mfma_f32_16x16x32_bf16 v[228:231], v[184:187], v[164:167], v[228:231]\n"
    "v_lshrrev_b32 v14, 26, v14\n"
    "v_mfma_f32_16x16x32_bf16 v[228:231], v[152:155], v[132:135], v[228:231]\n"
    "v_mfma_f32_16x16x32_bf16 v[232:235], v[152:155], v[136:139], v[232:235]\n"
    "s_add_u32 s40, s22, 3\n"
    "v_mfma_f32_16x16x32_bf16 v[232:235], v[184:187], v[168:171], v[232:235]\n"
    "s_and_b32 s40, s40, 3\n"
    "v_mfma_f32_16x16x32_bf16 v[236:239], v[184:187], v[172:175], v[236:239]\n"
    "v_mfma_f32_16x16x32_bf16 v[236:239], v[152:155], v[140:143], v[236:239]\n"
    "s_lshl2_add_u32 s40, s40, s22\n"
    "v_mfma_f32_16x16x32_bf16 v[252:255], v[156:159], v[140:143], v[252:255]\n"
    "v_mfma_f32_16x16x32_bf16 v[252:255], v[188:191], v[172:175], v[252:255]\n"
    "s_add_u32 s40, s40, s58\n"
    "v_mfma_f32_16x16x32_bf16 v[248:251], v[188:191], v[168:171], v[248:251]\n"
    "s_lshl_b32 s40, s40, 2\n"
    "v_mfma_f32_16x16x32_bf16 v[248:251], v[156:159], v[136:139], v[248:251]\n"
    "v_mfma_f32_16x16x32_bf16 v[244:247], v[156:159], v[132:135], v[244:247]\n"
    "s_add_u32 s40, s40, -450048\n"
    "v_mfma_f32_16x16x32_bf16 v[244:247], v[188:191], v[164:167], v[244:247]\n"
    "v_mfma_f32_16x16x32_bf16 v[240:243], v[188:191], v[160:163], v[240:243]\n"
    "v_mfma_f32_16x16x32_bf16 v[240:243], v[156:159], v[128:131], v[240:243]\n"
    "s_waitcnt vmcnt(6)\n"
    "s_barrier\n"
    "s_add_u32 m0, s35, 0\n"
    "s_nop 0\n"
    "global_load_lds_dwordx4 v46, s[30:31] offset:0\n"
    "ds_read_b128 v[128:131], v56 offset:49152\n"
    "ds_read_b128 v[132:135], v56 offset:51200\n"
    "ds_read_b128 v[136:139], v56 offset:53248\n"
    "ds_read_b128 v[140:143], v56 offset:55296\n"
    "global_load_lds_dwordx4 v47, s[30:31] offset:1024\n"
    "s_add_u32 s30, s30, 212992\n"
    "s_addc_u32 s31, s31, 0\n"
    "ds_read_b64_tr_b16 v[144:145], v76 offset:49152\n"
    "ds_read_b64_tr_b16 v[146:147], v76 offset:50176\n"
    "ds_read_b64_tr_b16 v[148:149], v77 offset:49152\n"
    "ds_read_b64_tr_b16 v[150:151], v77 offset:50176\n"
    "s_add_u32 m0, s34, 0\n"
    "s_nop 0\n"
    "global_load_lds_dwordx4 v52, s[36:37] offset:0\n"
    "ds_read_b64_tr_b16 v[152:153], v78 offset:49152\n"
    "ds_read_b64_tr_b16 v[154:155], v78 offset:50176\n"
    "ds_read_b64_tr_b16 v[156:157], v79 offset:49152\n"
    "ds_read_b64_tr_b16 v[158:159], v79 offset:50176\n"
    "global_load_lds_dwordx4 v53, s[36:37] offset:1024\n"
    "ds_read_b128 v[160:163], v57 offset:49152\n"
    "ds_read_b128 v[164:167], v57 offset:51200\n"
    "ds_read_b128 v[168:171], v57 offset:53248\n"
    "ds_read_b128 v[172:175], v57 offset:55296\n"
    "global_load_lds_dwordx4 v54, s[36:37] offset:2048\n"
    "ds_read_b64_tr_b16 v[176:177], v76 offset:57344\n"
    "ds_read_b64_tr_b16 v[178:179], v76 offset:58368\n"
    "ds_read_b64_tr_b16 v[180:181], v77 offset:57344\n"
    "ds_read_b64_tr_b16 v[182:183], v77 offset:58368\n"
    "global_load_lds_dwordx4 v55, s[36:37] offset:3072\n"
    "ds_read_b64_tr_b16 v[184:185], v78 offset:57344\n"
    "ds_read_b64_tr_b16 v[186:187], v78 offset:58368\n"
    "ds_read_b64_tr_b16 v[188:189], v79 offset:57344\n"
    "ds_read_b64_tr_b16 v[190:191], v79 offset:58368\n"
    "s_waitcnt vmcnt(10) lgkmcnt(0)\n"
    "s_barrier\n"
    "v_mfma_f32_16x16x32_bf16 v[192:195], v[144:147], v[128:131], v[192:195]\n"
    "v_mfma_f32_16x16x32_bf16 v[192:195], v[176:179], v[160:163], v[192:195]\n"
    "v_add_u32_dpp v52, v97, v48 quad_perm:[2,2,2,2] row_mask:0xf bank_mask:0xf\n"
    "v_mfma_f32_16x16x32_bf16 v[196:199], v[176:179], v[164:167], v[196:199]\n"
    "v_add_u32_dpp v53, v105, v49 quad_perm:[2,2,2,2] row_mask:0xf bank_mask:0xf\n"
    "v_mfma_f32_16x16x32_bf16 v[196:199], v[144:147], v[132:135], v[196:199]\n"
    "v_mfma_f32_16x16x32_bf16 v[200:203], v[144:147], v[136:139], v[200:203]\n"
    "v_add_u32_dpp v54, v113, v50 quad_perm:[2,2,2,2] row_mask:0xf bank_mask:0xf\n"
    "v_mfma_f32_16x16x32_bf16 v[200:203], v[176:179], v[168:171], v[200:203]\n"
    "v_add_u32_dpp v55, v121, v51 quad_perm:[2,2,2,2] row_mask:0xf bank_mask:0xf\n"
    "v_mfma_f32_16x16x32_bf16 v[204:207], v[176:179], v[172:175], v[204:207]\n"
    "v_mfma_f32_16x16x32_bf16 v[204:207], v[144:147], v[140:143], v[204:207]\n"
    "v_add_u32 v14, s40, v14\n"
    "v_mfma_f32_16x16x32_bf16 v[220:223], v[148:151], v[140:143], v[220:223]\n"
    "v_mfma_f32_16x16x32_bf16 v[220:223], v[180:183], v[172:175], v[220:223]\n"
    "v_and_b32 v2, 1, v1\n"
    "v_mfma_f32_16x16x32_bf16 v[216:219], v[180:183], v[168:171], v[216:219]\n"
    "v_mul_u32_u24 v2, 0xaaaa, v2\n"
    "v_mfma_f32_16x16x32_bf16 v[216:219], v[148:151], v[136:139], v[216:219]\n"
    "v_mfma_f32_16x16x32_bf16 v[212:215], v[148:151], v[132:135], v[212:215]\n"
    "v_add_u32 v2, 0x4444, v2\n"
    "v_mfma_f32_16x16x32_bf16 v[212:215], v[180:183], v[164:167], v[212:215]\n"
    "v_lshrrev_b32 v12, 3, v1\n"
    "v_mfma_f32_16x16x32_bf16 v[208:211], v[180:183], v[160:163], v[208:211]\n"
    "v_mfma_f32_16x16x32_bf16 v[208:211], v[148:151], v[128:131], v[208:211]\n"
    "v_mul_u32_u24 v17, 12, v12\n"
    "v_mfma_f32_16x16x32_bf16 v[224:227], v[152:155], v[128:131], v[224:227]\n"
    "v_mfma_f32_16x16x32_bf16 v[224:227], v[184:187], v[160:163], v[224:227]\n"
    "v_and_b32 v19, 7, v1\n"
    "v_mfma_f32_16x16x32_bf16 v[228:231], v[184:187], v[164:167], v[228:231]\n"
    "v_lshlrev_b32 v19, 4, v19\n"
    "v_mfma_f32_16x16x32_bf16 v[228:231], v[152:155], v[132:135], v[228:231]\n"
    "v_mfma_f32_16x16x32_bf16 v[232:235], v[152:155], v[136:139], v[232:235]\n"
    "v_mad_u32_u24 v15, v17, 40, v19\n"
    "v_mfma_f32_16x16x32_bf16 v[232:235], v[184:187], v[168:171], v[232:235]\n"
    "v_lshrrev_b32 v12, 5, v1\n"
    "v_mfma_f32_16x16x32_bf16 v[236:239], v[184:187], v[172:175], v[236:239]\n"
    "v_mfma_f32_16x16x32_bf16 v[236:239], v[152:155], v[140:143], v[236:239]\n"
    "v_lshlrev_b32 v18, 2, v12\n"
    "v_mfma_f32_16x16x32_bf16 v[252:255], v[156:159], v[140:143], v[252:255]\n"
    "v_mfma_f32_16x16x32_bf16 v[252:255], v[188:191], v[172:175], v[252:255]\n"
    "v_and_b32 v20, 31, v1\n"
    "v_mfma_f32_16x16x32_bf16 v[248:251], v[188:191], v[168:171], v[248:251]\n"
    "v_lshlrev_b32 v20, 2, v20\n"
    "v_mfma_f32_16x16x32_bf16 v[248:251], v[156:159], v[136:139], v[248:251]\n"
    "v_mfma_f32_16x16x32_bf16 v[244:247], v[156:159], v[132:135], v[244:247]\n"
    "v_mad_u32_u24 v16, v12, s41, v20\n"
    "v_mfma_f32_16x16x32_bf16 v[244:247], v[188:191], v[164:167], v[244:247]\n"
    "v_mfma_f32_16x16x32_bf16 v[240:243], v[188:191], v[160:163], v[240:243]\n"
    "v_mfma_f32_16x16x32_bf16 v[240:243], v[156:159], v[128:131], v[240:243]\n"
    "s_waitcnt vmcnt(6)\n"
    "s_barrier\n"
    "s_add_u32 m0, s35, 49152\n"
    "s_nop 0\n"
    "global_load_lds_dwordx4 v46, s[30:31] offset:0\n"
    "ds_read_b128 v[128:131], v58 offset:0\n"
    "ds_read_b128 v[132:135], v58 offset:2048\n"
    "ds_read_b128 v[136:139], v58 offset:4096\n"
    "ds_read_b128 v[140:143], v58 offset:6144\n"
    "global_load_lds_dwordx4 v47, s[30:31] offset:1024\n"
    "s_add_u32 s30, s30, 212992\n"
    "s_addc_u32 s31, s31, 0\n"
    "ds_read_b64_tr_b16 v[144:145], v80 offset:0\n"
    "ds_read_b64_tr_b16 v[146:147], v80 offset:1024\n"
    "ds_read_b64_tr_b16 v[148:149], v81 offset:0\n"
    "ds_read_b64_tr_b16 v[150:151], v81 offset:1024\n"
    "s_add_u32 m0, s34, 49152\n"
    "s_nop 0\n"
    "global_load_lds_dwordx4 v52, s[36:37] offset:0\n"
    "ds_read_b64_tr_b16 v[152:153], v82 offset:0\n"
    "ds_read_b64_tr_b16 v[154:155], v82 offset:1024\n"
    "ds_read_b64_tr_b16 v[156:157], v83 offset:0\n"
    "ds_read_b64_tr_b16 v[158:159], v83 offset:1024\n"
    "global_load_lds_dwordx4 v53, s[36:37] offset:1024\n"
    "ds_read_b128 v[160:163], v59 offset:0\n"
    "ds_read_b128 v[164:167], v59 offset:2048\n"
    "ds_read_b128 v[168:171], v59 offset:4096\n"
    "ds_read_b128 v[172:175], v59 offset:6144\n"
    "global_load_lds_dwordx4 v54, s[36:37] offset:2048\n"
    "ds_read_b64_tr_b16 v[176:177], v80 offset:8192\n"
    "ds_read_b64_tr_b16 v[178:179], v80 offset:9216\n"
    "ds_read_b64_tr_b16 v[180:181], v81 offset:8192\n"
    "ds_read_b64_tr_b16 v[182:183], v81 offset:9216\n"
    "global_load_lds_dwordx4 v55, s[36:37] offset:3072\n"
    "ds_read_b64_tr_b16 v[184:185], v82 offset:8192\n"
    "ds_read_b64_tr_b16 v[186:187], v82 offset:9216\n"
    "ds_read_b64_tr_b16 v[188:189], v83 offset:8192\n"
    "ds_read_b64_tr_b16 v[190:191], v83 offset:9216\n"
    "s_waitcnt vmcnt(10) lgkmcnt(0)\n"
    "s_barrier\n"
    "v_mfma_f32_16x16x32_bf16 v[192:195], v[144:147], v[128:131], v[192:195]\n"
    "v_mfma_f32_16x16x32_bf16 v[192:195], v[176:179], v[160:163], v[192:195]\n"
    "v_add_u32_dpp v52, v98, v48 quad_perm:[2,2,2,2] row_mask:0xf bank_mask:0xf\n"
    "v_mfma_f32_16x16x32_bf16 v[196:199], v[176:179], v[164:167], v[196:199]\n"
    "v_add_u32_dpp v53, v106, v49 quad_perm:[2,2,2,2] row_mask:0xf bank_mask:0xf\n"
    "v_mfma_f32_16x16x32_bf16 v[196:199], v[144:147], v[132:135], v[196:199]\n"
    "v_mfma_f32_16x16x32_bf16 v[200:203], v[144:147], v[136:139], v[200:203]\n"
    "v_add_u32_dpp v54, v114, v50 quad_perm:[2,2,2,2] row_mask:0xf bank_mask:0xf\n"
    "v_mfma_f32_16x16x32_bf16 v[200:203], v[176:179], v[168:171], v[200:203]\n"
    "v_add_u32_dpp v55, v122, v51 quad_perm:[2,2,2,2] row_mask:0xf bank_mask:0xf\n"
    "v_mfma_f32_16x16x32_bf16 v[204:207], v[176:179], v[172:175], v[204:207]\n"
    "v_mfma_f32_16x16x32_bf16 v[204:207], v[144:147], v[140:143], v[204:207]\n"
    "s_lshl2_add_u32 s40, s22, s58\n"
    "v_mfma_f32_16x16x32_bf16 v[220:223], v[148:151], v[140:143], v[220:223]\n"
    "v_mfma_f32_16x16x32_bf16 v[220:223], v[180:183], v[172:175], v[220:223]\n"
    "s_sub_u32 s40, s40, 20\n"
    "v_mfma_f32_16x16x32_bf16 v[216:219], v[180:183], v[168:171], v[216:219]\n"
    "v_mov_b32 v25, s40\n"
    "v_mfma_f32_16x16x32_bf16 v[216:219], v[148:151], v[136:139], v[216:219]\n"
    "v_mfma_f32_16x16x32_bf16 v[212:215], v[148:151], v[132:135], v[212:215]\n"
    "s_movk_i32 s41, 4480\n"
    "v_mfma_f32_16x16x32_bf16 v[212:215], v[180:183], v[164:167], v[212:215]\n"
    "v_lshrrev_b32 v12, 5, v19\n"
    "v_mfma_f32_16x16x32_bf16 v[208:211], v[180:183], v[160:163], v[208:211]\n"
    "v_mfma_f32_16x16x32_bf16 v[208:211], v[148:151], v[128:131], v[208:211]\n"
    "v_sub_u32 v12, 1, v12\n"
    "v_mfma_f32_16x16x32_bf16 v[224:227], v[152:155], v[128:131], v[224:227]\n"
    "v_mfma_f32_16x16x32_bf16 v[224:227], v[184:187], v[160:163], v[224:227]\n"
    "v_and_b32 v12, 3, v12\n"
    "v_mfma_f32_16x16x32_bf16 v[228:231], v[184:187], v[164:167], v[228:231]\n"
    "v_add_u32 v12, s22, v12\n"
    "v_mfma_f32_16x16x32_bf16 v[228:231], v[152:155], v[132:135], v[228:231]\n"
    "v_mfma_f32_16x16x32_bf16 v[232:235], v[152:155], v[136:139], v[232:235]\n"
    "v_add_u32 v15, s43, v15\n"
    "v_mfma_f32_16x16x32_bf16 v[232:235], v[184:187], v[168:171], v[232:235]\n"
    "v_xor_b32 v13, 4, v12\n"
    "v_mfma_f32_16x16x32_bf16 v[236:239], v[184:187], v[172:175], v[236:239]\n"
    "v_mfma_f32_16x16x32_bf16 v[236:239], v[152:155], v[140:143], v[236:239]\n"
    "v_mad_u32_u24 v0, v13, s41, v15\n"
    "v_mfma_f32_16x16x32_bf16 v[252:255], v[156:159], v[140:143], v[252:255]\n"
    "v_mfma_f32_16x16x32_bf16 v[252:255], v[188:191], v[172:175], v[252:255]\n"
    "v_mad_u32_u24 v15, v12, s41, v15\n"
    "v_mfma_f32_16x16x32_bf16 v[248:251], v[188:191], v[168:171], v[248:251]\n"
    "v_add_u32 v0, v0, v15\n"
    "v_mfma_f32_16x16x32_bf16 v[248:251], v[156:159], v[136:139], v[248:251]\n"
    "v_mfma_f32_16x16x32_bf16 v[244:247], v[156:159], v[132:135], v[244:247]\n"
    "v_lshrrev_b32 v12, 5, v20\n"
    "v_mfma_f32_16x16x32_bf16 v[244:247], v[188:191], v[164:167], v[244:247]\n"
    "v_mfma_f32_16x16x32_bf16 v[240:243], v[188:191], v[160:163], v[240:243]\n"
    "v_mfma_f32_16x16x32_bf16 v[240:243], v[156:159], v[128:131], v[240:243]\n"
    "s_waitcnt vmcnt(6)\n"
    "s_barrier\n"
    "s_add_u32 m0, s35, 98304\n"
    "s_nop 0\n"
    "global_load_lds_dwordx4 v46, s[30:31] offset:0\n"
    "ds_read_b128 v[128:131], v56 offset:0\n"
    "ds_read_b128 v[132:135], v56 offset:2048\n"
    "ds_read_b128 v[136:139], v56 offset:4096\n"
    "ds_read_b128 v[140:143], v56 offset:6144\n"
    "global_load_lds_dwordx4 v47, s[30:31] offset:1024\n"
    "s_add_u32 s30, s30, 212992\n"
    "s_addc_u32 s31, s31, 0\n"
    "ds_read_b64_tr_b16 v[144:145], v76 offset:0\n"
    "ds_read_b64_tr_b16 v[146:147], v76 offset:1024\n"
    "ds_read_b64_tr_b16 v[148:149], v77 offset:0\n"
    "ds_read_b64_tr_b16 v[150:151], v77 offset:1024\n"
    "s_add_u32 m0, s34, 98304\n"
    "s_nop 0\n"
    "global_load_lds_dwordx4 v52, s[36:37] offset:0\n"
    "ds_read_b64_tr_b16 v[152:153], v78 offset:0\n"
    "ds_read_b64_tr_b16 v[154:155], v78 offset:1024\n"
    "ds_read_b64_tr_b16 v[156:157], v79 offset:0\n"
    "ds_read_b64_tr_b16 v[158:159], v79 offset:1024\n"
    "global_load_lds_dwordx4 v53, s[36:37] offset:1024\n"
    "ds_read_b128 v[160:163], v57 offset:0\n"
    "ds_read_b128 v[164:167], v57 offset:2048\n"
    "ds_read_b128 v[168:171], v57 offset:4096\n"
    "ds_read_b128 v[172:175], v57 offset:6144\n"
    "global_load_lds_dwordx4 v54, s[36:37] offset:2048\n"
    "ds_read_b64_tr_b16 v[176:177], v76 offset:8192\n"
    "ds_read_b64_tr_b16 v[178:179], v76 offset:9216\n"
    "ds_read_b64_tr_b16 v[180:181], v77 offset:8192\n"
    "ds_read_b64_tr_b16 v[182:183], v77 offset:9216\n"
    "global_load_lds_dwordx4 v55, s[36:37] offset:3072\n"
    "ds_read_b64_tr_b16 v[184:185], v78 offset:8192\n"
    "ds_read_b64_tr_b16 v[186:187], v78 offset:9216\n"
    "ds_read_b64_tr_b16 v[188:189], v79 offset:8192\n"
    "ds_read_b64_tr_b16 v[190:191], v79 offset:9216\n"
    "s_waitcnt vmcnt(10) lgkmcnt(0)\n"
    "s_barrier\n"
    "v_mfma_f32_16x16x32_bf16 v[192:195], v[144:147], v[128:131], v[192:195]\n"
    "v_mfma_f32_16x16x32_bf16 v[192:195], v[176:179], v[160:163], v[192:195]\n"
    "v_add_u32_dpp v52, v99, v48 quad_perm:[2,2,2,2] row_mask:0xf bank_mask:0xf\n"
    "v_mfma_f32_16x16x32_bf16 v[196:199], v[176:179], v[164:167], v[196:199]\n"
    "v_add_u32_dpp v53, v107, v49 quad_perm:[2,2,2,2] row_mask:0xf bank_mask:0xf\n"
    "v_mfma_f32_16x16x32_bf16 v[196:199], v[144:147], v[132:135], v[196:199]\n"
    "v_mfma_f32_16x16x32_bf16 v[200:203], v[144:147], v[136:139], v[200:203]\n"
    "v_add_u32_dpp v54, v115, v50 quad_perm:[2,2,2,2] row_mask:0xf bank_mask:0xf\n"
    "v_mfma_f32_16x16x32_bf16 v[200:203], v[176:179], v[168:171], v[200:203]\n"
    "v_add_u32_dpp v55, v123, v51 quad_perm:[2,2,2,2] row_mask:0xf bank_mask:0xf\n"
    "v_mfma_f32_16x16x32_bf16 v[204:207], v[176:179], v[172:175], v[204:207]\n"
    "v_mfma_f32_16x16x32_bf16 v[204:207], v[144:147], v[140:143], v[204:207]\n"
    "v_sub_u32 v12, 1, v12\n"
    "v_mfma_f32_16x16x32_bf16 v[220:223], v[148:151], v[140:143], v[220:223]\n"
    "v_and_b32 v12, 3, v12\n"
    "v_mfma_f32_16x16x32_bf16 v[220:223], v[180:183], v[172:175], v[220:223]\n"
    "v_mfma_f32_16x16x32_bf16 v[216:219], v[180:183], v[168:171], v[216:219]\n"
    "v_add_u32 v12, s22, v12\n"
    "v_mfma_f32_16x16x32_bf16 v[216:219], v[148:151], v[136:139], v[216:219]\n"
    "v_add_u32 v16, s43, v16\n"
    "v_mfma_f32_16x16x32_bf16 v[212:215], v[148:151], v[132:135], v[212:215]\n"
    "v_mfma_f32_16x16x32_bf16 v[212:215], v[180:183], v[164:167], v[212:215]\n"
    "v_xor_b32 v13, 4, v12\n"
    "v_mfma_f32_16x16x32_bf16 v[208:211], v[180:183], v[160:163], v[208:211]\n"
    "v_mad_u32_u24 v23, v13, s41, v16\n"
    "v_mfma_f32_16x16x32_bf16 v[208:211], v[148:151], v[128:131], v[208:211]\n"
    "v_mfma_f32_16x16x32_bf16 v[224:227], v[152:155], v[128:131], v[224:227]\n"
    "v_mad_u32_u24 v16, v12, s41, v16\n"
    "v_mfma_f32_16x16x32_bf16 v[224:227], v[184:187], v[160:163], v[224:227]\n"
    "v_add_u32 v23, v23, v16\n"
    "v_mfma_f32_16x16x32_bf16 v[228:231], v[184:187], v[164:167], v[228:231]\n"
    "v_mfma_f32_16x16x32_bf16 v[228:231], v[152:155], v[132:135], v[228:231]\n"
    "s_mul_i32 s41, s47, 13\n"
    "v_mfma_f32_16x16x32_bf16 v[232:235], v[152:155], v[136:139], v[232:235]\n"
    "s_sub_u32 s41, s3, s41\n"
    "v_mfma_f32_16x16x32_bf16 v[232:235], v[184:187], v[168:171], v[232:235]\n"
    "v_mfma_f32_16x16x32_bf16 v[236:239], v[184:187], v[172:175], v[236:239]\n"
    "s_lshl1_add_u32 s41, s41, s21\n"
    "v_mfma_f32_16x16x32_bf16 v[236:239], v[152:155], v[140:143], v[236:239]\n"
    "s_mul_i32 s41, s41, 3\n"
    "v_mfma_f32_16x16x32_bf16 v[252:255], v[156:159], v[140:143], v[252:255]\n"
    "v_mfma_f32_16x16x32_bf16 v[252:255], v[188:191], v[172:175], v[252:255]\n"
    "s_add_u32 s41, s41, s22\n"
    "v_mfma_f32_16x16x32_bf16 v[248:251], v[188:191], v[168:171], v[248:251]\n"
    "s_cmp_eq_u32 s22, 3\n"
    "s_cselect_b32 s41, -1, s41\n"
    "v_mfma_f32_16x16x32_bf16 v[248:251], v[156:159], v[136:139], v[248:251]\n"
    "v_mfma_f32_16x16x32_bf16 v[244:247], v[156:159], v[132:135], v[244:247]\n"
    "v_mov_b32 v24, s41\n"
    "v_mfma_f32_16x16x32_bf16 v[244:247], v[188:191], v[164:167], v[244:247]\n"
    "v_mfma_f32_16x16x32_bf16 v[240:243], v[188:191], v[160:163], v[240:243]\n"
    "v_mfma_f32_16x16x32_bf16 v[240:243], v[156:159], v[128:131], v[240:243]\n"
    "s_waitcnt vmcnt(6)\n"
    "s_barrier\n"
    "s_add_u32 m0, s35, 0\n"
    "s_nop 0\n"
    "global_load_lds_dwordx4 v46, s[30:31] offset:0\n"
    "ds_read_b128 v[128:131], v56 offset:49152\n"
    "ds_read_b128 v[132:135], v56 offset:51200\n"
    "ds_read_b128 v[136:139], v56 offset:53248\n"
    "ds_read_b128 v[140:143], v56 offset:55296\n"
    "global_load_lds_dwordx4 v47, s[30:31] offset:1024\n"
    "s_bfe_u32 s66, s28, 0x80018\n"
    "s_mul_i32 s66, s66, 53248\n"
    "s_add_u32 s30, s6, s66\n"
    "s_addc_u32 s31, s7, 0\n"
    "ds_read_b64_tr_b16 v[144:145], v76 offset:49152\n"
    "ds_read_b64_tr_b16 v[146:147], v76 offset:50176\n"
    "ds_read_b64_tr_b16 v[148:149], v77 offset:49152\n"
    "ds_read_b64_tr_b16 v[150:151], v77 offset:50176\n"
    "s_add_u32 m0, s34, 0\n"
    "s_nop 0\n"
    "global_load_lds_dwordx4 v52, s[36:37] offset:0\n"
    "ds_read_b64_tr_b16 v[152:153], v78 offset:49152\n"
    "ds_read_b64_tr_b16 v[154:155], v78 offset:50176\n"
    "ds_read_b64_tr_b16 v[156:157], v79 offset:49152\n"
    "ds_read_b64_tr_b16 v[158:159], v79 offset:50176\n"
    "global_load_lds_dwordx4 v53, s[36:37] offset:1024\n"
    "ds_read_b128 v[160:163], v57 offset:49152\n"
    "ds_read_b128 v[164:167], v57 offset:51200\n"
    "ds_read_b128 v[168:171], v57 offset:53248\n"
    "ds_read_b128 v[172:175], v57 offset:55296\n"
    "global_load_lds_dwordx4 v54, s[36:37] offset:2048\n"
    "ds_read_b64_tr_b16 v[176:177], v76 offset:57344\n"
    "ds_read_b64_tr_b16 v[178:179], v76 offset:58368\n"
    "ds_read_b64_tr_b16 v[180:181], v77 offset:57344\n"
    "ds_read_b64_tr_b16 v[182:183], v77 offset:58368\n"
    "global_load_lds_dwordx4 v55, s[36:37] offset:3072\n"
    "ds_read_b64_tr_b16 v[184:185], v78 offset:57344\n"
    "ds_read_b64_tr_b16 v[186:187], v78 offset:58368\n"
    "ds_read_b64_tr_b16 v[188:189], v79 offset:57344\n"
    "ds_read_b64_tr_b16 v[190:191], v79 offset:58368\n"
    "s_waitcnt vmcnt(10) lgkmcnt(0)\n"
    "s_barrier\n"
    "v_mfma_f32_16x16x32_bf16 v[192:195], v[144:147], v[128:131], v[192:195]\n"
    "v_mfma_f32_16x16x32_bf16 v[192:195], v[176:179], v[160:163], v[192:195]\n"
    "v_add_u32_dpp v52, v96, v48 quad_perm:[3,3,3,3] row_mask:0xf bank_mask:0xf\n"
    "v_mfma_f32_16x16x32_bf16 v[196:199], v[176:179], v[164:167], v[196:199]\n"
    "v_mfma_f32_16x16x32_bf16 v[196:199], v[144:147], v[132:135], v[196:199]\n"
    "v_mfma_f32_16x16x32_bf16 v[200:203], v[144:147], v[136:139], v[200:203]\n"
    "v_mfma_f32_16x16x32_bf16 v[200:203], v[176:179], v[168:171], v[200:203]\n"
    "v_add_u32_dpp v53, v104, v49 quad_perm:[3,3,3,3] row_mask:0xf bank_mask:0xf\n"
    "v_mfma_f32_16x16x32_bf16 v[204:207], v[176:179], v[172:175], v[204:207]\n"
    "v_mfma_f32_16x16x32_bf16 v[204:207], v[144:147], v[140:143], v[204:207]\n"
    "v_mfma_f32_16x16x32_bf16 v[220:223], v[148:151], v[140:143], v[220:223]\n"
    "v_mfma_f32_16x16x32_bf16 v[220:223], v[180:183], v[172:175], v[220:223]\n"
    "v_mfma_f32_16x16x32_bf16 v[216:219], v[180:183], v[168:171], v[216:219]\n"
    "v_add_u32_dpp v54, v112, v50 quad_perm:[3,3,3,3] row_mask:0xf bank_mask:0xf\n"
    "v_mfma_f32_16x16x32_bf16 v[216:219], v[148:151], v[136:139], v[216:219]\n"
    "v_mfma_f32_16x16x32_bf16 v[212:215], v[148:151], v[132:135], v[212:215]\n"
    "v_mfma_f32_16x16x32_bf16 v[212:215], v[180:183], v[164:167], v[212:215]\n"
    "v_mfma_f32_16x16x32_bf16 v[208:211], v[180:183], v[160:163], v[208:211]\n"
    "v_mfma_f32_16x16x32_bf16 v[208:211], v[148:151], v[128:131], v[208:211]\n"
    "v_add_u32_dpp v55, v120, v51 quad_perm:[3,3,3,3] row_mask:0xf bank_mask:0xf\n"
    "v_mfma_f32_16x16x32_bf16 v[224:227], v[152:155], v[128:131], v[224:227]\n"
    "v_mfma_f32_16x16x32_bf16 v[224:227], v[184:187], v[160:163], v[224:227]\n"
    "v_mfma_f32_16x16x32_bf16 v[228:231], v[184:187], v[164:167], v[228:231]\n"
    "v_mfma_f32_16x16x32_bf16 v[228:231], v[152:155], v[132:135], v[228:231]\n"
    "v_mfma_f32_16x16x32_bf16 v[232:235], v[152:155], v[136:139], v[232:235]\n"
    "v_readfirstlane_b32 s40, v24\n"
    "s_or_b32 s40, s40, s44\n"
    "s_lshl1_add_u32 s41, s40, s54\n"
    "s_cmp_lt_u32 s40, 256\n"
    "s_cselect_b32 s40, s41, -1\n"
    "s_cselect_b32 s41, s41, 2\n"
    "s_cselect_b64 s[48:49], -1, 0\n"
    "v_mov_b32 v12, s40\n"
    "s_mul_i32 s41, s41, 52\n"
    "s_add_u32 s38, s4, s41\n"
    "s_addc_u32 s39, s5, 0\n"
    "v_mfma_f32_16x16x32_bf16 v[232:235], v[184:187], v[168:171], v[232:235]\n"
    "v_mfma_f32_16x16x32_bf16 v[236:239], v[184:187], v[172:175], v[236:239]\n"
    "v_mfma_f32_16x16x32_bf16 v[236:239], v[152:155], v[140:143], v[236:239]\n"
    "v_mfma_f32_16x16x32_bf16 v[252:255], v[156:159], v[140:143], v[252:255]\n"
    "v_mfma_f32_16x16x32_bf16 v[252:255], v[188:191], v[172:175], v[252:255]\n"
    "v_ashrrev_i32 v21, 31, v12\n"
    "s_mov_b64 exec, s[48:49]\n"
    "ds_write2_b32 v25, v12, v21 offset0:5 offset1:133\n"
    "s_mov_b64 exec, -1\n"
    "v_mfma_f32_16x16x32_bf16 v[248:251], v[188:191], v[168:171], v[248:251]\n"
    "v_mfma_f32_16x16x32_bf16 v[248:251], v[156:159], v[136:139], v[248:251]\n"
    "v_mfma_f32_16x16x32_bf16 v[244:247], v[156:159], v[132:135], v[244:247]\n"
    "v_mfma_f32_16x16x32_bf16 v[244:247], v[188:191], v[164:167], v[244:247]\n"
    "v_mfma_f32_16x16x32_bf16 v[240:243], v[188:191], v[160:163], v[240:243]\n"
    "v_mfma_f32_16x16x32_bf16 v[240:243], v[156:159], v[128:131], v[240:243]\n"
    "s_waitcnt vmcnt(6)\n"
    "s_barrier\n"
    "s_add_u32 m0, s35, 49152\n"
    "s_nop 0\n"
    "global_load_lds_dwordx4 v46, s[30:31] offset:0\n"
    "ds_read_b128 v[128:131], v58 offset:0\n"
    "ds_read_b128 v[132:135], v58 offset:2048\n"
    "ds_read_b128 v[136:139], v58 offset:4096\n"
    "global_load_lds_dwordx4 v47, s[30:31] offset:1024\n"
    "s_add_u32 s30, s30, 212992\n"
    "s_addc_u32 s31, s31, 0\n"
    "ds_read_b128 v[140:143], v58 offset:6144\n"
    "ds_read_b64_tr_b16 v[144:145], v80 offset:0\n"
    "ds_read_b64_tr_b16 v[146:147], v80 offset:1024\n"
    "s_add_u32 m0, s34, 49152\n"
    "s_nop 0\n"
    "global_load_lds_dwordx4 v52, s[36:37] offset:0\n"
    "ds_read_b64_tr_b16 v[148:149], v81 offset:0\n"
    "ds_read_b64_tr_b16 v[150:151], v81 offset:1024\n"
    "ds_read_b64_tr_b16 v[152:153], v82 offset:0\n"
    "global_load_lds_dwordx4 v53, s[36:37] offset:1024\n"
    "ds_read_b64_tr_b16 v[154:155], v82 offset:1024\n"
    "ds_read_b64_tr_b16 v[156:157], v83 offset:0\n"
    "ds_read_b64_tr_b16 v[158:159], v83 offset:1024\n"
    "global_load_lds_dwordx4 v54, s[36:37] offset:2048\n"
    "ds_read_b128 v[160:163], v59 offset:0\n"
    "ds_read_b128 v[164:167], v59 offset:2048\n"
    "ds_read_b128 v[168:171], v59 offset:4096\n"
    "global_load_lds_dwordx4 v55, s[36:37] offset:3072\n"
    "ds_read_b128 v[172:175], v59 offset:6144\n"
    "ds_read_b64_tr_b16 v[176:177], v80 offset:8192\n"
    "ds_read_b64_tr_b16 v[178:179], v80 offset:9216\n"
    "global_load_dwordx3 v[36:38], v17, s[38:39] offset:-104\n"
    "ds_read_b64_tr_b16 v[180:181], v81 offset:8192\n"
    "ds_read_b64_tr_b16 v[182:183], v81 offset:9216\n"
    "ds_read_b64_tr_b16 v[184:185], v82 offset:8192\n"
    "ds_read_b64_tr_b16 v[186:187], v82 offset:9216\n"
    "ds_read_b64_tr_b16 v[188:189], v83 offset:8192\n"
    "ds_read_b64_tr_b16 v[190:191], v83 offset:9216\n"
    "s_waitcnt vmcnt(11) lgkmcnt(0)\n"
    "s_barrier\n"
    "v_mfma_f32_16x16x32_bf16 v[192:195], v[144:147], v[128:131], v[192:195]\n"
    "v_mfma_f32_16x16x32_bf16 v[192:195], v[176:179], v[160:163], v[192:195]\n"
    "v_lshlrev_b32 v100, 7, v100\n"
    "v_mfma_f32_16x16x32_bf16 v[196:199], v[176:179], v[164:167], v[196:199]\n"
    "v_mfma_f32_16x16x32_bf16 v[196:199], v[144:147], v[132:135], v[196:199]\n"
    "v_mfma_f32_16x16x32_bf16 v[200:203], v[144:147], v[136:139], v[200:203]\n"
    "v_lshlrev_b32 v108, 7, v108\n"
    "v_mfma_f32_16x16x32_bf16 v[200:203], v[176:179], v[168:171], v[200:203]\n"
    "v_mfma_f32_16x16x32_bf16 v[204:207], v[176:179], v[172:175], v[204:207]\n"
    "v_mfma_f32_16x16x32_bf16 v[204:207], v[144:147], v[140:143], v[204:207]\n"
    "v_mfma_f32_16x16x32_bf16 v[220:223], v[148:151], v[140:143], v[220:223]\n"
    "v_lshlrev_b32 v116, 7, v116\n"
    "v_mfma_f32_16x16x32_bf16 v[220:223], v[180:183], v[172:175], v[220:223]\n"
    "v_mfma_f32_16x16x32_bf16 v[216:219], v[180:183], v[168:171], v[216:219]\n"
    "v_mfma_f32_16x16x32_bf16 v[216:219], v[148:151], v[136:139], v[216:219]\n"
    "v_lshlrev_b32 v124, 7, v124\n"
    "v_mfma_f32_16x16x32_bf16 v[212:215], v[148:151], v[132:135], v[212:215]\n"
    "v_mfma_f32_16x16x32_bf16 v[212:215], v[180:183], v[164:167], v[212:215]\n"
    "v_mfma_f32_16x16x32_bf16 v[208:211], v[180:183], v[160:163], v[208:211]\n"
    "v_mfma_f32_16x16x32_bf16 v[208:211], v[148:151], v[128:131], v[208:211]\n"
    "v_add_u32_dpp v52, v97, v48 quad_perm:[3,3,3,3] row_mask:0xf bank_mask:0xf\n"
    "v_mfma_f32_16x16x32_bf16 v[224:227], v[152:155], v[128:131], v[224:227]\n"
    "v_mfma_f32_16x16x32_bf16 v[224:227], v[184:187], v[160:163], v[224:227]\n"
    "v_mfma_f32_16x16x32_bf16 v[228:231], v[184:187], v[164:167], v[228:231]\n"
    "v_mfma_f32_16x16x32_bf16 v[228:231], v[152:155], v[132:135], v[228:231]\n"
    "v_add_u32_dpp v53, v105, v49 quad_perm:[3,3,3,3] row_mask:0xf bank_mask:0xf\n"
    "v_mfma_f32_16x16x32_bf16 v[232:235], v[152:155], v[136:139], v[232:235]\n"
    "v_mfma_f32_16x16x32_bf16 v[232:235], v[184:187], v[168:171], v[232:235]\n"
    "v_mfma_f32_16x16x32_bf16 v[236:239], v[184:187], v[172:175], v[236:239]\n"
    "v_add_u32_dpp v54, v113, v50 quad_perm:[3,3,3,3] row_mask:0xf bank_mask:0xf\n"
    "v_mfma_f32_16x16x32_bf16 v[236:239], v[152:155], v[140:143], v[236:239]\n"
    "v_mfma_f32_16x16x32_bf16 v[252:255], v[156:159], v[140:143], v[252:255]\n"
    "v_mfma_f32_16x16x32_bf16 v[252:255], v[188:191], v[172:175], v[252:255]\n"
    "v_mfma_f32_16x16x32_bf16 v[248:251], v[188:191], v[168:171], v[248:251]\n"
    "v_add_u32_dpp v55, v121, v51 quad_perm:[3,3,3,3] row_mask:0xf bank_mask:0xf\n"
    "v_mfma_f32_16x16x32_bf16 v[248:251], v[156:159], v[136:139], v[248:251]\n"
    "v_mfma_f32_16x16x32_bf16 v[244:247], v[156:159], v[132:135], v[244:247]\n"
    "v_mfma_f32_16x16x32_bf16 v[244:247], v[188:191], v[164:167], v[244:247]\n"
    "v_mfma_f32_16x16x32_bf16 v[240:243], v[188:191], v[160:163], v[240:243]\n"
    "v_mfma_f32_16x16x32_bf16 v[240:243], v[156:159], v[128:131], v[240:243]\n"
    "s_waitcnt vmcnt(7)\n"
    "s_barrier\n"
    "s_add_u32 m0, s35, 98304\n"
    "s_nop 0\n"
    "global_load_lds_dwordx4 v46, s[30:31] offset:0\n"
    "ds_read_b128 v[128:131], v56 offset:0\n"
    "ds_read_b128 v[132:135], v56 offset:2048\n"
    "ds_read_b128 v[136:139], v56 offset:4096\n"
    "global_load_lds_dwordx4 v47, s[30:31] offset:1024\n"
    "s_add_u32 s30, s30, 212992\n"
    "s_addc_u32 s31, s31, 0\n"
    "ds_read_b128 v[140:143], v56 offset:6144\n"
    "ds_read_b64_tr_b16 v[144:145], v76 offset:0\n"
    "ds_read_b64_tr_b16 v[146:147], v76 offset:1024\n"
    "s_add_u32 m0, s34, 98304\n"
    "s_nop 0\n"
    "global_load_lds_dwordx4 v52, s[36:37] offset:0\n"
    "ds_read_b64_tr_b16 v[148:149], v77 offset:0\n"
    "ds_read_b64_tr_b16 v[150:151], v77 offset:1024\n"
    "ds_read_b64_tr_b16 v[152:153], v78 offset:0\n"
    "global_load_lds_dwordx4 v53, s[36:37] offset:1024\n"
    "ds_read_b64_tr_b16 v[154:155], v78 offset:1024\n"
    "ds_read_b64_tr_b16 v[156:157], v79 offset:0\n"
    "ds_read_b64_tr_b16 v[158:159], v79 offset:1024\n"
    "global_load_lds_dwordx4 v54, s[36:37] offset:2048\n"
    "ds_read_b128 v[160:163], v57 offset:0\n"
    "ds_read_b128 v[164:167], v57 offset:2048\n"
    "ds_read_b128 v[168:171], v57 offset:4096\n"
    "global_load_lds_dwordx4 v55, s[36:37] offset:3072\n"
    "ds_read_b128 v[172:175], v57 offset:6144\n"
    "ds_read_b64_tr_b16 v[176:177], v76 offset:8192\n"
    "ds_read_b64_tr_b16 v[178:179], v76 offset:9216\n"
    "global_load_dword v41, v18, s[38:39] offset:-8\n"
    "ds_read_b64_tr_b16 v[180:181], v77 offset:8192\n"
    "ds_read_b64_tr_b16 v[182:183], v77 offset:9216\n"
    "ds_read_b64_tr_b16 v[184:185], v78 offset:8192\n"
    "ds_read_b64_tr_b16 v[186:187], v78 offset:9216\n"
    "ds_read_b64_tr_b16 v[188:189], v79 offset:8192\n"
    "ds_read_b64_tr_b16 v[190:191], v79 offset:9216\n"
    "s_waitcnt vmcnt(12) lgkmcnt(0)\n"
    "s_barrier\n"
    "v_mfma_f32_16x16x32_bf16 v[192:195], v[144:147], v[128:131], v[192:195]\n"
    "v_mfma_f32_16x16x32_bf16 v[192:195], v[176:179], v[160:163], v[192:195]\n"
    "v_lshlrev_b32 v101, 7, v101\n"
    "v_mfma_f32_16x16x32_bf16 v[196:199], v[176:179], v[164:167], v[196:199]\n"
    "v_mfma_f32_16x16x32_bf16 v[196:199], v[144:147], v[132:135], v[196:199]\n"
    "v_mfma_f32_16x16x32_bf16 v[200:203], v[144:147], v[136:139], v[200:203]\n"
    "v_lshlrev_b32 v109, 7, v109\n"
    "v_mfma_f32_16x16x32_bf16 v[200:203], v[176:179], v[168:171], v[200:203]\n"
    "v_mfma_f32_16x16x32_bf16 v[204:207], v[176:179], v[172:175], v[204:207]\n"
    "v_mfma_f32_16x16x32_bf16 v[204:207], v[144:147], v[140:143], v[204:207]\n"
    "v_mfma_f32_16x16x32_bf16 v[220:223], v[148:151], v[140:143], v[220:223]\n"
    "v_lshlrev_b32 v117, 7, v117\n"
    "v_mfma_f32_16x16x32_bf16 v[220:223], v[180:183], v[172:175], v[220:223]\n"
    "v_mfma_f32_16x16x32_bf16 v[216:219], v[180:183], v[168:171], v[216:219]\n"
    "v_mfma_f32_16x16x32_bf16 v[216:219], v[148:151], v[136:139], v[216:219]\n"
    "v_lshlrev_b32 v125, 7, v125\n"
    "v_mfma_f32_16x16x32_bf16 v[212:215], v[148:151], v[132:135], v[212:215]\n"
    "v_mfma_f32_16x16x32_bf16 v[212:215], v[180:183], v[164:167], v[212:215]\n"
    "v_mfma_f32_16x16x32_bf16 v[208:211], v[180:183], v[160:163], v[208:211]\n"
    "v_mfma_f32_16x16x32_bf16 v[208:211], v[148:151], v[128:131], v[208:211]\n"
    "v_add_u32_dpp v52, v98, v48 quad_perm:[3,3,3,3] row_mask:0xf bank_mask:0xf\n"
    "v_mfma_f32_16x16x32_bf16 v[224:227], v[152:155], v[128:131], v[224:227]\n"
    "v_mfma_f32_16x16x32_bf16 v[224:227], v[184:187], v[160:163], v[224:227]\n"
    "v_mfma_f32_16x16x32_bf16 v[228:231], v[184:187], v[164:167], v[228:231]\n"
    "v_mfma_f32_16x16x32_bf16 v[228:231], v[152:155], v[132:135], v[228:231]\n"
    "v_add_u32_dpp v53, v106, v49 quad_perm:[3,3,3,3] row_mask:0xf bank_mask:0xf\n"
    "v_mfma_f32_16x16x32_bf16 v[232:235], v[152:155], v[136:139], v[232:235]\n"
    "v_mfma_f32_16x16x32_bf16 v[232:235], v[184:187], v[168:171], v[232:235]\n"
    "v_mfma_f32_16x16x32_bf16 v[236:239], v[184:187], v[172:175], v[236:239]\n"
    "v_add_u32_dpp v54, v114, v50 quad_perm:[3,3,3,3] row_mask:0xf bank_mask:0xf\n"
    "v_mfma_f32_16x16x32_bf16 v[236:239], v[152:155], v[140:143], v[236:239]\n"
    "v_mfma_f32_16x16x32_bf16 v[252:255], v[156:159], v[140:143], v[252:255]\n"
    "v_mfma_f32_16x16x32_bf16 v[252:255], v[188:191], v[172:175], v[252:255]\n"
    "v_mfma_f32_16x16x32_bf16 v[248:251], v[188:191], v[168:171], v[248:251]\n"
    "v_add_u32_dpp v55, v122, v51 quad_perm:[3,3,3,3] row_mask:0xf bank_mask:0xf\n"
    "v_mfma_f32_16x16x32_bf16 v[248:251], v[156:159], v[136:139], v[248:251]\n"
    "v_mfma_f32_16x16x32_bf16 v[244:247], v[156:159], v[132:135], v[244:247]\n"
    "v_mfma_f32_16x16x32_bf16 v[244:247], v[188:191], v[164:167], v[244:247]\n"
    "v_mfma_f32_16x16x32_bf16 v[240:243], v[188:191], v[160:163], v[240:243]\n"
    "v_mfma_f32_16x16x32_bf16 v[240:243], v[156:159], v[128:131], v[240:243]\n"
    "s_waitcnt vmcnt(8)\n"
    "s_barrier\n"
    "s_add_u32 m0, s35, 0\n"
    "s_nop 0\n"
    "global_load_lds_dwordx4 v46, s[30:31] offset:0\n"
    "ds_read_b128 v[128:131], v56 offset:49152\n"
    "ds_read_b128 v[132:135], v56 offset:51200\n"
    "ds_read_b128 v[136:139], v56 offset:53248\n"
    "ds_read_b128 v[140:143], v56 offset:55296\n"
    "global_load_lds_dwordx4 v47, s[30:31] offset:1024\n"
    "s_add_u32 s30, s30, 212992\n"
    "s_addc_u32 s31, s31, 0\n"
    "ds_read_b64_tr_b16 v[144:145], v76 offset:49152\n"
    "ds_read_b64_tr_b16 v[146:147], v76 offset:50176\n"
    "ds_read_b64_tr_b16 v[148:149], v77 offset:49152\n"
    "ds_read_b64_tr_b16 v[150:151], v77 offset:50176\n"
    "s_add_u32 m0, s34, 0\n"
    "s_nop 0\n"
    "global_load_lds_dwordx4 v52, s[36:37] offset:0\n"
    "ds_read_b64_tr_b16 v[152:153], v78 offset:49152\n"
    "ds_read_b64_tr_b16 v[154:155], v78 offset:50176\n"
    "ds_read_b64_tr_b16 v[156:157], v79 offset:49152\n"
    "ds_read_b64_tr_b16 v[158:159], v79 offset:50176\n"
    "global_load_lds_dwordx4 v53, s[36:37] offset:1024\n"
    "ds_read_b128 v[160:163], v57 offset:49152\n"
    "ds_read_b128 v[164:167], v57 offset:51200\n"
    "ds_read_b128 v[168:171], v57 offset:53248\n"
    "ds_read_b128 v[172:175], v57 offset:55296\n"
    "global_load_lds_dwordx4 v54, s[36:37] offset:2048\n"
    "ds_read_b64_tr_b16 v[176:177], v76 offset:57344\n"
    "ds_read_b64_tr_b16 v[178:179], v76 offset:58368\n"
    "ds_read_b64_tr_b16 v[180:181], v77 offset:57344\n"
    "ds_read_b64_tr_b16 v[182:183], v77 offset:58368\n"
    "global_load_lds_dwordx4 v55, s[36:37] offset:3072\n"
    "ds_read_b64_tr_b16 v[184:185], v78 offset:57344\n"
    "ds_read_b64_tr_b16 v[186:187], v78 offset:58368\n"
    "ds_read_b64_tr_b16 v[188:189], v79 offset:57344\n"
    "ds_read_b64_tr_b16 v[190:191], v79 offset:58368\n"
    "s_waitcnt vmcnt(11) lgkmcnt(0)\n"
    "s_barrier\n"
    "v_mfma_f32_16x16x32_bf16 v[192:195], v[144:147], v[128:131], v[192:195]\n"
    "v_mfma_f32_16x16x32_bf16 v[192:195], v[176:179], v[160:163], v[192:195]\n"
    "v_lshlrev_b32 v102, 7, v102\n"
    "v_mfma_f32_16x16x32_bf16 v[196:199], v[176:179], v[164:167], v[196:199]\n"
    "v_mfma_f32_16x16x32_bf16 v[196:199], v[144:147], v[132:135], v[196:199]\n"
    "v_mfma_f32_16x16x32_bf16 v[200:203], v[144:147], v[136:139], v[200:203]\n"
    "v_lshlrev_b32 v110, 7, v110\n"
    "v_mfma_f32_16x16x32_bf16 v[200:203], v[176:179], v[168:171], v[200:203]\n"
    "v_mfma_f32_16x16x32_bf16 v[204:207], v[176:179], v[172:175], v[204:207]\n"
    "v_mfma_f32_16x16x32_bf16 v[204:207], v[144:147], v[140:143], v[204:207]\n"
    "v_mfma_f32_16x16x32_bf16 v[220:223], v[148:151], v[140:143], v[220:223]\n"
    "v_lshlrev_b32 v118, 7, v118\n"
    "v_mfma_f32_16x16x32_bf16 v[220:223], v[180:183], v[172:175], v[220:223]\n"
    "v_mfma_f32_16x16x32_bf16 v[216:219], v[180:183], v[168:171], v[216:219]\n"
    "v_mfma_f32_16x16x32_bf16 v[216:219], v[148:151], v[136:139], v[216:219]\n"
    "v_lshlrev_b32 v126, 7, v126\n"
    "v_mfma_f32_16x16x32_bf16 v[212:215], v[148:151], v[132:135], v[212:215]\n"
    "v_mfma_f32_16x16x32_bf16 v[212:215], v[180:183], v[164:167], v[212:215]\n"
    "v_mfma_f32_16x16x32_bf16 v[208:211], v[180:183], v[160:163], v[208:211]\n"
    "v_mfma_f32_16x16x32_bf16 v[208:211], v[148:151], v[128:131], v[208:211]\n"
    "v_add_u32_dpp v52, v99, v48 quad_perm:[3,3,3,3] row_mask:0xf bank_mask:0xf\n"
    "v_mfma_f32_16x16x32_bf16 v[224:227], v[152:155], v[128:131], v[224:227]\n"
    "v_mfma_f32_16x16x32_bf16 v[224:227], v[184:187], v[160:163], v[224:227]\n"
    "v_mfma_f32_16x16x32_bf16 v[228:231], v[184:187], v[164:167], v[228:231]\n"
    "v_mfma_f32_16x16x32_bf16 v[228:231], v[152:155], v[132:135], v[228:231]\n"
    "v_add_u32_dpp v53, v107, v49 quad_perm:[3,3,3,3] row_mask:0xf bank_mask:0xf\n"
    "v_mfma_f32_16x16x32_bf16 v[232:235], v[152:155], v[136:139], v[232:235]\n"
    "v_mfma_f32_16x16x32_bf16 v[232:235], v[184:187], v[168:171], v[232:235]\n"
    "v_mfma_f32_16x16x32_bf16 v[236:239], v[184:187], v[172:175], v[236:239]\n"
    "v_add_u32_dpp v54, v115, v50 quad_perm:[3,3,3,3] row_mask:0xf bank_mask:0xf\n"
    "v_mfma_f32_16x16x32_bf16 v[236:239], v[152:155], v[140:143], v[236:239]\n"
    "v_mfma_f32_16x16x32_bf16 v[252:255], v[156:159], v[140:143], v[252:255]\n"
    "v_mfma_f32_16x16x32_bf16 v[252:255], v[188:191], v[172:175], v[252:255]\n"
    "v_mfma_f32_16x16x32_bf16 v[248:251], v[188:191], v[168:171], v[248:251]\n"
    "v_add_u32_dpp v55, v123, v51 quad_perm:[3,3,3,3] row_mask:0xf bank_mask:0xf\n"
    "v_mfma_f32_16x16x32_bf16 v[248:251], v[156:159], v[136:139], v[248:251]\n"
    "v_mfma_f32_16x16x32_bf16 v[244:247], v[156:159], v[132:135], v[244:247]\n"
    "v_mfma_f32_16x16x32_bf16 v[244:247], v[188:191], v[164:167], v[244:247]\n"
    "v_mfma_f32_16x16x32_bf16 v[240:243], v[188:191], v[160:163], v[240:243]\n"
    "v_mfma_f32_16x16x32_bf16 v[240:243], v[156:159], v[128:131], v[240:243]\n"
    "s_waitcnt vmcnt(7)\n"
    "s_barrier\n"
    "s_add_u32 m0, s35, 49152\n"
    "s_nop 0\n"
    "global_load_lds_dwordx4 v46, s[30:31] offset:0\n"
    "ds_read_b128 v[128:131], v58 offset:0\n"
    "ds_read_b128 v[132:135], v58 offset:2048\n"
    "ds_read_b128 v[136:139], v58 offset:4096\n"
    "ds_read_b128 v[140:143], v58 offset:6144\n"
    "global_load_lds_dwordx4 v47, s[30:31] offset:1024\n"
    "s_bfe_u32 s66, s29, 0x80000\n"
    "s_mul_i32 s66, s66, 53248\n"
    "s_add_u32 s30, s6, s66\n"
    "s_addc_u32 s31, s7, 0\n"
    "ds_read_b64_tr_b16 v[144:145], v80 offset:0\n"
    "ds_read_b64_tr_b16 v[146:147], v80 offset:1024\n"
    "ds_read_b64_tr_b16 v[148:149], v81 offset:0\n"
    "ds_read_b64_tr_b16 v[150:151], v81 offset:1024\n"
    "s_add_u32 m0, s34, 49152\n"
    "s_nop 0\n"
    "global_load_lds_dwordx4 v52, s[36:37] offset:0\n"
    "ds_read_b64_tr_b16 v[152:153], v82 offset:0\n"
    "ds_read_b64_tr_b16 v[154:155], v82 offset:1024\n"
    "ds_read_b64_tr_b16 v[156:157], v83 offset:0\n"
    "ds_read_b64_tr_b16 v[158:159], v83 offset:1024\n"
    "global_load_lds_dwordx4 v53, s[36:37] offset:1024\n"
    "ds_read_b128 v[160:163], v59 offset:0\n"
    "ds_read_b128 v[164:167], v59 offset:2048\n"
    "ds_read_b128 v[168:171], v59 offset:4096\n"
    "ds_read_b128 v[172:175], v59 offset:6144\n"
    "global_load_lds_dwordx4 v54, s[36:37] offset:2048\n"
    "ds_read_b64_tr_b16 v[176:177], v80 offset:8192\n"
    "ds_read_b64_tr_b16 v[178:179], v80 offset:9216\n"
    "ds_read_b64_tr_b16 v[180:181], v81 offset:8192\n"
    "ds_read_b64_tr_b16 v[182:183], v81 offset:9216\n"
    "global_load_lds_dwordx4 v55, s[36:37] offset:3072\n"
    "ds_read_b64_tr_b16 v[184:185], v82 offset:8192\n"
    "ds_read_b64_tr_b16 v[186:187], v82 offset:9216\n"
    "ds_read_b64_tr_b16 v[188:189], v83 offset:8192\n"
    "ds_read_b64_tr_b16 v[190:191], v83 offset:9216\n"
    "s_waitcnt vmcnt(10) lgkmcnt(0)\n"
    "s_barrier\n"
    "v_mfma_f32_16x16x32_bf16 v[192:195], v[144:147], v[128:131], v[192:195]\n"
    "v_mfma_f32_16x16x32_bf16 v[192:195], v[176:179], v[160:163], v[192:195]\n"
    "v_add_u32_dpp v52, v100, v48 quad_perm:[0,0,0,0] row_mask:0xf bank_mask:0xf\n"
    "v_mfma_f32_16x16x32_bf16 v[196:199], v[176:179], v[164:167], v[196:199]\n"
    "v_mfma_f32_16x16x32_bf16 v[196:199], v[144:147], v[132:135], v[196:199]\n"
    "v_mfma_f32_16x16x32_bf16 v[200:203], v[144:147], v[136:139], v[200:203]\n"
    "v_add_u32_dpp v53, v108, v49 quad_perm:[0,0,0,0] row_mask:0xf bank_mask:0xf\n"
    "v_mfma_f32_16x16x32_bf16 v[200:203], v[176:179], v[168:171], v[200:203]\n"
    "v_mfma_f32_16x16x32_bf16 v[204:207], v[176:179], v[172:175], v[204:207]\n"
    "v_mfma_f32_16x16x32_bf16 v[204:207], v[144:147], v[140:143], v[204:207]\n"
    "v_mfma_f32_16x16x32_bf16 v[220:223], v[148:151], v[140:143], v[220:223]\n"
    "v_add_u32_dpp v54, v116, v50 quad_perm:[0,0,0,0] row_mask:0xf bank_mask:0xf\n"
    "v_mfma_f32_16x16x32_bf16 v[220:223], v[180:183], v[172:175], v[220:223]\n"
    "v_mfma_f32_16x16x32_bf16 v[216:219], v[180:183], v[168:171], v[216:219]\n"
    "v_mfma_f32_16x16x32_bf16 v[216:219], v[148:151], v[136:139], v[216:219]\n"
    "v_add_u32_dpp v55, v124, v51 quad_perm:[0,0,0,0] row_mask:0xf bank_mask:0xf\n"
    "v_mfma_f32_16x16x32_bf16 v[212:215], v[148:151], v[132:135], v[212:215]\n"
    "v_mfma_f32_16x16x32_bf16 v[212:215], v[180:183], v[164:167], v[212:215]\n"
    "v_mfma_f32_16x16x32_bf16 v[208:211], v[180:183], v[160:163], v[208:211]\n"
    "v_mfma_f32_16x16x32_bf16 v[208:211], v[148:151], v[128:131], v[208:211]\n"
    "v_lshl_or_b32 v36, v36, 7, v19\n"
    "v_mfma_f32_16x16x32_bf16 v[224:227], v[152:155], v[128:131], v[224:227]\n"
    "v_mfma_f32_16x16x32_bf16 v[224:227], v[184:187], v[160:163], v[224:227]\n"
    "v_mfma_f32_16x16x32_bf16 v[228:231], v[184:187], v[164:167], v[228:231]\n"
    "v_mfma_f32_16x16x32_bf16 v[228:231], v[152:155], v[132:135], v[228:231]\n"
    "v_lshl_or_b32 v37, v37, 7, v19\n"
    "v_mfma_f32_16x16x32_bf16 v[232:235], v[152:155], v[136:139], v[232:235]\n"
    "v_mfma_f32_16x16x32_bf16 v[232:235], v[184:187], v[168:171], v[232:235]\n"
    "v_mfma_f32_16x16x32_bf16 v[236:239], v[184:187], v[172:175], v[236:239]\n"
    "v_lshl_or_b32 v38, v38, 7, v19\n"
    "v_mfma_f32_16x16x32_bf16 v[236:239], v[152:155], v[140:143], v[236:239]\n"
    "v_mfma_f32_16x16x32_bf16 v[252:255], v[156:159], v[140:143], v[252:255]\n"
    "v_mfma_f32_16x16x32_bf16 v[252:255], v[188:191], v[172:175], v[252:255]\n"
    "v_mfma_f32_16x16x32_bf16 v[248:251], v[188:191], v[168:171], v[248:251]\n"
    "v_lshl_or_b32 v41, v41, 7, v20\n"
    "v_mfma_f32_16x16x32_bf16 v[248:251], v[156:159], v[136:139], v[248:251]\n"
    "v_mfma_f32_16x16x32_bf16 v[244:247], v[156:159], v[132:135], v[244:247]\n"
    "v_mfma_f32_16x16x32_bf16 v[244:247], v[188:191], v[164:167], v[244:247]\n"
    "v_mfma_f32_16x16x32_bf16 v[240:243], v[188:191], v[160:163], v[240:243]\n"
    "v_mfma_f32_16x16x32_bf16 v[240:243], v[156:159], v[128:131], v[240:243]\n"
    "s_waitcnt vmcnt(6)\n"
    "s_barrier\n"
    "s_add_u32 m0, s35, 98304\n"
    "s_nop 0\n"
    "global_load_lds_dwordx4 v46, s[30:31] offset:0\n"
    "ds_read_b128 v[128:131], v56 offset:0\n"
    "ds_read_b128 v[132:135], v56 offset:2048\n"
    "ds_read_b128 v[136:139], v56 offset:4096\n"
    "global_load_lds_dwordx4 v47, s[30:31] offset:1024\n"
    "s_add_u32 s30, s30, 212992\n"
    "s_addc_u32 s31, s31, 0\n"
    "ds_read_b128 v[140:143], v56 offset:6144\n"
    "ds_read_b64_tr_b16 v[144:145], v76 offset:0\n"
    "ds_read_b64_tr_b16 v[146:147], v76 offset:1024\n"
    "s_add_u32 m0, s34, 98304\n"
    "s_nop 0\n"
    "global_load_lds_dwordx4 v52, s[36:37] offset:0\n"
    "ds_read_b64_tr_b16 v[148:149], v77 offset:0\n"
    "ds_read_b64_tr_b16 v[150:151], v77 offset:1024\n"
    "ds_read_b64_tr_b16 v[152:153], v78 offset:0\n"
    "global_load_lds_dwordx4 v53, s[36:37] offset:1024\n"
    "ds_read_b64_tr_b16 v[154:155], v78 offset:1024\n"
    "ds_read_b64_tr_b16 v[156:157], v79 offset:0\n"
    "ds_read_b64_tr_b16 v[158:159], v79 offset:1024\n"
    "global_load_lds_dwordx4 v54, s[36:37] offset:2048\n"
    "ds_read_b128 v[160:163], v57 offset:0\n"
    "ds_read_b128 v[164:167], v57 offset:2048\n"
    "ds_read_b128 v[168:171], v57 offset:4096\n"
    "global_load_lds_dwordx4 v55, s[36:37] offset:3072\n"
    "ds_read_b128 v[172:175], v57 offset:6144\n"
    "ds_read_b64_tr_b16 v[176:177], v76 offset:8192\n"
    "ds_read_b64_tr_b16 v[178:179], v76 offset:9216\n"
    "global_load_dwordx4 v[28:31], v36, s[8:9]\n"
    "ds_read_b64_tr_b16 v[180:181], v77 offset:8192\n"
    "ds_read_b64_tr_b16 v[182:183], v77 offset:9216\n"
    "ds_read_b64_tr_b16 v[184:185], v78 offset:8192\n"
    "ds_read_b64_tr_b16 v[186:187], v78 offset:9216\n"
    "ds_read_b64_tr_b16 v[188:189], v79 offset:8192\n"
    "ds_read_b64_tr_b16 v[190:191], v79 offset:9216\n"
    "s_waitcnt vmcnt(11) lgkmcnt(0)\n"
    "s_barrier\n"
    "v_mfma_f32_16x16x32_bf16 v[192:195], v[144:147], v[128:131], v[192:195]\n"
    "v_mfma_f32_16x16x32_bf16 v[192:195], v[176:179], v[160:163], v[192:195]\n"
    "v_lshlrev_b32 v103, 7, v103\n"
    "v_mfma_f32_16x16x32_bf16 v[196:199], v[176:179], v[164:167], v[196:199]\n"
    "v_mfma_f32_16x16x32_bf16 v[196:199], v[144:147], v[132:135], v[196:199]\n"
    "v_mfma_f32_16x16x32_bf16 v[200:203], v[144:147], v[136:139], v[200:203]\n"
    "v_lshlrev_b32 v111, 7, v111\n"
    "v_mfma_f32_16x16x32_bf16 v[200:203], v[176:179], v[168:171], v[200:203]\n"
    "v_mfma_f32_16x16x32_bf16 v[204:207], v[176:179], v[172:175], v[204:207]\n"
    "v_mfma_f32_16x16x32_bf16 v[204:207], v[144:147], v[140:143], v[204:207]\n"
    "v_mfma_f32_16x16x32_bf16 v[220:223], v[148:151], v[140:143], v[220:223]\n"
    "v_lshlrev_b32 v119, 7, v119\n"
    "v_mfma_f32_16x16x32_bf16 v[220:223], v[180:183], v[172:175], v[220:223]\n"
    "v_mfma_f32_16x16x32_bf16 v[216:219], v[180:183], v[168:171], v[216:219]\n"
    "v_mfma_f32_16x16x32_bf16 v[216:219], v[148:151], v[136:139], v[216:219]\n"
    "v_lshlrev_b32 v127, 7, v127\n"
    "v_mfma_f32_16x16x32_bf16 v[212:215], v[148:151], v[132:135], v[212:215]\n"
    "v_mfma_f32_16x16x32_bf16 v[212:215], v[180:183], v[164:167], v[212:215]\n"
    "v_mfma_f32_16x16x32_bf16 v[208:211], v[180:183], v[160:163], v[208:211]\n"
    "v_mfma_f32_16x16x32_bf16 v[208:211], v[148:151], v[128:131], v[208:211]\n"
    "v_add_u32_dpp v52, v101, v48 quad_perm:[0,0,0,0] row_mask:0xf bank_mask:0xf\n"
    "v_mfma_f32_16x16x32_bf16 v[224:227], v[152:155], v[128:131], v[224:227]\n"
    "v_mfma_f32_16x16x32_bf16 v[224:227], v[184:187], v[160:163], v[224:227]\n"
    "v_mfma_f32_16x16x32_bf16 v[228:231], v[184:187], v[164:167], v[228:231]\n"
    "v_mfma_f32_16x16x32_bf16 v[228:231], v[152:155], v[132:135], v[228:231]\n"
    "v_add_u32_dpp v53, v109, v49 quad_perm:[0,0,0,0] row_mask:0xf bank_mask:0xf\n"
    "v_mfma_f32_16x16x32_bf16 v[232:235], v[152:155], v[136:139], v[232:235]\n"
    "v_mfma_f32_16x16x32_bf16 v[232:235], v[184:187], v[168:171], v[232:235]\n"
    "v_mfma_f32_16x16x32_bf16 v[236:239], v[184:187], v[172:175], v[236:239]\n"
    "v_add_u32_dpp v54, v117, v50 quad_perm:[0,0,0,0] row_mask:0xf bank_mask:0xf\n"
    "v_mfma_f32_16x16x32_bf16 v[236:239], v[152:155], v[140:143], v[236:239]\n"
    "v_mfma_f32_16x16x32_bf16 v[252:255], v[156:159], v[140:143], v[252:255]\n"
    "v_mfma_f32_16x16x32_bf16 v[252:255], v[188:191], v[172:175], v[252:255]\n"
    "v_mfma_f32_16x16x32_bf16 v[248:251], v[188:191], v[168:171], v[248:251]\n"
    "v_add_u32_dpp v55, v125, v51 quad_perm:[0,0,0,0] row_mask:0xf bank_mask:0xf\n"
    "v_mfma_f32_16x16x32_bf16 v[248:251], v[156:159], v[136:139], v[248:251]\n"
    "v_mfma_f32_16x16x32_bf16 v[244:247], v[156:159], v[132:135], v[244:247]\n"
    "v_mfma_f32_16x16x32_bf16 v[244:247], v[188:191], v[164:167], v[244:247]\n"
    "v_mfma_f32_16x16x32_bf16 v[240:243], v[188:191], v[160:163], v[240:243]\n"
    "v_mfma_f32_16x16x32_bf16 v[240:243], v[156:159], v[128:131], v[240:243]\n"
    "s_waitcnt vmcnt(7)\n"
    "s_barrier\n"
    "s_add_u32 m0, s35, 0\n"
    "s_nop 0\n"
    "global_load_lds_dwordx4 v46, s[30:31] offset:0\n"
    "ds_read_b128 v[128:131], v56 offset:49152\n"
    "ds_read_b128 v[132:135], v56 offset:51200\n"
    "ds_read_b128 v[136:139], v56 offset:53248\n"
    "global_load_lds_dwordx4 v47, s[30:31] offset:1024\n"
    "s_add_u32 s30, s30, 212992\n"
    "s_addc_u32 s31, s31, 0\n"
    "ds_read_b128 v[140:143], v56 offset:55296\n"
    "ds_read_b64_tr_b16 v[144:145], v76 offset:49152\n"
    "ds_read_b64_tr_b16 v[146:147], v76 offset:50176\n"
    "s_add_u32 m0, s34, 0\n"
    "s_nop 0\n"
    "global_load_lds_dwordx4 v52, s[36:37] offset:0\n"
    "ds_read_b64_tr_b16 v[148:149], v77 offset:49152\n"
    "ds_read_b64_tr_b16 v[150:151], v77 offset:50176\n"
    "ds_read_b64_tr_b16 v[152:153], v78 offset:49152\n"
    "global_load_lds_dwordx4 v53, s[36:37] offset:1024\n"
    "ds_read_b64_tr_b16 v[154:155], v78 offset:50176\n"
    "ds_read_b64_tr_b16 v[156:157], v79 offset:49152\n"
    "ds_read_b64_tr_b16 v[158:159], v79 offset:50176\n"
    "global_load_lds_dwordx4 v54, s[36:37] offset:2048\n"
    "ds_read_b128 v[160:163], v57 offset:49152\n"
    "ds_read_b128 v[164:167], v57 offset:51200\n"
    "ds_read_b128 v[168:171], v57 offset:53248\n"
    "global_load_lds_dwordx4 v55, s[36:37] offset:3072\n"
    "ds_read_b128 v[172:175], v57 offset:55296\n"
    "ds_read_b64_tr_b16 v[176:177], v76 offset:57344\n"
    "ds_read_b64_tr_b16 v[178:179], v76 offset:58368\n"
    "global_load_dwordx4 v[32:35], v37, s[8:9]\n"
    "ds_read_b64_tr_b16 v[180:181], v77 offset:57344\n"
    "ds_read_b64_tr_b16 v[182:183], v77 offset:58368\n"
    "ds_read_b64_tr_b16 v[184:185], v78 offset:57344\n"
    "ds_read_b64_tr_b16 v[186:187], v78 offset:58368\n"
    "ds_read_b64_tr_b16 v[188:189], v79 offset:57344\n"
    "ds_read_b64_tr_b16 v[190:191], v79 offset:58368\n"
    "s_waitcnt vmcnt(12) lgkmcnt(0)\n"
    "s_barrier\n"
    "v_mfma_f32_16x16x32_bf16 v[192:195], v[144:147], v[128:131], v[192:195]\n"
    "v_mfma_f32_16x16x32_bf16 v[192:195], v[176:179], v[160:163], v[192:195]\n"
    "v_add_u32_dpp v52, v102, v48 quad_perm:[0,0,0,0] row_mask:0xf bank_mask:0xf\n"
    "v_mfma_f32_16x16x32_bf16 v[196:199], v[176:179], v[164:167], v[196:199]\n"
    "v_mfma_f32_16x16x32_bf16 v[196:199], v[144:147], v[132:135], v[196:199]\n"
    "v_mfma_f32_16x16x32_bf16 v[200:203], v[144:147], v[136:139], v[200:203]\n"
    "v_mfma_f32_16x16x32_bf16 v[200:203], v[176:179], v[168:171], v[200:203]\n"
    "v_mfma_f32_16x16x32_bf16 v[204:207], v[176:179], v[172:175], v[204:207]\n"
    "v_mfma_f32_16x16x32_bf16 v[204:207], v[144:147], v[140:143], v[204:207]\n"
    "v_mfma_f32_16x16x32_bf16 v[220:223], v[148:151], v[140:143], v[220:223]\n"
    "v_add_u32_dpp v53, v110, v49 quad_perm:[0,0,0,0] row_mask:0xf bank_mask:0xf\n"
    "v_mfma_f32_16x16x32_bf16 v[220:223], v[180:183], v[172:175], v[220:223]\n"
    "v_mfma_f32_16x16x32_bf16 v[216:219], v[180:183], v[168:171], v[216:219]\n"
    "v_mfma_f32_16x16x32_bf16 v[216:219], v[148:151], v[136:139], v[216:219]\n"
    "v_mfma_f32_16x16x32_bf16 v[212:215], v[148:151], v[132:135], v[212:215]\n"
    "v_mfma_f32_16x16x32_bf16 v[212:215], v[180:183], v[164:167], v[212:215]\n"
    "v_mfma_f32_16x16x32_bf16 v[208:211], v[180:183], v[160:163], v[208:211]\n"
    "v_mfma_f32_16x16x32_bf16 v[208:211], v[148:151], v[128:131], v[208:211]\n"
    "v_add_u32_dpp v54, v118, v50 quad_perm:[0,0,0,0] row_mask:0xf bank_mask:0xf\n"
    "v_mfma_f32_16x16x32_bf16 v[224:227], v[152:155], v[128:131], v[224:227]\n"
    "v_mfma_f32_16x16x32_bf16 v[224:227], v[184:187], v[160:163], v[224:227]\n"
    "v_mfma_f32_16x16x32_bf16 v[228:231], v[184:187], v[164:167], v[228:231]\n"
    "v_mfma_f32_16x16x32_bf16 v[228:231], v[152:155], v[132:135], v[228:231]\n"
    "v_mfma_f32_16x16x32_bf16 v[232:235], v[152:155], v[136:139], v[232:235]\n"
    "v_mfma_f32_16x16x32_bf16 v[232:235], v[184:187], v[168:171], v[232:235]\n"
    "v_mfma_f32_16x16x32_bf16 v[236:239], v[184:187], v[172:175], v[236:239]\n"
    "v_add_u32_dpp v55, v126, v51 quad_perm:[0,0,0,0] row_mask:0xf bank_mask:0xf\n"
    "v_mfma_f32_16x16x32_bf16 v[236:239], v[152:155], v[140:143], v[236:239]\n"
    "v_mfma_f32_16x16x32_bf16 v[252:255], v[156:159], v[140:143], v[252:255]\n"
    "v_mfma_f32_16x16x32_bf16 v[252:255], v[188:191], v[172:175], v[252:255]\n"
    "v_mfma_f32_16x16x32_bf16 v[248:251], v[188:191], v[168:171], v[248:251]\n"
    "v_mfma_f32_16x16x32_bf16 v[248:251], v[156:159], v[136:139], v[248:251]\n"
    "v_mfma_f32_16x16x32_bf16 v[244:247], v[156:159], v[132:135], v[244:247]\n"
    "v_mfma_f32_16x16x32_bf16 v[244:247], v[188:191], v[164:167], v[244:247]\n"
    "v_mfma_f32_16x16x32_bf16 v[240:243], v[188:191], v[160:163], v[240:243]\n"
    "v_mfma_f32_16x16x32_bf16 v[240:243], v[156:159], v[128:131], v[240:243]\n"
    "s_waitcnt vmcnt(8)\n"
    "s_barrier\n"
    "s_add_u32 m0, s35, 49152\n"
    "s_nop 0\n"
    "global_load_lds_dwordx4 v46, s[30:31] offset:0\n"
    "ds_read_b128 v[128:131], v58 offset:0\n"
    "ds_read_b128 v[132:135], v58 offset:2048\n"
    "ds_read_b128 v[136:139], v58 offset:4096\n"
    "global_load_lds_dwordx4 v47, s[30:31] offset:1024\n"
    "s_add_u32 s30, s30, 212992\n"
    "s_addc_u32 s31, s31, 0\n"
    "ds_read_b128 v[140:143], v58 offset:6144\n"
    "ds_read_b64_tr_b16 v[144:145], v80 offset:0\n"
    "ds_read_b64_tr_b16 v[146:147], v80 offset:1024\n"
    "s_add_u32 m0, s34, 49152\n"
    "s_nop 0\n"
    "global_load_lds_dwordx4 v52, s[36:37] offset:0\n"
    "ds_read_b64_tr_b16 v[148:149], v81 offset:0\n"
    "ds_read_b64_tr_b16 v[150:151], v81 offset:1024\n"
    "ds_read_b64_tr_b16 v[152:153], v82 offset:0\n"
    "global_load_lds_dwordx4 v53, s[36:37] offset:1024\n"
    "ds_read_b64_tr_b16 v[154:155], v82 offset:1024\n"
    "ds_read_b64_tr_b16 v[156:157], v83 offset:0\n"
    "ds_read_b64_tr_b16 v[158:159], v83 offset:1024\n"
    "global_load_lds_dwordx4 v54, s[36:37] offset:2048\n"
    "ds_read_b128 v[160:163], v59 offset:0\n"
    "ds_read_b128 v[164:167], v59 offset:2048\n"
    "ds_read_b128 v[168:171], v59 offset:4096\n"
    "global_load_lds_dwordx4 v55, s[36:37] offset:3072\n"
    "ds_read_b128 v[172:175], v59 offset:6144\n"
    "ds_read_b64_tr_b16 v[176:177], v80 offset:8192\n"
    "ds_read_b64_tr_b16 v[178:179], v80 offset:9216\n"
    "global_load_dword v40, v41, s[8:9]\n"
    "ds_read_b64_tr_b16 v[180:181], v81 offset:8192\n"
    "ds_read_b64_tr_b16 v[182:183], v81 offset:9216\n"
    "ds_read_b64_tr_b16 v[184:185], v82 offset:8192\n"
    "ds_read_b64_tr_b16 v[186:187], v82 offset:9216\n"
    "ds_read_b64_tr_b16 v[188:189], v83 offset:8192\n"
    "ds_read_b64_tr_b16 v[190:191], v83 offset:9216\n"
    "s_waitcnt vmcnt(12) lgkmcnt(0)\n"
    "s_barrier\n"
    "v_mfma_f32_16x16x32_bf16 v[192:195], v[144:147], v[128:131], v[192:195]\n"
    "v_mfma_f32_16x16x32_bf16 v[192:195], v[176:179], v[160:163], v[192:195]\n"
    "v_add_u32_dpp v52, v103, v48 quad_perm:[0,0,0,0] row_mask:0xf bank_mask:0xf\n"
    "v_mfma_f32_16x16x32_bf16 v[196:199], v[176:179], v[164:167], v[196:199]\n"
    "v_mfma_f32_16x16x32_bf16 v[196:199], v[144:147], v[132:135], v[196:199]\n"
    "v_mfma_f32_16x16x32_bf16 v[200:203], v[144:147], v[136:139], v[200:203]\n"
    "v_mfma_f32_16x16x32_bf16 v[200:203], v[176:179], v[168:171], v[200:203]\n"
    "v_mfma_f32_16x16x32_bf16 v[204:207], v[176:179], v[172:175], v[204:207]\n"
    "v_mfma_f32_16x16x32_bf16 v[204:207], v[144:147], v[140:143], v[204:207]\n"
    "v_mfma_f32_16x16x32_bf16 v[220:223], v[148:151], v[140:143], v[220:223]\n"
    "v_add_u32_dpp v53, v111, v49 quad_perm:[0,0,0,0] row_mask:0xf bank_mask:0xf\n"
    "v_mfma_f32_16x16x32_bf16 v[220:223], v[180:183], v[172:175], v[220:223]\n"
    "v_mfma_f32_16x16x32_bf16 v[216:219], v[180:183], v[168:171], v[216:219]\n"
    "v_mfma_f32_16x16x32_bf16 v[216:219], v[148:151], v[136:139], v[216:219]\n"
    "v_mfma_f32_16x16x32_bf16 v[212:215], v[148:151], v[132:135], v[212:215]\n"
    "v_mfma_f32_16x16x32_bf16 v[212:215], v[180:183], v[164:167], v[212:215]\n"
    "v_mfma_f32_16x16x32_bf16 v[208:211], v[180:183], v[160:163], v[208:211]\n"
    "v_mfma_f32_16x16x32_bf16 v[208:211], v[148:151], v[128:131], v[208:211]\n"
    "v_add_u32_dpp v54, v119, v50 quad_perm:[0,0,0,0] row_mask:0xf bank_mask:0xf\n"
    "v_mfma_f32_16x16x32_bf16 v[224:227], v[152:155], v[128:131], v[224:227]\n"
    "v_mfma_f32_16x16x32_bf16 v[224:227], v[184:187], v[160:163], v[224:227]\n"
    "v_mfma_f32_16x16x32_bf16 v[228:231], v[184:187], v[164:167], v[228:231]\n"
    "v_mfma_f32_16x16x32_bf16 v[228:231], v[152:155], v[132:135], v[228:231]\n"
    "v_mfma_f32_16x16x32_bf16 v[232:235], v[152:155], v[136:139], v[232:235]\n"
    "v_mfma_f32_16x16x32_bf16 v[232:235], v[184:187], v[168:171], v[232:235]\n"
    "v_mfma_f32_16x16x32_bf16 v[236:239], v[184:187], v[172:175], v[236:239]\n"
    "v_add_u32_dpp v55, v127, v51 quad_perm:[0,0,0,0] row_mask:0xf bank_mask:0xf\n"
    "v_mfma_f32_16x16x32_bf16 v[236:239], v[152:155], v[140:143], v[236:239]\n"
    "v_mfma_f32_16x16x32_bf16 v[252:255], v[156:159], v[140:143], v[252:255]\n"
    "v_mfma_f32_16x16x32_bf16 v[252:255], v[188:191], v[172:175], v[252:255]\n"
    "v_mfma_f32_16x16x32_bf16 v[248:251], v[188:191], v[168:171], v[248:251]\n"
    "v_mfma_f32_16x16x32_bf16 v[248:251], v[156:159], v[136:139], v[248:251]\n"
    "v_mfma_f32_16x16x32_bf16 v[244:247], v[156:159], v[132:135], v[244:247]\n"
    "v_mfma_f32_16x16x32_bf16 v[244:247], v[188:191], v[164:167], v[244:247]\n"
    "v_mfma_f32_16x16x32_bf16 v[240:243], v[188:191], v[160:163], v[240:243]\n"
    "v_mfma_f32_16x16x32_bf16 v[240:243], v[156:159], v[128:131], v[240:243]\n"
    "s_waitcnt vmcnt(8)\n"
    "s_barrier\n"
    "s_cmp_eq_u32 s20, 3\n"
    "s_cbranch_scc1 .Lxd_on2\n"
    ".Lxd_on2r:\n"
    "s_add_u32 m0, s35, 98304\n"
    "s_nop 0\n"
    "global_load_lds_dwordx4 v46, s[30:31] offset:0\n"
    "ds_read_b128 v[128:131], v56 offset:0\n"
    "ds_read_b128 v[132:135], v56 offset:2048\n"
    "ds_read_b128 v[136:139], v56 offset:4096\n"
    "global_load_lds_dwordx4 v47, s[30:31] offset:1024\n"
    "s_bfe_u32 s66, s29, 0x80008\n"
    "s_mul_i32 s66, s66, 53248\n"
    "s_add_u32 s30, s6, s66\n"
    "s_addc_u32 s31, s7, 0\n"
    "ds_read_b128 v[140:143], v56 offset:6144\n"
    "ds_read_b64_tr_b16 v[144:145], v76 offset:0\n"
    "ds_read_b64_tr_b16 v[146:147], v76 offset:1024\n"
    "s_add_u32 m0, s34, 98304\n"
    "s_nop 0\n"
    "global_load_lds_dwordx4 v52, s[36:37] offset:0\n"
    "ds_read_b64_tr_b16 v[148:149], v77 offset:0\n"
    "ds_read_b64_tr_b16 v[150:151], v77 offset:1024\n"
    "ds_read_b64_tr_b16 v[152:153], v78 offset:0\n"
    "global_load_lds_dwordx4 v53, s[36:37] offset:1024\n"
    "ds_read_b64_tr_b16 v[154:155], v78 offset:1024\n"
    "ds_read_b64_tr_b16 v[156:157], v79 offset:0\n"
    "ds_read_b64_tr_b16 v[158:159], v79 offset:1024\n"
    "global_load_lds_dwordx4 v54, s[36:37] offset:2048\n"
    "ds_read_b128 v[160:163], v57 offset:0\n"
    "ds_read_b128 v[164:167], v57 offset:2048\n"
    "ds_read_b128 v[168:171], v57 offset:4096\n"
    "global_load_lds_dwordx4 v55, s[36:37] offset:3072\n"
    "ds_read_b128 v[172:175], v57 offset:6144\n"
    "ds_read_b64_tr_b16 v[176:177], v76 offset:8192\n"
    "ds_read_b64_tr_b16 v[178:179], v76 offset:9216\n"
    "global_load_dwordx4 v[36:39], v38, s[8:9]\n"
    "ds_read_b64_tr_b16 v[180:181], v77 offset:8192\n"
    "ds_read_b64_tr_b16 v[182:183], v77 offset:9216\n"
    "ds_read_b64_tr_b16 v[184:185], v78 offset:8192\n"
    "ds_read_b64_tr_b16 v[186:187], v78 offset:9216\n"
    "ds_read_b64_tr_b16 v[188:189], v79 offset:8192\n"
    "ds_read_b64_tr_b16 v[190:191], v79 offset:9216\n"
    "s_waitcnt vmcnt(12) lgkmcnt(0)\n"
    "s_barrier\n"
    "v_mfma_f32_16x16x32_bf16 v[192:195], v[144:147], v[128:131], v[192:195]\n"
    "v_mfma_f32_16x16x32_bf16 v[192:195], v[176:179], v[160:163], v[192:195]\n"
    "v_add_u32_dpp v52, v100, v48 quad_perm:[1,1,1,1] row_mask:0xf bank_mask:0xf\n"
    "v_mfma_f32_16x16x32_bf16 v[196:199], v[176:179], v[164:167], v[196:199]\n"
    "v_mfma_f32_16x16x32_bf16 v[196:199], v[144:147], v[132:135], v[196:199]\n"
    "v_mfma_f32_16x16x32_bf16 v[200:203], v[144:147], v[136:139], v[200:203]\n"
    "v_mfma_f32_16x16x32_bf16 v[200:203], v[176:179], v[168:171], v[200:203]\n"
    "v_mfma_f32_16x16x32_bf16 v[204:207], v[176:179], v[172:175], v[204:207]\n"
    "v_mfma_f32_16x16x32_bf16 v[204:207], v[144:147], v[140:143], v[204:207]\n"
    "v_mfma_f32_16x16x32_bf16 v[220:223], v[148:151], v[140:143], v[220:223]\n"
    "v_add_u32_dpp v53, v108, v49 quad_perm:[1,1,1,1] row_mask:0xf bank_mask:0xf\n"
    "v_mfma_f32_16x16x32_bf16 v[220:223], v[180:183], v[172:175], v[220:223]\n"
    "v_mfma_f32_16x16x32_bf16 v[216:219], v[180:183], v[168:171], v[216:219]\n"
    "v_mfma_f32_16x16x32_bf16 v[216:219], v[148:151], v[136:139], v[216:219]\n"
    "v_mfma_f32_16x16x32_bf16 v[212:215], v[148:151], v[132:135], v[212:215]\n"
    "v_mfma_f32_16x16x32_bf16 v[212:215], v[180:183], v[164:167], v[212:215]\n"
    "v_mfma_f32_16x16x32_bf16 v[208:211], v[180:183], v[160:163], v[208:211]\n"
    "v_mfma_f32_16x16x32_bf16 v[208:211], v[148:151], v[128:131], v[208:211]\n"
    "v_add_u32_dpp v54, v116, v50 quad_perm:[1,1,1,1] row_mask:0xf bank_mask:0xf\n"
    "v_mfma_f32_16x16x32_bf16 v[224:227], v[152:155], v[128:131], v[224:227]\n"
    "v_mfma_f32_16x16x32_bf16 v[224:227], v[184:187], v[160:163], v[224:227]\n"
    "v_mfma_f32_16x16x32_bf16 v[228:231], v[184:187], v[164:167], v[228:231]\n"
    "v_mfma_f32_16x16x32_bf16 v[228:231], v[152:155], v[132:135], v[228:231]\n"
    "v_mfma_f32_16x16x32_bf16 v[232:235], v[152:155], v[136:139], v[232:235]\n"
    "v_mfma_f32_16x16x32_bf16 v[232:235], v[184:187], v[168:171], v[232:235]\n"
    "v_mfma_f32_16x16x32_bf16 v[236:239], v[184:187], v[172:175], v[236:239]\n"
    "v_add_u32_dpp v55, v124, v51 quad_perm:[1,1,1,1] row_mask:0xf bank_mask:0xf\n"
    "v_mfma_f32_16x16x32_bf16 v[236:239], v[152:155], v[140:143], v[236:239]\n"
    "v_mfma_f32_16x16x32_bf16 v[252:255], v[156:159], v[140:143], v[252:255]\n"
    "v_mfma_f32_16x16x32_bf16 v[252:255], v[188:191], v[172:175], v[252:255]\n"
    "v_mfma_f32_16x16x32_bf16 v[248:251], v[188:191], v[168:171], v[248:251]\n"
    "v_mfma_f32_16x16x32_bf16 v[248:251], v[156:159], v[136:139], v[248:251]\n"
    "v_mfma_f32_16x16x32_bf16 v[244:247], v[156:159], v[132:135], v[244:247]\n"
    "v_mfma_f32_16x16x32_bf16 v[244:247], v[188:191], v[164:167], v[244:247]\n"
    "v_mfma_f32_16x16x32_bf16 v[240:243], v[188:191], v[160:163], v[240:243]\n"
    "v_mfma_f32_16x16x32_bf16 v[240:243], v[156:159], v[128:131], v[240:243]\n"
    "s_waitcnt vmcnt(8)\n"
    "s_barrier\n"
    "s_add_u32 m0, s35, 0\n"
    "s_nop 0\n"
    "global_load_lds_dwordx4 v46, s[30:31] offset:0\n"
    "ds_read_b128 v[128:131], v56 offset:49152\n"
    "ds_read_b128 v[132:135], v56 offset:51200\n"
    "ds_read_b128 v[136:139], v56 offset:53248\n"
    "ds_read_b128 v[140:143], v56 offset:55296\n"
    "global_load_lds_dwordx4 v47, s[30:31] offset:1024\n"
    "s_add_u32 s30, s30, 212992\n"
    "s_addc_u32 s31, s31, 0\n"
    "ds_read_b64_tr_b16 v[144:145], v76 offset:49152\n"
    "ds_read_b64_tr_b16 v[146:147], v76 offset:50176\n"
    "ds_read_b64_tr_b16 v[148:149], v77 offset:49152\n"
    "ds_read_b64_tr_b16 v[150:151], v77 offset:50176\n"
    "s_add_u32 m0, s34, 0\n"
    "s_nop 0\n"
    "global_load_lds_dwordx4 v52, s[36:37] offset:0\n"
    "ds_read_b64_tr_b16 v[152:153], v78 offset:49152\n"
    "ds_read_b64_tr_b16 v[154:155], v78 offset:50176\n"
    "ds_read_b64_tr_b16 v[156:157], v79 offset:49152\n"
    "ds_read_b64_tr_b16 v[158:159], v79 offset:50176\n"
    "global_load_lds_dwordx4 v53, s[36:37] offset:1024\n"
    "ds_read_b128 v[160:163], v57 offset:49152\n"
    "ds_read_b128 v[164:167], v57 offset:51200\n"
    "ds_read_b128 v[168:171], v57 offset:53248\n"
    "ds_read_b128 v[172:175], v57 offset:55296\n"
    "global_load_lds_dwordx4 v54, s[36:37] offset:2048\n"
    "ds_read_b64_tr_b16 v[176:177], v76 offset:57344\n"
    "ds_read_b64_tr_b16 v[178:179], v76 offset:58368\n"
    "ds_read_b64_tr_b16 v[180:181], v77 offset:57344\n"
    "ds_read_b64_tr_b16 v[182:183], v77 offset:58368\n"
    "global_load_lds_dwordx4 v55, s[36:37] offset:3072\n"
    "ds_read_b64_tr_b16 v[184:185], v78 offset:57344\n"
    "ds_read_b64_tr_b16 v[186:187], v78 offset:58368\n"
    "ds_read_b64_tr_b16 v[188:189], v79 offset:57344\n"
    "ds_read_b64_tr_b16 v[190:191], v79 offset:58368\n"
    "s_waitcnt vmcnt(11) lgkmcnt(0)\n"
    "s_barrier\n"
    "v_mfma_f32_16x16x32_bf16 v[192:195], v[144:147], v[128:131], v[192:195]\n"
    "v_mfma_f32_16x16x32_bf16 v[192:195], v[176:179], v[160:163], v[192:195]\n"
    "v_add_u32_dpp v52, v101, v48 quad_perm:[1,1,1,1] row_mask:0xf bank_mask:0xf\n"
    "v_mfma_f32_16x16x32_bf16 v[196:199], v[176:179], v[164:167], v[196:199]\n"
    "v_mfma_f32_16x16x32_bf16 v[196:199], v[144:147], v[132:135], v[196:199]\n"
    "v_mfma_f32_16x16x32_bf16 v[200:203], v[144:147], v[136:139], v[200:203]\n"
    "v_mfma_f32_16x16x32_bf16 v[200:203], v[176:179], v[168:171], v[200:203]\n"
    "v_mfma_f32_16x16x32_bf16 v[204:207], v[176:179], v[172:175], v[204:207]\n"
    "v_mfma_f32_16x16x32_bf16 v[204:207], v[144:147], v[140:143], v[204:207]\n"
    "v_mfma_f32_16x16x32_bf16 v[220:223], v[148:151], v[140:143], v[220:223]\n"
    "v_add_u32_dpp v53, v109, v49 quad_perm:[1,1,1,1] row_mask:0xf bank_mask:0xf\n"
    "v_mfma_f32_16x16x32_bf16 v[220:223], v[180:183], v[172:175], v[220:223]\n"
    "v_mfma_f32_16x16x32_bf16 v[216:219], v[180:183], v[168:171], v[216:219]\n"
    "v_mfma_f32_16x16x32_bf16 v[216:219], v[148:151], v[136:139], v[216:219]\n"
    "v_mfma_f32_16x16x32_bf16 v[212:215], v[148:151], v[132:135], v[212:215]\n"
    "v_mfma_f32_16x16x32_bf16 v[212:215], v[180:183], v[164:167], v[212:215]\n"
    "v_mfma_f32_16x16x32_bf16 v[208:211], v[180:183], v[160:163], v[208:211]\n"
    "v_mfma_f32_16x16x32_bf16 v[208:211], v[148:151], v[128:131], v[208:211]\n"
    "v_add_u32_dpp v54, v117, v50 quad_perm:[1,1,1,1] row_mask:0xf bank_mask:0xf\n"
    "v_mfma_f32_16x16x32_bf16 v[224:227], v[152:155], v[128:131], v[224:227]\n"
    "v_mfma_f32_16x16x32_bf16 v[224:227], v[184:187], v[160:163], v[224:227]\n"
    "v_mfma_f32_16x16x32_bf16 v[228:231], v[184:187], v[164:167], v[228:231]\n"
    "v_mfma_f32_16x16x32_bf16 v[228:231], v[152:155], v[132:135], v[228:231]\n"
    "v_mfma_f32_16x16x32_bf16 v[232:235], v[152:155], v[136:139], v[232:235]\n"
    "v_mfma_f32_16x16x32_bf16 v[232:235], v[184:187], v[168:171], v[232:235]\n"
    "v_mfma_f32_16x16x32_bf16 v[236:239], v[184:187], v[172:175], v[236:239]\n"
    "v_add_u32_dpp v55, v125, v51 quad_perm:[1,1,1,1] row_mask:0xf bank_mask:0xf\n"
    "v_mfma_f32_16x16x32_bf16 v[236:239], v[152:155], v[140:143], v[236:239]\n"
    "v_mfma_f32_16x16x32_bf16 v[252:255], v[156:159], v[140:143], v[252:255]\n"
    "v_mfma_f32_16x16x32_bf16 v[252:255], v[188:191], v[172:175], v[252:255]\n"
    "v_mfma_f32_16x16x32_bf16 v[248:251], v[188:191], v[168:171], v[248:251]\n"
    "v_mfma_f32_16x16x32_bf16 v[248:251], v[156:159], v[136:139], v[248:251]\n"
    "v_mfma_f32_16x16x32_bf16 v[244:247], v[156:159], v[132:135], v[244:247]\n"
    "v_mfma_f32_16x16x32_bf16 v[244:247], v[188:191], v[164:167], v[244:247]\n"
    "v_mfma_f32_16x16x32_bf16 v[240:243], v[188:191], v[160:163], v[240:243]\n"
    "v_mfma_f32_16x16x32_bf16 v[240:243], v[156:159], v[128:131], v[240:243]\n"
    "s_waitcnt vmcnt(7)\n"
    "s_barrier\n"
    "s_cmp_eq_u32 s20, 3\n"
    "s_cbranch_scc1 .Lxd_on4\n"
    ".Lxd_on4r:\n"
    "s_add_u32 m0, s35, 49152\n"
    "s_nop 0\n"
    "global_load_lds_dwordx4 v46, s[30:31] offset:0\n"
    "ds_read_b128 v[128:131], v58 offset:0\n"
    "ds_read_b128 v[132:135], v58 offset:2048\n"
    "ds_read_b128 v[136:139], v58 offset:4096\n"
    "ds_read_b128 v[140:143], v58 offset:6144\n"
    "global_load_lds_dwordx4 v47, s[30:31] offset:1024\n"
    "ds_read_b64_tr_b16 v[144:145], v80 offset:0\n"
    "ds_read_b64_tr_b16 v[146:147], v80 offset:1024\n"
    "ds_read_b64_tr_b16 v[148:149], v81 offset:0\n"
    "ds_read_b64_tr_b16 v[150:151], v81 offset:1024\n"
    "s_add_u32 m0, s34, 49152\n"
    "s_nop 0\n"
    "global_load_lds_dwordx4 v52, s[36:37] offset:0\n"
    "ds_read_b64_tr_b16 v[152:153], v82 offset:0\n"
    "ds_read_b64_tr_b16 v[154:155], v82 offset:1024\n"
    "ds_read_b64_tr_b16 v[156:157], v83 offset:0\n"
    "ds_read_b64_tr_b16 v[158:159], v83 offset:1024\n"
    "global_load_lds_dwordx4 v53, s[36:37] offset:1024\n"
    "ds_read_b128 v[160:163], v59 offset:0\n"
    "ds_read_b128 v[164:167], v59 offset:2048\n"
    "ds_read_b128 v[168:171], v59 offset:4096\n"
    "ds_read_b128 v[172:175], v59 offset:6144\n"
    "global_load_lds_dwordx4 v54, s[36:37] offset:2048\n"
    "ds_read_b64_tr_b16 v[176:177], v80 offset:8192\n"
    "ds_read_b64_tr_b16 v[178:179], v80 offset:9216\n"
    "ds_read_b64_tr_b16 v[180:181], v81 offset:8192\n"
    "ds_read_b64_tr_b16 v[182:183], v81 offset:9216\n"
    "global_load_lds_dwordx4 v55, s[36:37] offset:3072\n"
    "ds_read_b64_tr_b16 v[184:185], v82 offset:8192\n"
    "ds_read_b64_tr_b16 v[186:187], v82 offset:9216\n"
    "ds_read_b64_tr_b16 v[188:189], v83 offset:8192\n"
    "ds_read_b64_tr_b16 v[190:191], v83 offset:9216\n"
    "s_waitcnt vmcnt(10) lgkmcnt(0)\n"
    "s_barrier\n"
    "v_mfma_f32_16x16x32_bf16 v[192:195], v[144:147], v[128:131], v[192:195]\n"
    "v_mfma_f32_16x16x32_bf16 v[192:195], v[176:179], v[160:163], v[192:195]\n"
    "v_lshlrev_b32 v52, 4, v1\n"
    "s_movk_i32 s43, 15744\n"
    "v_pk_maximum3_f16 v21, v28, v28, s43 op_sel_hi:[1,1,0] neg_lo:[0,1,0] neg_hi:[0,1,0]\n"
    "v_mfma_f32_16x16x32_bf16 v[196:199], v[176:179], v[164:167], v[196:199]\n"
    "v_pk_maximum3_f16 v21, v29, v29, v21 neg_lo:[0,1,0] neg_hi:[0,1,0]\n"
    "v_mfma_f32_16x16x32_bf16 v[196:199], v[144:147], v[132:135], v[196:199]\n"
    "v_pk_maximum3_f16 v21, v30, v30, v21 neg_lo:[0,1,0] neg_hi:[0,1,0]\n"
    "v_mfma_f32_16x16x32_bf16 v[200:203], v[144:147], v[136:139], v[200:203]\n"
    "v_pk_maximum3_f16 v21, v31, v31, v21 neg_lo:[0,1,0] neg_hi:[0,1,0]\n"
    "v_mfma_f32_16x16x32_bf16 v[200:203], v[176:179], v[168:171], v[200:203]\n"
    "v_pk_maximum3_f16 v21, v32, v32, v21 neg_lo:[0,1,0] neg_hi:[0,1,0]\n"
    "v_mfma_f32_16x16x32_bf16 v[204:207], v[176:179], v[172:175], v[204:207]\n"
    "v_pk_maximum3_f16 v21, v33, v33, v21 neg_lo:[0,1,0] neg_hi:[0,1,0]\n"
    "v_mfma_f32_16x16x32_bf16 v[204:207], v[144:147], v[140:143], v[204:207]\n"
    "v_pk_maximum3_f16 v21, v34, v34, v21 neg_lo:[0,1,0] neg_hi:[0,1,0]\n"
    "v_mfma_f32_16x16x32_bf16 v[220:223], v[148:151], v[140:143], v[220:223]\n"
    "v_pk_maximum3_f16 v21, v35, v35, v21 neg_lo:[0,1,0] neg_hi:[0,1,0]\n"
    "v_mfma_f32_16x16x32_bf16 v[220:223], v[180:183], v[172:175], v[220:223]\n"
    "v_pk_maximum3_f16 v21, v36, v36, v21 neg_lo:[0,1,0] neg_hi:[0,1,0]\n"
    "v_mfma_f32_16x16x32_bf16 v[216:219], v[180:183], v[168:171], v[216:219]\n"
    "v_pk_maximum3_f16 v21, v37, v37, v21 neg_lo:[0,1,0] neg_hi:[0,1,0]\n"
    "v_mfma_f32_16x16x32_bf16 v[216:219], v[148:151], v[136:139], v[216:219]\n"
    "v_pk_maximum3_f16 v21, v38, v38, v21 neg_lo:[0,1,0] neg_hi:[0,1,0]\n"
    "v_mfma_f32_16x16x32_bf16 v[212:215], v[148:151], v[132:135], v[212:215]\n"
    "v_pk_maximum3_f16 v21, v39, v39, v21 neg_lo:[0,1,0] neg_hi:[0,1,0]\n"
    "v_mfma_f32_16x16x32_bf16 v[212:215], v[180:183], v[164:167], v[212:215]\n"
    "v_pk_maximum3_f16 v21, v40, v40, v21 neg_lo:[0,1,0] neg_hi:[0,1,0]\n"
    "v_mfma_f32_16x16x32_bf16 v[208:211], v[180:183], v[160:163], v[208:211]\n"
    "v_pk_max_u16 v21, v21, v21 op_sel:[0,1] op_sel_hi:[1,0]\n"
    "v_mfma_f32_16x16x32_bf16 v[208:211], v[148:151], v[128:131], v[208:211]\n"
    "v_add_u32 v53, 4096, v52\n"
    "v_mfma_f32_16x16x32_bf16 v[224:227], v[152:155], v[128:131], v[224:227]\n"
    "v_max_u32_dpp v21, v21, v21 quad_perm:[1,0,3,2] row_mask:0xf bank_mask:0xf\n"
    "v_mfma_f32_16x16x32_bf16 v[224:227], v[184:187], v[160:163], v[224:227]\n"
    "v_mfma_f32_16x16x32_bf16 v[228:231], v[184:187], v[164:167], v[228:231]\n"
    "v_max_u32_dpp v21, v21, v21 quad_perm:[2,3,0,1] row_mask:0xf bank_mask:0xf\n"
    "v_mfma_f32_16x16x32_bf16 v[228:231], v[152:155], v[132:135], v[228:231]\n"
    "v_mfma_f32_16x16x32_bf16 v[232:235], v[152:155], v[136:139], v[232:235]\n"
    "v_max_u32_dpp v21, v21, v21 row_half_mirror row_mask:0xf bank_mask:0xf\n"
    "v_mfma_f32_16x16x32_bf16 v[232:235], v[184:187], v[168:171], v[232:235]\n"
    "v_mfma_f32_16x16x32_bf16 v[236:239], v[184:187], v[172:175], v[236:239]\n"
    "v_max_u32_dpp v21, v21, v21 row_mirror row_mask:0xf bank_mask:0xf\n"
    "v_mfma_f32_16x16x32_bf16 v[236:239], v[152:155], v[140:143], v[236:239]\n"
    "v_mfma_f32_16x16x32_bf16 v[252:255], v[156:159], v[140:143], v[252:255]\n"
    "v_max_u32_dpp v21, v21, v21 row_bcast:15 row_mask:0xa bank_mask:0xf\n"
    "v_mfma_f32_16x16x32_bf16 v[252:255], v[188:191], v[172:175], v[252:255]\n"
    "v_mfma_f32_16x16x32_bf16 v[248:251], v[188:191], v[168:171], v[248:251]\n"
    "v_max_u32_dpp v21, v21, v21 row_bcast:31 row_mask:0xc bank_mask:0xf\n"
    "v_mfma_f32_16x16x32_bf16 v[248:251], v[156:159], v[136:139], v[248:251]\n"
    "v_mfma_f32_16x16x32_bf16 v[244:247], v[156:159], v[132:135], v[244:247]\n"
    "v_readlane_b32 s47, v21, 63\n"
    "v_mfma_f32_16x16x32_bf16 v[244:247], v[188:191], v[164:167], v[244:247]\n"
    "v_mfma_f32_16x16x32_bf16 v[240:243], v[188:191], v[160:163], v[240:243]\n"
    "s_and_b32 s50, s47, 0x7f80\n"
    "s_lshr_b32 s47, s50, 6\n"
    "s_sub_u32 s50, s50, 2816\n"
    "s_mul_i32 s51, s50, -8\n"
    "v_mfma_f32_16x16x32_bf16 v[240:243], v[156:159], v[128:131], v[240:243]\n"
    "s_waitcnt vmcnt(6)\n"
    "s_barrier\n"
    "s_mul_i32 s43, s20, 5376\n"
    "s_add_u32 s40, s43, 1048576\n"
    "s_add_u32 s38, s10, s40\n"
    "s_addc_u32 s39, s11, 0\n"
    "s_add_u32 m0, s43, 98304\n"
    "s_nop 0\n"
    "global_load_lds_dwordx4 v52, s[38:39] offset:0 sc1\n"
    "ds_read_b128 v[128:131], v56 offset:0\n"
    "ds_read_b128 v[132:135], v56 offset:2048\n"
    "ds_read_b128 v[136:139], v56 offset:4096\n"
    "ds_read_b128 v[140:143], v56 offset:6144\n"
    "global_load_lds_dwordx4 v52, s[38:39] offset:1024 sc1\n"
    "ds_read_b64_tr_b16 v[144:145], v76 offset:0\n"
    "ds_read_b64_tr_b16 v[146:147], v76 offset:1024\n"
    "ds_read_b64_tr_b16 v[148:149], v77 offset:0\n"
    "ds_read_b64_tr_b16 v[150:151], v77 offset:1024\n"
    "global_load_lds_dwordx4 v52, s[38:39] offset:2048 sc1\n"
    "ds_read_b64_tr_b16 v[152:153], v78 offset:0\n"
    "ds_read_b64_tr_b16 v[154:155], v78 offset:1024\n"
    "ds_read_b64_tr_b16 v[156:157], v79 offset:0\n"
    "ds_read_b64_tr_b16 v[158:159], v79 offset:1024\n"
    "global_load_lds_dwordx4 v52, s[38:39] offset:3072 sc1\n"
    "ds_read_b128 v[160:163], v57 offset:0\n"
    "ds_read_b128 v[164:167], v57 offset:2048\n"
    "ds_read_b128 v[168:171], v57 offset:4096\n"
    "ds_read_b128 v[172:175], v57 offset:6144\n"
    "s_add_u32 m0, s43, 102400\n"
    "s_nop 0\n"
    "global_load_lds_dwordx4 v53, s[38:39] offset:0 sc1\n"
    "ds_read_b64_tr_b16 v[176:177], v76 offset:8192\n"
    "ds_read_b64_tr_b16 v[178:179], v76 offset:9216\n"
    "ds_read_b64_tr_b16 v[180:181], v77 offset:8192\n"
    "ds_read_b64_tr_b16 v[182:183], v77 offset:9216\n"
    "global_load_lds_dwordx4 v53, s[38:39] offset:256 sc1\n"
    "ds_read_b64_tr_b16 v[184:185], v78 offset:8192\n"
    "ds_read_b64_tr_b16 v[186:187], v78 offset:9216\n"
    "ds_read_b64_tr_b16 v[188:189], v79 offset:8192\n"
    "ds_read_b64_tr_b16 v[190:191], v79 offset:9216\n"
    "s_waitcnt vmcnt(10) lgkmcnt(0)\n"
    "s_barrier\n"
    "v_mfma_f32_16x16x32_bf16 v[192:195], v[144:147], v[128:131], v[192:195]\n"
    "v_pk_maximum3_f16 v21, v28, v28, s50 op_sel_hi:[1,1,0] neg_lo:[0,1,0] neg_hi:[0,1,0]\n"
    "v_pk_mad_u16 v21, v21, 8, s51 op_sel_hi:[1,0,0]\n"
    "v_mfma_f32_16x16x32_bf16 v[192:195], v[176:179], v[160:163], v[192:195]\n"
    "v_bfi_b32 v28, s57, v21, v28\n"
    "v_mfma_f32_16x16x32_bf16 v[196:199], v[176:179], v[164:167], v[196:199]\n"
    "v_pk_maximum3_f16 v12, v29, v29, s50 op_sel_hi:[1,1,0] neg_lo:[0,1,0] neg_hi:[0,1,0]\n"
    "v_mfma_f32_16x16x32_bf16 v[196:199], v[144:147], v[132:135], v[196:199]\n"
    "v_pk_mad_u16 v12, v12, 8, s51 op_sel_hi:[1,0,0]\n"
    "v_bfi_b32 v29, s57, v12, v29\n"
    "v_mfma_f32_16x16x32_bf16 v[200:203], v[144:147], v[136:139], v[200:203]\n"
    "v_pk_maximum3_f16 v21, v30, v30, s50 op_sel_hi:[1,1,0] neg_lo:[0,1,0] neg_hi:[0,1,0]\n"
    "v_mfma_f32_16x16x32_bf16 v[200:203], v[176:179], v[168:171], v[200:203]\n"
    "v_pk_mad_u16 v21, v21, 8, s51 op_sel_hi:[1,0,0]\n"
    "v_mfma_f32_16x16x32_bf16 v[204:207], v[176:179], v[172:175], v[204:207]\n"
    "v_bfi_b32 v30, s57, v21, v30\n"
    "v_mfma_f32_16x16x32_bf16 v[204:207], v[144:147], v[140:143], v[204:207]\n"
    "v_pk_maximum3_f16 v12, v31, v31, s50 op_sel_hi:[1,1,0] neg_lo:[0,1,0] neg_hi:[0,1,0]\n"
    "v_pk_mad_u16 v12, v12, 8, s51 op_sel_hi:[1,0,0]\n"
    "v_mfma_f32_16x16x32_bf16 v[220:223], v[148:151], v[140:143], v[220:223]\n"
    "v_bfi_b32 v31, s57, v12, v31\n"
    "v_mfma_f32_16x16x32_bf16 v[220:223], v[180:183], v[172:175], v[220:223]\n"
    "v_pk_maximum3_f16 v21, v32, v32, s50 op_sel_hi:[1,1,0] neg_lo:[0,1,0] neg_hi:[0,1,0]\n"
    "v_mfma_f32_16x16x32_bf16 v[216:219], v[180:183], v[168:171], v[216:219]\n"
    "v_pk_mad_u16 v21, v21, 8, s51 op_sel_hi:[1,0,0]\n"
    "v_mfma_f32_16x16x32_bf16 v[216:219], v[148:151], v[136:139], v[216:219]\n"
    "v_bfi_b32 v32, s57, v21, v32\n"
    "v_pk_maximum3_f16 v12, v33, v33, s50 op_sel_hi:[1,1,0] neg_lo:[0,1,0] neg_hi:[0,1,0]\n"
    "v_mfma_f32_16x16x32_bf16 v[212:215], v[148:151], v[132:135], v[212:215]\n"
    "v_pk_mad_u16 v12, v12, 8, s51 op_sel_hi:[1,0,0]\n"
    "v_mfma_f32_16x16x32_bf16 v[212:215], v[180:183], v[164:167], v[212:215]\n"
    "v_bfi_b32 v33, s57, v12, v33\n"
    "v_mfma_f32_16x16x32_bf16 v[208:211], v[180:183], v[160:163], v[208:211]\n"
    "v_pk_maximum3_f16 v21, v34, v34, s50 op_sel_hi:[1,1,0] neg_lo:[0,1,0] neg_hi:[0,1,0]\n"
    "v_mfma_f32_16x16x32_bf16 v[208:211], v[148:151], v[128:131], v[208:211]\n"
    "v_pk_mad_u16 v21, v21, 8, s51 op_sel_hi:[1,0,0]\n"
    "v_bfi_b32 v34, s57, v21, v34\n"
    "v_mfma_f32_16x16x32_bf16 v[224:227], v[152:155], v[128:131], v[224:227]\n"
    "v_pk_maximum3_f16 v12, v35, v35, s50 op_sel_hi:[1,1,0] neg_lo:[0,1,0] neg_hi:[0,1,0]\n"
    "v_mfma_f32_16x16x32_bf16 v[224:227], v[184:187], v[160:163], v[224:227]\n"
    "v_pk_mad_u16 v12, v12, 8, s51 op_sel_hi:[1,0,0]\n"
    "v_mfma_f32_16x16x32_bf16 v[228:231], v[184:187], v[164:167], v[228:231]\n"
    "v_bfi_b32 v35, s57, v12, v35\n"
    "v_mfma_f32_16x16x32_bf16 v[228:231], v[152:155], v[132:135], v[228:231]\n"
    "v_pk_maximum3_f16 v21, v36, v36, s50 op_sel_hi:[1,1,0] neg_lo:[0,1,0] neg_hi:[0,1,0]\n"
    "v_pk_mad_u16 v21, v21, 8, s51 op_sel_hi:[1,0,0]\n"
    "v_mfma_f32_16x16x32_bf16 v[232:235], v[152:155], v[136:139], v[232:235]\n"
    "v_bfi_b32 v36, s57, v21, v36\n"
    "v_mfma_f32_16x16x32_bf16 v[232:235], v[184:187], v[168:171], v[232:235]\n"
    "v_pk_maximum3_f16 v12, v37, v37, s50 op_sel_hi:[1,1,0] neg_lo:[0,1,0] neg_hi:[0,1,0]\n"
    "v_mfma_f32_16x16x32_bf16 v[236:239], v[184:187], v[172:175], v[236:239]\n"
    "v_pk_mad_u16 v12, v12, 8, s51 op_sel_hi:[1,0,0]\n"
    "v_mfma_f32_16x16x32_bf16 v[236:239], v[152:155], v[140:143], v[236:239]\n"
    "v_bfi_b32 v37, s57, v12, v37\n"
    "v_pk_maximum3_f16 v21, v38, v38, s50 op_sel_hi:[1,1,0] neg_lo:[0,1,0] neg_hi:[0,1,0]\n"
    "v_mfma_f32_16x16x32_bf16 v[252:255], v[156:159], v[140:143], v[252:255]\n"
    "v_pk_mad_u16 v21, v21, 8, s51 op_sel_hi:[1,0,0]\n"
    "v_mfma_f32_16x16x32_bf16 v[252:255], v[188:191], v[172:175], v[252:255]\n"
    "v_bfi_b32 v38, s57, v21, v38\n"
    "v_mfma_f32_16x16x32_bf16 v[248:251], v[188:191], v[168:171], v[248:251]\n"
    "v_pk_maximum3_f16 v12, v39, v39, s50 op_sel_hi:[1,1,0] neg_lo:[0,1,0] neg_hi:[0,1,0]\n"
    "v_mfma_f32_16x16x32_bf16 v[248:251], v[156:159], v[136:139], v[248:251]\n"
    "v_pk_mad_u16 v12, v12, 8, s51 op_sel_hi:[1,0,0]\n"
    "v_bfi_b32 v39, s57, v12, v39\n"
    "v_mfma_f32_16x16x32_bf16 v[244:247], v[156:159], v[132:135], v[244:247]\n"
    "v_pk_maximum3_f16 v21, v40, v40, s50 op_sel_hi:[1,1,0] neg_lo:[0,1,0] neg_hi:[0,1,0]\n"
    "v_mfma_f32_16x16x32_bf16 v[244:247], v[188:191], v[164:167], v[244:247]\n"
    "v_pk_mad_u16 v21, v21, 8, s51 op_sel_hi:[1,0,0]\n"
    "v_mfma_f32_16x16x32_bf16 v[240:243], v[188:191], v[160:163], v[240:243]\n"
    "v_bfi_b32 v40, s57, v21, v40\n"
    "v_mfma_f32_16x16x32_bf16 v[240:243], v[156:159], v[128:131], v[240:243]\n"
    "s_waitcnt vmcnt(6)\n"
    "s_barrier\n"
    "ds_read_b64 v[84:85], v42 offset:0\n"
    "ds_read_b64 v[86:87], v42 offset:32\n"
    "ds_read_b64 v[88:89], v42 offset:64\n"
    "ds_read_b64 v[90:91], v42 offset:96\n"
    "ds_read_b32 v41, v25 offset:24\n"
    "ds_read_b128 v[128:131], v56 offset:49152\n"
    "ds_read_b128 v[132:135], v56 offset:51200\n"
    "ds_read_b128 v[136:139], v56 offset:53248\n"
    "ds_read_b128 v[140:143], v56 offset:55296\n"
    "ds_read_b64_tr_b16 v[144:145], v76 offset:49152\n"
    "ds_read_b64_tr_b16 v[146:147], v76 offset:50176\n"
    "ds_read_b64_tr_b16 v[148:149], v77 offset:49152\n"
    "ds_read_b64_tr_b16 v[150:151], v77 offset:50176\n"
    "ds_read_b64_tr_b16 v[152:153], v78 offset:49152\n"
    "ds_read_b64_tr_b16 v[154:155], v78 offset:50176\n"
    "ds_read_b64_tr_b16 v[156:157], v79 offset:49152\n"
    "ds_read_b64_tr_b16 v[158:159], v79 offset:50176\n"
    "ds_read_b128 v[160:163], v57 offset:49152\n"
    "ds_read_b128 v[164:167], v57 offset:51200\n"
    "ds_read_b128 v[168:171], v57 offset:53248\n"
    "ds_read_b128 v[172:175], v57 offset:55296\n"
    "ds_read_b64_tr_b16 v[176:177], v76 offset:57344\n"
    "ds_read_b64_tr_b16 v[178:179], v76 offset:58368\n"
    "ds_read_b64_tr_b16 v[180:181], v77 offset:57344\n"
    "ds_read_b64_tr_b16 v[182:183], v77 offset:58368\n"
    "ds_read_b64_tr_b16 v[184:185], v78 offset:57344\n"
    "ds_read_b64_tr_b16 v[186:187], v78 offset:58368\n"
    "ds_read_b64_tr_b16 v[188:189], v79 offset:57344\n"
    "ds_read_b64_tr_b16 v[190:191], v79 offset:58368\n"
    "s_waitcnt vmcnt(0) lgkmcnt(0)\n"
    "s_cmp_eq_u32 s22, 3\n"
    "s_cbranch_scc1 .Lxd_on5\n"
    ".Lxd_on5r:\n"
    "s_barrier\n"
    "v_mfma_f32_16x16x32_bf16 v[192:195], v[144:147], v[128:131], v[192:195]\n"
    "v_lshlrev_b32 v60, 16, v84\n"
    "v_mfma_f32_16x16x32_bf16 v[192:195], v[176:179], v[160:163], v[192:195]\n"
    "v_and_b32 v61, 0xffff0000, v84\n"
    "v_mfma_f32_16x16x32_bf16 v[196:199], v[176:179], v[164:167], v[196:199]\n"
    "v_mfma_f32_16x16x32_bf16 v[196:199], v[144:147], v[132:135], v[196:199]\n"
    "v_lshlrev_b32 v62, 16, v85\n"
    "v_mfma_f32_16x16x32_bf16 v[200:203], v[144:147], v[136:139], v[200:203]\n"
    "v_and_b32 v63, 0xffff0000, v85\n"
    "v_mfma_f32_16x16x32_bf16 v[200:203], v[176:179], v[168:171], v[200:203]\n"
    "v_mfma_f32_16x16x32_bf16 v[204:207], v[176:179], v[172:175], v[204:207]\n"
    "v_lshlrev_b32 v64, 16, v86\n"
    "v_mfma_f32_16x16x32_bf16 v[204:207], v[144:147], v[140:143], v[204:207]\n"
    "v_and_b32 v65, 0xffff0000, v86\n"
    "v_mfma_f32_16x16x32_bf16 v[220:223], v[148:151], v[140:143], v[220:223]\n"
    "v_mfma_f32_16x16x32_bf16 v[220:223], v[180:183], v[172:175], v[220:223]\n"
    "v_lshlrev_b32 v66, 16, v87\n"
    "v_mfma_f32_16x16x32_bf16 v[216:219], v[180:183], v[168:171], v[216:219]\n"
    "v_and_b32 v67, 0xffff0000, v87\n"
    "v_mfma_f32_16x16x32_bf16 v[216:219], v[148:151], v[136:139], v[216:219]\n"
    "v_mfma_f32_16x16x32_bf16 v[212:215], v[148:151], v[132:135], v[212:215]\n"
    "v_lshlrev_b32 v68, 16, v88\n"
    "v_mfma_f32_16x16x32_bf16 v[212:215], v[180:183], v[164:167], v[212:215]\n"
    "v_and_b32 v69, 0xffff0000, v88\n"
    "v_mfma_f32_16x16x32_bf16 v[208:211], v[180:183], v[160:163], v[208:211]\n"
    "v_mfma_f32_16x16x32_bf16 v[208:211], v[148:151], v[128:131], v[208:211]\n"
    "v_lshlrev_b32 v70, 16, v89\n"
    "v_mfma_f32_16x16x32_bf16 v[224:227], v[152:155], v[128:131], v[224:227]\n"
    "v_and_b32 v71, 0xffff0000, v89\n"
    "v_mfma_f32_16x16x32_bf16 v[224:227], v[184:187], v[160:163], v[224:227]\n"
    "v_mfma_f32_16x16x32_bf16 v[228:231], v[184:187], v[164:167], v[228:231]\n"
    "v_lshlrev_b32 v72, 16, v90\n"
    "v_mfma_f32_16x16x32_bf16 v[228:231], v[152:155], v[132:135], v[228:231]\n"
    "v_and_b32 v73, 0xffff0000, v90\n"
    "v_mfma_f32_16x16x32_bf16 v[232:235], v[152:155], v[136:139], v[232:235]\n"
    "v_mfma_f32_16x16x32_bf16 v[232:235], v[184:187], v[168:171], v[232:235]\n"
    "v_lshlrev_b32 v74, 16, v91\n"
    "v_mfma_f32_16x16x32_bf16 v[236:239], v[184:187], v[172:175], v[236:239]\n"
    "v_and_b32 v75, 0xffff0000, v91\n"
    "v_mfma_f32_16x16x32_bf16 v[236:239], v[152:155], v[140:143], v[236:239]\n"
    "v_mfma_f32_16x16x32_bf16 v[252:255], v[156:159], v[140:143], v[252:255]\n"
    "s_sub_u32 s43, 514, s47\n"
    "s_lshl_b32 s43, s43, 22\n"
    "v_cvt_pkrtz_f16_f32 v12, s43, s43\n"
    "v_mfma_f32_16x16x32_bf16 v[252:255], v[188:191], v[172:175], v[252:255]\n"
    "v_lshl_or_b32 v21, v12, 17, s47\n"
    "v_mfma_f32_16x16x32_bf16 v[248:251], v[188:191], v[168:171], v[248:251]\n"
    "v_mfma_f32_16x16x32_bf16 v[248:251], v[156:159], v[136:139], v[248:251]\n"
    "v_alignbit_b32 v21, v41, v21, 1\n"
    "v_mfma_f32_16x16x32_bf16 v[244:247], v[156:159], v[132:135], v[244:247]\n"
    "s_mov_b64 exec, s[48:49]\n"
    "ds_write_b128 v15, v[28:31] offset:0\n"
    "ds_write_b128 v15, v[32:35] offset:160\n"
    "ds_write_b128 v15, v[36:39] offset:320\n"
    "ds_write_b32 v16, v40 offset:3840\n"
    "ds_write_b32 v16, v12 offset:4160\n"
    "ds_write_b32 v25, v21 offset:532\n"
    "s_mov_b64 exec, -1\n"
    "v_mfma_f32_16x16x32_bf16 v[244:247], v[188:191], v[164:167], v[244:247]\n"
    "v_mfma_f32_16x16x32_bf16 v[240:243], v[188:191], v[160:163], v[240:243]\n"
    "v_mfma_f32_16x16x32_bf16 v[240:243], v[156:159], v[128:131], v[240:243]\n"
    "s_cmp_lg_u32 s21, 0\n"
    "s_cbranch_scc1 .Lxd_g1\n"
    "s_barrier\n"
    ".Lxd_g1:\n"
    "v_lshlrev_b32 v42, 4, v1\n"
    "v_add_u32 v42, 98304, v42\n"
    "v_mov_b32 v12, 0xc0135761\n"
    "s_mov_b32 s64, 0xbdd2d3e8\n"
    "s_cmp_lt_u32 s22, 2\n"
    "s_cselect_b32 s40, 16, 0\n"
    "v_add_u32 v25, s40, v25\n"
    "s_mov_b64 s[48:49], 0\n"
    "s_cmp_eq_u32 s21, 0\n"
    "s_cselect_b64 vcc, 0, -1\n"
    "s_cbranch_scc1 .Lxd_gp\n"
    "s_setprio 1\n"
    ".Lxd_gp:\n"
    "ds_read_b128 v[92:95], v42 offset:0\n"
    "ds_read_b128 v[96:99], v42 offset:1040\n"
    "ds_read_b128 v[100:103], v42 offset:2080\n"
    "ds_read_b128 v[104:107], v42 offset:3120\n"
    "ds_read_b128 v[108:111], v42 offset:4160\n"
    "s_setprio 3\n"
    "v_pk_mul_f32 v[76:77], v[192:193], v[192:193]\n"
    "v_pk_mul_f32 v[78:79], v[196:197], v[196:197]\n"
    "v_pk_mul_f32 v[80:81], v[200:201], v[200:201]\n"
    "v_pk_mul_f32 v[82:83], v[204:205], v[204:205]\n"
    "v_pk_fma_f32 v[76:77], v[76:77], s[64:65], v[12:13] op_sel_hi:[1,0,0]\n"
    "v_pk_fma_f32 v[78:79], v[78:79], s[64:65], v[12:13] op_sel_hi:[1,0,0]\n"
    "v_pk_fma_f32 v[80:81], v[80:81], s[64:65], v[12:13] op_sel_hi:[1,0,0]\n"
    "v_pk_fma_f32 v[82:83], v[82:83], s[64:65], v[12:13] op_sel_hi:[1,0,0]\n"
    "v_pk_mul_f32 v[76:77], v[76:77], v[192:193]\n"
    "v_pk_mul_f32 v[78:79], v[78:79], v[196:197]\n"
    "v_pk_mul_f32 v[80:81], v[80:81], v[200:201]\n"
    "v_pk_mul_f32 v[82:83], v[82:83], v[204:205]\n"
    "v_exp_f32 v76, v76\n"
    "v_exp_f32 v77, v77\n"
    "v_exp_f32 v78, v78\n"
    "v_exp_f32 v79, v79\n"
    "v_exp_f32 v80, v80\n"
    "v_exp_f32 v81, v81\n"
    "v_exp_f32 v82, v82\n"
    "v_exp_f32 v83, v83\n"
    "v_pk_mul_f32 v[192:193], v[192:193], v[60:61]\n"
    "v_pk_mul_f32 v[196:197], v[196:197], v[60:61]\n"
    "v_pk_mul_f32 v[200:201], v[200:201], v[60:61]\n"
    "v_pk_mul_f32 v[204:205], v[204:205], v[60:61]\n"
    "v_pk_add_f32 v[76:77], v[76:77], 1.0 op_sel_hi:[1,0]\n"
    "v_pk_add_f32 v[78:79], v[78:79], 1.0 op_sel_hi:[1,0]\n"
    "v_pk_add_f32 v[80:81], v[80:81], 1.0 op_sel_hi:[1,0]\n"
    "v_pk_add_f32 v[82:83], v[82:83], 1.0 op_sel_hi:[1,0]\n"
    "v_rcp_f32 v76, v76\n"
    "v_rcp_f32 v77, v77\n"
    "v_rcp_f32 v78, v78\n"
    "v_rcp_f32 v79, v79\n"
    "v_rcp_f32 v80, v80\n"
    "v_rcp_f32 v81, v81\n"
    "v_rcp_f32 v82, v82\n"
    "v_rcp_f32 v83, v83\n"
    "v_pk_mul_f32 v[84:85], v[192:193], v[76:77]\n"
    "v_pk_mul_f32 v[86:87], v[196:197], v[78:79]\n"
    "v_pk_mul_f32 v[88:89], v[200:201], v[80:81]\n"
    "v_pk_mul_f32 v[90:91], v[204:205], v[82:83]\n"
    "ds_read_b128 v[112:115], v42 offset:5200\n"
    "ds_read_b128 v[116:119], v42 offset:6240\n"
    "ds_read_b128 v[120:123], v42 offset:7280\n"
    "ds_read_b128 v[124:127], v42 offset:8320\n"
    "ds_read_b128 v[128:131], v42 offset:9360\n"
    "s_cmp_eq_u32 s22, 3\n"
    "s_cbranch_scc1 .Lxd_on6\n"
    ".Lxd_on6r:\n"
    "s_cmp_eq_u32 s22, 0\n"
    "s_cbranch_scc1 .Lxd_on7\n"
    ".Lxd_on7r:\n"
    "s_setprio 3\n"
    "v_pk_mul_f32 v[76:77], v[194:195], v[194:195]\n"
    "v_pk_mul_f32 v[78:79], v[198:199], v[198:199]\n"
    "v_pk_mul_f32 v[80:81], v[202:203], v[202:203]\n"
    "v_pk_mul_f32 v[82:83], v[206:207], v[206:207]\n"
    "v_pk_fma_f32 v[76:77], v[76:77], s[64:65], v[12:13] op_sel_hi:[1,0,0]\n"
    "v_pk_fma_f32 v[78:79], v[78:79], s[64:65], v[12:13] op_sel_hi:[1,0,0]\n"
    "v_pk_fma_f32 v[80:81], v[80:81], s[64:65], v[12:13] op_sel_hi:[1,0,0]\n"
    "v_pk_fma_f32 v[82:83], v[82:83], s[64:65], v[12:13] op_sel_hi:[1,0,0]\n"
    "v_pk_mul_f32 v[76:77], v[76:77], v[194:195]\n"
    "v_pk_mul_f32 v[78:79], v[78:79], v[198:199]\n"
    "v_pk_mul_f32 v[80:81], v[80:81], v[202:203]\n"
    "v_pk_mul_f32 v[82:83], v[82:83], v[206:207]\n"
    "v_exp_f32 v76, v76\n"
    "v_exp_f32 v77, v77\n"
    "v_exp_f32 v78, v78\n"
    "v_exp_f32 v79, v79\n"
    "v_exp_f32 v80, v80\n"
    "v_exp_f32 v81, v81\n"
    "v_exp_f32 v82, v82\n"
    "v_exp_f32 v83, v83\n"
    "v_pk_mul_f32 v[194:195], v[194:195], v[62:63]\n"
    "v_pk_mul_f32 v[198:199], v[198:199], v[62:63]\n"
    "v_pk_mul_f32 v[202:203], v[202:203], v[62:63]\n"
    "v_pk_mul_f32 v[206:207], v[206:207], v[62:63]\n"
    "v_pk_add_f32 v[76:77], v[76:77], 1.0 op_sel_hi:[1,0]\n"
    "v_pk_add_f32 v[78:79], v[78:79], 1.0 op_sel_hi:[1,0]\n"
    "v_pk_add_f32 v[80:81], v[80:81], 1.0 op_sel_hi:[1,0]\n"
    "v_pk_add_f32 v[82:83], v[82:83], 1.0 op_sel_hi:[1,0]\n"
    "v_rcp_f32 v76, v76\n"
    "v_rcp_f32 v77, v77\n"
    "v_rcp_f32 v78, v78\n"
    "v_rcp_f32 v79, v79\n"
    "v_rcp_f32 v80, v80\n"
    "v_rcp_f32 v81, v81\n"
    "v_rcp_f32 v82, v82\n"
    "v_rcp_f32 v83, v83\n"
    "v_pk_fma_f32 v[84:85], v[194:195], v[76:77], v[84:85]\n"
    "v_pk_fma_f32 v[86:87], v[198:199], v[78:79], v[86:87]\n"
    "v_pk_fma_f32 v[88:89], v[202:203], v[80:81], v[88:89]\n"
    "v_pk_fma_f32 v[90:91], v[206:207], v[82:83], v[90:91]\n"
    "ds_read_b128 v[192:195], v42 offset:26000\n"
    "ds_read_b128 v[196:199], v42 offset:27040\n"
    "ds_read_b128 v[200:203], v42 offset:28080\n"
    "ds_read_b128 v[204:207], v42 offset:29120\n"
    "ds_read_b128 v[132:135], v42 offset:10400\n"
    "ds_read_b128 v[136:139], v42 offset:11440\n"
    "ds_read_b128 v[140:143], v42 offset:12480\n"
    "ds_read_b128 v[144:147], v42 offset:13520\n"
    "ds_read_b128 v[148:151], v42 offset:14560\n"
    "s_setprio 2\n"
    "v_pk_mul_f32 v[76:77], v[208:209], v[208:209]\n"
    "v_pk_mul_f32 v[78:79], v[212:213], v[212:213]\n"
    "v_pk_mul_f32 v[80:81], v[216:217], v[216:217]\n"
    "v_pk_mul_f32 v[82:83], v[220:221], v[220:221]\n"
    "v_pk_fma_f32 v[76:77], v[76:77], s[64:65], v[12:13] op_sel_hi:[1,0,0]\n"
    "v_pk_fma_f32 v[78:79], v[78:79], s[64:65], v[12:13] op_sel_hi:[1,0,0]\n"
    "v_pk_fma_f32 v[80:81], v[80:81], s[64:65], v[12:13] op_sel_hi:[1,0,0]\n"
    "v_pk_fma_f32 v[82:83], v[82:83], s[64:65], v[12:13] op_sel_hi:[1,0,0]\n"
    "v_pk_mul_f32 v[76:77], v[76:77], v[208:209]\n"
    "v_pk_mul_f32 v[78:79], v[78:79], v[212:213]\n"
    "v_pk_mul_f32 v[80:81], v[80:81], v[216:217]\n"
    "v_pk_mul_f32 v[82:83], v[82:83], v[220:221]\n"
    "v_exp_f32 v76, v76\n"
    "v_exp_f32 v77, v77\n"
    "v_exp_f32 v78, v78\n"
    "v_exp_f32 v79, v79\n"
    "v_exp_f32 v80, v80\n"
    "v_exp_f32 v81, v81\n"
    "v_exp_f32 v82, v82\n"
    "v_exp_f32 v83, v83\n"
    "v_pk_mul_f32 v[208:209], v[208:209], v[64:65]\n"
    "v_pk_mul_f32 v[212:213], v[212:213], v[64:65]\n"
    "v_pk_mul_f32 v[216:217], v[216:217], v[64:65]\n"
    "v_pk_mul_f32 v[220:221], v[220:221], v[64:65]\n"
    "v_pk_add_f32 v[76:77], v[76:77], 1.0 op_sel_hi:[1,0]\n"
    "v_pk_add_f32 v[78:79], v[78:79], 1.0 op_sel_hi:[1,0]\n"
    "v_pk_add_f32 v[80:81], v[80:81], 1.0 op_sel_hi:[1,0]\n"
    "v_pk_add_f32 v[82:83], v[82:83], 1.0 op_sel_hi:[1,0]\n"
    "v_rcp_f32 v76, v76\n"
    "v_rcp_f32 v77, v77\n"
    "v_rcp_f32 v78, v78\n"
    "v_rcp_f32 v79, v79\n"
    "v_rcp_f32 v80, v80\n"
    "v_rcp_f32 v81, v81\n"
    "v_rcp_f32 v82, v82\n"
    "v_rcp_f32 v83, v83\n"
    "v_pk_fma_f32 v[84:85], v[208:209], v[76:77], v[84:85]\n"
    "v_pk_fma_f32 v[86:87], v[212:213], v[78:79], v[86:87]\n"
    "v_pk_fma_f32 v[88:89], v[216:217], v[80:81], v[88:89]\n"
    "v_pk_fma_f32 v[90:91], v[220:221], v[82:83], v[90:91]\n"
    "ds_read_b128 v[152:155], v42 offset:15600\n"
    "ds_read_b128 v[156:159], v42 offset:16640\n"
    "ds_read_b128 v[160:163], v42 offset:17680\n"
    "ds_read_b128 v[164:167], v42 offset:18720\n"
    "ds_read_b128 v[168:171], v42 offset:19760\n"
    "v_mov_b32 v13, s58\n"
    "ds_read_b32 v22, v13 offset:512\n"
    "s_setprio 2\n"
    "v_pk_mul_f32 v[76:77], v[210:211], v[210:211]\n"
    "v_pk_mul_f32 v[78:79], v[214:215], v[214:215]\n"
    "v_pk_mul_f32 v[80:81], v[218:219], v[218:219]\n"
    "v_pk_mul_f32 v[82:83], v[222:223], v[222:223]\n"
    "v_pk_fma_f32 v[76:77], v[76:77], s[64:65], v[12:13] op_sel_hi:[1,0,0]\n"
    "v_pk_fma_f32 v[78:79], v[78:79], s[64:65], v[12:13] op_sel_hi:[1,0,0]\n"
    "v_pk_fma_f32 v[80:81], v[80:81], s[64:65], v[12:13] op_sel_hi:[1,0,0]\n"
    "v_pk_fma_f32 v[82:83], v[82:83], s[64:65], v[12:13] op_sel_hi:[1,0,0]\n"
    "v_pk_mul_f32 v[76:77], v[76:77], v[210:211]\n"
    "v_pk_mul_f32 v[78:79], v[78:79], v[214:215]\n"
    "v_pk_mul_f32 v[80:81], v[80:81], v[218:219]\n"
    "v_pk_mul_f32 v[82:83], v[82:83], v[222:223]\n"
    "v_exp_f32 v76, v76\n"
    "v_exp_f32 v77, v77\n"
    "v_exp_f32 v78, v78\n"
    "v_exp_f32 v79, v79\n"
    "v_exp_f32 v80, v80\n"
    "v_exp_f32 v81, v81\n"
    "v_exp_f32 v82, v82\n"
    "v_exp_f32 v83, v83\n"
    "v_pk_mul_f32 v[210:211], v[210:211], v[66:67]\n"
    "v_pk_mul_f32 v[214:215], v[214:215], v[66:67]\n"
    "v_pk_mul_f32 v[218:219], v[218:219], v[66:67]\n"
    "v_pk_mul_f32 v[222:223], v[222:223], v[66:67]\n"
    "v_pk_add_f32 v[76:77], v[76:77], 1.0 op_sel_hi:[1,0]\n"
    "v_pk_add_f32 v[78:79], v[78:79], 1.0 op_sel_hi:[1,0]\n"
    "v_pk_add_f32 v[80:81], v[80:81], 1.0 op_sel_hi:[1,0]\n"
    "v_pk_add_f32 v[82:83], v[82:83], 1.0 op_sel_hi:[1,0]\n"
    "v_rcp_f32 v76, v76\n"
    "v_rcp_f32 v77, v77\n"
    "v_rcp_f32 v78, v78\n"
    "v_rcp_f32 v79, v79\n"
    "v_rcp_f32 v80, v80\n"
    "v_rcp_f32 v81, v81\n"
    "v_rcp_f32 v82, v82\n"
    "v_rcp_f32 v83, v83\n"
    "v_pk_fma_f32 v[84:85], v[210:211], v[76:77], v[84:85]\n"
    "v_pk_fma_f32 v[86:87], v[214:215], v[78:79], v[86:87]\n"
    "v_pk_fma_f32 v[88:89], v[218:219], v[80:81], v[88:89]\n"
    "v_pk_fma_f32 v[90:91], v[222:223], v[82:83], v[90:91]\n"
    "ds_read_b128 v[208:211], v42 offset:30160\n"
    "ds_read_b128 v[212:215], v42 offset:31200\n"
    "ds_read_b128 v[216:219], v42 offset:32240\n"
    "ds_read_b128 v[220:223], v42 offset:33280\n"
    "ds_read_b128 v[172:175], v42 offset:20800\n"
    "ds_read_b128 v[176:179], v42 offset:21840\n"
    "ds_read_b128 v[180:183], v42 offset:22880\n"
    "ds_read_b128 v[184:187], v42 offset:23920\n"
    "ds_read_b128 v[188:191], v42 offset:24960\n"
    "s_waitcnt lgkmcnt(9)\n"
    "v_readfirstlane_b32 s45, v22\n"
    "s_cmp_lt_i32 s45, 1\n"
    "s_cbranch_scc1 .Lxd_ck8\n"
    ".Lxd_ck8r:\n"
    "s_sub_u32 s40, 1, s22\n"
    "s_and_b32 s40, s40, 3\n"
    "s_mul_i32 s40, s40, 4480\n"
    "v_add_u32 v7, s40, v3\n"
    "v_add_u32 v8, s40, v4\n"
    "v_add_u32 v9, s40, v5\n"
    "v_add_u32 v10, s40, v6\n"
    "ds_read_b64_tr_b16 v[44:45], v9 offset:0\n"
    "ds_read_b64_tr_b16 v[46:47], v10 offset:0\n"
    "ds_read_b64_tr_b16 v[48:49], v7 offset:0\n"
    "ds_read_b64_tr_b16 v[50:51], v8 offset:640\n"
    "ds_read_b64_tr_b16 v[52:53], v8 offset:1280\n"
    "ds_read_b64_tr_b16 v[54:55], v8 offset:1920\n"
    "ds_read_b64_tr_b16 v[56:57], v8 offset:2560\n"
    "ds_read_b64_tr_b16 v[58:59], v8 offset:3200\n"
    "s_setprio 1\n"
    "v_pk_mul_f32 v[76:77], v[224:225], v[224:225]\n"
    "v_pk_mul_f32 v[78:79], v[228:229], v[228:229]\n"
    "v_pk_mul_f32 v[80:81], v[232:233], v[232:233]\n"
    "v_pk_mul_f32 v[82:83], v[236:237], v[236:237]\n"
    "v_pk_fma_f32 v[76:77], v[76:77], s[64:65], v[12:13] op_sel_hi:[1,0,0]\n"
    "v_pk_fma_f32 v[78:79], v[78:79], s[64:65], v[12:13] op_sel_hi:[1,0,0]\n"
    "v_pk_fma_f32 v[80:81], v[80:81], s[64:65], v[12:13] op_sel_hi:[1,0,0]\n"
    "v_pk_fma_f32 v[82:83], v[82:83], s[64:65], v[12:13] op_sel_hi:[1,0,0]\n"
    "v_pk_mul_f32 v[76:77], v[76:77], v[224:225]\n"
    "v_pk_mul_f32 v[78:79], v[78:79], v[228:229]\n"
    "v_pk_mul_f32 v[80:81], v[80:81], v[232:233]\n"
    "v_pk_mul_f32 v[82:83], v[82:83], v[236:237]\n"
    "v_exp_f32 v76, v76\n"
    "v_exp_f32 v77, v77\n"
    "v_exp_f32 v78, v78\n"
    "v_exp_f32 v79, v79\n"
    "v_exp_f32 v80, v80\n"
    "v_exp_f32 v81, v81\n"
    "v_exp_f32 v82, v82\n"
    "v_exp_f32 v83, v83\n"
    "v_pk_mul_f32 v[224:225], v[224:225], v[68:69]\n"
    "v_pk_mul_f32 v[228:229], v[228:229], v[68:69]\n"
    "v_pk_mul_f32 v[232:233], v[232:233], v[68:69]\n"
    "v_pk_mul_f32 v[236:237], v[236:237], v[68:69]\n"
    "v_pk_add_f32 v[76:77], v[76:77], 1.0 op_sel_hi:[1,0]\n"
    "v_pk_add_f32 v[78:79], v[78:79], 1.0 op_sel_hi:[1,0]\n"
    "v_pk_add_f32 v[80:81], v[80:81], 1.0 op_sel_hi:[1,0]\n"
    "v_pk_add_f32 v[82:83], v[82:83], 1.0 op_sel_hi:[1,0]\n"
    "v_rcp_f32 v76, v76\n"
    "v_rcp_f32 v77, v77\n"
    "v_rcp_f32 v78, v78\n"
    "v_rcp_f32 v79, v79\n"
    "v_rcp_f32 v80, v80\n"
    "v_rcp_f32 v81, v81\n"
    "v_rcp_f32 v82, v82\n"
    "v_rcp_f32 v83, v83\n"
    "v_pk_fma_f32 v[84:85], v[224:225], v[76:77], v[84:85]\n"
    "v_pk_fma_f32 v[86:87], v[228:229], v[78:79], v[86:87]\n"
    "v_pk_fma_f32 v[88:89], v[232:233], v[80:81], v[88:89]\n"
    "v_pk_fma_f32 v[90:91], v[236:237], v[82:83], v[90:91]\n"
    "s_cmp_eq_u32 s22, 3\n"
    "s_cbranch_scc1 .Lxd_on9\n"
    ".Lxd_on9r:\n"
    "s_cmp_eq_u32 s22, 0\n"
    "s_cbranch_scc1 .Lxd_on10\n"
    ".Lxd_on10r:\n"
    "s_cmp_eq_u32 s22, 1\n"
    "s_cbranch_scc1 .Lxd_on11\n"
    ".Lxd_on11r:\n"
    "s_setprio 1\n"
    "v_pk_mul_f32 v[76:77], v[226:227], v[226:227]\n"
    "v_pk_mul_f32 v[78:79], v[230:231], v[230:231]\n"
    "v_pk_mul_f32 v[80:81], v[234:235], v[234:235]\n"
    "v_pk_mul_f32 v[82:83], v[238:239], v[238:239]\n"
    "v_pk_fma_f32 v[76:77], v[76:77], s[64:65], v[12:13] op_sel_hi:[1,0,0]\n"
    "v_pk_fma_f32 v[78:79], v[78:79], s[64:65], v[12:13] op_sel_hi:[1,0,0]\n"
    "v_pk_fma_f32 v[80:81], v[80:81], s[64:65], v[12:13] op_sel_hi:[1,0,0]\n"
    "v_pk_fma_f32 v[82:83], v[82:83], s[64:65], v[12:13] op_sel_hi:[1,0,0]\n"
    "v_pk_mul_f32 v[76:77], v[76:77], v[226:227]\n"
    "v_pk_mul_f32 v[78:79], v[78:79], v[230:231]\n"
    "v_pk_mul_f32 v[80:81], v[80:81], v[234:235]\n"
    "v_pk_mul_f32 v[82:83], v[82:83], v[238:239]\n"
    "v_exp_f32 v76, v76\n"
    "v_exp_f32 v77, v77\n"
    "v_exp_f32 v78, v78\n"
    "v_exp_f32 v79, v79\n"
    "v_exp_f32 v80, v80\n"
    "v_exp_f32 v81, v81\n"
    "v_exp_f32 v82, v82\n"
    "v_exp_f32 v83, v83\n"
    "v_pk_mul_f32 v[226:227], v[226:227], v[70:71]\n"
    "v_pk_mul_f32 v[230:231], v[230:231], v[70:71]\n"
    "v_pk_mul_f32 v[234:235], v[234:235], v[70:71]\n"
    "v_pk_mul_f32 v[238:239], v[238:239], v[70:71]\n"
    "v_pk_add_f32 v[76:77], v[76:77], 1.0 op_sel_hi:[1,0]\n"
    "v_pk_add_f32 v[78:79], v[78:79], 1.0 op_sel_hi:[1,0]\n"
    "v_pk_add_f32 v[80:81], v[80:81], 1.0 op_sel_hi:[1,0]\n"
    "v_pk_add_f32 v[82:83], v[82:83], 1.0 op_sel_hi:[1,0]\n"
    "v_rcp_f32 v76, v76\n"
    "v_rcp_f32 v77, v77\n"
    "v_rcp_f32 v78, v78\n"
    "v_rcp_f32 v79, v79\n"
    "v_rcp_f32 v80, v80\n"
    "v_rcp_f32 v81, v81\n"
    "v_rcp_f32 v82, v82\n"
    "v_rcp_f32 v83, v83\n"
    "v_pk_fma_f32 v[84:85], v[226:227], v[76:77], v[84:85]\n"
    "v_pk_fma_f32 v[86:87], v[230:231], v[78:79], v[86:87]\n"
    "v_pk_fma_f32 v[88:89], v[234:235], v[80:81], v[88:89]\n"
    "v_pk_fma_f32 v[90:91], v[238:239], v[82:83], v[90:91]\n"
    "ds_read_b128 v[224:227], v42 offset:34320\n"
    "ds_read_b128 v[228:231], v42 offset:35360\n"
    "ds_read_b128 v[232:235], v42 offset:36400\n"
    "ds_read_b128 v[236:239], v42 offset:37440\n"
    "s_setprio 0\n"
    "v_pk_mul_f32 v[76:77], v[240:241], v[240:241]\n"
    "v_pk_mul_f32 v[78:79], v[244:245], v[244:245]\n"
    "v_pk_mul_f32 v[80:81], v[248:249], v[248:249]\n"
    "v_pk_mul_f32 v[82:83], v[252:253], v[252:253]\n"
    "v_pk_fma_f32 v[76:77], v[76:77], s[64:65], v[12:13] op_sel_hi:[1,0,0]\n"
    "v_pk_fma_f32 v[78:79], v[78:79], s[64:65], v[12:13] op_sel_hi:[1,0,0]\n"
    "v_pk_fma_f32 v[80:81], v[80:81], s[64:65], v[12:13] op_sel_hi:[1,0,0]\n"
    "v_pk_fma_f32 v[82:83], v[82:83], s[64:65], v[12:13] op_sel_hi:[1,0,0]\n"
    "v_pk_mul_f32 v[76:77], v[76:77], v[240:241]\n"
    "v_pk_mul_f32 v[78:79], v[78:79], v[244:245]\n"
    "v_pk_mul_f32 v[80:81], v[80:81], v[248:249]\n"
    "v_pk_mul_f32 v[82:83], v[82:83], v[252:253]\n"
    "v_exp_f32 v76, v76\n"
    "v_exp_f32 v77, v77\n"
    "v_exp_f32 v78, v78\n"
    "v_exp_f32 v79, v79\n"
    "v_exp_f32 v80, v80\n"
    "v_exp_f32 v81, v81\n"
    "v_exp_f32 v82, v82\n"
    "v_exp_f32 v83, v83\n"
    "v_pk_mul_f32 v[240:241], v[240:241], v[72:73]\n"
    "v_pk_mul_f32 v[244:245], v[244:245], v[72:73]\n"
    "v_pk_mul_f32 v[248:249], v[248:249], v[72:73]\n"
    "v_pk_mul_f32 v[252:253], v[252:253], v[72:73]\n"
    "v_pk_add_f32 v[76:77], v[76:77], 1.0 op_sel_hi:[1,0]\n"
    "v_pk_add_f32 v[78:79], v[78:79], 1.0 op_sel_hi:[1,0]\n"
    "v_pk_add_f32 v[80:81], v[80:81], 1.0 op_sel_hi:[1,0]\n"
    "v_pk_add_f32 v[82:83], v[82:83], 1.0 op_sel_hi:[1,0]\n"
    "v_rcp_f32 v76, v76\n"
    "v_rcp_f32 v77, v77\n"
    "v_rcp_f32 v78, v78\n"
    "v_rcp_f32 v79, v79\n"
    "v_rcp_f32 v80, v80\n"
    "v_rcp_f32 v81, v81\n"
    "v_rcp_f32 v82, v82\n"
    "v_rcp_f32 v83, v83\n"
    "v_pk_fma_f32 v[84:85], v[240:241], v[76:77], v[84:85]\n"
    "v_pk_fma_f32 v[86:87], v[244:245], v[78:79], v[86:87]\n"
    "v_pk_fma_f32 v[88:89], v[248:249], v[80:81], v[88:89]\n"
    "v_pk_fma_f32 v[90:91], v[252:253], v[82:83], v[90:91]\n"
    "v_mov_b32 v13, 154112\n"
    "ds_read_b64 v[26:27], v13\n"
    "s_setprio 0\n"
    "v_pk_mul_f32 v[76:77], v[242:243], v[242:243]\n"
    "v_pk_mul_f32 v[78:79], v[246:247], v[246:247]\n"
    "v_pk_mul_f32 v[80:81], v[250:251], v[250:251]\n"
    "v_pk_mul_f32 v[82:83], v[254:255], v[254:255]\n"
    "v_pk_fma_f32 v[76:77], v[76:77], s[64:65], v[12:13] op_sel_hi:[1,0,0]\n"
    "v_pk_fma_f32 v[78:79], v[78:79], s[64:65], v[12:13] op_sel_hi:[1,0,0]\n"
    "v_pk_fma_f32 v[80:81], v[80:81], s[64:65], v[12:13] op_sel_hi:[1,0,0]\n"
    "v_pk_fma_f32 v[82:83], v[82:83], s[64:65], v[12:13] op_sel_hi:[1,0,0]\n"
    "v_pk_mul_f32 v[76:77], v[76:77], v[242:243]\n"
    "v_pk_mul_f32 v[78:79], v[78:79], v[246:247]\n"
    "v_pk_mul_f32 v[80:81], v[80:81], v[250:251]\n"
    "v_pk_mul_f32 v[82:83], v[82:83], v[254:255]\n"
    "v_exp_f32 v76, v76\n"
    "v_exp_f32 v77, v77\n"
    "v_exp_f32 v78, v78\n"
    "v_exp_f32 v79, v79\n"
    "v_exp_f32 v80, v80\n"
    "v_exp_f32 v81, v81\n"
    "v_exp_f32 v82, v82\n"
    "v_exp_f32 v83, v83\n"
    "v_pk_mul_f32 v[242:243], v[242:243], v[74:75]\n"
    "v_pk_mul_f32 v[246:247], v[246:247], v[74:75]\n"
    "v_pk_mul_f32 v[250:251], v[250:251], v[74:75]\n"
    "v_pk_mul_f32 v[254:255], v[254:255], v[74:75]\n"
    "v_pk_add_f32 v[76:77], v[76:77], 1.0 op_sel_hi:[1,0]\n"
    "v_pk_add_f32 v[78:79], v[78:79], 1.0 op_sel_hi:[1,0]\n"
    "v_pk_add_f32 v[80:81], v[80:81], 1.0 op_sel_hi:[1,0]\n"
    "v_pk_add_f32 v[82:83], v[82:83], 1.0 op_sel_hi:[1,0]\n"
    "v_rcp_f32 v76, v76\n"
    "v_rcp_f32 v77, v77\n"
    "v_rcp_f32 v78, v78\n"
    "v_rcp_f32 v79, v79\n"
    "v_rcp_f32 v80, v80\n"
    "v_rcp_f32 v81, v81\n"
    "v_rcp_f32 v82, v82\n"
    "v_rcp_f32 v83, v83\n"
    "v_pk_fma_f32 v[84:85], v[242:243], v[76:77], v[84:85]\n"
    "v_pk_fma_f32 v[86:87], v[246:247], v[78:79], v[86:87]\n"
    "v_pk_fma_f32 v[88:89], v[250:251], v[80:81], v[88:89]\n"
    "v_pk_fma_f32 v[90:91], v[254:255], v[82:83], v[90:91]\n"
    "ds_read_b128 v[240:243], v42 offset:38480\n"
    "ds_read_b128 v[244:247], v42 offset:39520\n"
    "ds_read_b128 v[248:251], v42 offset:40560\n"
    "ds_read_b128 v[252:255], v42 offset:41600\n"
    "v_add_f32 v84, v84, v85\n"
    "v_add_f32 v86, v86, v87\n"
    "v_add_f32 v88, v88, v89\n"
    "v_add_f32 v90, v90, v91\n"
    "s_lshl_b32 s40, s21, 1\n"
    "s_and_b32 s41, s20, 1\n"
    "s_add_u32 s40, s40, s41\n"
    "v_permlane32_swap_b32 v84, v88\n"
    "v_permlane32_swap_b32 v86, v90\n"
    "v_add_f32 v84, v84, v88\n"
    "v_add_f32 v86, v86, v90\n"
    "s_lshl_b32 s41, s40, 8\n"
    "s_add_u32 s41, s41, 149504\n"
    "v_permlane16_swap_b32 v84, v86\n"
    "v_add_f32 v84, v84, v86\n"
    "v_lshl_add_u32 v85, v1, 2, s41\n"
    "v_and_b32 v84, -2, v84\n"
    "ds_wrxchg_rtn_b32 v85, v85, v84\n"
    "s_lshl_b32 s40, s40, 6\n"
    "s_add_u32 s40, s40, s24\n"
    "s_lshl_b32 s41, s23, 12\n"
    "s_add_u32 s40, s40, s41\n"
    "s_lshl_b32 s40, s40, 3\n"
    "s_add_u32 s40, s40, 65536\n"
    "s_add_u32 s40, s40, s10\n"
    "s_addc_u32 s41, s11, 0\n"
    "s_waitcnt lgkmcnt(10)\n"
    "v_pk_mul_f16 v60, v44, s45 op_sel:[0,1]\n"
    "v_pk_mul_f16 v61, v45, s45 op_sel:[0,1]\n"
    "v_pk_mul_f16 v62, v46, s45 op_sel:[0,1]\n"
    "v_pk_mul_f16 v63, v47, s45 op_sel:[0,1]\n"
    "v_pk_mul_f16 v64, v44, v46\n"
    "v_pk_mul_f16 v65, v45, v47\n"
    "v_pk_mul_f16 v66, v48, v48 op_sel_hi:[0,1]\n"
    "v_pk_mul_f16 v67, v48, v49 op_sel_hi:[0,1]\n"
    "v_lshlrev_b32 v86, 3, v1\n"
    "v_mov_b64 v[88:89], 0\n"
    "v_mov_b64 v[90:91], 0\n"
    "s_waitcnt lgkmcnt(0)\n"
    "v_cmp_ne_u32 vcc, -1, v85\n"
    "v_add_f32 v84, v84, v85\n"
    "v_add_u32 v84, 0x80, v84\n"
    "v_and_b32 v84, 0xffffff00, v84\n"
    "v_or_b32 v26, v84, v26\n"
    "s_nop 0\n"
    "s_cbranch_vccz .Lxd_nost\n"
    "s_mov_b64 exec, vcc\n"
    "global_store_dwordx2 v86, v[26:27], s[40:41] sc1\n"
    "s_mov_b64 exec, -1\n"
    ".Lxd_nost:\n"
    "s_barrier\n"
    ".Lxd_enter:\n"
    "s_cmp_eq_u32 s21, 0\n"
    "s_cselect_b64 vcc, 0, -1\n"
    "s_cbranch_scc1 .Lxd_pr\n"
    "s_setprio 1\n"
    ".Lxd_pr:\n"
    "s_cmp_eq_u32 s22, 0\n"
    "s_cbranch_scc1 .Lxd_ph1\n"
    "s_cmp_eq_u32 s22, 3\n"
    "s_cbranch_scc1 .Lxd_ph2\n"
    "s_cmp_eq_u32 s22, 2\n"
    "s_cbranch_scc1 .Lxd_ph3\n"
    ".Lxd_ph0:\n"
    "s_cbranch_vccnz .Lxd_pr12\n"
    "s_setprio 2\n"
    ".Lxd_pr12:\n"
    "v_smfmac_f32_16x16x64_f16 v[88:91], v[112:115], v[60:67], v2\n"
    "v_fmac_f32 v11, v76, v80\n"
    "v_fmac_f32 v11, v77, v81\n"
    "v_fmac_f32 v11, v78, v82\n"
    "v_fmac_f32 v11, v79, v83\n"
    "v_mfma_f32_16x16x32_f16 v[80:83], v[92:95], v[60:63], 0\n"
    "v_pk_mul_f16 v68, v58, v58 op_sel_hi:[0,1]\n"
    "v_pk_mul_f16 v69, v58, v49 op_sel_hi:[0,1]\n"
    "v_mfma_f32_16x16x32_f16 v[80:83], v[104:107], v[64:67], v[80:83]\n"
    "v_pk_mul_f16 v70, v48, v58 op_sel_hi:[0,1]\n"
    "v_pk_mul_f16 v71, v48, v59 op_sel_hi:[0,1]\n"
    "v_mfma_f32_16x16x32_f16 v[76:79], v[100:103], v[64:67], 0\n"
    "v_fmac_f32 v11, v84, v85\n"
    "v_fma_f32 v10, v86, v87, v11\n"
    "v_mfma_f32_16x16x32_f16 v[84:87], v[108:111], v[64:67], 0\n"
    "v_pk_mul_f16 v72, v50, v58 op_sel_hi:[0,1]\n"
    "v_pk_mul_f16 v73, v50, v59 op_sel_hi:[0,1]\n"
    "v_mfma_f32_16x16x32_f16 v[84:87], v[96:99], v[60:63], v[84:87]\n"
    "v_pk_mul_f16 v74, v52, v58 op_sel_hi:[0,1]\n"
    "v_pk_mul_f16 v75, v52, v59 op_sel_hi:[0,1]\n"
    "s_waitcnt vmcnt(0)\n"
    "v_mfma_f32_16x16x32_f16 v[76:79], v[116:119], v[68:71], v[76:79]\n"
    "v_pk_mul_f16 v60, v54, v58 op_sel_hi:[0,1]\n"
    "v_pk_mul_f16 v61, v54, v59 op_sel_hi:[0,1]\n"
    "v_mfma_f32_16x16x32_f16 v[76:79], v[128:131], v[72:75], v[76:79]\n"
    "v_pk_mul_f16 v62, v56, v58 op_sel_hi:[0,1]\n"
    "v_readfirstlane_b32 s40, v24\n"
    "s_or_b32 s40, s40, s44\n"
    "s_lshl1_add_u32 s41, s40, s54\n"
    "s_cmp_lt_u32 s40, 256\n"
    "s_cselect_b32 s40, s41, -1\n"
    "s_cselect_b32 s41, s41, 2\n"
    "s_cselect_b64 s[48:49], -1, 0\n"
    "v_mov_b32 v12, s40\n"
    "s_mul_i32 s41, s41, 52\n"
    "s_add_u32 s38, s4, s41\n"
    "s_addc_u32 s39, s5, 0\n"
    "v_mfma_f32_16x16x32_f16 v[80:83], v[132:135], v[72:75], v[80:83]\n"
    "v_pk_mul_f16 v63, v56, v59 op_sel_hi:[0,1]\n"
    "v_ashrrev_i32 v21, 31, v12\n"
    "ds_write2_b32 v25, v12, v21 offset0:5 offset1:133\n"
    "global_load_dwordx3 v[36:38], v17, s[38:39] offset:-104\n"
    "global_load_dword v41, v18, s[38:39] offset:-8\n"
    "v_mfma_f32_16x16x32_f16 v[80:83], v[120:123], v[68:71], v[80:83]\n"
    "v_pk_mul_f16 v64, v56, v56 op_sel_hi:[0,1]\n"
    "v_sub_u32 v15, v0, v15\n"
    "v_mfma_f32_16x16x32_f16 v[84:87], v[124:127], v[68:71], v[84:87]\n"
    "v_pk_mul_f16 v65, v56, v49 op_sel_hi:[0,1]\n"
    "v_sub_u32 v16, v23, v16\n"
    "v_mfma_f32_16x16x32_f16 v[84:87], v[136:139], v[72:75], v[84:87]\n"
    "v_pk_mul_f16 v66, v48, v56 op_sel_hi:[0,1]\n"
    "v_permlane32_swap_b32 v7, v8\n"
    "v_permlane32_swap_b32 v9, v10\n"
    "v_smfmac_f32_16x16x64_f16 v[88:91], v[140:143], v[68:75], v2\n"
    "v_pk_mul_f16 v67, v48, v57 op_sel_hi:[0,1]\n"
    "v_add_f32 v7, v7, v8\n"
    "v_add_f32 v9, v9, v10\n"
    "v_mfma_f32_16x16x32_f16 v[76:79], v[144:147], v[60:63], v[76:79]\n"
    "v_pk_mul_f16 v68, v50, v56 op_sel_hi:[0,1]\n"
    "v_pk_mul_f16 v69, v50, v57 op_sel_hi:[0,1]\n"
    "v_mfma_f32_16x16x32_f16 v[76:79], v[156:159], v[64:67], v[76:79]\n"
    "v_pk_mul_f16 v70, v52, v56 op_sel_hi:[0,1]\n"
    "v_permlane16_swap_b32 v7, v9\n"
    "v_mfma_f32_16x16x32_f16 v[80:83], v[160:163], v[64:67], v[80:83]\n"
    "v_pk_mul_f16 v71, v52, v57 op_sel_hi:[0,1]\n"
    "v_add_f32 v7, v7, v9\n"
    "v_mfma_f32_16x16x32_f16 v[80:83], v[148:151], v[60:63], v[80:83]\n"
    "v_pk_mul_f16 v72, v54, v56 op_sel_hi:[0,1]\n"
    "ds_read_b32 v22, v25 offset:516\n"
    "v_mfma_f32_16x16x32_f16 v[84:87], v[152:155], v[60:63], v[84:87]\n"
    "v_pk_mul_f16 v73, v54, v57 op_sel_hi:[0,1]\n"
    "v_add_f32_dpp v7, v7, v7 quad_perm:[1,0,3,2] row_mask:0xf bank_mask:0xf\n"
    "v_mfma_f32_16x16x32_f16 v[84:87], v[164:167], v[64:67], v[84:87]\n"
    "v_pk_mul_f16 v74, v54, v54 op_sel_hi:[0,1]\n"
    "v_add_f32_dpp v7, v7, v7 quad_perm:[2,3,0,1] row_mask:0xf bank_mask:0xf\n"
    "v_smfmac_f32_16x16x64_f16 v[88:91], v[168:171], v[60:67], v2\n"
    "v_pk_mul_f16 v75, v54, v49 op_sel_hi:[0,1]\n"
    "v_add_f32_dpp v7, v7, v7 row_half_mirror row_mask:0xf bank_mask:0xf\n"
    "v_mfma_f32_16x16x32_f16 v[76:79], v[172:175], v[68:71], v[76:79]\n"
    "v_pk_mul_f16 v60, v48, v54 op_sel_hi:[0,1]\n"
    "v_pk_mul_f16 v61, v48, v55 op_sel_hi:[0,1]\n"
    "v_mfma_f32_16x16x32_f16 v[76:79], v[184:187], v[72:75], v[76:79]\n"
    "v_pk_mul_f16 v62, v50, v54 op_sel_hi:[0,1]\n"
    "v_add_f32_dpp v7, v7, v7 row_mirror row_mask:0xf bank_mask:0xf\n"
    "v_mfma_f32_16x16x32_f16 v[80:83], v[188:191], v[72:75], v[80:83]\n"
    "v_pk_mul_f16 v63, v50, v55 op_sel_hi:[0,1]\n"
    "s_waitcnt lgkmcnt(0)\n"
    "v_readfirstlane_b32 s45, v22\n"
    "s_cmp_lt_i32 s45, 1\n"
    "s_cbranch_scc1 .Lxd_ck14\n"
    ".Lxd_ck14r:\n"
    "v_mfma_f32_16x16x32_f16 v[80:83], v[176:179], v[68:71], v[80:83]\n"
    "v_pk_mul_f16 v64, v52, v54 op_sel_hi:[0,1]\n"
    "ds_read_b64_tr_b16 v[44:45], v5 offset:4480\n"
    "v_mfma_f32_16x16x32_f16 v[84:87], v[180:183], v[68:71], v[84:87]\n"
    "v_pk_mul_f16 v65, v52, v55 op_sel_hi:[0,1]\n"
    "ds_read_b64_tr_b16 v[46:47], v6 offset:4480\n"
    "v_mfma_f32_16x16x32_f16 v[84:87], v[192:195], v[72:75], v[84:87]\n"
    "v_pk_mul_f16 v66, v52, v52 op_sel_hi:[0,1]\n"
    "ds_read_b64_tr_b16 v[58:59], v4 offset:7680\n"
    "s_cbranch_vccnz .Lxd_pr13\n"
    "s_setprio 0\n"
    ".Lxd_pr13:\n"
    "v_smfmac_f32_16x16x64_f16 v[88:91], v[196:199], v[68:75], v2\n"
    "v_pk_mul_f16 v67, v52, v49 op_sel_hi:[0,1]\n"
    "ds_read_b64_tr_b16 v[56:57], v4 offset:7040\n"
    "v_mfma_f32_16x16x32_f16 v[76:79], v[200:203], v[60:63], v[76:79]\n"
    "v_pk_mul_f16 v68, v48, v52 op_sel_hi:[0,1]\n"
    "v_pk_mul_f16 v69, v48, v53 op_sel_hi:[0,1]\n"
    "v_mfma_f32_16x16x32_f16 v[76:79], v[212:215], v[64:67], v[76:79]\n"
    "v_pk_mul_f16 v70, v50, v52 op_sel_hi:[0,1]\n"
    "ds_read_b64_tr_b16 v[54:55], v4 offset:6400\n"
    "v_mfma_f32_16x16x32_f16 v[80:83], v[216:219], v[64:67], v[80:83]\n"
    "v_pk_mul_f16 v71, v50, v53 op_sel_hi:[0,1]\n"
    "ds_write_b32 v14, v7\n"
    "v_add_u32 v14, 64, v14\n"
    "v_mfma_f32_16x16x32_f16 v[80:83], v[204:207], v[60:63], v[80:83]\n"
    "v_pk_mul_f16 v72, v50, v50 op_sel_hi:[0,1]\n"
    "v_mfma_f32_16x16x32_f16 v[84:87], v[208:211], v[60:63], v[84:87]\n"
    "v_pk_mul_f16 v73, v50, v49 op_sel_hi:[0,1]\n"
    "v_pk_mul_f16 v74, v48, v50 op_sel_hi:[0,1]\n"
    "v_mfma_f32_16x16x32_f16 v[84:87], v[220:223], v[64:67], v[84:87]\n"
    "v_pk_mul_f16 v75, v48, v51 op_sel_hi:[0,1]\n"
    "v_smfmac_f32_16x16x64_f16 v[88:91], v[224:227], v[60:67], v2\n"
    "ds_read_b64_tr_b16 v[48:49], v3 offset:4480\n"
    "ds_read_b64_tr_b16 v[50:51], v4 offset:5120\n"
    "ds_read_b64_tr_b16 v[52:53], v4 offset:5760\n"
    "v_smfmac_f32_16x16x64_f16 v[88:91], v[252:255], v[68:75], v2\n"
    "s_waitcnt lgkmcnt(7)\n"
    "v_pk_mul_f16 v60, v44, s45 op_sel:[0,1]\n"
    "v_pk_mul_f16 v61, v45, s45 op_sel:[0,1]\n"
    "v_mfma_f32_16x16x32_f16 v[76:79], v[228:231], v[68:71], v[76:79]\n"
    "v_pk_mul_f16 v62, v46, s45 op_sel:[0,1]\n"
    "v_pk_mul_f16 v63, v47, s45 op_sel:[0,1]\n"
    "v_mfma_f32_16x16x32_f16 v[76:79], v[240:243], v[72:75], v[76:79]\n"
    "v_pk_mul_f16 v64, v44, v46\n"
    "v_pk_mul_f16 v65, v45, v47\n"
    "v_mfma_f32_16x16x32_f16 v[80:83], v[244:247], v[72:75], v[80:83]\n"
    "v_add_f32 v11, v88, v89\n"
    "v_add_f32 v13, v90, v91\n"
    "v_mfma_f32_16x16x32_f16 v[80:83], v[232:235], v[68:71], v[80:83]\n"
    "v_mov_b64 v[88:89], 0\n"
    "v_mov_b64 v[90:91], 0\n"
    "v_mfma_f32_16x16x32_f16 v[84:87], v[236:239], v[68:71], v[84:87]\n"
    "s_waitcnt lgkmcnt(0)\n"
    "v_pk_mul_f16 v66, v48, v48 op_sel_hi:[0,1]\n"
    "v_pk_mul_f16 v67, v48, v49 op_sel_hi:[0,1]\n"
    "v_mfma_f32_16x16x32_f16 v[84:87], v[248:251], v[72:75], v[84:87]\n"
    "v_mul_f32 v11, v11, v13\n"
    "s_cmp_eq_u32 s45, -1\n"
    "s_cbranch_scc1 .Lxd_ex0\n"
    ".Lxd_ph1:\n"
    "s_cbranch_vccnz .Lxd_pr15\n"
    "s_setprio 2\n"
    ".Lxd_pr15:\n"
    "v_smfmac_f32_16x16x64_f16 v[88:91], v[112:115], v[60:67], v2\n"
    "v_fmac_f32 v11, v76, v80\n"
    "v_fmac_f32 v11, v77, v81\n"
    "v_fmac_f32 v11, v78, v82\n"
    "v_fmac_f32 v11, v79, v83\n"
    "v_mfma_f32_16x16x32_f16 v[80:83], v[92:95], v[60:63], 0\n"
    "v_pk_mul_f16 v68, v58, v58 op_sel_hi:[0,1]\n"
    "v_pk_mul_f16 v69, v58, v49 op_sel_hi:[0,1]\n"
    "v_mfma_f32_16x16x32_f16 v[80:83], v[104:107], v[64:67], v[80:83]\n"
    "v_pk_mul_f16 v70, v48, v58 op_sel_hi:[0,1]\n"
    "v_pk_mul_f16 v71, v48, v59 op_sel_hi:[0,1]\n"
    "v_mfma_f32_16x16x32_f16 v[76:79], v[100:103], v[64:67], 0\n"
    "v_fmac_f32 v11, v84, v85\n"
    "v_fma_f32 v7, v86, v87, v11\n"
    "v_mfma_f32_16x16x32_f16 v[84:87], v[108:111], v[64:67], 0\n"
    "v_pk_mul_f16 v72, v50, v58 op_sel_hi:[0,1]\n"
    "v_pk_mul_f16 v73, v50, v59 op_sel_hi:[0,1]\n"
    "v_mfma_f32_16x16x32_f16 v[84:87], v[96:99], v[60:63], v[84:87]\n"
    "v_pk_mul_f16 v74, v52, v58 op_sel_hi:[0,1]\n"
    "v_pk_mul_f16 v75, v52, v59 op_sel_hi:[0,1]\n"
    "s_waitcnt vmcnt(0)\n"
    "v_mfma_f32_16x16x32_f16 v[76:79], v[116:119], v[68:71], v[76:79]\n"
    "v_pk_mul_f16 v60, v54, v58 op_sel_hi:[0,1]\n"
    "v_pk_mul_f16 v61, v54, v59 op_sel_hi:[0,1]\n"
    "v_mfma_f32_16x16x32_f16 v[76:79], v[128:131], v[72:75], v[76:79]\n"
    "v_pk_mul_f16 v62, v56, v58 op_sel_hi:[0,1]\n"
    "v_lshl_or_b32 v36, v36, 7, v19\n"
    "global_load_dwordx4 v[28:31], v36, s[8:9]\n"
    "v_mfma_f32_16x16x32_f16 v[80:83], v[132:135], v[72:75], v[80:83]\n"
    "v_pk_mul_f16 v63, v56, v59 op_sel_hi:[0,1]\n"
    "v_lshl_or_b32 v37, v37, 7, v19\n"
    "global_load_dwordx4 v[32:35], v37, s[8:9]\n"
    "v_mfma_f32_16x16x32_f16 v[80:83], v[120:123], v[68:71], v[80:83]\n"
    "v_pk_mul_f16 v64, v56, v56 op_sel_hi:[0,1]\n"
    "v_lshl_or_b32 v41, v41, 7, v20\n"
    "global_load_dword v40, v41, s[8:9]\n"
    "v_mfma_f32_16x16x32_f16 v[84:87], v[124:127], v[68:71], v[84:87]\n"
    "v_pk_mul_f16 v65, v56, v49 op_sel_hi:[0,1]\n"
    "v_lshl_or_b32 v38, v38, 7, v19\n"
    "global_load_dwordx4 v[36:39], v38, s[8:9]\n"
    "v_mfma_f32_16x16x32_f16 v[84:87], v[136:139], v[72:75], v[84:87]\n"
    "v_pk_mul_f16 v66, v48, v56 op_sel_hi:[0,1]\n"
    "v_smfmac_f32_16x16x64_f16 v[88:91], v[140:143], v[68:75], v2\n"
    "v_pk_mul_f16 v67, v48, v57 op_sel_hi:[0,1]\n"
    "v_mfma_f32_16x16x32_f16 v[76:79], v[144:147], v[60:63], v[76:79]\n"
    "v_pk_mul_f16 v68, v50, v56 op_sel_hi:[0,1]\n"
    "v_pk_mul_f16 v69, v50, v57 op_sel_hi:[0,1]\n"
    "v_mfma_f32_16x16x32_f16 v[76:79], v[156:159], v[64:67], v[76:79]\n"
    "v_pk_mul_f16 v70, v52, v56 op_sel_hi:[0,1]\n"
    "v_mfma_f32_16x16x32_f16 v[80:83], v[160:163], v[64:67], v[80:83]\n"
    "v_pk_mul_f16 v71, v52, v57 op_sel_hi:[0,1]\n"
    "v_mfma_f32_16x16x32_f16 v[80:83], v[148:151], v[60:63], v[80:83]\n"
    "v_pk_mul_f16 v72, v54, v56 op_sel_hi:[0,1]\n"
    "ds_read_b32 v22, v25 offset:520\n"
    "v_mfma_f32_16x16x32_f16 v[84:87], v[152:155], v[60:63], v[84:87]\n"
    "v_pk_mul_f16 v73, v54, v57 op_sel_hi:[0,1]\n"
    "v_mfma_f32_16x16x32_f16 v[84:87], v[164:167], v[64:67], v[84:87]\n"
    "v_pk_mul_f16 v74, v54, v54 op_sel_hi:[0,1]\n"
    "v_smfmac_f32_16x16x64_f16 v[88:91], v[168:171], v[60:67], v2\n"
    "v_pk_mul_f16 v75, v54, v49 op_sel_hi:[0,1]\n"
    "v_mfma_f32_16x16x32_f16 v[76:79], v[172:175], v[68:71], v[76:79]\n"
    "v_pk_mul_f16 v60, v48, v54 op_sel_hi:[0,1]\n"
    "v_pk_mul_f16 v61, v48, v55 op_sel_hi:[0,1]\n"
    "v_mfma_f32_16x16x32_f16 v[76:79], v[184:187], v[72:75], v[76:79]\n"
    "v_pk_mul_f16 v62, v50, v54 op_sel_hi:[0,1]\n"
    "v_mfma_f32_16x16x32_f16 v[80:83], v[188:191], v[72:75], v[80:83]\n"
    "v_pk_mul_f16 v63, v50, v55 op_sel_hi:[0,1]\n"
    "s_waitcnt lgkmcnt(0)\n"
    "v_readfirstlane_b32 s45, v22\n"
    "s_cmp_lt_i32 s45, 1\n"
    "s_cbranch_scc1 .Lxd_ck17\n"
    ".Lxd_ck17r:\n"
    "v_mfma_f32_16x16x32_f16 v[80:83], v[176:179], v[68:71], v[80:83]\n"
    "v_pk_mul_f16 v64, v52, v54 op_sel_hi:[0,1]\n"
    "ds_read_b64_tr_b16 v[44:45], v5 offset:8960\n"
    "v_mfma_f32_16x16x32_f16 v[84:87], v[180:183], v[68:71], v[84:87]\n"
    "v_pk_mul_f16 v65, v52, v55 op_sel_hi:[0,1]\n"
    "ds_read_b64_tr_b16 v[46:47], v6 offset:8960\n"
    "v_mfma_f32_16x16x32_f16 v[84:87], v[192:195], v[72:75], v[84:87]\n"
    "v_pk_mul_f16 v66, v52, v52 op_sel_hi:[0,1]\n"
    "ds_read_b64_tr_b16 v[58:59], v4 offset:12160\n"
    "s_cbranch_vccnz .Lxd_pr16\n"
    "s_setprio 0\n"
    ".Lxd_pr16:\n"
    "v_smfmac_f32_16x16x64_f16 v[88:91], v[196:199], v[68:75], v2\n"
    "v_pk_mul_f16 v67, v52, v49 op_sel_hi:[0,1]\n"
    "ds_read_b64_tr_b16 v[56:57], v4 offset:11520\n"
    "v_mfma_f32_16x16x32_f16 v[76:79], v[200:203], v[60:63], v[76:79]\n"
    "v_pk_mul_f16 v68, v48, v52 op_sel_hi:[0,1]\n"
    "v_pk_mul_f16 v69, v48, v53 op_sel_hi:[0,1]\n"
    "v_mfma_f32_16x16x32_f16 v[76:79], v[212:215], v[64:67], v[76:79]\n"
    "v_pk_mul_f16 v70, v50, v52 op_sel_hi:[0,1]\n"
    "ds_read_b64_tr_b16 v[54:55], v4 offset:10880\n"
    "v_mfma_f32_16x16x32_f16 v[80:83], v[216:219], v[64:67], v[80:83]\n"
    "v_pk_mul_f16 v71, v50, v53 op_sel_hi:[0,1]\n"
    "v_mfma_f32_16x16x32_f16 v[80:83], v[204:207], v[60:63], v[80:83]\n"
    "v_pk_mul_f16 v72, v50, v50 op_sel_hi:[0,1]\n"
    "v_mfma_f32_16x16x32_f16 v[84:87], v[208:211], v[60:63], v[84:87]\n"
    "v_pk_mul_f16 v73, v50, v49 op_sel_hi:[0,1]\n"
    "v_pk_mul_f16 v74, v48, v50 op_sel_hi:[0,1]\n"
    "v_mfma_f32_16x16x32_f16 v[84:87], v[220:223], v[64:67], v[84:87]\n"
    "v_pk_mul_f16 v75, v48, v51 op_sel_hi:[0,1]\n"
    "v_smfmac_f32_16x16x64_f16 v[88:91], v[224:227], v[60:67], v2\n"
    "ds_read_b64_tr_b16 v[48:49], v3 offset:8960\n"
    "ds_read_b64_tr_b16 v[50:51], v4 offset:9600\n"
    "ds_read_b64_tr_b16 v[52:53], v4 offset:10240\n"
    "v_smfmac_f32_16x16x64_f16 v[88:91], v[252:255], v[68:75], v2\n"
    "s_waitcnt lgkmcnt(6)\n"
    "v_pk_mul_f16 v60, v44, s45 op_sel:[0,1]\n"
    "v_pk_mul_f16 v61, v45, s45 op_sel:[0,1]\n"
    "v_mfma_f32_16x16x32_f16 v[76:79], v[228:231], v[68:71], v[76:79]\n"
    "v_pk_mul_f16 v62, v46, s45 op_sel:[0,1]\n"
    "v_pk_mul_f16 v63, v47, s45 op_sel:[0,1]\n"
    "v_mfma_f32_16x16x32_f16 v[76:79], v[240:243], v[72:75], v[76:79]\n"
    "v_pk_mul_f16 v64, v44, v46\n"
    "v_pk_mul_f16 v65, v45, v47\n"
    "v_mfma_f32_16x16x32_f16 v[80:83], v[244:247], v[72:75], v[80:83]\n"
    "v_add_f32 v11, v88, v89\n"
    "v_add_f32 v13, v90, v91\n"
    "v_mfma_f32_16x16x32_f16 v[80:83], v[232:235], v[68:71], v[80:83]\n"
    "v_mov_b64 v[88:89], 0\n"
    "v_mov_b64 v[90:91], 0\n"
    "v_mfma_f32_16x16x32_f16 v[84:87], v[236:239], v[68:71], v[84:87]\n"
    "s_waitcnt lgkmcnt(0)\n"
    "v_pk_mul_f16 v66, v48, v48 op_sel_hi:[0,1]\n"
    "v_pk_mul_f16 v67, v48, v49 op_sel_hi:[0,1]\n"
    "v_mfma_f32_16x16x32_f16 v[84:87], v[248:251], v[72:75], v[84:87]\n"
    "v_mul_f32 v11, v11, v13\n"
    "s_cmp_eq_u32 s45, -1\n"
    "s_cbranch_scc1 .Lxd_ex1\n"
    ".Lxd_ph2:\n"
    "s_cbranch_vccnz .Lxd_pr18\n"
    "s_setprio 2\n"
    ".Lxd_pr18:\n"
    "v_smfmac_f32_16x16x64_f16 v[88:91], v[112:115], v[60:67], v2\n"
    "v_fmac_f32 v11, v76, v80\n"
    "v_fmac_f32 v11, v77, v81\n"
    "v_fmac_f32 v11, v78, v82\n"
    "v_fmac_f32 v11, v79, v83\n"
    "v_mfma_f32_16x16x32_f16 v[80:83], v[92:95], v[60:63], 0\n"
    "v_pk_mul_f16 v68, v58, v58 op_sel_hi:[0,1]\n"
    "v_pk_mul_f16 v69, v58, v49 op_sel_hi:[0,1]\n"
    "v_mfma_f32_16x16x32_f16 v[80:83], v[104:107], v[64:67], v[80:83]\n"
    "v_pk_mul_f16 v70, v48, v58 op_sel_hi:[0,1]\n"
    "v_pk_mul_f16 v71, v48, v59 op_sel_hi:[0,1]\n"
    "v_mfma_f32_16x16x32_f16 v[76:79], v[100:103], v[64:67], 0\n"
    "v_fmac_f32 v11, v84, v85\n"
    "v_fma_f32 v8, v86, v87, v11\n"
    "v_mfma_f32_16x16x32_f16 v[84:87], v[108:111], v[64:67], 0\n"
    "v_pk_mul_f16 v72, v50, v58 op_sel_hi:[0,1]\n"
    "v_pk_mul_f16 v73, v50, v59 op_sel_hi:[0,1]\n"
    "v_mfma_f32_16x16x32_f16 v[84:87], v[96:99], v[60:63], v[84:87]\n"
    "v_pk_mul_f16 v74, v52, v58 op_sel_hi:[0,1]\n"
    "v_pk_mul_f16 v75, v52, v59 op_sel_hi:[0,1]\n"
    "s_waitcnt vmcnt(0)\n"
    "v_mfma_f32_16x16x32_f16 v[76:79], v[116:119], v[68:71], v[76:79]\n"
    "v_pk_mul_f16 v60, v54, v58 op_sel_hi:[0,1]\n"
    "v_pk_mul_f16 v61, v54, v59 op_sel_hi:[0,1]\n"
    "s_movk_i32 s43, 15744\n"
    "v_pk_maximum3_f16 v21, v28, v28, s43 op_sel_hi:[1,1,0] neg_lo:[0,1,0] neg_hi:[0,1,0]\n"
    "v_mfma_f32_16x16x32_f16 v[76:79], v[128:131], v[72:75], v[76:79]\n"
    "v_pk_mul_f16 v62, v56, v58 op_sel_hi:[0,1]\n"
    "v_pk_maximum3_f16 v21, v29, v29, v21 neg_lo:[0,1,0] neg_hi:[0,1,0]\n"
    "v_mfma_f32_16x16x32_f16 v[80:83], v[132:135], v[72:75], v[80:83]\n"
    "v_pk_mul_f16 v63, v56, v59 op_sel_hi:[0,1]\n"
    "v_pk_maximum3_f16 v21, v30, v30, v21 neg_lo:[0,1,0] neg_hi:[0,1,0]\n"
    "v_mfma_f32_16x16x32_f16 v[80:83], v[120:123], v[68:71], v[80:83]\n"
    "v_pk_mul_f16 v64, v56, v56 op_sel_hi:[0,1]\n"
    "v_pk_maximum3_f16 v21, v31, v31, v21 neg_lo:[0,1,0] neg_hi:[0,1,0]\n"
    "v_mfma_f32_16x16x32_f16 v[84:87], v[124:127], v[68:71], v[84:87]\n"
    "v_pk_mul_f16 v65, v56, v49 op_sel_hi:[0,1]\n"
    "v_pk_maximum3_f16 v21, v32, v32, v21 neg_lo:[0,1,0] neg_hi:[0,1,0]\n"
    "v_mfma_f32_16x16x32_f16 v[84:87], v[136:139], v[72:75], v[84:87]\n"
    "v_pk_mul_f16 v66, v48, v56 op_sel_hi:[0,1]\n"
    "v_pk_maximum3_f16 v21, v33, v33, v21 neg_lo:[0,1,0] neg_hi:[0,1,0]\n"
    "v_smfmac_f32_16x16x64_f16 v[88:91], v[140:143], v[68:75], v2\n"
    "v_pk_mul_f16 v67, v48, v57 op_sel_hi:[0,1]\n"
    "v_pk_maximum3_f16 v21, v34, v34, v21 neg_lo:[0,1,0] neg_hi:[0,1,0]\n"
    "v_mfma_f32_16x16x32_f16 v[76:79], v[144:147], v[60:63], v[76:79]\n"
    "v_pk_mul_f16 v68, v50, v56 op_sel_hi:[0,1]\n"
    "v_pk_mul_f16 v69, v50, v57 op_sel_hi:[0,1]\n"
    "v_pk_maximum3_f16 v21, v35, v35, v21 neg_lo:[0,1,0] neg_hi:[0,1,0]\n"
    "v_mfma_f32_16x16x32_f16 v[76:79], v[156:159], v[64:67], v[76:79]\n"
    "v_pk_mul_f16 v70, v52, v56 op_sel_hi:[0,1]\n"
    "v_pk_maximum3_f16 v21, v36, v36, v21 neg_lo:[0,1,0] neg_hi:[0,1,0]\n"
    "v_mfma_f32_16x16x32_f16 v[80:83], v[160:163], v[64:67], v[80:83]\n"
    "v_pk_mul_f16 v71, v52, v57 op_sel_hi:[0,1]\n"
    "v_pk_maximum3_f16 v21, v37, v37, v21 neg_lo:[0,1,0] neg_hi:[0,1,0]\n"
    "v_mfma_f32_16x16x32_f16 v[80:83], v[148:151], v[60:63], v[80:83]\n"
    "v_pk_mul_f16 v72, v54, v56 op_sel_hi:[0,1]\n"
    "ds_read_b32 v22, v25 offset:524\n"
    "v_pk_maximum3_f16 v21, v38, v38, v21 neg_lo:[0,1,0] neg_hi:[0,1,0]\n"
    "v_mfma_f32_16x16x32_f16 v[84:87], v[152:155], v[60:63], v[84:87]\n"
    "v_pk_mul_f16 v73, v54, v57 op_sel_hi:[0,1]\n"
    "v_pk_maximum3_f16 v21, v39, v39, v21 neg_lo:[0,1,0] neg_hi:[0,1,0]\n"
    "v_mfma_f32_16x16x32_f16 v[84:87], v[164:167], v[64:67], v[84:87]\n"
    "v_pk_mul_f16 v74, v54, v54 op_sel_hi:[0,1]\n"
    "v_pk_maximum3_f16 v21, v40, v40, v21 neg_lo:[0,1,0] neg_hi:[0,1,0]\n"
    "v_smfmac_f32_16x16x64_f16 v[88:91], v[168:171], v[60:67], v2\n"
    "v_pk_mul_f16 v75, v54, v49 op_sel_hi:[0,1]\n"
    "v_pk_max_u16 v21, v21, v21 op_sel:[0,1] op_sel_hi:[1,0]\n"
    "v_mfma_f32_16x16x32_f16 v[76:79], v[172:175], v[68:71], v[76:79]\n"
    "v_pk_mul_f16 v60, v48, v54 op_sel_hi:[0,1]\n"
    "v_pk_mul_f16 v61, v48, v55 op_sel_hi:[0,1]\n"
    "v_max_u32_dpp v21, v21, v21 quad_perm:[1,0,3,2] row_mask:0xf bank_mask:0xf\n"
    "v_mfma_f32_16x16x32_f16 v[76:79], v[184:187], v[72:75], v[76:79]\n"
    "v_pk_mul_f16 v62, v50, v54 op_sel_hi:[0,1]\n"
    "v_max_u32_dpp v21, v21, v21 quad_perm:[2,3,0,1] row_mask:0xf bank_mask:0xf\n"
    "v_mfma_f32_16x16x32_f16 v[80:83], v[188:191], v[72:75], v[80:83]\n"
    "v_pk_mul_f16 v63, v50, v55 op_sel_hi:[0,1]\n"
    "s_waitcnt lgkmcnt(0)\n"
    "v_readfirstlane_b32 s45, v22\n"
    "s_cmp_lt_i32 s45, 1\n"
    "s_cbranch_scc1 .Lxd_ck20\n"
    ".Lxd_ck20r:\n"
    "v_max_u32_dpp v21, v21, v21 row_half_mirror row_mask:0xf bank_mask:0xf\n"
    "v_mfma_f32_16x16x32_f16 v[80:83], v[176:179], v[68:71], v[80:83]\n"
    "v_pk_mul_f16 v64, v52, v54 op_sel_hi:[0,1]\n"
    "ds_read_b64_tr_b16 v[44:45], v5 offset:13440\n"
    "v_max_u32_dpp v21, v21, v21 row_mirror row_mask:0xf bank_mask:0xf\n"
    "v_mfma_f32_16x16x32_f16 v[84:87], v[180:183], v[68:71], v[84:87]\n"
    "v_pk_mul_f16 v65, v52, v55 op_sel_hi:[0,1]\n"
    "ds_read_b64_tr_b16 v[46:47], v6 offset:13440\n"
    "v_max_u32_dpp v21, v21, v21 row_bcast:15 row_mask:0xa bank_mask:0xf\n"
    "v_mfma_f32_16x16x32_f16 v[84:87], v[192:195], v[72:75], v[84:87]\n"
    "v_pk_mul_f16 v66, v52, v52 op_sel_hi:[0,1]\n"
    "ds_read_b64_tr_b16 v[58:59], v4 offset:16640\n"
    "v_max_u32_dpp v21, v21, v21 row_bcast:31 row_mask:0xc bank_mask:0xf\n"
    "s_cbranch_vccnz .Lxd_pr19\n"
    "s_setprio 0\n"
    ".Lxd_pr19:\n"
    "v_smfmac_f32_16x16x64_f16 v[88:91], v[196:199], v[68:75], v2\n"
    "v_pk_mul_f16 v67, v52, v49 op_sel_hi:[0,1]\n"
    "ds_read_b64_tr_b16 v[56:57], v4 offset:16000\n"
    "v_readlane_b32 s47, v21, 63\n"
    "v_mfma_f32_16x16x32_f16 v[76:79], v[200:203], v[60:63], v[76:79]\n"
    "v_pk_mul_f16 v68, v48, v52 op_sel_hi:[0,1]\n"
    "v_pk_mul_f16 v69, v48, v53 op_sel_hi:[0,1]\n"
    "s_and_b32 s50, s47, 0x7f80\n"
    "s_lshr_b32 s47, s50, 6\n"
    "s_sub_u32 s50, s50, 2816\n"
    "s_mul_i32 s51, s50, -8\n"
    "v_mfma_f32_16x16x32_f16 v[76:79], v[212:215], v[64:67], v[76:79]\n"
    "v_pk_mul_f16 v70, v50, v52 op_sel_hi:[0,1]\n"
    "ds_read_b64_tr_b16 v[54:55], v4 offset:15360\n"
    "v_pk_maximum3_f16 v21, v28, v28, s50 op_sel_hi:[1,1,0] neg_lo:[0,1,0] neg_hi:[0,1,0]\n"
    "v_mfma_f32_16x16x32_f16 v[80:83], v[216:219], v[64:67], v[80:83]\n"
    "v_pk_mul_f16 v71, v50, v53 op_sel_hi:[0,1]\n"
    "v_pk_mad_u16 v21, v21, 8, s51 op_sel_hi:[1,0,0]\n"
    "v_mfma_f32_16x16x32_f16 v[80:83], v[204:207], v[60:63], v[80:83]\n"
    "v_pk_mul_f16 v72, v50, v50 op_sel_hi:[0,1]\n"
    "v_bfi_b32 v28, s57, v21, v28\n"
    "v_mfma_f32_16x16x32_f16 v[84:87], v[208:211], v[60:63], v[84:87]\n"
    "v_pk_mul_f16 v73, v50, v49 op_sel_hi:[0,1]\n"
    "v_pk_mul_f16 v74, v48, v50 op_sel_hi:[0,1]\n"
    "v_pk_maximum3_f16 v12, v29, v29, s50 op_sel_hi:[1,1,0] neg_lo:[0,1,0] neg_hi:[0,1,0]\n"
    "v_mfma_f32_16x16x32_f16 v[84:87], v[220:223], v[64:67], v[84:87]\n"
    "v_pk_mul_f16 v75, v48, v51 op_sel_hi:[0,1]\n"
    "v_pk_mad_u16 v12, v12, 8, s51 op_sel_hi:[1,0,0]\n"
    "v_smfmac_f32_16x16x64_f16 v[88:91], v[224:227], v[60:67], v2\n"
    "ds_read_b64_tr_b16 v[48:49], v3 offset:13440\n"
    "ds_read_b64_tr_b16 v[50:51], v4 offset:14080\n"
    "ds_read_b64_tr_b16 v[52:53], v4 offset:14720\n"
    "v_bfi_b32 v29, s57, v12, v29\n"
    "v_smfmac_f32_16x16x64_f16 v[88:91], v[252:255], v[68:75], v2\n"
    "s_waitcnt lgkmcnt(6)\n"
    "v_pk_mul_f16 v60, v44, s45 op_sel:[0,1]\n"
    "v_pk_mul_f16 v61, v45, s45 op_sel:[0,1]\n"
    "v_mfma_f32_16x16x32_f16 v[76:79], v[228:231], v[68:71], v[76:79]\n"
    "v_pk_mul_f16 v62, v46, s45 op_sel:[0,1]\n"
    "v_pk_mul_f16 v63, v47, s45 op_sel:[0,1]\n"
    "v_mfma_f32_16x16x32_f16 v[76:79], v[240:243], v[72:75], v[76:79]\n"
    "v_pk_mul_f16 v64, v44, v46\n"
    "v_pk_mul_f16 v65, v45, v47\n"
    "v_mfma_f32_16x16x32_f16 v[80:83], v[244:247], v[72:75], v[80:83]\n"
    "v_add_f32 v11, v88, v89\n"
    "v_add_f32 v13, v90, v91\n"
    "v_mfma_f32_16x16x32_f16 v[80:83], v[232:235], v[68:71], v[80:83]\n"
    "v_mov_b64 v[88:89], 0\n"
    "v_mov_b64 v[90:91], 0\n"
    "v_mfma_f32_16x16x32_f16 v[84:87], v[236:239], v[68:71], v[84:87]\n"
    "s_waitcnt lgkmcnt(0)\n"
    "v_pk_mul_f16 v66, v48, v48 op_sel_hi:[0,1]\n"
    "v_pk_mul_f16 v67, v48, v49 op_sel_hi:[0,1]\n"
    "v_mfma_f32_16x16x32_f16 v[84:87], v[248:251], v[72:75], v[84:87]\n"
    "v_mul_f32 v11, v11, v13\n"
    "s_cmp_eq_u32 s45, -1\n"
    "s_cbranch_scc1 .Lxd_ex2\n"
    ".Lxd_ph3:\n"
    "s_cbranch_vccnz .Lxd_pr21\n"
    "s_setprio 2\n"
    ".Lxd_pr21:\n"
    "v_smfmac_f32_16x16x64_f16 v[88:91], v[112:115], v[60:67], v2\n"
    "v_fmac_f32 v11, v76, v80\n"
    "v_fmac_f32 v11, v77, v81\n"
    "v_fmac_f32 v11, v78, v82\n"
    "v_fmac_f32 v11, v79, v83\n"
    "v_mfma_f32_16x16x32_f16 v[80:83], v[92:95], v[60:63], 0\n"
    "v_pk_mul_f16 v68, v58, v58 op_sel_hi:[0,1]\n"
    "v_pk_mul_f16 v69, v58, v49 op_sel_hi:[0,1]\n"
    "v_add_u32 v3, s46, v3\n"
    "v_add_u32 v4, s46, v4\n"
    "v_mfma_f32_16x16x32_f16 v[80:83], v[104:107], v[64:67], v[80:83]\n"
    "v_pk_mul_f16 v70, v48, v58 op_sel_hi:[0,1]\n"
    "v_pk_mul_f16 v71, v48, v59 op_sel_hi:[0,1]\n"
    "v_add_u32 v5, s46, v5\n"
    "v_mfma_f32_16x16x32_f16 v[76:79], v[100:103], v[64:67], 0\n"
    "v_fmac_f32 v11, v84, v85\n"
    "v_fma_f32 v9, v86, v87, v11\n"
    "v_add_u32 v6, s46, v6\n"
    "s_sub_u32 s46, 0, s46\n"
    "v_mfma_f32_16x16x32_f16 v[84:87], v[108:111], v[64:67], 0\n"
    "v_pk_mul_f16 v72, v50, v58 op_sel_hi:[0,1]\n"
    "v_pk_mul_f16 v73, v50, v59 op_sel_hi:[0,1]\n"
    "v_pk_maximum3_f16 v21, v30, v30, s50 op_sel_hi:[1,1,0] neg_lo:[0,1,0] neg_hi:[0,1,0]\n"
    "v_pk_mad_u16 v21, v21, 8, s51 op_sel_hi:[1,0,0]\n"
    "v_mfma_f32_16x16x32_f16 v[84:87], v[96:99], v[60:63], v[84:87]\n"
    "v_pk_mul_f16 v74, v52, v58 op_sel_hi:[0,1]\n"
    "v_pk_mul_f16 v75, v52, v59 op_sel_hi:[0,1]\n"
    "v_bfi_b32 v30, s57, v21, v30\n"
    "v_mfma_f32_16x16x32_f16 v[76:79], v[116:119], v[68:71], v[76:79]\n"
    "v_pk_mul_f16 v60, v54, v58 op_sel_hi:[0,1]\n"
    "v_pk_mul_f16 v61, v54, v59 op_sel_hi:[0,1]\n"
    "v_pk_maximum3_f16 v12, v31, v31, s50 op_sel_hi:[1,1,0] neg_lo:[0,1,0] neg_hi:[0,1,0]\n"
    "v_mfma_f32_16x16x32_f16 v[76:79], v[128:131], v[72:75], v[76:79]\n"
    "v_pk_mul_f16 v62, v56, v58 op_sel_hi:[0,1]\n"
    "s_cmp_lt_u32 s55, 32\n"
    "s_cselect_b32 s44, 0, -1\n"
    "s_add_u32 s55, s55, 4\n"
    "v_add_u32_e64 v24, s44, 1\n"
    "s_mov_b64 exec, 1\n"
    "global_atomic_add v24, v17, v24, s[52:53] sc0 sc1\n"
    "s_mov_b64 exec, -1\n"
    "v_pk_mad_u16 v12, v12, 8, s51 op_sel_hi:[1,0,0]\n"
    "v_mfma_f32_16x16x32_f16 v[80:83], v[132:135], v[72:75], v[80:83]\n"
    "v_pk_mul_f16 v63, v56, v59 op_sel_hi:[0,1]\n"
    "v_bfi_b32 v31, s57, v12, v31\n"
    "v_pk_maximum3_f16 v21, v32, v32, s50 op_sel_hi:[1,1,0] neg_lo:[0,1,0] neg_hi:[0,1,0]\n"
    "v_mfma_f32_16x16x32_f16 v[80:83], v[120:123], v[68:71], v[80:83]\n"
    "v_pk_mul_f16 v64, v56, v56 op_sel_hi:[0,1]\n"
    "v_pk_mad_u16 v21, v21, 8, s51 op_sel_hi:[1,0,0]\n"
    "v_mfma_f32_16x16x32_f16 v[84:87], v[124:127], v[68:71], v[84:87]\n"
    "v_pk_mul_f16 v65, v56, v49 op_sel_hi:[0,1]\n"
    "v_bfi_b32 v32, s57, v21, v32\n"
    "v_mfma_f32_16x16x32_f16 v[84:87], v[136:139], v[72:75], v[84:87]\n"
    "v_pk_mul_f16 v66, v48, v56 op_sel_hi:[0,1]\n"
    "v_pk_maximum3_f16 v12, v33, v33, s50 op_sel_hi:[1,1,0] neg_lo:[0,1,0] neg_hi:[0,1,0]\n"
    "v_pk_mad_u16 v12, v12, 8, s51 op_sel_hi:[1,0,0]\n"
    "v_smfmac_f32_16x16x64_f16 v[88:91], v[140:143], v[68:75], v2\n"
    "v_pk_mul_f16 v67, v48, v57 op_sel_hi:[0,1]\n"
    "v_bfi_b32 v33, s57, v12, v33\n"
    "v_mfma_f32_16x16x32_f16 v[76:79], v[144:147], v[60:63], v[76:79]\n"
    "v_pk_mul_f16 v68, v50, v56 op_sel_hi:[0,1]\n"
    "v_pk_mul_f16 v69, v50, v57 op_sel_hi:[0,1]\n"
    "v_pk_maximum3_f16 v21, v34, v34, s50 op_sel_hi:[1,1,0] neg_lo:[0,1,0] neg_hi:[0,1,0]\n"
    "v_mfma_f32_16x16x32_f16 v[76:79], v[156:159], v[64:67], v[76:79]\n"
    "v_pk_mul_f16 v70, v52, v56 op_sel_hi:[0,1]\n"
    "v_pk_mad_u16 v21, v21, 8, s51 op_sel_hi:[1,0,0]\n"
    "v_mfma_f32_16x16x32_f16 v[80:83], v[160:163], v[64:67], v[80:83]\n"
    "v_pk_mul_f16 v71, v52, v57 op_sel_hi:[0,1]\n"
    "v_bfi_b32 v34, s57, v21, v34\n"
    "v_pk_maximum3_f16 v12, v35, v35, s50 op_sel_hi:[1,1,0] neg_lo:[0,1,0] neg_hi:[0,1,0]\n"
    "v_mfma_f32_16x16x32_f16 v[80:83], v[148:151], v[60:63], v[80:83]\n"
    "v_pk_mul_f16 v72, v54, v56 op_sel_hi:[0,1]\n"
    "ds_read_b32 v22, v25 offset:528\n"
    "v_pk_mad_u16 v12, v12, 8, s51 op_sel_hi:[1,0,0]\n"
    "v_mfma_f32_16x16x32_f16 v[84:87], v[152:155], v[60:63], v[84:87]\n"
    "v_pk_mul_f16 v73, v54, v57 op_sel_hi:[0,1]\n"
    "v_bfi_b32 v35, s57, v12, v35\n"
    "v_mfma_f32_16x16x32_f16 v[84:87], v[164:167], v[64:67], v[84:87]\n"
    "v_pk_mul_f16 v74, v54, v54 op_sel_hi:[0,1]\n"
    "v_pk_maximum3_f16 v21, v36, v36, s50 op_sel_hi:[1,1,0] neg_lo:[0,1,0] neg_hi:[0,1,0]\n"
    "v_pk_mad_u16 v21, v21, 8, s51 op_sel_hi:[1,0,0]\n"
    "v_smfmac_f32_16x16x64_f16 v[88:91], v[168:171], v[60:67], v2\n"
    "v_pk_mul_f16 v75, v54, v49 op_sel_hi:[0,1]\n"
    "ds_read_b32 v41, v25 offset:24\n"
    "v_bfi_b32 v36, s57, v21, v36\n"
    "v_mfma_f32_16x16x32_f16 v[76:79], v[172:175], v[68:71], v[76:79]\n"
    "v_pk_mul_f16 v60, v48, v54 op_sel_hi:[0,1]\n"
    "v_pk_mul_f16 v61, v48, v55 op_sel_hi:[0,1]\n"
    "v_pk_maximum3_f16 v12, v37, v37, s50 op_sel_hi:[1,1,0] neg_lo:[0,1,0] neg_hi:[0,1,0]\n"
    "v_mfma_f32_16x16x32_f16 v[76:79], v[184:187], v[72:75], v[76:79]\n"
    "v_pk_mul_f16 v62, v50, v54 op_sel_hi:[0,1]\n"
    "v_pk_mad_u16 v12, v12, 8, s51 op_sel_hi:[1,0,0]\n"
    "v_mfma_f32_16x16x32_f16 v[80:83], v[188:191], v[72:75], v[80:83]\n"
    "v_pk_mul_f16 v63, v50, v55 op_sel_hi:[0,1]\n"
    "s_waitcnt lgkmcnt(1)\n"
    "v_readfirstlane_b32 s45, v22\n"
    "s_cmp_lt_i32 s45, 1\n"
    "s_cbranch_scc1 .Lxd_ck23\n"
    ".Lxd_ck23r:\n"
    "v_bfi_b32 v37, s57, v12, v37\n"
    "v_pk_maximum3_f16 v21, v38, v38, s50 op_sel_hi:[1,1,0] neg_lo:[0,1,0] neg_hi:[0,1,0]\n"
    "v_mfma_f32_16x16x32_f16 v[80:83], v[176:179], v[68:71], v[80:83]\n"
    "v_pk_mul_f16 v64, v52, v54 op_sel_hi:[0,1]\n"
    "ds_read_b64_tr_b16 v[44:45], v5 offset:0\n"
    "v_pk_mad_u16 v21, v21, 8, s51 op_sel_hi:[1,0,0]\n"
    "v_mfma_f32_16x16x32_f16 v[84:87], v[180:183], v[68:71], v[84:87]\n"
    "v_pk_mul_f16 v65, v52, v55 op_sel_hi:[0,1]\n"
    "ds_read_b64_tr_b16 v[46:47], v6 offset:0\n"
    "v_bfi_b32 v38, s57, v21, v38\n"
    "v_mfma_f32_16x16x32_f16 v[84:87], v[192:195], v[72:75], v[84:87]\n"
    "v_pk_mul_f16 v66, v52, v52 op_sel_hi:[0,1]\n"
    "ds_read_b64_tr_b16 v[58:59], v4 offset:3200\n"
    "v_pk_maximum3_f16 v12, v39, v39, s50 op_sel_hi:[1,1,0] neg_lo:[0,1,0] neg_hi:[0,1,0]\n"
    "v_pk_mad_u16 v12, v12, 8, s51 op_sel_hi:[1,0,0]\n"
    "s_cbranch_vccnz .Lxd_pr22\n"
    "s_setprio 0\n"
    ".Lxd_pr22:\n"
    "v_smfmac_f32_16x16x64_f16 v[88:91], v[196:199], v[68:75], v2\n"
    "v_pk_mul_f16 v67, v52, v49 op_sel_hi:[0,1]\n"
    "ds_read_b64_tr_b16 v[56:57], v4 offset:2560\n"
    "v_bfi_b32 v39, s57, v12, v39\n"
    "v_mfma_f32_16x16x32_f16 v[76:79], v[200:203], v[60:63], v[76:79]\n"
    "v_pk_mul_f16 v68, v48, v52 op_sel_hi:[0,1]\n"
    "v_pk_mul_f16 v69, v48, v53 op_sel_hi:[0,1]\n"
    "v_pk_maximum3_f16 v21, v40, v40, s50 op_sel_hi:[1,1,0] neg_lo:[0,1,0] neg_hi:[0,1,0]\n"
    "v_mfma_f32_16x16x32_f16 v[76:79], v[212:215], v[64:67], v[76:79]\n"
    "v_pk_mul_f16 v70, v50, v52 op_sel_hi:[0,1]\n"
    "ds_read_b64_tr_b16 v[54:55], v4 offset:1920\n"
    "v_pk_mad_u16 v21, v21, 8, s51 op_sel_hi:[1,0,0]\n"
    "v_mfma_f32_16x16x32_f16 v[80:83], v[216:219], v[64:67], v[80:83]\n"
    "v_pk_mul_f16 v71, v50, v53 op_sel_hi:[0,1]\n"
    "v_bfi_b32 v40, s57, v21, v40\n"
    "s_sub_u32 s43, 514, s47\n"
    "s_lshl_b32 s43, s43, 22\n"
    "v_cvt_pkrtz_f16_f32 v12, s43, s43\n"
    "v_mfma_f32_16x16x32_f16 v[80:83], v[204:207], v[60:63], v[80:83]\n"
    "v_pk_mul_f16 v72, v50, v50 op_sel_hi:[0,1]\n"
    "v_lshl_or_b32 v21, v12, 17, s47\n"
    "v_mfma_f32_16x16x32_f16 v[84:87], v[208:211], v[60:63], v[84:87]\n"
    "v_pk_mul_f16 v73, v50, v49 op_sel_hi:[0,1]\n"
    "v_pk_mul_f16 v74, v48, v50 op_sel_hi:[0,1]\n"
    "s_waitcnt lgkmcnt(5)\n"
    "v_alignbit_b32 v21, v41, v21, 1\n"
    "v_mfma_f32_16x16x32_f16 v[84:87], v[220:223], v[64:67], v[84:87]\n"
    "v_pk_mul_f16 v75, v48, v51 op_sel_hi:[0,1]\n"
    "s_mov_b64 exec, s[48:49]\n"
    "ds_write_b128 v15, v[28:31] offset:0\n"
    "ds_write_b128 v15, v[32:35] offset:160\n"
    "ds_write_b128 v15, v[36:39] offset:320\n"
    "ds_write_b32 v16, v40 offset:3840\n"
    "ds_write_b32 v16, v12 offset:4160\n"
    "ds_write_b32 v25, v21 offset:532\n"
    "s_mov_b64 exec, -1\n"
    "v_smfmac_f32_16x16x64_f16 v[88:91], v[224:227], v[60:67], v2\n"
    "ds_read_b64_tr_b16 v[48:49], v3 offset:0\n"
    "ds_read_b64_tr_b16 v[50:51], v4 offset:640\n"
    "ds_read_b64_tr_b16 v[52:53], v4 offset:1280\n"
    "v_smfmac_f32_16x16x64_f16 v[88:91], v[252:255], v[68:75], v2\n"
    "s_waitcnt lgkmcnt(12)\n"
    "v_pk_mul_f16 v60, v44, s45 op_sel:[0,1]\n"
    "v_pk_mul_f16 v61, v45, s45 op_sel:[0,1]\n"
    "v_mfma_f32_16x16x32_f16 v[76:79], v[228:231], v[68:71], v[76:79]\n"
    "v_pk_mul_f16 v62, v46, s45 op_sel:[0,1]\n"
    "v_pk_mul_f16 v63, v47, s45 op_sel:[0,1]\n"
    "v_mfma_f32_16x16x32_f16 v[76:79], v[240:243], v[72:75], v[76:79]\n"
    "v_pk_mul_f16 v64, v44, v46\n"
    "v_pk_mul_f16 v65, v45, v47\n"
    "v_mfma_f32_16x16x32_f16 v[80:83], v[244:247], v[72:75], v[80:83]\n"
    "v_add_f32 v11, v88, v89\n"
    "v_add_f32 v13, v90, v91\n"
    "v_mfma_f32_16x16x32_f16 v[80:83], v[232:235], v[68:71], v[80:83]\n"
    "v_mov_b64 v[88:89], 0\n"
    "v_mov_b64 v[90:91], 0\n"
    "v_mfma_f32_16x16x32_f16 v[84:87], v[236:239], v[68:71], v[84:87]\n"
    "s_waitcnt lgkmcnt(0)\n"
    "v_pk_mul_f16 v66, v48, v48 op_sel_hi:[0,1]\n"
    "v_pk_mul_f16 v67, v48, v49 op_sel_hi:[0,1]\n"
    "v_mfma_f32_16x16x32_f16 v[84:87], v[248:251], v[72:75], v[84:87]\n"
    "v_mul_f32 v11, v11, v13\n"
    "v_add_u32 v25, 16, v25\n"
    "s_cmp_eq_u32 s45, -1\n"
    "s_cbranch_scc0 .Lxd_ph0\n"
    ".Lxd_ex3:\n"
    "v_fmac_f32 v11, v76, v80\n"
    "v_fmac_f32 v11, v77, v81\n"
    "v_fmac_f32 v11, v78, v82\n"
    "v_fmac_f32 v11, v79, v83\n"
    "s_nop 0\n"
    "v_fmac_f32 v11, v84, v85\n"
    "v_fma_f32 v10, v86, v87, v11\n"
    "s_branch .Lxd_fin\n"
    ".Lxd_ex2:\n"
    "v_fmac_f32 v11, v76, v80\n"
    "v_fmac_f32 v11, v77, v81\n"
    "v_fmac_f32 v11, v78, v82\n"
    "v_fmac_f32 v11, v79, v83\n"
    "s_nop 0\n"
    "v_fmac_f32 v11, v84, v85\n"
    "v_fma_f32 v9, v86, v87, v11\n"
    "s_branch .Lxd_fin\n"
    ".Lxd_ex1:\n"
    "v_fmac_f32 v11, v76, v80\n"
    "v_fmac_f32 v11, v77, v81\n"
    "v_fmac_f32 v11, v78, v82\n"
    "v_fmac_f32 v11, v79, v83\n"
    "s_nop 0\n"
    "v_fmac_f32 v11, v84, v85\n"
    "v_fma_f32 v8, v86, v87, v11\n"
    "s_branch .Lxd_fin\n"
    ".Lxd_ex0:\n"
    "v_fmac_f32 v11, v76, v80\n"
    "v_fmac_f32 v11, v77, v81\n"
    "v_fmac_f32 v11, v78, v82\n"
    "v_fmac_f32 v11, v79, v83\n"
    "s_nop 0\n"
    "v_fmac_f32 v11, v84, v85\n"
    "v_fma_f32 v7, v86, v87, v11\n"
    ".Lxd_fin:\n"
    "v_mov_b32 v13, 154112\n"
    "ds_read_b64 v[44:45], v13\n"
    "s_nop 1\n"
    "v_permlane32_swap_b32 v7, v8\n"
    "v_permlane32_swap_b32 v9, v10\n"
    "v_add_f32 v7, v7, v8\n"
    "v_add_f32 v9, v9, v10\n"
    "s_nop 1\n"
    "v_permlane16_swap_b32 v7, v9\n"
    "v_add_f32 v7, v7, v9\n"
    "s_nop 1\n"
    "v_add_f32_dpp v7, v7, v7 quad_perm:[1,0,3,2] row_mask:0xf bank_mask:0xf\n"
    "s_nop 1\n"
    "v_add_f32_dpp v7, v7, v7 quad_perm:[2,3,0,1] row_mask:0xf bank_mask:0xf\n"
    "s_nop 1\n"
    "v_add_f32_dpp v7, v7, v7 row_half_mirror row_mask:0xf bank_mask:0xf\n"
    "s_nop 1\n"
    "v_add_f32_dpp v7, v7, v7 row_mirror row_mask:0xf bank_mask:0xf\n"
    "ds_write_b32 v14, v7\n"
    "v_add_u32 v14, 64, v14\n"
    "s_mov_b32 s59, 0x20000\n"
    ".Lxd_f2:\n"
    "s_waitcnt vmcnt(0) lgkmcnt(0)\n"
    "v_xor_b32 v12, v42, v44\n"
    "v_xor_b32 v13, v26, v44\n"
    "v_or_b32 v12, v12, v13\n"
    "v_and_b32 v12, 0xff, v12\n"
    "v_xor_b32 v13, v43, v45\n"
    "v_xor_b32 v21, v27, v45\n"
    "v_or3_b32 v12, v12, v13, v21\n"
    "v_cmp_ne_u32 vcc, 0, v12\n"
    "v_and_b32 v13, 15, v1\n"
    "s_cmp_eq_u64 vcc, 0\n"
    "s_cbranch_scc1 .Lxd_f3\n"
    "s_sleep 2\n"
    "s_call_b64 s[60:61], .Lxd_gran\n"
    "s_sub_u32 s59, s59, 1\n"
    "s_cbranch_scc0 .Lxd_f2\n"
    "s_waitcnt vmcnt(0)\n"
    ".Lxd_f3:\n"
    "v_cmp_gt_u32 vcc, 13, v13\n"
    "v_and_b32 v42, 0xffffff00, v42\n"
    "v_and_b32 v26, 0xffffff00, v26\n"
    "v_cndmask_b32 v42, 0, v42, vcc\n"
    "v_cndmask_b32 v26, 0, v26, vcc\n"
    "s_lshl4_add_u32 s40, s22, s58\n"
    "v_lshrrev_b32 v12, 4, v1\n"
    "v_add_f32_dpp v42, v42, v42 quad_perm:[1,0,3,2] row_mask:0xf bank_mask:0xf\n"
    "v_add_f32_dpp v26, v26, v26 quad_perm:[1,0,3,2] row_mask:0xf bank_mask:0xf\n"
    "s_nop 0\n"
    "v_add_f32_dpp v42, v42, v42 quad_perm:[2,3,0,1] row_mask:0xf bank_mask:0xf\n"
    "v_add_f32_dpp v26, v26, v26 quad_perm:[2,3,0,1] row_mask:0xf bank_mask:0xf\n"
    "s_nop 0\n"
    "v_add_f32_dpp v42, v42, v42 row_half_mirror row_mask:0xf bank_mask:0xf\n"
    "v_add_f32_dpp v26, v26, v26 row_half_mirror row_mask:0xf bank_mask:0xf\n"
    "s_nop 0\n"
    "v_add_f32_dpp v42, v42, v42 row_mirror row_mask:0xf bank_mask:0xf\n"
    "v_add_f32_dpp v26, v26, v26 row_mirror row_mask:0xf bank_mask:0xf\n"
    "s_nop 0\n"
    "v_lshl_add_u32 v12, v12, 2, s40\n"
    "ds_write_b32 v12, v42 offset:1024\n"
    "ds_write_b32 v12, v26 offset:1088\n"
    "v_lshl_add_u32 v12, v18, 6, v20\n"
    "v_lshlrev_b32 v13, 2, v12\n"
    "v_add_u32 v12, 150528, v12\n"
    "v_add_u32 v13, 152128, v13\n"
    "v_mov_b32 v21, 140944\n"
    "s_lshl_b32 s26, s26, 16\n"
    "s_waitcnt lgkmcnt(0)\n"
    "s_barrier\n"
    "s_cmp_lg_u32 s20, 0\n"
    "s_cbranch_scc1 .Lxd_done\n"
    "ds_read_b128 v[28:31], v13\n"
    "ds_read_b32 v32, v12\n"
    "ds_read_u16 v33, v12 offset:512\n"
    "ds_read_b32 v34, v12 offset:1024\n"
    "ds_read_b32 v35, v21\n"
    "s_waitcnt lgkmcnt(0)\n"
    "v_add_f32 v28, v28, v29\n"
    "v_add_f32 v30, v30, v31\n"
    "v_cmp_lt_i32 vcc, 0, v32\n"
    "v_add_f32 v28, v28, v30\n"
    "v_lshl_add_u32 v33, v33, 2, v35\n"
    "v_add_f32 v34, s26, v34\n"
    "v_ldexp_f32 v28, v28, v33\n"
    "v_add_f32 v28, v28, v34\n"
    "v_cvt_pk_bf16_f32 v28, v28, v28\n"
    "s_mov_b64 exec, vcc\n"
    "global_store_short v32, v28, s[18:19] offset:-2\n"
    "s_mov_b64 exec, -1\n"
    ".Lxd_done:\n"
    "s_endpgm\n"
    ".Lxd_cin:\n"
    "s_load_dwordx4 s[28:31], s[0:1], 0x40\n"
    "s_getpc_b64 s[36:37]\n"
    "s_add_u32 s36, s36, xd_tab@rel32@lo+4\n"
    "s_addc_u32 s37, s37, xd_tab@rel32@hi+12\n"
    "v_lshlrev_b32 v42, 4, v1\n"
    "s_mul_i32 s40, s20, 4480\n"
    "s_min_u32 s40, s40, 30672\n"
    "s_mov_b32 m0, s40\n"
    "s_add_u32 s40, s40, 70304\n"
    "v_lshrrev_b32 v94, 2, v1\n"
    "v_mul_u32_u24 v94, 52, v94\n"
    "v_and_b32 v95, 48, v42\n"
    "v_add_u32 v94, v94, v95\n"
    "v_lshrrev_b32 v92, 5, v1\n"
    "v_and_b32 v96, 31, v0\n"
    "v_min_u32 v95, 25, v96\n"
    "v_mul_u32_u24 v95, 52, v95\n"
    "v_lshl_add_u32 v95, v92, 4, v95\n"
    "s_waitcnt lgkmcnt(0)\n"
    "s_add_u32 s38, s28, s40\n"
    "s_addc_u32 s39, s29, 0\n"
    "global_load_lds_dwordx4 v42, s[38:39] offset:0\n"
    "global_load_lds_dwordx4 v42, s[38:39] offset:1024\n"
    "global_load_lds_dwordx4 v42, s[38:39] offset:2048\n"
    "global_load_lds_dwordx4 v42, s[38:39] offset:3072\n"
    "global_load_lds_dwordx4 v42, s[38:39] offset:3456\n"
    "s_lshl_b32 s40, s2, 2\n"
    "s_load_dword s25, s[10:11], s40\n"
    "s_load_dword s26, s[16:17], 0\n"
    "s_cmp_eq_u32 s20, 3\n"
    "s_cbranch_scc1 .Lxd_on24\n"
    ".Lxd_on24r:\n"
    "s_or_b32 s66, s20, s2\n"
    "s_setprio 0\n"
    "s_cmp_eq_u32 s66, 0\n"
    "s_cbranch_scc1 .Lxd_on25\n"
    ".Lxd_on25r:\n"
    "s_mul_i32 s40, s20, 1352\n"
    "s_add_u32 s38, s28, s40\n"
    "s_addc_u32 s39, s29, 0\n"
    "global_load_dwordx4 v[112:115], v95, s[38:39] offset:0\n"
    "global_load_dwordx4 v[116:119], v95, s[38:39] offset:32\n"
    "s_add_u32 s40, s40, 10816\n"
    "s_add_u32 s38, s28, s40\n"
    "s_addc_u32 s39, s29, 0\n"
    "global_load_dwordx4 v[120:123], v95, s[38:39] offset:0\n"
    "global_load_dwordx4 v[124:127], v95, s[38:39] offset:32\n"
    "s_add_u32 s40, s40, 10816\n"
    "s_add_u32 s38, s28, s40\n"
    "s_addc_u32 s39, s29, 0\n"
    "global_load_dwordx4 v[128:131], v95, s[38:39] offset:0\n"
    "global_load_dwordx4 v[132:135], v95, s[38:39] offset:32\n"
    "s_bitcmp1_b32 s20, 1\n"
    "s_cselect_b32 s40, 1352, 0\n"
    "s_add_u32 s40, s40, 32448\n"
    "s_add_u32 s38, s28, s40\n"
    "s_addc_u32 s39, s29, 0\n"
    "global_load_dwordx4 v[136:139], v95, s[38:39] offset:0\n"
    "global_load_dwordx4 v[140:143], v95, s[38:39] offset:32\n"
    "global_load_dwordx4 v[240:243], v42, s[36:37] offset:0\n"
    "global_load_dwordx4 v[244:247], v42, s[36:37] offset:1024\n"
    "global_load_dwordx4 v[248:251], v42, s[36:37] offset:2048\n"
    "global_load_dwordx4 v[252:255], v42, s[36:37] offset:3072\n"
    "s_mul_i32 s40, s20, 832\n"
    "s_add_u32 s40, s40, 35152\n"
    "s_add_u32 s38, s28, s40\n"
    "s_addc_u32 s39, s29, 0\n"
    "s_lshl_b32 s41, s20, 10\n"
    "s_add_u32 m0, s41, 49152\n"
    "s_nop 0\n"
    "global_load_lds_dwordx4 v94, s[38:39]\n"
    "v_add_u32 v94, 6656, v94\n"
    "s_add_u32 m0, s41, 57344\n"
    "s_nop 0\n"
    "global_load_lds_dwordx4 v94, s[38:39]\n"
    "v_add_u32 v94, 6656, v94\n"
    "s_add_u32 m0, s41, 65536\n"
    "s_nop 0\n"
    "global_load_lds_dwordx4 v94, s[38:39]\n"
    "v_add_u32 v94, 6656, v94\n"
    "s_add_u32 m0, s41, 73728\n"
    "s_nop 0\n"
    "global_load_lds_dwordx4 v94, s[38:39]\n"
    "v_add_u32 v94, 6656, v94\n"
    "s_add_u32 m0, s41, 81920\n"
    "s_nop 0\n"
    "global_load_lds_dwordx4 v94, s[38:39]\n"
    "v_add_u32 v94, 6656, v94\n"
    "s_add_u32 m0, s41, 90112\n"
    "s_cmp_ge_u32 s20, 3\n"
    "s_cbranch_scc1 .Lxd_b1\n"
    "global_load_lds_dwordx4 v94, s[38:39]\n"
    ".Lxd_b1:\n"
    "v_mov_b32 v96, 0\n"
    "v_mov_b32 v97, 0\n"
    "v_mov_b32 v98, 0\n"
    "v_mov_b32 v99, 0\n"
    "v_lshlrev_b32 v12, 3, v0\n"
    "v_lshlrev_b32 v13, 4, v0\n"
    "v_add_u32 v13, 98304, v13\n"
    "v_lshlrev_b32 v21, 2, v0\n"
    "v_add_u32 v21, 149504, v21\n"
    "ds_write_b64 v12, v[96:97] offset:35840\n"
    "ds_write_b32 v21, v96\n"
    "ds_write_b128 v13, v[96:99] offset:0\n"
    "ds_write_b128 v13, v[96:99] offset:8192\n"
    "ds_write_b128 v13, v[96:99] offset:16384\n"
    "ds_write_b128 v13, v[96:99] offset:24576\n"
    "ds_write_b128 v13, v[96:99] offset:32768\n"
    "ds_write_b128 v13, v[96:99] offset:40960\n"
    "s_mul_i32 s47, s2, 86\n"
    "s_lshr_b32 s47, s47, 8\n"
    "s_mul_i32 s3, s47, 3\n"
    "s_sub_u32 s3, s2, s3\n"
    "s_lshr_b32 s21, s20, 2\n"
    "s_lshl1_add_u32 s22, s21, s20\n"
    "s_and_b32 s22, s22, 3\n"
    "s_mul_i32 s43, s21, 49152\n"
    "s_lshl_b32 s58, s21, 8\n"
    "s_add_u32 s58, s58, 150528\n"
    "s_lshl_b32 s54, s47, 9\n"
    "s_add_u32 s54, s54, 2\n"
    "s_lshl_b32 s40, s47, 7\n"
    "s_add_u32 s40, s40, 32768\n"
    "s_add_u32 s52, s10, s40\n"
    "s_addc_u32 s53, s11, 0\n"
    "s_mov_b32 s57, 0x7fff7fff\n"
    "s_mov_b32 s56, 0\n"
    "s_mov_b32 s27, 0\n"
    "s_mov_b32 s55, 9\n"
    "s_mov_b32 s44, 0\n"
    "s_movk_i32 s46, 17920\n"
    "v_lshrrev_b32 v12, 4, v1\n"
    "v_bfe_u32 v13, v1, 2, 2\n"
    "v_and_b32 v21, 3, v1\n"
    "s_lshl_b32 s40, s22, 5\n"
    "s_add_u32 s40, s40, s43\n"
    "v_lshl_add_u32 v21, v21, 3, s40\n"
    "s_movk_i32 s41, 160\n"
    "v_add_u32 v22, v12, v13\n"
    "v_and_b32 v22, 3, v22\n"
    "v_mad_u32_u24 v4, v22, s41, v21\n"
    "v_cmp_gt_u32 vcc, 2, v13\n"
    "v_add_u32 v24, 22, v13\n"
    "v_lshlrev_b32 v13, 3, v13\n"
    "v_cndmask_b32 v22, v24, v22, vcc\n"
    "v_mad_u32_u24 v3, v22, s41, v21\n"
    "v_cmp_eq_u32 vcc, 3, v12\n"
    "v_xor_b32 v22, 0, v12\n"
    "v_and_b32 v22, 1, v22\n"
    "v_lshlrev_b32 v22, 1, v22\n"
    "v_lshl_add_u32 v22, v12, 3, v22\n"
    "s_mov_b32 s40, 0x05040100\n"
    "v_bfe_u32 v24, s40, v13, 8\n"
    "v_add_u32 v22, v22, v24\n"
    "s_mov_b32 s40, 0x1a191818\n"
    "v_bfe_u32 v24, s40, v13, 8\n"
    "v_cndmask_b32 v22, v22, v24, vcc\n"
    "v_mad_u32_u24 v5, v22, s41, v21\n"
    "v_xor_b32 v22, 1, v12\n"
    "v_and_b32 v22, 1, v22\n"
    "v_lshlrev_b32 v22, 1, v22\n"
    "v_lshl_add_u32 v22, v12, 3, v22\n"
    "s_mov_b32 s40, 0x05040100\n"
    "v_bfe_u32 v24, s40, v13, 8\n"
    "v_add_u32 v22, v22, v24\n"
    "s_mov_b32 s40, 0x1a191918\n"
    "v_bfe_u32 v24, s40, v13, 8\n"
    "v_cndmask_b32 v22, v22, v24, vcc\n"
    "v_mad_u32_u24 v6, v22, s41, v21\n"
    "v_bfrev_b32 v14, v12\n"
    "v_lshrrev_b32 v14, 26, v14\n"
    "s_add_u32 s40, s22, 3\n"
    "s_and_b32 s40, s40, 3\n"
    "s_lshl2_add_u32 s40, s40, s22\n"
    "s_add_u32 s40, s40, s58\n"
    "s_lshl_b32 s40, s40, 2\n"
    "s_add_u32 s40, s40, -450048\n"
    "v_add_u32 v14, s40, v14\n"
    "v_and_b32 v2, 1, v1\n"
    "v_mul_u32_u24 v2, 0xaaaa, v2\n"
    "v_add_u32 v2, 0x4444, v2\n"
    "v_lshrrev_b32 v12, 3, v1\n"
    "v_mul_u32_u24 v17, 12, v12\n"
    "v_and_b32 v19, 7, v1\n"
    "v_lshlrev_b32 v19, 4, v19\n"
    "v_mad_u32_u24 v15, v17, 40, v19\n"
    "v_lshrrev_b32 v12, 5, v1\n"
    "v_lshlrev_b32 v18, 2, v12\n"
    "v_and_b32 v20, 31, v1\n"
    "v_lshlrev_b32 v20, 2, v20\n"
    "v_mad_u32_u24 v16, v12, s41, v20\n"
    "s_lshl2_add_u32 s40, s22, s58\n"
    "s_sub_u32 s40, s40, 20\n"
    "v_mov_b32 v25, s40\n"
    "s_movk_i32 s41, 4480\n"
    "v_lshrrev_b32 v12, 5, v19\n"
    "v_sub_u32 v12, 1, v12\n"
    "v_and_b32 v12, 3, v12\n"
    "v_add_u32 v12, s22, v12\n"
    "v_add_u32 v15, s43, v15\n"
    "v_xor_b32 v13, 4, v12\n"
    "v_mad_u32_u24 v0, v13, s41, v15\n"
    "v_mad_u32_u24 v15, v12, s41, v15\n"
    "v_add_u32 v0, v0, v15\n"
    "v_lshrrev_b32 v12, 5, v20\n"
    "v_sub_u32 v12, 1, v12\n"
    "v_and_b32 v12, 3, v12\n"
    "v_add_u32 v12, s22, v12\n"
    "v_add_u32 v16, s43, v16\n"
    "v_xor_b32 v13, 4, v12\n"
    "v_mad_u32_u24 v23, v13, s41, v16\n"
    "v_mad_u32_u24 v16, v12, s41, v16\n"
    "v_add_u32 v23, v23, v16\n"
    "s_lshl1_add_u32 s23, s3, s21\n"
    "s_mul_i32 s23, s23, 6\n"
    "s_add_u32 s23, s23, s22\n"
    "s_add_u32 s23, s23, 78\n"
    "v_mov_b32 v24, s23\n"
    "s_nop 0\n"
    "v_readfirstlane_b32 s40, v24\n"
    "s_or_b32 s40, s40, s44\n"
    "s_lshl1_add_u32 s41, s40, s54\n"
    "s_cmp_lt_u32 s40, 256\n"
    "s_cselect_b32 s40, s41, -1\n"
    "s_cselect_b32 s41, s41, 2\n"
    "s_cselect_b64 s[48:49], -1, 0\n"
    "v_mov_b32 v12, s40\n"
    "s_mul_i32 s41, s41, 52\n"
    "s_add_u32 s38, s4, s41\n"
    "s_addc_u32 s39, s5, 0\n"
    "global_load_dwordx3 v[36:38], v17, s[38:39] offset:-104\n"
    "global_load_dword v41, v18, s[38:39] offset:-8\n"
    "v_lshrrev_b32 v90, 4, v1\n"
    "v_and_b32 v91, 15, v1\n"
    "v_lshrrev_b32 v13, 2, v91\n"
    "v_lshl_add_u32 v93, v90, 3, v13\n"
    "v_add_u32 v94, 4, v93\n"
    "v_and_b32 v22, 3, v1\n"
    "s_lshl_b32 s40, s20, 5\n"
    "v_lshl_add_u32 v21, v22, 3, s40\n"
    "s_movk_i32 s41, 1352\n"
    "v_min_u32 v93, 25, v93\n"
    "v_mad_u32_u24 v93, v93, s41, v21\n"
    "v_min_u32 v94, 25, v94\n"
    "v_mad_u32_u24 v94, v94, s41, v21\n"
    "s_lshl_b32 s40, s20, 4\n"
    "v_add_u32 v21, s40, v91\n"
    "v_mul_u32_u24 v76, 2521, v21\n"
    "v_lshrrev_b32 v76, 16, v76\n"
    "v_mad_u32_u24 v76, v76, 6, v21\n"
    "v_lshlrev_b32 v76, 1, v76\n"
    "v_add_u32 v21, 128, v21\n"
    "v_mul_u32_u24 v77, 2521, v21\n"
    "v_lshrrev_b32 v77, 16, v77\n"
    "v_mad_u32_u24 v77, v77, 6, v21\n"
    "v_lshlrev_b32 v77, 1, v77\n"
    "v_add_u32 v21, 128, v21\n"
    "v_mul_u32_u24 v78, 2521, v21\n"
    "v_lshrrev_b32 v78, 16, v78\n"
    "v_mad_u32_u24 v78, v78, 6, v21\n"
    "v_lshlrev_b32 v78, 1, v78\n"
    "v_add_u32 v21, 128, v21\n"
    "v_mul_u32_u24 v79, 2521, v21\n"
    "v_lshrrev_b32 v79, 16, v79\n"
    "v_mad_u32_u24 v79, v79, 6, v21\n"
    "v_lshlrev_b32 v79, 1, v79\n"
    "v_add_u32 v21, 128, v21\n"
    "v_mul_u32_u24 v80, 2521, v21\n"
    "v_lshrrev_b32 v80, 16, v80\n"
    "v_mad_u32_u24 v80, v80, 6, v21\n"
    "v_lshlrev_b32 v80, 1, v80\n"
    "v_add_u32 v21, 128, v21\n"
    "v_mul_u32_u24 v81, 2521, v21\n"
    "v_lshrrev_b32 v81, 16, v81\n"
    "v_mad_u32_u24 v81, v81, 6, v21\n"
    "v_lshlrev_b32 v81, 1, v81\n"
    "v_cmp_gt_u32 vcc, 676, v21\n"
    "v_lshlrev_b32 v95, 4, v90\n"
    "v_mov_b32 v21, 0x40000\n"
    "v_cndmask_b32 v81, v21, v81, vcc\n"
    "v_cmp_gt_u32 vcc, 26, v1\n"
    "v_lshlrev_b32 v82, 6, v1\n"
    "v_lshlrev_b32 v83, 1, v1\n"
    "v_cndmask_b32 v82, v21, v82, vcc\n"
    "v_bfe_u32 v21, v1, 1, 4\n"
    "v_lshlrev_b32 v104, 2, v92\n"
    "v_sub_u32 v21, v21, v104\n"
    "v_and_b32 v104, 16, v42\n"
    "s_movk_i32 s40, 0x3f80\n"
    "v_lshlrev_b32_e64 v96, v104, s40\n"
    "v_cmp_eq_u32 s[60:61], 0, v21\n"
    "v_cmp_eq_u32 s[62:63], 1, v21\n"
    "v_cmp_eq_u32 s[64:65], 2, v21\n"
    "v_cmp_eq_u32 s[50:51], 3, v21\n"
    "v_cndmask_b32 v104, 0, v96, s[60:61]\n"
    "v_cndmask_b32 v105, 0, v96, s[62:63]\n"
    "v_cndmask_b32 v106, 0, v96, s[64:65]\n"
    "v_cndmask_b32 v107, 0, v96, s[50:51]\n"
    "v_cmp_eq_u32 s[60:61], 8, v21\n"
    "v_cmp_eq_u32 s[62:63], 9, v21\n"
    "v_cmp_eq_u32 s[64:65], 10, v21\n"
    "v_cmp_eq_u32 s[50:51], 11, v21\n"
    "v_cndmask_b32 v108, 0, v96, s[60:61]\n"
    "v_cndmask_b32 v109, 0, v96, s[62:63]\n"
    "v_cndmask_b32 v110, 0, v96, s[64:65]\n"
    "v_cndmask_b32 v111, 0, v96, s[50:51]\n"
    "s_waitcnt vmcnt(19) lgkmcnt(0)\n"
    "s_barrier\n"
    "v_ashrrev_i32 v21, 31, v12\n"
    "ds_write2_b32 v25, v12, v21 offset0:5 offset1:133\n"
    "v_add_u32 v95, 39936, v95\n"
    "ds_read2_b64 v[96:99], v95 offset0:13 offset1:14\n"
    "ds_read_u16 v84, v83 offset:39988\n"
    "ds_read_u16 v85, v83 offset:39936\n"
    "ds_read_b64_tr_b16 v[192:193], v93 offset:0\n"
    "ds_read_b64_tr_b16 v[194:195], v94 offset:0\n"
    "ds_read_b64_tr_b16 v[196:197], v93 offset:256\n"
    "ds_read_b64_tr_b16 v[198:199], v94 offset:256\n"
    "ds_read_b64_tr_b16 v[200:201], v93 offset:512\n"
    "ds_read_b64_tr_b16 v[202:203], v94 offset:512\n"
    "ds_read_b64_tr_b16 v[204:205], v93 offset:768\n"
    "ds_read_b64_tr_b16 v[206:207], v94 offset:768\n"
    "ds_read_b64_tr_b16 v[208:209], v93 offset:1024\n"
    "ds_read_b64_tr_b16 v[210:211], v94 offset:1024\n"
    "ds_read_b64_tr_b16 v[212:213], v93 offset:1280\n"
    "ds_read_b64_tr_b16 v[214:215], v94 offset:1280\n"
    "s_cmp_eq_u32 s66, 0\n"
    "s_cbranch_scc1 .Lxd_on26\n"
    ".Lxd_on26r:\n"
    "v_cmp_gt_u32 vcc, 3, v90\n"
    "s_waitcnt lgkmcnt(12)\n"
    "v_lshlrev_b32 v84, 16, v84\n"
    "v_cndmask_b32 v97, 0, v97, vcc\n"
    "v_cndmask_b32 v98, 0, v98, vcc\n"
    "v_cndmask_b32 v99, 0, v99, vcc\n"
    "v_mul_f32 v84, 0x41800000, v84\n"
    "v_lshlrev_b32 v95, 16, v85\n"
    "s_waitcnt lgkmcnt(10)\n"
    "v_mfma_f32_16x16x32_bf16 v[216:219], v[96:99], v[192:195], 0\n"
    "s_waitcnt lgkmcnt(8)\n"
    "v_mfma_f32_16x16x32_bf16 v[220:223], v[96:99], v[196:199], 0\n"
    "s_waitcnt lgkmcnt(6)\n"
    "v_mfma_f32_16x16x32_bf16 v[224:227], v[96:99], v[200:203], 0\n"
    "s_waitcnt lgkmcnt(4)\n"
    "v_mfma_f32_16x16x32_bf16 v[228:231], v[96:99], v[204:207], 0\n"
    "s_waitcnt lgkmcnt(2)\n"
    "v_mfma_f32_16x16x32_bf16 v[232:235], v[96:99], v[208:211], 0\n"
    "s_waitcnt lgkmcnt(0)\n"
    "v_mfma_f32_16x16x32_bf16 v[236:239], v[96:99], v[212:215], 0\n"
    "ds_write_b16_d16_hi v82, v84 offset:35896\n"
    "v_mul_f32 v95, 0x43800000, v95\n"
    "s_nop 0\n"
    "v_cvt_pk_bf16_f32 v216, v216, v216\n"
    "ds_write_b16 v76, v216 offset:35840\n"
    "v_cvt_pk_bf16_f32 v220, v220, v220\n"
    "ds_write_b16 v77, v220 offset:35840\n"
    "v_cvt_pk_bf16_f32 v224, v224, v224\n"
    "ds_write_b16 v78, v224 offset:35840\n"
    "v_cvt_pk_bf16_f32 v228, v228, v228\n"
    "ds_write_b16 v79, v228 offset:35840\n"
    "v_cvt_pk_bf16_f32 v232, v232, v232\n"
    "ds_write_b16 v80, v232 offset:35840\n"
    "v_cvt_pk_bf16_f32 v236, v236, v236\n"
    "ds_write_b16 v81, v236 offset:35840\n"
    "s_waitcnt vmcnt(7)\n"
    "s_lshl_b32 s40, s20, 5\n"
    "s_add_u32 s40, s40, 97264\n"
    "s_and_b32 s24, s20, 5\n"
    "v_mfma_f32_32x32x16_bf16 v[144:159], v[104:107], v[112:115], 0\n"
    "v_mad_u32_u16 v44, v240, 2, s40 op_sel:[0,0,0,0]\n"
    "v_mad_u32_u16 v45, v240, 2, s40 op_sel:[1,0,0,0]\n"
    "v_mfma_f32_32x32x16_bf16 v[144:159], v[112:115], v[104:107], v[144:159]\n"
    "v_mad_u32_u16 v46, v241, 2, s40 op_sel:[0,0,0,0]\n"
    "v_mad_u32_u16 v47, v241, 2, s40 op_sel:[1,0,0,0]\n"
    "v_mad_u32_u16 v48, v242, 2, s40 op_sel:[0,0,0,0]\n"
    "v_mfma_f32_32x32x16_bf16 v[144:159], v[108:111], v[116:119], v[144:159]\n"
    "v_mad_u32_u16 v49, v242, 2, s40 op_sel:[1,0,0,0]\n"
    "v_mad_u32_u16 v50, v243, 2, s40 op_sel:[0,0,0,0]\n"
    "v_mfma_f32_32x32x16_bf16 v[144:159], v[116:119], v[108:111], v[144:159]\n"
    "v_mad_u32_u16 v51, v243, 2, s40 op_sel:[1,0,0,0]\n"
    "v_mad_u32_u16 v52, v244, 2, s40 op_sel:[0,0,0,0]\n"
    "v_mad_u32_u16 v53, v244, 2, s40 op_sel:[1,0,0,0]\n"
    "v_mfma_f32_32x32x16_bf16 v[160:175], v[104:107], v[120:123], 0\n"
    "v_mad_u32_u16 v54, v245, 2, s40 op_sel:[0,0,0,0]\n"
    "v_mad_u32_u16 v55, v245, 2, s40 op_sel:[1,0,0,0]\n"
    "v_mfma_f32_32x32x16_bf16 v[160:175], v[120:123], v[104:107], v[160:175]\n"
    "v_mad_u32_u16 v56, v246, 2, s40 op_sel:[0,0,0,0]\n"
    "v_mad_u32_u16 v57, v246, 2, s40 op_sel:[1,0,0,0]\n"
    "v_mad_u32_u16 v58, v247, 2, s40 op_sel:[0,0,0,0]\n"
    "v_mfma_f32_32x32x16_bf16 v[160:175], v[108:111], v[124:127], v[160:175]\n"
    "v_mad_u32_u16 v60, v248, 2, s40 op_sel:[0,0,0,0]\n"
    "v_mad_u32_u16 v61, v248, 2, s40 op_sel:[1,0,0,0]\n"
    "v_mfma_f32_32x32x16_bf16 v[160:175], v[124:127], v[108:111], v[160:175]\n"
    "v_mad_u32_u16 v62, v249, 2, s40 op_sel:[0,0,0,0]\n"
    "v_mad_u32_u16 v63, v249, 2, s40 op_sel:[1,0,0,0]\n"
    "v_mad_u32_u16 v64, v250, 2, s40 op_sel:[0,0,0,0]\n"
    "v_mfma_f32_32x32x16_bf16 v[176:191], v[104:107], v[128:131], 0\n"
    "v_mad_u32_u16 v65, v250, 2, s40 op_sel:[1,0,0,0]\n"
    "v_mad_u32_u16 v66, v251, 2, s40 op_sel:[0,0,0,0]\n"
    "v_mfma_f32_32x32x16_bf16 v[176:191], v[128:131], v[104:107], v[176:191]\n"
    "v_mad_u32_u16 v67, v251, 2, s40 op_sel:[1,0,0,0]\n"
    "v_mad_u32_u16 v68, v252, 2, s40 op_sel:[0,0,0,0]\n"
    "v_mad_u32_u16 v69, v252, 2, s40 op_sel:[1,0,0,0]\n"
    "v_mfma_f32_32x32x16_bf16 v[176:191], v[108:111], v[132:135], v[176:191]\n"
    "v_mad_u32_u16 v70, v253, 2, s40 op_sel:[0,0,0,0]\n"
    "v_mad_u32_u16 v71, v253, 2, s40 op_sel:[1,0,0,0]\n"
    "v_mfma_f32_32x32x16_bf16 v[176:191], v[132:135], v[108:111], v[176:191]\n"
    "v_mad_u32_u16 v72, v254, 2, s40 op_sel:[0,0,0,0]\n"
    "v_mad_u32_u16 v73, v254, 2, s40 op_sel:[1,0,0,0]\n"
    "v_mad_u32_u16 v74, v255, 2, s40 op_sel:[0,0,0,0]\n"
    "s_cmp_eq_u32 s24, 0\n"
    "s_cbranch_scc1 .Lxd_on27\n"
    ".Lxd_on27r:\n"
    "v_lshrrev_b32 v13, 2, v91\n"
    "v_lshl_add_u32 v13, v92, 3, v13\n"
    "v_and_b32 v21, 1, v90\n"
    "v_lshl_add_u32 v21, v21, 2, v22\n"
    "v_cmp_eq_u32 vcc, 7, v21\n"
    "v_lshlrev_b32 v21, 3, v21\n"
    "v_lshl_add_u32 v200, v13, 6, v21\n"
    "s_lshl_b32 s40, s20, 6\n"
    "s_add_u32 s40, s40, 49152\n"
    "v_add_u32 v21, s40, v21\n"
    "s_movk_i32 s41, 1664\n"
    "v_mov_b32 v97, 37888\n"
    "s_bfe_u32 s42, s20, 0x10001\n"
    "s_add_u32 s34, s42, 24\n"
    "s_sub_u32 s42, s34, s20\n"
    "s_lshl_b32 s42, s42, 6\n"
    "v_add_u32 v192, 0, v13\n"
    "v_min_u32 v192, 25, v192\n"
    "v_mad_u32_u24 v192, v192, s41, v21\n"
    "v_cndmask_b32 v192, v192, v97, vcc\n"
    "v_add_u32 v196, s42, v192\n"
    "v_add_u32 v193, 4, v13\n"
    "v_min_u32 v193, 25, v193\n"
    "v_mad_u32_u24 v193, v193, s41, v21\n"
    "v_cndmask_b32 v193, v193, v97, vcc\n"
    "v_add_u32 v197, s42, v193\n"
    "v_add_u32 v194, 16, v13\n"
    "v_min_u32 v194, 25, v194\n"
    "v_mad_u32_u24 v194, v194, s41, v21\n"
    "v_cndmask_b32 v194, v194, v97, vcc\n"
    "v_add_u32 v198, s42, v194\n"
    "v_add_u32 v195, 20, v13\n"
    "v_min_u32 v195, 25, v195\n"
    "v_mad_u32_u24 v195, v195, s41, v21\n"
    "v_cndmask_b32 v195, v195, v97, vcc\n"
    "v_add_u32 v199, s42, v195\n"
    "s_waitcnt vmcnt(0)\n"
    "v_lshl_or_b32 v36, v36, 7, v19\n"
    "v_lshl_or_b32 v37, v37, 7, v19\n"
    "v_lshl_or_b32 v38, v38, 7, v19\n"
    "v_lshl_or_b32 v41, v41, 7, v20\n"
    "global_load_dwordx4 v[28:31], v36, s[8:9]\n"
    "global_load_dwordx4 v[32:35], v37, s[8:9]\n"
    "global_load_dword v40, v41, s[8:9]\n"
    "global_load_dwordx4 v[36:39], v38, s[8:9]\n"
    "v_max3_f32 v96, |v144|, |v145|, 0\n"
    "v_max3_f32 v96, |v146|, |v147|, v96\n"
    "v_max3_f32 v96, |v148|, |v149|, v96\n"
    "v_max3_f32 v96, |v150|, |v151|, v96\n"
    "v_max3_f32 v96, |v152|, |v153|, v96\n"
    "v_max3_f32 v96, |v154|, |v155|, v96\n"
    "v_max3_f32 v96, |v156|, |v157|, v96\n"
    "v_max3_f32 v96, |v160|, |v161|, v96\n"
    "v_max3_f32 v96, |v162|, |v163|, v96\n"
    "v_max3_f32 v96, |v164|, |v165|, v96\n"
    "v_max3_f32 v96, |v166|, |v167|, v96\n"
    "v_max3_f32 v96, |v168|, |v169|, v96\n"
    "v_max3_f32 v96, |v170|, |v171|, v96\n"
    "v_max3_f32 v96, |v172|, |v173|, v96\n"
    "v_max3_f32 v96, |v176|, |v177|, v96\n"
    "v_max3_f32 v96, |v178|, |v179|, v96\n"
    "v_max3_f32 v96, |v180|, |v181|, v96\n"
    "v_max3_f32 v96, |v182|, |v183|, v96\n"
    "v_max3_f32 v96, |v184|, |v185|, v96\n"
    "v_max3_f32 v96, |v186|, |v187|, v96\n"
    "v_max3_f32 v96, |v188|, |v189|, v96\n"
    "s_cmp_eq_u32 s24, 0\n"
    "s_cbranch_scc1 .Lxd_on28\n"
    ".Lxd_on28r:\n"
    "s_nop 1\n"
    "v_max_u32_dpp v96, v96, v96 quad_perm:[1,0,3,2] row_mask:0xf bank_mask:0xf\n"
    "s_nop 1\n"
    "v_max_u32_dpp v96, v96, v96 quad_perm:[2,3,0,1] row_mask:0xf bank_mask:0xf\n"
    "s_nop 1\n"
    "v_max_u32_dpp v96, v96, v96 row_half_mirror row_mask:0xf bank_mask:0xf\n"
    "s_nop 1\n"
    "v_max_u32_dpp v96, v96, v96 row_mirror row_mask:0xf bank_mask:0xf\n"
    "s_nop 1\n"
    "v_max_u32_dpp v96, v96, v96 row_bcast:15 row_mask:0xa bank_mask:0xf\n"
    "s_nop 1\n"
    "v_max_u32_dpp v96, v96, v96 row_bcast:31 row_mask:0xc bank_mask:0xf\n"
    "v_mov_b32 v97, 39920\n"
    "s_brev_b64 exec, 1\n"
    "ds_max_u32 v97, v96 offset:0\n"
    "s_mov_b64 exec, -1\n"
    "v_lshrrev_b32 v13, 2, v20\n"
    "v_sub_u32 v13, v13, v18\n"
    "v_cmp_eq_u32 vcc, 60, v1\n"
    "s_nop 1\n"
    "v_cndmask_b32 v13, v13, v1, vcc\n"
    "v_cmp_eq_u32 s[60:61], 0, v13\n"
    "v_cmp_eq_u32 s[62:63], 1, v13\n"
    "v_cmp_eq_u32 s[64:65], 2, v13\n"
    "v_cmp_eq_u32 s[50:51], 3, v13\n"
    "v_cndmask_b32 v76, 1.0, 0.5, s[60:61]\n"
    "v_cndmask_b32 v77, 1.0, 0.5, s[62:63]\n"
    "v_cndmask_b32 v78, 1.0, 0.5, s[64:65]\n"
    "v_cndmask_b32 v79, 1.0, 0.5, s[50:51]\n"
    "v_cmp_eq_u32 s[60:61], 8, v13\n"
    "v_cmp_eq_u32 s[62:63], 9, v13\n"
    "v_cmp_eq_u32 s[64:65], 10, v13\n"
    "v_cmp_eq_u32 s[50:51], 11, v13\n"
    "v_cndmask_b32 v80, 1.0, 0.5, s[60:61]\n"
    "v_cndmask_b32 v81, 1.0, 0.5, s[62:63]\n"
    "v_cndmask_b32 v82, 1.0, 0.5, s[64:65]\n"
    "v_cndmask_b32 v83, 1.0, 0.5, s[50:51]\n"
    "v_cmp_eq_u32 s[60:61], 16, v13\n"
    "v_cmp_eq_u32 s[62:63], 17, v13\n"
    "v_cmp_eq_u32 s[64:65], 18, v13\n"
    "v_cmp_eq_u32 s[50:51], 19, v13\n"
    "v_cndmask_b32 v84, 1.0, 0.5, s[60:61]\n"
    "v_cndmask_b32 v85, 1.0, 0.5, s[62:63]\n"
    "v_cndmask_b32 v86, 1.0, 0.5, s[64:65]\n"
    "v_cndmask_b32 v87, 1.0, 0.5, s[50:51]\n"
    "v_cmp_eq_u32 s[60:61], 24, v13\n"
    "v_cmp_eq_u32 s[62:63], 25, v13\n"
    "s_nop 0\n"
    "v_cndmask_b32 v88, 1.0, 0.5, s[60:61]\n"
    "v_cndmask_b32 v89, 1.0, 0.5, s[62:63]\n"
    "s_cmp_eq_u32 s20, 7\n"
    "s_cbranch_scc1 .Lxd_on29\n"
    ".Lxd_on29r:\n"
    "s_waitcnt lgkmcnt(0)\n"
    "s_barrier\n"
    "ds_read_b32 v98, v97\n"
    "ds_read_b64_tr_b16 v[104:105], v200 offset:35840\n"
    "ds_read_b64_tr_b16 v[106:107], v200 offset:36096\n"
    "ds_read_b64_tr_b16 v[108:109], v200 offset:36864\n"
    "ds_read_b64_tr_b16 v[110:111], v200 offset:37120\n"
    "ds_read_b64_tr_b16 v[112:113], v192 offset:0\n"
    "ds_read_b64_tr_b16 v[114:115], v193 offset:0\n"
    "ds_read_b64_tr_b16 v[116:117], v194 offset:0\n"
    "ds_read_b64_tr_b16 v[118:119], v195 offset:0\n"
    "ds_read_b64_tr_b16 v[120:121], v192 offset:512\n"
    "ds_read_b64_tr_b16 v[122:123], v193 offset:512\n"
    "ds_read_b64_tr_b16 v[124:125], v194 offset:512\n"
    "ds_read_b64_tr_b16 v[126:127], v195 offset:512\n"
    "ds_read_b64_tr_b16 v[128:129], v192 offset:1024\n"
    "ds_read_b64_tr_b16 v[130:131], v193 offset:1024\n"
    "ds_read_b64_tr_b16 v[132:133], v194 offset:1024\n"
    "ds_read_b64_tr_b16 v[134:135], v195 offset:1024\n"
    "ds_read_b64_tr_b16 v[136:137], v196 offset:0\n"
    "ds_read_b64_tr_b16 v[138:139], v197 offset:0\n"
    "ds_read_b64_tr_b16 v[140:141], v198 offset:0\n"
    "ds_read_b64_tr_b16 v[142:143], v199 offset:0\n"
    "s_waitcnt lgkmcnt(15)\n"
    "v_lshrrev_b32 v98, 23, v98\n"
    "v_max_u32 v98, 40, v98\n"
    "v_min_u32 v98, 250, v98\n"
    "v_sub_u32 v96, 141, v98\n"
    "v_ldexp_f32 v76, v76, v96\n"
    "v_ldexp_f32 v77, v77, v96\n"
    "v_ldexp_f32 v78, v78, v96\n"
    "v_ldexp_f32 v79, v79, v96\n"
    "v_ldexp_f32 v80, v80, v96\n"
    "v_ldexp_f32 v81, v81, v96\n"
    "v_ldexp_f32 v82, v82, v96\n"
    "v_ldexp_f32 v83, v83, v96\n"
    "v_ldexp_f32 v84, v84, v96\n"
    "v_ldexp_f32 v85, v85, v96\n"
    "v_ldexp_f32 v86, v86, v96\n"
    "v_ldexp_f32 v87, v87, v96\n"
    "v_ldexp_f32 v88, v88, v96\n"
    "v_ldexp_f32 v89, v89, v96\n"
    "s_waitcnt lgkmcnt(0)\n"
    "v_mfma_f32_32x32x16_bf16 v[192:207], v[104:107], v[112:115], 0\n"
    "v_pk_mul_f32 v[144:145], v[144:145], v[76:77]\n"
    "v_cvt_pk_f16_f32 v144, v144, v145\n"
    "ds_write_b16 v44, v144 offset:0\n"
    "ds_write_b16_d16_hi v45, v144 offset:0\n"
    "v_pk_mul_f32 v[146:147], v[146:147], v[78:79]\n"
    "v_cvt_pk_f16_f32 v146, v146, v147\n"
    "ds_write_b16 v46, v146 offset:0\n"
    "v_mfma_f32_32x32x16_bf16 v[192:207], v[112:115], v[104:107], v[192:207]\n"
    "ds_write_b16_d16_hi v47, v146 offset:0\n"
    "v_pk_mul_f32 v[148:149], v[148:149], v[80:81]\n"
    "v_cvt_pk_f16_f32 v148, v148, v149\n"
    "ds_write_b16 v48, v148 offset:0\n"
    "ds_write_b16_d16_hi v49, v148 offset:0\n"
    "v_pk_mul_f32 v[150:151], v[150:151], v[82:83]\n"
    "v_cvt_pk_f16_f32 v150, v150, v151\n"
    "v_mfma_f32_32x32x16_bf16 v[192:207], v[108:111], v[116:119], v[192:207]\n"
    "ds_write_b16 v50, v150 offset:0\n"
    "ds_write_b16_d16_hi v51, v150 offset:0\n"
    "v_pk_mul_f32 v[152:153], v[152:153], v[84:85]\n"
    "v_cvt_pk_f16_f32 v152, v152, v153\n"
    "ds_write_b16 v52, v152 offset:0\n"
    "ds_write_b16_d16_hi v53, v152 offset:0\n"
    "v_pk_mul_f32 v[154:155], v[154:155], v[86:87]\n"
    "v_mfma_f32_32x32x16_bf16 v[192:207], v[116:119], v[108:111], v[192:207]\n"
    "v_cvt_pk_f16_f32 v154, v154, v155\n"
    "ds_write_b16 v54, v154 offset:0\n"
    "ds_write_b16_d16_hi v55, v154 offset:0\n"
    "v_pk_mul_f32 v[156:157], v[156:157], v[88:89]\n"
    "v_cvt_pk_f16_f32 v156, v156, v157\n"
    "ds_write_b16 v58, v156 offset:0\n"
    "ds_write_b16_d16_hi v57, v156 offset:0\n"
    "v_mfma_f32_32x32x16_bf16 v[208:223], v[104:107], v[120:123], 0\n"
    "v_pk_mul_f32 v[160:161], v[160:161], v[76:77]\n"
    "v_cvt_pk_f16_f32 v160, v160, v161\n"
    "ds_write_b16 v44, v160 offset:16\n"
    "ds_write_b16_d16_hi v45, v160 offset:16\n"
    "v_pk_mul_f32 v[162:163], v[162:163], v[78:79]\n"
    "v_cvt_pk_f16_f32 v162, v162, v163\n"
    "ds_write_b16 v46, v162 offset:16\n"
    "v_mfma_f32_32x32x16_bf16 v[208:223], v[120:123], v[104:107], v[208:223]\n"
    "ds_write_b16_d16_hi v47, v162 offset:16\n"
    "v_pk_mul_f32 v[164:165], v[164:165], v[80:81]\n"
    "v_cvt_pk_f16_f32 v164, v164, v165\n"
    "ds_write_b16 v48, v164 offset:16\n"
    "ds_write_b16_d16_hi v49, v164 offset:16\n"
    "v_pk_mul_f32 v[166:167], v[166:167], v[82:83]\n"
    "v_cvt_pk_f16_f32 v166, v166, v167\n"
    "v_mfma_f32_32x32x16_bf16 v[208:223], v[108:111], v[124:127], v[208:223]\n"
    "ds_write_b16 v50, v166 offset:16\n"
    "ds_write_b16_d16_hi v51, v166 offset:16\n"
    "v_pk_mul_f32 v[168:169], v[168:169], v[84:85]\n"
    "v_cvt_pk_f16_f32 v168, v168, v169\n"
    "ds_write_b16 v52, v168 offset:16\n"
    "ds_write_b16_d16_hi v53, v168 offset:16\n"
    "v_pk_mul_f32 v[170:171], v[170:171], v[86:87]\n"
    "v_mfma_f32_32x32x16_bf16 v[208:223], v[124:127], v[108:111], v[208:223]\n"
    "v_cvt_pk_f16_f32 v170, v170, v171\n"
    "ds_write_b16 v54, v170 offset:16\n"
    "ds_write_b16_d16_hi v55, v170 offset:16\n"
    "v_pk_mul_f32 v[172:173], v[172:173], v[88:89]\n"
    "v_cvt_pk_f16_f32 v172, v172, v173\n"
    "ds_write_b16 v58, v172 offset:16\n"
    "ds_write_b16_d16_hi v57, v172 offset:16\n"
    "v_mfma_f32_32x32x16_bf16 v[224:239], v[104:107], v[128:131], 0\n"
    "v_pk_mul_f32 v[176:177], v[176:177], v[76:77]\n"
    "v_cvt_pk_f16_f32 v176, v176, v177\n"
    "ds_write_b16 v44, v176 offset:2080\n"
    "ds_write_b16_d16_hi v45, v176 offset:2080\n"
    "v_pk_mul_f32 v[178:179], v[178:179], v[78:79]\n"
    "v_cvt_pk_f16_f32 v178, v178, v179\n"
    "ds_write_b16 v46, v178 offset:2080\n"
    "v_mfma_f32_32x32x16_bf16 v[224:239], v[128:131], v[104:107], v[224:239]\n"
    "ds_write_b16_d16_hi v47, v178 offset:2080\n"
    "v_pk_mul_f32 v[180:181], v[180:181], v[80:81]\n"
    "v_cvt_pk_f16_f32 v180, v180, v181\n"
    "ds_write_b16 v48, v180 offset:2080\n"
    "ds_write_b16_d16_hi v49, v180 offset:2080\n"
    "v_pk_mul_f32 v[182:183], v[182:183], v[82:83]\n"
    "v_cvt_pk_f16_f32 v182, v182, v183\n"
    "v_mfma_f32_32x32x16_bf16 v[224:239], v[108:111], v[132:135], v[224:239]\n"
    "ds_write_b16 v50, v182 offset:2080\n"
    "ds_write_b16_d16_hi v51, v182 offset:2080\n"
    "v_pk_mul_f32 v[184:185], v[184:185], v[84:85]\n"
    "v_cvt_pk_f16_f32 v184, v184, v185\n"
    "ds_write_b16 v52, v184 offset:2080\n"
    "ds_write_b16_d16_hi v53, v184 offset:2080\n"
    "v_pk_mul_f32 v[186:187], v[186:187], v[86:87]\n"
    "v_mfma_f32_32x32x16_bf16 v[224:239], v[132:135], v[108:111], v[224:239]\n"
    "v_cvt_pk_f16_f32 v186, v186, v187\n"
    "ds_write_b16 v54, v186 offset:2080\n"
    "ds_write_b16_d16_hi v55, v186 offset:2080\n"
    "v_pk_mul_f32 v[188:189], v[188:189], v[88:89]\n"
    "v_cvt_pk_f16_f32 v188, v188, v189\n"
    "ds_write_b16 v58, v188 offset:2080\n"
    "ds_write_b16_d16_hi v57, v188 offset:2080\n"
    "s_cmp_eq_u32 s24, 0\n"
    "s_cbranch_scc1 .Lxd_on30\n"
    ".Lxd_on30r:\n"
    "s_cmp_eq_u32 s24, 1\n"
    "s_cbranch_scc1 .Lxd_on31\n"
    ".Lxd_on31r:\n"
    "s_add_u32 s40, s20, 0\n"
    "v_readlane_b32 s41, v95, s40\n"
    "s_nop 3\n"
    "v_writelane_b32 v204, s41, 60\n"
    "v_max3_f32 v96, |v192|, |v193|, 0\n"
    "v_max3_f32 v96, |v194|, |v195|, v96\n"
    "v_max3_f32 v96, |v196|, |v197|, v96\n"
    "v_max3_f32 v96, |v198|, |v199|, v96\n"
    "v_max3_f32 v96, |v200|, |v201|, v96\n"
    "v_max3_f32 v96, |v202|, |v203|, v96\n"
    "v_max3_f32 v96, |v204|, |v205|, v96\n"
    "s_add_u32 s40, s20, 8\n"
    "v_readlane_b32 s41, v95, s40\n"
    "s_nop 3\n"
    "v_writelane_b32 v220, s41, 60\n"
    "v_max3_f32 v96, |v208|, |v209|, v96\n"
    "v_max3_f32 v96, |v210|, |v211|, v96\n"
    "v_max3_f32 v96, |v212|, |v213|, v96\n"
    "v_max3_f32 v96, |v214|, |v215|, v96\n"
    "v_max3_f32 v96, |v216|, |v217|, v96\n"
    "v_max3_f32 v96, |v218|, |v219|, v96\n"
    "v_max3_f32 v96, |v220|, |v221|, v96\n"
    "s_add_u32 s40, s20, 16\n"
    "v_readlane_b32 s41, v95, s40\n"
    "s_nop 3\n"
    "v_writelane_b32 v236, s41, 60\n"
    "v_max3_f32 v96, |v224|, |v225|, v96\n"
    "v_max3_f32 v96, |v226|, |v227|, v96\n"
    "v_max3_f32 v96, |v228|, |v229|, v96\n"
    "v_max3_f32 v96, |v230|, |v231|, v96\n"
    "v_max3_f32 v96, |v232|, |v233|, v96\n"
    "v_max3_f32 v96, |v234|, |v235|, v96\n"
    "v_max3_f32 v96, |v236|, |v237|, v96\n"
    "s_cmp_eq_u32 s24, 1\n"
    "s_cbranch_scc1 .Lxd_on32\n"
    ".Lxd_on32r:\n"
    "s_nop 1\n"
    "v_max_u32_dpp v96, v96, v96 quad_perm:[1,0,3,2] row_mask:0xf bank_mask:0xf\n"
    "s_nop 1\n"
    "v_max_u32_dpp v96, v96, v96 quad_perm:[2,3,0,1] row_mask:0xf bank_mask:0xf\n"
    "s_nop 1\n"
    "v_max_u32_dpp v96, v96, v96 row_half_mirror row_mask:0xf bank_mask:0xf\n"
    "s_nop 1\n"
    "v_max_u32_dpp v96, v96, v96 row_mirror row_mask:0xf bank_mask:0xf\n"
    "s_nop 1\n"
    "v_max_u32_dpp v96, v96, v96 row_bcast:15 row_mask:0xa bank_mask:0xf\n"
    "s_nop 1\n"
    "v_max_u32_dpp v96, v96, v96 row_bcast:31 row_mask:0xc bank_mask:0xf\n"
    "v_mov_b32 v97, 39920\n"
    "s_brev_b64 exec, 1\n"
    "ds_max_u32 v97, v96 offset:4\n"
    "s_mov_b64 exec, -1\n"
    "s_waitcnt vmcnt(0)\n"
    "s_cmp_eq_u32 s20, 7\n"
    "s_cbranch_scc1 .Lxd_on34\n"
    ".Lxd_on34r:\n"
    "s_movk_i32 s43, 15744\n"
    "v_pk_maximum3_f16 v21, v28, v28, s43 op_sel_hi:[1,1,0] neg_lo:[0,1,0] neg_hi:[0,1,0]\n"
    "v_pk_maximum3_f16 v21, v29, v29, v21 neg_lo:[0,1,0] neg_hi:[0,1,0]\n"
    "v_pk_maximum3_f16 v21, v30, v30, v21 neg_lo:[0,1,0] neg_hi:[0,1,0]\n"
    "v_pk_maximum3_f16 v21, v31, v31, v21 neg_lo:[0,1,0] neg_hi:[0,1,0]\n"
    "v_pk_maximum3_f16 v21, v32, v32, v21 neg_lo:[0,1,0] neg_hi:[0,1,0]\n"
    "v_pk_maximum3_f16 v21, v33, v33, v21 neg_lo:[0,1,0] neg_hi:[0,1,0]\n"
    "v_pk_maximum3_f16 v21, v34, v34, v21 neg_lo:[0,1,0] neg_hi:[0,1,0]\n"
    "v_pk_maximum3_f16 v21, v35, v35, v21 neg_lo:[0,1,0] neg_hi:[0,1,0]\n"
    "v_pk_maximum3_f16 v21, v36, v36, v21 neg_lo:[0,1,0] neg_hi:[0,1,0]\n"
    "v_pk_maximum3_f16 v21, v37, v37, v21 neg_lo:[0,1,0] neg_hi:[0,1,0]\n"
    "v_pk_maximum3_f16 v21, v38, v38, v21 neg_lo:[0,1,0] neg_hi:[0,1,0]\n"
    "v_pk_maximum3_f16 v21, v39, v39, v21 neg_lo:[0,1,0] neg_hi:[0,1,0]\n"
    "v_pk_maximum3_f16 v21, v40, v40, v21 neg_lo:[0,1,0] neg_hi:[0,1,0]\n"
    "v_pk_max_u16 v21, v21, v21 op_sel:[0,1] op_sel_hi:[1,0]\n"
    "s_nop 1\n"
    "v_max_u32_dpp v21, v21, v21 quad_perm:[1,0,3,2] row_mask:0xf bank_mask:0xf\n"
    "s_nop 1\n"
    "v_max_u32_dpp v21, v21, v21 quad_perm:[2,3,0,1] row_mask:0xf bank_mask:0xf\n"
    "s_nop 1\n"
    "v_max_u32_dpp v21, v21, v21 row_half_mirror row_mask:0xf bank_mask:0xf\n"
    "s_nop 1\n"
    "v_max_u32_dpp v21, v21, v21 row_mirror row_mask:0xf bank_mask:0xf\n"
    "s_nop 1\n"
    "v_max_u32_dpp v21, v21, v21 row_bcast:15 row_mask:0xa bank_mask:0xf\n"
    "s_nop 1\n"
    "v_max_u32_dpp v21, v21, v21 row_bcast:31 row_mask:0xc bank_mask:0xf\n"
    "s_nop 1\n"
    "v_readlane_b32 s47, v21, 63\n"
    "s_and_b32 s50, s47, 0x7f80\n"
    "s_lshr_b32 s47, s50, 6\n"
    "s_sub_u32 s50, s50, 2816\n"
    "s_mul_i32 s51, s50, -8\n"
    "v_pk_maximum3_f16 v21, v28, v28, s50 op_sel_hi:[1,1,0] neg_lo:[0,1,0] neg_hi:[0,1,0]\n"
    "v_pk_mad_u16 v21, v21, 8, s51 op_sel_hi:[1,0,0]\n"
    "v_bfi_b32 v28, s57, v21, v28\n"
    "v_pk_maximum3_f16 v12, v29, v29, s50 op_sel_hi:[1,1,0] neg_lo:[0,1,0] neg_hi:[0,1,0]\n"
    "v_pk_mad_u16 v12, v12, 8, s51 op_sel_hi:[1,0,0]\n"
    "v_bfi_b32 v29, s57, v12, v29\n"
    "v_pk_maximum3_f16 v21, v30, v30, s50 op_sel_hi:[1,1,0] neg_lo:[0,1,0] neg_hi:[0,1,0]\n"
    "v_pk_mad_u16 v21, v21, 8, s51 op_sel_hi:[1,0,0]\n"
    "v_bfi_b32 v30, s57, v21, v30\n"
    "v_pk_maximum3_f16 v12, v31, v31, s50 op_sel_hi:[1,1,0] neg_lo:[0,1,0] neg_hi:[0,1,0]\n"
    "v_pk_mad_u16 v12, v12, 8, s51 op_sel_hi:[1,0,0]\n"
    "v_bfi_b32 v31, s57, v12, v31\n"
    "v_pk_maximum3_f16 v21, v32, v32, s50 op_sel_hi:[1,1,0] neg_lo:[0,1,0] neg_hi:[0,1,0]\n"
    "v_pk_mad_u16 v21, v21, 8, s51 op_sel_hi:[1,0,0]\n"
    "v_bfi_b32 v32, s57, v21, v32\n"
    "v_pk_maximum3_f16 v12, v33, v33, s50 op_sel_hi:[1,1,0] neg_lo:[0,1,0] neg_hi:[0,1,0]\n"
    "v_pk_mad_u16 v12, v12, 8, s51 op_sel_hi:[1,0,0]\n"
    "v_bfi_b32 v33, s57, v12, v33\n"
    "v_pk_maximum3_f16 v21, v34, v34, s50 op_sel_hi:[1,1,0] neg_lo:[0,1,0] neg_hi:[0,1,0]\n"
    "v_pk_mad_u16 v21, v21, 8, s51 op_sel_hi:[1,0,0]\n"
    "v_bfi_b32 v34, s57, v21, v34\n"
    "v_pk_maximum3_f16 v12, v35, v35, s50 op_sel_hi:[1,1,0] neg_lo:[0,1,0] neg_hi:[0,1,0]\n"
    "v_pk_mad_u16 v12, v12, 8, s51 op_sel_hi:[1,0,0]\n"
    "v_bfi_b32 v35, s57, v12, v35\n"
    "v_pk_maximum3_f16 v21, v36, v36, s50 op_sel_hi:[1,1,0] neg_lo:[0,1,0] neg_hi:[0,1,0]\n"
    "v_pk_mad_u16 v21, v21, 8, s51 op_sel_hi:[1,0,0]\n"
    "v_bfi_b32 v36, s57, v21, v36\n"
    "v_pk_maximum3_f16 v12, v37, v37, s50 op_sel_hi:[1,1,0] neg_lo:[0,1,0] neg_hi:[0,1,0]\n"
    "v_pk_mad_u16 v12, v12, 8, s51 op_sel_hi:[1,0,0]\n"
    "v_bfi_b32 v37, s57, v12, v37\n"
    "v_pk_maximum3_f16 v21, v38, v38, s50 op_sel_hi:[1,1,0] neg_lo:[0,1,0] neg_hi:[0,1,0]\n"
    "v_pk_mad_u16 v21, v21, 8, s51 op_sel_hi:[1,0,0]\n"
    "v_bfi_b32 v38, s57, v21, v38\n"
    "v_pk_maximum3_f16 v12, v39, v39, s50 op_sel_hi:[1,1,0] neg_lo:[0,1,0] neg_hi:[0,1,0]\n"
    "v_pk_mad_u16 v12, v12, 8, s51 op_sel_hi:[1,0,0]\n"
    "v_bfi_b32 v39, s57, v12, v39\n"
    "v_pk_maximum3_f16 v21, v40, v40, s50 op_sel_hi:[1,1,0] neg_lo:[0,1,0] neg_hi:[0,1,0]\n"
    "v_pk_mad_u16 v21, v21, 8, s51 op_sel_hi:[1,0,0]\n"
    "v_bfi_b32 v40, s57, v21, v40\n"
    "s_waitcnt lgkmcnt(0)\n"
    "s_barrier\n"
    "ds_read_b32 v99, v97 offset:4\n"
    "ds_read_b32 v41, v25 offset:24\n"
    "s_sub_u32 s43, 514, s47\n"
    "s_lshl_b32 s43, s43, 22\n"
    "v_cvt_pkrtz_f16_f32 v12, s43, s43\n"
    "v_lshl_or_b32 v21, v12, 17, s47\n"
    "s_waitcnt lgkmcnt(0)\n"
    "v_alignbit_b32 v21, v41, v21, 1\n"
    "s_mov_b64 exec, s[48:49]\n"
    "ds_write_b128 v15, v[28:31] offset:0\n"
    "ds_write_b128 v15, v[32:35] offset:160\n"
    "ds_write_b128 v15, v[36:39] offset:320\n"
    "ds_write_b32 v16, v40 offset:3840\n"
    "ds_write_b32 v16, v12 offset:4160\n"
    "ds_write_b32 v25, v21 offset:532\n"
    "s_mov_b64 exec, -1\n"
    "v_lshrrev_b32 v99, 23, v99\n"
    "v_max_u32 v99, 40, v99\n"
    "v_min_u32 v99, 250, v99\n"
    "v_sub_u32 v96, v98, v99\n"
    "v_ldexp_f32 v76, v76, v96\n"
    "v_ldexp_f32 v77, v77, v96\n"
    "v_ldexp_f32 v78, v78, v96\n"
    "v_ldexp_f32 v79, v79, v96\n"
    "v_ldexp_f32 v80, v80, v96\n"
    "v_ldexp_f32 v81, v81, v96\n"
    "v_ldexp_f32 v82, v82, v96\n"
    "v_ldexp_f32 v83, v83, v96\n"
    "v_ldexp_f32 v84, v84, v96\n"
    "v_ldexp_f32 v85, v85, v96\n"
    "v_ldexp_f32 v86, v86, v96\n"
    "v_ldexp_f32 v87, v87, v96\n"
    "v_ldexp_f32 v88, v88, v96\n"
    "v_ldexp_f32 v89, v89, v96\n"
    "v_pk_mul_f32 v[192:193], v[192:193], v[76:77]\n"
    "v_cvt_pk_f16_f32 v192, v192, v193\n"
    "ds_write_b16 v44, v192 offset:1040\n"
    "ds_write_b16_d16_hi v45, v192 offset:1040\n"
    "v_pk_mul_f32 v[194:195], v[194:195], v[78:79]\n"
    "v_cvt_pk_f16_f32 v194, v194, v195\n"
    "ds_write_b16 v46, v194 offset:1040\n"
    "ds_write_b16_d16_hi v47, v194 offset:1040\n"
    "v_pk_mul_f32 v[196:197], v[196:197], v[80:81]\n"
    "v_cvt_pk_f16_f32 v196, v196, v197\n"
    "ds_write_b16 v48, v196 offset:1040\n"
    "ds_write_b16_d16_hi v49, v196 offset:1040\n"
    "v_pk_mul_f32 v[198:199], v[198:199], v[82:83]\n"
    "v_cvt_pk_f16_f32 v198, v198, v199\n"
    "ds_write_b16 v50, v198 offset:1040\n"
    "ds_write_b16_d16_hi v51, v198 offset:1040\n"
    "v_pk_mul_f32 v[200:201], v[200:201], v[84:85]\n"
    "v_cvt_pk_f16_f32 v200, v200, v201\n"
    "ds_write_b16 v52, v200 offset:1040\n"
    "ds_write_b16_d16_hi v53, v200 offset:1040\n"
    "v_pk_mul_f32 v[202:203], v[202:203], v[86:87]\n"
    "v_cvt_pk_f16_f32 v202, v202, v203\n"
    "ds_write_b16 v54, v202 offset:1040\n"
    "ds_write_b16_d16_hi v55, v202 offset:1040\n"
    "v_pk_mul_f32 v[204:205], v[204:205], v[88:89]\n"
    "v_cvt_pk_f16_f32 v204, v204, v205\n"
    "ds_write_b16 v56, v204 offset:1040\n"
    "ds_write_b16_d16_hi v57, v204 offset:1040\n"
    "v_pk_mul_f32 v[208:209], v[208:209], v[76:77]\n"
    "v_cvt_pk_f16_f32 v208, v208, v209\n"
    "ds_write_b16 v44, v208 offset:1056\n"
    "ds_write_b16_d16_hi v45, v208 offset:1056\n"
    "v_pk_mul_f32 v[210:211], v[210:211], v[78:79]\n"
    "v_cvt_pk_f16_f32 v210, v210, v211\n"
    "ds_write_b16 v46, v210 offset:1056\n"
    "ds_write_b16_d16_hi v47, v210 offset:1056\n"
    "v_pk_mul_f32 v[212:213], v[212:213], v[80:81]\n"
    "v_cvt_pk_f16_f32 v212, v212, v213\n"
    "ds_write_b16 v48, v212 offset:1056\n"
    "ds_write_b16_d16_hi v49, v212 offset:1056\n"
    "v_pk_mul_f32 v[214:215], v[214:215], v[82:83]\n"
    "v_cvt_pk_f16_f32 v214, v214, v215\n"
    "ds_write_b16 v50, v214 offset:1056\n"
    "ds_write_b16_d16_hi v51, v214 offset:1056\n"
    "v_pk_mul_f32 v[216:217], v[216:217], v[84:85]\n"
    "v_cvt_pk_f16_f32 v216, v216, v217\n"
    "ds_write_b16 v52, v216 offset:1056\n"
    "ds_write_b16_d16_hi v53, v216 offset:1056\n"
    "v_pk_mul_f32 v[218:219], v[218:219], v[86:87]\n"
    "v_cvt_pk_f16_f32 v218, v218, v219\n"
    "ds_write_b16 v54, v218 offset:1056\n"
    "ds_write_b16_d16_hi v55, v218 offset:1056\n"
    "v_pk_mul_f32 v[220:221], v[220:221], v[88:89]\n"
    "v_cvt_pk_f16_f32 v220, v220, v221\n"
    "ds_write_b16 v56, v220 offset:1056\n"
    "ds_write_b16_d16_hi v57, v220 offset:1056\n"
    "v_pk_mul_f32 v[224:225], v[224:225], v[76:77]\n"
    "v_cvt_pk_f16_f32 v224, v224, v225\n"
    "ds_write_b16 v44, v224 offset:2096\n"
    "ds_write_b16_d16_hi v45, v224 offset:2096\n"
    "v_pk_mul_f32 v[226:227], v[226:227], v[78:79]\n"
    "v_cvt_pk_f16_f32 v226, v226, v227\n"
    "ds_write_b16 v46, v226 offset:2096\n"
    "ds_write_b16_d16_hi v47, v226 offset:2096\n"
    "v_pk_mul_f32 v[228:229], v[228:229], v[80:81]\n"
    "v_cvt_pk_f16_f32 v228, v228, v229\n"
    "ds_write_b16 v48, v228 offset:2096\n"
    "ds_write_b16_d16_hi v49, v228 offset:2096\n"
    "v_pk_mul_f32 v[230:231], v[230:231], v[82:83]\n"
    "v_cvt_pk_f16_f32 v230, v230, v231\n"
    "ds_write_b16 v50, v230 offset:2096\n"
    "ds_write_b16_d16_hi v51, v230 offset:2096\n"
    "v_pk_mul_f32 v[232:233], v[232:233], v[84:85]\n"
    "v_cvt_pk_f16_f32 v232, v232, v233\n"
    "ds_write_b16 v52, v232 offset:2096\n"
    "ds_write_b16_d16_hi v53, v232 offset:2096\n"
    "v_pk_mul_f32 v[234:235], v[234:235], v[86:87]\n"
    "v_cvt_pk_f16_f32 v234, v234, v235\n"
    "ds_write_b16 v54, v234 offset:2096\n"
    "ds_write_b16_d16_hi v55, v234 offset:2096\n"
    "v_pk_mul_f32 v[236:237], v[236:237], v[88:89]\n"
    "v_cvt_pk_f16_f32 v236, v236, v237\n"
    "ds_write_b16 v56, v236 offset:2096\n"
    "ds_write_b16_d16_hi v57, v236 offset:2096\n"
    "s_cmp_eq_u32 s24, 1\n"
    "s_cbranch_scc1 .Lxd_on35\n"
    ".Lxd_on35r:\n"
    "v_add_u32 v98, v98, v99\n"
    "v_mov_b32 v97, 140944\n"
    "v_subrev_u32 v98, 818, v98\n"
    "ds_write_b32 v97, v98\n"
    "s_cmp_lt_u32 s22, 2\n"
    "s_cselect_b32 s40, 16, 0\n"
    "v_add_u32 v25, s40, v25\n"
    "s_mov_b64 s[48:49], 0\n"
    "s_add_u32 s23, s23, 4\n"
    "v_mov_b32 v24, s23\n"
    "v_add_u32 v42, 98304, v42\n"
    "v_mov_b32 v13, s58\n"
    "s_cmp_eq_u32 s22, 0\n"
    "s_cbranch_scc1 .Lxd_on36\n"
    ".Lxd_on36r:\n"
    "s_waitcnt lgkmcnt(0)\n"
    "s_barrier\n"
    "s_cmp_eq_u32 s2, 0\n"
    "s_cbranch_scc1 .Lxd_on37\n"
    ".Lxd_on37r:\n"
    "ds_read_b32 v22, v13 offset:512\n"
    "s_sub_u32 s40, 1, s22\n"
    "s_and_b32 s40, s40, 3\n"
    "s_mul_i32 s40, s40, 4480\n"
    "v_add_u32 v7, s40, v3\n"
    "v_add_u32 v8, s40, v4\n"
    "v_add_u32 v9, s40, v5\n"
    "v_add_u32 v10, s40, v6\n"
    "ds_read_b64_tr_b16 v[44:45], v9 offset:0\n"
    "ds_read_b64_tr_b16 v[46:47], v10 offset:0\n"
    "ds_read_b64_tr_b16 v[48:49], v7 offset:0\n"
    "ds_read_b64_tr_b16 v[50:51], v8 offset:640\n"
    "ds_read_b64_tr_b16 v[52:53], v8 offset:1280\n"
    "ds_read_b64_tr_b16 v[54:55], v8 offset:1920\n"
    "ds_read_b64_tr_b16 v[56:57], v8 offset:2560\n"
    "ds_read_b64_tr_b16 v[58:59], v8 offset:3200\n"
    "ds_read_b128 v[92:95], v42 offset:0\n"
    "ds_read_b128 v[96:99], v42 offset:1040\n"
    "ds_read_b128 v[100:103], v42 offset:2080\n"
    "ds_read_b128 v[104:107], v42 offset:3120\n"
    "ds_read_b128 v[108:111], v42 offset:4160\n"
    "ds_read_b128 v[112:115], v42 offset:5200\n"
    "ds_read_b128 v[116:119], v42 offset:6240\n"
    "ds_read_b128 v[120:123], v42 offset:7280\n"
    "ds_read_b128 v[124:127], v42 offset:8320\n"
    "ds_read_b128 v[128:131], v42 offset:9360\n"
    "ds_read_b128 v[132:135], v42 offset:10400\n"
    "ds_read_b128 v[136:139], v42 offset:11440\n"
    "ds_read_b128 v[140:143], v42 offset:12480\n"
    "ds_read_b128 v[144:147], v42 offset:13520\n"
    "ds_read_b128 v[148:151], v42 offset:14560\n"
    "ds_read_b128 v[152:155], v42 offset:15600\n"
    "ds_read_b128 v[156:159], v42 offset:16640\n"
    "ds_read_b128 v[160:163], v42 offset:17680\n"
    "ds_read_b128 v[164:167], v42 offset:18720\n"
    "ds_read_b128 v[168:171], v42 offset:19760\n"
    "ds_read_b128 v[172:175], v42 offset:20800\n"
    "ds_read_b128 v[176:179], v42 offset:21840\n"
    "ds_read_b128 v[180:183], v42 offset:22880\n"
    "ds_read_b128 v[184:187], v42 offset:23920\n"
    "ds_read_b128 v[188:191], v42 offset:24960\n"
    "ds_read_b128 v[192:195], v42 offset:26000\n"
    "ds_read_b128 v[196:199], v42 offset:27040\n"
    "ds_read_b128 v[200:203], v42 offset:28080\n"
    "ds_read_b128 v[204:207], v42 offset:29120\n"
    "ds_read_b128 v[208:211], v42 offset:30160\n"
    "ds_read_b128 v[212:215], v42 offset:31200\n"
    "ds_read_b128 v[216:219], v42 offset:32240\n"
    "ds_read_b128 v[220:223], v42 offset:33280\n"
    "ds_read_b128 v[224:227], v42 offset:34320\n"
    "ds_read_b128 v[228:231], v42 offset:35360\n"
    "ds_read_b128 v[232:235], v42 offset:36400\n"
    "ds_read_b128 v[236:239], v42 offset:37440\n"
    "ds_read_b128 v[240:243], v42 offset:38480\n"
    "ds_read_b128 v[244:247], v42 offset:39520\n"
    "ds_read_b128 v[248:251], v42 offset:40560\n"
    "ds_read_b128 v[252:255], v42 offset:41600\n"
    "v_mov_b64 v[88:89], 0\n"
    "v_mov_b64 v[90:91], 0\n"
    "s_waitcnt lgkmcnt(15)\n"
    "v_readfirstlane_b32 s45, v22\n"
    "v_pk_mul_f16 v64, v44, v46\n"
    "v_pk_mul_f16 v65, v45, v47\n"
    "v_pk_mul_f16 v66, v48, v48 op_sel_hi:[0,1]\n"
    "v_pk_mul_f16 v67, v48, v49 op_sel_hi:[0,1]\n"
    "v_pk_mul_f16 v60, v44, s45 op_sel:[0,1]\n"
    "v_pk_mul_f16 v61, v45, s45 op_sel:[0,1]\n"
    "v_pk_mul_f16 v62, v46, s45 op_sel:[0,1]\n"
    "v_pk_mul_f16 v63, v47, s45 op_sel:[0,1]\n"
    "s_waitcnt lgkmcnt(0)\n"
    "s_branch .Lxd_enter\n"
    ".Lxd_slow:\n"
    "s_mov_b32 s59, 0x100000\n"
    "s_cmp_lg_u32 s45, 0\n"
    "s_cbranch_scc1 .Lxd_sl2\n"
    ".Lxd_sl1:\n"
    "s_sleep 1\n"
    "ds_read_b32 v22, v13 offset:512\n"
    "s_waitcnt lgkmcnt(0)\n"
    "v_readfirstlane_b32 s45, v22\n"
    "s_sub_u32 s59, s59, 1\n"
    "s_cbranch_scc1 .Lxd_sl5\n"
    "s_cmp_eq_u32 s45, 0\n"
    "s_cbranch_scc1 .Lxd_sl1\n"
    "s_cmp_gt_i32 s45, 0\n"
    "s_cbranch_scc1 .Lxd_sl9\n"
    ".Lxd_sl2:\n"
    "s_cmp_eq_u32 s45, -1\n"
    "s_cbranch_scc1 .Lxd_sl3\n"
    "s_bitset0_b32 s45, 31\n"
    "s_branch .Lxd_sl6\n"
    ".Lxd_sl3:\n"
    "v_lshl_add_u32 v13, v1, 2, v13\n"
    "s_mov_b64 exec, 7\n"
    ".Lxd_sl4:\n"
    "ds_read_b32 v22, v13 offset:4\n"
    "s_waitcnt lgkmcnt(0)\n"
    "v_cmp_eq_u32 s[40:41], 0, v22\n"
    "v_cmp_ne_u32 s[42:43], -1, v22\n"
    "s_sub_u32 s59, s59, 1\n"
    "s_cbranch_scc1 .Lxd_sl5\n"
    "s_cmp_lg_u32 s40, 0\n"
    "s_cbranch_scc1 .Lxd_sl4\n"
    "s_cmp_eq_u32 s42, 0\n"
    "s_cbranch_scc1 .Lxd_sl5\n"
    "s_mov_b64 exec, -1\n"
    "s_mov_b32 s45, 1\n"
    "s_mov_b32 s56, 0\n"
    "s_setpc_b64 s[62:63]\n"
    ".Lxd_sl5:\n"
    "s_mov_b64 exec, -1\n"
    "s_mov_b32 s45, -1\n"
    ".Lxd_sl6:\n"
    "s_cmp_lg_u32 s56, 0\n"
    "s_cbranch_scc1 .Lxd_sl9\n"
    "s_call_b64 s[60:61], .Lxd_gran\n"
    ".Lxd_sl9:\n"
    "s_setpc_b64 s[62:63]\n"
    ".Lxd_gran:\n"
    "s_mov_b32 s56, 1\n"
    "s_lshl4_add_u32 s40, s22, s58\n"
    "v_lshrrev_b32 v42, 4, v1\n"
    "v_lshl_add_u32 v42, v42, 2, s40\n"
    "ds_read2_b32 v[26:27], v42 offset1:16\n"
    "v_and_b32 v42, 15, v1\n"
    "v_min_u32 v42, 12, v42\n"
    "v_lshlrev_b32 v42, 15, v42\n"
    "s_add_u32 s40, s10, 65528\n"
    "s_addc_u32 s41, s11, 0\n"
    "s_waitcnt lgkmcnt(0)\n"
    "v_max_i32 v26, 2, v26\n"
    "v_max_i32 v27, 2, v27\n"
    "v_lshl_add_u32 v43, v26, 2, v42\n"
    "v_lshl_add_u32 v26, v27, 2, v42\n"
    "global_load_dwordx2 v[42:43], v43, s[40:41] sc1\n"
    "global_load_dwordx2 v[26:27], v26, s[40:41] sc1\n"
    "s_setpc_b64 s[60:61]\n"
    ".Lxd_on1:\n"
    "s_and_b32 s40, s3, 0xf8\n"
    "s_mul_i32 s40, s40, 11\n"
    "s_lshr_b32 s40, s40, 3\n"
    "v_min_u32 v7, 10, v1\n"
    "v_add_lshl_u32 v7, v7, s40, 7\n"
    "s_getpc_b64 s[60:61]\n"
    ".Lxd_pc0:\n"
    "s_sub_u32 s60, s60, .Lxd_pc0-.Lxd_g0\n"
    "s_subb_u32 s61, s61, 0\n"
    "v_min_u32 v7, .Lxd_end-.Lxd_g0-4, v7\n"
    "global_load_dword v7, v7, s[60:61]\n"
    "s_branch .Lxd_on1r\n"
    ".Lxd_on2:\n"
    "s_lshl_b32 s40, s2, 3\n"
    "s_add_u32 s40, s40, 4096\n"
    "v_mov_b32 v13, 40960\n"
    "v_writelane_b32 v13, s40, 0\n"
    "s_mov_b64 exec, 3\n"
    "global_load_dwordx2 v[26:27], v13, s[10:11] sc1\n"
    "s_mov_b64 exec, -1\n"
    "s_branch .Lxd_on2r\n"
    ".Lxd_on4:\n"
    "s_mov_b32 s59, 0x20000\n"
    ".Lxd_pv3:\n"
    "v_readlane_b32 s40, v26, 0\n"
    "v_readlane_b32 s41, v27, 0\n"
    "s_mul_i32 s42, s25, 0x9e3779b1\n"
    "s_lshr_b32 s42, s42, 24\n"
    "s_xor_b32 s42, s42, s40\n"
    "s_and_b32 s42, s42, 0xff\n"
    "s_xor_b32 s41, s41, s25\n"
    "s_xor_b32 s41, s41, 0xa5c30f96\n"
    "s_or_b32 s42, s41, s42\n"
    "s_lshr_b32 s41, s40, 8\n"
    "s_mul_i32 s40, s41, 0x9e3779b1\n"
    "s_lshr_b32 s40, s40, 24\n"
    "s_xor_b32 s41, s41, 0xa5c30f96\n"
    "v_readlane_b32 s66, v26, 1\n"
    "v_readlane_b32 s67, v27, 1\n"
    "s_cmp_eq_u64 s[66:67], s[40:41]\n"
    "s_cselect_b32 s42, s42, 1\n"
    "s_cmp_eq_u32 s42, 0\n"
    "s_cbranch_scc1 .Lxd_pv3k\n"
    "s_sleep 2\n"
    "s_lshl_b32 s40, s2, 3\n"
    "s_add_u32 s40, s40, 4096\n"
    "v_mov_b32 v13, 40960\n"
    "v_writelane_b32 v13, s40, 0\n"
    "s_mov_b64 exec, 3\n"
    "global_load_dwordx2 v[26:27], v13, s[10:11] sc1\n"
    "s_mov_b64 exec, -1\n"
    "s_waitcnt vmcnt(0)\n"
    "s_sub_u32 s59, s59, 1\n"
    "s_cbranch_scc0 .Lxd_pv3\n"
    ".Lxd_pv3k:\n"
    "v_mov_b32 v26, s40\n"
    "v_mov_b32 v27, s41\n"
    "v_mov_b32 v13, 154112\n"
    "ds_write_b64 v13, v[26:27]\n"
    "s_add_u32 s40, s25, 1\n"
    "s_lshl_b32 s41, s2, 2\n"
    "v_mov_b32 v43, s40\n"
    "v_mov_b32 v13, s41\n"
    "s_mov_b64 exec, 1\n"
    "global_store_dword v13, v43, s[10:11] sc1\n"
    "s_mov_b64 exec, -1\n"
    "s_branch .Lxd_on4r\n"
    ".Lxd_on5:\n"
    "v_mov_b32 v24, 1\n"
    "s_mov_b64 exec, 1\n"
    "global_atomic_add v24, v17, v24, s[52:53] sc0 sc1\n"
    "s_mov_b64 exec, -1\n"
    "s_branch .Lxd_on5r\n"
    ".Lxd_on6:\n"
    "s_waitcnt vmcnt(0)\n"
    "v_readfirstlane_b32 s40, v24\n"
    "s_or_b32 s40, s40, s44\n"
    "s_lshl1_add_u32 s41, s40, s54\n"
    "s_cmp_lt_u32 s40, 256\n"
    "s_cselect_b32 s40, s41, -1\n"
    "s_cselect_b32 s41, s41, 2\n"
    "s_cselect_b64 s[48:49], -1, 0\n"
    "v_mov_b32 v12, s40\n"
    "s_mul_i32 s41, s41, 52\n"
    "s_add_u32 s38, s4, s41\n"
    "s_addc_u32 s39, s5, 0\n"
    "v_ashrrev_i32 v21, 31, v12\n"
    "ds_write2_b32 v25, v12, v21 offset0:5 offset1:133\n"
    "global_load_dwordx3 v[36:38], v17, s[38:39] offset:-104\n"
    "global_load_dword v41, v18, s[38:39] offset:-8\n"
    "v_mov_b32 v12, 0xc0135761\n"
    "s_branch .Lxd_on6r\n"
    ".Lxd_on7:\n"
    "v_mov_b32 v24, 1\n"
    "s_mov_b64 exec, 1\n"
    "global_atomic_add v24, v17, v24, s[52:53] sc0 sc1\n"
    "s_mov_b64 exec, -1\n"
    "s_branch .Lxd_on7r\n"
    ".Lxd_ck8:\n"
    "v_mov_b32 v13, s58\n"
    "s_call_b64 s[62:63], .Lxd_slow\n"
    "s_branch .Lxd_ck8r\n"
    ".Lxd_on9:\n"
    "s_waitcnt vmcnt(0)\n"
    "v_lshl_or_b32 v36, v36, 7, v19\n"
    "v_lshl_or_b32 v37, v37, 7, v19\n"
    "v_lshl_or_b32 v38, v38, 7, v19\n"
    "v_lshl_or_b32 v41, v41, 7, v20\n"
    "global_load_dwordx4 v[28:31], v36, s[8:9]\n"
    "global_load_dwordx4 v[32:35], v37, s[8:9]\n"
    "global_load_dword v40, v41, s[8:9]\n"
    "global_load_dwordx4 v[36:39], v38, s[8:9]\n"
    "s_branch .Lxd_on9r\n"
    ".Lxd_on10:\n"
    "s_waitcnt vmcnt(0)\n"
    "v_readfirstlane_b32 s40, v24\n"
    "s_or_b32 s40, s40, s44\n"
    "s_lshl1_add_u32 s41, s40, s54\n"
    "s_cmp_lt_u32 s40, 256\n"
    "s_cselect_b32 s40, s41, -1\n"
    "s_cselect_b32 s41, s41, 2\n"
    "s_cselect_b64 s[48:49], -1, 0\n"
    "v_mov_b32 v12, s40\n"
    "s_mul_i32 s41, s41, 52\n"
    "s_add_u32 s38, s4, s41\n"
    "s_addc_u32 s39, s5, 0\n"
    "v_ashrrev_i32 v21, 31, v12\n"
    "ds_write2_b32 v25, v12, v21 offset0:5 offset1:133\n"
    "v_sub_u32 v15, v0, v15\n"
    "v_sub_u32 v16, v23, v16\n"
    "global_load_dwordx3 v[36:38], v17, s[38:39] offset:-104\n"
    "global_load_dword v41, v18, s[38:39] offset:-8\n"
    "v_mov_b32 v12, 0xc0135761\n"
    "s_branch .Lxd_on10r\n"
    ".Lxd_on11:\n"
    "v_mov_b32 v24, 1\n"
    "s_mov_b64 exec, 1\n"
    "global_atomic_add v24, v17, v24, s[52:53] sc0 sc1\n"
    "s_mov_b64 exec, -1\n"
    "s_branch .Lxd_on11r\n"
    ".Lxd_ck14:\n"
    "v_add_u32 v13, 4, v25\n"
    "s_call_b64 s[62:63], .Lxd_slow\n"
    "s_branch .Lxd_ck14r\n"
    ".Lxd_ck17:\n"
    "v_add_u32 v13, 8, v25\n"
    "s_call_b64 s[62:63], .Lxd_slow\n"
    "s_branch .Lxd_ck17r\n"
    ".Lxd_ck20:\n"
    "v_add_u32 v13, 12, v25\n"
    "s_call_b64 s[62:63], .Lxd_slow\n"
    "s_branch .Lxd_ck20r\n"
    ".Lxd_ck23:\n"
    "v_add_u32 v13, 16, v25\n"
    "s_call_b64 s[62:63], .Lxd_slow\n"
    "s_branch .Lxd_ck23r\n"
    ".Lxd_on24:\n"
    "v_min_u32 v12, 38, v1\n"
    "s_mov_b32 m0, 39936\n"
    "v_lshlrev_b32 v12, 2, v12\n"
    "global_load_lds_dword v12, s[30:31]\n"
    "s_branch .Lxd_on24r\n"
    ".Lxd_on25:\n"
    "v_and_b32 v12, 15, v0\n"
    "v_lshlrev_b32 v12, 7, v12\n"
    "v_add_u32 v12, 32768, v12\n"
    "v_mov_b32 v26, 114\n"
    "global_store_dword v12, v26, s[10:11] sc1\n"
    "global_load_dwordx4 v[100:103], v42, s[10:11] sc1\n"
    "s_branch .Lxd_on25r\n"
    ".Lxd_on26:\n"
    "s_mov_b32 s40, 0x9e3779b1\n"
    "v_mul_lo_u32 v28, v100, s40\n"
    "v_xor_b32 v29, 0xa5c30f96, v100\n"
    "v_alignbit_b32 v28, s25, v28, 24\n"
    "v_mul_lo_u32 v30, v101, s40\n"
    "v_xor_b32 v31, 0xa5c30f96, v101\n"
    "v_alignbit_b32 v30, s25, v30, 24\n"
    "v_mul_lo_u32 v32, v102, s40\n"
    "v_xor_b32 v33, 0xa5c30f96, v102\n"
    "v_alignbit_b32 v32, s25, v32, 24\n"
    "v_mul_lo_u32 v34, v103, s40\n"
    "v_xor_b32 v35, 0xa5c30f96, v103\n"
    "v_alignbit_b32 v34, s25, v34, 24\n"
    "v_lshlrev_b32 v100, 1, v42\n"
    "s_add_u32 s40, s10, 4096\n"
    "s_addc_u32 s41, s11, 0\n"
    "global_store_dwordx4 v100, v[28:31], s[40:41] sc1\n"
    "global_store_dwordx4 v100, v[32:35], s[40:41] offset:16 sc1\n"
    "s_branch .Lxd_on26r\n"
    ".Lxd_on27:\n"
    "v_mfma_f32_32x32x16_bf16 v[240:255], v[104:107], v[136:139], 0\n"
    "v_mfma_f32_32x32x16_bf16 v[240:255], v[136:139], v[104:107], v[240:255]\n"
    "v_mfma_f32_32x32x16_bf16 v[240:255], v[108:111], v[140:143], v[240:255]\n"
    "v_mfma_f32_32x32x16_bf16 v[240:255], v[140:143], v[108:111], v[240:255]\n"
    "s_branch .Lxd_on27r\n"
    ".Lxd_on28:\n"
    "v_max3_f32 v96, |v240|, |v241|, v96\n"
    "v_max3_f32 v96, |v242|, |v243|, v96\n"
    "v_max3_f32 v96, |v244|, |v245|, v96\n"
    "v_max3_f32 v96, |v246|, |v247|, v96\n"
    "v_max3_f32 v96, |v248|, |v249|, v96\n"
    "v_max3_f32 v96, |v250|, |v251|, v96\n"
    "v_max3_f32 v96, |v252|, |v253|, v96\n"
    "s_branch .Lxd_on28r\n"
    ".Lxd_on29:\n"
    "s_lshl_b32 s40, s2, 3\n"
    "s_add_u32 s40, s40, 4096\n"
    "v_mov_b32 v13, 40960\n"
    "v_writelane_b32 v13, s40, 0\n"
    "s_mov_b64 exec, 3\n"
    "global_load_dwordx2 v[26:27], v13, s[10:11] sc1\n"
    "s_mov_b64 exec, -1\n"
    "s_branch .Lxd_on29r\n"
    ".Lxd_on30:\n"
    "v_pk_mul_f32 v[240:241], v[240:241], v[76:77]\n"
    "v_cvt_pk_f16_f32 v240, v240, v241\n"
    "ds_write_b16 v60, v240 offset:0\n"
    "ds_write_b16_d16_hi v61, v240 offset:0\n"
    "v_pk_mul_f32 v[242:243], v[242:243], v[78:79]\n"
    "v_cvt_pk_f16_f32 v242, v242, v243\n"
    "ds_write_b16 v62, v242 offset:0\n"
    "ds_write_b16_d16_hi v63, v242 offset:0\n"
    "v_pk_mul_f32 v[244:245], v[244:245], v[80:81]\n"
    "v_cvt_pk_f16_f32 v244, v244, v245\n"
    "ds_write_b16 v64, v244 offset:0\n"
    "ds_write_b16_d16_hi v65, v244 offset:0\n"
    "v_pk_mul_f32 v[246:247], v[246:247], v[82:83]\n"
    "v_cvt_pk_f16_f32 v246, v246, v247\n"
    "ds_write_b16 v66, v246 offset:0\n"
    "ds_write_b16_d16_hi v67, v246 offset:0\n"
    "v_pk_mul_f32 v[248:249], v[248:249], v[84:85]\n"
    "v_cvt_pk_f16_f32 v248, v248, v249\n"
    "ds_write_b16 v68, v248 offset:0\n"
    "ds_write_b16_d16_hi v69, v248 offset:0\n"
    "v_pk_mul_f32 v[250:251], v[250:251], v[86:87]\n"
    "v_cvt_pk_f16_f32 v250, v250, v251\n"
    "ds_write_b16 v70, v250 offset:0\n"
    "ds_write_b16_d16_hi v71, v250 offset:0\n"
    "v_pk_mul_f32 v[252:253], v[252:253], v[88:89]\n"
    "v_cvt_pk_f16_f32 v252, v252, v253\n"
    "ds_write_b16 v74, v252 offset:0\n"
    "ds_write_b16_d16_hi v73, v252 offset:0\n"
    "s_branch .Lxd_on30r\n"
    ".Lxd_on31:\n"
    "v_mfma_f32_32x32x16_bf16 v[240:255], v[104:107], v[136:139], 0\n"
    "v_mfma_f32_32x32x16_bf16 v[240:255], v[136:139], v[104:107], v[240:255]\n"
    "v_mfma_f32_32x32x16_bf16 v[240:255], v[108:111], v[140:143], v[240:255]\n"
    "v_mfma_f32_32x32x16_bf16 v[240:255], v[140:143], v[108:111], v[240:255]\n"
    "s_branch .Lxd_on31r\n"
    ".Lxd_on32:\n"
    "v_readlane_b32 s41, v95, s34\n"
    "s_nop 3\n"
    "v_writelane_b32 v252, s41, 60\n"
    "v_max3_f32 v96, |v240|, |v241|, v96\n"
    "v_max3_f32 v96, |v242|, |v243|, v96\n"
    "v_max3_f32 v96, |v244|, |v245|, v96\n"
    "v_max3_f32 v96, |v246|, |v247|, v96\n"
    "v_max3_f32 v96, |v248|, |v249|, v96\n"
    "v_max3_f32 v96, |v250|, |v251|, v96\n"
    "v_max3_f32 v96, |v252|, |v253|, v96\n"
    "s_branch .Lxd_on32r\n"
    ".Lxd_on34:\n"
    "s_mov_b32 s59, 0x20000\n"
    ".Lxd_pv33:\n"
    "v_readlane_b32 s40, v26, 0\n"
    "v_readlane_b32 s41, v27, 0\n"
    "s_mul_i32 s42, s25, 0x9e3779b1\n"
    "s_lshr_b32 s42, s42, 24\n"
    "s_xor_b32 s42, s42, s40\n"
    "s_and_b32 s42, s42, 0xff\n"
    "s_xor_b32 s41, s41, s25\n"
    "s_xor_b32 s41, s41, 0xa5c30f96\n"
    "s_or_b32 s42, s41, s42\n"
    "s_lshr_b32 s41, s40, 8\n"
    "s_mul_i32 s40, s41, 0x9e3779b1\n"
    "s_lshr_b32 s40, s40, 24\n"
    "s_xor_b32 s41, s41, 0xa5c30f96\n"
    "s_cmp_eq_u32 s42, 0\n"
    "s_cbranch_scc1 .Lxd_pv33k\n"
    "s_sleep 2\n"
    "s_lshl_b32 s40, s2, 3\n"
    "s_add_u32 s40, s40, 4096\n"
    "v_mov_b32 v13, 40960\n"
    "v_writelane_b32 v13, s40, 0\n"
    "s_mov_b64 exec, 3\n"
    "global_load_dwordx2 v[26:27], v13, s[10:11] sc1\n"
    "s_mov_b64 exec, -1\n"
    "s_waitcnt vmcnt(0)\n"
    "s_sub_u32 s59, s59, 1\n"
    "s_cbranch_scc0 .Lxd_pv33\n"
    ".Lxd_pv33k:\n"
    "v_mov_b32 v26, s40\n"
    "v_mov_b32 v27, s41\n"
    "v_mov_b32 v13, 154112\n"
    "ds_write_b64 v13, v[26:27]\n"
    "s_add_u32 s40, s25, 1\n"
    "s_lshl_b32 s41, s2, 2\n"
    "v_mov_b32 v43, s40\n"
    "v_mov_b32 v13, s41\n"
    "s_mov_b64 exec, 1\n"
    "global_store_dword v13, v43, s[10:11] sc1\n"
    "s_mov_b64 exec, -1\n"
    "s_branch .Lxd_on34r\n"
    ".Lxd_on35:\n"
    "v_pk_mul_f32 v[240:241], v[240:241], v[76:77]\n"
    "v_cvt_pk_f16_f32 v240, v240, v241\n"
    "ds_write_b16 v60, v240 offset:0\n"
    "ds_write_b16_d16_hi v61, v240 offset:0\n"
    "v_pk_mul_f32 v[242:243], v[242:243], v[78:79]\n"
    "v_cvt_pk_f16_f32 v242, v242, v243\n"
    "ds_write_b16 v62, v242 offset:0\n"
    "ds_write_b16_d16_hi v63, v242 offset:0\n"
    "v_pk_mul_f32 v[244:245], v[244:245], v[80:81]\n"
    "v_cvt_pk_f16_f32 v244, v244, v245\n"
    "ds_write_b16 v64, v244 offset:0\n"
    "ds_write_b16_d16_hi v65, v244 offset:0\n"
    "v_pk_mul_f32 v[246:247], v[246:247], v[82:83]\n"
    "v_cvt_pk_f16_f32 v246, v246, v247\n"
    "ds_write_b16 v66, v246 offset:0\n"
    "ds_write_b16_d16_hi v67, v246 offset:0\n"
    "v_pk_mul_f32 v[248:249], v[248:249], v[84:85]\n"
    "v_cvt_pk_f16_f32 v248, v248, v249\n"
    "ds_write_b16 v68, v248 offset:0\n"
    "ds_write_b16_d16_hi v69, v248 offset:0\n"
    "v_pk_mul_f32 v[250:251], v[250:251], v[86:87]\n"
    "v_cvt_pk_f16_f32 v250, v250, v251\n"
    "ds_write_b16 v70, v250 offset:0\n"
    "ds_write_b16_d16_hi v71, v250 offset:0\n"
    "v_pk_mul_f32 v[252:253], v[252:253], v[88:89]\n"
    "v_cvt_pk_f16_f32 v252, v252, v253\n"
    "ds_write_b16 v72, v252 offset:0\n"
    "ds_write_b16_d16_hi v73, v252 offset:0\n"
    "s_branch .Lxd_on35r\n"
    ".Lxd_on36:\n"
    "v_readfirstlane_b32 s40, v24\n"
    "s_or_b32 s40, s40, s44\n"
    "s_lshl1_add_u32 s41, s40, s54\n"
    "s_cmp_lt_u32 s40, 256\n"
    "s_cselect_b32 s40, s41, -1\n"
    "s_cselect_b32 s41, s41, 2\n"
    "s_cselect_b64 s[48:49], -1, 0\n"
    "v_mov_b32 v12, s40\n"
    "s_mul_i32 s41, s41, 52\n"
    "s_add_u32 s38, s4, s41\n"
    "s_addc_u32 s39, s5, 0\n"
    "v_ashrrev_i32 v21, 31, v12\n"
    "ds_write2_b32 v25, v12, v21 offset0:5 offset1:133\n"
    "v_sub_u32 v15, v0, v15\n"
    "v_sub_u32 v16, v23, v16\n"
    "global_load_dwordx3 v[36:38], v17, s[38:39] offset:-104\n"
    "global_load_dword v41, v18, s[38:39] offset:-8\n"
    "s_branch .Lxd_on36r\n"
    ".Lxd_on37:\n"
    "s_mul_i32 s40, s20, 5376\n"
    "v_add_u32 v12, s40, v42\n"
    "ds_read_b128 v[92:95], v12 offset:0\n"
    "ds_read_b128 v[96:99], v12 offset:1024\n"
    "ds_read_b128 v[100:103], v12 offset:2048\n"
    "ds_read_b128 v[104:107], v12 offset:3072\n"
    "ds_read_b128 v[108:111], v12 offset:4096\n"
    "ds_read_b128 v[112:115], v12 offset:4352\n"
    "v_mov_b32 v21, 154112\n"
    "ds_read_b64 v[116:117], v21\n"
    "s_add_u32 s40, s10, 951296\n"
    "s_addc_u32 s41, s11, 0\n"
    "v_mov_b32 v21, 40960\n"
    "s_waitcnt lgkmcnt(0)\n"
    "global_store_dwordx4 v12, v[92:95], s[40:41] offset:-1024 sc1\n"
    "global_store_dwordx4 v12, v[96:99], s[40:41] offset:0 sc1\n"
    "global_store_dwordx4 v12, v[100:103], s[40:41] offset:1024 sc1\n"
    "global_store_dwordx4 v12, v[104:107], s[40:41] offset:2048 sc1\n"
    "global_store_dwordx4 v12, v[108:111], s[40:41] offset:3072 sc1\n"
    "global_store_dwordx4 v12, v[112:115], s[40:41] offset:3328 sc1\n"
    "s_waitcnt vmcnt(0)\n"
    "s_barrier\n"
    "s_cmp_lg_u32 s20, 0\n"
    "s_cbranch_scc1 .Lxd_b2\n"
    "s_mov_b64 exec, 1\n"
    "global_store_dwordx2 v21, v[116:117], s[10:11] sc1\n"
    "s_mov_b64 exec, -1\n"
    "s_waitcnt vmcnt(0)\n"
    ".Lxd_b2:\n"
    "s_branch .Lxd_on37r\n"
    ".Lxd_end:\n"
    : "+{v0}"(tid) : "{s[0:1]}"(__builtin_amdgcn_kernarg_segment_ptr()), "{s2}"(blockIdx.x)
    : "memory", "vcc", "scc", "v1", "v2", "v3", "v4", "v5", "v6", "v7", "v8", "v9", "v10", "v11", "v12", "v13", "v14", "v15", "v16", "v17", "v18", "v19", "v20", "v21", "v22", "v23", "v24", "v25", "v26", "v27", "v28", "v29", "v30", "v31", "v32", "v33", "v34", "v35", "v36", "v37", "v38", "v39", "v40", "v41", "v42", "v43", "v44", "v45", "v46", "v47", "v48", "v49", "v50", "v51", "v52", "v53", "v54", "v55", "v56", "v57", "v58", "v59", "v60", "v61", "v62", "v63", "v64", "v65", "v66", "v67", "v68", "v69", "v70", "v71", "v72", "v73", "v74", "v75", "v76", "v77", "v78", "v79", "v80", "v81", "v82", "v83", "v84", "v85", "v86", "v87", "v88", "v89", "v90", "v91", "v92", "v93", "v94", "v95", "v96", "v97", "v98", "v99", "v100", "v101", "v102", "v103", "v104", "v105", "v106", "v107", "v108", "v109", "v110", "v111", "v112", "v113", "v114", "v115", "v116", "v117", "v118", "v119", "v120", "v121", "v122", "v123", "v124", "v125", "v126", "v127", "v128", "v129", "v130", "v131", "v132", "v133", "v134", "v135", "v136", "v137", "v138", "v139", "v140", "v141", "v142", "v143", "v144", "v145", "v146", "v147", "v148", "v149", "v150", "v151", "v152", "v153", "v154", "v155", "v156", "v157", "v158", "v159", "v160", "v161", "v162", "v163", "v164", "v165", "v166", "v167", "v168", "v169", "v170", "v171", "v172", "v173", "v174", "v175", "v176", "v177", "v178", "v179", "v180", "v181", "v182", "v183", "v184", "v185", "v186", "v187", "v188", "v189", "v190", "v191", "v192", "v193", "v194", "v195", "v196", "v197", "v198", "v199", "v200", "v201", "v202", "v203", "v204", "v205", "v206", "v207", "v208", "v209", "v210", "v211", "v212", "v213", "v214", "v215", "v216", "v217", "v218", "v219", "v220", "v221", "v222", "v223", "v224", "v225", "v226", "v227", "v228", "v229", "v230", "v231", "v232", "v233", "v234", "v235", "v236", "v237", "v238", "v239", "v240", "v241", "v242", "v243", "v244", "v245", "v246", "v247", "v248", "v249", "v250", "v251", "v252", "v253", "v254", "v255", "s3", "s4", "s5", "s6", "s7", "s8", "s9", "s10", "s11", "s12", "s13", "s14", "s15", "s16", "s17", "s18", "s19", "s20", "s21", "s22", "s23", "s24", "s25", "s26", "s27", "s28", "s29", "s30", "s31", "s34", "s35", "s36", "s37", "s38", "s39", "s40", "s41", "s42", "s43", "s44", "s45", "s46", "s47", "s48", "s49", "s50", "s51", "s52", "s53", "s54", "s55", "s56", "s57", "s58", "s59", "s60", "s61", "s62", "s63", "s64", "s65", "s66", "s67");
}

extern "C" void kernel_launch(void* const* d_in, const int* in_sizes, int n_in, void* d_out, int out_size,
                              void* d_ws, size_t ws_size, hipStream_t stream) {
  xd_kernel<<<256, 512, 0, stream>>>((const int*)d_in[0], (const u16*)d_in[4], (const u16*)d_in[1], (char*)d_ws, (const u16*)d_in[5], (const u16*)d_in[6],
                                     (const u16*)d_in[7], (u16*)d_out, (const u16*)d_in[2], (const u16*)d_in[3]);
}
```

```cpp
#include <hip/hip_runtime.h>
typedef unsigned short u16;

__device__ const __attribute__((aligned(16), used, visibility("hidden"))) u16 xd_tab[2048] asm("xd_tab") = {1564, 65535, 1560, 1949, 65535, 65535, 65535, 65535, 1565, 1692, 65535, 1561, 65535, 65535, 65535, 65535, 65535, 1693, 1820, 65535, 65535, 65535, 65535, 65535, 65535, 65535, 1821, 1948, 18585, 65535, 65535, 18584, 19764, 19895, 20022, 20149, 65535, 65535, 65535, 65535, 19765, 19892, 20023, 20150, 65535, 65535, 65535, 65535, 19766, 19893, 20020, 20151, 65535, 65535, 65535, 65535, 19767, 19894, 20021, 20148, 65535, 65535, 65535, 65535, 18200, 18331, 18458, 65535, 16124, 65535, 1688, 16509, 18201, 18328, 18459, 18586, 16125, 16252, 65535, 65535, 18202, 18329, 18456, 18587, 65535, 16253, 16380, 65535, 18203, 18330, 18457, 65535, 65535, 1689, 16381, 16508, 14560, 14691, 14818, 14945, 16120, 65535, 16378, 16505, 14561, 14688, 14819, 14946, 65535, 16248, 16379, 16506, 14562, 14689, 14816, 14947, 16122, 16249, 16376, 16507, 14563, 14690, 14817, 14944, 16123, 65535, 16377, 16504, 8844, 8975, 9102, 9229, 10924, 11055, 11182, 11309, 8845, 8972, 9103, 9230, 10925, 11052, 11183, 11310, 8846, 8973, 9100, 9231, 10926, 11053, 11180, 11311, 8847, 8974, 9101, 9228, 10927, 11054, 11181, 11308, 3644, 3775, 3902, 4029, 5204, 5335, 5462, 5589, 3645, 3772, 3903, 4030, 5205, 5332, 5463, 5590, 65535, 3773, 3900, 4031, 5206, 5333, 5460, 5591, 65535, 3774, 3901, 4028, 5207, 5334, 5461, 5588, 1566, 1694, 1822, 1950, 16126, 16254, 16382, 16510, 1567, 1695, 1823, 1951, 16127, 16255, 16383, 16511, 65535, 65535, 65535, 65535, 65535, 65535, 65535, 65535, 65535, 65535, 65535, 65535, 65535, 65535, 65535, 65535, 65535, 65535, 65535, 65535, 65535, 65535, 65535, 65535, 65535, 65535, 65535, 65535, 65535, 65535, 65535, 65535, 65535, 65535, 65535, 65535, 65535, 65535, 65535, 65535, 65535, 65535, 65535, 65535, 65535, 65535, 65535, 65535, 65535, 65535, 65535, 65535, 65535, 65535, 65535, 65535, 65535, 65535, 65535, 65535, 65535, 65535, 65535, 65535, 65535, 65535, 65535, 65535, 65535, 65535, 65535, 65535, 65535, 65535, 65535, 65535, 65535, 65535, 65535, 65535, 19760, 65535, 65535, 20145, 65535, 65535, 65535, 65535, 19761, 19888, 65535, 65535, 14695, 65535, 65535, 65535, 1562, 19889, 20016, 65535, 65535, 65535, 65535, 65535, 65535, 1563, 20017, 20144, 65535, 65535, 65535, 65535, 18204, 18335, 18462, 18589, 65535, 16121, 65535, 65535, 18205, 18332, 18463, 18590, 16251, 65535, 65535, 16250, 18206, 18333, 18460, 18591, 65535, 65535, 65535, 65535, 18207, 18334, 18461, 18588, 65535, 65535, 65535, 65535, 14564, 65535, 14822, 14949, 12484, 65535, 1690, 12869, 14565, 14692, 14823, 14950, 12485, 12612, 65535, 1691, 14566, 14693, 14820, 14951, 65535, 12613, 12740, 65535, 14567, 14694, 14821, 14948, 65535, 65535, 12741, 12868, 65535, 11051, 11178, 11305, 12480, 12611, 12738, 12865, 65535, 11048, 11179, 11306, 12481, 12608, 12739, 12866, 10922, 11049, 11176, 11307, 12482, 12609, 65535, 12867, 10923, 11050, 11177, 11304, 12483, 12610, 12737, 12864, 5200, 5331, 5458, 5585, 7280, 7411, 7538, 7665, 5201, 5328, 5459, 5586, 7281, 7408, 7539, 7666, 5202, 5329, 5456, 5587, 7282, 7409, 7536, 7667, 5203, 5330, 5457, 5584, 7283, 7410, 7537, 7664, 19762, 19890, 20018, 20146, 12486, 12614, 12742, 12870, 19763, 19891, 20019, 20147, 12487, 12615, 12743, 12871, 65535, 65535, 65535, 65535, 65535, 65535, 65535, 65535, 65535, 65535, 65535, 65535, 65535, 65535, 65535, 65535, 65535, 65535, 65535, 65535, 65535, 65535, 65535, 65535, 65535, 65535, 65535, 65535, 65535, 65535, 65535, 65535, 65535, 65535, 65535, 65535, 65535, 65535, 65535, 65535, 65535, 65535, 65535, 65535, 65535, 65535, 65535, 65535, 65535, 65535, 65535, 65535, 65535, 65535, 65535, 65535, 65535, 65535, 65535, 65535, 65535, 65535, 65535, 65535, 65535, 65535, 65535, 65535, 65535, 65535, 65535, 65535, 65535, 65535, 65535, 65535, 65535, 65535, 65535, 65535, 10920, 10921, 65535, 65535, 65535, 65535, 65535, 65535, 65535, 65535, 65535, 65535, 65535, 65535, 65535, 65535, 65535, 65535, 65535, 65535, 65535, 65535, 65535, 65535, 65535, 65535, 65535, 65535, 65535, 65535, 65535, 65535, 65535, 65535, 65535, 65535, 65535, 65535, 65535, 65535, 65535, 65535, 65535, 65535, 65535, 65535, 65535, 65535, 65535, 65535, 65535, 65535, 65535, 65535, 65535, 65535, 65535, 65535, 65535, 65535, 65535, 65535, 65535, 65535, 65535, 65535, 65535, 65535, 65535, 65535, 65535, 65535, 65535, 65535, 65535, 65535, 65535, 65535, 65535, 65535, 65535, 65535, 12736, 65535, 65535, 65535, 65535, 65535, 65535, 65535, 65535, 65535, 65535, 65535, 65535, 65535, 8840, 65535, 1816, 9225, 65535, 65535, 65535, 65535, 8841, 8968, 65535, 1817, 65535, 65535, 65535, 65535, 65535, 8969, 9096, 65535, 65535, 65535, 65535, 65535, 65535, 65535, 9097, 9224, 65535, 65535, 65535, 65535, 7284, 65535, 7542, 7669, 65535, 65535, 65535, 65535, 7285, 7412, 7543, 7670, 65535, 65535, 65535, 65535, 7286, 65535, 7540, 7671, 65535, 65535, 65535, 65535, 7287, 65535, 7541, 7668, 65535, 65535, 65535, 65535, 8842, 8970, 9098, 9226, 1944, 65535, 1944, 65535, 8843, 8971, 9099, 9227, 1945, 1946, 1945, 65535, 65535, 65535, 65535, 65535, 65535, 65535, 65535, 65535, 65535, 65535, 65535, 65535, 65535, 65535, 65535, 65535, 65535, 65535, 65535, 65535, 65535, 65535, 65535, 65535, 65535, 65535, 65535, 65535, 65535, 65535, 65535, 65535, 65535, 65535, 65535, 65535, 65535, 65535, 65535, 65535, 65535, 65535, 65535, 65535, 65535, 65535, 65535, 65535, 65535, 65535, 3646, 3647, 0, 65535, 65535, 65535, 65535, 65535, 65535, 65535, 1, 65535, 65535, 65535, 65535, 65535, 65535, 65535, 4, 65535, 65535, 65535, 65535, 65535, 65535, 65535, 5, 65535, 65535, 65535, 65535, 65535, 65535, 65535, 2, 65535, 65535, 65535, 65535, 65535, 65535, 65535, 3, 65535, 65535, 65535, 65535, 65535, 65535, 65535, 6, 65535, 65535, 65535, 65535, 65535, 65535, 65535, 7, 65535, 65535, 65535, 65535, 65535, 65535, 65535, 132, 65535, 65535, 65535, 65535, 65535, 65535, 65535, 133, 65535, 65535, 65535, 65535, 65535, 65535, 65535, 128, 65535, 65535, 65535, 65535, 65535, 65535, 65535, 129, 65535, 65535, 65535, 65535, 65535, 65535, 65535, 134, 65535, 65535, 65535, 65535, 65535, 65535, 65535, 135, 65535, 65535, 65535, 65535, 65535, 65535, 65535, 130, 65535, 65535, 65535, 65535, 65535, 65535, 65535, 131, 65535, 65535, 65535, 65535, 65535, 65535, 65535, 256, 65535, 65535, 65535, 7415, 65535, 7413, 7414, 257, 65535, 65535, 65535, 65535, 65535, 65535, 65535, 260, 65535, 65535, 65535, 65535, 65535, 65535, 65535, 261, 65535, 65535, 65535, 3640, 65535, 1818, 4025, 258, 65535, 65535, 65535, 3641, 3768, 65535, 1819, 259, 65535, 65535, 65535, 65535, 3769, 3896, 65535, 262, 65535, 65535, 65535, 65535, 65535, 3897, 4024, 263, 65535, 65535, 65535, 3642, 3770, 3898, 4026, 384, 65535, 65535, 65535, 3643, 3771, 3899, 4027, 386, 65535, 65535, 65535, 65535, 65535, 65535, 65535, 65535, 65535, 65535, 65535, 65535, 65535, 65535, 65535, 65535, 65535, 65535, 65535, 65535, 65535, 65535, 65535, 1947, 65535, 65535, 65535, 65535, 65535, 65535, 65535, 65535, 65535, 65535, 65535, 65535, 65535, 65535, 65535, 65535, 65535, 65535, 65535, 65535, 65535, 65535, 65535, 65535, 65535, 65535, 65535, 3378, 65535, 3376, 3511, 65535, 65535, 65535, 65535, 3379, 3382, 65535, 65535, 21333, 65535, 65535, 21332, 65535, 3383, 3506, 65535, 65535, 65535, 65535, 65535, 65535, 3377, 3507, 3510, 65535, 65535, 65535, 65535, 21578, 21591, 21714, 21711, 65535, 65535, 65535, 65535, 21579, 21582, 21715, 21718, 65535, 65535, 65535, 65535, 21586, 21583, 21706, 21719, 65535, 65535, 65535, 65535, 21587, 21590, 21707, 21710, 65535, 65535, 65535, 65535, 21320, 65535, 21456, 21453, 17938, 65535, 3380, 18071, 21321, 21324, 21457, 21460, 17939, 17942, 65535, 65535, 21328, 21325, 21448, 21461, 65535, 17943, 18066, 65535, 21329, 65535, 21449, 21452, 65535, 3381, 18067, 18070, 17680, 65535, 17816, 17813, 17936, 17949, 18072, 65535, 17681, 17684, 17817, 17820, 17937, 17940, 18073, 18076, 17688, 17685, 65535, 17821, 17944, 17941, 18064, 18077, 17689, 17692, 65535, 17812, 17945, 17948, 18065, 65535, 10658, 10671, 10794, 10791, 14042, 14055, 14178, 65535, 10659, 10662, 10795, 65535, 14043, 14046, 14179, 14182, 10666, 10663, 10786, 65535, 14050, 14047, 65535, 14183, 10667, 10670, 10787, 10790, 14051, 14054, 65535, 65535, 65535, 6775, 6898, 6895, 7018, 7031, 7154, 7151, 65535, 6766, 6899, 6902, 7019, 7022, 7155, 7158, 6770, 6767, 65535, 6903, 7026, 7023, 7146, 7159, 6771, 6774, 65535, 6894, 7027, 7030, 7147, 7150, 3386, 3390, 3514, 3518, 17946, 65535, 18074, 18078, 3387, 3391, 3515, 3519, 17947, 65535, 18075, 18079, 65535, 65535, 65535, 65535, 65535, 65535, 65535, 65535, 65535, 65535, 65535, 65535, 65535, 65535, 65535, 65535, 65535, 65535, 65535, 65535, 65535, 65535, 65535, 65535, 65535, 65535, 65535, 65535, 65535, 65535, 65535, 65535, 65535, 65535, 65535, 65535, 65535, 65535, 65535, 65535, 65535, 65535, 65535, 65535, 65535, 65535, 65535, 65535, 65535, 65535, 65535, 65535, 65535, 65535, 65535, 65535, 65535, 65535, 65535, 65535, 17693, 65535, 65535, 65535, 65535, 65535, 65535, 65535, 65535, 65535, 17808, 17809, 65535, 65535, 65535, 65535, 65535, 65535, 65535, 65535, 21576, 65535, 65535, 21709, 17682, 17683, 65535, 65535, 21577, 21580, 65535, 65535, 65535, 65535, 65535, 65535, 3384, 21581, 21704, 65535, 65535, 65535, 65535, 65535, 65535, 3385, 21705, 21708, 17815, 65535, 65535, 17814, 21322, 21335, 21458, 21455, 65535, 65535, 65535, 65535, 21323, 21326, 21459, 21462, 65535, 65535, 65535, 65535, 21330, 21327, 21450, 21463, 65535, 65535, 65535, 65535, 21331, 21334, 21451, 21454, 18069, 65535, 65535, 18068, 65535, 17695, 17818, 65535, 14298, 65535, 65535, 14431, 65535, 17686, 17819, 17822, 14299, 14302, 65535, 3389, 17690, 17687, 17810, 17823, 3388, 14303, 14426, 65535, 17691, 17694, 17811, 65535, 65535, 65535, 14427, 14430, 14040, 14053, 14176, 14173, 14296, 14309, 14432, 14429, 65535, 14044, 14177, 14180, 14297, 14300, 65535, 14436, 14048, 14045, 14168, 14181, 14304, 14301, 14424, 14437, 14049, 14052, 14169, 14172, 14305, 14308, 14425, 14428, 7016, 7029, 7152, 7149, 10400, 10413, 10536, 65535, 7017, 7020, 7153, 7156, 10401, 10404, 10537, 10540, 65535, 7021, 7144, 7157, 10408, 10405, 10528, 10541, 65535, 7028, 7145, 7148, 10409, 10412, 10529, 65535, 21584, 21588, 21712, 21716, 65535, 14310, 14434, 14438, 21585, 21589, 21713, 21717, 14307, 14311, 14435, 14439, 65535, 65535, 65535, 65535, 65535, 65535, 65535, 65535, 65535, 65535, 65535, 65535, 65535, 65535, 65535, 65535, 65535, 65535, 65535, 65535, 65535, 65535, 65535, 65535, 65535, 65535, 65535, 65535, 65535, 65535, 65535, 65535, 65535, 65535, 65535, 65535, 65535, 65535, 65535, 65535, 65535, 65535, 65535, 65535, 65535, 65535, 65535, 65535, 65535, 65535, 65535, 65535, 65535, 65535, 65535, 65535, 65535, 65535, 65535, 65535, 65535, 65535, 65535, 65535, 65535, 65535, 65535, 65535, 65535, 65535, 65535, 65535, 65535, 10798, 10799, 65535, 65535, 65535, 65535, 65535, 65535, 14041, 65535, 65535, 65535, 65535, 65535, 65535, 65535, 65535, 65535, 65535, 65535, 65535, 65535, 65535, 65535, 65535, 65535, 65535, 65535, 65535, 65535, 65535, 65535, 65535, 65535, 65535, 65535, 65535, 65535, 65535, 65535, 65535, 65535, 65535, 65535, 65535, 65535, 65535, 65535, 65535, 65535, 65535, 17950, 17951, 17950, 65535, 65535, 65535, 14170, 14171, 65535, 65535, 65535, 65535, 14175, 65535, 65535, 14174, 65535, 65535, 65535, 65535, 65535, 65535, 65535, 65535, 14306, 65535, 14306, 65535, 65535, 65535, 65535, 65535, 65535, 65535, 65535, 65535, 65535, 14433, 65535, 65535, 65535, 65535, 65535, 65535, 65535, 65535, 65535, 65535, 65535, 65535, 65535, 65535, 10656, 65535, 65535, 10789, 65535, 65535, 65535, 65535, 10657, 10660, 65535, 65535, 65535, 65535, 65535, 65535, 3504, 10661, 10784, 65535, 65535, 65535, 65535, 65535, 65535, 3505, 10785, 10788, 65535, 65535, 65535, 65535, 10402, 65535, 10538, 65535, 65535, 65535, 65535, 65535, 10403, 10406, 10539, 10542, 65535, 65535, 65535, 65535, 10410, 10407, 10530, 10543, 6896, 6897, 6896, 65535, 10411, 65535, 10531, 65535, 65535, 65535, 65535, 65535, 10664, 10668, 10792, 10796, 3508, 65535, 3508, 65535, 10665, 10669, 10793, 10797, 3509, 3516, 3509, 65535, 65535, 65535, 65535, 65535, 65535, 65535, 65535, 65535, 65535, 65535, 65535, 65535, 65535, 65535, 65535, 65535, 65535, 65535, 65535, 65535, 65535, 65535, 65535, 65535, 65535, 65535, 65535, 65535, 65535, 65535, 65535, 65535, 65535, 65535, 65535, 65535, 65535, 65535, 65535, 65535, 65535, 65535, 65535, 65535, 65535, 65535, 65535, 65535, 6762, 6763, 65535, 65535, 3120, 65535, 65535, 65535, 65535, 65535, 65535, 65535, 3121, 65535, 65535, 65535, 65535, 65535, 6890, 6891, 3122, 65535, 65535, 65535, 65535, 65535, 65535, 65535, 3123, 65535, 65535, 65535, 65535, 65535, 7024, 7025, 3128, 65535, 65535, 65535, 65535, 65535, 65535, 65535, 3129, 65535, 65535, 65535, 65535, 65535, 65535, 65535, 3130, 65535, 65535, 65535, 65535, 65535, 65535, 65535, 3131, 65535, 65535, 65535, 65535, 65535, 65535, 65535, 3126, 65535, 65535, 65535, 65535, 65535, 65535, 65535, 3127, 65535, 65535, 65535, 65535, 65535, 65535, 65535, 3124, 65535, 65535, 65535, 65535, 65535, 65535, 65535, 3125, 65535, 65535, 65535, 65535, 65535, 65535, 65535, 3134, 65535, 65535, 65535, 65535, 65535, 65535, 65535, 3135, 65535, 65535, 65535, 65535, 65535, 65535, 65535, 3132, 65535, 65535, 65535, 10533, 65535, 65535, 10532, 3133, 65535, 65535, 65535, 65535, 65535, 65535, 65535, 3248, 65535, 65535, 65535, 10415, 65535, 65535, 10414, 3249, 65535, 65535, 65535, 65535, 65535, 65535, 65535, 3250, 65535, 65535, 65535, 10535, 65535, 65535, 10534, 3251, 65535, 65535, 65535, 6760, 65535, 65535, 6893, 3256, 65535, 65535, 65535, 6761, 6764, 65535, 3513, 3257, 65535, 65535, 65535, 3512, 6765, 6888, 65535, 3258, 65535, 65535, 65535, 65535, 65535, 6889, 6892, 3259, 65535, 65535, 65535, 6768, 6772, 65535, 6900, 3252, 65535, 65535, 65535, 6769, 6773, 65535, 6901, 3260, 65535, 65535, 65535, 65535, 65535, 65535, 65535, 65535, 65535, 65535, 65535, 65535, 65535, 65535, 65535, 65535, 65535, 65535, 65535, 65535, 65535, 65535, 65535, 3517, 65535, 65535, 65535, 65535, 65535, 65535, 65535, 65535, 65535, 65535, 65535, 65535, 65535, 65535, 65535, 65535, 65535, 65535, 65535, 65535, 65535, 65535, 65535, 65535, 65535, 65535, 65535};

__global__ __launch_bounds__(512) void xd_kernel(const int* idx, const u16* w1, const u16* table, char* ws, const u16* b1, const u16* w2, const u16* b_out, u16* out,
                                                 const u16* ws_cin, const u16* wout) {
  __shared__ __attribute__((aligned(1024))) char shm[163840];
  asm volatile("" :: "v"((__attribute__((address_space(3))) char*)shm));
  unsigned tid = threadIdx.x;
  asm volatile(
    "s_load_dwordx16 s[4:19], s[0:1], 0\n"
    "s_setprio 3\n"
    "v_readfirstlane_b32 s20, v0\n"
    "v_and_b32 v1, 63, v0\n"
    "s_sub_u32 s3, s2, 48\n"
    "s_lshr_b32 s20, s20, 6\n"
    "s_cmp_lt_u32 s2, 48\n"
    "s_cbranch_scc1 .Lxd_cin\n"
    "s_and_b32 s40, s2, 7\n"
    "s_lshr_b32 s23, s3, 4\n"
    "s_bfe_u32 s41, s3, 0x10003\n"
    "s_lshl1_add_u32 s24, s40, s41\n"
    "s_lshl_b32 s24, s24, 8\n"
    "s_mul_i32 s40, s40, 3\n"
    "s_and_b32 s41, s23, 15\n"
    "s_mul_i32 s41, s41, 2\n"
    "s_add_u32 s43, s40, s41\n"
    "s_lshr_b32 s43, s43, 1\n"
    "s_sub_u32 s40, s43, 12\n"
    "s_min_u32 s43, s43, s40\n"
    "s_lshr_b32 s40, 2048, s43\n"
    "s_and_b32 s40, s40, 1\n"
    "s_and_b32 s42, s43, 14\n"
    "s_or_b32 s42, s42, s40\n"
    "s_lshl_b32 s42, s42, 3\n"
    "s_lshl_b32 s40, s20, 5\n"
    "s_add_u32 s40, s40, s24\n"
    "s_mul_i32 s40, s40, 104\n"
    "v_lshlrev_b32 v86, 4, v1\n"
    "s_lshl_b32 s34, s20, 12\n"
    "s_add_u32 m0, s34, 98304\n"
    "v_lshrrev_b32 v85, 4, v1\n"
    "v_bfe_u32 v87, v0, 4, 3\n"
    "v_lshlrev_b32 v87, 5, v87\n"
    "v_and_b32 v88, 15, v0\n"
    "v_lshlrev_b32 v47, 4, v88\n"
    "v_xor_b32 v87, v87, v47\n"
    "s_movk_i32 s41, 3328\n"
    "v_mad_u32_u24 v46, v85, s41, v87\n"
    "v_add_u32 v47, 12288, v46\n"
    "s_mul_i32 s41, s20, 26624\n"
    "s_lshl_b32 s66, s23, 8\n"
    "s_add_u32 s41, s41, s66\n"
    "s_mul_i32 s30, s42, 53248\n"
    "s_lshl_b32 s35, s20, 11\n"
    "s_add_u32 s35, s35, 32768\n"
    "s_waitcnt lgkmcnt(0)\n"
    "s_add_u32 s38, s4, s40\n"
    "s_addc_u32 s39, s5, 0\n"
    "global_load_lds_dwordx4 v86, s[38:39] offset:0\n"
    "global_load_lds_dwordx4 v86, s[38:39] offset:1024\n"
    "global_load_lds_dwordx4 v86, s[38:39] offset:2048\n"
    "global_load_lds_dwordx4 v86, s[38:39] offset:2304\n"
    "s_add_u32 s6, s6, s41\n"
    "s_addc_u32 s7, s7, 0\n"
    "s_add_u32 s30, s6, s30\n"
    "s_addc_u32 s31, s7, 0\n"
    "s_add_u32 m0, s35, 0\n"
    "s_nop 0\n"
    "global_load_lds_dwordx4 v46, s[30:31] offset:0\n"
    "global_load_lds_dwordx4 v47, s[30:31] offset:1024\n"
    "s_add_u32 s30, s30, 212992\n"
    "s_addc_u32 s31, s31, 0\n"
    "s_setprio 1\n"
    "s_and_b32 s40, s20, 2\n"
    "s_lshl_b32 s40, s40, 6\n"
    "s_add_u32 s40, s40, s66\n"
    "v_and_b32 v43, 31, v0\n"
    "s_lshl_b32 s64, s20, 8\n"
    "s_add_u32 s64, s64, 147456\n"
    "s_mov_b32 m0, s64\n"
    "v_lshl_add_u32 v43, v43, 2, s40\n"
    "global_load_lds_dword v43, s[12:13]\n"
    "s_sub_u32 s36, s8, 3072\n"
    "s_subb_u32 s37, s9, 0\n"
    "v_and_b32 v84, 3, v0\n"
    "v_lshrrev_b32 v44, 3, v1\n"
    "v_lshl_add_u32 v44, v84, 3, v44\n"
    "v_mul_u32_u24 v44, 104, v44\n"
    "s_add_u32 s40, s34, 98304\n"
    "v_add_u32 v44, s40, v44\n"
    "v_add_u32 v45, s42, v44\n"
    "v_and_b32 v86, 7, v0\n"
    "v_xor_b32 v86, v86, v85\n"
    "v_lshlrev_b32 v86, 4, v86\n"
    "v_xor_b32 v51, 64, v86\n"
    "v_add_u32 v48, 3072, v86\n"
    "v_add_u32 v49, 2048, v51\n"
    "v_add_u32 v50, 1024, v86\n"
    "s_setprio 0\n"
    "s_cmp_eq_u32 s20, 7\n"
    "s_cbranch_scc1 .Lxd_on1\n"
    ".Lxd_on1r:\n"
    "v_lshrrev_b32 v86, 1, v88\n"
    "v_xor_b32 v86, v86, v85\n"
    "v_lshlrev_b32 v86, 4, v86\n"
    "v_lshl_add_u32 v86, v88, 7, v86\n"
    "s_lshr_b32 s21, s20, 2\n"
    "s_and_b32 s41, s20, 1\n"
    "s_lshl1_add_u32 s40, s21, s41\n"
    "s_lshl_b32 s40, s40, 13\n"
    "v_add_u32 v56, s40, v86\n"
    "v_xor_b32 v57, 64, v56\n"
    "v_lshrrev_b32 v86, 2, v88\n"
    "v_lshl_add_u32 v88, v85, 3, v86\n"
    "v_bfe_u32 v86, v0, 2, 3\n"
    "s_and_b32 s40, s20, 2\n"
    "s_lshl1_add_u32 s40, s40, 1024\n"
    "v_xor_b32 v86, s40, v86\n"
    "v_lshlrev_b32 v86, 5, v86\n"
    "v_lshl_add_u32 v86, v88, 8, v86\n"
    "v_lshl_add_u32 v76, v84, 3, v86\n"
    "v_xor_b32 v77, 32, v76\n"
    "v_xor_b32 v78, 64, v76\n"
    "v_xor_b32 v79, 96, v76\n"
    "v_lshl_add_u32 v42, v85, 3, s64\n"
    "s_lshr_b32 s41, s43, 1\n"
    "s_lshl3_add_u32 s41, s41, 8\n"
    "s_bitcmp1_b32 s43, 0\n"
    "s_cselect_b32 s28, 0x30201000, 0\n"
    "s_cselect_b32 s29, 0x5840, 0\n"
    "s_cselect_b32 s40, 20480, 0\n"
    "s_cselect_b32 s40, s40, 24576\n"
    "s_cselect_b32 s28, s28, 0x30201000\n"
    "s_cselect_b32 s29, s29, 0x5040\n"
    "s_lshr_b64 s[42:43], s[28:29], s41\n"
    "s_sub_u32 s41, 48, s41\n"
    "s_lshl_b64 s[28:29], s[28:29], s41\n"
    "s_or_b64 s[28:29], s[28:29], s[42:43]\n"
    "s_and_b32 s29, s29, 0xff\n"
    "s_or_b32 s29, s29, s40\n"
    "s_setprio 3\n"
    "s_waitcnt vmcnt(3)\n"
    "ds_read2_b64 v[96:99], v45 offset1:1\n"
    "s_waitcnt lgkmcnt(0)\n"
    "v_lshlrev_b32 v96, 7, v96\n"
    "v_lshlrev_b32 v97, 7, v97\n"
    "v_lshlrev_b32 v98, 7, v98\n"
    "v_add_u32_dpp v52, v96, v48 quad_perm:[0,0,0,0] row_mask:0xf bank_mask:0xf\n"
    "v_add_u32_dpp v53, v96, v49 quad_perm:[1,1,1,1] row_mask:0xf bank_mask:0xf\n"
    "v_add_u32_dpp v54, v96, v50 quad_perm:[2,2,2,2] row_mask:0xf bank_mask:0xf\n"
    "v_add_u32_dpp v55, v96, v51 quad_perm:[3,3,3,3] row_mask:0xf bank_mask:0xf\n"
    "s_add_u32 m0, s34, 0\n"
    "s_nop 0\n"
    "global_load_lds_dwordx4 v52, s[36:37] offset:0\n"
    "global_load_lds_dwordx4 v53, s[36:37] offset:1024\n"
    "global_load_lds_dwordx4 v54, s[36:37] offset:2048\n"
    "global_load_lds_dwordx4 v55, s[36:37] offset:3072\n"
    "s_setprio 1\n"
    "s_bfe_u32 s40, s28, 0x80000\n"
    "v_add_u32 v45, s40, v44\n"
    "ds_read2_b64 v[100:103], v45 offset1:1\n"
    "s_bfe_u32 s40, s28, 0x80008\n"
    "v_add_u32 v45, s40, v44\n"
    "ds_read2_b64 v[104:107], v45 offset1:1\n"
    "s_bfe_u32 s40, s28, 0x80010\n"
    "v_add_u32 v45, s40, v44\n"
    "ds_read2_b64 v[108:111], v45 offset1:1\n"
    "s_bfe_u32 s40, s28, 0x80018\n"
    "v_add_u32 v45, s40, v44\n"
    "ds_read2_b64 v[112:115], v45 offset1:1\n"
    "s_bfe_u32 s40, s29, 0x80000\n"
    "v_add_u32 v45, s40, v44\n"
    "ds_read2_b64 v[116:119], v45 offset1:1\n"
    "s_bfe_u32 s40, s29, 0x80008\n"
    "v_add_u32 v45, s40, v44\n"
    "ds_read_b64 v[120:121], v45\n"
    "s_add_u32 m0, s35, 49152\n"
    "s_nop 0\n"
    "global_load_lds_dwordx4 v46, s[30:31] offset:0\n"
    "global_load_lds_dwordx4 v47, s[30:31] offset:1024\n"
    "s_add_u32 s30, s30, 212992\n"
    "s_addc_u32 s31, s31, 0\n"
    "v_add_u32_dpp v52, v97, v48 quad_perm:[0,0,0,0] row_mask:0xf bank_mask:0xf\n"
    "v_add_u32_dpp v53, v97, v49 quad_perm:[1,1,1,1] row_mask:0xf bank_mask:0xf\n"
    "v_add_u32_dpp v54, v97, v50 quad_perm:[2,2,2,2] row_mask:0xf bank_mask:0xf\n"
    "v_add_u32_dpp v55, v97, v51 quad_perm:[3,3,3,3] row_mask:0xf bank_mask:0xf\n"
    "s_add_u32 m0, s34, 49152\n"
    "s_nop 0\n"
    "global_load_lds_dwordx4 v52, s[36:37] offset:0\n"
    "global_load_lds_dwordx4 v53, s[36:37] offset:1024\n"
    "global_load_lds_dwordx4 v54, s[36:37] offset:2048\n"
    "global_load_lds_dwordx4 v55, s[36:37] offset:3072\n"
    "s_setprio 0\n"
    "s_waitcnt vmcnt(10)\n"
    "ds_read_b64 v[84:85], v42 offset:0\n"
    "ds_read_b64 v[86:87], v42 offset:32\n"
    "ds_read_b64 v[88:89], v42 offset:64\n"
    "ds_read_b64 v[90:91], v42 offset:96\n"
    "v_add_u32_dpp v52, v98, v48 quad_perm:[0,0,0,0] row_mask:0xf bank_mask:0xf\n"
    "v_add_u32_dpp v53, v98, v49 quad_perm:[1,1,1,1] row_mask:0xf bank_mask:0xf\n"
    "v_add_u32_dpp v54, v98, v50 quad_perm:[2,2,2,2] row_mask:0xf bank_mask:0xf\n"
    "v_add_u32_dpp v55, v98, v51 quad_perm:[3,3,3,3] row_mask:0xf bank_mask:0xf\n"
    "v_lshlrev_b32 v99, 7, v99\n"
    "s_waitcnt lgkmcnt(0)\n"
    "v_lshlrev_b32 v60, 16, v84\n"
    "v_and_b32 v61, 0xffff0000, v84\n"
    "v_lshlrev_b32 v62, 16, v85\n"
    "v_and_b32 v63, 0xffff0000, v85\n"
    "v_lshlrev_b32 v64, 16, v86\n"
    "v_and_b32 v65, 0xffff0000, v86\n"
    "v_lshlrev_b32 v66, 16, v87\n"
    "v_and_b32 v67, 0xffff0000, v87\n"
    "v_lshlrev_b32 v68, 16, v88\n"
    "v_and_b32 v69, 0xffff0000, v88\n"
    "v_lshlrev_b32 v70, 16, v89\n"
    "v_and_b32 v71, 0xffff0000, v89\n"
    "v_lshlrev_b32 v72, 16, v90\n"
    "v_and_b32 v73, 0xffff0000, v90\n"
    "v_lshlrev_b32 v74, 16, v91\n"
    "v_and_b32 v75, 0xffff0000, v91\n"
    "s_cmp_eq_u32 s21, 0\n"
    "s_cbranch_scc1 .Lxd_g0\n"
    "s_barrier\n"
    ".Lxd_g0:\n"
    "s_waitcnt vmcnt(6)\n"
    "s_barrier\n"
    "s_add_u32 m0, s35, 98304\n"
    "s_nop 0\n"
    "global_load_lds_dwordx4 v46, s[30:31] offset:0\n"
    "ds_read_b128 v[128:131], v56 offset:0\n"
    "ds_read_b128 v[132:135], v56 offset:2048\n"
    "ds_read_b128 v[136:139], v56 offset:4096\n"
    "ds_read_b128 v[140:143], v56 offset:6144\n"
    "global_load_lds_dwordx4 v47, s[30:31] offset:1024\n"
    "s_add_u32 s30, s30, 212992\n"
    "s_addc_u32 s31, s31, 0\n"
    "ds_read_b64_tr_b16 v[144:145], v76 offset:0\n"
    "ds_read_b64_tr_b16 v[146:147], v76 offset:1024\n"
    "ds_read_b64_tr_b16 v[148:149], v77 offset:0\n"
    "ds_read_b64_tr_b16 v[150:151], v77 offset:1024\n"
    "s_add_u32 m0, s34, 98304\n"
    "s_nop 0\n"
    "global_load_lds_dwordx4 v52, s[36:37] offset:0\n"
    "ds_read_b64_tr_b16 v[152:153], v78 offset:0\n"
    "ds_read_b64_tr_b16 v[154:155], v78 offset:1024\n"
    "ds_read_b64_tr_b16 v[156:157], v79 offset:0\n"
    "ds_read_b64_tr_b16 v[158:159], v79 offset:1024\n"
    "global_load_lds_dwordx4 v53, s[36:37] offset:1024\n"
    "ds_read_b128 v[160:163], v57 offset:0\n"
    "ds_read_b128 v[164:167], v57 offset:2048\n"
    "ds_read_b128 v[168:171], v57 offset:4096\n"
    "ds_read_b128 v[172:175], v57 offset:6144\n"
    "global_load_lds_dwordx4 v54, s[36:37] offset:2048\n"
    "ds_read_b64_tr_b16 v[176:177], v76 offset:8192\n"
    "ds_read_b64_tr_b16 v[178:179], v76 offset:9216\n"
    "ds_read_b64_tr_b16 v[180:181], v77 offset:8192\n"
    "ds_read_b64_tr_b16 v[182:183], v77 offset:9216\n"
    "global_load_lds_dwordx4 v55, s[36:37] offset:3072\n"
    "ds_read_b64_tr_b16 v[184:185], v78 offset:8192\n"
    "ds_read_b64_tr_b16 v[186:187], v78 offset:9216\n"
    "ds_read_b64_tr_b16 v[188:189], v79 offset:8192\n"
    "ds_read_b64_tr_b16 v[190:191], v79 offset:9216\n"
    "s_waitcnt vmcnt(10) lgkmcnt(0)\n"
    "s_barrier\n"
    "v_mfma_f32_16x16x32_bf16 v[192:195], v[144:147], v[128:131], v[60:63]\n"
    "v_lshlrev_b32 v100, 7, v100\n"
    "v_mfma_f32_16x16x32_bf16 v[192:195], v[176:179], v[160:163], v[192:195]\n"
    "v_mfma_f32_16x16x32_bf16 v[196:199], v[176:179], v[164:167], v[60:63]\n"
    "v_add_u32_dpp v52, v99, v48 quad_perm:[0,0,0,0] row_mask:0xf bank_mask:0xf\n"
    "v_mfma_f32_16x16x32_bf16 v[196:199], v[144:147], v[132:135], v[196:199]\n"
    "v_mfma_f32_16x16x32_bf16 v[200:203], v[144:147], v[136:139], v[60:63]\n"
    "v_mfma_f32_16x16x32_bf16 v[200:203], v[176:179], v[168:171], v[200:203]\n"
    "v_add_u32_dpp v53, v99, v49 quad_perm:[1,1,1,1] row_mask:0xf bank_mask:0xf\n"
    "v_mfma_f32_16x16x32_bf16 v[204:207], v[176:179], v[172:175], v[60:63]\n"
    "v_mfma_f32_16x16x32_bf16 v[204:207], v[144:147], v[140:143], v[204:207]\n"
    "v_mfma_f32_16x16x32_bf16 v[220:223], v[148:151], v[140:143], v[64:67]\n"
    "v_add_u32_dpp v54, v99, v50 quad_perm:[2,2,2,2] row_mask:0xf bank_mask:0xf\n"
    "v_mfma_f32_16x16x32_bf16 v[220:223], v[180:183], v[172:175], v[220:223]\n"
    "v_mfma_f32_16x16x32_bf16 v[216:219], v[180:183], v[168:171], v[64:67]\n"
    "v_mfma_f32_16x16x32_bf16 v[216:219], v[148:151], v[136:139], v[216:219]\n"
    "v_add_u32_dpp v55, v99, v51 quad_perm:[3,3,3,3] row_mask:0xf bank_mask:0xf\n"
    "v_mfma_f32_16x16x32_bf16 v[212:215], v[148:151], v[132:135], v[64:67]\n"
    "v_mfma_f32_16x16x32_bf16 v[212:215], v[180:183], v[164:167], v[212:215]\n"
    "v_mfma_f32_16x16x32_bf16 v[208:211], v[180:183], v[160:163], v[64:67]\n"
    "v_add_u32 v58, 98304, v56\n"
    "v_mfma_f32_16x16x32_bf16 v[208:211], v[148:151], v[128:131], v[208:211]\n"
    "v_mfma_f32_16x16x32_bf16 v[224:227], v[152:155], v[128:131], v[68:71]\n"
    "v_add_u32 v59, 98304, v57\n"
    "v_mfma_f32_16x16x32_bf16 v[224:227], v[184:187], v[160:163], v[224:227]\n"
    "v_mfma_f32_16x16x32_bf16 v[228:231], v[184:187], v[164:167], v[68:71]\n"
    "v_mfma_f32_16x16x32_bf16 v[228:231], v[152:155], v[132:135], v[228:231]\n"
    "v_add_u32 v80, 98304, v76\n"
    "v_mfma_f32_16x16x32_bf16 v[232:235], v[152:155], v[136:139], v[68:71]\n"
    "v_mfma_f32_16x16x32_bf16 v[232:235], v[184:187], v[168:171], v[232:235]\n"
    "v_mfma_f32_16x16x32_bf16 v[236:239], v[184:187], v[172:175], v[68:71]\n"
    "v_add_u32 v81, 98304, v77\n"
    "v_mfma_f32_16x16x32_bf16 v[236:239], v[152:155], v[140:143], v[236:239]\n"
    "v_mfma_f32_16x16x32_bf16 v[252:255], v[156:159], v[140:143], v[72:75]\n"
    "v_mfma_f32_16x16x32_bf16 v[252:255], v[188:191], v[172:175], v[252:255]\n"
    "v_add_u32 v82, 98304, v78\n"
    "v_mfma_f32_16x16x32_bf16 v[248:251], v[188:191], v[168:171], v[72:75]\n"
    "v_mfma_f32_16x16x32_bf16 v[248:251], v[156:159], v[136:139], v[248:251]\n"
    "v_mfma_f32_16x16x32_bf16 v[244:247], v[156:159], v[132:135], v[72:75]\n"
    "v_add_u32 v83, 98304, v79\n"
    "v_mfma_f32_16x16x32_bf16 v[244:247], v[188:191], v[164:167], v[244:247]\n"
    "v_mfma_f32_16x16x32_bf16 v[240:243], v[188:191], v[160:163], v[72:75]\n"
    "v_mfma_f32_16x16x32_bf16 v[240:243], v[156:159], v[128:131], v[240:243]\n"
    "s_waitcnt vmcnt(6)\n"
    "s_barrier\n"
    "s_add_u32 m0, s35, 0\n"
    "s_nop 0\n"
    "global_load_lds_dwordx4 v46, s[30:31] offset:0\n"
    "ds_read_b128 v[128:131], v56 offset:49152\n"
    "ds_read_b128 v[132:135], v56 offset:51200\n"
    "ds_read_b128 v[136:139], v56 offset:53248\n"
    "ds_read_b128 v[140:143], v56 offset:55296\n"
    "global_load_lds_dwordx4 v47, s[30:31] offset:1024\n"
    "s_bfe_u32 s66, s28, 0x80000\n"
    "s_mul_i32 s66, s66, 53248\n"
    "s_add_u32 s30, s6, s66\n"
    "s_addc_u32 s31, s7, 0\n"
    "ds_read_b64_tr_b16 v[144:145], v76 offset:49152\n"
    "ds_read_b64_tr_b16 v[146:147], v76 offset:50176\n"
    "ds_read_b64_tr_b16 v[148:149], v77 offset:49152\n"
    "ds_read_b64_tr_b16 v[150:151], v77 offset:50176\n"
    "s_add_u32 m0, s34, 0\n"
    "s_nop 0\n"
    "global_load_lds_dwordx4 v52, s[36:37] offset:0\n"
    "ds_read_b64_tr_b16 v[152:153], v78 offset:49152\n"
    "ds_read_b64_tr_b16 v[154:155], v78 offset:50176\n"
    "ds_read_b64_tr_b16 v[156:157], v79 offset:49152\n"
    "ds_read_b64_tr_b16 v[158:159], v79 offset:50176\n"
    "global_load_lds_dwordx4 v53, s[36:37] offset:1024\n"
    "ds_read_b128 v[160:163], v57 offset:49152\n"
    "ds_read_b128 v[164:167], v57 offset:51200\n"
    "ds_read_b128 v[168:171], v57 offset:53248\n"
    "ds_read_b128 v[172:175], v57 offset:55296\n"
    "global_load_lds_dwordx4 v54, s[36:37] offset:2048\n"
    "ds_read_b64_tr_b16 v[176:177], v76 offset:57344\n"
    "ds_read_b64_tr_b16 v[178:179], v76 offset:58368\n"
    "ds_read_b64_tr_b16 v[180:181], v77 offset:57344\n"
    "ds_read_b64_tr_b16 v[182:183], v77 offset:58368\n"
    "global_load_lds_dwordx4 v55, s[36:37] offset:3072\n"
    "ds_read_b64_tr_b16 v[184:185], v78 offset:57344\n"
    "ds_read_b64_tr_b16 v[186:187], v78 offset:58368\n"
    "ds_read_b64_tr_b16 v[188:189], v79 offset:57344\n"
    "ds_read_b64_tr_b16 v[190:191], v79 offset:58368\n"
    "s_waitcnt vmcnt(10) lgkmcnt(0)\n"
    "s_barrier\n"
    "v_mfma_f32_16x16x32_bf16 v[192:195], v[144:147], v[128:131], v[192:195]\n"
    "v_lshlrev_b32 v101, 7, v101\n"
    "v_mfma_f32_16x16x32_bf16 v[192:195], v[176:179], v[160:163], v[192:195]\n"
    "v_add_u32_dpp v52, v100, v48 quad_perm:[0,0,0,0] row_mask:0xf bank_mask:0xf\n"
    "v_mfma_f32_16x16x32_bf16 v[196:199], v[176:179], v[164:167], v[196:199]\n"
    "v_mfma_f32_16x16x32_bf16 v[196:199], v[144:147], v[132:135], v[196:199]\n"
    "v_add_u32_dpp v53, v100, v49 quad_perm:[1,1,1,1] row_mask:0xf bank_mask:0xf\n"
    "v_mfma_f32_16x16x32_bf16 v[200:203], v[144:147], v[136:139], v[200:203]\n"
    "v_mfma_f32_16x16x32_bf16 v[200:203], v[176:179], v[168:171], v[200:203]\n"
    "v_add_u32_dpp v54, v100, v50 quad_perm:[2,2,2,2] row_mask:0xf bank_mask:0xf\n"
    "v_mfma_f32_16x16x32_bf16 v[204:207], v[176:179], v[172:175], v[204:207]\n"
    "v_mfma_f32_16x16x32_bf16 v[204:207], v[144:147], v[140:143], v[204:207]\n"
    "v_add_u32_dpp v55, v100, v51 quad_perm:[3,3,3,3] row_mask:0xf bank_mask:0xf\n"
    "v_mfma_f32_16x16x32_bf16 v[220:223], v[148:151], v[140:143], v[220:223]\n"
    "v_mfma_f32_16x16x32_bf16 v[220:223], v[180:183], v[172:175], v[220:223]\n"
    "s_cmp_lt_u32 s20, 4\n"
    "s_cselect_b32 s40, -1, 0\n"
    "v_mfma_f32_16x16x32_bf16 v[216:219], v[180:183], v[168:171], v[216:219]\n"
    "v_mfma_f32_16x16x32_bf16 v[216:219], v[148:151], v[136:139], v[216:219]\n"
    "v_lshlrev_b32 v28, 2, v0\n"
    "v_mfma_f32_16x16x32_bf16 v[212:215], v[148:151], v[132:135], v[212:215]\n"
    "v_mfma_f32_16x16x32_bf16 v[212:215], v[180:183], v[164:167], v[212:215]\n"
    "v_mov_b32 v29, s40\n"
    "v_mfma_f32_16x16x32_bf16 v[208:211], v[180:183], v[160:163], v[208:211]\n"
    "v_mfma_f32_16x16x32_bf16 v[208:211], v[148:151], v[128:131], v[208:211]\n"
    "v_add_u32 v28, 149504, v28\n"
    "v_mfma_f32_16x16x32_bf16 v[224:227], v[152:155], v[128:131], v[224:227]\n"
    "v_mfma_f32_16x16x32_bf16 v[224:227], v[184:187], v[160:163], v[224:227]\n"
    "s_mul_i32 s47, s3, 5042\n"
    "v_mfma_f32_16x16x32_bf16 v[228:231], v[184:187], v[164:167], v[228:231]\n"
    "v_mfma_f32_16x16x32_bf16 v[228:231], v[152:155], v[132:135], v[228:231]\n"
    "s_lshr_b32 s47, s47, 16\n"
    "v_mfma_f32_16x16x32_bf16 v[232:235], v[152:155], v[136:139], v[232:235]\n"
    "v_mfma_f32_16x16x32_bf16 v[232:235], v[184:187], v[168:171], v[232:235]\n"
    "s_lshr_b32 s21, s20, 2\n"
    "v_mfma_f32_16x16x32_bf16 v[236:239], v[184:187], v[172:175], v[236:239]\n"
    "v_mfma_f32_16x16x32_bf16 v[236:239], v[152:155], v[140:143], v[236:239]\n"
    "s_lshl1_add_u32 s22, s21, s20\n"
    "v_mfma_f32_16x16x32_bf16 v[252:255], v[156:159], v[140:143], v[252:255]\n"
    "v_mfma_f32_16x16x32_bf16 v[252:255], v[188:191], v[172:175], v[252:255]\n"
    "s_and_b32 s22, s22, 3\n"
    "v_mfma_f32_16x16x32_bf16 v[248:251], v[188:191], v[168:171], v[248:251]\n"
    "v_mfma_f32_16x16x32_bf16 v[248:251], v[156:159], v[136:139], v[248:251]\n"
    "s_lshl_b32 s58, s21, 8\n"
    "v_mfma_f32_16x16x32_bf16 v[244:247], v[156:159], v[132:135], v[244:247]\n"
    "v_mfma_f32_16x16x32_bf16 v[244:247], v[188:191], v[164:167], v[244:247]\n"
    "s_add_u32 s58, s58, 150528\n"
    "v_mfma_f32_16x16x32_bf16 v[240:243], v[188:191], v[160:163], v[240:243]\n"
    "v_mfma_f32_16x16x32_bf16 v[240:243], v[156:159], v[128:131], v[240:243]\n"
    "s_waitcnt vmcnt(6)\n"
    "s_barrier\n"
    "s_add_u32 m0, s35, 49152\n"
    "s_nop 0\n"
    "global_load_lds_dwordx4 v46, s[30:31] offset:0\n"
    "ds_read_b128 v[128:131], v58 offset:0\n"
    "ds_read_b128 v[132:135], v58 offset:2048\n"
    "ds_read_b128 v[136:139], v58 offset:4096\n"
    "ds_read_b128 v[140:143], v58 offset:6144\n"
    "global_load_lds_dwordx4 v47, s[30:31] offset:1024\n"
    "s_add_u32 s30, s30, 212992\n"
    "s_addc_u32 s31, s31, 0\n"
    "ds_read_b64_tr_b16 v[144:145], v80 offset:0\n"
    "ds_read_b64_tr_b16 v[146:147], v80 offset:1024\n"
    "ds_read_b64_tr_b16 v[148:149], v81 offset:0\n"
    "ds_read_b64_tr_b16 v[150:151], v81 offset:1024\n"
    "s_add_u32 m0, s34, 49152\n"
    "s_nop 0\n"
    "global_load_lds_dwordx4 v52, s[36:37] offset:0\n"
    "ds_read_b64_tr_b16 v[152:153], v82 offset:0\n"
    "ds_read_b64_tr_b16 v[154:155], v82 offset:1024\n"
    "ds_read_b64_tr_b16 v[156:157], v83 offset:0\n"
    "ds_read_b64_tr_b16 v[158:159], v83 offset:1024\n"
    "global_load_lds_dwordx4 v53, s[36:37] offset:1024\n"
    "ds_read_b128 v[160:163], v59 offset:0\n"
    "ds_read_b128 v[164:167], v59 offset:2048\n"
    "ds_read_b128 v[168:171], v59 offset:4096\n"
    "ds_read_b128 v[172:175], v59 offset:6144\n"
    "global_load_lds_dwordx4 v54, s[36:37] offset:2048\n"
    "ds_read_b64_tr_b16 v[176:177], v80 offset:8192\n"
    "ds_read_b64_tr_b16 v[178:179], v80 offset:9216\n"
    "ds_read_b64_tr_b16 v[180:181], v81 offset:8192\n"
    "ds_read_b64_tr_b16 v[182:183], v81 offset:9216\n"
    "global_load_lds_dwordx4 v55, s[36:37] offset:3072\n"
    "ds_read_b64_tr_b16 v[184:185], v82 offset:8192\n"
    "ds_read_b64_tr_b16 v[186:187], v82 offset:9216\n"
    "ds_read_b64_tr_b16 v[188:189], v83 offset:8192\n"
    "ds_read_b64_tr_b16 v[190:191], v83 offset:9216\n"
    "s_waitcnt vmcnt(10) lgkmcnt(0)\n"
    "s_barrier\n"
    "v_mfma_f32_16x16x32_bf16 v[192:195], v[144:147], v[128:131], v[192:195]\n"
    "v_lshlrev_b32 v102, 7, v102\n"
    "v_mfma_f32_16x16x32_bf16 v[192:195], v[176:179], v[160:163], v[192:195]\n"
    "v_add_u32_dpp v52, v101, v48 quad_perm:[0,0,0,0] row_mask:0xf bank_mask:0xf\n"
    "v_mfma_f32_16x16x32_bf16 v[196:199], v[176:179], v[164:167], v[196:199]\n"
    "v_mfma_f32_16x16x32_bf16 v[196:199], v[144:147], v[132:135], v[196:199]\n"
    "v_add_u32_dpp v53, v101, v49 quad_perm:[1,1,1,1] row_mask:0xf bank_mask:0xf\n"
    "v_mfma_f32_16x16x32_bf16 v[200:203], v[144:147], v[136:139], v[200:203]\n"
    "v_mfma_f32_16x16x32_bf16 v[200:203], v[176:179], v[168:171], v[200:203]\n"
    "v_add_u32_dpp v54, v101, v50 quad_perm:[2,2,2,2] row_mask:0xf bank_mask:0xf\n"
    "v_mfma_f32_16x16x32_bf16 v[204:207], v[176:179], v[172:175], v[204:207]\n"
    "v_mfma_f32_16x16x32_bf16 v[204:207], v[144:147], v[140:143], v[204:207]\n"
    "v_add_u32_dpp v55, v101, v51 quad_perm:[3,3,3,3] row_mask:0xf bank_mask:0xf\n"
    "v_mfma_f32_16x16x32_bf16 v[220:223], v[148:151], v[140:143], v[220:223]\n"
    "v_mfma_f32_16x16x32_bf16 v[220:223], v[180:183], v[172:175], v[220:223]\n"
    "s_lshl_b32 s54, s47, 9\n"
    "v_mfma_f32_16x16x32_bf16 v[216:219], v[180:183], v[168:171], v[216:219]\n"
    "v_mfma_f32_16x16x32_bf16 v[216:219], v[148:151], v[136:139], v[216:219]\n"
    "s_add_u32 s54, s54, 2\n"
    "v_mfma_f32_16x16x32_bf16 v[212:215], v[148:151], v[132:135], v[212:215]\n"
    "v_mfma_f32_16x16x32_bf16 v[212:215], v[180:183], v[164:167], v[212:215]\n"
    "s_lshl_b32 s40, s47, 7\n"
    "v_mfma_f32_16x16x32_bf16 v[208:211], v[180:183], v[160:163], v[208:211]\n"
    "v_mfma_f32_16x16x32_bf16 v[208:211], v[148:151], v[128:131], v[208:211]\n"
    "s_add_u32 s40, s40, 32768\n"
    "v_mfma_f32_16x16x32_bf16 v[224:227], v[152:155], v[128:131], v[224:227]\n"
    "v_mfma_f32_16x16x32_bf16 v[224:227], v[184:187], v[160:163], v[224:227]\n"
    "s_add_u32 s52, s10, s40\n"
    "s_addc_u32 s53, s11, 0\n"
    "v_mfma_f32_16x16x32_bf16 v[228:231], v[184:187], v[164:167], v[228:231]\n"
    "v_mfma_f32_16x16x32_bf16 v[228:231], v[152:155], v[132:135], v[228:231]\n"
    "s_mov_b32 s57, 0x7fff7fff\n"
    "v_mfma_f32_16x16x32_bf16 v[232:235], v[152:155], v[136:139], v[232:235]\n"
    "v_mfma_f32_16x16x32_bf16 v[232:235], v[184:187], v[168:171], v[232:235]\n"
    "s_mov_b32 s56, 0\n"
    "v_mfma_f32_16x16x32_bf16 v[236:239], v[184:187], v[172:175], v[236:239]\n"
    "v_mfma_f32_16x16x32_bf16 v[236:239], v[152:155], v[140:143], v[236:239]\n"
    "s_mov_b32 s27, 0\n"
    "v_mfma_f32_16x16x32_bf16 v[252:255], v[156:159], v[140:143], v[252:255]\n"
    "v_mfma_f32_16x16x32_bf16 v[252:255], v[188:191], v[172:175], v[252:255]\n"
    "s_mov_b32 s55, 9\n"
    "v_mfma_f32_16x16x32_bf16 v[248:251], v[188:191], v[168:171], v[248:251]\n"
    "v_mfma_f32_16x16x32_bf16 v[248:251], v[156:159], v[136:139], v[248:251]\n"
    "s_mov_b32 s44, 0\n"
    "v_mfma_f32_16x16x32_bf16 v[244:247], v[156:159], v[132:135], v[244:247]\n"
    "v_mfma_f32_16x16x32_bf16 v[244:247], v[188:191], v[164:167], v[244:247]\n"
    "s_movk_i32 s46, 17920\n"
    "v_mfma_f32_16x16x32_bf16 v[240:243], v[188:191], v[160:163], v[240:243]\n"
    "v_mfma_f32_16x16x32_bf16 v[240:243], v[156:159], v[128:131], v[240:243]\n"
    "s_waitcnt vmcnt(6)\n"
    "s_barrier\n"
    "ds_write_b32 v28, v29\n"
    "s_add_u32 m0, s35, 98304\n"
    "s_nop 0\n"
    "global_load_lds_dwordx4 v46, s[30:31] offset:0\n"
    "ds_read_b128 v[128:131], v56 offset:0\n"
    "ds_read_b128 v[132:135], v56 offset:2048\n"
    "ds_read_b128 v[136:139], v56 offset:4096\n"
    "ds_read_b128 v[140:143], v56 offset:6144\n"
    "global_load_lds_dwordx4 v47, s[30:31] offset:1024\n"
    "s_add_u32 s30, s30, 212992\n"
    "s_addc_u32 s31, s31, 0\n"
    "ds_read_b64_tr_b16 v[144:145], v76 offset:0\n"
    "ds_read_b64_tr_b16 v[146:147], v76 offset:1024\n"
    "ds_read_b64_tr_b16 v[148:149], v77 offset:0\n"
    "ds_read_b64_tr_b16 v[150:151], v77 offset:1024\n"
    "s_add_u32 m0, s34, 98304\n"
    "s_nop 0\n"
    "global_load_lds_dwordx4 v52, s[36:37] offset:0\n"
    "ds_read_b64_tr_b16 v[152:153], v78 offset:0\n"
    "ds_read_b64_tr_b16 v[154:155], v78 offset:1024\n"
    "ds_read_b64_tr_b16 v[156:157], v79 offset:0\n"
    "ds_read_b64_tr_b16 v[158:159], v79 offset:1024\n"
    "global_load_lds_dwordx4 v53, s[36:37] offset:1024\n"
    "ds_read_b128 v[160:163], v57 offset:0\n"
    "ds_read_b128 v[164:167], v57 offset:2048\n"
    "ds_read_b128 v[168:171], v57 offset:4096\n"
    "ds_read_b128 v[172:175], v57 offset:6144\n"
    "global_load_lds_dwordx4 v54, s[36:37] offset:2048\n"
    "ds_read_b64_tr_b16 v[176:177], v76 offset:8192\n"
    "ds_read_b64_tr_b16 v[178:179], v76 offset:9216\n"
    "ds_read_b64_tr_b16 v[180:181], v77 offset:8192\n"
    "ds_read_b64_tr_b16 v[182:183], v77 offset:9216\n"
    "global_load_lds_dwordx4 v55, s[36:37] offset:3072\n"
    "ds_read_b64_tr_b16 v[184:185], v78 offset:8192\n"
    "ds_read_b64_tr_b16 v[186:187], v78 offset:9216\n"
    "ds_read_b64_tr_b16 v[188:189], v79 offset:8192\n"
    "ds_read_b64_tr_b16 v[190:191], v79 offset:9216\n"
    "s_waitcnt vmcnt(10) lgkmcnt(0)\n"
    "s_barrier\n"
    "v_mfma_f32_16x16x32_bf16 v[192:195], v[144:147], v[128:131], v[192:195]\n"
    "v_lshlrev_b32 v103, 7, v103\n"
    "v_mfma_f32_16x16x32_bf16 v[192:195], v[176:179], v[160:163], v[192:195]\n"
    "v_add_u32_dpp v52, v102, v48 quad_perm:[0,0,0,0] row_mask:0xf bank_mask:0xf\n"
    "v_mfma_f32_16x16x32_bf16 v[196:199], v[176:179], v[164:167], v[196:199]\n"
    "v_mfma_f32_16x16x32_bf16 v[196:199], v[144:147], v[132:135], v[196:199]\n"
    "v_add_u32_dpp v53, v102, v49 quad_perm:[1,1,1,1] row_mask:0xf bank_mask:0xf\n"
    "v_mfma_f32_16x16x32_bf16 v[200:203], v[144:147], v[136:139], v[200:203]\n"
    "v_mfma_f32_16x16x32_bf16 v[200:203], v[176:179], v[168:171], v[200:203]\n"
    "v_add_u32_dpp v54, v102, v50 quad_perm:[2,2,2,2] row_mask:0xf bank_mask:0xf\n"
    "v_mfma_f32_16x16x32_bf16 v[204:207], v[176:179], v[172:175], v[204:207]\n"
    "v_mfma_f32_16x16x32_bf16 v[204:207], v[144:147], v[140:143], v[204:207]\n"
    "v_add_u32_dpp v55, v102, v51 quad_perm:[3,3,3,3] row_mask:0xf bank_mask:0xf\n"
    "v_mfma_f32_16x16x32_bf16 v[220:223], v[148:151], v[140:143], v[220:223]\n"
    "v_mfma_f32_16x16x32_bf16 v[220:223], v[180:183], v[172:175], v[220:223]\n"
    "v_lshrrev_b32 v17, 3, v1\n"
    "v_mfma_f32_16x16x32_bf16 v[216:219], v[180:183], v[168:171], v[216:219]\n"
    "v_mfma_f32_16x16x32_bf16 v[216:219], v[148:151], v[136:139], v[216:219]\n"
    "v_mul_u32_u24 v17, 12, v17\n"
    "v_mfma_f32_16x16x32_bf16 v[212:215], v[148:151], v[132:135], v[212:215]\n"
    "v_mfma_f32_16x16x32_bf16 v[212:215], v[180:183], v[164:167], v[212:215]\n"
    "v_and_b32 v19, 7, v1\n"
    "v_mfma_f32_16x16x32_bf16 v[208:211], v[180:183], v[160:163], v[208:211]\n"
    "v_mfma_f32_16x16x32_bf16 v[208:211], v[148:151], v[128:131], v[208:211]\n"
    "v_lshlrev_b32 v19, 4, v19\n"
    "v_mfma_f32_16x16x32_bf16 v[224:227], v[152:155], v[128:131], v[224:227]\n"
    "v_mfma_f32_16x16x32_bf16 v[224:227], v[184:187], v[160:163], v[224:227]\n"
    "v_lshrrev_b32 v18, 5, v1\n"
    "v_mfma_f32_16x16x32_bf16 v[228:231], v[184:187], v[164:167], v[228:231]\n"
    "v_mfma_f32_16x16x32_bf16 v[228:231], v[152:155], v[132:135], v[228:231]\n"
    "v_lshlrev_b32 v18, 2, v18\n"
    "v_mfma_f32_16x16x32_bf16 v[232:235], v[152:155], v[136:139], v[232:235]\n"
    "v_mfma_f32_16x16x32_bf16 v[232:235], v[184:187], v[168:171], v[232:235]\n"
    "v_and_b32 v20, 31, v1\n"
    "v_mfma_f32_16x16x32_bf16 v[236:239], v[184:187], v[172:175], v[236:239]\n"
    "v_mfma_f32_16x16x32_bf16 v[236:239], v[152:155], v[140:143], v[236:239]\n"
    "v_lshlrev_b32 v20, 2, v20\n"
    "v_mfma_f32_16x16x32_bf16 v[252:255], v[156:159], v[140:143], v[252:255]\n"
    "v_mfma_f32_16x16x32_bf16 v[252:255], v[188:191], v[172:175], v[252:255]\n"
    "s_lshl2_add_u32 s40, s22, s58\n"
    "v_mfma_f32_16x16x32_bf16 v[248:251], v[188:191], v[168:171], v[248:251]\n"
    "v_mfma_f32_16x16x32_bf16 v[248:251], v[156:159], v[136:139], v[248:251]\n"
    "s_sub_u32 s40, s40, 20\n"
    "v_mfma_f32_16x16x32_bf16 v[244:247], v[156:159], v[132:135], v[244:247]\n"
    "v_mfma_f32_16x16x32_bf16 v[244:247], v[188:191], v[164:167], v[244:247]\n"
    "v_mov_b32 v25, s40\n"
    "v_mfma_f32_16x16x32_bf16 v[240:243], v[188:191], v[160:163], v[240:243]\n"
    "v_mfma_f32_16x16x32_bf16 v[240:243], v[156:159], v[128:131], v[240:243]\n"
    "s_waitcnt vmcnt(6)\n"
    "s_barrier\n"
    "s_add_u32 m0, s35, 0\n"
    "s_nop 0\n"
    "global_load_lds_dwordx4 v46, s[30:31] offset:0\n"
    "ds_read_b128 v[128:131], v56 offset:49152\n"
    "ds_read_b128 v[132:135], v56 offset:51200\n"
    "ds_read_b128 v[136:139], v56 offset:53248\n"
    "ds_read_b128 v[140:143], v56 offset:55296\n"
    "global_load_lds_dwordx4 v47, s[30:31] offset:1024\n"
    "s_add_u32 s30, s30, 212992\n"
    "s_addc_u32 s31, s31, 0\n"
    "ds_read_b64_tr_b16 v[144:145], v76 offset:49152\n"
    "ds_read_b64_tr_b16 v[146:147], v76 offset:50176\n"
    "ds_read_b64_tr_b16 v[148:149], v77 offset:49152\n"
    "ds_read_b64_tr_b16 v[150:151], v77 offset:50176\n"
    "s_add_u32 m0, s34, 0\n"
    "s_nop 0\n"
    "global_load_lds_dwordx4 v52, s[36:37] offset:0\n"
    "ds_read_b64_tr_b16 v[152:153], v78 offset:49152\n"
    "ds_read_b64_tr_b16 v[154:155], v78 offset:50176\n"
    "ds_read_b64_tr_b16 v[156:157], v79 offset:49152\n"
    "ds_read_b64_tr_b16 v[158:159], v79 offset:50176\n"
    "global_load_lds_dwordx4 v53, s[36:37] offset:1024\n"
    "ds_read_b128 v[160:163], v57 offset:49152\n"
    "ds_read_b128 v[164:167], v57 offset:51200\n"
    "ds_read_b128 v[168:171], v57 offset:53248\n"
    "ds_read_b128 v[172:175], v57 offset:55296\n"
    "global_load_lds_dwordx4 v54, s[36:37] offset:2048\n"
    "ds_read_b64_tr_b16 v[176:177], v76 offset:57344\n"
    "ds_read_b64_tr_b16 v[178:179], v76 offset:58368\n"
    "ds_read_b64_tr_b16 v[180:181], v77 offset:57344\n"
    "ds_read_b64_tr_b16 v[182:183], v77 offset:58368\n"
    "global_load_lds_dwordx4 v55, s[36:37] offset:3072\n"
    "ds_read_b64_tr_b16 v[184:185], v78 offset:57344\n"
    "ds_read_b64_tr_b16 v[186:187], v78 offset:58368\n"
    "ds_read_b64_tr_b16 v[188:189], v79 offset:57344\n"
    "ds_read_b64_tr_b16 v[190:191], v79 offset:58368\n"
    "s_waitcnt vmcnt(10) lgkmcnt(0)\n"
    "s_barrier\n"
    "v_mfma_f32_16x16x32_bf16 v[192:195], v[144:147], v[128:131], v[192:195]\n"
    "v_lshlrev_b32 v104, 7, v104\n"
    "v_mfma_f32_16x16x32_bf16 v[192:195], v[176:179], v[160:163], v[192:195]\n"
    "v_add_u32_dpp v52, v103, v48 quad_perm:[0,0,0,0] row_mask:0xf bank_mask:0xf\n"
    "v_mfma_f32_16x16x32_bf16 v[196:199], v[176:179], v[164:167], v[196:199]\n"
    "v_mfma_f32_16x16x32_bf16 v[196:199], v[144:147], v[132:135], v[196:199]\n"
    "v_add_u32_dpp v53, v103, v49 quad_perm:[1,1,1,1] row_mask:0xf bank_mask:0xf\n"
    "v_mfma_f32_16x16x32_bf16 v[200:203], v[144:147], v[136:139], v[200:203]\n"
    "v_mfma_f32_16x16x32_bf16 v[200:203], v[176:179], v[168:171], v[200:203]\n"
    "v_add_u32_dpp v54, v103, v50 quad_perm:[2,2,2,2] row_mask:0xf bank_mask:0xf\n"
    "v_mfma_f32_16x16x32_bf16 v[204:207], v[176:179], v[172:175], v[204:207]\n"
    "v_mfma_f32_16x16x32_bf16 v[204:207], v[144:147], v[140:143], v[204:207]\n"
    "v_add_u32_dpp v55, v103, v51 quad_perm:[3,3,3,3] row_mask:0xf bank_mask:0xf\n"
    "v_mfma_f32_16x16x32_bf16 v[220:223], v[148:151], v[140:143], v[220:223]\n"
    "v_mfma_f32_16x16x32_bf16 v[220:223], v[180:183], v[172:175], v[220:223]\n"
    "s_mul_i32 s43, s21, 49152\n"
    "v_mfma_f32_16x16x32_bf16 v[216:219], v[180:183], v[168:171], v[216:219]\n"
    "v_mfma_f32_16x16x32_bf16 v[216:219], v[148:151], v[136:139], v[216:219]\n"
    "v_lshrrev_b32 v12, 4, v1\n"
    "v_mfma_f32_16x16x32_bf16 v[212:215], v[148:151], v[132:135], v[212:215]\n"
    "v_mfma_f32_16x16x32_bf16 v[212:215], v[180:183], v[164:167], v[212:215]\n"
    "v_bfe_u32 v13, v1, 2, 2\n"
    "v_mfma_f32_16x16x32_bf16 v[208:211], v[180:183], v[160:163], v[208:211]\n"
    "v_mfma_f32_16x16x32_bf16 v[208:211], v[148:151], v[128:131], v[208:211]\n"
    "v_and_b32 v21, 3, v1\n"
    "v_mfma_f32_16x16x32_bf16 v[224:227], v[152:155], v[128:131], v[224:227]\n"
    "v_mfma_f32_16x16x32_bf16 v[224:227], v[184:187], v[160:163], v[224:227]\n"
    "s_lshl_b32 s40, s22, 5\n"
    "v_mfma_f32_16x16x32_bf16 v[228:231], v[184:187], v[164:167], v[228:231]\n"
    "v_mfma_f32_16x16x32_bf16 v[228:231], v[152:155], v[132:135], v[228:231]\n"
    "s_add_u32 s40, s40, s43\n"
    "v_mfma_f32_16x16x32_bf16 v[232:235], v[152:155], v[136:139], v[232:235]\n"
    "v_mfma_f32_16x16x32_bf16 v[232:235], v[184:187], v[168:171], v[232:235]\n"
    "v_lshl_add_u32 v21, v21, 3, s40\n"
    "v_mfma_f32_16x16x32_bf16 v[236:239], v[184:187], v[172:175], v[236:239]\n"
    "v_mfma_f32_16x16x32_bf16 v[236:239], v[152:155], v[140:143], v[236:239]\n"
    "s_movk_i32 s41, 160\n"
    "v_mfma_f32_16x16x32_bf16 v[252:255], v[156:159], v[140:143], v[252:255]\n"
    "v_mfma_f32_16x16x32_bf16 v[252:255], v[188:191], v[172:175], v[252:255]\n"
    "v_add_u32 v22, v12, v13\n"
    "v_mfma_f32_16x16x32_bf16 v[248:251], v[188:191], v[168:171], v[248:251]\n"
    "v_mfma_f32_16x16x32_bf16 v[248:251], v[156:159], v[136:139], v[248:251]\n"
    "v_and_b32 v22, 3, v22\n"
    "v_mfma_f32_16x16x32_bf16 v[244:247], v[156:159], v[132:135], v[244:247]\n"
    "v_mfma_f32_16x16x32_bf16 v[244:247], v[188:191], v[164:167], v[244:247]\n"
    "v_mad_u32_u24 v4, v22, s41, v21\n"
    "v_mfma_f32_16x16x32_bf16 v[240:243], v[188:191], v[160:163], v[240:243]\n"
    "v_mfma_f32_16x16x32_bf16 v[240:243], v[156:159], v[128:131], v[240:243]\n"
    "s_waitcnt vmcnt(6)\n"
    "s_barrier\n"
    "s_add_u32 m0, s35, 49152\n"
    "s_nop 0\n"
    "global_load_lds_dwordx4 v46, s[30:31] offset:0\n"
    "ds_read_b128 v[128:131], v58 offset:0\n"
    "ds_read_b128 v[132:135], v58 offset:2048\n"
    "ds_read_b128 v[136:139], v58 offset:4096\n"
    "ds_read_b128 v[140:143], v58 offset:6144\n"
    "global_load_lds_dwordx4 v47, s[30:31] offset:1024\n"
    "s_bfe_u32 s66, s28, 0x80008\n"
    "s_mul_i32 s66, s66, 53248\n"
    "s_add_u32 s30, s6, s66\n"
    "s_addc_u32 s31, s7, 0\n"
    "ds_read_b64_tr_b16 v[144:145], v80 offset:0\n"
    "ds_read_b64_tr_b16 v[146:147], v80 offset:1024\n"
    "ds_read_b64_tr_b16 v[148:149], v81 offset:0\n"
    "ds_read_b64_tr_b16 v[150:151], v81 offset:1024\n"
    "s_add_u32 m0, s34, 49152\n"
    "s_nop 0\n"
    "global_load_lds_dwordx4 v52, s[36:37] offset:0\n"
    "ds_read_b64_tr_b16 v[152:153], v82 offset:0\n"
    "ds_read_b64_tr_b16 v[154:155], v82 offset:1024\n"
    "ds_read_b64_tr_b16 v[156:157], v83 offset:0\n"
    "ds_read_b64_tr_b16 v[158:159], v83 offset:1024\n"
    "global_load_lds_dwordx4 v53, s[36:37] offset:1024\n"
    "ds_read_b128 v[160:163], v59 offset:0\n"
    "ds_read_b128 v[164:167], v59 offset:2048\n"
    "ds_read_b128 v[168:171], v59 offset:4096\n"
    "ds_read_b128 v[172:175], v59 offset:6144\n"
    "global_load_lds_dwordx4 v54, s[36:37] offset:2048\n"
    "ds_read_b64_tr_b16 v[176:177], v80 offset:8192\n"
    "ds_read_b64_tr_b16 v[178:179], v80 offset:9216\n"
    "ds_read_b64_tr_b16 v[180:181], v81 offset:8192\n"
    "ds_read_b64_tr_b16 v[182:183], v81 offset:9216\n"
    "global_load_lds_dwordx4 v55, s[36:37] offset:3072\n"
    "ds_read_b64_tr_b16 v[184:185], v82 offset:8192\n"
    "ds_read_b64_tr_b16 v[186:187], v82 offset:9216\n"
    "ds_read_b64_tr_b16 v[188:189], v83 offset:8192\n"
    "ds_read_b64_tr_b16 v[190:191], v83 offset:9216\n"
    "s_waitcnt vmcnt(10) lgkmcnt(0)\n"
    "s_barrier\n"
    "v_mfma_f32_16x16x32_bf16 v[192:195], v[144:147], v[128:131], v[192:195]\n"
    "v_lshlrev_b32 v105, 7, v105\n"
    "v_mfma_f32_16x16x32_bf16 v[192:195], v[176:179], v[160:163], v[192:195]\n"
    "v_add_u32_dpp v52, v104, v48 quad_perm:[0,0,0,0] row_mask:0xf bank_mask:0xf\n"
    "v_mfma_f32_16x16x32_bf16 v[196:199], v[176:179], v[164:167], v[196:199]\n"
    "v_mfma_f32_16x16x32_bf16 v[196:199], v[144:147], v[132:135], v[196:199]\n"
    "v_add_u32_dpp v53, v104, v49 quad_perm:[1,1,1,1] row_mask:0xf bank_mask:0xf\n"
    "v_mfma_f32_16x16x32_bf16 v[200:203], v[144:147], v[136:139], v[200:203]\n"
    "v_mfma_f32_16x16x32_bf16 v[200:203], v[176:179], v[168:171], v[200:203]\n"
    "v_add_u32_dpp v54, v104, v50 quad_perm:[2,2,2,2] row_mask:0xf bank_mask:0xf\n"
    "v_mfma_f32_16x16x32_bf16 v[204:207], v[176:179], v[172:175], v[204:207]\n"
    "v_mfma_f32_16x16x32_bf16 v[204:207], v[144:147], v[140:143], v[204:207]\n"
    "v_add_u32_dpp v55, v104, v51 quad_perm:[3,3,3,3] row_mask:0xf bank_mask:0xf\n"
    "v_mfma_f32_16x16x32_bf16 v[220:223], v[148:151], v[140:143], v[220:223]\n"
    "v_mfma_f32_16x16x32_bf16 v[220:223], v[180:183], v[172:175], v[220:223]\n"
    "v_cmp_gt_u32 vcc, 2, v13\n"
    "v_mfma_f32_16x16x32_bf16 v[216:219], v[180:183], v[168:171], v[216:219]\n"
    "v_add_u32 v24, 22, v13\n"
    "v_mfma_f32_16x16x32_bf16 v[216:219], v[148:151], v[136:139], v[216:219]\n"
    "v_mfma_f32_16x16x32_bf16 v[212:215], v[148:151], v[132:135], v[212:215]\n"
    "v_lshlrev_b32 v13, 3, v13\n"
    "v_mfma_f32_16x16x32_bf16 v[212:215], v[180:183], v[164:167], v[212:215]\n"
    "v_mfma_f32_16x16x32_bf16 v[208:211], v[180:183], v[160:163], v[208:211]\n"
    "v_cndmask_b32 v22, v24, v22, vcc\n"
    "v_mfma_f32_16x16x32_bf16 v[208:211], v[148:151], v[128:131], v[208:211]\n"
    "v_mfma_f32_16x16x32_bf16 v[224:227], v[152:155], v[128:131], v[224:227]\n"
    "v_mad_u32_u24 v3, v22, s41, v21\n"
    "v_mfma_f32_16x16x32_bf16 v[224:227], v[184:187], v[160:163], v[224:227]\n"
    "v_mfma_f32_16x16x32_bf16 v[228:231], v[184:187], v[164:167], v[228:231]\n"
    "v_cmp_eq_u32 vcc, 3, v12\n"
    "v_mfma_f32_16x16x32_bf16 v[228:231], v[152:155], v[132:135], v[228:231]\n"
    "v_mfma_f32_16x16x32_bf16 v[232:235], v[152:155], v[136:139], v[232:235]\n"
    "v_xor_b32 v22, 0, v12\n"
    "v_mfma_f32_16x16x32_bf16 v[232:235], v[184:187], v[168:171], v[232:235]\n"
    "v_and_b32 v22, 1, v22\n"
    "v_mfma_f32_16x16x32_bf16 v[236:239], v[184:187], v[172:175], v[236:239]\n"
    "v_mfma_f32_16x16x32_bf16 v[236:239], v[152:155], v[140:143], v[236:239]\n"
    "v_lshlrev_b32 v22, 1, v22\n"
    "v_mfma_f32_16x16x32_bf16 v[252:255], v[156:159], v[140:143], v[252:255]\n"
    "v_mfma_f32_16x16x32_bf16 v[252:255], v[188:191], v[172:175], v[252:255]\n"
    "v_lshl_add_u32 v22, v12, 3, v22\n"
    "v_mfma_f32_16x16x32_bf16 v[248:251], v[188:191], v[168:171], v[248:251]\n"
    "v_mfma_f32_16x16x32_bf16 v[248:251], v[156:159], v[136:139], v[248:251]\n"
    "s_mov_b32 s40, 0x05040100\n"
    "v_mfma_f32_16x16x32_bf16 v[244:247], v[156:159], v[132:135], v[244:247]\n"
    "v_mfma_f32_16x16x32_bf16 v[244:247], v[188:191], v[164:167], v[244:247]\n"
    "v_bfe_u32 v24, s40, v13, 8\n"
    "v_mfma_f32_16x16x32_bf16 v[240:243], v[188:191], v[160:163], v[240:243]\n"
    "v_mfma_f32_16x16x32_bf16 v[240:243], v[156:159], v[128:131], v[240:243]\n"
    "s_waitcnt vmcnt(6)\n"
    "s_barrier\n"
    "s_add_u32 m0, s35, 98304\n"
    "s_nop 0\n"
    "global_load_lds_dwordx4 v46, s[30:31] offset:0\n"
    "ds_read_b128 v[128:131], v56 offset:0\n"
    "ds_read_b128 v[132:135], v56 offset:2048\n"
    "ds_read_b128 v[136:139], v56 offset:4096\n"
    "global_load_lds_dwordx4 v47, s[30:31] offset:1024\n"
    "s_add_u32 s30, s30, 212992\n"
    "s_addc_u32 s31, s31, 0\n"
    "ds_read_b128 v[140:143], v56 offset:6144\n"
    "ds_read_b64_tr_b16 v[144:145], v76 offset:0\n"
    "ds_read_b64_tr_b16 v[146:147], v76 offset:1024\n"
    "s_add_u32 m0, s34, 98304\n"
    "s_nop 0\n"
    "global_load_lds_dwordx4 v52, s[36:37] offset:0\n"
    "ds_read_b64_tr_b16 v[148:149], v77 offset:0\n"
    "ds_read_b64_tr_b16 v[150:151], v77 offset:1024\n"
    "ds_read_b64_tr_b16 v[152:153], v78 offset:0\n"
    "global_load_lds_dwordx4 v53, s[36:37] offset:1024\n"
    "ds_read_b64_tr_b16 v[154:155], v78 offset:1024\n"
    "ds_read_b64_tr_b16 v[156:157], v79 offset:0\n"
    "ds_read_b64_tr_b16 v[158:159], v79 offset:1024\n"
    "global_load_lds_dwordx4 v54, s[36:37] offset:2048\n"
    "ds_read_b128 v[160:163], v57 offset:0\n"
    "ds_read_b128 v[164:167], v57 offset:2048\n"
    "ds_read_b128 v[168:171], v57 offset:4096\n"
    "global_load_lds_dwordx4 v55, s[36:37] offset:3072\n"
    "ds_read_b128 v[172:175], v57 offset:6144\n"
    "ds_read_b64_tr_b16 v[176:177], v76 offset:8192\n"
    "ds_read_b64_tr_b16 v[178:179], v76 offset:9216\n"
    "s_mov_b32 m0, s64\n"
    "s_nop 0\n"
    "global_load_lds_dword v43, s[14:15]\n"
    "ds_read_b64_tr_b16 v[180:181], v77 offset:8192\n"
    "ds_read_b64_tr_b16 v[182:183], v77 offset:9216\n"
    "ds_read_b64_tr_b16 v[184:185], v78 offset:8192\n"
    "ds_read_b64_tr_b16 v[186:187], v78 offset:9216\n"
    "ds_read_b64_tr_b16 v[188:189], v79 offset:8192\n"
    "ds_read_b64_tr_b16 v[190:191], v79 offset:9216\n"
    "s_waitcnt vmcnt(11) lgkmcnt(0)\n"
    "s_barrier\n"
    "v_mfma_f32_16x16x32_bf16 v[192:195], v[144:147], v[128:131], v[192:195]\n"
    "v_lshlrev_b32 v106, 7, v106\n"
    "v_mfma_f32_16x16x32_bf16 v[192:195], v[176:179], v[160:163], v[192:195]\n"
    "v_add_u32_dpp v52, v105, v48 quad_perm:[0,0,0,0] row_mask:0xf bank_mask:0xf\n"
    "v_mfma_f32_16x16x32_bf16 v[196:199], v[176:179], v[164:167], v[196:199]\n"
    "v_mfma_f32_16x16x32_bf16 v[196:199], v[144:147], v[132:135], v[196:199]\n"
    "v_add_u32_dpp v53, v105, v49 quad_perm:[1,1,1,1] row_mask:0xf bank_mask:0xf\n"
    "v_mfma_f32_16x16x32_bf16 v[200:203], v[144:147], v[136:139], v[200:203]\n"
    "v_mfma_f32_16x16x32_bf16 v[200:203], v[176:179], v[168:171], v[200:203]\n"
    "v_add_u32_dpp v54, v105, v50 quad_perm:[2,2,2,2] row_mask:0xf bank_mask:0xf\n"
    "v_mfma_f32_16x16x32_bf16 v[204:207], v[176:179], v[172:175], v[204:207]\n"
    "v_mfma_f32_16x16x32_bf16 v[204:207], v[144:147], v[140:143], v[204:207]\n"
    "v_add_u32_dpp v55, v105, v51 quad_perm:[3,3,3,3] row_mask:0xf bank_mask:0xf\n"
    "v_mfma_f32_16x16x32_bf16 v[220:223], v[148:151], v[140:143], v[220:223]\n"
    "v_mfma_f32_16x16x32_bf16 v[220:223], v[180:183], v[172:175], v[220:223]\n"
    "v_add_u32 v22, v22, v24\n"
    "v_mfma_f32_16x16x32_bf16 v[216:219], v[180:183], v[168:171], v[216:219]\n"
    "v_mfma_f32_16x16x32_bf16 v[216:219], v[148:151], v[136:139], v[216:219]\n"
    "s_mov_b32 s40, 0x1a191818\n"
    "v_mfma_f32_16x16x32_bf16 v[212:215], v[148:151], v[132:135], v[212:215]\n"
    "v_mfma_f32_16x16x32_bf16 v[212:215], v[180:183], v[164:167], v[212:215]\n"
    "v_bfe_u32 v24, s40, v13, 8\n"
    "v_mfma_f32_16x16x32_bf16 v[208:211], v[180:183], v[160:163], v[208:211]\n"
    "v_mfma_f32_16x16x32_bf16 v[208:211], v[148:151], v[128:131], v[208:211]\n"
    "v_cndmask_b32 v22, v22, v24, vcc\n"
    "v_mfma_f32_16x16x32_bf16 v[224:227], v[152:155], v[128:131], v[224:227]\n"
    "v_mfma_f32_16x16x32_bf16 v[224:227], v[184:187], v[160:163], v[224:227]\n"
    "v_mad_u32_u24 v5, v22, s41, v21\n"
    "v_mfma_f32_16x16x32_bf16 v[228:231], v[184:187], v[164:167], v[228:231]\n"
    "v_mfma_f32_16x16x32_bf16 v[228:231], v[152:155], v[132:135], v[228:231]\n"
    "v_xor_b32 v22, 1, v12\n"
    "v_mfma_f32_16x16x32_bf16 v[232:235], v[152:155], v[136:139], v[232:235]\n"
    "v_mfma_f32_16x16x32_bf16 v[232:235], v[184:187], v[168:171], v[232:235]\n"
    "v_and_b32 v22, 1, v22\n"
    "v_mfma_f32_16x16x32_bf16 v[236:239], v[184:187], v[172:175], v[236:239]\n"
    "v_mfma_f32_16x16x32_bf16 v[236:239], v[152:155], v[140:143], v[236:239]\n"
    "v_lshlrev_b32 v22, 1, v22\n"
    "v_mfma_f32_16x16x32_bf16 v[252:255], v[156:159], v[140:143], v[252:255]\n"
    "v_mfma_f32_16x16x32_bf16 v[252:255], v[188:191], v[172:175], v[252:255]\n"
    "v_lshl_add_u32 v22, v12, 3, v22\n"
    "v_mfma_f32_16x16x32_bf16 v[248:251], v[188:191], v[168:171], v[248:251]\n"
    "v_mfma_f32_16x16x32_bf16 v[248:251], v[156:159], v[136:139], v[248:251]\n"
    "s_mov_b32 s40, 0x05040100\n"
    "v_mfma_f32_16x16x32_bf16 v[244:247], v[156:159], v[132:135], v[244:247]\n"
    "v_mfma_f32_16x16x32_bf16 v[244:247], v[188:191], v[164:167], v[244:247]\n"
    "v_bfe_u32 v24, s40, v13, 8\n"
    "v_mfma_f32_16x16x32_bf16 v[240:243], v[188:191], v[160:163], v[240:243]\n"
    "v_mfma_f32_16x16x32_bf16 v[240:243], v[156:159], v[128:131], v[240:243]\n"
    "s_waitcnt vmcnt(7)\n"
    "s_barrier\n"
    "s_add_u32 m0, s35, 0\n"
    "s_nop 0\n"
    "global_load_lds_dwordx4 v46, s[30:31] offset:0\n"
    "ds_read_b128 v[128:131], v56 offset:49152\n"
    "ds_read_b128 v[132:135], v56 offset:51200\n"
    "ds_read_b128 v[136:139], v56 offset:53248\n"
    "ds_read_b128 v[140:143], v56 offset:55296\n"
    "global_load_lds_dwordx4 v47, s[30:31] offset:1024\n"
    "s_add_u32 s30, s30, 212992\n"
    "s_addc_u32 s31, s31, 0\n"
    "ds_read_b64_tr_b16 v[144:145], v76 offset:49152\n"
    "ds_read_b64_tr_b16 v[146:147], v76 offset:50176\n"
    "ds_read_b64_tr_b16 v[148:149], v77 offset:49152\n"
    "ds_read_b64_tr_b16 v[150:151], v77 offset:50176\n"
    "s_add_u32 m0, s34, 0\n"
    "s_nop 0\n"
    "global_load_lds_dwordx4 v52, s[36:37] offset:0\n"
    "ds_read_b64_tr_b16 v[152:153], v78 offset:49152\n"
    "ds_read_b64_tr_b16 v[154:155], v78 offset:50176\n"
    "ds_read_b64_tr_b16 v[156:157], v79 offset:49152\n"
    "ds_read_b64_tr_b16 v[158:159], v79 offset:50176\n"
    "global_load_lds_dwordx4 v53, s[36:37] offset:1024\n"
    "ds_read_b128 v[160:163], v57 offset:49152\n"
    "ds_read_b128 v[164:167], v57 offset:51200\n"
    "ds_read_b128 v[168:171], v57 offset:53248\n"
    "ds_read_b128 v[172:175], v57 offset:55296\n"
    "global_load_lds_dwordx4 v54, s[36:37] offset:2048\n"
    "ds_read_b64_tr_b16 v[176:177], v76 offset:57344\n"
    "ds_read_b64_tr_b16 v[178:179], v76 offset:58368\n"
    "ds_read_b64_tr_b16 v[180:181], v77 offset:57344\n"
    "ds_read_b64_tr_b16 v[182:183], v77 offset:58368\n"
    "global_load_lds_dwordx4 v55, s[36:37] offset:3072\n"
    "ds_read_b64_tr_b16 v[184:185], v78 offset:57344\n"
    "ds_read_b64_tr_b16 v[186:187], v78 offset:58368\n"
    "ds_read_b64_tr_b16 v[188:189], v79 offset:57344\n"
    "ds_read_b64_tr_b16 v[190:191], v79 offset:58368\n"
    "s_waitcnt vmcnt(11) lgkmcnt(0)\n"
    "s_barrier\n"
    "v_mfma_f32_16x16x32_bf16 v[192:195], v[144:147], v[128:131], v[192:195]\n"
    "v_lshlrev_b32 v107, 7, v107\n"
    "v_mfma_f32_16x16x32_bf16 v[192:195], v[176:179], v[160:163], v[192:195]\n"
    "v_add_u32_dpp v52, v106, v48 quad_perm:[0,0,0,0] row_mask:0xf bank_mask:0xf\n"
    "v_mfma_f32_16x16x32_bf16 v[196:199], v[176:179], v[164:167], v[196:199]\n"
    "v_mfma_f32_16x16x32_bf16 v[196:199], v[144:147], v[132:135], v[196:199]\n"
    "v_add_u32_dpp v53, v106, v49 quad_perm:[1,1,1,1] row_mask:0xf bank_mask:0xf\n"
    "v_mfma_f32_16x16x32_bf16 v[200:203], v[144:147], v[136:139], v[200:203]\n"
    "v_mfma_f32_16x16x32_bf16 v[200:203], v[176:179], v[168:171], v[200:203]\n"
    "v_add_u32_dpp v54, v106, v50 quad_perm:[2,2,2,2] row_mask:0xf bank_mask:0xf\n"
    "v_mfma_f32_16x16x32_bf16 v[204:207], v[176:179], v[172:175], v[204:207]\n"
    "v_mfma_f32_16x16x32_bf16 v[204:207], v[144:147], v[140:143], v[204:207]\n"
    "v_add_u32_dpp v55, v106, v51 quad_perm:[3,3,3,3] row_mask:0xf bank_mask:0xf\n"
    "v_mfma_f32_16x16x32_bf16 v[220:223], v[148:151], v[140:143], v[220:223]\n"
    "v_mfma_f32_16x16x32_bf16 v[220:223], v[180:183], v[172:175], v[220:223]\n"
    "v_add_u32 v22, v22, v24\n"
    "v_mfma_f32_16x16x32_bf16 v[216:219], v[180:183], v[168:171], v[216:219]\n"
    "v_mfma_f32_16x16x32_bf16 v[216:219], v[148:151], v[136:139], v[216:219]\n"
    "s_mov_b32 s40, 0x1a191918\n"
    "v_mfma_f32_16x16x32_bf16 v[212:215], v[148:151], v[132:135], v[212:215]\n"
    "v_mfma_f32_16x16x32_bf16 v[212:215], v[180:183], v[164:167], v[212:215]\n"
    "v_bfe_u32 v24, s40, v13, 8\n"
    "v_mfma_f32_16x16x32_bf16 v[208:211], v[180:183], v[160:163], v[208:211]\n"
    "v_mfma_f32_16x16x32_bf16 v[208:211], v[148:151], v[128:131], v[208:211]\n"
    "v_cndmask_b32 v22, v22, v24, vcc\n"
    "v_mfma_f32_16x16x32_bf16 v[224:227], v[152:155], v[128:131], v[224:227]\n"
    "v_mfma_f32_16x16x32_bf16 v[224:227], v[184:187], v[160:163], v[224:227]\n"
    "v_mad_u32_u24 v6, v22, s41, v21\n"
    "v_mfma_f32_16x16x32_bf16 v[228:231], v[184:187], v[164:167], v[228:231]\n"
    "v_mfma_f32_16x16x32_bf16 v[228:231], v[152:155], v[132:135], v[228:231]\n"
    "v_bfrev_b32 v14, v12\n"
    "v_mfma_f32_16x16x32_bf16 v[232:235], v[152:155], v[136:139], v[232:235]\n"
    "v_mfma_f32_16x16x32_bf16 v[232:235], v[184:187], v[168:171], v[232:235]\n"
    "v_lshrrev_b32 v14, 26, v14\n"
    "v_mfma_f32_16x16x32_bf16 v[236:239], v[184:187], v[172:175], v[236:239]\n"
    "v_mfma_f32_16x16x32_bf16 v[236:239], v[152:155], v[140:143], v[236:239]\n"
    "s_add_u32 s40, s22, 3\n"
    "v_mfma_f32_16x16x32_bf16 v[252:255], v[156:159], v[140:143], v[252:255]\n"
    "v_mfma_f32_16x16x32_bf16 v[252:255], v[188:191], v[172:175], v[252:255]\n"
    "s_and_b32 s40, s40, 3\n"
    "v_mfma_f32_16x16x32_bf16 v[248:251], v[188:191], v[168:171], v[248:251]\n"
    "v_mfma_f32_16x16x32_bf16 v[248:251], v[156:159], v[136:139], v[248:251]\n"
    "s_lshl2_add_u32 s40, s40, s22\n"
    "v_mfma_f32_16x16x32_bf16 v[244:247], v[156:159], v[132:135], v[244:247]\n"
    "v_mfma_f32_16x16x32_bf16 v[244:247], v[188:191], v[164:167], v[244:247]\n"
    "s_add_u32 s40, s40, s58\n"
    "v_mfma_f32_16x16x32_bf16 v[240:243], v[188:191], v[160:163], v[240:243]\n"
    "v_mfma_f32_16x16x32_bf16 v[240:243], v[156:159], v[128:131], v[240:243]\n"
    "s_waitcnt vmcnt(7)\n"
    "s_barrier\n"
    "s_add_u32 m0, s35, 49152\n"
    "s_nop 0\n"
    "global_load_lds_dwordx4 v46, s[30:31] offset:0\n"
    "ds_read_b128 v[128:131], v58 offset:0\n"
    "ds_read_b128 v[132:135], v58 offset:2048\n"
    "ds_read_b128 v[136:139], v58 offset:4096\n"
    "ds_read_b128 v[140:143], v58 offset:6144\n"
    "global_load_lds_dwordx4 v47, s[30:31] offset:1024\n"
    "s_add_u32 s30, s30, 212992\n"
    "s_addc_u32 s31, s31, 0\n"
    "ds_read_b64_tr_b16 v[144:145], v80 offset:0\n"
    "ds_read_b64_tr_b16 v[146:147], v80 offset:1024\n"
    "ds_read_b64_tr_b16 v[148:149], v81 offset:0\n"
    "ds_read_b64_tr_b16 v[150:151], v81 offset:1024\n"
    "s_add_u32 m0, s34, 49152\n"
    "s_nop 0\n"
    "global_load_lds_dwordx4 v52, s[36:37] offset:0\n"
    "ds_read_b64_tr_b16 v[152:153], v82 offset:0\n"
    "ds_read_b64_tr_b16 v[154:155], v82 offset:1024\n"
    "ds_read_b64_tr_b16 v[156:157], v83 offset:0\n"
    "ds_read_b64_tr_b16 v[158:159], v83 offset:1024\n"
    "global_load_lds_dwordx4 v53, s[36:37] offset:1024\n"
    "ds_read_b128 v[160:163], v59 offset:0\n"
    "ds_read_b128 v[164:167], v59 offset:2048\n"
    "ds_read_b128 v[168:171], v59 offset:4096\n"
    "ds_read_b128 v[172:175], v59 offset:6144\n"
    "global_load_lds_dwordx4 v54, s[36:37] offset:2048\n"
    "ds_read_b64_tr_b16 v[176:177], v80 offset:8192\n"
    "ds_read_b64_tr_b16 v[178:179], v80 offset:9216\n"
    "ds_read_b64_tr_b16 v[180:181], v81 offset:8192\n"
    "ds_read_b64_tr_b16 v[182:183], v81 offset:9216\n"
    "global_load_lds_dwordx4 v55, s[36:37] offset:3072\n"
    "ds_read_b64_tr_b16 v[184:185], v82 offset:8192\n"
    "ds_read_b64_tr_b16 v[186:187], v82 offset:9216\n"
    "ds_read_b64_tr_b16 v[188:189], v83 offset:8192\n"
    "ds_read_b64_tr_b16 v[190:191], v83 offset:9216\n"
    "s_waitcnt vmcnt(10) lgkmcnt(0)\n"
    "s_barrier\n"
    "v_mfma_f32_16x16x32_bf16 v[192:195], v[144:147], v[128:131], v[192:195]\n"
    "v_lshlrev_b32 v108, 7, v108\n"
    "v_mfma_f32_16x16x32_bf16 v[192:195], v[176:179], v[160:163], v[192:195]\n"
    "v_add_u32_dpp v52, v107, v48 quad_perm:[0,0,0,0] row_mask:0xf bank_mask:0xf\n"
    "v_mfma_f32_16x16x32_bf16 v[196:199], v[176:179], v[164:167], v[196:199]\n"
    "v_mfma_f32_16x16x32_bf16 v[196:199], v[144:147], v[132:135], v[196:199]\n"
    "v_add_u32_dpp v53, v107, v49 quad_perm:[1,1,1,1] row_mask:0xf bank_mask:0xf\n"
    "v_mfma_f32_16x16x32_bf16 v[200:203], v[144:147], v[136:139], v[200:203]\n"
    "v_mfma_f32_16x16x32_bf16 v[200:203], v[176:179], v[168:171], v[200:203]\n"
    "v_add_u32_dpp v54, v107, v50 quad_perm:[2,2,2,2] row_mask:0xf bank_mask:0xf\n"
    "v_mfma_f32_16x16x32_bf16 v[204:207], v[176:179], v[172:175], v[204:207]\n"
    "v_mfma_f32_16x16x32_bf16 v[204:207], v[144:147], v[140:143], v[204:207]\n"
    "v_add_u32_dpp v55, v107, v51 quad_perm:[3,3,3,3] row_mask:0xf bank_mask:0xf\n"
    "v_mfma_f32_16x16x32_bf16 v[220:223], v[148:151], v[140:143], v[220:223]\n"
    "v_mfma_f32_16x16x32_bf16 v[220:223], v[180:183], v[172:175], v[220:223]\n"
    "s_lshl_b32 s40, s40, 2\n"
    "v_mfma_f32_16x16x32_bf16 v[216:219], v[180:183], v[168:171], v[216:219]\n"
    "v_mfma_f32_16x16x32_bf16 v[216:219], v[148:151], v[136:139], v[216:219]\n"
    "s_add_u32 s40, s40, -450048\n"
    "v_mfma_f32_16x16x32_bf16 v[212:215], v[148:151], v[132:135], v[212:215]\n"
    "v_mfma_f32_16x16x32_bf16 v[212:215], v[180:183], v[164:167], v[212:215]\n"
    "v_add_u32 v14, s40, v14\n"
    "v_mfma_f32_16x16x32_bf16 v[208:211], v[180:183], v[160:163], v[208:211]\n"
    "v_mfma_f32_16x16x32_bf16 v[208:211], v[148:151], v[128:131], v[208:211]\n"
    "v_and_b32 v2, 1, v1\n"
    "v_mfma_f32_16x16x32_bf16 v[224:227], v[152:155], v[128:131], v[224:227]\n"
    "v_mfma_f32_16x16x32_bf16 v[224:227], v[184:187], v[160:163], v[224:227]\n"
    "v_mul_u32_u24 v2, 0xaaaa, v2\n"
    "v_mfma_f32_16x16x32_bf16 v[228:231], v[184:187], v[164:167], v[228:231]\n"
    "v_mfma_f32_16x16x32_bf16 v[228:231], v[152:155], v[132:135], v[228:231]\n"
    "v_add_u32 v2, 0x4444, v2\n"
    "v_mfma_f32_16x16x32_bf16 v[232:235], v[152:155], v[136:139], v[232:235]\n"
    "v_mfma_f32_16x16x32_bf16 v[232:235], v[184:187], v[168:171], v[232:235]\n"
    "v_mad_u32_u24 v15, v17, 40, v19\n"
    "v_mfma_f32_16x16x32_bf16 v[236:239], v[184:187], v[172:175], v[236:239]\n"
    "v_mfma_f32_16x16x32_bf16 v[236:239], v[152:155], v[140:143], v[236:239]\n"
    "v_mad_u32_u24 v16, v18, 40, v20\n"
    "v_mfma_f32_16x16x32_bf16 v[252:255], v[156:159], v[140:143], v[252:255]\n"
    "v_mfma_f32_16x16x32_bf16 v[252:255], v[188:191], v[172:175], v[252:255]\n"
    "s_movk_i32 s41, 4480\n"
    "v_mfma_f32_16x16x32_bf16 v[248:251], v[188:191], v[168:171], v[248:251]\n"
    "v_mfma_f32_16x16x32_bf16 v[248:251], v[156:159], v[136:139], v[248:251]\n"
    "v_lshrrev_b32 v12, 5, v19\n"
    "v_mfma_f32_16x16x32_bf16 v[244:247], v[156:159], v[132:135], v[244:247]\n"
    "v_mfma_f32_16x16x32_bf16 v[244:247], v[188:191], v[164:167], v[244:247]\n"
    "v_sub_u32 v12, 1, v12\n"
    "v_mfma_f32_16x16x32_bf16 v[240:243], v[188:191], v[160:163], v[240:243]\n"
    "v_mfma_f32_16x16x32_bf16 v[240:243], v[156:159], v[128:131], v[240:243]\n"
    "s_waitcnt vmcnt(6)\n"
    "s_barrier\n"
    "s_add_u32 m0, s35, 98304\n"
    "s_nop 0\n"
    "global_load_lds_dwordx4 v46, s[30:31] offset:0\n"
    "ds_read_b128 v[128:131], v56 offset:0\n"
    "ds_read_b128 v[132:135], v56 offset:2048\n"
    "ds_read_b128 v[136:139], v56 offset:4096\n"
    "ds_read_b128 v[140:143], v56 offset:6144\n"
    "global_load_lds_dwordx4 v47, s[30:31] offset:1024\n"
    "s_bfe_u32 s66, s28, 0x80010\n"
    "s_mul_i32 s66, s66, 53248\n"
    "s_add_u32 s30, s6, s66\n"
    "s_addc_u32 s31, s7, 0\n"
    "ds_read_b64_tr_b16 v[144:145], v76 offset:0\n"
    "ds_read_b64_tr_b16 v[146:147], v76 offset:1024\n"
    "ds_read_b64_tr_b16 v[148:149], v77 offset:0\n"
    "ds_read_b64_tr_b16 v[150:151], v77 offset:1024\n"
    "s_add_u32 m0, s34, 98304\n"
    "s_nop 0\n"
    "global_load_lds_dwordx4 v52, s[36:37] offset:0\n"
    "ds_read_b64_tr_b16 v[152:153], v78 offset:0\n"
    "ds_read_b64_tr_b16 v[154:155], v78 offset:1024\n"
    "ds_read_b64_tr_b16 v[156:157], v79 offset:0\n"
    "ds_read_b64_tr_b16 v[158:159], v79 offset:1024\n"
    "global_load_lds_dwordx4 v53, s[36:37] offset:1024\n"
    "ds_read_b128 v[160:163], v57 offset:0\n"
    "ds_read_b128 v[164:167], v57 offset:2048\n"
    "ds_read_b128 v[168:171], v57 offset:4096\n"
    "ds_read_b128 v[172:175], v57 offset:6144\n"
    "global_load_lds_dwordx4 v54, s[36:37] offset:2048\n"
    "ds_read_b64_tr_b16 v[176:177], v76 offset:8192\n"
    "ds_read_b64_tr_b16 v[178:179], v76 offset:9216\n"
    "ds_read_b64_tr_b16 v[180:181], v77 offset:8192\n"
    "ds_read_b64_tr_b16 v[182:183], v77 offset:9216\n"
    "global_load_lds_dwordx4 v55, s[36:37] offset:3072\n"
    "ds_read_b64_tr_b16 v[184:185], v78 offset:8192\n"
    "ds_read_b64_tr_b16 v[186:187], v78 offset:9216\n"
    "ds_read_b64_tr_b16 v[188:189], v79 offset:8192\n"
    "ds_read_b64_tr_b16 v[190:191], v79 offset:9216\n"
    "s_waitcnt vmcnt(10) lgkmcnt(0)\n"
    "s_barrier\n"
    "v_mfma_f32_16x16x32_bf16 v[192:195], v[144:147], v[128:131], v[192:195]\n"
    "v_lshlrev_b32 v109, 7, v109\n"
    "v_mfma_f32_16x16x32_bf16 v[192:195], v[176:179], v[160:163], v[192:195]\n"
    "v_add_u32_dpp v52, v108, v48 quad_perm:[0,0,0,0] row_mask:0xf bank_mask:0xf\n"
    "v_mfma_f32_16x16x32_bf16 v[196:199], v[176:179], v[164:167], v[196:199]\n"
    "v_mfma_f32_16x16x32_bf16 v[196:199], v[144:147], v[132:135], v[196:199]\n"
    "v_add_u32_dpp v53, v108, v49 quad_perm:[1,1,1,1] row_mask:0xf bank_mask:0xf\n"
    "v_mfma_f32_16x16x32_bf16 v[200:203], v[144:147], v[136:139], v[200:203]\n"
    "v_mfma_f32_16x16x32_bf16 v[200:203], v[176:179], v[168:171], v[200:203]\n"
    "v_add_u32_dpp v54, v108, v50 quad_perm:[2,2,2,2] row_mask:0xf bank_mask:0xf\n"
    "v_mfma_f32_16x16x32_bf16 v[204:207], v[176:179], v[172:175], v[204:207]\n"
    "v_mfma_f32_16x16x32_bf16 v[204:207], v[144:147], v[140:143], v[204:207]\n"
    "v_add_u32_dpp v55, v108, v51 quad_perm:[3,3,3,3] row_mask:0xf bank_mask:0xf\n"
    "v_mfma_f32_16x16x32_bf16 v[220:223], v[148:151], v[140:143], v[220:223]\n"
    "v_mfma_f32_16x16x32_bf16 v[220:223], v[180:183], v[172:175], v[220:223]\n"
    "v_and_b32 v12, 3, v12\n"
    "v_mfma_f32_16x16x32_bf16 v[216:219], v[180:183], v[168:171], v[216:219]\n"
    "v_mfma_f32_16x16x32_bf16 v[216:219], v[148:151], v[136:139], v[216:219]\n"
    "v_add_u32 v12, s22, v12\n"
    "v_mfma_f32_16x16x32_bf16 v[212:215], v[148:151], v[132:135], v[212:215]\n"
    "v_mfma_f32_16x16x32_bf16 v[212:215], v[180:183], v[164:167], v[212:215]\n"
    "v_add_u32 v15, s43, v15\n"
    "v_mfma_f32_16x16x32_bf16 v[208:211], v[180:183], v[160:163], v[208:211]\n"
    "v_mfma_f32_16x16x32_bf16 v[208:211], v[148:151], v[128:131], v[208:211]\n"
    "v_xor_b32 v13, 4, v12\n"
    "v_mfma_f32_16x16x32_bf16 v[224:227], v[152:155], v[128:131], v[224:227]\n"
    "v_mfma_f32_16x16x32_bf16 v[224:227], v[184:187], v[160:163], v[224:227]\n"
    "v_mad_u32_u24 v0, v13, s41, v15\n"
    "v_mfma_f32_16x16x32_bf16 v[228:231], v[184:187], v[164:167], v[228:231]\n"
    "v_mfma_f32_16x16x32_bf16 v[228:231], v[152:155], v[132:135], v[228:231]\n"
    "v_mad_u32_u24 v15, v12, s41, v15\n"
    "v_mfma_f32_16x16x32_bf16 v[232:235], v[152:155], v[136:139], v[232:235]\n"
    "v_mfma_f32_16x16x32_bf16 v[232:235], v[184:187], v[168:171], v[232:235]\n"
    "v_add_u32 v0, v0, v15\n"
    "v_mfma_f32_16x16x32_bf16 v[236:239], v[184:187], v[172:175], v[236:239]\n"
    "v_mfma_f32_16x16x32_bf16 v[236:239], v[152:155], v[140:143], v[236:239]\n"
    "v_lshrrev_b32 v12, 5, v20\n"
    "v_mfma_f32_16x16x32_bf16 v[252:255], v[156:159], v[140:143], v[252:255]\n"
    "v_mfma_f32_16x16x32_bf16 v[252:255], v[188:191], v[172:175], v[252:255]\n"
    "v_sub_u32 v12, 1, v12\n"
    "v_mfma_f32_16x16x32_bf16 v[248:251], v[188:191], v[168:171], v[248:251]\n"
    "v_mfma_f32_16x16x32_bf16 v[248:251], v[156:159], v[136:139], v[248:251]\n"
    "v_and_b32 v12, 3, v12\n"
    "v_mfma_f32_16x16x32_bf16 v[244:247], v[156:159], v[132:135], v[244:247]\n"
    "v_mfma_f32_16x16x32_bf16 v[244:247], v[188:191], v[164:167], v[244:247]\n"
    "v_add_u32 v12, s22, v12\n"
    "v_mfma_f32_16x16x32_bf16 v[240:243], v[188:191], v[160:163], v[240:243]\n"
    "v_mfma_f32_16x16x32_bf16 v[240:243], v[156:159], v[128:131], v[240:243]\n"
    "s_waitcnt vmcnt(6)\n"
    "s_barrier\n"
    "s_add_u32 m0, s35, 0\n"
    "s_nop 0\n"
    "global_load_lds_dwordx4 v46, s[30:31] offset:0\n"
    "ds_read_b128 v[128:131], v56 offset:49152\n"
    "ds_read_b128 v[132:135], v56 offset:51200\n"
    "ds_read_b128 v[136:139], v56 offset:53248\n"
    "ds_read_b128 v[140:143], v56 offset:55296\n"
    "global_load_lds_dwordx4 v47, s[30:31] offset:1024\n"
    "s_add_u32 s30, s30, 212992\n"
    "s_addc_u32 s31, s31, 0\n"
    "ds_read_b64_tr_b16 v[144:145], v76 offset:49152\n"
    "ds_read_b64_tr_b16 v[146:147], v76 offset:50176\n"
    "ds_read_b64_tr_b16 v[148:149], v77 offset:49152\n"
    "ds_read_b64_tr_b16 v[150:151], v77 offset:50176\n"
    "s_add_u32 m0, s34, 0\n"
    "s_nop 0\n"
    "global_load_lds_dwordx4 v52, s[36:37] offset:0\n"
    "ds_read_b64_tr_b16 v[152:153], v78 offset:49152\n"
    "ds_read_b64_tr_b16 v[154:155], v78 offset:50176\n"
    "ds_read_b64_tr_b16 v[156:157], v79 offset:49152\n"
    "ds_read_b64_tr_b16 v[158:159], v79 offset:50176\n"
    "global_load_lds_dwordx4 v53, s[36:37] offset:1024\n"
    "ds_read_b128 v[160:163], v57 offset:49152\n"
    "ds_read_b128 v[164:167], v57 offset:51200\n"
    "ds_read_b128 v[168:171], v57 offset:53248\n"
    "ds_read_b128 v[172:175], v57 offset:55296\n"
    "global_load_lds_dwordx4 v54, s[36:37] offset:2048\n"
    "ds_read_b64_tr_b16 v[176:177], v76 offset:57344\n"
    "ds_read_b64_tr_b16 v[178:179], v76 offset:58368\n"
    "ds_read_b64_tr_b16 v[180:181], v77 offset:57344\n"
    "ds_read_b64_tr_b16 v[182:183], v77 offset:58368\n"
    "global_load_lds_dwordx4 v55, s[36:37] offset:3072\n"
    "ds_read_b64_tr_b16 v[184:185], v78 offset:57344\n"
    "ds_read_b64_tr_b16 v[186:187], v78 offset:58368\n"
    "ds_read_b64_tr_b16 v[188:189], v79 offset:57344\n"
    "ds_read_b64_tr_b16 v[190:191], v79 offset:58368\n"
    "s_waitcnt vmcnt(10) lgkmcnt(0)\n"
    "s_barrier\n"
    "v_mfma_f32_16x16x32_bf16 v[192:195], v[144:147], v[128:131], v[192:195]\n"
    "v_lshlrev_b32 v110, 7, v110\n"
    "v_mfma_f32_16x16x32_bf16 v[192:195], v[176:179], v[160:163], v[192:195]\n"
    "v_add_u32_dpp v52, v109, v48 quad_perm:[0,0,0,0] row_mask:0xf bank_mask:0xf\n"
    "v_mfma_f32_16x16x32_bf16 v[196:199], v[176:179], v[164:167], v[196:199]\n"
    "v_mfma_f32_16x16x32_bf16 v[196:199], v[144:147], v[132:135], v[196:199]\n"
    "v_add_u32_dpp v53, v109, v49 quad_perm:[1,1,1,1] row_mask:0xf bank_mask:0xf\n"
    "v_mfma_f32_16x16x32_bf16 v[200:203], v[144:147], v[136:139], v[200:203]\n"
    "v_mfma_f32_16x16x32_bf16 v[200:203], v[176:179], v[168:171], v[200:203]\n"
    "v_add_u32_dpp v54, v109, v50 quad_perm:[2,2,2,2] row_mask:0xf bank_mask:0xf\n"
    "v_mfma_f32_16x16x32_bf16 v[204:207], v[176:179], v[172:175], v[204:207]\n"
    "v_mfma_f32_16x16x32_bf16 v[204:207], v[144:147], v[140:143], v[204:207]\n"
    "v_add_u32_dpp v55, v109, v51 quad_perm:[3,3,3,3] row_mask:0xf bank_mask:0xf\n"
    "v_mfma_f32_16x16x32_bf16 v[220:223], v[148:151], v[140:143], v[220:223]\n"
    "v_mfma_f32_16x16x32_bf16 v[220:223], v[180:183], v[172:175], v[220:223]\n"
    "v_add_u32 v16, s43, v16\n"
    "v_mfma_f32_16x16x32_bf16 v[216:219], v[180:183], v[168:171], v[216:219]\n"
    "v_xor_b32 v13, 4, v12\n"
    "v_mfma_f32_16x16x32_bf16 v[216:219], v[148:151], v[136:139], v[216:219]\n"
    "v_mfma_f32_16x16x32_bf16 v[212:215], v[148:151], v[132:135], v[212:215]\n"
    "v_mad_u32_u24 v23, v13, s41, v16\n"
    "v_mfma_f32_16x16x32_bf16 v[212:215], v[180:183], v[164:167], v[212:215]\n"
    "v_mfma_f32_16x16x32_bf16 v[208:211], v[180:183], v[160:163], v[208:211]\n"
    "v_mad_u32_u24 v16, v12, s41, v16\n"
    "v_mfma_f32_16x16x32_bf16 v[208:211], v[148:151], v[128:131], v[208:211]\n"
    "v_mfma_f32_16x16x32_bf16 v[224:227], v[152:155], v[128:131], v[224:227]\n"
    "v_add_u32 v23, v23, v16\n"
    "v_mfma_f32_16x16x32_bf16 v[224:227], v[184:187], v[160:163], v[224:227]\n"
    "v_mfma_f32_16x16x32_bf16 v[228:231], v[184:187], v[164:167], v[228:231]\n"
    "s_mul_i32 s41, s47, 13\n"
    "v_mfma_f32_16x16x32_bf16 v[228:231], v[152:155], v[132:135], v[228:231]\n"
    "v_mfma_f32_16x16x32_bf16 v[232:235], v[152:155], v[136:139], v[232:235]\n"
    "s_sub_u32 s41, s3, s41\n"
    "v_mfma_f32_16x16x32_bf16 v[232:235], v[184:187], v[168:171], v[232:235]\n"
    "s_lshl1_add_u32 s41, s41, s21\n"
    "v_mfma_f32_16x16x32_bf16 v[236:239], v[184:187], v[172:175], v[236:239]\n"
    "v_mfma_f32_16x16x32_bf16 v[236:239], v[152:155], v[140:143], v[236:239]\n"
    "s_mul_i32 s41, s41, 3\n"
    "v_mfma_f32_16x16x32_bf16 v[252:255], v[156:159], v[140:143], v[252:255]\n"
    "v_mfma_f32_16x16x32_bf16 v[252:255], v[188:191], v[172:175], v[252:255]\n"
    "s_add_u32 s41, s41, s22\n"
    "v_mfma_f32_16x16x32_bf16 v[248:251], v[188:191], v[168:171], v[248:251]\n"
    "v_mfma_f32_16x16x32_bf16 v[248:251], v[156:159], v[136:139], v[248:251]\n"
    "s_cmp_eq_u32 s22, 3\n"
    "s_cselect_b32 s41, -1, s41\n"
    "v_mfma_f32_16x16x32_bf16 v[244:247], v[156:159], v[132:135], v[244:247]\n"
    "v_mfma_f32_16x16x32_bf16 v[244:247], v[188:191], v[164:167], v[244:247]\n"
    "v_mov_b32 v24, s41\n"
    "v_mfma_f32_16x16x32_bf16 v[240:243], v[188:191], v[160:163], v[240:243]\n"
    "v_mfma_f32_16x16x32_bf16 v[240:243], v[156:159], v[128:131], v[240:243]\n"
    "s_waitcnt vmcnt(6)\n"
    "s_barrier\n"
    "s_add_u32 m0, s35, 49152\n"
    "s_nop 0\n"
    "global_load_lds_dwordx4 v46, s[30:31] offset:0\n"
    "ds_read_b128 v[128:131], v58 offset:0\n"
    "ds_read_b128 v[132:135], v58 offset:2048\n"
    "ds_read_b128 v[136:139], v58 offset:4096\n"
    "ds_read_b128 v[140:143], v58 offset:6144\n"
    "global_load_lds_dwordx4 v47, s[30:31] offset:1024\n"
    "s_add_u32 s30, s30, 212992\n"
    "s_addc_u32 s31, s31, 0\n"
    "ds_read_b64_tr_b16 v[144:145], v80 offset:0\n"
    "ds_read_b64_tr_b16 v[146:147], v80 offset:1024\n"
    "ds_read_b64_tr_b16 v[148:149], v81 offset:0\n"
    "ds_read_b64_tr_b16 v[150:151], v81 offset:1024\n"
    "s_add_u32 m0, s34, 49152\n"
    "s_nop 0\n"
    "global_load_lds_dwordx4 v52, s[36:37] offset:0\n"
    "ds_read_b64_tr_b16 v[152:153], v82 offset:0\n"
    "ds_read_b64_tr_b16 v[154:155], v82 offset:1024\n"
    "ds_read_b64_tr_b16 v[156:157], v83 offset:0\n"
    "ds_read_b64_tr_b16 v[158:159], v83 offset:1024\n"
    "global_load_lds_dwordx4 v53, s[36:37] offset:1024\n"
    "ds_read_b128 v[160:163], v59 offset:0\n"
    "ds_read_b128 v[164:167], v59 offset:2048\n"
    "ds_read_b128 v[168:171], v59 offset:4096\n"
    "ds_read_b128 v[172:175], v59 offset:6144\n"
    "global_load_lds_dwordx4 v54, s[36:37] offset:2048\n"
    "ds_read_b64_tr_b16 v[176:177], v80 offset:8192\n"
    "ds_read_b64_tr_b16 v[178:179], v80 offset:9216\n"
    "ds_read_b64_tr_b16 v[180:181], v81 offset:8192\n"
    "ds_read_b64_tr_b16 v[182:183], v81 offset:9216\n"
    "global_load_lds_dwordx4 v55, s[36:37] offset:3072\n"
    "ds_read_b64_tr_b16 v[184:185], v82 offset:8192\n"
    "ds_read_b64_tr_b16 v[186:187], v82 offset:9216\n"
    "ds_read_b64_tr_b16 v[188:189], v83 offset:8192\n"
    "ds_read_b64_tr_b16 v[190:191], v83 offset:9216\n"
    "s_waitcnt vmcnt(10) lgkmcnt(0)\n"
    "s_barrier\n"
    "v_mfma_f32_16x16x32_bf16 v[192:195], v[144:147], v[128:131], v[192:195]\n"
    "v_lshlrev_b32 v111, 7, v111\n"
    "v_mfma_f32_16x16x32_bf16 v[192:195], v[176:179], v[160:163], v[192:195]\n"
    "v_mfma_f32_16x16x32_bf16 v[196:199], v[176:179], v[164:167], v[196:199]\n"
    "v_add_u32_dpp v52, v110, v48 quad_perm:[0,0,0,0] row_mask:0xf bank_mask:0xf\n"
    "v_mfma_f32_16x16x32_bf16 v[196:199], v[144:147], v[132:135], v[196:199]\n"
    "v_mfma_f32_16x16x32_bf16 v[200:203], v[144:147], v[136:139], v[200:203]\n"
    "v_add_u32_dpp v53, v110, v49 quad_perm:[1,1,1,1] row_mask:0xf bank_mask:0xf\n"
    "v_mfma_f32_16x16x32_bf16 v[200:203], v[176:179], v[168:171], v[200:203]\n"
    "v_mfma_f32_16x16x32_bf16 v[204:207], v[176:179], v[172:175], v[204:207]\n"
    "v_add_u32_dpp v54, v110, v50 quad_perm:[2,2,2,2] row_mask:0xf bank_mask:0xf\n"
    "v_mfma_f32_16x16x32_bf16 v[204:207], v[144:147], v[140:143], v[204:207]\n"
    "v_mfma_f32_16x16x32_bf16 v[220:223], v[148:151], v[140:143], v[220:223]\n"
    "v_add_u32_dpp v55, v110, v51 quad_perm:[3,3,3,3] row_mask:0xf bank_mask:0xf\n"
    "v_mfma_f32_16x16x32_bf16 v[220:223], v[180:183], v[172:175], v[220:223]\n"
    "v_mfma_f32_16x16x32_bf16 v[216:219], v[180:183], v[168:171], v[216:219]\n"
    "v_readfirstlane_b32 s40, v24\n"
    "v_mfma_f32_16x16x32_bf16 v[216:219], v[148:151], v[136:139], v[216:219]\n"
    "v_mfma_f32_16x16x32_bf16 v[212:215], v[148:151], v[132:135], v[212:215]\n"
    "s_or_b32 s40, s40, s44\n"
    "v_mfma_f32_16x16x32_bf16 v[212:215], v[180:183], v[164:167], v[212:215]\n"
    "v_mfma_f32_16x16x32_bf16 v[208:211], v[180:183], v[160:163], v[208:211]\n"
    "s_lshl1_add_u32 s41, s40, s54\n"
    "v_mfma_f32_16x16x32_bf16 v[208:211], v[148:151], v[128:131], v[208:211]\n"
    "v_mfma_f32_16x16x32_bf16 v[224:227], v[152:155], v[128:131], v[224:227]\n"
    "s_cmp_lt_u32 s40, 256\n"
    "v_mfma_f32_16x16x32_bf16 v[224:227], v[184:187], v[160:163], v[224:227]\n"
    "v_mfma_f32_16x16x32_bf16 v[228:231], v[184:187], v[164:167], v[228:231]\n"
    "s_cselect_b32 s41, s41, 2\n"
    "v_mfma_f32_16x16x32_bf16 v[228:231], v[152:155], v[132:135], v[228:231]\n"
    "v_mfma_f32_16x16x32_bf16 v[232:235], v[152:155], v[136:139], v[232:235]\n"
    "s_cselect_b64 s[48:49], -1, 0\n"
    "v_mfma_f32_16x16x32_bf16 v[232:235], v[184:187], v[168:171], v[232:235]\n"
    "v_mfma_f32_16x16x32_bf16 v[236:239], v[184:187], v[172:175], v[236:239]\n"
    "v_mov_b32 v12, s41\n"
    "v_mfma_f32_16x16x32_bf16 v[236:239], v[152:155], v[140:143], v[236:239]\n"
    "v_mfma_f32_16x16x32_bf16 v[252:255], v[156:159], v[140:143], v[252:255]\n"
    "s_mul_i32 s41, s41, 52\n"
    "v_mfma_f32_16x16x32_bf16 v[252:255], v[188:191], v[172:175], v[252:255]\n"
    "v_mfma_f32_16x16x32_bf16 v[248:251], v[188:191], v[168:171], v[248:251]\n"
    "s_add_u32 s38, s4, s41\n"
    "v_mfma_f32_16x16x32_bf16 v[248:251], v[156:159], v[136:139], v[248:251]\n"
    "v_mfma_f32_16x16x32_bf16 v[244:247], v[156:159], v[132:135], v[244:247]\n"
    "s_addc_u32 s39, s5, 0\n"
    "v_mfma_f32_16x16x32_bf16 v[244:247], v[188:191], v[164:167], v[244:247]\n"
    "v_mfma_f32_16x16x32_bf16 v[240:243], v[188:191], v[160:163], v[240:243]\n"
    "v_mfma_f32_16x16x32_bf16 v[240:243], v[156:159], v[128:131], v[240:243]\n"
    "s_waitcnt vmcnt(6)\n"
    "s_barrier\n"
    "ds_read_b64 v[84:85], v42 offset:0\n"
    "ds_read_b64 v[86:87], v42 offset:32\n"
    "ds_read_b64 v[88:89], v42 offset:64\n"
    "ds_read_b64 v[90:91], v42 offset:96\n"
    "s_add_u32 m0, s35, 98304\n"
    "s_nop 0\n"
    "global_load_lds_dwordx4 v46, s[30:31] offset:0\n"
    "ds_read_b128 v[128:131], v56 offset:0\n"
    "ds_read_b128 v[132:135], v56 offset:2048\n"
    "ds_read_b128 v[136:139], v56 offset:4096\n"
    "global_load_lds_dwordx4 v47, s[30:31] offset:1024\n"
    "s_add_u32 s30, s30, 212992\n"
    "s_addc_u32 s31, s31, 0\n"
    "ds_read_b128 v[140:143], v56 offset:6144\n"
    "ds_read_b64_tr_b16 v[144:145], v76 offset:0\n"
    "ds_read_b64_tr_b16 v[146:147], v76 offset:1024\n"
    "s_add_u32 m0, s34, 98304\n"
    "s_nop 0\n"
    "global_load_lds_dwordx4 v52, s[36:37] offset:0\n"
    "ds_read_b64_tr_b16 v[148:149], v77 offset:0\n"
    "ds_read_b64_tr_b16 v[150:151], v77 offset:1024\n"
    "ds_read_b64_tr_b16 v[152:153], v78 offset:0\n"
    "global_load_lds_dwordx4 v53, s[36:37] offset:1024\n"
    "ds_read_b64_tr_b16 v[154:155], v78 offset:1024\n"
    "ds_read_b64_tr_b16 v[156:157], v79 offset:0\n"
    "ds_read_b64_tr_b16 v[158:159], v79 offset:1024\n"
    "global_load_lds_dwordx4 v54, s[36:37] offset:2048\n"
    "ds_read_b128 v[160:163], v57 offset:0\n"
    "ds_read_b128 v[164:167], v57 offset:2048\n"
    "ds_read_b128 v[168:171], v57 offset:4096\n"
    "global_load_lds_dwordx4 v55, s[36:37] offset:3072\n"
    "ds_read_b128 v[172:175], v57 offset:6144\n"
    "ds_read_b64_tr_b16 v[176:177], v76 offset:8192\n"
    "ds_read_b64_tr_b16 v[178:179], v76 offset:9216\n"
    "global_load_dwordx3 v[36:38], v17, s[38:39] offset:-104\n"
    "ds_read_b64_tr_b16 v[180:181], v77 offset:8192\n"
    "ds_read_b64_tr_b16 v[182:183], v77 offset:9216\n"
    "ds_read_b64_tr_b16 v[184:185], v78 offset:8192\n"
    "ds_read_b64_tr_b16 v[186:187], v78 offset:9216\n"
    "ds_read_b64_tr_b16 v[188:189], v79 offset:8192\n"
    "ds_read_b64_tr_b16 v[190:191], v79 offset:9216\n"
    "s_waitcnt vmcnt(11) lgkmcnt(0)\n"
    "s_barrier\n"
    "v_mfma_f32_16x16x32_bf16 v[192:195], v[144:147], v[128:131], v[192:195]\n"
    "v_lshlrev_b32 v112, 7, v112\n"
    "v_mfma_f32_16x16x32_bf16 v[192:195], v[176:179], v[160:163], v[192:195]\n"
    "v_add_u32_dpp v52, v111, v48 quad_perm:[0,0,0,0] row_mask:0xf bank_mask:0xf\n"
    "v_mfma_f32_16x16x32_bf16 v[196:199], v[176:179], v[164:167], v[196:199]\n"
    "v_add_u32_dpp v53, v111, v49 quad_perm:[1,1,1,1] row_mask:0xf bank_mask:0xf\n"
    "v_mfma_f32_16x16x32_bf16 v[196:199], v[144:147], v[132:135], v[196:199]\n"
    "v_add_u32_dpp v54, v111, v50 quad_perm:[2,2,2,2] row_mask:0xf bank_mask:0xf\n"
    "v_mfma_f32_16x16x32_bf16 v[200:203], v[144:147], v[136:139], v[200:203]\n"
    "v_add_u32_dpp v55, v111, v51 quad_perm:[3,3,3,3] row_mask:0xf bank_mask:0xf\n"
    "v_mfma_f32_16x16x32_bf16 v[200:203], v[176:179], v[168:171], v[200:203]\n"
    "v_lshlrev_b32 v60, 16, v84\n"
    "v_mfma_f32_16x16x32_bf16 v[204:207], v[176:179], v[172:175], v[204:207]\n"
    "v_mfma_f32_16x16x32_bf16 v[204:207], v[144:147], v[140:143], v[204:207]\n"
    "v_and_b32 v61, 0xffff0000, v84\n"
    "v_mfma_f32_16x16x32_bf16 v[220:223], v[148:151], v[140:143], v[220:223]\n"
    "v_lshlrev_b32 v62, 16, v85\n"
    "v_mfma_f32_16x16x32_bf16 v[220:223], v[180:183], v[172:175], v[220:223]\n"
    "v_and_b32 v63, 0xffff0000, v85\n"
    "v_mfma_f32_16x16x32_bf16 v[216:219], v[180:183], v[168:171], v[216:219]\n"
    "v_lshlrev_b32 v64, 16, v86\n"
    "v_mfma_f32_16x16x32_bf16 v[216:219], v[148:151], v[136:139], v[216:219]\n"
    "v_and_b32 v65, 0xffff0000, v86\n"
    "v_mfma_f32_16x16x32_bf16 v[212:215], v[148:151], v[132:135], v[212:215]\n"
    "v_mfma_f32_16x16x32_bf16 v[212:215], v[180:183], v[164:167], v[212:215]\n"
    "v_lshlrev_b32 v66, 16, v87\n"
    "v_mfma_f32_16x16x32_bf16 v[208:211], v[180:183], v[160:163], v[208:211]\n"
    "v_and_b32 v67, 0xffff0000, v87\n"
    "v_mfma_f32_16x16x32_bf16 v[208:211], v[148:151], v[128:131], v[208:211]\n"
    "v_lshlrev_b32 v68, 16, v88\n"
    "v_mfma_f32_16x16x32_bf16 v[224:227], v[152:155], v[128:131], v[224:227]\n"
    "v_and_b32 v69, 0xffff0000, v88\n"
    "v_mfma_f32_16x16x32_bf16 v[224:227], v[184:187], v[160:163], v[224:227]\n"
    "v_lshlrev_b32 v70, 16, v89\n"
    "v_mfma_f32_16x16x32_bf16 v[228:231], v[184:187], v[164:167], v[228:231]\n"
    "v_mfma_f32_16x16x32_bf16 v[228:231], v[152:155], v[132:135], v[228:231]\n"
    "v_and_b32 v71, 0xffff0000, v89\n"
    "v_mfma_f32_16x16x32_bf16 v[232:235], v[152:155], v[136:139], v[232:235]\n"
    "v_lshlrev_b32 v72, 16, v90\n"
    "v_mfma_f32_16x16x32_bf16 v[232:235], v[184:187], v[168:171], v[232:235]\n"
    "v_and_b32 v73, 0xffff0000, v90\n"
    "v_mfma_f32_16x16x32_bf16 v[236:239], v[184:187], v[172:175], v[236:239]\n"
    "v_lshlrev_b32 v74, 16, v91\n"
    "v_mfma_f32_16x16x32_bf16 v[236:239], v[152:155], v[140:143], v[236:239]\n"
    "v_and_b32 v75, 0xffff0000, v91\n"
    "v_mfma_f32_16x16x32_bf16 v[252:255], v[156:159], v[140:143], v[252:255]\n"
    "v_mfma_f32_16x16x32_bf16 v[252:255], v[188:191], v[172:175], v[252:255]\n"
    "s_lshl3_add_u32 s40, s2, 4096\n"
    "v_mfma_f32_16x16x32_bf16 v[248:251], v[188:191], v[168:171], v[248:251]\n"
    "s_lshl_b32 s41, s2, 2\n"
    "v_mfma_f32_16x16x32_bf16 v[248:251], v[156:159], v[136:139], v[248:251]\n"
    "v_mov_b32 v11, 40960\n"
    "v_mfma_f32_16x16x32_bf16 v[244:247], v[156:159], v[132:135], v[244:247]\n"
    "v_writelane_b32 v11, s40, 0\n"
    "v_mfma_f32_16x16x32_bf16 v[244:247], v[188:191], v[164:167], v[244:247]\n"
    "v_writelane_b32 v11, s41, 2\n"
    "v_mfma_f32_16x16x32_bf16 v[240:243], v[188:191], v[160:163], v[240:243]\n"
    "v_mfma_f32_16x16x32_bf16 v[240:243], v[156:159], v[128:131], v[240:243]\n"
    "s_waitcnt vmcnt(7)\n"
    "s_barrier\n"
    "s_mov_b64 exec, s[48:49]\n"
    "ds_write_b32 v25, v12 offset:20\n"
    "s_mov_b64 exec, -1\n"
    "s_add_u32 m0, s35, 0\n"
    "s_nop 0\n"
    "global_load_lds_dwordx4 v46, s[30:31] offset:0\n"
    "ds_read_b128 v[128:131], v56 offset:49152\n"
    "ds_read_b128 v[132:135], v56 offset:51200\n"
    "ds_read_b128 v[136:139], v56 offset:53248\n"
    "global_load_lds_dwordx4 v47, s[30:31] offset:1024\n"
    "s_bfe_u32 s66, s28, 0x80018\n"
    "s_mul_i32 s66, s66, 53248\n"
    "s_add_u32 s30, s6, s66\n"
    "s_addc_u32 s31, s7, 0\n"
    "ds_read_b128 v[140:143], v56 offset:55296\n"
    "ds_read_b64_tr_b16 v[144:145], v76 offset:49152\n"
    "ds_read_b64_tr_b16 v[146:147], v76 offset:50176\n"
    "s_add_u32 m0, s34, 0\n"
    "s_nop 0\n"
    "global_load_lds_dwordx4 v52, s[36:37] offset:0\n"
    "ds_read_b64_tr_b16 v[148:149], v77 offset:49152\n"
    "ds_read_b64_tr_b16 v[150:151], v77 offset:50176\n"
    "ds_read_b64_tr_b16 v[152:153], v78 offset:49152\n"
    "global_load_lds_dwordx4 v53, s[36:37] offset:1024\n"
    "ds_read_b64_tr_b16 v[154:155], v78 offset:50176\n"
    "ds_read_b64_tr_b16 v[156:157], v79 offset:49152\n"
    "ds_read_b64_tr_b16 v[158:159], v79 offset:50176\n"
    "global_load_lds_dwordx4 v54, s[36:37] offset:2048\n"
    "ds_read_b128 v[160:163], v57 offset:49152\n"
    "ds_read_b128 v[164:167], v57 offset:51200\n"
    "ds_read_b128 v[168:171], v57 offset:53248\n"
    "global_load_lds_dwordx4 v55, s[36:37] offset:3072\n"
    "ds_read_b128 v[172:175], v57 offset:55296\n"
    "ds_read_b64_tr_b16 v[176:177], v76 offset:57344\n"
    "ds_read_b64_tr_b16 v[178:179], v76 offset:58368\n"
    "global_load_dword v41, v18, s[38:39] offset:-8\n"
    "ds_read_b64_tr_b16 v[180:181], v77 offset:57344\n"
    "ds_read_b64_tr_b16 v[182:183], v77 offset:58368\n"
    "ds_read_b64_tr_b16 v[184:185], v78 offset:57344\n"
    "ds_read_b64_tr_b16 v[186:187], v78 offset:58368\n"
    "ds_read_b64_tr_b16 v[188:189], v79 offset:57344\n"
    "ds_read_b64_tr_b16 v[190:191], v79 offset:58368\n"
    "s_waitcnt vmcnt(12) lgkmcnt(0)\n"
    "s_barrier\n"
    "v_mfma_f32_16x16x32_bf16 v[192:195], v[144:147], v[128:131], v[192:195]\n"
    "v_lshlrev_b32 v113, 7, v113\n"
    "v_mfma_f32_16x16x32_bf16 v[192:195], v[176:179], v[160:163], v[192:195]\n"
    "v_mfma_f32_16x16x32_bf16 v[196:199], v[176:179], v[164:167], v[196:199]\n"
    "v_mfma_f32_16x16x32_bf16 v[196:199], v[144:147], v[132:135], v[196:199]\n"
    "v_mfma_f32_16x16x32_bf16 v[200:203], v[144:147], v[136:139], v[200:203]\n"
    "v_mfma_f32_16x16x32_bf16 v[200:203], v[176:179], v[168:171], v[200:203]\n"
    "v_mfma_f32_16x16x32_bf16 v[204:207], v[176:179], v[172:175], v[204:207]\n"
    "v_add_u32_dpp v52, v112, v48 quad_perm:[0,0,0,0] row_mask:0xf bank_mask:0xf\n"
    "v_mfma_f32_16x16x32_bf16 v[204:207], v[144:147], v[140:143], v[204:207]\n"
    "v_mfma_f32_16x16x32_bf16 v[220:223], v[148:151], v[140:143], v[220:223]\n"
    "v_mfma_f32_16x16x32_bf16 v[220:223], v[180:183], v[172:175], v[220:223]\n"
    "v_mfma_f32_16x16x32_bf16 v[216:219], v[180:183], v[168:171], v[216:219]\n"
    "v_mfma_f32_16x16x32_bf16 v[216:219], v[148:151], v[136:139], v[216:219]\n"
    "v_mfma_f32_16x16x32_bf16 v[212:215], v[148:151], v[132:135], v[212:215]\n"
    "v_add_u32_dpp v53, v112, v49 quad_perm:[1,1,1,1] row_mask:0xf bank_mask:0xf\n"
    "v_mfma_f32_16x16x32_bf16 v[212:215], v[180:183], v[164:167], v[212:215]\n"
    "v_mfma_f32_16x16x32_bf16 v[208:211], v[180:183], v[160:163], v[208:211]\n"
    "v_mfma_f32_16x16x32_bf16 v[208:211], v[148:151], v[128:131], v[208:211]\n"
    "v_mfma_f32_16x16x32_bf16 v[224:227], v[152:155], v[128:131], v[224:227]\n"
    "v_mfma_f32_16x16x32_bf16 v[224:227], v[184:187], v[160:163], v[224:227]\n"
    "v_mfma_f32_16x16x32_bf16 v[228:231], v[184:187], v[164:167], v[228:231]\n"
    "v_add_u32_dpp v54, v112, v50 quad_perm:[2,2,2,2] row_mask:0xf bank_mask:0xf\n"
    "v_mfma_f32_16x16x32_bf16 v[228:231], v[152:155], v[132:135], v[228:231]\n"
    "v_mfma_f32_16x16x32_bf16 v[232:235], v[152:155], v[136:139], v[232:235]\n"
    "v_mfma_f32_16x16x32_bf16 v[232:235], v[184:187], v[168:171], v[232:235]\n"
    "v_mfma_f32_16x16x32_bf16 v[236:239], v[184:187], v[172:175], v[236:239]\n"
    "v_mfma_f32_16x16x32_bf16 v[236:239], v[152:155], v[140:143], v[236:239]\n"
    "v_mfma_f32_16x16x32_bf16 v[252:255], v[156:159], v[140:143], v[252:255]\n"
    "v_add_u32_dpp v55, v112, v51 quad_perm:[3,3,3,3] row_mask:0xf bank_mask:0xf\n"
    "v_mfma_f32_16x16x32_bf16 v[252:255], v[188:191], v[172:175], v[252:255]\n"
    "v_mfma_f32_16x16x32_bf16 v[248:251], v[188:191], v[168:171], v[248:251]\n"
    "v_mfma_f32_16x16x32_bf16 v[248:251], v[156:159], v[136:139], v[248:251]\n"
    "v_mfma_f32_16x16x32_bf16 v[244:247], v[156:159], v[132:135], v[244:247]\n"
    "v_mfma_f32_16x16x32_bf16 v[244:247], v[188:191], v[164:167], v[244:247]\n"
    "v_mfma_f32_16x16x32_bf16 v[240:243], v[188:191], v[160:163], v[240:243]\n"
    "v_mfma_f32_16x16x32_bf16 v[240:243], v[156:159], v[128:131], v[240:243]\n"
    "s_waitcnt vmcnt(8)\n"
    "s_barrier\n"
    "s_add_u32 m0, s35, 49152\n"
    "s_nop 0\n"
    "global_load_lds_dwordx4 v46, s[30:31] offset:0\n"
    "ds_read_b128 v[128:131], v58 offset:0\n"
    "ds_read_b128 v[132:135], v58 offset:2048\n"
    "ds_read_b128 v[136:139], v58 offset:4096\n"
    "ds_read_b128 v[140:143], v58 offset:6144\n"
    "global_load_lds_dwordx4 v47, s[30:31] offset:1024\n"
    "s_add_u32 s30, s30, 212992\n"
    "s_addc_u32 s31, s31, 0\n"
    "ds_read_b64_tr_b16 v[144:145], v80 offset:0\n"
    "ds_read_b64_tr_b16 v[146:147], v80 offset:1024\n"
    "ds_read_b64_tr_b16 v[148:149], v81 offset:0\n"
    "ds_read_b64_tr_b16 v[150:151], v81 offset:1024\n"
    "s_add_u32 m0, s34, 49152\n"
    "s_nop 0\n"
    "global_load_lds_dwordx4 v52, s[36:37] offset:0\n"
    "ds_read_b64_tr_b16 v[152:153], v82 offset:0\n"
    "ds_read_b64_tr_b16 v[154:155], v82 offset:1024\n"
    "ds_read_b64_tr_b16 v[156:157], v83 offset:0\n"
    "ds_read_b64_tr_b16 v[158:159], v83 offset:1024\n"
    "global_load_lds_dwordx4 v53, s[36:37] offset:1024\n"
    "ds_read_b128 v[160:163], v59 offset:0\n"
    "ds_read_b128 v[164:167], v59 offset:2048\n"
    "ds_read_b128 v[168:171], v59 offset:4096\n"
    "ds_read_b128 v[172:175], v59 offset:6144\n"
    "global_load_lds_dwordx4 v54, s[36:37] offset:2048\n"
    "ds_read_b64_tr_b16 v[176:177], v80 offset:8192\n"
    "ds_read_b64_tr_b16 v[178:179], v80 offset:9216\n"
    "ds_read_b64_tr_b16 v[180:181], v81 offset:8192\n"
    "ds_read_b64_tr_b16 v[182:183], v81 offset:9216\n"
    "global_load_lds_dwordx4 v55, s[36:37] offset:3072\n"
    "ds_read_b64_tr_b16 v[184:185], v82 offset:8192\n"
    "ds_read_b64_tr_b16 v[186:187], v82 offset:9216\n"
    "ds_read_b64_tr_b16 v[188:189], v83 offset:8192\n"
    "ds_read_b64_tr_b16 v[190:191], v83 offset:9216\n"
    "s_waitcnt vmcnt(11) lgkmcnt(0)\n"
    "s_barrier\n"
    "v_mfma_f32_16x16x32_bf16 v[192:195], v[144:147], v[128:131], v[192:195]\n"
    "v_lshlrev_b32 v114, 7, v114\n"
    "v_mfma_f32_16x16x32_bf16 v[192:195], v[176:179], v[160:163], v[192:195]\n"
    "v_mfma_f32_16x16x32_bf16 v[196:199], v[176:179], v[164:167], v[196:199]\n"
    "v_mfma_f32_16x16x32_bf16 v[196:199], v[144:147], v[132:135], v[196:199]\n"
    "v_mfma_f32_16x16x32_bf16 v[200:203], v[144:147], v[136:139], v[200:203]\n"
    "v_mfma_f32_16x16x32_bf16 v[200:203], v[176:179], v[168:171], v[200:203]\n"
    "v_mfma_f32_16x16x32_bf16 v[204:207], v[176:179], v[172:175], v[204:207]\n"
    "v_add_u32_dpp v52, v113, v48 quad_perm:[0,0,0,0] row_mask:0xf bank_mask:0xf\n"
    "v_mfma_f32_16x16x32_bf16 v[204:207], v[144:147], v[140:143], v[204:207]\n"
    "v_mfma_f32_16x16x32_bf16 v[220:223], v[148:151], v[140:143], v[220:223]\n"
    "v_mfma_f32_16x16x32_bf16 v[220:223], v[180:183], v[172:175], v[220:223]\n"
    "v_mfma_f32_16x16x32_bf16 v[216:219], v[180:183], v[168:171], v[216:219]\n"
    "v_mfma_f32_16x16x32_bf16 v[216:219], v[148:151], v[136:139], v[216:219]\n"
    "v_mfma_f32_16x16x32_bf16 v[212:215], v[148:151], v[132:135], v[212:215]\n"
    "v_add_u32_dpp v53, v113, v49 quad_perm:[1,1,1,1] row_mask:0xf bank_mask:0xf\n"
    "v_mfma_f32_16x16x32_bf16 v[212:215], v[180:183], v[164:167], v[212:215]\n"
    "v_mfma_f32_16x16x32_bf16 v[208:211], v[180:183], v[160:163], v[208:211]\n"
    "v_mfma_f32_16x16x32_bf16 v[208:211], v[148:151], v[128:131], v[208:211]\n"
    "v_mfma_f32_16x16x32_bf16 v[224:227], v[152:155], v[128:131], v[224:227]\n"
    "v_mfma_f32_16x16x32_bf16 v[224:227], v[184:187], v[160:163], v[224:227]\n"
    "v_mfma_f32_16x16x32_bf16 v[228:231], v[184:187], v[164:167], v[228:231]\n"
    "v_add_u32_dpp v54, v113, v50 quad_perm:[2,2,2,2] row_mask:0xf bank_mask:0xf\n"
    "v_mfma_f32_16x16x32_bf16 v[228:231], v[152:155], v[132:135], v[228:231]\n"
    "v_mfma_f32_16x16x32_bf16 v[232:235], v[152:155], v[136:139], v[232:235]\n"
    "v_mfma_f32_16x16x32_bf16 v[232:235], v[184:187], v[168:171], v[232:235]\n"
    "v_mfma_f32_16x16x32_bf16 v[236:239], v[184:187], v[172:175], v[236:239]\n"
    "v_mfma_f32_16x16x32_bf16 v[236:239], v[152:155], v[140:143], v[236:239]\n"
    "v_mfma_f32_16x16x32_bf16 v[252:255], v[156:159], v[140:143], v[252:255]\n"
    "v_add_u32_dpp v55, v113, v51 quad_perm:[3,3,3,3] row_mask:0xf bank_mask:0xf\n"
    "v_mfma_f32_16x16x32_bf16 v[252:255], v[188:191], v[172:175], v[252:255]\n"
    "v_mfma_f32_16x16x32_bf16 v[248:251], v[188:191], v[168:171], v[248:251]\n"
    "v_mfma_f32_16x16x32_bf16 v[248:251], v[156:159], v[136:139], v[248:251]\n"
    "v_mfma_f32_16x16x32_bf16 v[244:247], v[156:159], v[132:135], v[244:247]\n"
    "v_mfma_f32_16x16x32_bf16 v[244:247], v[188:191], v[164:167], v[244:247]\n"
    "v_mfma_f32_16x16x32_bf16 v[240:243], v[188:191], v[160:163], v[240:243]\n"
    "v_mfma_f32_16x16x32_bf16 v[240:243], v[156:159], v[128:131], v[240:243]\n"
    "s_waitcnt vmcnt(7)\n"
    "s_barrier\n"
    "s_add_u32 m0, s35, 98304\n"
    "s_nop 0\n"
    "global_load_lds_dwordx4 v46, s[30:31] offset:0\n"
    "ds_read_b128 v[128:131], v56 offset:0\n"
    "ds_read_b128 v[132:135], v56 offset:2048\n"
    "ds_read_b128 v[136:139], v56 offset:4096\n"
    "ds_read_b128 v[140:143], v56 offset:6144\n"
    "global_load_lds_dwordx4 v47, s[30:31] offset:1024\n"
    "s_add_u32 s30, s30, 212992\n"
    "s_addc_u32 s31, s31, 0\n"
    "ds_read_b64_tr_b16 v[144:145], v76 offset:0\n"
    "ds_read_b64_tr_b16 v[146:147], v76 offset:1024\n"
    "ds_read_b64_tr_b16 v[148:149], v77 offset:0\n"
    "ds_read_b64_tr_b16 v[150:151], v77 offset:1024\n"
    "s_add_u32 m0, s34, 98304\n"
    "s_nop 0\n"
    "global_load_lds_dwordx4 v52, s[36:37] offset:0\n"
    "ds_read_b64_tr_b16 v[152:153], v78 offset:0\n"
    "ds_read_b64_tr_b16 v[154:155], v78 offset:1024\n"
    "ds_read_b64_tr_b16 v[156:157], v79 offset:0\n"
    "ds_read_b64_tr_b16 v[158:159], v79 offset:1024\n"
    "global_load_lds_dwordx4 v53, s[36:37] offset:1024\n"
    "ds_read_b128 v[160:163], v57 offset:0\n"
    "ds_read_b128 v[164:167], v57 offset:2048\n"
    "ds_read_b128 v[168:171], v57 offset:4096\n"
    "ds_read_b128 v[172:175], v57 offset:6144\n"
    "global_load_lds_dwordx4 v54, s[36:37] offset:2048\n"
    "ds_read_b64_tr_b16 v[176:177], v76 offset:8192\n"
    "ds_read_b64_tr_b16 v[178:179], v76 offset:9216\n"
    "ds_read_b64_tr_b16 v[180:181], v77 offset:8192\n"
    "ds_read_b64_tr_b16 v[182:183], v77 offset:9216\n"
    "global_load_lds_dwordx4 v55, s[36:37] offset:3072\n"
    "ds_read_b64_tr_b16 v[184:185], v78 offset:8192\n"
    "ds_read_b64_tr_b16 v[186:187], v78 offset:9216\n"
    "ds_read_b64_tr_b16 v[188:189], v79 offset:8192\n"
    "ds_read_b64_tr_b16 v[190:191], v79 offset:9216\n"
    "s_waitcnt vmcnt(10) lgkmcnt(0)\n"
    "s_barrier\n"
    "v_mfma_f32_16x16x32_bf16 v[192:195], v[144:147], v[128:131], v[192:195]\n"
    "v_lshlrev_b32 v115, 7, v115\n"
    "v_mfma_f32_16x16x32_bf16 v[192:195], v[176:179], v[160:163], v[192:195]\n"
    "v_mfma_f32_16x16x32_bf16 v[196:199], v[176:179], v[164:167], v[196:199]\n"
    "v_mfma_f32_16x16x32_bf16 v[196:199], v[144:147], v[132:135], v[196:199]\n"
    "v_add_u32_dpp v52, v114, v48 quad_perm:[0,0,0,0] row_mask:0xf bank_mask:0xf\n"
    "v_mfma_f32_16x16x32_bf16 v[200:203], v[144:147], v[136:139], v[200:203]\n"
    "v_mfma_f32_16x16x32_bf16 v[200:203], v[176:179], v[168:171], v[200:203]\n"
    "v_mfma_f32_16x16x32_bf16 v[204:207], v[176:179], v[172:175], v[204:207]\n"
    "v_add_u32_dpp v53, v114, v49 quad_perm:[1,1,1,1] row_mask:0xf bank_mask:0xf\n"
    "v_mfma_f32_16x16x32_bf16 v[204:207], v[144:147], v[140:143], v[204:207]\n"
    "v_mfma_f32_16x16x32_bf16 v[220:223], v[148:151], v[140:143], v[220:223]\n"
    "v_mfma_f32_16x16x32_bf16 v[220:223], v[180:183], v[172:175], v[220:223]\n"
    "v_mfma_f32_16x16x32_bf16 v[216:219], v[180:183], v[168:171], v[216:219]\n"
    "v_add_u32_dpp v54, v114, v50 quad_perm:[2,2,2,2] row_mask:0xf bank_mask:0xf\n"
    "v_mfma_f32_16x16x32_bf16 v[216:219], v[148:151], v[136:139], v[216:219]\n"
    "v_mfma_f32_16x16x32_bf16 v[212:215], v[148:151], v[132:135], v[212:215]\n"
    "v_mfma_f32_16x16x32_bf16 v[212:215], v[180:183], v[164:167], v[212:215]\n"
    "v_add_u32_dpp v55, v114, v51 quad_perm:[3,3,3,3] row_mask:0xf bank_mask:0xf\n"
    "v_mfma_f32_16x16x32_bf16 v[208:211], v[180:183], v[160:163], v[208:211]\n"
    "v_mfma_f32_16x16x32_bf16 v[208:211], v[148:151], v[128:131], v[208:211]\n"
    "v_mfma_f32_16x16x32_bf16 v[224:227], v[152:155], v[128:131], v[224:227]\n"
    "v_mfma_f32_16x16x32_bf16 v[224:227], v[184:187], v[160:163], v[224:227]\n"
    "v_lshl_or_b32 v36, v36, 7, v19\n"
    "v_mfma_f32_16x16x32_bf16 v[228:231], v[184:187], v[164:167], v[228:231]\n"
    "v_mfma_f32_16x16x32_bf16 v[228:231], v[152:155], v[132:135], v[228:231]\n"
    "v_mfma_f32_16x16x32_bf16 v[232:235], v[152:155], v[136:139], v[232:235]\n"
    "v_lshl_or_b32 v37, v37, 7, v19\n"
    "v_mfma_f32_16x16x32_bf16 v[232:235], v[184:187], v[168:171], v[232:235]\n"
    "v_mfma_f32_16x16x32_bf16 v[236:239], v[184:187], v[172:175], v[236:239]\n"
    "v_mfma_f32_16x16x32_bf16 v[236:239], v[152:155], v[140:143], v[236:239]\n"
    "v_mfma_f32_16x16x32_bf16 v[252:255], v[156:159], v[140:143], v[252:255]\n"
    "v_lshl_or_b32 v38, v38, 7, v19\n"
    "v_mfma_f32_16x16x32_bf16 v[252:255], v[188:191], v[172:175], v[252:255]\n"
    "v_mfma_f32_16x16x32_bf16 v[248:251], v[188:191], v[168:171], v[248:251]\n"
    "v_mfma_f32_16x16x32_bf16 v[248:251], v[156:159], v[136:139], v[248:251]\n"
    "v_lshl_or_b32 v41, v41, 7, v20\n"
    "v_mfma_f32_16x16x32_bf16 v[244:247], v[156:159], v[132:135], v[244:247]\n"
    "v_mfma_f32_16x16x32_bf16 v[244:247], v[188:191], v[164:167], v[244:247]\n"
    "v_mfma_f32_16x16x32_bf16 v[240:243], v[188:191], v[160:163], v[240:243]\n"
    "v_mfma_f32_16x16x32_bf16 v[240:243], v[156:159], v[128:131], v[240:243]\n"
    "s_waitcnt vmcnt(6)\n"
    "s_barrier\n"
    "s_add_u32 m0, s35, 0\n"
    "s_nop 0\n"
    "global_load_lds_dwordx4 v46, s[30:31] offset:0\n"
    "ds_read_b128 v[128:131], v56 offset:49152\n"
    "ds_read_b128 v[132:135], v56 offset:51200\n"
    "ds_read_b128 v[136:139], v56 offset:53248\n"
    "global_load_lds_dwordx4 v47, s[30:31] offset:1024\n"
    "s_add_u32 s30, s30, 212992\n"
    "s_addc_u32 s31, s31, 0\n"
    "ds_read_b128 v[140:143], v56 offset:55296\n"
    "ds_read_b64_tr_b16 v[144:145], v76 offset:49152\n"
    "ds_read_b64_tr_b16 v[146:147], v76 offset:50176\n"
    "s_add_u32 m0, s34, 0\n"
    "s_nop 0\n"
    "global_load_lds_dwordx4 v52, s[36:37] offset:0\n"
    "ds_read_b64_tr_b16 v[148:149], v77 offset:49152\n"
    "ds_read_b64_tr_b16 v[150:151], v77 offset:50176\n"
    "ds_read_b64_tr_b16 v[152:153], v78 offset:49152\n"
    "global_load_lds_dwordx4 v53, s[36:37] offset:1024\n"
    "ds_read_b64_tr_b16 v[154:155], v78 offset:50176\n"
    "ds_read_b64_tr_b16 v[156:157], v79 offset:49152\n"
    "ds_read_b64_tr_b16 v[158:159], v79 offset:50176\n"
    "global_load_lds_dwordx4 v54, s[36:37] offset:2048\n"
    "ds_read_b128 v[160:163], v57 offset:49152\n"
    "ds_read_b128 v[164:167], v57 offset:51200\n"
    "ds_read_b128 v[168:171], v57 offset:53248\n"
    "global_load_lds_dwordx4 v55, s[36:37] offset:3072\n"
    "ds_read_b128 v[172:175], v57 offset:55296\n"
    "ds_read_b64_tr_b16 v[176:177], v76 offset:57344\n"
    "ds_read_b64_tr_b16 v[178:179], v76 offset:58368\n"
    "global_load_dwordx4 v[28:31], v36, s[8:9]\n"
    "ds_read_b64_tr_b16 v[180:181], v77 offset:57344\n"
    "ds_read_b64_tr_b16 v[182:183], v77 offset:58368\n"
    "ds_read_b64_tr_b16 v[184:185], v78 offset:57344\n"
    "ds_read_b64_tr_b16 v[186:187], v78 offset:58368\n"
    "ds_read_b64_tr_b16 v[188:189], v79 offset:57344\n"
    "ds_read_b64_tr_b16 v[190:191], v79 offset:58368\n"
    "s_waitcnt vmcnt(11) lgkmcnt(0)\n"
    "s_barrier\n"
    "v_mfma_f32_16x16x32_bf16 v[192:195], v[144:147], v[128:131], v[192:195]\n"
    "v_lshlrev_b32 v116, 7, v116\n"
    "v_mfma_f32_16x16x32_bf16 v[192:195], v[176:179], v[160:163], v[192:195]\n"
    "v_mfma_f32_16x16x32_bf16 v[196:199], v[176:179], v[164:167], v[196:199]\n"
    "v_mfma_f32_16x16x32_bf16 v[196:199], v[144:147], v[132:135], v[196:199]\n"
    "v_mfma_f32_16x16x32_bf16 v[200:203], v[144:147], v[136:139], v[200:203]\n"
    "v_mfma_f32_16x16x32_bf16 v[200:203], v[176:179], v[168:171], v[200:203]\n"
    "v_mfma_f32_16x16x32_bf16 v[204:207], v[176:179], v[172:175], v[204:207]\n"
    "v_add_u32_dpp v52, v115, v48 quad_perm:[0,0,0,0] row_mask:0xf bank_mask:0xf\n"
    "v_mfma_f32_16x16x32_bf16 v[204:207], v[144:147], v[140:143], v[204:207]\n"
    "v_mfma_f32_16x16x32_bf16 v[220:223], v[148:151], v[140:143], v[220:223]\n"
    "v_mfma_f32_16x16x32_bf16 v[220:223], v[180:183], v[172:175], v[220:223]\n"
    "v_mfma_f32_16x16x32_bf16 v[216:219], v[180:183], v[168:171], v[216:219]\n"
    "v_mfma_f32_16x16x32_bf16 v[216:219], v[148:151], v[136:139], v[216:219]\n"
    "v_mfma_f32_16x16x32_bf16 v[212:215], v[148:151], v[132:135], v[212:215]\n"
    "v_add_u32_dpp v53, v115, v49 quad_perm:[1,1,1,1] row_mask:0xf bank_mask:0xf\n"
    "v_mfma_f32_16x16x32_bf16 v[212:215], v[180:183], v[164:167], v[212:215]\n"
    "v_mfma_f32_16x16x32_bf16 v[208:211], v[180:183], v[160:163], v[208:211]\n"
    "v_mfma_f32_16x16x32_bf16 v[208:211], v[148:151], v[128:131], v[208:211]\n"
    "v_mfma_f32_16x16x32_bf16 v[224:227], v[152:155], v[128:131], v[224:227]\n"
    "v_mfma_f32_16x16x32_bf16 v[224:227], v[184:187], v[160:163], v[224:227]\n"
    "v_mfma_f32_16x16x32_bf16 v[228:231], v[184:187], v[164:167], v[228:231]\n"
    "v_add_u32_dpp v54, v115, v50 quad_perm:[2,2,2,2] row_mask:0xf bank_mask:0xf\n"
    "v_mfma_f32_16x16x32_bf16 v[228:231], v[152:155], v[132:135], v[228:231]\n"
    "v_mfma_f32_16x16x32_bf16 v[232:235], v[152:155], v[136:139], v[232:235]\n"
    "v_mfma_f32_16x16x32_bf16 v[232:235], v[184:187], v[168:171], v[232:235]\n"
    "v_mfma_f32_16x16x32_bf16 v[236:239], v[184:187], v[172:175], v[236:239]\n"
    "v_mfma_f32_16x16x32_bf16 v[236:239], v[152:155], v[140:143], v[236:239]\n"
    "v_mfma_f32_16x16x32_bf16 v[252:255], v[156:159], v[140:143], v[252:255]\n"
    "v_add_u32_dpp v55, v115, v51 quad_perm:[3,3,3,3] row_mask:0xf bank_mask:0xf\n"
    "v_mfma_f32_16x16x32_bf16 v[252:255], v[188:191], v[172:175], v[252:255]\n"
    "v_mfma_f32_16x16x32_bf16 v[248:251], v[188:191], v[168:171], v[248:251]\n"
    "v_mfma_f32_16x16x32_bf16 v[248:251], v[156:159], v[136:139], v[248:251]\n"
    "v_mfma_f32_16x16x32_bf16 v[244:247], v[156:159], v[132:135], v[244:247]\n"
    "v_mfma_f32_16x16x32_bf16 v[244:247], v[188:191], v[164:167], v[244:247]\n"
    "v_mfma_f32_16x16x32_bf16 v[240:243], v[188:191], v[160:163], v[240:243]\n"
    "v_mfma_f32_16x16x32_bf16 v[240:243], v[156:159], v[128:131], v[240:243]\n"
    "s_waitcnt vmcnt(7)\n"
    "s_barrier\n"
    "s_add_u32 m0, s35, 49152\n"
    "s_nop 0\n"
    "global_load_lds_dwordx4 v46, s[30:31] offset:0\n"
    "ds_read_b128 v[128:131], v58 offset:0\n"
    "ds_read_b128 v[132:135], v58 offset:2048\n"
    "ds_read_b128 v[136:139], v58 offset:4096\n"
    "global_load_lds_dwordx4 v47, s[30:31] offset:1024\n"
    "s_bfe_u32 s66, s29, 0x80000\n"
    "s_mul_i32 s66, s66, 53248\n"
    "s_add_u32 s30, s6, s66\n"
    "s_addc_u32 s31, s7, 0\n"
    "ds_read_b128 v[140:143], v58 offset:6144\n"
    "ds_read_b64_tr_b16 v[144:145], v80 offset:0\n"
    "ds_read_b64_tr_b16 v[146:147], v80 offset:1024\n"
    "s_add_u32 m0, s34, 49152\n"
    "s_nop 0\n"
    "global_load_lds_dwordx4 v52, s[36:37] offset:0\n"
    "ds_read_b64_tr_b16 v[148:149], v81 offset:0\n"
    "ds_read_b64_tr_b16 v[150:151], v81 offset:1024\n"
    "ds_read_b64_tr_b16 v[152:153], v82 offset:0\n"
    "global_load_lds_dwordx4 v53, s[36:37] offset:1024\n"
    "ds_read_b64_tr_b16 v[154:155], v82 offset:1024\n"
    "ds_read_b64_tr_b16 v[156:157], v83 offset:0\n"
    "ds_read_b64_tr_b16 v[158:159], v83 offset:1024\n"
    "global_load_lds_dwordx4 v54, s[36:37] offset:2048\n"
    "ds_read_b128 v[160:163], v59 offset:0\n"
    "ds_read_b128 v[164:167], v59 offset:2048\n"
    "ds_read_b128 v[168:171], v59 offset:4096\n"
    "global_load_lds_dwordx4 v55, s[36:37] offset:3072\n"
    "ds_read_b128 v[172:175], v59 offset:6144\n"
    "ds_read_b64_tr_b16 v[176:177], v80 offset:8192\n"
    "ds_read_b64_tr_b16 v[178:179], v80 offset:9216\n"
    "global_load_dwordx4 v[32:35], v37, s[8:9]\n"
    "ds_read_b64_tr_b16 v[180:181], v81 offset:8192\n"
    "ds_read_b64_tr_b16 v[182:183], v81 offset:9216\n"
    "ds_read_b64_tr_b16 v[184:185], v82 offset:8192\n"
    "ds_read_b64_tr_b16 v[186:187], v82 offset:9216\n"
    "ds_read_b64_tr_b16 v[188:189], v83 offset:8192\n"
    "ds_read_b64_tr_b16 v[190:191], v83 offset:9216\n"
    "s_waitcnt vmcnt(12) lgkmcnt(0)\n"
    "s_barrier\n"
    "v_mfma_f32_16x16x32_bf16 v[192:195], v[144:147], v[128:131], v[192:195]\n"
    "v_lshlrev_b32 v117, 7, v117\n"
    "v_mfma_f32_16x16x32_bf16 v[192:195], v[176:179], v[160:163], v[192:195]\n"
    "v_mfma_f32_16x16x32_bf16 v[196:199], v[176:179], v[164:167], v[196:199]\n"
    "v_mfma_f32_16x16x32_bf16 v[196:199], v[144:147], v[132:135], v[196:199]\n"
    "v_mfma_f32_16x16x32_bf16 v[200:203], v[144:147], v[136:139], v[200:203]\n"
    "v_mfma_f32_16x16x32_bf16 v[200:203], v[176:179], v[168:171], v[200:203]\n"
    "v_mfma_f32_16x16x32_bf16 v[204:207], v[176:179], v[172:175], v[204:207]\n"
    "v_add_u32_dpp v52, v116, v48 quad_perm:[0,0,0,0] row_mask:0xf bank_mask:0xf\n"
    "v_mfma_f32_16x16x32_bf16 v[204:207], v[144:147], v[140:143], v[204:207]\n"
    "v_mfma_f32_16x16x32_bf16 v[220:223], v[148:151], v[140:143], v[220:223]\n"
    "v_mfma_f32_16x16x32_bf16 v[220:223], v[180:183], v[172:175], v[220:223]\n"
    "v_mfma_f32_16x16x32_bf16 v[216:219], v[180:183], v[168:171], v[216:219]\n"
    "v_mfma_f32_16x16x32_bf16 v[216:219], v[148:151], v[136:139], v[216:219]\n"
    "v_mfma_f32_16x16x32_bf16 v[212:215], v[148:151], v[132:135], v[212:215]\n"
    "v_add_u32_dpp v53, v116, v49 quad_perm:[1,1,1,1] row_mask:0xf bank_mask:0xf\n"
    "v_mfma_f32_16x16x32_bf16 v[212:215], v[180:183], v[164:167], v[212:215]\n"
    "v_mfma_f32_16x16x32_bf16 v[208:211], v[180:183], v[160:163], v[208:211]\n"
    "v_mfma_f32_16x16x32_bf16 v[208:211], v[148:151], v[128:131], v[208:211]\n"
    "v_mfma_f32_16x16x32_bf16 v[224:227], v[152:155], v[128:131], v[224:227]\n"
    "v_mfma_f32_16x16x32_bf16 v[224:227], v[184:187], v[160:163], v[224:227]\n"
    "v_mfma_f32_16x16x32_bf16 v[228:231], v[184:187], v[164:167], v[228:231]\n"
    "v_add_u32_dpp v54, v116, v50 quad_perm:[2,2,2,2] row_mask:0xf bank_mask:0xf\n"
    "v_mfma_f32_16x16x32_bf16 v[228:231], v[152:155], v[132:135], v[228:231]\n"
    "v_mfma_f32_16x16x32_bf16 v[232:235], v[152:155], v[136:139], v[232:235]\n"
    "v_mfma_f32_16x16x32_bf16 v[232:235], v[184:187], v[168:171], v[232:235]\n"
    "v_mfma_f32_16x16x32_bf16 v[236:239], v[184:187], v[172:175], v[236:239]\n"
    "v_mfma_f32_16x16x32_bf16 v[236:239], v[152:155], v[140:143], v[236:239]\n"
    "v_mfma_f32_16x16x32_bf16 v[252:255], v[156:159], v[140:143], v[252:255]\n"
    "v_add_u32_dpp v55, v116, v51 quad_perm:[3,3,3,3] row_mask:0xf bank_mask:0xf\n"
    "v_mfma_f32_16x16x32_bf16 v[252:255], v[188:191], v[172:175], v[252:255]\n"
    "v_mfma_f32_16x16x32_bf16 v[248:251], v[188:191], v[168:171], v[248:251]\n"
    "v_mfma_f32_16x16x32_bf16 v[248:251], v[156:159], v[136:139], v[248:251]\n"
    "v_mfma_f32_16x16x32_bf16 v[244:247], v[156:159], v[132:135], v[244:247]\n"
    "v_mfma_f32_16x16x32_bf16 v[244:247], v[188:191], v[164:167], v[244:247]\n"
    "v_mfma_f32_16x16x32_bf16 v[240:243], v[188:191], v[160:163], v[240:243]\n"
    "v_mfma_f32_16x16x32_bf16 v[240:243], v[156:159], v[128:131], v[240:243]\n"
    "s_waitcnt vmcnt(8)\n"
    "s_barrier\n"
    "s_cmp_eq_u32 s20, 3\n"
    "s_cbranch_scc1 .Lxd_on2\n"
    ".Lxd_on2r:\n"
    "s_add_u32 m0, s35, 98304\n"
    "s_nop 0\n"
    "global_load_lds_dwordx4 v46, s[30:31] offset:0\n"
    "ds_read_b128 v[128:131], v56 offset:0\n"
    "ds_read_b128 v[132:135], v56 offset:2048\n"
    "ds_read_b128 v[136:139], v56 offset:4096\n"
    "global_load_lds_dwordx4 v47, s[30:31] offset:1024\n"
    "s_add_u32 s30, s30, 212992\n"
    "s_addc_u32 s31, s31, 0\n"
    "ds_read_b128 v[140:143], v56 offset:6144\n"
    "ds_read_b64_tr_b16 v[144:145], v76 offset:0\n"
    "ds_read_b64_tr_b16 v[146:147], v76 offset:1024\n"
    "s_add_u32 m0, s34, 98304\n"
    "s_nop 0\n"
    "global_load_lds_dwordx4 v52, s[36:37] offset:0\n"
    "ds_read_b64_tr_b16 v[148:149], v77 offset:0\n"
    "ds_read_b64_tr_b16 v[150:151], v77 offset:1024\n"
    "ds_read_b64_tr_b16 v[152:153], v78 offset:0\n"
    "global_load_lds_dwordx4 v53, s[36:37] offset:1024\n"
    "ds_read_b64_tr_b16 v[154:155], v78 offset:1024\n"
    "ds_read_b64_tr_b16 v[156:157], v79 offset:0\n"
    "ds_read_b64_tr_b16 v[158:159], v79 offset:1024\n"
    "global_load_lds_dwordx4 v54, s[36:37] offset:2048\n"
    "ds_read_b128 v[160:163], v57 offset:0\n"
    "ds_read_b128 v[164:167], v57 offset:2048\n"
    "ds_read_b128 v[168:171], v57 offset:4096\n"
    "global_load_lds_dwordx4 v55, s[36:37] offset:3072\n"
    "ds_read_b128 v[172:175], v57 offset:6144\n"
    "ds_read_b64_tr_b16 v[176:177], v76 offset:8192\n"
    "ds_read_b64_tr_b16 v[178:179], v76 offset:9216\n"
    "global_load_dword v40, v41, s[8:9]\n"
    "ds_read_b64_tr_b16 v[180:181], v77 offset:8192\n"
    "ds_read_b64_tr_b16 v[182:183], v77 offset:9216\n"
    "ds_read_b64_tr_b16 v[184:185], v78 offset:8192\n"
    "ds_read_b64_tr_b16 v[186:187], v78 offset:9216\n"
    "ds_read_b64_tr_b16 v[188:189], v79 offset:8192\n"
    "ds_read_b64_tr_b16 v[190:191], v79 offset:9216\n"
    "s_waitcnt vmcnt(12) lgkmcnt(0)\n"
    "s_barrier\n"
    "v_mfma_f32_16x16x32_bf16 v[192:195], v[144:147], v[128:131], v[192:195]\n"
    "v_lshlrev_b32 v118, 7, v118\n"
    "v_mfma_f32_16x16x32_bf16 v[192:195], v[176:179], v[160:163], v[192:195]\n"
    "v_mfma_f32_16x16x32_bf16 v[196:199], v[176:179], v[164:167], v[196:199]\n"
    "v_mfma_f32_16x16x32_bf16 v[196:199], v[144:147], v[132:135], v[196:199]\n"
    "v_mfma_f32_16x16x32_bf16 v[200:203], v[144:147], v[136:139], v[200:203]\n"
    "v_mfma_f32_16x16x32_bf16 v[200:203], v[176:179], v[168:171], v[200:203]\n"
    "v_mfma_f32_16x16x32_bf16 v[204:207], v[176:179], v[172:175], v[204:207]\n"
    "v_add_u32_dpp v52, v117, v48 quad_perm:[0,0,0,0] row_mask:0xf bank_mask:0xf\n"
    "v_mfma_f32_16x16x32_bf16 v[204:207], v[144:147], v[140:143], v[204:207]\n"
    "v_mfma_f32_16x16x32_bf16 v[220:223], v[148:151], v[140:143], v[220:223]\n"
    "v_mfma_f32_16x16x32_bf16 v[220:223], v[180:183], v[172:175], v[220:223]\n"
    "v_mfma_f32_16x16x32_bf16 v[216:219], v[180:183], v[168:171], v[216:219]\n"
    "v_mfma_f32_16x16x32_bf16 v[216:219], v[148:151], v[136:139], v[216:219]\n"
    "v_mfma_f32_16x16x32_bf16 v[212:215], v[148:151], v[132:135], v[212:215]\n"
    "v_add_u32_dpp v53, v117, v49 quad_perm:[1,1,1,1] row_mask:0xf bank_mask:0xf\n"
    "v_mfma_f32_16x16x32_bf16 v[212:215], v[180:183], v[164:167], v[212:215]\n"
    "v_mfma_f32_16x16x32_bf16 v[208:211], v[180:183], v[160:163], v[208:211]\n"
    "v_mfma_f32_16x16x32_bf16 v[208:211], v[148:151], v[128:131], v[208:211]\n"
    "v_mfma_f32_16x16x32_bf16 v[224:227], v[152:155], v[128:131], v[224:227]\n"
    "v_mfma_f32_16x16x32_bf16 v[224:227], v[184:187], v[160:163], v[224:227]\n"
    "v_mfma_f32_16x16x32_bf16 v[228:231], v[184:187], v[164:167], v[228:231]\n"
    "v_add_u32_dpp v54, v117, v50 quad_perm:[2,2,2,2] row_mask:0xf bank_mask:0xf\n"
    "v_mfma_f32_16x16x32_bf16 v[228:231], v[152:155], v[132:135], v[228:231]\n"
    "v_mfma_f32_16x16x32_bf16 v[232:235], v[152:155], v[136:139], v[232:235]\n"
    "v_mfma_f32_16x16x32_bf16 v[232:235], v[184:187], v[168:171], v[232:235]\n"
    "v_mfma_f32_16x16x32_bf16 v[236:239], v[184:187], v[172:175], v[236:239]\n"
    "v_mfma_f32_16x16x32_bf16 v[236:239], v[152:155], v[140:143], v[236:239]\n"
    "v_mfma_f32_16x16x32_bf16 v[252:255], v[156:159], v[140:143], v[252:255]\n"
    "v_add_u32_dpp v55, v117, v51 quad_perm:[3,3,3,3] row_mask:0xf bank_mask:0xf\n"
    "v_mfma_f32_16x16x32_bf16 v[252:255], v[188:191], v[172:175], v[252:255]\n"
    "v_mfma_f32_16x16x32_bf16 v[248:251], v[188:191], v[168:171], v[248:251]\n"
    "v_mfma_f32_16x16x32_bf16 v[248:251], v[156:159], v[136:139], v[248:251]\n"
    "v_mfma_f32_16x16x32_bf16 v[244:247], v[156:159], v[132:135], v[244:247]\n"
    "v_mfma_f32_16x16x32_bf16 v[244:247], v[188:191], v[164:167], v[244:247]\n"
    "v_mfma_f32_16x16x32_bf16 v[240:243], v[188:191], v[160:163], v[240:243]\n"
    "v_mfma_f32_16x16x32_bf16 v[240:243], v[156:159], v[128:131], v[240:243]\n"
    "s_waitcnt vmcnt(8)\n"
    "s_barrier\n"
    "s_add_u32 m0, s35, 0\n"
    "s_nop 0\n"
    "global_load_lds_dwordx4 v46, s[30:31] offset:0\n"
    "ds_read_b128 v[128:131], v56 offset:49152\n"
    "ds_read_b128 v[132:135], v56 offset:51200\n"
    "ds_read_b128 v[136:139], v56 offset:53248\n"
    "global_load_lds_dwordx4 v47, s[30:31] offset:1024\n"
    "s_add_u32 s30, s30, 212992\n"
    "s_addc_u32 s31, s31, 0\n"
    "ds_read_b128 v[140:143], v56 offset:55296\n"
    "ds_read_b64_tr_b16 v[144:145], v76 offset:49152\n"
    "ds_read_b64_tr_b16 v[146:147], v76 offset:50176\n"
    "s_add_u32 m0, s34, 0\n"
    "s_nop 0\n"
    "global_load_lds_dwordx4 v52, s[36:37] offset:0\n"
    "ds_read_b64_tr_b16 v[148:149], v77 offset:49152\n"
    "ds_read_b64_tr_b16 v[150:151], v77 offset:50176\n"
    "ds_read_b64_tr_b16 v[152:153], v78 offset:49152\n"
    "global_load_lds_dwordx4 v53, s[36:37] offset:1024\n"
    "ds_read_b64_tr_b16 v[154:155], v78 offset:50176\n"
    "ds_read_b64_tr_b16 v[156:157], v79 offset:49152\n"
    "ds_read_b64_tr_b16 v[158:159], v79 offset:50176\n"
    "global_load_lds_dwordx4 v54, s[36:37] offset:2048\n"
    "ds_read_b128 v[160:163], v57 offset:49152\n"
    "ds_read_b128 v[164:167], v57 offset:51200\n"
    "ds_read_b128 v[168:171], v57 offset:53248\n"
    "global_load_lds_dwordx4 v55, s[36:37] offset:3072\n"
    "ds_read_b128 v[172:175], v57 offset:55296\n"
    "ds_read_b64_tr_b16 v[176:177], v76 offset:57344\n"
    "ds_read_b64_tr_b16 v[178:179], v76 offset:58368\n"
    "global_load_dwordx4 v[36:39], v38, s[8:9]\n"
    "ds_read_b64_tr_b16 v[180:181], v77 offset:57344\n"
    "ds_read_b64_tr_b16 v[182:183], v77 offset:58368\n"
    "ds_read_b64_tr_b16 v[184:185], v78 offset:57344\n"
    "ds_read_b64_tr_b16 v[186:187], v78 offset:58368\n"
    "ds_read_b64_tr_b16 v[188:189], v79 offset:57344\n"
    "ds_read_b64_tr_b16 v[190:191], v79 offset:58368\n"
    "s_waitcnt vmcnt(12) lgkmcnt(0)\n"
    "s_barrier\n"
    "v_mfma_f32_16x16x32_bf16 v[192:195], v[144:147], v[128:131], v[192:195]\n"
    "v_lshlrev_b32 v119, 7, v119\n"
    "v_mfma_f32_16x16x32_bf16 v[192:195], v[176:179], v[160:163], v[192:195]\n"
    "v_mfma_f32_16x16x32_bf16 v[196:199], v[176:179], v[164:167], v[196:199]\n"
    "v_mfma_f32_16x16x32_bf16 v[196:199], v[144:147], v[132:135], v[196:199]\n"
    "v_mfma_f32_16x16x32_bf16 v[200:203], v[144:147], v[136:139], v[200:203]\n"
    "v_mfma_f32_16x16x32_bf16 v[200:203], v[176:179], v[168:171], v[200:203]\n"
    "v_mfma_f32_16x16x32_bf16 v[204:207], v[176:179], v[172:175], v[204:207]\n"
    "v_add_u32_dpp v52, v118, v48 quad_perm:[0,0,0,0] row_mask:0xf bank_mask:0xf\n"
    "v_mfma_f32_16x16x32_bf16 v[204:207], v[144:147], v[140:143], v[204:207]\n"
    "v_mfma_f32_16x16x32_bf16 v[220:223], v[148:151], v[140:143], v[220:223]\n"
    "v_mfma_f32_16x16x32_bf16 v[220:223], v[180:183], v[172:175], v[220:223]\n"
    "v_mfma_f32_16x16x32_bf16 v[216:219], v[180:183], v[168:171], v[216:219]\n"
    "v_mfma_f32_16x16x32_bf16 v[216:219], v[148:151], v[136:139], v[216:219]\n"
    "v_mfma_f32_16x16x32_bf16 v[212:215], v[148:151], v[132:135], v[212:215]\n"
    "v_add_u32_dpp v53, v118, v49 quad_perm:[1,1,1,1] row_mask:0xf bank_mask:0xf\n"
    "v_mfma_f32_16x16x32_bf16 v[212:215], v[180:183], v[164:167], v[212:215]\n"
    "v_mfma_f32_16x16x32_bf16 v[208:211], v[180:183], v[160:163], v[208:211]\n"
    "v_mfma_f32_16x16x32_bf16 v[208:211], v[148:151], v[128:131], v[208:211]\n"
    "v_mfma_f32_16x16x32_bf16 v[224:227], v[152:155], v[128:131], v[224:227]\n"
    "v_mfma_f32_16x16x32_bf16 v[224:227], v[184:187], v[160:163], v[224:227]\n"
    "v_mfma_f32_16x16x32_bf16 v[228:231], v[184:187], v[164:167], v[228:231]\n"
    "v_add_u32_dpp v54, v118, v50 quad_perm:[2,2,2,2] row_mask:0xf bank_mask:0xf\n"
    "v_mfma_f32_16x16x32_bf16 v[228:231], v[152:155], v[132:135], v[228:231]\n"
    "v_mfma_f32_16x16x32_bf16 v[232:235], v[152:155], v[136:139], v[232:235]\n"
    "v_mfma_f32_16x16x32_bf16 v[232:235], v[184:187], v[168:171], v[232:235]\n"
    "v_mfma_f32_16x16x32_bf16 v[236:239], v[184:187], v[172:175], v[236:239]\n"
    "v_mfma_f32_16x16x32_bf16 v[236:239], v[152:155], v[140:143], v[236:239]\n"
    "v_mfma_f32_16x16x32_bf16 v[252:255], v[156:159], v[140:143], v[252:255]\n"
    "v_add_u32_dpp v55, v118, v51 quad_perm:[3,3,3,3] row_mask:0xf bank_mask:0xf\n"
    "v_mfma_f32_16x16x32_bf16 v[252:255], v[188:191], v[172:175], v[252:255]\n"
    "v_mfma_f32_16x16x32_bf16 v[248:251], v[188:191], v[168:171], v[248:251]\n"
    "v_mfma_f32_16x16x32_bf16 v[248:251], v[156:159], v[136:139], v[248:251]\n"
    "v_mfma_f32_16x16x32_bf16 v[244:247], v[156:159], v[132:135], v[244:247]\n"
    "v_mfma_f32_16x16x32_bf16 v[244:247], v[188:191], v[164:167], v[244:247]\n"
    "v_mfma_f32_16x16x32_bf16 v[240:243], v[188:191], v[160:163], v[240:243]\n"
    "v_mfma_f32_16x16x32_bf16 v[240:243], v[156:159], v[128:131], v[240:243]\n"
    "s_waitcnt vmcnt(8)\n"
    "s_barrier\n"
    "s_add_u32 m0, s35, 49152\n"
    "s_nop 0\n"
    "global_load_lds_dwordx4 v46, s[30:31] offset:0\n"
    "ds_read_b128 v[128:131], v58 offset:0\n"
    "ds_read_b128 v[132:135], v58 offset:2048\n"
    "ds_read_b128 v[136:139], v58 offset:4096\n"
    "ds_read_b128 v[140:143], v58 offset:6144\n"
    "global_load_lds_dwordx4 v47, s[30:31] offset:1024\n"
    "s_add_u32 s30, s30, 212992\n"
    "s_addc_u32 s31, s31, 0\n"
    "ds_read_b64_tr_b16 v[144:145], v80 offset:0\n"
    "ds_read_b64_tr_b16 v[146:147], v80 offset:1024\n"
    "ds_read_b64_tr_b16 v[148:149], v81 offset:0\n"
    "ds_read_b64_tr_b16 v[150:151], v81 offset:1024\n"
    "s_add_u32 m0, s34, 49152\n"
    "s_nop 0\n"
    "global_load_lds_dwordx4 v52, s[36:37] offset:0\n"
    "ds_read_b64_tr_b16 v[152:153], v82 offset:0\n"
    "ds_read_b64_tr_b16 v[154:155], v82 offset:1024\n"
    "ds_read_b64_tr_b16 v[156:157], v83 offset:0\n"
    "ds_read_b64_tr_b16 v[158:159], v83 offset:1024\n"
    "global_load_lds_dwordx4 v53, s[36:37] offset:1024\n"
    "ds_read_b128 v[160:163], v59 offset:0\n"
    "ds_read_b128 v[164:167], v59 offset:2048\n"
    "ds_read_b128 v[168:171], v59 offset:4096\n"
    "ds_read_b128 v[172:175], v59 offset:6144\n"
    "global_load_lds_dwordx4 v54, s[36:37] offset:2048\n"
    "ds_read_b64_tr_b16 v[176:177], v80 offset:8192\n"
    "ds_read_b64_tr_b16 v[178:179], v80 offset:9216\n"
    "ds_read_b64_tr_b16 v[180:181], v81 offset:8192\n"
    "ds_read_b64_tr_b16 v[182:183], v81 offset:9216\n"
    "global_load_lds_dwordx4 v55, s[36:37] offset:3072\n"
    "ds_read_b64_tr_b16 v[184:185], v82 offset:8192\n"
    "ds_read_b64_tr_b16 v[186:187], v82 offset:9216\n"
    "ds_read_b64_tr_b16 v[188:189], v83 offset:8192\n"
    "ds_read_b64_tr_b16 v[190:191], v83 offset:9216\n"
    "s_waitcnt vmcnt(11) lgkmcnt(0)\n"
    "s_barrier\n"
    "v_mfma_f32_16x16x32_bf16 v[192:195], v[144:147], v[128:131], v[192:195]\n"
    "v_lshlrev_b32 v120, 7, v120\n"
    "v_mfma_f32_16x16x32_bf16 v[192:195], v[176:179], v[160:163], v[192:195]\n"
    "v_add_u32_dpp v52, v119, v48 quad_perm:[0,0,0,0] row_mask:0xf bank_mask:0xf\n"
    "v_mfma_f32_16x16x32_bf16 v[196:199], v[176:179], v[164:167], v[196:199]\n"
    "v_add_u32_dpp v53, v119, v49 quad_perm:[1,1,1,1] row_mask:0xf bank_mask:0xf\n"
    "v_mfma_f32_16x16x32_bf16 v[196:199], v[144:147], v[132:135], v[196:199]\n"
    "v_add_u32_dpp v54, v119, v50 quad_perm:[2,2,2,2] row_mask:0xf bank_mask:0xf\n"
    "v_mfma_f32_16x16x32_bf16 v[200:203], v[144:147], v[136:139], v[200:203]\n"
    "v_add_u32_dpp v55, v119, v51 quad_perm:[3,3,3,3] row_mask:0xf bank_mask:0xf\n"
    "v_mfma_f32_16x16x32_bf16 v[200:203], v[176:179], v[168:171], v[200:203]\n"
    "v_readlane_b32 s25, v26, 2\n"
    "v_mfma_f32_16x16x32_bf16 v[204:207], v[176:179], v[172:175], v[204:207]\n"
    "v_readlane_b32 s40, v26, 0\n"
    "v_mfma_f32_16x16x32_bf16 v[204:207], v[144:147], v[140:143], v[204:207]\n"
    "v_readlane_b32 s41, v27, 0\n"
    "v_mfma_f32_16x16x32_bf16 v[220:223], v[148:151], v[140:143], v[220:223]\n"
    "v_readlane_b32 s66, v26, 1\n"
    "v_mfma_f32_16x16x32_bf16 v[220:223], v[180:183], v[172:175], v[220:223]\n"
    "v_readlane_b32 s67, v27, 1\n"
    "v_mfma_f32_16x16x32_bf16 v[216:219], v[180:183], v[168:171], v[216:219]\n"
    "s_mul_i32 s42, s25, 0x9e3779b1\n"
    "v_mfma_f32_16x16x32_bf16 v[216:219], v[148:151], v[136:139], v[216:219]\n"
    "s_lshr_b32 s42, s42, 24\n"
    "v_mfma_f32_16x16x32_bf16 v[212:215], v[148:151], v[132:135], v[212:215]\n"
    "s_xor_b32 s42, s42, s40\n"
    "v_mfma_f32_16x16x32_bf16 v[212:215], v[180:183], v[164:167], v[212:215]\n"
    "s_and_b32 s42, s42, 0xff\n"
    "v_mfma_f32_16x16x32_bf16 v[208:211], v[180:183], v[160:163], v[208:211]\n"
    "s_xor_b32 s41, s41, s25\n"
    "v_mfma_f32_16x16x32_bf16 v[208:211], v[148:151], v[128:131], v[208:211]\n"
    "s_xor_b32 s41, s41, 0xa5c30f96\n"
    "v_mfma_f32_16x16x32_bf16 v[224:227], v[152:155], v[128:131], v[224:227]\n"
    "s_or_b32 s42, s41, s42\n"
    "v_mfma_f32_16x16x32_bf16 v[224:227], v[184:187], v[160:163], v[224:227]\n"
    "s_lshr_b32 s41, s40, 8\n"
    "v_mfma_f32_16x16x32_bf16 v[228:231], v[184:187], v[164:167], v[228:231]\n"
    "s_mul_i32 s40, s41, 0x9e3779b1\n"
    "v_mfma_f32_16x16x32_bf16 v[228:231], v[152:155], v[132:135], v[228:231]\n"
    "s_lshr_b32 s40, s40, 24\n"
    "v_mfma_f32_16x16x32_bf16 v[232:235], v[152:155], v[136:139], v[232:235]\n"
    "s_xor_b32 s41, s41, 0xa5c30f96\n"
    "v_mfma_f32_16x16x32_bf16 v[232:235], v[184:187], v[168:171], v[232:235]\n"
    "s_cmp_eq_u64 s[66:67], s[40:41]\n"
    "v_mfma_f32_16x16x32_bf16 v[236:239], v[184:187], v[172:175], v[236:239]\n"
    "s_cselect_b32 s42, s42, 1\n"
    "v_mfma_f32_16x16x32_bf16 v[236:239], v[152:155], v[140:143], v[236:239]\n"
    "s_cmp_eq_u32 s20, 3\n"
    "v_mfma_f32_16x16x32_bf16 v[252:255], v[156:159], v[140:143], v[252:255]\n"
    "s_cselect_b32 s42, s42, 0\n"
    "v_mfma_f32_16x16x32_bf16 v[252:255], v[188:191], v[172:175], v[252:255]\n"
    "s_cmp_lg_u32 s42, 0\n"
    "v_mfma_f32_16x16x32_bf16 v[248:251], v[188:191], v[168:171], v[248:251]\n"
    "s_cbranch_scc1 .Lxd_pv3\n"
    ".Lxd_pv3r:\n"
    "v_mfma_f32_16x16x32_bf16 v[248:251], v[156:159], v[136:139], v[248:251]\n"
    "v_mov_b32 v26, s40\n"
    "v_mfma_f32_16x16x32_bf16 v[244:247], v[156:159], v[132:135], v[244:247]\n"
    "v_mov_b32 v27, s41\n"
    "v_mfma_f32_16x16x32_bf16 v[244:247], v[188:191], v[164:167], v[244:247]\n"
    "v_mov_b32 v13, 154112\n"
    "v_mfma_f32_16x16x32_bf16 v[240:243], v[188:191], v[160:163], v[240:243]\n"
    "v_add_u32_e64 v43, s25, 1\n"
    "v_mfma_f32_16x16x32_bf16 v[240:243], v[156:159], v[128:131], v[240:243]\n"
    "s_waitcnt vmcnt(7)\n"
    "s_barrier\n"
    "s_cmp_eq_u32 s20, 3\n"
    "s_cbranch_scc1 .Lxd_on4\n"
    ".Lxd_on4r:\n"
    "s_add_u32 m0, s35, 98304\n"
    "s_nop 0\n"
    "global_load_lds_dwordx4 v46, s[30:31] offset:0\n"
    "ds_read_b128 v[128:131], v56 offset:0\n"
    "ds_read_b128 v[132:135], v56 offset:2048\n"
    "ds_read_b128 v[136:139], v56 offset:4096\n"
    "ds_read_b128 v[140:143], v56 offset:6144\n"
    "global_load_lds_dwordx4 v47, s[30:31] offset:1024\n"
    "s_bfe_u32 s66, s29, 0x80008\n"
    "s_mul_i32 s66, s66, 53248\n"
    "s_add_u32 s30, s6, s66\n"
    "s_addc_u32 s31, s7, 0\n"
    "ds_read_b64_tr_b16 v[144:145], v76 offset:0\n"
    "ds_read_b64_tr_b16 v[146:147], v76 offset:1024\n"
    "ds_read_b64_tr_b16 v[148:149], v77 offset:0\n"
    "ds_read_b64_tr_b16 v[150:151], v77 offset:1024\n"
    "s_add_u32 m0, s34, 98304\n"
    "s_nop 0\n"
    "global_load_lds_dwordx4 v52, s[36:37] offset:0\n"
    "ds_read_b64_tr_b16 v[152:153], v78 offset:0\n"
    "ds_read_b64_tr_b16 v[154:155], v78 offset:1024\n"
    "ds_read_b64_tr_b16 v[156:157], v79 offset:0\n"
    "ds_read_b64_tr_b16 v[158:159], v79 offset:1024\n"
    "global_load_lds_dwordx4 v53, s[36:37] offset:1024\n"
    "ds_read_b128 v[160:163], v57 offset:0\n"
    "ds_read_b128 v[164:167], v57 offset:2048\n"
    "ds_read_b128 v[168:171], v57 offset:4096\n"
    "ds_read_b128 v[172:175], v57 offset:6144\n"
    "global_load_lds_dwordx4 v54, s[36:37] offset:2048\n"
    "ds_read_b64_tr_b16 v[176:177], v76 offset:8192\n"
    "ds_read_b64_tr_b16 v[178:179], v76 offset:9216\n"
    "ds_read_b64_tr_b16 v[180:181], v77 offset:8192\n"
    "ds_read_b64_tr_b16 v[182:183], v77 offset:9216\n"
    "global_load_lds_dwordx4 v55, s[36:37] offset:3072\n"
    "ds_read_b64_tr_b16 v[184:185], v78 offset:8192\n"
    "ds_read_b64_tr_b16 v[186:187], v78 offset:9216\n"
    "ds_read_b64_tr_b16 v[188:189], v79 offset:8192\n"
    "ds_read_b64_tr_b16 v[190:191], v79 offset:9216\n"
    "s_waitcnt vmcnt(10) lgkmcnt(0)\n"
    "s_barrier\n"
    "v_mfma_f32_16x16x32_bf16 v[192:195], v[144:147], v[128:131], v[192:195]\n"
    "v_lshlrev_b32 v121, 7, v121\n"
    "v_mfma_f32_16x16x32_bf16 v[192:195], v[176:179], v[160:163], v[192:195]\n"
    "v_add_u32_dpp v52, v120, v48 quad_perm:[0,0,0,0] row_mask:0xf bank_mask:0xf\n"
    "v_mfma_f32_16x16x32_bf16 v[196:199], v[176:179], v[164:167], v[196:199]\n"
    "v_mfma_f32_16x16x32_bf16 v[196:199], v[144:147], v[132:135], v[196:199]\n"
    "v_add_u32_dpp v53, v120, v49 quad_perm:[1,1,1,1] row_mask:0xf bank_mask:0xf\n"
    "v_mfma_f32_16x16x32_bf16 v[200:203], v[144:147], v[136:139], v[200:203]\n"
    "v_add_u32_dpp v54, v120, v50 quad_perm:[2,2,2,2] row_mask:0xf bank_mask:0xf\n"
    "v_mfma_f32_16x16x32_bf16 v[200:203], v[176:179], v[168:171], v[200:203]\n"
    "v_mfma_f32_16x16x32_bf16 v[204:207], v[176:179], v[172:175], v[204:207]\n"
    "v_add_u32_dpp v55, v120, v51 quad_perm:[3,3,3,3] row_mask:0xf bank_mask:0xf\n"
    "v_mfma_f32_16x16x32_bf16 v[204:207], v[144:147], v[140:143], v[204:207]\n"
    "v_mfma_f32_16x16x32_bf16 v[220:223], v[148:151], v[140:143], v[220:223]\n"
    "s_movk_i32 s43, 15744\n"
    "v_pk_maximum3_f16 v21, v28, v28, s43 op_sel_hi:[1,1,0] neg_lo:[0,1,0] neg_hi:[0,1,0]\n"
    "v_mfma_f32_16x16x32_bf16 v[220:223], v[180:183], v[172:175], v[220:223]\n"
    "v_pk_maximum3_f16 v21, v29, v29, v21 neg_lo:[0,1,0] neg_hi:[0,1,0]\n"
    "v_mfma_f32_16x16x32_bf16 v[216:219], v[180:183], v[168:171], v[216:219]\n"
    "v_mfma_f32_16x16x32_bf16 v[216:219], v[148:151], v[136:139], v[216:219]\n"
    "v_pk_maximum3_f16 v21, v30, v30, v21 neg_lo:[0,1,0] neg_hi:[0,1,0]\n"
    "v_mfma_f32_16x16x32_bf16 v[212:215], v[148:151], v[132:135], v[212:215]\n"
    "v_mfma_f32_16x16x32_bf16 v[212:215], v[180:183], v[164:167], v[212:215]\n"
    "v_pk_maximum3_f16 v21, v31, v31, v21 neg_lo:[0,1,0] neg_hi:[0,1,0]\n"
    "v_mfma_f32_16x16x32_bf16 v[208:211], v[180:183], v[160:163], v[208:211]\n"
    "v_pk_maximum3_f16 v21, v32, v32, v21 neg_lo:[0,1,0] neg_hi:[0,1,0]\n"
    "v_mfma_f32_16x16x32_bf16 v[208:211], v[148:151], v[128:131], v[208:211]\n"
    "v_mfma_f32_16x16x32_bf16 v[224:227], v[152:155], v[128:131], v[224:227]\n"
    "v_pk_maximum3_f16 v21, v33, v33, v21 neg_lo:[0,1,0] neg_hi:[0,1,0]\n"
    "v_mfma_f32_16x16x32_bf16 v[224:227], v[184:187], v[160:163], v[224:227]\n"
    "v_pk_maximum3_f16 v21, v34, v34, v21 neg_lo:[0,1,0] neg_hi:[0,1,0]\n"
    "v_mfma_f32_16x16x32_bf16 v[228:231], v[184:187], v[164:167], v[228:231]\n"
    "v_mfma_f32_16x16x32_bf16 v[228:231], v[152:155], v[132:135], v[228:231]\n"
    "v_pk_maximum3_f16 v21, v35, v35, v21 neg_lo:[0,1,0] neg_hi:[0,1,0]\n"
    "v_mfma_f32_16x16x32_bf16 v[232:235], v[152:155], v[136:139], v[232:235]\n"
    "v_mfma_f32_16x16x32_bf16 v[232:235], v[184:187], v[168:171], v[232:235]\n"
    "v_pk_maximum3_f16 v21, v36, v36, v21 neg_lo:[0,1,0] neg_hi:[0,1,0]\n"
    "v_mfma_f32_16x16x32_bf16 v[236:239], v[184:187], v[172:175], v[236:239]\n"
    "v_pk_maximum3_f16 v21, v37, v37, v21 neg_lo:[0,1,0] neg_hi:[0,1,0]\n"
    "v_mfma_f32_16x16x32_bf16 v[236:239], v[152:155], v[140:143], v[236:239]\n"
    "v_mfma_f32_16x16x32_bf16 v[252:255], v[156:159], v[140:143], v[252:255]\n"
    "v_pk_maximum3_f16 v21, v38, v38, v21 neg_lo:[0,1,0] neg_hi:[0,1,0]\n"
    "v_mfma_f32_16x16x32_bf16 v[252:255], v[188:191], v[172:175], v[252:255]\n"
    "v_mfma_f32_16x16x32_bf16 v[248:251], v[188:191], v[168:171], v[248:251]\n"
    "v_pk_maximum3_f16 v21, v39, v39, v21 neg_lo:[0,1,0] neg_hi:[0,1,0]\n"
    "v_mfma_f32_16x16x32_bf16 v[248:251], v[156:159], v[136:139], v[248:251]\n"
    "v_pk_maximum3_f16 v21, v40, v40, v21 neg_lo:[0,1,0] neg_hi:[0,1,0]\n"
    "v_mfma_f32_16x16x32_bf16 v[244:247], v[156:159], v[132:135], v[244:247]\n"
    "v_mfma_f32_16x16x32_bf16 v[244:247], v[188:191], v[164:167], v[244:247]\n"
    "v_pk_max_u16 v21, v21, v21 op_sel:[0,1] op_sel_hi:[1,0]\n"
    "v_mfma_f32_16x16x32_bf16 v[240:243], v[188:191], v[160:163], v[240:243]\n"
    "v_mfma_f32_16x16x32_bf16 v[240:243], v[156:159], v[128:131], v[240:243]\n"
    "s_waitcnt vmcnt(6)\n"
    "s_barrier\n"
    "s_add_u32 m0, s35, 0\n"
    "s_nop 0\n"
    "global_load_lds_dwordx4 v46, s[30:31] offset:0\n"
    "ds_read_b128 v[128:131], v56 offset:49152\n"
    "ds_read_b128 v[132:135], v56 offset:51200\n"
    "ds_read_b128 v[136:139], v56 offset:53248\n"
    "ds_read_b128 v[140:143], v56 offset:55296\n"
    "global_load_lds_dwordx4 v47, s[30:31] offset:1024\n"
    "s_add_u32 s30, s30, 212992\n"
    "s_addc_u32 s31, s31, 0\n"
    "ds_read_b64_tr_b16 v[144:145], v76 offset:49152\n"
    "ds_read_b64_tr_b16 v[146:147], v76 offset:50176\n"
    "ds_read_b64_tr_b16 v[148:149], v77 offset:49152\n"
    "ds_read_b64_tr_b16 v[150:151], v77 offset:50176\n"
    "s_add_u32 m0, s34, 0\n"
    "s_nop 0\n"
    "global_load_lds_dwordx4 v52, s[36:37] offset:0\n"
    "ds_read_b64_tr_b16 v[152:153], v78 offset:49152\n"
    "ds_read_b64_tr_b16 v[154:155], v78 offset:50176\n"
    "ds_read_b64_tr_b16 v[156:157], v79 offset:49152\n"
    "ds_read_b64_tr_b16 v[158:159], v79 offset:50176\n"
    "global_load_lds_dwordx4 v53, s[36:37] offset:1024\n"
    "ds_read_b128 v[160:163], v57 offset:49152\n"
    "ds_read_b128 v[164:167], v57 offset:51200\n"
    "ds_read_b128 v[168:171], v57 offset:53248\n"
    "ds_read_b128 v[172:175], v57 offset:55296\n"
    "global_load_lds_dwordx4 v54, s[36:37] offset:2048\n"
    "ds_read_b64_tr_b16 v[176:177], v76 offset:57344\n"
    "ds_read_b64_tr_b16 v[178:179], v76 offset:58368\n"
    "ds_read_b64_tr_b16 v[180:181], v77 offset:57344\n"
    "ds_read_b64_tr_b16 v[182:183], v77 offset:58368\n"
    "global_load_lds_dwordx4 v55, s[36:37] offset:3072\n"
    "ds_read_b64_tr_b16 v[184:185], v78 offset:57344\n"
    "ds_read_b64_tr_b16 v[186:187], v78 offset:58368\n"
    "ds_read_b64_tr_b16 v[188:189], v79 offset:57344\n"
    "ds_read_b64_tr_b16 v[190:191], v79 offset:58368\n"
    "s_waitcnt vmcnt(10) lgkmcnt(0)\n"
    "s_barrier\n"
    "v_mfma_f32_16x16x32_bf16 v[192:195], v[144:147], v[128:131], v[192:195]\n"
    "v_add_u32_dpp v52, v121, v48 quad_perm:[0,0,0,0] row_mask:0xf bank_mask:0xf\n"
    "v_mfma_f32_16x16x32_bf16 v[192:195], v[176:179], v[160:163], v[192:195]\n"
    "v_add_u32_dpp v53, v121, v49 quad_perm:[1,1,1,1] row_mask:0xf bank_mask:0xf\n"
    "v_mfma_f32_16x16x32_bf16 v[196:199], v[176:179], v[164:167], v[196:199]\n"
    "v_add_u32_dpp v54, v121, v50 quad_perm:[2,2,2,2] row_mask:0xf bank_mask:0xf\n"
    "v_mfma_f32_16x16x32_bf16 v[196:199], v[144:147], v[132:135], v[196:199]\n"
    "v_add_u32_dpp v55, v121, v51 quad_perm:[3,3,3,3] row_mask:0xf bank_mask:0xf\n"
    "v_mfma_f32_16x16x32_bf16 v[200:203], v[144:147], v[136:139], v[200:203]\n"
    "v_mfma_f32_16x16x32_bf16 v[200:203], v[176:179], v[168:171], v[200:203]\n"
    "v_max_u32_dpp v21, v21, v21 quad_perm:[1,0,3,2] row_mask:0xf bank_mask:0xf\n"
    "v_mfma_f32_16x16x32_bf16 v[204:207], v[176:179], v[172:175], v[204:207]\n"
    "v_mfma_f32_16x16x32_bf16 v[204:207], v[144:147], v[140:143], v[204:207]\n"
    "v_max_u32_dpp v21, v21, v21 quad_perm:[2,3,0,1] row_mask:0xf bank_mask:0xf\n"
    "v_mfma_f32_16x16x32_bf16 v[220:223], v[148:151], v[140:143], v[220:223]\n"
    "v_mfma_f32_16x16x32_bf16 v[220:223], v[180:183], v[172:175], v[220:223]\n"
    "v_max_u32_dpp v21, v21, v21 row_half_mirror row_mask:0xf bank_mask:0xf\n"
    "v_mfma_f32_16x16x32_bf16 v[216:219], v[180:183], v[168:171], v[216:219]\n"
    "v_mfma_f32_16x16x32_bf16 v[216:219], v[148:151], v[136:139], v[216:219]\n"
    "v_max_u32_dpp v21, v21, v21 row_mirror row_mask:0xf bank_mask:0xf\n"
    "v_mfma_f32_16x16x32_bf16 v[212:215], v[148:151], v[132:135], v[212:215]\n"
    "v_mfma_f32_16x16x32_bf16 v[212:215], v[180:183], v[164:167], v[212:215]\n"
    "v_max_u32_dpp v21, v21, v21 row_bcast:15 row_mask:0xa bank_mask:0xf\n"
    "v_mfma_f32_16x16x32_bf16 v[208:211], v[180:183], v[160:163], v[208:211]\n"
    "v_mfma_f32_16x16x32_bf16 v[208:211], v[148:151], v[128:131], v[208:211]\n"
    "v_max_u32_dpp v21, v21, v21 row_bcast:31 row_mask:0xc bank_mask:0xf\n"
    "v_mfma_f32_16x16x32_bf16 v[224:227], v[152:155], v[128:131], v[224:227]\n"
    "v_mfma_f32_16x16x32_bf16 v[224:227], v[184:187], v[160:163], v[224:227]\n"
    "v_readlane_b32 s47, v21, 63\n"
    "v_mfma_f32_16x16x32_bf16 v[228:231], v[184:187], v[164:167], v[228:231]\n"
    "v_mfma_f32_16x16x32_bf16 v[228:231], v[152:155], v[132:135], v[228:231]\n"
    "s_and_b32 s50, s47, 0x7f80\n"
    "v_mfma_f32_16x16x32_bf16 v[232:235], v[152:155], v[136:139], v[232:235]\n"
    "s_lshr_b32 s47, s50, 6\n"
    "v_mfma_f32_16x16x32_bf16 v[232:235], v[184:187], v[168:171], v[232:235]\n"
    "s_sub_u32 s50, s50, 2816\n"
    "v_mfma_f32_16x16x32_bf16 v[236:239], v[184:187], v[172:175], v[236:239]\n"
    "s_mul_i32 s51, s50, -8\n"
    "v_mfma_f32_16x16x32_bf16 v[236:239], v[152:155], v[140:143], v[236:239]\n"
    "v_mfma_f32_16x16x32_bf16 v[252:255], v[156:159], v[140:143], v[252:255]\n"
    "v_mfma_f32_16x16x32_bf16 v[252:255], v[188:191], v[172:175], v[252:255]\n"
    "v_mfma_f32_16x16x32_bf16 v[248:251], v[188:191], v[168:171], v[248:251]\n"
    "v_mfma_f32_16x16x32_bf16 v[248:251], v[156:159], v[136:139], v[248:251]\n"
    "v_mfma_f32_16x16x32_bf16 v[244:247], v[156:159], v[132:135], v[244:247]\n"
    "v_mfma_f32_16x16x32_bf16 v[244:247], v[188:191], v[164:167], v[244:247]\n"
    "v_mfma_f32_16x16x32_bf16 v[240:243], v[188:191], v[160:163], v[240:243]\n"
    "v_mfma_f32_16x16x32_bf16 v[240:243], v[156:159], v[128:131], v[240:243]\n"
    "s_waitcnt vmcnt(6)\n"
    "s_barrier\n"
    "s_add_u32 m0, s35, 49152\n"
    "s_nop 0\n"
    "global_load_lds_dwordx4 v46, s[30:31] offset:0\n"
    "ds_read_b128 v[128:131], v58 offset:0\n"
    "ds_read_b128 v[132:135], v58 offset:2048\n"
    "ds_read_b128 v[136:139], v58 offset:4096\n"
    "ds_read_b128 v[140:143], v58 offset:6144\n"
    "global_load_lds_dwordx4 v47, s[30:31] offset:1024\n"
    "ds_read_b64_tr_b16 v[144:145], v80 offset:0\n"
    "ds_read_b64_tr_b16 v[146:147], v80 offset:1024\n"
    "ds_read_b64_tr_b16 v[148:149], v81 offset:0\n"
    "ds_read_b64_tr_b16 v[150:151], v81 offset:1024\n"
    "s_add_u32 m0, s34, 49152\n"
    "s_nop 0\n"
    "global_load_lds_dwordx4 v52, s[36:37] offset:0\n"
    "ds_read_b64_tr_b16 v[152:153], v82 offset:0\n"
    "ds_read_b64_tr_b16 v[154:155], v82 offset:1024\n"
    "ds_read_b64_tr_b16 v[156:157], v83 offset:0\n"
    "ds_read_b64_tr_b16 v[158:159], v83 offset:1024\n"
    "global_load_lds_dwordx4 v53, s[36:37] offset:1024\n"
    "ds_read_b128 v[160:163], v59 offset:0\n"
    "ds_read_b128 v[164:167], v59 offset:2048\n"
    "ds_read_b128 v[168:171], v59 offset:4096\n"
    "ds_read_b128 v[172:175], v59 offset:6144\n"
    "global_load_lds_dwordx4 v54, s[36:37] offset:2048\n"
    "ds_read_b64_tr_b16 v[176:177], v80 offset:8192\n"
    "ds_read_b64_tr_b16 v[178:179], v80 offset:9216\n"
    "ds_read_b64_tr_b16 v[180:181], v81 offset:8192\n"
    "ds_read_b64_tr_b16 v[182:183], v81 offset:9216\n"
    "global_load_lds_dwordx4 v55, s[36:37] offset:3072\n"
    "ds_read_b64_tr_b16 v[184:185], v82 offset:8192\n"
    "ds_read_b64_tr_b16 v[186:187], v82 offset:9216\n"
    "ds_read_b64_tr_b16 v[188:189], v83 offset:8192\n"
    "ds_read_b64_tr_b16 v[190:191], v83 offset:9216\n"
    "s_waitcnt vmcnt(10) lgkmcnt(0)\n"
    "s_barrier\n"
    "v_mfma_f32_16x16x32_bf16 v[192:195], v[144:147], v[128:131], v[192:195]\n"
    "v_pk_maximum3_f16 v21, v28, v28, s50 op_sel_hi:[1,1,0] neg_lo:[0,1,0] neg_hi:[0,1,0]\n"
    "v_mfma_f32_16x16x32_bf16 v[192:195], v[176:179], v[160:163], v[192:195]\n"
    "v_pk_mad_u16 v21, v21, 8, s51 op_sel_hi:[1,0,0]\n"
    "v_mfma_f32_16x16x32_bf16 v[196:199], v[176:179], v[164:167], v[196:199]\n"
    "v_bfi_b32 v28, s57, v21, v28\n"
    "v_mfma_f32_16x16x32_bf16 v[196:199], v[144:147], v[132:135], v[196:199]\n"
    "v_mfma_f32_16x16x32_bf16 v[200:203], v[144:147], v[136:139], v[200:203]\n"
    "v_pk_maximum3_f16 v12, v29, v29, s50 op_sel_hi:[1,1,0] neg_lo:[0,1,0] neg_hi:[0,1,0]\n"
    "v_mfma_f32_16x16x32_bf16 v[200:203], v[176:179], v[168:171], v[200:203]\n"
    "v_pk_mad_u16 v12, v12, 8, s51 op_sel_hi:[1,0,0]\n"
    "v_mfma_f32_16x16x32_bf16 v[204:207], v[176:179], v[172:175], v[204:207]\n"
    "v_bfi_b32 v29, s57, v12, v29\n"
    "v_mfma_f32_16x16x32_bf16 v[204:207], v[144:147], v[140:143], v[204:207]\n"
    "v_mfma_f32_16x16x32_bf16 v[220:223], v[148:151], v[140:143], v[220:223]\n"
    "v_pk_maximum3_f16 v21, v30, v30, s50 op_sel_hi:[1,1,0] neg_lo:[0,1,0] neg_hi:[0,1,0]\n"
    "v_mfma_f32_16x16x32_bf16 v[220:223], v[180:183], v[172:175], v[220:223]\n"
    "v_pk_mad_u16 v21, v21, 8, s51 op_sel_hi:[1,0,0]\n"
    "v_mfma_f32_16x16x32_bf16 v[216:219], v[180:183], v[168:171], v[216:219]\n"
    "v_bfi_b32 v30, s57, v21, v30\n"
    "v_mfma_f32_16x16x32_bf16 v[216:219], v[148:151], v[136:139], v[216:219]\n"
    "v_mfma_f32_16x16x32_bf16 v[212:215], v[148:151], v[132:135], v[212:215]\n"
    "v_pk_maximum3_f16 v12, v31, v31, s50 op_sel_hi:[1,1,0] neg_lo:[0,1,0] neg_hi:[0,1,0]\n"
    "v_mfma_f32_16x16x32_bf16 v[212:215], v[180:183], v[164:167], v[212:215]\n"
    "v_pk_mad_u16 v12, v12, 8, s51 op_sel_hi:[1,0,0]\n"
    "v_mfma_f32_16x16x32_bf16 v[208:211], v[180:183], v[160:163], v[208:211]\n"
    "v_bfi_b32 v31, s57, v12, v31\n"
    "v_mfma_f32_16x16x32_bf16 v[208:211], v[148:151], v[128:131], v[208:211]\n"
    "v_mfma_f32_16x16x32_bf16 v[224:227], v[152:155], v[128:131], v[224:227]\n"
    "v_pk_maximum3_f16 v21, v32, v32, s50 op_sel_hi:[1,1,0] neg_lo:[0,1,0] neg_hi:[0,1,0]\n"
    "v_mfma_f32_16x16x32_bf16 v[224:227], v[184:187], v[160:163], v[224:227]\n"
    "v_pk_mad_u16 v21, v21, 8, s51 op_sel_hi:[1,0,0]\n"
    "v_mfma_f32_16x16x32_bf16 v[228:231], v[184:187], v[164:167], v[228:231]\n"
    "v_bfi_b32 v32, s57, v21, v32\n"
    "v_mfma_f32_16x16x32_bf16 v[228:231], v[152:155], v[132:135], v[228:231]\n"
    "v_mfma_f32_16x16x32_bf16 v[232:235], v[152:155], v[136:139], v[232:235]\n"
    "v_pk_maximum3_f16 v12, v33, v33, s50 op_sel_hi:[1,1,0] neg_lo:[0,1,0] neg_hi:[0,1,0]\n"
    "v_mfma_f32_16x16x32_bf16 v[232:235], v[184:187], v[168:171], v[232:235]\n"
    "v_pk_mad_u16 v12, v12, 8, s51 op_sel_hi:[1,0,0]\n"
    "v_mfma_f32_16x16x32_bf16 v[236:239], v[184:187], v[172:175], v[236:239]\n"
    "v_bfi_b32 v33, s57, v12, v33\n"
    "v_mfma_f32_16x16x32_bf16 v[236:239], v[152:155], v[140:143], v[236:239]\n"
    "v_mfma_f32_16x16x32_bf16 v[252:255], v[156:159], v[140:143], v[252:255]\n"
    "v_pk_maximum3_f16 v21, v34, v34, s50 op_sel_hi:[1,1,0] neg_lo:[0,1,0] neg_hi:[0,1,0]\n"
    "v_mfma_f32_16x16x32_bf16 v[252:255], v[188:191], v[172:175], v[252:255]\n"
    "v_pk_mad_u16 v21, v21, 8, s51 op_sel_hi:[1,0,0]\n"
    "v_mfma_f32_16x16x32_bf16 v[248:251], v[188:191], v[168:171], v[248:251]\n"
    "v_bfi_b32 v34, s57, v21, v34\n"
    "v_mfma_f32_16x16x32_bf16 v[248:251], v[156:159], v[136:139], v[248:251]\n"
    "v_mfma_f32_16x16x32_bf16 v[244:247], v[156:159], v[132:135], v[244:247]\n"
    "v_lshlrev_b32 v52, 4, v1\n"
    "v_mfma_f32_16x16x32_bf16 v[244:247], v[188:191], v[164:167], v[244:247]\n"
    "v_add_u32 v53, 4096, v52\n"
    "v_mfma_f32_16x16x32_bf16 v[240:243], v[188:191], v[160:163], v[240:243]\n"
    "v_mfma_f32_16x16x32_bf16 v[240:243], v[156:159], v[128:131], v[240:243]\n"
    "s_waitcnt vmcnt(6)\n"
    "s_barrier\n"
    "ds_read_b64 v[26:27], v13\n"
    "ds_read_b32 v41, v25 offset:24\n"
    "s_mul_i32 s42, s20, 5376\n"
    "s_add_u32 s40, s42, 1048576\n"
    "s_add_u32 s38, s10, s40\n"
    "s_addc_u32 s39, s11, 0\n"
    "s_add_u32 m0, s42, 98304\n"
    "s_nop 0\n"
    "global_load_lds_dwordx4 v52, s[38:39] offset:0 sc1\n"
    "ds_read_b128 v[128:131], v56 offset:0\n"
    "ds_read_b128 v[132:135], v56 offset:2048\n"
    "ds_read_b128 v[136:139], v56 offset:4096\n"
    "ds_read_b128 v[140:143], v56 offset:6144\n"
    "global_load_lds_dwordx4 v52, s[38:39] offset:1024 sc1\n"
    "ds_read_b64_tr_b16 v[144:145], v76 offset:0\n"
    "ds_read_b64_tr_b16 v[146:147], v76 offset:1024\n"
    "ds_read_b64_tr_b16 v[148:149], v77 offset:0\n"
    "ds_read_b64_tr_b16 v[150:151], v77 offset:1024\n"
    "global_load_lds_dwordx4 v52, s[38:39] offset:2048 sc1\n"
    "ds_read_b64_tr_b16 v[152:153], v78 offset:0\n"
    "ds_read_b64_tr_b16 v[154:155], v78 offset:1024\n"
    "ds_read_b64_tr_b16 v[156:157], v79 offset:0\n"
    "ds_read_b64_tr_b16 v[158:159], v79 offset:1024\n"
    "global_load_lds_dwordx4 v52, s[38:39] offset:3072 sc1\n"
    "ds_read_b128 v[160:163], v57 offset:0\n"
    "ds_read_b128 v[164:167], v57 offset:2048\n"
    "ds_read_b128 v[168:171], v57 offset:4096\n"
    "ds_read_b128 v[172:175], v57 offset:6144\n"
    "s_add_u32 m0, s42, 102400\n"
    "s_nop 0\n"
    "global_load_lds_dwordx4 v53, s[38:39] offset:0 sc1\n"
    "ds_read_b64_tr_b16 v[176:177], v76 offset:8192\n"
    "ds_read_b64_tr_b16 v[178:179], v76 offset:9216\n"
    "ds_read_b64_tr_b16 v[180:181], v77 offset:8192\n"
    "ds_read_b64_tr_b16 v[182:183], v77 offset:9216\n"
    "global_load_lds_dwordx4 v53, s[38:39] offset:256 sc1\n"
    "ds_read_b64_tr_b16 v[184:185], v78 offset:8192\n"
    "ds_read_b64_tr_b16 v[186:187], v78 offset:9216\n"
    "ds_read_b64_tr_b16 v[188:189], v79 offset:8192\n"
    "ds_read_b64_tr_b16 v[190:191], v79 offset:9216\n"
    "s_waitcnt vmcnt(10) lgkmcnt(0)\n"
    "s_barrier\n"
    "v_mfma_f32_16x16x32_bf16 v[192:195], v[144:147], v[128:131], v[192:195]\n"
    "v_pk_maximum3_f16 v12, v35, v35, s50 op_sel_hi:[1,1,0] neg_lo:[0,1,0] neg_hi:[0,1,0]\n"
    "v_mfma_f32_16x16x32_bf16 v[192:195], v[176:179], v[160:163], v[192:195]\n"
    "v_pk_mad_u16 v12, v12, 8, s51 op_sel_hi:[1,0,0]\n"
    "v_mfma_f32_16x16x32_bf16 v[196:199], v[176:179], v[164:167], v[196:199]\n"
    "v_bfi_b32 v35, s57, v12, v35\n"
    "v_mfma_f32_16x16x32_bf16 v[196:199], v[144:147], v[132:135], v[196:199]\n"
    "v_mfma_f32_16x16x32_bf16 v[200:203], v[144:147], v[136:139], v[200:203]\n"
    "v_pk_maximum3_f16 v21, v36, v36, s50 op_sel_hi:[1,1,0] neg_lo:[0,1,0] neg_hi:[0,1,0]\n"
    "v_mfma_f32_16x16x32_bf16 v[200:203], v[176:179], v[168:171], v[200:203]\n"
    "v_pk_mad_u16 v21, v21, 8, s51 op_sel_hi:[1,0,0]\n"
    "v_mfma_f32_16x16x32_bf16 v[204:207], v[176:179], v[172:175], v[204:207]\n"
    "v_mfma_f32_16x16x32_bf16 v[204:207], v[144:147], v[140:143], v[204:207]\n"
    "v_bfi_b32 v36, s57, v21, v36\n"
    "v_mfma_f32_16x16x32_bf16 v[220:223], v[148:151], v[140:143], v[220:223]\n"
    "v_pk_maximum3_f16 v12, v37, v37, s50 op_sel_hi:[1,1,0] neg_lo:[0,1,0] neg_hi:[0,1,0]\n"
    "v_mfma_f32_16x16x32_bf16 v[220:223], v[180:183], v[172:175], v[220:223]\n"
    "v_mfma_f32_16x16x32_bf16 v[216:219], v[180:183], v[168:171], v[216:219]\n"
    "v_pk_mad_u16 v12, v12, 8, s51 op_sel_hi:[1,0,0]\n"
    "v_mfma_f32_16x16x32_bf16 v[216:219], v[148:151], v[136:139], v[216:219]\n"
    "v_bfi_b32 v37, s57, v12, v37\n"
    "v_mfma_f32_16x16x32_bf16 v[212:215], v[148:151], v[132:135], v[212:215]\n"
    "v_mfma_f32_16x16x32_bf16 v[212:215], v[180:183], v[164:167], v[212:215]\n"
    "v_pk_maximum3_f16 v21, v38, v38, s50 op_sel_hi:[1,1,0] neg_lo:[0,1,0] neg_hi:[0,1,0]\n"
    "v_mfma_f32_16x16x32_bf16 v[208:211], v[180:183], v[160:163], v[208:211]\n"
    "v_pk_mad_u16 v21, v21, 8, s51 op_sel_hi:[1,0,0]\n"
    "v_mfma_f32_16x16x32_bf16 v[208:211], v[148:151], v[128:131], v[208:211]\n"
    "v_mfma_f32_16x16x32_bf16 v[224:227], v[152:155], v[128:131], v[224:227]\n"
    "v_bfi_b32 v38, s57, v21, v38\n"
    "v_mfma_f32_16x16x32_bf16 v[224:227], v[184:187], v[160:163], v[224:227]\n"
    "v_pk_maximum3_f16 v12, v39, v39, s50 op_sel_hi:[1,1,0] neg_lo:[0,1,0] neg_hi:[0,1,0]\n"
    "v_mfma_f32_16x16x32_bf16 v[228:231], v[184:187], v[164:167], v[228:231]\n"
    "v_mfma_f32_16x16x32_bf16 v[228:231], v[152:155], v[132:135], v[228:231]\n"
    "v_pk_mad_u16 v12, v12, 8, s51 op_sel_hi:[1,0,0]\n"
    "v_mfma_f32_16x16x32_bf16 v[232:235], v[152:155], v[136:139], v[232:235]\n"
    "v_bfi_b32 v39, s57, v12, v39\n"
    "v_mfma_f32_16x16x32_bf16 v[232:235], v[184:187], v[168:171], v[232:235]\n"
    "v_mfma_f32_16x16x32_bf16 v[236:239], v[184:187], v[172:175], v[236:239]\n"
    "v_pk_maximum3_f16 v21, v40, v40, s50 op_sel_hi:[1,1,0] neg_lo:[0,1,0] neg_hi:[0,1,0]\n"
    "v_mfma_f32_16x16x32_bf16 v[236:239], v[152:155], v[140:143], v[236:239]\n"
    "v_pk_mad_u16 v21, v21, 8, s51 op_sel_hi:[1,0,0]\n"
    "v_mfma_f32_16x16x32_bf16 v[252:255], v[156:159], v[140:143], v[252:255]\n"
    "v_mfma_f32_16x16x32_bf16 v[252:255], v[188:191], v[172:175], v[252:255]\n"
    "v_bfi_b32 v40, s57, v21, v40\n"
    "v_mfma_f32_16x16x32_bf16 v[248:251], v[188:191], v[168:171], v[248:251]\n"
    "s_sub_u32 s43, 514, s47\n"
    "s_lshl_b32 s43, s43, 22\n"
    "v_cvt_pkrtz_f16_f32 v12, s43, s43\n"
    "v_mfma_f32_16x16x32_bf16 v[248:251], v[156:159], v[136:139], v[248:251]\n"
    "v_mfma_f32_16x16x32_bf16 v[244:247], v[156:159], v[132:135], v[244:247]\n"
    "v_lshl_or_b32 v21, v12, 17, s47\n"
    "v_mfma_f32_16x16x32_bf16 v[244:247], v[188:191], v[164:167], v[244:247]\n"
    "v_alignbit_b32 v21, v41, v21, 1\n"
    "v_mfma_f32_16x16x32_bf16 v[240:243], v[188:191], v[160:163], v[240:243]\n"
    "v_mfma_f32_16x16x32_bf16 v[240:243], v[156:159], v[128:131], v[240:243]\n"
    "s_waitcnt vmcnt(6)\n"
    "s_barrier\n"
    "ds_read_b128 v[128:131], v56 offset:49152\n"
    "ds_read_b128 v[132:135], v56 offset:51200\n"
    "ds_read_b128 v[136:139], v56 offset:53248\n"
    "ds_read_b128 v[140:143], v56 offset:55296\n"
    "ds_read_b64_tr_b16 v[144:145], v76 offset:49152\n"
    "ds_read_b64_tr_b16 v[146:147], v76 offset:50176\n"
    "ds_read_b64_tr_b16 v[148:149], v77 offset:49152\n"
    "ds_read_b64_tr_b16 v[150:151], v77 offset:50176\n"
    "ds_read_b64_tr_b16 v[152:153], v78 offset:49152\n"
    "ds_read_b64_tr_b16 v[154:155], v78 offset:50176\n"
    "ds_read_b64_tr_b16 v[156:157], v79 offset:49152\n"
    "ds_read_b64_tr_b16 v[158:159], v79 offset:50176\n"
    "ds_read_b128 v[160:163], v57 offset:49152\n"
    "ds_read_b128 v[164:167], v57 offset:51200\n"
    "ds_read_b128 v[168:171], v57 offset:53248\n"
    "ds_read_b128 v[172:175], v57 offset:55296\n"
    "ds_read_b64_tr_b16 v[176:177], v76 offset:57344\n"
    "ds_read_b64_tr_b16 v[178:179], v76 offset:58368\n"
    "ds_read_b64_tr_b16 v[180:181], v77 offset:57344\n"
    "ds_read_b64_tr_b16 v[182:183], v77 offset:58368\n"
    "ds_read_b64_tr_b16 v[184:185], v78 offset:57344\n"
    "ds_read_b64_tr_b16 v[186:187], v78 offset:58368\n"
    "ds_read_b64_tr_b16 v[188:189], v79 offset:57344\n"
    "ds_read_b64_tr_b16 v[190:191], v79 offset:58368\n"
    "s_waitcnt vmcnt(0) lgkmcnt(0)\n"
    "s_cmp_eq_u32 s22, 3\n"
    "s_cbranch_scc1 .Lxd_on5\n"
    ".Lxd_on5r:\n"
    "s_barrier\n"
    "s_setprio 3\n"
    "v_mfma_f32_16x16x32_bf16 v[192:195], v[144:147], v[128:131], v[192:195]\n"
    "v_add_u32 v42, 98304, v52\n"
    "v_mfma_f32_16x16x32_bf16 v[192:195], v[176:179], v[160:163], v[192:195]\n"
    "s_mov_b32 s64, 0xbdd2d3e8\n"
    "v_mfma_f32_16x16x32_bf16 v[196:199], v[176:179], v[164:167], v[196:199]\n"
    "s_mov_b32 s65, 0xc0135761\n"
    "v_mfma_f32_16x16x32_bf16 v[196:199], v[144:147], v[132:135], v[196:199]\n"
    "s_cmp_lt_u32 s22, 2\n"
    "v_mfma_f32_16x16x32_bf16 v[200:203], v[144:147], v[136:139], v[200:203]\n"
    "s_cselect_b32 s43, 16, 0\n"
    "v_mfma_f32_16x16x32_bf16 v[200:203], v[176:179], v[168:171], v[200:203]\n"
    "s_and_b32 s41, s20, 1\n"
    "v_mfma_f32_16x16x32_bf16 v[204:207], v[176:179], v[172:175], v[204:207]\n"
    "v_mfma_f32_16x16x32_bf16 v[204:207], v[144:147], v[140:143], v[204:207]\n"
    "s_lshl1_add_u32 s40, s21, s41\n"
    "v_mfma_f32_16x16x32_bf16 v[220:223], v[148:151], v[140:143], v[220:223]\n"
    "s_lshl_b32 s41, s40, 8\n"
    "v_mfma_f32_16x16x32_bf16 v[220:223], v[180:183], v[172:175], v[220:223]\n"
    "s_add_u32 s41, s41, 149504\n"
    "v_mfma_f32_16x16x32_bf16 v[216:219], v[180:183], v[168:171], v[216:219]\n"
    "v_lshl_add_u32 v11, v1, 2, s41\n"
    "v_mfma_f32_16x16x32_bf16 v[216:219], v[148:151], v[136:139], v[216:219]\n"
    "s_lshl_b32 s40, s40, 6\n"
    "v_mfma_f32_16x16x32_bf16 v[212:215], v[148:151], v[132:135], v[212:215]\n"
    "v_mfma_f32_16x16x32_bf16 v[212:215], v[180:183], v[164:167], v[212:215]\n"
    "s_add_u32 s40, s40, s24\n"
    "v_mfma_f32_16x16x32_bf16 v[208:211], v[180:183], v[160:163], v[208:211]\n"
    "s_lshl_b32 s41, s23, 12\n"
    "v_mfma_f32_16x16x32_bf16 v[208:211], v[148:151], v[128:131], v[208:211]\n"
    "s_add_u32 s40, s40, s41\n"
    "v_mfma_f32_16x16x32_bf16 v[224:227], v[152:155], v[128:131], v[224:227]\n"
    "s_lshl3_add_u32 s40, s40, 65536\n"
    "v_mfma_f32_16x16x32_bf16 v[224:227], v[184:187], v[160:163], v[224:227]\n"
    "s_add_u32 s34, s40, s10\n"
    "v_mfma_f32_16x16x32_bf16 v[228:231], v[184:187], v[164:167], v[228:231]\n"
    "v_mfma_f32_16x16x32_bf16 v[228:231], v[152:155], v[132:135], v[228:231]\n"
    "s_addc_u32 s35, s11, 0\n"
    "v_mfma_f32_16x16x32_bf16 v[232:235], v[152:155], v[136:139], v[232:235]\n"
    "v_mov_b32 v13, s58\n"
    "v_mfma_f32_16x16x32_bf16 v[232:235], v[184:187], v[168:171], v[232:235]\n"
    "s_mov_b32 s36, 0\n"
    "v_mfma_f32_16x16x32_bf16 v[236:239], v[184:187], v[172:175], v[236:239]\n"
    "s_sub_u32 s40, 1, s22\n"
    "v_mfma_f32_16x16x32_bf16 v[236:239], v[152:155], v[140:143], v[236:239]\n"
    "s_and_b32 s40, s40, 3\n"
    "v_mfma_f32_16x16x32_bf16 v[252:255], v[156:159], v[140:143], v[252:255]\n"
    "v_mfma_f32_16x16x32_bf16 v[252:255], v[188:191], v[172:175], v[252:255]\n"
    "s_mul_i32 s40, s40, 4480\n"
    "v_mfma_f32_16x16x32_bf16 v[248:251], v[188:191], v[168:171], v[248:251]\n"
    "v_add_u32 v7, s40, v3\n"
    "v_mfma_f32_16x16x32_bf16 v[248:251], v[156:159], v[136:139], v[248:251]\n"
    "v_add_u32 v8, s40, v4\n"
    "v_mfma_f32_16x16x32_bf16 v[244:247], v[156:159], v[132:135], v[244:247]\n"
    "v_add_u32 v9, s40, v5\n"
    "v_mfma_f32_16x16x32_bf16 v[244:247], v[188:191], v[164:167], v[244:247]\n"
    "v_add_u32 v10, s40, v6\n"
    "v_mfma_f32_16x16x32_bf16 v[240:243], v[188:191], v[160:163], v[240:243]\n"
    "v_mfma_f32_16x16x32_bf16 v[240:243], v[156:159], v[128:131], v[240:243]\n"
    "s_cmp_lg_u32 s21, 0\n"
    "s_cbranch_scc1 .Lxd_g1\n"
    "s_barrier\n"
    ".Lxd_g1:\n"
    "s_setprio 2\n"
    "s_mov_b64 exec, s[48:49]\n"
    "ds_write_b128 v15, v[28:31] offset:0\n"
    "ds_write_b128 v15, v[32:35] offset:160\n"
    "ds_write_b128 v15, v[36:39] offset:320\n"
    "ds_write_b32 v16, v40 offset:3840\n"
    "ds_write_b32 v16, v12 offset:4160\n"
    "ds_write_b32 v25, v21 offset:532\n"
    "s_mov_b64 exec, -1\n"
    "s_load_dword s26, s[16:17], 0\n"
    "v_add_u32 v25, s43, v25\n"
    "s_mov_b64 s[48:49], 0\n"
    "ds_read_b128 v[92:95], v42 offset:0\n"
    "ds_read_b128 v[96:99], v42 offset:1040\n"
    "ds_read_b128 v[100:103], v42 offset:2080\n"
    "ds_read_b128 v[104:107], v42 offset:3120\n"
    "ds_read_b128 v[108:111], v42 offset:4160\n"
    "ds_read_b128 v[112:115], v42 offset:5200\n"
    "ds_read_b128 v[116:119], v42 offset:6240\n"
    "ds_read_b128 v[120:123], v42 offset:7280\n"
    "ds_read_b128 v[124:127], v42 offset:8320\n"
    "v_pk_mul_f32 v[76:77], v[192:193], v[192:193]\n"
    "v_pk_mul_f32 v[78:79], v[196:197], v[196:197]\n"
    "v_pk_mul_f32 v[80:81], v[200:201], v[200:201]\n"
    "v_pk_mul_f32 v[82:83], v[204:205], v[204:205]\n"
    "v_pk_fma_f32 v[76:77], v[76:77], s[64:65], s[64:65] op_sel:[0,0,1] op_sel_hi:[1,0,1]\n"
    "v_pk_fma_f32 v[78:79], v[78:79], s[64:65], s[64:65] op_sel:[0,0,1] op_sel_hi:[1,0,1]\n"
    "v_pk_fma_f32 v[80:81], v[80:81], s[64:65], s[64:65] op_sel:[0,0,1] op_sel_hi:[1,0,1]\n"
    "v_pk_fma_f32 v[82:83], v[82:83], s[64:65], s[64:65] op_sel:[0,0,1] op_sel_hi:[1,0,1]\n"
    "v_pk_mul_f32 v[76:77], v[76:77], v[192:193]\n"
    "v_pk_mul_f32 v[78:79], v[78:79], v[196:197]\n"
    "v_pk_mul_f32 v[80:81], v[80:81], v[200:201]\n"
    "v_pk_mul_f32 v[82:83], v[82:83], v[204:205]\n"
    "v_exp_f32 v76, v76\n"
    "v_exp_f32 v77, v77\n"
    "ds_read_b128 v[128:131], v42 offset:9360\n"
    "v_exp_f32 v78, v78\n"
    "v_exp_f32 v79, v79\n"
    "v_exp_f32 v80, v80\n"
    "ds_read_b128 v[132:135], v42 offset:10400\n"
    "v_exp_f32 v81, v81\n"
    "v_exp_f32 v82, v82\n"
    "v_exp_f32 v83, v83\n"
    "ds_read_b128 v[136:139], v42 offset:11440\n"
    "v_pk_mul_f32 v[192:193], v[192:193], v[60:61]\n"
    "v_pk_mul_f32 v[196:197], v[196:197], v[60:61]\n"
    "v_pk_mul_f32 v[200:201], v[200:201], v[60:61]\n"
    "v_pk_mul_f32 v[204:205], v[204:205], v[60:61]\n"
    "v_pk_add_f32 v[76:77], v[76:77], 1.0 op_sel_hi:[1,0]\n"
    "v_pk_add_f32 v[78:79], v[78:79], 1.0 op_sel_hi:[1,0]\n"
    "v_pk_add_f32 v[80:81], v[80:81], 1.0 op_sel_hi:[1,0]\n"
    "v_pk_add_f32 v[82:83], v[82:83], 1.0 op_sel_hi:[1,0]\n"
    "v_rcp_f32 v76, v76\n"
    "v_rcp_f32 v77, v77\n"
    "ds_read_b128 v[140:143], v42 offset:12480\n"
    "v_rcp_f32 v78, v78\n"
    "v_rcp_f32 v79, v79\n"
    "v_rcp_f32 v80, v80\n"
    "v_rcp_f32 v81, v81\n"
    "ds_read_b128 v[144:147], v42 offset:13520\n"
    "v_rcp_f32 v82, v82\n"
    "v_rcp_f32 v83, v83\n"
    "v_pk_mul_f32 v[84:85], v[192:193], v[76:77]\n"
    "v_pk_mul_f32 v[86:87], v[196:197], v[78:79]\n"
    "v_pk_mul_f32 v[88:89], v[200:201], v[80:81]\n"
    "v_pk_mul_f32 v[90:91], v[204:205], v[82:83]\n"
    "s_cmp_eq_u32 s22, 3\n"
    "s_cbranch_scc1 .Lxd_on7\n"
    ".Lxd_on7r:\n"
    "s_cmp_eq_u32 s22, 0\n"
    "s_cbranch_scc1 .Lxd_on8\n"
    ".Lxd_on8r:\n"
    "v_pk_mul_f32 v[76:77], v[194:195], v[194:195]\n"
    "v_pk_mul_f32 v[78:79], v[198:199], v[198:199]\n"
    "v_pk_mul_f32 v[80:81], v[202:203], v[202:203]\n"
    "v_pk_mul_f32 v[82:83], v[206:207], v[206:207]\n"
    "v_pk_fma_f32 v[76:77], v[76:77], s[64:65], s[64:65] op_sel:[0,0,1] op_sel_hi:[1,0,1]\n"
    "v_pk_fma_f32 v[78:79], v[78:79], s[64:65], s[64:65] op_sel:[0,0,1] op_sel_hi:[1,0,1]\n"
    "v_pk_fma_f32 v[80:81], v[80:81], s[64:65], s[64:65] op_sel:[0,0,1] op_sel_hi:[1,0,1]\n"
    "v_pk_fma_f32 v[82:83], v[82:83], s[64:65], s[64:65] op_sel:[0,0,1] op_sel_hi:[1,0,1]\n"
    "v_pk_mul_f32 v[76:77], v[76:77], v[194:195]\n"
    "v_pk_mul_f32 v[78:79], v[78:79], v[198:199]\n"
    "v_pk_mul_f32 v[80:81], v[80:81], v[202:203]\n"
    "v_pk_mul_f32 v[82:83], v[82:83], v[206:207]\n"
    "v_exp_f32 v76, v76\n"
    "v_exp_f32 v77, v77\n"
    "ds_read_b128 v[148:151], v42 offset:14560\n"
    "v_exp_f32 v78, v78\n"
    "v_exp_f32 v79, v79\n"
    "v_exp_f32 v80, v80\n"
    "ds_read_b128 v[152:155], v42 offset:15600\n"
    "v_exp_f32 v81, v81\n"
    "v_exp_f32 v82, v82\n"
    "v_exp_f32 v83, v83\n"
    "ds_read_b128 v[156:159], v42 offset:16640\n"
    "v_pk_mul_f32 v[194:195], v[194:195], v[62:63]\n"
    "v_pk_mul_f32 v[198:199], v[198:199], v[62:63]\n"
    "v_pk_mul_f32 v[202:203], v[202:203], v[62:63]\n"
    "v_pk_mul_f32 v[206:207], v[206:207], v[62:63]\n"
    "v_pk_add_f32 v[76:77], v[76:77], 1.0 op_sel_hi:[1,0]\n"
    "v_pk_add_f32 v[78:79], v[78:79], 1.0 op_sel_hi:[1,0]\n"
    "v_pk_add_f32 v[80:81], v[80:81], 1.0 op_sel_hi:[1,0]\n"
    "v_pk_add_f32 v[82:83], v[82:83], 1.0 op_sel_hi:[1,0]\n"
    "v_rcp_f32 v76, v76\n"
    "v_rcp_f32 v77, v77\n"
    "ds_read_b128 v[160:163], v42 offset:17680\n"
    "v_rcp_f32 v78, v78\n"
    "v_rcp_f32 v79, v79\n"
    "v_rcp_f32 v80, v80\n"
    "v_rcp_f32 v81, v81\n"
    "ds_read_b128 v[164:167], v42 offset:18720\n"
    "v_rcp_f32 v82, v82\n"
    "v_rcp_f32 v83, v83\n"
    "v_pk_fma_f32 v[84:85], v[194:195], v[76:77], v[84:85]\n"
    "v_pk_fma_f32 v[86:87], v[198:199], v[78:79], v[86:87]\n"
    "v_pk_fma_f32 v[88:89], v[202:203], v[80:81], v[88:89]\n"
    "v_pk_fma_f32 v[90:91], v[206:207], v[82:83], v[90:91]\n"
    "ds_read_b32 v22, v13 offset:512\n"
    "v_pk_mul_f32 v[76:77], v[208:209], v[208:209]\n"
    "v_pk_mul_f32 v[78:79], v[212:213], v[212:213]\n"
    "v_pk_mul_f32 v[80:81], v[216:217], v[216:217]\n"
    "v_pk_mul_f32 v[82:83], v[220:221], v[220:221]\n"
    "v_pk_fma_f32 v[76:77], v[76:77], s[64:65], s[64:65] op_sel:[0,0,1] op_sel_hi:[1,0,1]\n"
    "v_pk_fma_f32 v[78:79], v[78:79], s[64:65], s[64:65] op_sel:[0,0,1] op_sel_hi:[1,0,1]\n"
    "v_pk_fma_f32 v[80:81], v[80:81], s[64:65], s[64:65] op_sel:[0,0,1] op_sel_hi:[1,0,1]\n"
    "v_pk_fma_f32 v[82:83], v[82:83], s[64:65], s[64:65] op_sel:[0,0,1] op_sel_hi:[1,0,1]\n"
    "v_pk_mul_f32 v[76:77], v[76:77], v[208:209]\n"
    "v_pk_mul_f32 v[78:79], v[78:79], v[212:213]\n"
    "v_pk_mul_f32 v[80:81], v[80:81], v[216:217]\n"
    "v_pk_mul_f32 v[82:83], v[82:83], v[220:221]\n"
    "v_exp_f32 v76, v76\n"
    "v_exp_f32 v77, v77\n"
    "ds_read_b128 v[168:171], v42 offset:19760\n"
    "v_exp_f32 v78, v78\n"
    "v_exp_f32 v79, v79\n"
    "v_exp_f32 v80, v80\n"
    "ds_read_b128 v[172:175], v42 offset:20800\n"
    "v_exp_f32 v81, v81\n"
    "v_exp_f32 v82, v82\n"
    "v_exp_f32 v83, v83\n"
    "ds_read_b128 v[176:179], v42 offset:21840\n"
    "v_pk_mul_f32 v[208:209], v[208:209], v[64:65]\n"
    "v_pk_mul_f32 v[212:213], v[212:213], v[64:65]\n"
    "v_pk_mul_f32 v[216:217], v[216:217], v[64:65]\n"
    "v_pk_mul_f32 v[220:221], v[220:221], v[64:65]\n"
    "v_pk_add_f32 v[76:77], v[76:77], 1.0 op_sel_hi:[1,0]\n"
    "v_pk_add_f32 v[78:79], v[78:79], 1.0 op_sel_hi:[1,0]\n"
    "v_pk_add_f32 v[80:81], v[80:81], 1.0 op_sel_hi:[1,0]\n"
    "v_pk_add_f32 v[82:83], v[82:83], 1.0 op_sel_hi:[1,0]\n"
    "v_rcp_f32 v76, v76\n"
    "v_rcp_f32 v77, v77\n"
    "ds_read_b128 v[180:183], v42 offset:22880\n"
    "v_rcp_f32 v78, v78\n"
    "v_rcp_f32 v79, v79\n"
    "v_rcp_f32 v80, v80\n"
    "v_rcp_f32 v81, v81\n"
    "ds_read_b128 v[184:187], v42 offset:23920\n"
    "v_rcp_f32 v82, v82\n"
    "v_rcp_f32 v83, v83\n"
    "v_pk_fma_f32 v[84:85], v[208:209], v[76:77], v[84:85]\n"
    "v_pk_fma_f32 v[86:87], v[212:213], v[78:79], v[86:87]\n"
    "v_pk_fma_f32 v[88:89], v[216:217], v[80:81], v[88:89]\n"
    "v_pk_fma_f32 v[90:91], v[220:221], v[82:83], v[90:91]\n"
    "s_waitcnt lgkmcnt(5)\n"
    "v_readfirstlane_b32 s45, v22\n"
    "s_cmp_lt_i32 s45, 1\n"
    "s_cbranch_scc1 .Lxd_ck9\n"
    ".Lxd_ck9r:\n"
    "ds_read_b64_tr_b16 v[44:45], v9 offset:0\n"
    "ds_read_b64_tr_b16 v[46:47], v10 offset:0\n"
    "ds_read_b64_tr_b16 v[48:49], v7 offset:0\n"
    "ds_read_b64_tr_b16 v[50:51], v8 offset:640\n"
    "ds_read_b64_tr_b16 v[52:53], v8 offset:1280\n"
    "ds_read_b64_tr_b16 v[54:55], v8 offset:1920\n"
    "ds_read_b64_tr_b16 v[56:57], v8 offset:2560\n"
    "ds_read_b64_tr_b16 v[58:59], v8 offset:3200\n"
    "s_setprio 1\n"
    "v_pk_mul_f32 v[76:77], v[210:211], v[210:211]\n"
    "v_pk_mul_f32 v[78:79], v[214:215], v[214:215]\n"
    "v_pk_mul_f32 v[80:81], v[218:219], v[218:219]\n"
    "v_pk_mul_f32 v[82:83], v[222:223], v[222:223]\n"
    "v_pk_fma_f32 v[76:77], v[76:77], s[64:65], s[64:65] op_sel:[0,0,1] op_sel_hi:[1,0,1]\n"
    "v_pk_fma_f32 v[78:79], v[78:79], s[64:65], s[64:65] op_sel:[0,0,1] op_sel_hi:[1,0,1]\n"
    "v_pk_fma_f32 v[80:81], v[80:81], s[64:65], s[64:65] op_sel:[0,0,1] op_sel_hi:[1,0,1]\n"
    "v_pk_fma_f32 v[82:83], v[82:83], s[64:65], s[64:65] op_sel:[0,0,1] op_sel_hi:[1,0,1]\n"
    "v_pk_mul_f32 v[76:77], v[76:77], v[210:211]\n"
    "v_pk_mul_f32 v[78:79], v[78:79], v[214:215]\n"
    "v_pk_mul_f32 v[80:81], v[80:81], v[218:219]\n"
    "v_pk_mul_f32 v[82:83], v[82:83], v[222:223]\n"
    "v_exp_f32 v76, v76\n"
    "v_exp_f32 v77, v77\n"
    "ds_read_b128 v[188:191], v42 offset:24960\n"
    "v_exp_f32 v78, v78\n"
    "v_exp_f32 v79, v79\n"
    "v_exp_f32 v80, v80\n"
    "ds_read_b128 v[192:195], v42 offset:26000\n"
    "v_exp_f32 v81, v81\n"
    "v_exp_f32 v82, v82\n"
    "v_exp_f32 v83, v83\n"
    "ds_read_b128 v[196:199], v42 offset:27040\n"
    "v_pk_mul_f32 v[210:211], v[210:211], v[66:67]\n"
    "v_pk_mul_f32 v[214:215], v[214:215], v[66:67]\n"
    "v_pk_mul_f32 v[218:219], v[218:219], v[66:67]\n"
    "v_pk_mul_f32 v[222:223], v[222:223], v[66:67]\n"
    "v_pk_add_f32 v[76:77], v[76:77], 1.0 op_sel_hi:[1,0]\n"
    "v_pk_add_f32 v[78:79], v[78:79], 1.0 op_sel_hi:[1,0]\n"
    "v_pk_add_f32 v[80:81], v[80:81], 1.0 op_sel_hi:[1,0]\n"
    "v_pk_add_f32 v[82:83], v[82:83], 1.0 op_sel_hi:[1,0]\n"
    "v_rcp_f32 v76, v76\n"
    "v_rcp_f32 v77, v77\n"
    "ds_read_b128 v[200:203], v42 offset:28080\n"
    "v_rcp_f32 v78, v78\n"
    "v_rcp_f32 v79, v79\n"
    "v_rcp_f32 v80, v80\n"
    "v_rcp_f32 v81, v81\n"
    "ds_read_b128 v[204:207], v42 offset:29120\n"
    "v_rcp_f32 v82, v82\n"
    "v_rcp_f32 v83, v83\n"
    "v_pk_fma_f32 v[84:85], v[210:211], v[76:77], v[84:85]\n"
    "v_pk_fma_f32 v[86:87], v[214:215], v[78:79], v[86:87]\n"
    "v_pk_fma_f32 v[88:89], v[218:219], v[80:81], v[88:89]\n"
    "v_pk_fma_f32 v[90:91], v[222:223], v[82:83], v[90:91]\n"
    "v_pk_mul_f32 v[76:77], v[224:225], v[224:225]\n"
    "v_pk_mul_f32 v[78:79], v[228:229], v[228:229]\n"
    "v_pk_mul_f32 v[80:81], v[232:233], v[232:233]\n"
    "v_pk_mul_f32 v[82:83], v[236:237], v[236:237]\n"
    "v_pk_fma_f32 v[76:77], v[76:77], s[64:65], s[64:65] op_sel:[0,0,1] op_sel_hi:[1,0,1]\n"
    "v_pk_fma_f32 v[78:79], v[78:79], s[64:65], s[64:65] op_sel:[0,0,1] op_sel_hi:[1,0,1]\n"
    "v_pk_fma_f32 v[80:81], v[80:81], s[64:65], s[64:65] op_sel:[0,0,1] op_sel_hi:[1,0,1]\n"
    "v_pk_fma_f32 v[82:83], v[82:83], s[64:65], s[64:65] op_sel:[0,0,1] op_sel_hi:[1,0,1]\n"
    "v_pk_mul_f32 v[76:77], v[76:77], v[224:225]\n"
    "v_pk_mul_f32 v[78:79], v[78:79], v[228:229]\n"
    "v_pk_mul_f32 v[80:81], v[80:81], v[232:233]\n"
    "v_pk_mul_f32 v[82:83], v[82:83], v[236:237]\n"
    "v_exp_f32 v76, v76\n"
    "v_exp_f32 v77, v77\n"
    "ds_read_b128 v[208:211], v42 offset:30160\n"
    "v_exp_f32 v78, v78\n"
    "v_exp_f32 v79, v79\n"
    "v_exp_f32 v80, v80\n"
    "ds_read_b128 v[212:215], v42 offset:31200\n"
    "v_exp_f32 v81, v81\n"
    "v_exp_f32 v82, v82\n"
    "v_exp_f32 v83, v83\n"
    "ds_read_b128 v[216:219], v42 offset:32240\n"
    "v_pk_mul_f32 v[224:225], v[224:225], v[68:69]\n"
    "v_pk_mul_f32 v[228:229], v[228:229], v[68:69]\n"
    "v_pk_mul_f32 v[232:233], v[232:233], v[68:69]\n"
    "v_pk_mul_f32 v[236:237], v[236:237], v[68:69]\n"
    "v_pk_add_f32 v[76:77], v[76:77], 1.0 op_sel_hi:[1,0]\n"
    "v_pk_add_f32 v[78:79], v[78:79], 1.0 op_sel_hi:[1,0]\n"
    "v_pk_add_f32 v[80:81], v[80:81], 1.0 op_sel_hi:[1,0]\n"
    "v_pk_add_f32 v[82:83], v[82:83], 1.0 op_sel_hi:[1,0]\n"
    "v_rcp_f32 v76, v76\n"
    "v_rcp_f32 v77, v77\n"
    "ds_read_b128 v[220:223], v42 offset:33280\n"
    "v_rcp_f32 v78, v78\n"
    "v_rcp_f32 v79, v79\n"
    "v_rcp_f32 v80, v80\n"
    "v_rcp_f32 v81, v81\n"
    "v_rcp_f32 v82, v82\n"
    "v_rcp_f32 v83, v83\n"
    "v_pk_fma_f32 v[84:85], v[224:225], v[76:77], v[84:85]\n"
    "v_pk_fma_f32 v[86:87], v[228:229], v[78:79], v[86:87]\n"
    "v_pk_fma_f32 v[88:89], v[232:233], v[80:81], v[88:89]\n"
    "v_pk_fma_f32 v[90:91], v[236:237], v[82:83], v[90:91]\n"
    "s_cmp_eq_u32 s22, 3\n"
    "s_cbranch_scc1 .Lxd_on10\n"
    ".Lxd_on10r:\n"
    "s_cmp_eq_u32 s22, 0\n"
    "s_cbranch_scc1 .Lxd_on12\n"
    ".Lxd_on12r:\n"
    "s_cmp_eq_u32 s22, 1\n"
    "s_cbranch_scc1 .Lxd_on13\n"
    ".Lxd_on13r:\n"
    "v_pk_mul_f32 v[76:77], v[226:227], v[226:227]\n"
    "v_pk_mul_f32 v[78:79], v[230:231], v[230:231]\n"
    "v_pk_mul_f32 v[80:81], v[234:235], v[234:235]\n"
    "v_pk_mul_f32 v[82:83], v[238:239], v[238:239]\n"
    "v_pk_fma_f32 v[76:77], v[76:77], s[64:65], s[64:65] op_sel:[0,0,1] op_sel_hi:[1,0,1]\n"
    "v_pk_fma_f32 v[78:79], v[78:79], s[64:65], s[64:65] op_sel:[0,0,1] op_sel_hi:[1,0,1]\n"
    "v_pk_fma_f32 v[80:81], v[80:81], s[64:65], s[64:65] op_sel:[0,0,1] op_sel_hi:[1,0,1]\n"
    "v_pk_fma_f32 v[82:83], v[82:83], s[64:65], s[64:65] op_sel:[0,0,1] op_sel_hi:[1,0,1]\n"
    "v_pk_mul_f32 v[76:77], v[76:77], v[226:227]\n"
    "v_pk_mul_f32 v[78:79], v[78:79], v[230:231]\n"
    "v_pk_mul_f32 v[80:81], v[80:81], v[234:235]\n"
    "v_pk_mul_f32 v[82:83], v[82:83], v[238:239]\n"
    "v_exp_f32 v76, v76\n"
    "v_exp_f32 v77, v77\n"
    "v_exp_f32 v78, v78\n"
    "v_exp_f32 v79, v79\n"
    "v_exp_f32 v80, v80\n"
    "v_exp_f32 v81, v81\n"
    "v_exp_f32 v82, v82\n"
    "v_exp_f32 v83, v83\n"
    "v_pk_mul_f32 v[226:227], v[226:227], v[70:71]\n"
    "v_pk_mul_f32 v[230:231], v[230:231], v[70:71]\n"
    "v_pk_mul_f32 v[234:235], v[234:235], v[70:71]\n"
    "v_pk_mul_f32 v[238:239], v[238:239], v[70:71]\n"
    "v_pk_add_f32 v[76:77], v[76:77], 1.0 op_sel_hi:[1,0]\n"
    "v_pk_add_f32 v[78:79], v[78:79], 1.0 op_sel_hi:[1,0]\n"
    "v_pk_add_f32 v[80:81], v[80:81], 1.0 op_sel_hi:[1,0]\n"
    "v_pk_add_f32 v[82:83], v[82:83], 1.0 op_sel_hi:[1,0]\n"
    "v_rcp_f32 v76, v76\n"
    "v_rcp_f32 v77, v77\n"
    "v_rcp_f32 v78, v78\n"
    "v_rcp_f32 v79, v79\n"
    "v_rcp_f32 v80, v80\n"
    "v_rcp_f32 v81, v81\n"
    "v_rcp_f32 v82, v82\n"
    "v_rcp_f32 v83, v83\n"
    "v_pk_fma_f32 v[84:85], v[226:227], v[76:77], v[84:85]\n"
    "v_pk_fma_f32 v[86:87], v[230:231], v[78:79], v[86:87]\n"
    "v_pk_fma_f32 v[88:89], v[234:235], v[80:81], v[88:89]\n"
    "v_pk_fma_f32 v[90:91], v[238:239], v[82:83], v[90:91]\n"
    "s_setprio 0\n"
    "v_pk_mul_f32 v[76:77], v[240:241], v[240:241]\n"
    "v_pk_mul_f32 v[78:79], v[244:245], v[244:245]\n"
    "v_pk_mul_f32 v[80:81], v[248:249], v[248:249]\n"
    "v_pk_mul_f32 v[82:83], v[252:253], v[252:253]\n"
    "v_pk_fma_f32 v[76:77], v[76:77], s[64:65], s[64:65] op_sel:[0,0,1] op_sel_hi:[1,0,1]\n"
    "v_pk_fma_f32 v[78:79], v[78:79], s[64:65], s[64:65] op_sel:[0,0,1] op_sel_hi:[1,0,1]\n"
    "v_pk_fma_f32 v[80:81], v[80:81], s[64:65], s[64:65] op_sel:[0,0,1] op_sel_hi:[1,0,1]\n"
    "v_pk_fma_f32 v[82:83], v[82:83], s[64:65], s[64:65] op_sel:[0,0,1] op_sel_hi:[1,0,1]\n"
    "v_pk_mul_f32 v[76:77], v[76:77], v[240:241]\n"
    "v_pk_mul_f32 v[78:79], v[78:79], v[244:245]\n"
    "v_pk_mul_f32 v[80:81], v[80:81], v[248:249]\n"
    "v_pk_mul_f32 v[82:83], v[82:83], v[252:253]\n"
    "v_exp_f32 v76, v76\n"
    "v_exp_f32 v77, v77\n"
    "ds_read_b128 v[224:227], v42 offset:34320\n"
    "v_exp_f32 v78, v78\n"
    "v_exp_f32 v79, v79\n"
    "v_exp_f32 v80, v80\n"
    "ds_read_b128 v[228:231], v42 offset:35360\n"
    "v_exp_f32 v81, v81\n"
    "v_exp_f32 v82, v82\n"
    "v_exp_f32 v83, v83\n"
    "ds_read_b128 v[232:235], v42 offset:36400\n"
    "v_pk_mul_f32 v[240:241], v[240:241], v[72:73]\n"
    "v_pk_mul_f32 v[244:245], v[244:245], v[72:73]\n"
    "v_pk_mul_f32 v[248:249], v[248:249], v[72:73]\n"
    "v_pk_mul_f32 v[252:253], v[252:253], v[72:73]\n"
    "v_pk_add_f32 v[76:77], v[76:77], 1.0 op_sel_hi:[1,0]\n"
    "v_pk_add_f32 v[78:79], v[78:79], 1.0 op_sel_hi:[1,0]\n"
    "v_pk_add_f32 v[80:81], v[80:81], 1.0 op_sel_hi:[1,0]\n"
    "v_pk_add_f32 v[82:83], v[82:83], 1.0 op_sel_hi:[1,0]\n"
    "v_rcp_f32 v76, v76\n"
    "v_rcp_f32 v77, v77\n"
    "ds_read_b128 v[236:239], v42 offset:37440\n"
    "v_rcp_f32 v78, v78\n"
    "v_rcp_f32 v79, v79\n"
    "v_rcp_f32 v80, v80\n"
    "v_rcp_f32 v81, v81\n"
    "v_rcp_f32 v82, v82\n"
    "v_rcp_f32 v83, v83\n"
    "v_pk_fma_f32 v[84:85], v[240:241], v[76:77], v[84:85]\n"
    "v_pk_fma_f32 v[86:87], v[244:245], v[78:79], v[86:87]\n"
    "v_pk_fma_f32 v[88:89], v[248:249], v[80:81], v[88:89]\n"
    "v_pk_fma_f32 v[90:91], v[252:253], v[82:83], v[90:91]\n"
    "v_pk_mul_f32 v[76:77], v[242:243], v[242:243]\n"
    "v_pk_mul_f32 v[78:79], v[246:247], v[246:247]\n"
    "v_pk_mul_f32 v[80:81], v[250:251], v[250:251]\n"
    "v_pk_mul_f32 v[82:83], v[254:255], v[254:255]\n"
    "v_pk_fma_f32 v[76:77], v[76:77], s[64:65], s[64:65] op_sel:[0,0,1] op_sel_hi:[1,0,1]\n"
    "v_pk_fma_f32 v[78:79], v[78:79], s[64:65], s[64:65] op_sel:[0,0,1] op_sel_hi:[1,0,1]\n"
    "v_pk_fma_f32 v[80:81], v[80:81], s[64:65], s[64:65] op_sel:[0,0,1] op_sel_hi:[1,0,1]\n"
    "v_pk_fma_f32 v[82:83], v[82:83], s[64:65], s[64:65] op_sel:[0,0,1] op_sel_hi:[1,0,1]\n"
    "v_pk_mul_f32 v[76:77], v[76:77], v[242:243]\n"
    "v_pk_mul_f32 v[78:79], v[78:79], v[246:247]\n"
    "v_pk_mul_f32 v[80:81], v[80:81], v[250:251]\n"
    "v_pk_mul_f32 v[82:83], v[82:83], v[254:255]\n"
    "v_exp_f32 v76, v76\n"
    "v_exp_f32 v77, v77\n"
    "v_exp_f32 v78, v78\n"
    "v_exp_f32 v79, v79\n"
    "v_exp_f32 v80, v80\n"
    "v_exp_f32 v81, v81\n"
    "v_exp_f32 v82, v82\n"
    "v_exp_f32 v83, v83\n"
    "v_pk_mul_f32 v[242:243], v[242:243], v[74:75]\n"
    "v_pk_mul_f32 v[246:247], v[246:247], v[74:75]\n"
    "v_pk_mul_f32 v[250:251], v[250:251], v[74:75]\n"
    "v_pk_mul_f32 v[254:255], v[254:255], v[74:75]\n"
    "v_pk_add_f32 v[76:77], v[76:77], 1.0 op_sel_hi:[1,0]\n"
    "v_pk_add_f32 v[78:79], v[78:79], 1.0 op_sel_hi:[1,0]\n"
    "v_pk_add_f32 v[80:81], v[80:81], 1.0 op_sel_hi:[1,0]\n"
    "v_pk_add_f32 v[82:83], v[82:83], 1.0 op_sel_hi:[1,0]\n"
    "v_rcp_f32 v76, v76\n"
    "v_rcp_f32 v77, v77\n"
    "v_rcp_f32 v78, v78\n"
    "v_rcp_f32 v79, v79\n"
    "v_rcp_f32 v80, v80\n"
    "v_rcp_f32 v81, v81\n"
    "v_rcp_f32 v82, v82\n"
    "v_rcp_f32 v83, v83\n"
    "v_pk_fma_f32 v[84:85], v[242:243], v[76:77], v[84:85]\n"
    "v_pk_fma_f32 v[86:87], v[246:247], v[78:79], v[86:87]\n"
    "v_pk_fma_f32 v[88:89], v[250:251], v[80:81], v[88:89]\n"
    "v_pk_fma_f32 v[90:91], v[254:255], v[82:83], v[90:91]\n"
    "ds_read_b128 v[240:243], v42 offset:38480\n"
    "ds_read_b128 v[244:247], v42 offset:39520\n"
    "ds_read_b128 v[248:251], v42 offset:40560\n"
    "ds_read_b128 v[252:255], v42 offset:41600\n"
    "s_cmp_eq_u32 s21, 0\n"
    "s_cbranch_scc1 .Lxd_gb0\n"
    "s_barrier\n"
    "s_setprio 3\n"
    ".Lxd_gb0:\n"
    "v_add_f32 v84, v84, v85\n"
    "v_add_f32 v86, v86, v87\n"
    "v_add_f32 v88, v88, v89\n"
    "v_add_f32 v90, v90, v91\n"
    "s_waitcnt lgkmcnt(15)\n"
    "v_pk_mul_f16 v60, v44, s45 op_sel:[0,1]\n"
    "v_pk_mul_f16 v61, v45, s45 op_sel:[0,1]\n"
    "v_permlane32_swap_b32 v84, v88\n"
    "v_permlane32_swap_b32 v86, v90\n"
    "v_add_f32 v84, v84, v88\n"
    "v_add_f32 v86, v86, v90\n"
    "v_pk_mul_f16 v62, v46, s45 op_sel:[0,1]\n"
    "v_pk_mul_f16 v63, v47, s45 op_sel:[0,1]\n"
    "v_permlane16_swap_b32 v84, v86\n"
    "v_add_f32 v84, v84, v86\n"
    "v_and_b32 v84, -2, v84\n"
    "ds_wrxchg_rtn_b32 v85, v11, v84\n"
    "v_pk_mul_f16 v64, v44, v46\n"
    "v_pk_mul_f16 v65, v45, v47\n"
    "v_pk_mul_f16 v66, v48, v48 op_sel_hi:[0,1]\n"
    "v_pk_mul_f16 v67, v48, v49 op_sel_hi:[0,1]\n"
    "v_lshlrev_b32 v86, 3, v1\n"
    "v_mov_b64 v[88:89], 0\n"
    "v_mov_b64 v[90:91], 0\n"
    "s_waitcnt lgkmcnt(0)\n"
    "v_cmp_ne_u32 vcc, -1, v85\n"
    "v_add_f32 v84, v84, v85\n"
    "v_add_u32 v84, 0x80, v84\n"
    "v_and_b32 v84, 0xffffff00, v84\n"
    "v_or_b32 v26, v84, v26\n"
    "s_nop 0\n"
    "s_cbranch_vccz .Lxd_nost\n"
    "s_mov_b64 exec, vcc\n"
    "global_store_dwordx2 v86, v[26:27], s[34:35] sc1\n"
    "s_mov_b64 exec, -1\n"
    ".Lxd_nost:\n"
    "s_cmp_lg_u32 s21, 0\n"
    "s_cbranch_scc1 .Lxd_gb1\n"
    "s_barrier\n"
    ".Lxd_gb1:\n"
    "s_cmp_eq_u32 s22, 2\n"
    "s_cbranch_scc1 .Lxd_on14\n"
    ".Lxd_on14r:\n"
    ".Lxd_enter:\n"
    "s_cmp_eq_u32 s21, 0\n"
    "s_cselect_b64 vcc, 0, -1\n"
    "s_cbranch_scc1 .Lxd_pr\n"
    "s_setprio 1\n"
    ".Lxd_pr:\n"
    "s_cmp_eq_u32 s22, 2\n"
    "s_cbranch_scc1 .Lxd_pl3\n"
    "s_and_b32 s40, s48, 4\n"
    "s_or_b32 s40, s40, s22\n"
    "s_cmp_eq_u32 s40, 0\n"
    "s_cbranch_scc1 .Lxd_pl1\n"
    "s_cmp_eq_u32 s40, 4\n"
    "s_cbranch_scc1 .Lxd_ph1\n"
    "s_cmp_eq_u32 s40, 3\n"
    "s_cbranch_scc1 .Lxd_pl2\n"
    "s_cmp_eq_u32 s40, 7\n"
    "s_cbranch_scc1 .Lxd_ph2\n"
    ".Lxd_ph0:\n"
    "s_cbranch_vccnz .Lxd_pr16\n"
    "s_setprio 2\n"
    ".Lxd_pr16:\n"
    "v_smfmac_f32_16x16x64_f16 v[88:91], v[112:115], v[60:67], v2\n"
    "v_fmac_f32 v11, v76, v80\n"
    "v_fmac_f32 v11, v77, v81\n"
    "v_fmac_f32 v11, v78, v82\n"
    "v_fmac_f32 v11, v79, v83\n"
    "v_mfma_f32_16x16x32_f16 v[80:83], v[92:95], v[60:63], 0\n"
    "v_pk_mul_f16 v68, v58, v58 op_sel_hi:[0,1]\n"
    "v_pk_mul_f16 v69, v58, v49 op_sel_hi:[0,1]\n"
    "v_mfma_f32_16x16x32_f16 v[80:83], v[104:107], v[64:67], v[80:83]\n"
    "v_pk_mul_f16 v70, v48, v58 op_sel_hi:[0,1]\n"
    "v_pk_mul_f16 v71, v48, v59 op_sel_hi:[0,1]\n"
    "v_mfma_f32_16x16x32_f16 v[76:79], v[100:103], v[64:67], 0\n"
    "v_fmac_f32 v11, v84, v85\n"
    "v_fma_f32 v10, v86, v87, v11\n"
    "v_mfma_f32_16x16x32_f16 v[84:87], v[108:111], v[64:67], 0\n"
    "v_pk_mul_f16 v72, v50, v58 op_sel_hi:[0,1]\n"
    "v_pk_mul_f16 v73, v50, v59 op_sel_hi:[0,1]\n"
    "v_mfma_f32_16x16x32_f16 v[84:87], v[96:99], v[60:63], v[84:87]\n"
    "v_pk_mul_f16 v74, v52, v58 op_sel_hi:[0,1]\n"
    "v_pk_mul_f16 v75, v52, v59 op_sel_hi:[0,1]\n"
    "s_waitcnt vmcnt(0)\n"
    "v_mfma_f32_16x16x32_f16 v[76:79], v[116:119], v[68:71], v[76:79]\n"
    "v_pk_mul_f16 v60, v54, v58 op_sel_hi:[0,1]\n"
    "v_pk_mul_f16 v61, v54, v59 op_sel_hi:[0,1]\n"
    "v_mfma_f32_16x16x32_f16 v[76:79], v[128:131], v[72:75], v[76:79]\n"
    "v_pk_mul_f16 v62, v56, v58 op_sel_hi:[0,1]\n"
    "v_readfirstlane_b32 s40, v24\n"
    "s_or_b32 s40, s40, s44\n"
    "s_lshl1_add_u32 s41, s40, s54\n"
    "s_cmp_lt_u32 s40, 256\n"
    "s_cselect_b32 s41, s41, 2\n"
    "s_cselect_b64 s[48:49], -1, 0\n"
    "v_mov_b32 v12, s41\n"
    "s_cbranch_scc0 .Lxd_tf15\n"
    ".Lxd_tf15r:\n"
    "s_mul_i32 s41, s41, 52\n"
    "s_add_u32 s38, s4, s41\n"
    "s_addc_u32 s39, s5, 0\n"
    "v_mfma_f32_16x16x32_f16 v[80:83], v[132:135], v[72:75], v[80:83]\n"
    "v_pk_mul_f16 v63, v56, v59 op_sel_hi:[0,1]\n"
    "ds_write_b32 v25, v12 offset:20\n"
    "global_load_dwordx3 v[36:38], v17, s[38:39] offset:-104\n"
    "global_load_dword v41, v18, s[38:39] offset:-8\n"
    "v_mfma_f32_16x16x32_f16 v[80:83], v[120:123], v[68:71], v[80:83]\n"
    "v_pk_mul_f16 v64, v56, v56 op_sel_hi:[0,1]\n"
    "v_sub_u32 v15, v0, v15\n"
    "v_mfma_f32_16x16x32_f16 v[84:87], v[124:127], v[68:71], v[84:87]\n"
    "v_pk_mul_f16 v65, v56, v49 op_sel_hi:[0,1]\n"
    "v_sub_u32 v16, v23, v16\n"
    "v_mfma_f32_16x16x32_f16 v[84:87], v[136:139], v[72:75], v[84:87]\n"
    "v_pk_mul_f16 v66, v48, v56 op_sel_hi:[0,1]\n"
    "v_permlane32_swap_b32 v7, v8\n"
    "v_permlane32_swap_b32 v9, v10\n"
    "v_smfmac_f32_16x16x64_f16 v[88:91], v[140:143], v[68:75], v2\n"
    "v_pk_mul_f16 v67, v48, v57 op_sel_hi:[0,1]\n"
    "v_add_f32 v7, v7, v8\n"
    "v_add_f32 v9, v9, v10\n"
    "v_mfma_f32_16x16x32_f16 v[76:79], v[144:147], v[60:63], v[76:79]\n"
    "v_pk_mul_f16 v68, v50, v56 op_sel_hi:[0,1]\n"
    "v_pk_mul_f16 v69, v50, v57 op_sel_hi:[0,1]\n"
    "v_mfma_f32_16x16x32_f16 v[76:79], v[156:159], v[64:67], v[76:79]\n"
    "v_pk_mul_f16 v70, v52, v56 op_sel_hi:[0,1]\n"
    "v_permlane16_swap_b32 v7, v9\n"
    "v_mfma_f32_16x16x32_f16 v[80:83], v[160:163], v[64:67], v[80:83]\n"
    "v_pk_mul_f16 v71, v52, v57 op_sel_hi:[0,1]\n"
    "v_add_f32 v7, v7, v9\n"
    "v_mfma_f32_16x16x32_f16 v[80:83], v[148:151], v[60:63], v[80:83]\n"
    "v_pk_mul_f16 v72, v54, v56 op_sel_hi:[0,1]\n"
    "ds_read_b32 v22, v25 offset:516\n"
    "v_mfma_f32_16x16x32_f16 v[84:87], v[152:155], v[60:63], v[84:87]\n"
    "v_pk_mul_f16 v73, v54, v57 op_sel_hi:[0,1]\n"
    "v_add_f32_dpp v7, v7, v7 quad_perm:[1,0,3,2] row_mask:0xf bank_mask:0xf\n"
    "v_mfma_f32_16x16x32_f16 v[84:87], v[164:167], v[64:67], v[84:87]\n"
    "v_pk_mul_f16 v74, v54, v54 op_sel_hi:[0,1]\n"
    "v_add_f32_dpp v7, v7, v7 quad_perm:[2,3,0,1] row_mask:0xf bank_mask:0xf\n"
    "v_smfmac_f32_16x16x64_f16 v[88:91], v[168:171], v[60:67], v2\n"
    "v_pk_mul_f16 v75, v54, v49 op_sel_hi:[0,1]\n"
    "v_add_f32_dpp v7, v7, v7 row_half_mirror row_mask:0xf bank_mask:0xf\n"
    "v_mfma_f32_16x16x32_f16 v[76:79], v[172:175], v[68:71], v[76:79]\n"
    "v_pk_mul_f16 v60, v48, v54 op_sel_hi:[0,1]\n"
    "v_pk_mul_f16 v61, v48, v55 op_sel_hi:[0,1]\n"
    "v_mfma_f32_16x16x32_f16 v[76:79], v[184:187], v[72:75], v[76:79]\n"
    "v_pk_mul_f16 v62, v50, v54 op_sel_hi:[0,1]\n"
    "v_add_f32_dpp v7, v7, v7 row_mirror row_mask:0xf bank_mask:0xf\n"
    "v_mfma_f32_16x16x32_f16 v[80:83], v[188:191], v[72:75], v[80:83]\n"
    "v_pk_mul_f16 v63, v50, v55 op_sel_hi:[0,1]\n"
    "s_waitcnt lgkmcnt(0)\n"
    "v_readfirstlane_b32 s45, v22\n"
    "s_cmp_lt_i32 s45, 1\n"
    "s_cbranch_scc1 .Lxd_ck18\n"
    ".Lxd_ck18r:\n"
    "v_mfma_f32_16x16x32_f16 v[80:83], v[176:179], v[68:71], v[80:83]\n"
    "v_pk_mul_f16 v64, v52, v54 op_sel_hi:[0,1]\n"
    "ds_read_b64_tr_b16 v[44:45], v5 offset:4480\n"
    "v_mfma_f32_16x16x32_f16 v[84:87], v[180:183], v[68:71], v[84:87]\n"
    "v_pk_mul_f16 v65, v52, v55 op_sel_hi:[0,1]\n"
    "ds_read_b64_tr_b16 v[46:47], v6 offset:4480\n"
    "v_mfma_f32_16x16x32_f16 v[84:87], v[192:195], v[72:75], v[84:87]\n"
    "v_pk_mul_f16 v66, v52, v52 op_sel_hi:[0,1]\n"
    "ds_read_b64_tr_b16 v[58:59], v4 offset:7680\n"
    "s_cbranch_vccnz .Lxd_pr17\n"
    "s_setprio 0\n"
    ".Lxd_pr17:\n"
    "v_smfmac_f32_16x16x64_f16 v[88:91], v[196:199], v[68:75], v2\n"
    "v_pk_mul_f16 v67, v52, v49 op_sel_hi:[0,1]\n"
    "ds_read_b64_tr_b16 v[56:57], v4 offset:7040\n"
    "v_mfma_f32_16x16x32_f16 v[76:79], v[200:203], v[60:63], v[76:79]\n"
    "v_pk_mul_f16 v68, v48, v52 op_sel_hi:[0,1]\n"
    "v_pk_mul_f16 v69, v48, v53 op_sel_hi:[0,1]\n"
    "v_mfma_f32_16x16x32_f16 v[76:79], v[212:215], v[64:67], v[76:79]\n"
    "v_pk_mul_f16 v70, v50, v52 op_sel_hi:[0,1]\n"
    "ds_read_b64_tr_b16 v[54:55], v4 offset:6400\n"
    "v_mfma_f32_16x16x32_f16 v[80:83], v[216:219], v[64:67], v[80:83]\n"
    "v_pk_mul_f16 v71, v50, v53 op_sel_hi:[0,1]\n"
    "ds_write_b32 v14, v7\n"
    "v_add_u32 v14, 64, v14\n"
    "v_mfma_f32_16x16x32_f16 v[80:83], v[204:207], v[60:63], v[80:83]\n"
    "v_pk_mul_f16 v72, v50, v50 op_sel_hi:[0,1]\n"
    "v_mfma_f32_16x16x32_f16 v[84:87], v[208:211], v[60:63], v[84:87]\n"
    "v_pk_mul_f16 v73, v50, v49 op_sel_hi:[0,1]\n"
    "v_pk_mul_f16 v74, v48, v50 op_sel_hi:[0,1]\n"
    "v_mfma_f32_16x16x32_f16 v[84:87], v[220:223], v[64:67], v[84:87]\n"
    "v_pk_mul_f16 v75, v48, v51 op_sel_hi:[0,1]\n"
    "v_smfmac_f32_16x16x64_f16 v[88:91], v[224:227], v[60:67], v2\n"
    "ds_read_b64_tr_b16 v[48:49], v3 offset:4480\n"
    "ds_read_b64_tr_b16 v[50:51], v4 offset:5120\n"
    "ds_read_b64_tr_b16 v[52:53], v4 offset:5760\n"
    "v_smfmac_f32_16x16x64_f16 v[88:91], v[252:255], v[68:75], v2\n"
    "s_waitcnt lgkmcnt(7)\n"
    "v_pk_mul_f16 v60, v44, s45 op_sel:[0,1]\n"
    "v_pk_mul_f16 v61, v45, s45 op_sel:[0,1]\n"
    "v_mfma_f32_16x16x32_f16 v[76:79], v[228:231], v[68:71], v[76:79]\n"
    "v_pk_mul_f16 v62, v46, s45 op_sel:[0,1]\n"
    "v_pk_mul_f16 v63, v47, s45 op_sel:[0,1]\n"
    "v_mfma_f32_16x16x32_f16 v[76:79], v[240:243], v[72:75], v[76:79]\n"
    "v_pk_mul_f16 v64, v44, v46\n"
    "v_pk_mul_f16 v65, v45, v47\n"
    "v_mfma_f32_16x16x32_f16 v[80:83], v[244:247], v[72:75], v[80:83]\n"
    "v_add_f32 v11, v88, v89\n"
    "v_add_f32 v13, v90, v91\n"
    "v_mfma_f32_16x16x32_f16 v[80:83], v[232:235], v[68:71], v[80:83]\n"
    "v_mov_b64 v[88:89], 0\n"
    "v_mov_b64 v[90:91], 0\n"
    "v_mfma_f32_16x16x32_f16 v[84:87], v[236:239], v[68:71], v[84:87]\n"
    "s_waitcnt lgkmcnt(0)\n"
    "v_pk_mul_f16 v66, v48, v48 op_sel_hi:[0,1]\n"
    "v_pk_mul_f16 v67, v48, v49 op_sel_hi:[0,1]\n"
    "v_mfma_f32_16x16x32_f16 v[84:87], v[248:251], v[72:75], v[84:87]\n"
    "v_mul_f32 v11, v11, v13\n"
    "s_cmp_eq_u32 s45, -1\n"
    "s_cbranch_scc1 .Lxd_ex0\n"
    "s_cmp_lg_u32 s36, 0\n"
    "s_cbranch_scc1 .Lxd_pl1\n"
    ".Lxd_ph1:\n"
    "s_cbranch_vccnz .Lxd_pr19\n"
    "s_setprio 2\n"
    ".Lxd_pr19:\n"
    "v_smfmac_f32_16x16x64_f16 v[88:91], v[112:115], v[60:67], v2\n"
    "v_fmac_f32 v11, v76, v80\n"
    "v_fmac_f32 v11, v77, v81\n"
    "v_fmac_f32 v11, v78, v82\n"
    "v_fmac_f32 v11, v79, v83\n"
    "v_mfma_f32_16x16x32_f16 v[80:83], v[92:95], v[60:63], 0\n"
    "v_pk_mul_f16 v68, v58, v58 op_sel_hi:[0,1]\n"
    "v_pk_mul_f16 v69, v58, v49 op_sel_hi:[0,1]\n"
    "v_mfma_f32_16x16x32_f16 v[80:83], v[104:107], v[64:67], v[80:83]\n"
    "v_pk_mul_f16 v70, v48, v58 op_sel_hi:[0,1]\n"
    "v_pk_mul_f16 v71, v48, v59 op_sel_hi:[0,1]\n"
    "v_mfma_f32_16x16x32_f16 v[76:79], v[100:103], v[64:67], 0\n"
    "v_fmac_f32 v11, v84, v85\n"
    "v_fma_f32 v7, v86, v87, v11\n"
    "v_mfma_f32_16x16x32_f16 v[84:87], v[108:111], v[64:67], 0\n"
    "v_pk_mul_f16 v72, v50, v58 op_sel_hi:[0,1]\n"
    "v_pk_mul_f16 v73, v50, v59 op_sel_hi:[0,1]\n"
    "v_mfma_f32_16x16x32_f16 v[84:87], v[96:99], v[60:63], v[84:87]\n"
    "v_pk_mul_f16 v74, v52, v58 op_sel_hi:[0,1]\n"
    "v_pk_mul_f16 v75, v52, v59 op_sel_hi:[0,1]\n"
    "s_waitcnt vmcnt(0)\n"
    "v_mfma_f32_16x16x32_f16 v[76:79], v[116:119], v[68:71], v[76:79]\n"
    "v_pk_mul_f16 v60, v54, v58 op_sel_hi:[0,1]\n"
    "v_pk_mul_f16 v61, v54, v59 op_sel_hi:[0,1]\n"
    "v_mfma_f32_16x16x32_f16 v[76:79], v[128:131], v[72:75], v[76:79]\n"
    "v_pk_mul_f16 v62, v56, v58 op_sel_hi:[0,1]\n"
    "v_lshl_or_b32 v36, v36, 7, v19\n"
    "global_load_dwordx4 v[28:31], v36, s[8:9]\n"
    "v_mfma_f32_16x16x32_f16 v[80:83], v[132:135], v[72:75], v[80:83]\n"
    "v_pk_mul_f16 v63, v56, v59 op_sel_hi:[0,1]\n"
    "v_lshl_or_b32 v37, v37, 7, v19\n"
    "global_load_dwordx4 v[32:35], v37, s[8:9]\n"
    "v_mfma_f32_16x16x32_f16 v[80:83], v[120:123], v[68:71], v[80:83]\n"
    "v_pk_mul_f16 v64, v56, v56 op_sel_hi:[0,1]\n"
    "v_lshl_or_b32 v41, v41, 7, v20\n"
    "global_load_dword v40, v41, s[8:9]\n"
    "v_mfma_f32_16x16x32_f16 v[84:87], v[124:127], v[68:71], v[84:87]\n"
    "v_pk_mul_f16 v65, v56, v49 op_sel_hi:[0,1]\n"
    "v_lshl_or_b32 v38, v38, 7, v19\n"
    "global_load_dwordx4 v[36:39], v38, s[8:9]\n"
    "v_mfma_f32_16x16x32_f16 v[84:87], v[136:139], v[72:75], v[84:87]\n"
    "v_pk_mul_f16 v66, v48, v56 op_sel_hi:[0,1]\n"
    "v_smfmac_f32_16x16x64_f16 v[88:91], v[140:143], v[68:75], v2\n"
    "v_pk_mul_f16 v67, v48, v57 op_sel_hi:[0,1]\n"
    "v_mfma_f32_16x16x32_f16 v[76:79], v[144:147], v[60:63], v[76:79]\n"
    "v_pk_mul_f16 v68, v50, v56 op_sel_hi:[0,1]\n"
    "v_pk_mul_f16 v69, v50, v57 op_sel_hi:[0,1]\n"
    "v_mfma_f32_16x16x32_f16 v[76:79], v[156:159], v[64:67], v[76:79]\n"
    "v_pk_mul_f16 v70, v52, v56 op_sel_hi:[0,1]\n"
    "v_mfma_f32_16x16x32_f16 v[80:83], v[160:163], v[64:67], v[80:83]\n"
    "v_pk_mul_f16 v71, v52, v57 op_sel_hi:[0,1]\n"
    "v_mfma_f32_16x16x32_f16 v[80:83], v[148:151], v[60:63], v[80:83]\n"
    "v_pk_mul_f16 v72, v54, v56 op_sel_hi:[0,1]\n"
    "ds_read_b32 v22, v25 offset:520\n"
    "v_mfma_f32_16x16x32_f16 v[84:87], v[152:155], v[60:63], v[84:87]\n"
    "v_pk_mul_f16 v73, v54, v57 op_sel_hi:[0,1]\n"
    "v_mfma_f32_16x16x32_f16 v[84:87], v[164:167], v[64:67], v[84:87]\n"
    "v_pk_mul_f16 v74, v54, v54 op_sel_hi:[0,1]\n"
    "v_smfmac_f32_16x16x64_f16 v[88:91], v[168:171], v[60:67], v2\n"
    "v_pk_mul_f16 v75, v54, v49 op_sel_hi:[0,1]\n"
    "v_mfma_f32_16x16x32_f16 v[76:79], v[172:175], v[68:71], v[76:79]\n"
    "v_pk_mul_f16 v60, v48, v54 op_sel_hi:[0,1]\n"
    "v_pk_mul_f16 v61, v48, v55 op_sel_hi:[0,1]\n"
    "v_mfma_f32_16x16x32_f16 v[76:79], v[184:187], v[72:75], v[76:79]\n"
    "v_pk_mul_f16 v62, v50, v54 op_sel_hi:[0,1]\n"
    "v_mfma_f32_16x16x32_f16 v[80:83], v[188:191], v[72:75], v[80:83]\n"
    "v_pk_mul_f16 v63, v50, v55 op_sel_hi:[0,1]\n"
    "s_waitcnt lgkmcnt(0)\n"
    "v_readfirstlane_b32 s45, v22\n"
    "s_cmp_lt_i32 s45, 1\n"
    "s_cbranch_scc1 .Lxd_ck21\n"
    ".Lxd_ck21r:\n"
    "v_mfma_f32_16x16x32_f16 v[80:83], v[176:179], v[68:71], v[80:83]\n"
    "v_pk_mul_f16 v64, v52, v54 op_sel_hi:[0,1]\n"
    "ds_read_b64_tr_b16 v[44:45], v5 offset:8960\n"
    "v_mfma_f32_16x16x32_f16 v[84:87], v[180:183], v[68:71], v[84:87]\n"
    "v_pk_mul_f16 v65, v52, v55 op_sel_hi:[0,1]\n"
    "ds_read_b64_tr_b16 v[46:47], v6 offset:8960\n"
    "v_mfma_f32_16x16x32_f16 v[84:87], v[192:195], v[72:75], v[84:87]\n"
    "v_pk_mul_f16 v66, v52, v52 op_sel_hi:[0,1]\n"
    "ds_read_b64_tr_b16 v[58:59], v4 offset:12160\n"
    "s_cbranch_vccnz .Lxd_pr20\n"
    "s_setprio 0\n"
    ".Lxd_pr20:\n"
    "v_smfmac_f32_16x16x64_f16 v[88:91], v[196:199], v[68:75], v2\n"
    "v_pk_mul_f16 v67, v52, v49 op_sel_hi:[0,1]\n"
    "ds_read_b64_tr_b16 v[56:57], v4 offset:11520\n"
    "v_mfma_f32_16x16x32_f16 v[76:79], v[200:203], v[60:63], v[76:79]\n"
    "v_pk_mul_f16 v68, v48, v52 op_sel_hi:[0,1]\n"
    "v_pk_mul_f16 v69, v48, v53 op_sel_hi:[0,1]\n"
    "v_mfma_f32_16x16x32_f16 v[76:79], v[212:215], v[64:67], v[76:79]\n"
    "v_pk_mul_f16 v70, v50, v52 op_sel_hi:[0,1]\n"
    "ds_read_b64_tr_b16 v[54:55], v4 offset:10880\n"
    "v_mfma_f32_16x16x32_f16 v[80:83], v[216:219], v[64:67], v[80:83]\n"
    "v_pk_mul_f16 v71, v50, v53 op_sel_hi:[0,1]\n"
    "v_mfma_f32_16x16x32_f16 v[80:83], v[204:207], v[60:63], v[80:83]\n"
    "v_pk_mul_f16 v72, v50, v50 op_sel_hi:[0,1]\n"
    "v_mfma_f32_16x16x32_f16 v[84:87], v[208:211], v[60:63], v[84:87]\n"
    "v_pk_mul_f16 v73, v50, v49 op_sel_hi:[0,1]\n"
    "v_pk_mul_f16 v74, v48, v50 op_sel_hi:[0,1]\n"
    "v_mfma_f32_16x16x32_f16 v[84:87], v[220:223], v[64:67], v[84:87]\n"
    "v_pk_mul_f16 v75, v48, v51 op_sel_hi:[0,1]\n"
    "v_smfmac_f32_16x16x64_f16 v[88:91], v[224:227], v[60:67], v2\n"
    "ds_read_b64_tr_b16 v[48:49], v3 offset:8960\n"
    "ds_read_b64_tr_b16 v[50:51], v4 offset:9600\n"
    "ds_read_b64_tr_b16 v[52:53], v4 offset:10240\n"
    "v_smfmac_f32_16x16x64_f16 v[88:91], v[252:255], v[68:75], v2\n"
    "s_waitcnt lgkmcnt(6)\n"
    "v_pk_mul_f16 v60, v44, s45 op_sel:[0,1]\n"
    "v_pk_mul_f16 v61, v45, s45 op_sel:[0,1]\n"
    "v_mfma_f32_16x16x32_f16 v[76:79], v[228:231], v[68:71], v[76:79]\n"
    "v_pk_mul_f16 v62, v46, s45 op_sel:[0,1]\n"
    "v_pk_mul_f16 v63, v47, s45 op_sel:[0,1]\n"
    "v_mfma_f32_16x16x32_f16 v[76:79], v[240:243], v[72:75], v[76:79]\n"
    "v_pk_mul_f16 v64, v44, v46\n"
    "v_pk_mul_f16 v65, v45, v47\n"
    "v_mfma_f32_16x16x32_f16 v[80:83], v[244:247], v[72:75], v[80:83]\n"
    "v_add_f32 v11, v88, v89\n"
    "v_add_f32 v13, v90, v91\n"
    "v_mfma_f32_16x16x32_f16 v[80:83], v[232:235], v[68:71], v[80:83]\n"
    "v_mov_b64 v[88:89], 0\n"
    "v_mov_b64 v[90:91], 0\n"
    "v_mfma_f32_16x16x32_f16 v[84:87], v[236:239], v[68:71], v[84:87]\n"
    "s_waitcnt lgkmcnt(0)\n"
    "v_pk_mul_f16 v66, v48, v48 op_sel_hi:[0,1]\n"
    "v_pk_mul_f16 v67, v48, v49 op_sel_hi:[0,1]\n"
    "v_mfma_f32_16x16x32_f16 v[84:87], v[248:251], v[72:75], v[84:87]\n"
    "v_mul_f32 v11, v11, v13\n"
    "s_cmp_eq_u32 s45, -1\n"
    "s_cbranch_scc1 .Lxd_ex1\n"
    ".Lxd_ph2:\n"
    "s_cbranch_vccnz .Lxd_pr22\n"
    "s_setprio 2\n"
    ".Lxd_pr22:\n"
    "v_smfmac_f32_16x16x64_f16 v[88:91], v[112:115], v[60:67], v2\n"
    "v_fmac_f32 v11, v76, v80\n"
    "v_fmac_f32 v11, v77, v81\n"
    "v_fmac_f32 v11, v78, v82\n"
    "v_fmac_f32 v11, v79, v83\n"
    "v_mfma_f32_16x16x32_f16 v[80:83], v[92:95], v[60:63], 0\n"
    "v_pk_mul_f16 v68, v58, v58 op_sel_hi:[0,1]\n"
    "v_pk_mul_f16 v69, v58, v49 op_sel_hi:[0,1]\n"
    "v_mfma_f32_16x16x32_f16 v[80:83], v[104:107], v[64:67], v[80:83]\n"
    "v_pk_mul_f16 v70, v48, v58 op_sel_hi:[0,1]\n"
    "v_pk_mul_f16 v71, v48, v59 op_sel_hi:[0,1]\n"
    "v_mfma_f32_16x16x32_f16 v[76:79], v[100:103], v[64:67], 0\n"
    "v_fmac_f32 v11, v84, v85\n"
    "v_fma_f32 v8, v86, v87, v11\n"
    "v_mfma_f32_16x16x32_f16 v[84:87], v[108:111], v[64:67], 0\n"
    "v_pk_mul_f16 v72, v50, v58 op_sel_hi:[0,1]\n"
    "v_pk_mul_f16 v73, v50, v59 op_sel_hi:[0,1]\n"
    "v_mfma_f32_16x16x32_f16 v[84:87], v[96:99], v[60:63], v[84:87]\n"
    "v_pk_mul_f16 v74, v52, v58 op_sel_hi:[0,1]\n"
    "v_pk_mul_f16 v75, v52, v59 op_sel_hi:[0,1]\n"
    "s_waitcnt vmcnt(0)\n"
    "v_mfma_f32_16x16x32_f16 v[76:79], v[116:119], v[68:71], v[76:79]\n"
    "v_pk_mul_f16 v60, v54, v58 op_sel_hi:[0,1]\n"
    "v_pk_mul_f16 v61, v54, v59 op_sel_hi:[0,1]\n"
    "s_movk_i32 s43, 15744\n"
    "v_pk_maximum3_f16 v21, v28, v28, s43 op_sel_hi:[1,1,0] neg_lo:[0,1,0] neg_hi:[0,1,0]\n"
    "v_mfma_f32_16x16x32_f16 v[76:79], v[128:131], v[72:75], v[76:79]\n"
    "v_pk_mul_f16 v62, v56, v58 op_sel_hi:[0,1]\n"
    "v_pk_maximum3_f16 v21, v29, v29, v21 neg_lo:[0,1,0] neg_hi:[0,1,0]\n"
    "v_mfma_f32_16x16x32_f16 v[80:83], v[132:135], v[72:75], v[80:83]\n"
    "v_pk_mul_f16 v63, v56, v59 op_sel_hi:[0,1]\n"
    "v_pk_maximum3_f16 v21, v30, v30, v21 neg_lo:[0,1,0] neg_hi:[0,1,0]\n"
    "v_mfma_f32_16x16x32_f16 v[80:83], v[120:123], v[68:71], v[80:83]\n"
    "v_pk_mul_f16 v64, v56, v56 op_sel_hi:[0,1]\n"
    "v_pk_maximum3_f16 v21, v31, v31, v21 neg_lo:[0,1,0] neg_hi:[0,1,0]\n"
    "v_mfma_f32_16x16x32_f16 v[84:87], v[124:127], v[68:71], v[84:87]\n"
    "v_pk_mul_f16 v65, v56, v49 op_sel_hi:[0,1]\n"
    "v_pk_maximum3_f16 v21, v32, v32, v21 neg_lo:[0,1,0] neg_hi:[0,1,0]\n"
    "v_mfma_f32_16x16x32_f16 v[84:87], v[136:139], v[72:75], v[84:87]\n"
    "v_pk_mul_f16 v66, v48, v56 op_sel_hi:[0,1]\n"
    "v_pk_maximum3_f16 v21, v33, v33, v21 neg_lo:[0,1,0] neg_hi:[0,1,0]\n"
    "v_smfmac_f32_16x16x64_f16 v[88:91], v[140:143], v[68:75], v2\n"
    "v_pk_mul_f16 v67, v48, v57 op_sel_hi:[0,1]\n"
    "v_pk_maximum3_f16 v21, v34, v34, v21 neg_lo:[0,1,0] neg_hi:[0,1,0]\n"
    "v_mfma_f32_16x16x32_f16 v[76:79], v[144:147], v[60:63], v[76:79]\n"
    "v_pk_mul_f16 v68, v50, v56 op_sel_hi:[0,1]\n"
    "v_pk_mul_f16 v69, v50, v57 op_sel_hi:[0,1]\n"
    "v_pk_maximum3_f16 v21, v35, v35, v21 neg_lo:[0,1,0] neg_hi:[0,1,0]\n"
    "v_mfma_f32_16x16x32_f16 v[76:79], v[156:159], v[64:67], v[76:79]\n"
    "v_pk_mul_f16 v70, v52, v56 op_sel_hi:[0,1]\n"
    "v_pk_maximum3_f16 v21, v36, v36, v21 neg_lo:[0,1,0] neg_hi:[0,1,0]\n"
    "v_mfma_f32_16x16x32_f16 v[80:83], v[160:163], v[64:67], v[80:83]\n"
    "v_pk_mul_f16 v71, v52, v57 op_sel_hi:[0,1]\n"
    "v_pk_maximum3_f16 v21, v37, v37, v21 neg_lo:[0,1,0] neg_hi:[0,1,0]\n"
    "v_mfma_f32_16x16x32_f16 v[80:83], v[148:151], v[60:63], v[80:83]\n"
    "v_pk_mul_f16 v72, v54, v56 op_sel_hi:[0,1]\n"
    "ds_read_b32 v22, v25 offset:524\n"
    "v_pk_maximum3_f16 v21, v38, v38, v21 neg_lo:[0,1,0] neg_hi:[0,1,0]\n"
    "v_mfma_f32_16x16x32_f16 v[84:87], v[152:155], v[60:63], v[84:87]\n"
    "v_pk_mul_f16 v73, v54, v57 op_sel_hi:[0,1]\n"
    "v_pk_maximum3_f16 v21, v39, v39, v21 neg_lo:[0,1,0] neg_hi:[0,1,0]\n"
    "v_mfma_f32_16x16x32_f16 v[84:87], v[164:167], v[64:67], v[84:87]\n"
    "v_pk_mul_f16 v74, v54, v54 op_sel_hi:[0,1]\n"
    "v_pk_maximum3_f16 v21, v40, v40, v21 neg_lo:[0,1,0] neg_hi:[0,1,0]\n"
    "v_smfmac_f32_16x16x64_f16 v[88:91], v[168:171], v[60:67], v2\n"
    "v_pk_mul_f16 v75, v54, v49 op_sel_hi:[0,1]\n"
    "v_pk_max_u16 v21, v21, v21 op_sel:[0,1] op_sel_hi:[1,0]\n"
    "v_mfma_f32_16x16x32_f16 v[76:79], v[172:175], v[68:71], v[76:79]\n"
    "v_pk_mul_f16 v60, v48, v54 op_sel_hi:[0,1]\n"
    "v_pk_mul_f16 v61, v48, v55 op_sel_hi:[0,1]\n"
    "v_max_u32_dpp v21, v21, v21 quad_perm:[1,0,3,2] row_mask:0xf bank_mask:0xf\n"
    "v_mfma_f32_16x16x32_f16 v[76:79], v[184:187], v[72:75], v[76:79]\n"
    "v_pk_mul_f16 v62, v50, v54 op_sel_hi:[0,1]\n"
    "v_max_u32_dpp v21, v21, v21 quad_perm:[2,3,0,1] row_mask:0xf bank_mask:0xf\n"
    "v_mfma_f32_16x16x32_f16 v[80:83], v[188:191], v[72:75], v[80:83]\n"
    "v_pk_mul_f16 v63, v50, v55 op_sel_hi:[0,1]\n"
    "s_waitcnt lgkmcnt(0)\n"
    "v_readfirstlane_b32 s45, v22\n"
    "s_cmp_lt_i32 s45, 1\n"
    "s_cbranch_scc1 .Lxd_ck24\n"
    ".Lxd_ck24r:\n"
    "v_max_u32_dpp v21, v21, v21 row_half_mirror row_mask:0xf bank_mask:0xf\n"
    "v_mfma_f32_16x16x32_f16 v[80:83], v[176:179], v[68:71], v[80:83]\n"
    "v_pk_mul_f16 v64, v52, v54 op_sel_hi:[0,1]\n"
    "ds_read_b64_tr_b16 v[44:45], v5 offset:13440\n"
    "v_max_u32_dpp v21, v21, v21 row_mirror row_mask:0xf bank_mask:0xf\n"
    "v_mfma_f32_16x16x32_f16 v[84:87], v[180:183], v[68:71], v[84:87]\n"
    "v_pk_mul_f16 v65, v52, v55 op_sel_hi:[0,1]\n"
    "ds_read_b64_tr_b16 v[46:47], v6 offset:13440\n"
    "v_max_u32_dpp v21, v21, v21 row_bcast:15 row_mask:0xa bank_mask:0xf\n"
    "v_mfma_f32_16x16x32_f16 v[84:87], v[192:195], v[72:75], v[84:87]\n"
    "v_pk_mul_f16 v66, v52, v52 op_sel_hi:[0,1]\n"
    "ds_read_b64_tr_b16 v[58:59], v4 offset:16640\n"
    "v_max_u32_dpp v21, v21, v21 row_bcast:31 row_mask:0xc bank_mask:0xf\n"
    "s_cbranch_vccnz .Lxd_pr23\n"
    "s_setprio 0\n"
    ".Lxd_pr23:\n"
    "v_smfmac_f32_16x16x64_f16 v[88:91], v[196:199], v[68:75], v2\n"
    "v_pk_mul_f16 v67, v52, v49 op_sel_hi:[0,1]\n"
    "ds_read_b64_tr_b16 v[56:57], v4 offset:16000\n"
    "v_readlane_b32 s47, v21, 63\n"
    "v_mfma_f32_16x16x32_f16 v[76:79], v[200:203], v[60:63], v[76:79]\n"
    "v_pk_mul_f16 v68, v48, v52 op_sel_hi:[0,1]\n"
    "v_pk_mul_f16 v69, v48, v53 op_sel_hi:[0,1]\n"
    "s_and_b32 s50, s47, 0x7f80\n"
    "s_lshr_b32 s47, s50, 6\n"
    "s_sub_u32 s50, s50, 2816\n"
    "s_mul_i32 s51, s50, -8\n"
    "v_mfma_f32_16x16x32_f16 v[76:79], v[212:215], v[64:67], v[76:79]\n"
    "v_pk_mul_f16 v70, v50, v52 op_sel_hi:[0,1]\n"
    "ds_read_b64_tr_b16 v[54:55], v4 offset:15360\n"
    "v_pk_maximum3_f16 v21, v28, v28, s50 op_sel_hi:[1,1,0] neg_lo:[0,1,0] neg_hi:[0,1,0]\n"
    "v_mfma_f32_16x16x32_f16 v[80:83], v[216:219], v[64:67], v[80:83]\n"
    "v_pk_mul_f16 v71, v50, v53 op_sel_hi:[0,1]\n"
    "v_pk_mad_u16 v21, v21, 8, s51 op_sel_hi:[1,0,0]\n"
    "v_mfma_f32_16x16x32_f16 v[80:83], v[204:207], v[60:63], v[80:83]\n"
    "v_pk_mul_f16 v72, v50, v50 op_sel_hi:[0,1]\n"
    "v_bfi_b32 v28, s57, v21, v28\n"
    "v_mfma_f32_16x16x32_f16 v[84:87], v[208:211], v[60:63], v[84:87]\n"
    "v_pk_mul_f16 v73, v50, v49 op_sel_hi:[0,1]\n"
    "v_pk_mul_f16 v74, v48, v50 op_sel_hi:[0,1]\n"
    "v_pk_maximum3_f16 v12, v29, v29, s50 op_sel_hi:[1,1,0] neg_lo:[0,1,0] neg_hi:[0,1,0]\n"
    "v_mfma_f32_16x16x32_f16 v[84:87], v[220:223], v[64:67], v[84:87]\n"
    "v_pk_mul_f16 v75, v48, v51 op_sel_hi:[0,1]\n"
    "v_pk_mad_u16 v12, v12, 8, s51 op_sel_hi:[1,0,0]\n"
    "v_smfmac_f32_16x16x64_f16 v[88:91], v[224:227], v[60:67], v2\n"
    "ds_read_b64_tr_b16 v[48:49], v3 offset:13440\n"
    "ds_read_b64_tr_b16 v[50:51], v4 offset:14080\n"
    "ds_read_b64_tr_b16 v[52:53], v4 offset:14720\n"
    "v_bfi_b32 v29, s57, v12, v29\n"
    "v_smfmac_f32_16x16x64_f16 v[88:91], v[252:255], v[68:75], v2\n"
    "s_waitcnt lgkmcnt(6)\n"
    "v_pk_mul_f16 v60, v44, s45 op_sel:[0,1]\n"
    "v_pk_mul_f16 v61, v45, s45 op_sel:[0,1]\n"
    "v_mfma_f32_16x16x32_f16 v[76:79], v[228:231], v[68:71], v[76:79]\n"
    "v_pk_mul_f16 v62, v46, s45 op_sel:[0,1]\n"
    "v_pk_mul_f16 v63, v47, s45 op_sel:[0,1]\n"
    "v_mfma_f32_16x16x32_f16 v[76:79], v[240:243], v[72:75], v[76:79]\n"
    "v_pk_mul_f16 v64, v44, v46\n"
    "v_pk_mul_f16 v65, v45, v47\n"
    "v_mfma_f32_16x16x32_f16 v[80:83], v[244:247], v[72:75], v[80:83]\n"
    "v_add_f32 v11, v88, v89\n"
    "v_add_f32 v13, v90, v91\n"
    "v_mfma_f32_16x16x32_f16 v[80:83], v[232:235], v[68:71], v[80:83]\n"
    "v_mov_b64 v[88:89], 0\n"
    "v_mov_b64 v[90:91], 0\n"
    "v_mfma_f32_16x16x32_f16 v[84:87], v[236:239], v[68:71], v[84:87]\n"
    "s_waitcnt lgkmcnt(0)\n"
    "v_pk_mul_f16 v66, v48, v48 op_sel_hi:[0,1]\n"
    "v_pk_mul_f16 v67, v48, v49 op_sel_hi:[0,1]\n"
    "v_mfma_f32_16x16x32_f16 v[84:87], v[248:251], v[72:75], v[84:87]\n"
    "v_mul_f32 v11, v11, v13\n"
    "s_cmp_eq_u32 s45, -1\n"
    "s_cbranch_scc1 .Lxd_ex2\n"
    ".Lxd_ph3:\n"
    "s_cbranch_vccnz .Lxd_pr25\n"
    "s_setprio 2\n"
    ".Lxd_pr25:\n"
    "v_smfmac_f32_16x16x64_f16 v[88:91], v[112:115], v[60:67], v2\n"
    "v_fmac_f32 v11, v76, v80\n"
    "v_fmac_f32 v11, v77, v81\n"
    "v_fmac_f32 v11, v78, v82\n"
    "v_fmac_f32 v11, v79, v83\n"
    "v_mfma_f32_16x16x32_f16 v[80:83], v[92:95], v[60:63], 0\n"
    "v_pk_mul_f16 v68, v58, v58 op_sel_hi:[0,1]\n"
    "v_pk_mul_f16 v69, v58, v49 op_sel_hi:[0,1]\n"
    "v_add_u32 v3, s46, v3\n"
    "v_add_u32 v4, s46, v4\n"
    "v_mfma_f32_16x16x32_f16 v[80:83], v[104:107], v[64:67], v[80:83]\n"
    "v_pk_mul_f16 v70, v48, v58 op_sel_hi:[0,1]\n"
    "v_pk_mul_f16 v71, v48, v59 op_sel_hi:[0,1]\n"
    "v_add_u32 v5, s46, v5\n"
    "v_mfma_f32_16x16x32_f16 v[76:79], v[100:103], v[64:67], 0\n"
    "v_fmac_f32 v11, v84, v85\n"
    "v_fma_f32 v9, v86, v87, v11\n"
    "v_add_u32 v6, s46, v6\n"
    "s_sub_u32 s46, 0, s46\n"
    "v_mfma_f32_16x16x32_f16 v[84:87], v[108:111], v[64:67], 0\n"
    "v_pk_mul_f16 v72, v50, v58 op_sel_hi:[0,1]\n"
    "v_pk_mul_f16 v73, v50, v59 op_sel_hi:[0,1]\n"
    "v_pk_maximum3_f16 v21, v30, v30, s50 op_sel_hi:[1,1,0] neg_lo:[0,1,0] neg_hi:[0,1,0]\n"
    "v_pk_mad_u16 v21, v21, 8, s51 op_sel_hi:[1,0,0]\n"
    "v_mfma_f32_16x16x32_f16 v[84:87], v[96:99], v[60:63], v[84:87]\n"
    "v_pk_mul_f16 v74, v52, v58 op_sel_hi:[0,1]\n"
    "v_pk_mul_f16 v75, v52, v59 op_sel_hi:[0,1]\n"
    "v_bfi_b32 v30, s57, v21, v30\n"
    "v_mfma_f32_16x16x32_f16 v[76:79], v[116:119], v[68:71], v[76:79]\n"
    "v_pk_mul_f16 v60, v54, v58 op_sel_hi:[0,1]\n"
    "v_pk_mul_f16 v61, v54, v59 op_sel_hi:[0,1]\n"
    "v_pk_maximum3_f16 v12, v31, v31, s50 op_sel_hi:[1,1,0] neg_lo:[0,1,0] neg_hi:[0,1,0]\n"
    "v_mfma_f32_16x16x32_f16 v[76:79], v[128:131], v[72:75], v[76:79]\n"
    "v_pk_mul_f16 v62, v56, v58 op_sel_hi:[0,1]\n"
    "s_cmp_lt_u32 s55, 32\n"
    "s_cselect_b32 s44, 0, -1\n"
    "s_add_u32 s55, s55, 4\n"
    "v_add_u32_e64 v24, s44, 1\n"
    "s_mov_b64 exec, 1\n"
    "global_atomic_add v24, v17, v24, s[52:53] sc0 sc1\n"
    "s_mov_b64 exec, -1\n"
    "v_pk_mad_u16 v12, v12, 8, s51 op_sel_hi:[1,0,0]\n"
    "v_mfma_f32_16x16x32_f16 v[80:83], v[132:135], v[72:75], v[80:83]\n"
    "v_pk_mul_f16 v63, v56, v59 op_sel_hi:[0,1]\n"
    "v_bfi_b32 v31, s57, v12, v31\n"
    "v_pk_maximum3_f16 v21, v32, v32, s50 op_sel_hi:[1,1,0] neg_lo:[0,1,0] neg_hi:[0,1,0]\n"
    "v_mfma_f32_16x16x32_f16 v[80:83], v[120:123], v[68:71], v[80:83]\n"
    "v_pk_mul_f16 v64, v56, v56 op_sel_hi:[0,1]\n"
    "v_pk_mad_u16 v21, v21, 8, s51 op_sel_hi:[1,0,0]\n"
    "v_mfma_f32_16x16x32_f16 v[84:87], v[124:127], v[68:71], v[84:87]\n"
    "v_pk_mul_f16 v65, v56, v49 op_sel_hi:[0,1]\n"
    "v_bfi_b32 v32, s57, v21, v32\n"
    "v_mfma_f32_16x16x32_f16 v[84:87], v[136:139], v[72:75], v[84:87]\n"
    "v_pk_mul_f16 v66, v48, v56 op_sel_hi:[0,1]\n"
    "v_pk_maximum3_f16 v12, v33, v33, s50 op_sel_hi:[1,1,0] neg_lo:[0,1,0] neg_hi:[0,1,0]\n"
    "v_pk_mad_u16 v12, v12, 8, s51 op_sel_hi:[1,0,0]\n"
    "v_smfmac_f32_16x16x64_f16 v[88:91], v[140:143], v[68:75], v2\n"
    "v_pk_mul_f16 v67, v48, v57 op_sel_hi:[0,1]\n"
    "v_bfi_b32 v33, s57, v12, v33\n"
    "v_mfma_f32_16x16x32_f16 v[76:79], v[144:147], v[60:63], v[76:79]\n"
    "v_pk_mul_f16 v68, v50, v56 op_sel_hi:[0,1]\n"
    "v_pk_mul_f16 v69, v50, v57 op_sel_hi:[0,1]\n"
    "v_pk_maximum3_f16 v21, v34, v34, s50 op_sel_hi:[1,1,0] neg_lo:[0,1,0] neg_hi:[0,1,0]\n"
    "v_mfma_f32_16x16x32_f16 v[76:79], v[156:159], v[64:67], v[76:79]\n"
    "v_pk_mul_f16 v70, v52, v56 op_sel_hi:[0,1]\n"
    "v_pk_mad_u16 v21, v21, 8, s51 op_sel_hi:[1,0,0]\n"
    "v_mfma_f32_16x16x32_f16 v[80:83], v[160:163], v[64:67], v[80:83]\n"
    "v_pk_mul_f16 v71, v52, v57 op_sel_hi:[0,1]\n"
    "v_bfi_b32 v34, s57, v21, v34\n"
    "v_pk_maximum3_f16 v12, v35, v35, s50 op_sel_hi:[1,1,0] neg_lo:[0,1,0] neg_hi:[0,1,0]\n"
    "v_mfma_f32_16x16x32_f16 v[80:83], v[148:151], v[60:63], v[80:83]\n"
    "v_pk_mul_f16 v72, v54, v56 op_sel_hi:[0,1]\n"
    "ds_read_b32 v22, v25 offset:528\n"
    "v_pk_mad_u16 v12, v12, 8, s51 op_sel_hi:[1,0,0]\n"
    "v_mfma_f32_16x16x32_f16 v[84:87], v[152:155], v[60:63], v[84:87]\n"
    "v_pk_mul_f16 v73, v54, v57 op_sel_hi:[0,1]\n"
    "v_bfi_b32 v35, s57, v12, v35\n"
    "v_mfma_f32_16x16x32_f16 v[84:87], v[164:167], v[64:67], v[84:87]\n"
    "v_pk_mul_f16 v74, v54, v54 op_sel_hi:[0,1]\n"
    "v_pk_maximum3_f16 v21, v36, v36, s50 op_sel_hi:[1,1,0] neg_lo:[0,1,0] neg_hi:[0,1,0]\n"
    "v_pk_mad_u16 v21, v21, 8, s51 op_sel_hi:[1,0,0]\n"
    "v_smfmac_f32_16x16x64_f16 v[88:91], v[168:171], v[60:67], v2\n"
    "v_pk_mul_f16 v75, v54, v49 op_sel_hi:[0,1]\n"
    "ds_read_b32 v41, v25 offset:24\n"
    "v_bfi_b32 v36, s57, v21, v36\n"
    "v_mfma_f32_16x16x32_f16 v[76:79], v[172:175], v[68:71], v[76:79]\n"
    "v_pk_mul_f16 v60, v48, v54 op_sel_hi:[0,1]\n"
    "v_pk_mul_f16 v61, v48, v55 op_sel_hi:[0,1]\n"
    "v_pk_maximum3_f16 v12, v37, v37, s50 op_sel_hi:[1,1,0] neg_lo:[0,1,0] neg_hi:[0,1,0]\n"
    "v_mfma_f32_16x16x32_f16 v[76:79], v[184:187], v[72:75], v[76:79]\n"
    "v_pk_mul_f16 v62, v50, v54 op_sel_hi:[0,1]\n"
    "v_pk_mad_u16 v12, v12, 8, s51 op_sel_hi:[1,0,0]\n"
    "v_mfma_f32_16x16x32_f16 v[80:83], v[188:191], v[72:75], v[80:83]\n"
    "v_pk_mul_f16 v63, v50, v55 op_sel_hi:[0,1]\n"
    "s_waitcnt lgkmcnt(1)\n"
    "v_readfirstlane_b32 s45, v22\n"
    "s_cmp_lt_i32 s45, 1\n"
    "s_cbranch_scc1 .Lxd_ck27\n"
    ".Lxd_ck27r:\n"
    "v_bfi_b32 v37, s57, v12, v37\n"
    "v_pk_maximum3_f16 v21, v38, v38, s50 op_sel_hi:[1,1,0] neg_lo:[0,1,0] neg_hi:[0,1,0]\n"
    "v_mfma_f32_16x16x32_f16 v[80:83], v[176:179], v[68:71], v[80:83]\n"
    "v_pk_mul_f16 v64, v52, v54 op_sel_hi:[0,1]\n"
    "ds_read_b64_tr_b16 v[44:45], v5 offset:0\n"
    "v_pk_mad_u16 v21, v21, 8, s51 op_sel_hi:[1,0,0]\n"
    "v_mfma_f32_16x16x32_f16 v[84:87], v[180:183], v[68:71], v[84:87]\n"
    "v_pk_mul_f16 v65, v52, v55 op_sel_hi:[0,1]\n"
    "ds_read_b64_tr_b16 v[46:47], v6 offset:0\n"
    "v_bfi_b32 v38, s57, v21, v38\n"
    "v_mfma_f32_16x16x32_f16 v[84:87], v[192:195], v[72:75], v[84:87]\n"
    "v_pk_mul_f16 v66, v52, v52 op_sel_hi:[0,1]\n"
    "ds_read_b64_tr_b16 v[58:59], v4 offset:3200\n"
    "v_pk_maximum3_f16 v12, v39, v39, s50 op_sel_hi:[1,1,0] neg_lo:[0,1,0] neg_hi:[0,1,0]\n"
    "v_pk_mad_u16 v12, v12, 8, s51 op_sel_hi:[1,0,0]\n"
    "s_cbranch_vccnz .Lxd_pr26\n"
    "s_setprio 0\n"
    ".Lxd_pr26:\n"
    "v_smfmac_f32_16x16x64_f16 v[88:91], v[196:199], v[68:75], v2\n"
    "v_pk_mul_f16 v67, v52, v49 op_sel_hi:[0,1]\n"
    "ds_read_b64_tr_b16 v[56:57], v4 offset:2560\n"
    "v_bfi_b32 v39, s57, v12, v39\n"
    "v_mfma_f32_16x16x32_f16 v[76:79], v[200:203], v[60:63], v[76:79]\n"
    "v_pk_mul_f16 v68, v48, v52 op_sel_hi:[0,1]\n"
    "v_pk_mul_f16 v69, v48, v53 op_sel_hi:[0,1]\n"
    "v_pk_maximum3_f16 v21, v40, v40, s50 op_sel_hi:[1,1,0] neg_lo:[0,1,0] neg_hi:[0,1,0]\n"
    "v_mfma_f32_16x16x32_f16 v[76:79], v[212:215], v[64:67], v[76:79]\n"
    "v_pk_mul_f16 v70, v50, v52 op_sel_hi:[0,1]\n"
    "ds_read_b64_tr_b16 v[54:55], v4 offset:1920\n"
    "v_pk_mad_u16 v21, v21, 8, s51 op_sel_hi:[1,0,0]\n"
    "v_mfma_f32_16x16x32_f16 v[80:83], v[216:219], v[64:67], v[80:83]\n"
    "v_pk_mul_f16 v71, v50, v53 op_sel_hi:[0,1]\n"
    "v_bfi_b32 v40, s57, v21, v40\n"
    "s_sub_u32 s43, 514, s47\n"
    "s_lshl_b32 s43, s43, 22\n"
    "v_cvt_pkrtz_f16_f32 v12, s43, s43\n"
    "v_mfma_f32_16x16x32_f16 v[80:83], v[204:207], v[60:63], v[80:83]\n"
    "v_pk_mul_f16 v72, v50, v50 op_sel_hi:[0,1]\n"
    "v_lshl_or_b32 v21, v12, 17, s47\n"
    "v_mfma_f32_16x16x32_f16 v[84:87], v[208:211], v[60:63], v[84:87]\n"
    "v_pk_mul_f16 v73, v50, v49 op_sel_hi:[0,1]\n"
    "v_pk_mul_f16 v74, v48, v50 op_sel_hi:[0,1]\n"
    "s_waitcnt lgkmcnt(5)\n"
    "v_alignbit_b32 v21, v41, v21, 1\n"
    "v_mfma_f32_16x16x32_f16 v[84:87], v[220:223], v[64:67], v[84:87]\n"
    "v_pk_mul_f16 v75, v48, v51 op_sel_hi:[0,1]\n"
    "s_mov_b64 exec, s[48:49]\n"
    "ds_write_b128 v15, v[28:31] offset:0\n"
    "ds_write_b128 v15, v[32:35] offset:160\n"
    "ds_write_b128 v15, v[36:39] offset:320\n"
    "ds_write_b32 v16, v40 offset:3840\n"
    "ds_write_b32 v16, v12 offset:4160\n"
    "ds_write_b32 v25, v21 offset:532\n"
    "s_mov_b64 exec, -1\n"
    "v_smfmac_f32_16x16x64_f16 v[88:91], v[224:227], v[60:67], v2\n"
    "ds_read_b64_tr_b16 v[48:49], v3 offset:0\n"
    "ds_read_b64_tr_b16 v[50:51], v4 offset:640\n"
    "ds_read_b64_tr_b16 v[52:53], v4 offset:1280\n"
    "v_smfmac_f32_16x16x64_f16 v[88:91], v[252:255], v[68:75], v2\n"
    "s_waitcnt lgkmcnt(12)\n"
    "v_pk_mul_f16 v60, v44, s45 op_sel:[0,1]\n"
    "v_pk_mul_f16 v61, v45, s45 op_sel:[0,1]\n"
    "v_mfma_f32_16x16x32_f16 v[76:79], v[228:231], v[68:71], v[76:79]\n"
    "v_pk_mul_f16 v62, v46, s45 op_sel:[0,1]\n"
    "v_pk_mul_f16 v63, v47, s45 op_sel:[0,1]\n"
    "v_mfma_f32_16x16x32_f16 v[76:79], v[240:243], v[72:75], v[76:79]\n"
    "v_pk_mul_f16 v64, v44, v46\n"
    "v_pk_mul_f16 v65, v45, v47\n"
    "v_mfma_f32_16x16x32_f16 v[80:83], v[244:247], v[72:75], v[80:83]\n"
    "v_add_f32 v11, v88, v89\n"
    "v_add_f32 v13, v90, v91\n"
    "v_mfma_f32_16x16x32_f16 v[80:83], v[232:235], v[68:71], v[80:83]\n"
    "v_mov_b64 v[88:89], 0\n"
    "v_mov_b64 v[90:91], 0\n"
    "v_mfma_f32_16x16x32_f16 v[84:87], v[236:239], v[68:71], v[84:87]\n"
    "s_waitcnt lgkmcnt(0)\n"
    "v_pk_mul_f16 v66, v48, v48 op_sel_hi:[0,1]\n"
    "v_pk_mul_f16 v67, v48, v49 op_sel_hi:[0,1]\n"
    "v_mfma_f32_16x16x32_f16 v[84:87], v[248:251], v[72:75], v[84:87]\n"
    "v_mul_f32 v11, v11, v13\n"
    "v_add_u32 v25, 16, v25\n"
    "s_cmp_eq_u32 s45, -1\n"
    "s_cbranch_scc0 .Lxd_ph0\n"
    "s_branch .Lxd_ex3\n"
    ".Lxd_pl0:\n"
    "s_cbranch_vccnz .Lxd_pr28\n"
    "s_setprio 2\n"
    ".Lxd_pr28:\n"
    "v_smfmac_f32_16x16x64_f16 v[88:91], v[112:115], v[60:67], v2\n"
    "v_fmac_f32 v11, v76, v80\n"
    "v_fmac_f32 v11, v77, v81\n"
    "v_fmac_f32 v11, v78, v82\n"
    "v_fmac_f32 v11, v79, v83\n"
    "v_mfma_f32_16x16x32_f16 v[80:83], v[92:95], v[60:63], 0\n"
    "v_pk_mul_f16 v68, v58, v58 op_sel_hi:[0,1]\n"
    "v_pk_mul_f16 v69, v58, v49 op_sel_hi:[0,1]\n"
    "v_mfma_f32_16x16x32_f16 v[80:83], v[104:107], v[64:67], v[80:83]\n"
    "v_pk_mul_f16 v70, v48, v58 op_sel_hi:[0,1]\n"
    "v_pk_mul_f16 v71, v48, v59 op_sel_hi:[0,1]\n"
    "v_mfma_f32_16x16x32_f16 v[76:79], v[100:103], v[64:67], 0\n"
    "v_fmac_f32 v11, v84, v85\n"
    "v_fma_f32 v10, v86, v87, v11\n"
    "v_mfma_f32_16x16x32_f16 v[84:87], v[108:111], v[64:67], 0\n"
    "v_pk_mul_f16 v72, v50, v58 op_sel_hi:[0,1]\n"
    "v_pk_mul_f16 v73, v50, v59 op_sel_hi:[0,1]\n"
    "v_mfma_f32_16x16x32_f16 v[84:87], v[96:99], v[60:63], v[84:87]\n"
    "v_pk_mul_f16 v74, v52, v58 op_sel_hi:[0,1]\n"
    "v_pk_mul_f16 v75, v52, v59 op_sel_hi:[0,1]\n"
    "v_mfma_f32_16x16x32_f16 v[76:79], v[116:119], v[68:71], v[76:79]\n"
    "v_pk_mul_f16 v60, v54, v58 op_sel_hi:[0,1]\n"
    "v_pk_mul_f16 v61, v54, v59 op_sel_hi:[0,1]\n"
    "v_mfma_f32_16x16x32_f16 v[76:79], v[128:131], v[72:75], v[76:79]\n"
    "v_pk_mul_f16 v62, v56, v58 op_sel_hi:[0,1]\n"
    "v_mfma_f32_16x16x32_f16 v[80:83], v[132:135], v[72:75], v[80:83]\n"
    "v_pk_mul_f16 v63, v56, v59 op_sel_hi:[0,1]\n"
    "v_mfma_f32_16x16x32_f16 v[80:83], v[120:123], v[68:71], v[80:83]\n"
    "v_pk_mul_f16 v64, v56, v56 op_sel_hi:[0,1]\n"
    "v_mfma_f32_16x16x32_f16 v[84:87], v[124:127], v[68:71], v[84:87]\n"
    "v_pk_mul_f16 v65, v56, v49 op_sel_hi:[0,1]\n"
    "v_mfma_f32_16x16x32_f16 v[84:87], v[136:139], v[72:75], v[84:87]\n"
    "v_pk_mul_f16 v66, v48, v56 op_sel_hi:[0,1]\n"
    "v_permlane32_swap_b32 v7, v8\n"
    "v_permlane32_swap_b32 v9, v10\n"
    "v_smfmac_f32_16x16x64_f16 v[88:91], v[140:143], v[68:75], v2\n"
    "v_pk_mul_f16 v67, v48, v57 op_sel_hi:[0,1]\n"
    "v_add_f32 v7, v7, v8\n"
    "v_add_f32 v9, v9, v10\n"
    "v_mfma_f32_16x16x32_f16 v[76:79], v[144:147], v[60:63], v[76:79]\n"
    "v_pk_mul_f16 v68, v50, v56 op_sel_hi:[0,1]\n"
    "v_pk_mul_f16 v69, v50, v57 op_sel_hi:[0,1]\n"
    "v_mfma_f32_16x16x32_f16 v[76:79], v[156:159], v[64:67], v[76:79]\n"
    "v_pk_mul_f16 v70, v52, v56 op_sel_hi:[0,1]\n"
    "v_permlane16_swap_b32 v7, v9\n"
    "v_mfma_f32_16x16x32_f16 v[80:83], v[160:163], v[64:67], v[80:83]\n"
    "v_pk_mul_f16 v71, v52, v57 op_sel_hi:[0,1]\n"
    "v_add_f32 v7, v7, v9\n"
    "v_mfma_f32_16x16x32_f16 v[80:83], v[148:151], v[60:63], v[80:83]\n"
    "v_pk_mul_f16 v72, v54, v56 op_sel_hi:[0,1]\n"
    "ds_read_b32 v22, v25 offset:516\n"
    "v_mfma_f32_16x16x32_f16 v[84:87], v[152:155], v[60:63], v[84:87]\n"
    "v_pk_mul_f16 v73, v54, v57 op_sel_hi:[0,1]\n"
    "v_add_f32_dpp v7, v7, v7 quad_perm:[1,0,3,2] row_mask:0xf bank_mask:0xf\n"
    "v_mfma_f32_16x16x32_f16 v[84:87], v[164:167], v[64:67], v[84:87]\n"
    "v_pk_mul_f16 v74, v54, v54 op_sel_hi:[0,1]\n"
    "v_add_f32_dpp v7, v7, v7 quad_perm:[2,3,0,1] row_mask:0xf bank_mask:0xf\n"
    "v_smfmac_f32_16x16x64_f16 v[88:91], v[168:171], v[60:67], v2\n"
    "v_pk_mul_f16 v75, v54, v49 op_sel_hi:[0,1]\n"
    "v_add_f32_dpp v7, v7, v7 row_half_mirror row_mask:0xf bank_mask:0xf\n"
    "v_mfma_f32_16x16x32_f16 v[76:79], v[172:175], v[68:71], v[76:79]\n"
    "v_pk_mul_f16 v60, v48, v54 op_sel_hi:[0,1]\n"
    "v_pk_mul_f16 v61, v48, v55 op_sel_hi:[0,1]\n"
    "v_mfma_f32_16x16x32_f16 v[76:79], v[184:187], v[72:75], v[76:79]\n"
    "v_pk_mul_f16 v62, v50, v54 op_sel_hi:[0,1]\n"
    "v_add_f32_dpp v7, v7, v7 row_mirror row_mask:0xf bank_mask:0xf\n"
    "v_mfma_f32_16x16x32_f16 v[80:83], v[188:191], v[72:75], v[80:83]\n"
    "v_pk_mul_f16 v63, v50, v55 op_sel_hi:[0,1]\n"
    "s_waitcnt lgkmcnt(0)\n"
    "v_readfirstlane_b32 s45, v22\n"
    "s_cmp_lt_i32 s45, 1\n"
    "s_cbranch_scc1 .Lxd_ck30\n"
    ".Lxd_ck30r:\n"
    "v_mfma_f32_16x16x32_f16 v[80:83], v[176:179], v[68:71], v[80:83]\n"
    "v_pk_mul_f16 v64, v52, v54 op_sel_hi:[0,1]\n"
    "ds_read_b64_tr_b16 v[44:45], v5 offset:4480\n"
    "v_mfma_f32_16x16x32_f16 v[84:87], v[180:183], v[68:71], v[84:87]\n"
    "v_pk_mul_f16 v65, v52, v55 op_sel_hi:[0,1]\n"
    "ds_read_b64_tr_b16 v[46:47], v6 offset:4480\n"
    "v_mfma_f32_16x16x32_f16 v[84:87], v[192:195], v[72:75], v[84:87]\n"
    "v_pk_mul_f16 v66, v52, v52 op_sel_hi:[0,1]\n"
    "ds_read_b64_tr_b16 v[58:59], v4 offset:7680\n"
    "s_cbranch_vccnz .Lxd_pr29\n"
    "s_setprio 0\n"
    ".Lxd_pr29:\n"
    "v_smfmac_f32_16x16x64_f16 v[88:91], v[196:199], v[68:75], v2\n"
    "v_pk_mul_f16 v67, v52, v49 op_sel_hi:[0,1]\n"
    "ds_read_b64_tr_b16 v[56:57], v4 offset:7040\n"
    "v_mfma_f32_16x16x32_f16 v[76:79], v[200:203], v[60:63], v[76:79]\n"
    "v_pk_mul_f16 v68, v48, v52 op_sel_hi:[0,1]\n"
    "v_pk_mul_f16 v69, v48, v53 op_sel_hi:[0,1]\n"
    "v_mfma_f32_16x16x32_f16 v[76:79], v[212:215], v[64:67], v[76:79]\n"
    "v_pk_mul_f16 v70, v50, v52 op_sel_hi:[0,1]\n"
    "ds_read_b64_tr_b16 v[54:55], v4 offset:6400\n"
    "v_mfma_f32_16x16x32_f16 v[80:83], v[216:219], v[64:67], v[80:83]\n"
    "v_pk_mul_f16 v71, v50, v53 op_sel_hi:[0,1]\n"
    "ds_write_b32 v14, v7\n"
    "v_add_u32 v14, 64, v14\n"
    "v_mfma_f32_16x16x32_f16 v[80:83], v[204:207], v[60:63], v[80:83]\n"
    "v_pk_mul_f16 v72, v50, v50 op_sel_hi:[0,1]\n"
    "v_mfma_f32_16x16x32_f16 v[84:87], v[208:211], v[60:63], v[84:87]\n"
    "v_pk_mul_f16 v73, v50, v49 op_sel_hi:[0,1]\n"
    "v_pk_mul_f16 v74, v48, v50 op_sel_hi:[0,1]\n"
    "v_mfma_f32_16x16x32_f16 v[84:87], v[220:223], v[64:67], v[84:87]\n"
    "v_pk_mul_f16 v75, v48, v51 op_sel_hi:[0,1]\n"
    "v_smfmac_f32_16x16x64_f16 v[88:91], v[224:227], v[60:67], v2\n"
    "ds_read_b64_tr_b16 v[48:49], v3 offset:4480\n"
    "ds_read_b64_tr_b16 v[50:51], v4 offset:5120\n"
    "ds_read_b64_tr_b16 v[52:53], v4 offset:5760\n"
    "v_smfmac_f32_16x16x64_f16 v[88:91], v[252:255], v[68:75], v2\n"
    "s_waitcnt lgkmcnt(7)\n"
    "v_pk_mul_f16 v60, v44, s45 op_sel:[0,1]\n"
    "v_pk_mul_f16 v61, v45, s45 op_sel:[0,1]\n"
    "v_mfma_f32_16x16x32_f16 v[76:79], v[228:231], v[68:71], v[76:79]\n"
    "v_pk_mul_f16 v62, v46, s45 op_sel:[0,1]\n"
    "v_pk_mul_f16 v63, v47, s45 op_sel:[0,1]\n"
    "v_mfma_f32_16x16x32_f16 v[76:79], v[240:243], v[72:75], v[76:79]\n"
    "v_pk_mul_f16 v64, v44, v46\n"
    "v_pk_mul_f16 v65, v45, v47\n"
    "v_mfma_f32_16x16x32_f16 v[80:83], v[244:247], v[72:75], v[80:83]\n"
    "v_add_f32 v11, v88, v89\n"
    "v_add_f32 v13, v90, v91\n"
    "v_mfma_f32_16x16x32_f16 v[80:83], v[232:235], v[68:71], v[80:83]\n"
    "v_mov_b64 v[88:89], 0\n"
    "v_mov_b64 v[90:91], 0\n"
    "v_mfma_f32_16x16x32_f16 v[84:87], v[236:239], v[68:71], v[84:87]\n"
    "s_waitcnt lgkmcnt(0)\n"
    "v_pk_mul_f16 v66, v48, v48 op_sel_hi:[0,1]\n"
    "v_pk_mul_f16 v67, v48, v49 op_sel_hi:[0,1]\n"
    "v_mfma_f32_16x16x32_f16 v[84:87], v[248:251], v[72:75], v[84:87]\n"
    "v_mul_f32 v11, v11, v13\n"
    "s_cmp_eq_u32 s45, -1\n"
    "s_cbranch_scc1 .Lxd_ex0\n"
    ".Lxd_pl1:\n"
    "s_cbranch_vccnz .Lxd_pr31\n"
    "s_setprio 2\n"
    ".Lxd_pr31:\n"
    "v_smfmac_f32_16x16x64_f16 v[88:91], v[112:115], v[60:67], v2\n"
    "v_fmac_f32 v11, v76, v80\n"
    "v_fmac_f32 v11, v77, v81\n"
    "v_fmac_f32 v11, v78, v82\n"
    "v_fmac_f32 v11, v79, v83\n"
    "v_mfma_f32_16x16x32_f16 v[80:83], v[92:95], v[60:63], 0\n"
    "v_pk_mul_f16 v68, v58, v58 op_sel_hi:[0,1]\n"
    "v_pk_mul_f16 v69, v58, v49 op_sel_hi:[0,1]\n"
    "v_mfma_f32_16x16x32_f16 v[80:83], v[104:107], v[64:67], v[80:83]\n"
    "v_pk_mul_f16 v70, v48, v58 op_sel_hi:[0,1]\n"
    "v_pk_mul_f16 v71, v48, v59 op_sel_hi:[0,1]\n"
    "v_mfma_f32_16x16x32_f16 v[76:79], v[100:103], v[64:67], 0\n"
    "v_fmac_f32 v11, v84, v85\n"
    "v_fma_f32 v7, v86, v87, v11\n"
    "v_mfma_f32_16x16x32_f16 v[84:87], v[108:111], v[64:67], 0\n"
    "v_pk_mul_f16 v72, v50, v58 op_sel_hi:[0,1]\n"
    "v_pk_mul_f16 v73, v50, v59 op_sel_hi:[0,1]\n"
    "v_mfma_f32_16x16x32_f16 v[84:87], v[96:99], v[60:63], v[84:87]\n"
    "v_pk_mul_f16 v74, v52, v58 op_sel_hi:[0,1]\n"
    "v_pk_mul_f16 v75, v52, v59 op_sel_hi:[0,1]\n"
    "v_mfma_f32_16x16x32_f16 v[76:79], v[116:119], v[68:71], v[76:79]\n"
    "v_pk_mul_f16 v60, v54, v58 op_sel_hi:[0,1]\n"
    "v_pk_mul_f16 v61, v54, v59 op_sel_hi:[0,1]\n"
    "v_mfma_f32_16x16x32_f16 v[76:79], v[128:131], v[72:75], v[76:79]\n"
    "v_pk_mul_f16 v62, v56, v58 op_sel_hi:[0,1]\n"
    "v_mfma_f32_16x16x32_f16 v[80:83], v[132:135], v[72:75], v[80:83]\n"
    "v_pk_mul_f16 v63, v56, v59 op_sel_hi:[0,1]\n"
    "v_mfma_f32_16x16x32_f16 v[80:83], v[120:123], v[68:71], v[80:83]\n"
    "v_pk_mul_f16 v64, v56, v56 op_sel_hi:[0,1]\n"
    "v_mfma_f32_16x16x32_f16 v[84:87], v[124:127], v[68:71], v[84:87]\n"
    "v_pk_mul_f16 v65, v56, v49 op_sel_hi:[0,1]\n"
    "v_mfma_f32_16x16x32_f16 v[84:87], v[136:139], v[72:75], v[84:87]\n"
    "v_pk_mul_f16 v66, v48, v56 op_sel_hi:[0,1]\n"
    "v_smfmac_f32_16x16x64_f16 v[88:91], v[140:143], v[68:75], v2\n"
    "v_pk_mul_f16 v67, v48, v57 op_sel_hi:[0,1]\n"
    "v_mfma_f32_16x16x32_f16 v[76:79], v[144:147], v[60:63], v[76:79]\n"
    "v_pk_mul_f16 v68, v50, v56 op_sel_hi:[0,1]\n"
    "v_pk_mul_f16 v69, v50, v57 op_sel_hi:[0,1]\n"
    "v_mfma_f32_16x16x32_f16 v[76:79], v[156:159], v[64:67], v[76:79]\n"
    "v_pk_mul_f16 v70, v52, v56 op_sel_hi:[0,1]\n"
    "v_mfma_f32_16x16x32_f16 v[80:83], v[160:163], v[64:67], v[80:83]\n"
    "v_pk_mul_f16 v71, v52, v57 op_sel_hi:[0,1]\n"
    "v_mfma_f32_16x16x32_f16 v[80:83], v[148:151], v[60:63], v[80:83]\n"
    "v_pk_mul_f16 v72, v54, v56 op_sel_hi:[0,1]\n"
    "ds_read_b32 v22, v25 offset:520\n"
    "v_mfma_f32_16x16x32_f16 v[84:87], v[152:155], v[60:63], v[84:87]\n"
    "v_pk_mul_f16 v73, v54, v57 op_sel_hi:[0,1]\n"
    "v_mfma_f32_16x16x32_f16 v[84:87], v[164:167], v[64:67], v[84:87]\n"
    "v_pk_mul_f16 v74, v54, v54 op_sel_hi:[0,1]\n"
    "v_smfmac_f32_16x16x64_f16 v[88:91], v[168:171], v[60:67], v2\n"
    "v_pk_mul_f16 v75, v54, v49 op_sel_hi:[0,1]\n"
    "v_mfma_f32_16x16x32_f16 v[76:79], v[172:175], v[68:71], v[76:79]\n"
    "v_pk_mul_f16 v60, v48, v54 op_sel_hi:[0,1]\n"
    "v_pk_mul_f16 v61, v48, v55 op_sel_hi:[0,1]\n"
    "v_mfma_f32_16x16x32_f16 v[76:79], v[184:187], v[72:75], v[76:79]\n"
    "v_pk_mul_f16 v62, v50, v54 op_sel_hi:[0,1]\n"
    "v_mfma_f32_16x16x32_f16 v[80:83], v[188:191], v[72:75], v[80:83]\n"
    "v_pk_mul_f16 v63, v50, v55 op_sel_hi:[0,1]\n"
    "s_waitcnt lgkmcnt(0)\n"
    "v_readfirstlane_b32 s45, v22\n"
    "s_cmp_lt_i32 s45, 1\n"
    "s_cbranch_scc1 .Lxd_ck33\n"
    ".Lxd_ck33r:\n"
    "v_mfma_f32_16x16x32_f16 v[80:83], v[176:179], v[68:71], v[80:83]\n"
    "v_pk_mul_f16 v64, v52, v54 op_sel_hi:[0,1]\n"
    "ds_read_b64_tr_b16 v[44:45], v5 offset:8960\n"
    "v_mfma_f32_16x16x32_f16 v[84:87], v[180:183], v[68:71], v[84:87]\n"
    "v_pk_mul_f16 v65, v52, v55 op_sel_hi:[0,1]\n"
    "ds_read_b64_tr_b16 v[46:47], v6 offset:8960\n"
    "v_mfma_f32_16x16x32_f16 v[84:87], v[192:195], v[72:75], v[84:87]\n"
    "v_pk_mul_f16 v66, v52, v52 op_sel_hi:[0,1]\n"
    "ds_read_b64_tr_b16 v[58:59], v4 offset:12160\n"
    "s_cbranch_vccnz .Lxd_pr32\n"
    "s_setprio 0\n"
    ".Lxd_pr32:\n"
    "v_smfmac_f32_16x16x64_f16 v[88:91], v[196:199], v[68:75], v2\n"
    "v_pk_mul_f16 v67, v52, v49 op_sel_hi:[0,1]\n"
    "ds_read_b64_tr_b16 v[56:57], v4 offset:11520\n"
    "v_mfma_f32_16x16x32_f16 v[76:79], v[200:203], v[60:63], v[76:79]\n"
    "v_pk_mul_f16 v68, v48, v52 op_sel_hi:[0,1]\n"
    "v_pk_mul_f16 v69, v48, v53 op_sel_hi:[0,1]\n"
    "v_mfma_f32_16x16x32_f16 v[76:79], v[212:215], v[64:67], v[76:79]\n"
    "v_pk_mul_f16 v70, v50, v52 op_sel_hi:[0,1]\n"
    "ds_read_b64_tr_b16 v[54:55], v4 offset:10880\n"
    "v_mfma_f32_16x16x32_f16 v[80:83], v[216:219], v[64:67], v[80:83]\n"
    "v_pk_mul_f16 v71, v50, v53 op_sel_hi:[0,1]\n"
    "v_mfma_f32_16x16x32_f16 v[80:83], v[204:207], v[60:63], v[80:83]\n"
    "v_pk_mul_f16 v72, v50, v50 op_sel_hi:[0,1]\n"
    "v_mfma_f32_16x16x32_f16 v[84:87], v[208:211], v[60:63], v[84:87]\n"
    "v_pk_mul_f16 v73, v50, v49 op_sel_hi:[0,1]\n"
    "v_pk_mul_f16 v74, v48, v50 op_sel_hi:[0,1]\n"
    "v_mfma_f32_16x16x32_f16 v[84:87], v[220:223], v[64:67], v[84:87]\n"
    "v_pk_mul_f16 v75, v48, v51 op_sel_hi:[0,1]\n"
    "v_smfmac_f32_16x16x64_f16 v[88:91], v[224:227], v[60:67], v2\n"
    "ds_read_b64_tr_b16 v[48:49], v3 offset:8960\n"
    "ds_read_b64_tr_b16 v[50:51], v4 offset:9600\n"
    "ds_read_b64_tr_b16 v[52:53], v4 offset:10240\n"
    "v_smfmac_f32_16x16x64_f16 v[88:91], v[252:255], v[68:75], v2\n"
    "s_waitcnt lgkmcnt(6)\n"
    "v_pk_mul_f16 v60, v44, s45 op_sel:[0,1]\n"
    "v_pk_mul_f16 v61, v45, s45 op_sel:[0,1]\n"
    "v_mfma_f32_16x16x32_f16 v[76:79], v[228:231], v[68:71], v[76:79]\n"
    "v_pk_mul_f16 v62, v46, s45 op_sel:[0,1]\n"
    "v_pk_mul_f16 v63, v47, s45 op_sel:[0,1]\n"
    "v_mfma_f32_16x16x32_f16 v[76:79], v[240:243], v[72:75], v[76:79]\n"
    "v_pk_mul_f16 v64, v44, v46\n"
    "v_pk_mul_f16 v65, v45, v47\n"
    "v_mfma_f32_16x16x32_f16 v[80:83], v[244:247], v[72:75], v[80:83]\n"
    "v_add_f32 v11, v88, v89\n"
    "v_add_f32 v13, v90, v91\n"
    "v_mfma_f32_16x16x32_f16 v[80:83], v[232:235], v[68:71], v[80:83]\n"
    "v_mov_b64 v[88:89], 0\n"
    "v_mov_b64 v[90:91], 0\n"
    "v_mfma_f32_16x16x32_f16 v[84:87], v[236:239], v[68:71], v[84:87]\n"
    "s_waitcnt lgkmcnt(0)\n"
    "v_pk_mul_f16 v66, v48, v48 op_sel_hi:[0,1]\n"
    "v_pk_mul_f16 v67, v48, v49 op_sel_hi:[0,1]\n"
    "v_mfma_f32_16x16x32_f16 v[84:87], v[248:251], v[72:75], v[84:87]\n"
    "v_mul_f32 v11, v11, v13\n"
    "s_cmp_eq_u32 s45, -1\n"
    "s_cbranch_scc1 .Lxd_ex1\n"
    ".Lxd_pl2:\n"
    "s_cbranch_vccnz .Lxd_pr34\n"
    "s_setprio 2\n"
    ".Lxd_pr34:\n"
    "v_smfmac_f32_16x16x64_f16 v[88:91], v[112:115], v[60:67], v2\n"
    "v_fmac_f32 v11, v76, v80\n"
    "v_fmac_f32 v11, v77, v81\n"
    "v_fmac_f32 v11, v78, v82\n"
    "v_fmac_f32 v11, v79, v83\n"
    "v_mfma_f32_16x16x32_f16 v[80:83], v[92:95], v[60:63], 0\n"
    "v_pk_mul_f16 v68, v58, v58 op_sel_hi:[0,1]\n"
    "v_pk_mul_f16 v69, v58, v49 op_sel_hi:[0,1]\n"
    "v_mfma_f32_16x16x32_f16 v[80:83], v[104:107], v[64:67], v[80:83]\n"
    "v_pk_mul_f16 v70, v48, v58 op_sel_hi:[0,1]\n"
    "v_pk_mul_f16 v71, v48, v59 op_sel_hi:[0,1]\n"
    "v_mfma_f32_16x16x32_f16 v[76:79], v[100:103], v[64:67], 0\n"
    "v_fmac_f32 v11, v84, v85\n"
    "v_fma_f32 v8, v86, v87, v11\n"
    "v_mfma_f32_16x16x32_f16 v[84:87], v[108:111], v[64:67], 0\n"
    "v_pk_mul_f16 v72, v50, v58 op_sel_hi:[0,1]\n"
    "v_pk_mul_f16 v73, v50, v59 op_sel_hi:[0,1]\n"
    "v_mfma_f32_16x16x32_f16 v[84:87], v[96:99], v[60:63], v[84:87]\n"
    "v_pk_mul_f16 v74, v52, v58 op_sel_hi:[0,1]\n"
    "v_pk_mul_f16 v75, v52, v59 op_sel_hi:[0,1]\n"
    "v_mfma_f32_16x16x32_f16 v[76:79], v[116:119], v[68:71], v[76:79]\n"
    "v_pk_mul_f16 v60, v54, v58 op_sel_hi:[0,1]\n"
    "v_pk_mul_f16 v61, v54, v59 op_sel_hi:[0,1]\n"
    "v_mfma_f32_16x16x32_f16 v[76:79], v[128:131], v[72:75], v[76:79]\n"
    "v_pk_mul_f16 v62, v56, v58 op_sel_hi:[0,1]\n"
    "v_mfma_f32_16x16x32_f16 v[80:83], v[132:135], v[72:75], v[80:83]\n"
    "v_pk_mul_f16 v63, v56, v59 op_sel_hi:[0,1]\n"
    "v_mfma_f32_16x16x32_f16 v[80:83], v[120:123], v[68:71], v[80:83]\n"
    "v_pk_mul_f16 v64, v56, v56 op_sel_hi:[0,1]\n"
    "v_mfma_f32_16x16x32_f16 v[84:87], v[124:127], v[68:71], v[84:87]\n"
    "v_pk_mul_f16 v65, v56, v49 op_sel_hi:[0,1]\n"
    "v_mfma_f32_16x16x32_f16 v[84:87], v[136:139], v[72:75], v[84:87]\n"
    "v_pk_mul_f16 v66, v48, v56 op_sel_hi:[0,1]\n"
    "v_smfmac_f32_16x16x64_f16 v[88:91], v[140:143], v[68:75], v2\n"
    "v_pk_mul_f16 v67, v48, v57 op_sel_hi:[0,1]\n"
    "v_mfma_f32_16x16x32_f16 v[76:79], v[144:147], v[60:63], v[76:79]\n"
    "v_pk_mul_f16 v68, v50, v56 op_sel_hi:[0,1]\n"
    "v_pk_mul_f16 v69, v50, v57 op_sel_hi:[0,1]\n"
    "v_mfma_f32_16x16x32_f16 v[76:79], v[156:159], v[64:67], v[76:79]\n"
    "v_pk_mul_f16 v70, v52, v56 op_sel_hi:[0,1]\n"
    "v_mfma_f32_16x16x32_f16 v[80:83], v[160:163], v[64:67], v[80:83]\n"
    "v_pk_mul_f16 v71, v52, v57 op_sel_hi:[0,1]\n"
    "v_mfma_f32_16x16x32_f16 v[80:83], v[148:151], v[60:63], v[80:83]\n"
    "v_pk_mul_f16 v72, v54, v56 op_sel_hi:[0,1]\n"
    "ds_read_b32 v22, v25 offset:524\n"
    "v_mfma_f32_16x16x32_f16 v[84:87], v[152:155], v[60:63], v[84:87]\n"
    "v_pk_mul_f16 v73, v54, v57 op_sel_hi:[0,1]\n"
    "v_mfma_f32_16x16x32_f16 v[84:87], v[164:167], v[64:67], v[84:87]\n"
    "v_pk_mul_f16 v74, v54, v54 op_sel_hi:[0,1]\n"
    "v_smfmac_f32_16x16x64_f16 v[88:91], v[168:171], v[60:67], v2\n"
    "v_pk_mul_f16 v75, v54, v49 op_sel_hi:[0,1]\n"
    "v_mfma_f32_16x16x32_f16 v[76:79], v[172:175], v[68:71], v[76:79]\n"
    "v_pk_mul_f16 v60, v48, v54 op_sel_hi:[0,1]\n"
    "v_pk_mul_f16 v61, v48, v55 op_sel_hi:[0,1]\n"
    "v_mfma_f32_16x16x32_f16 v[76:79], v[184:187], v[72:75], v[76:79]\n"
    "v_pk_mul_f16 v62, v50, v54 op_sel_hi:[0,1]\n"
    "v_mfma_f32_16x16x32_f16 v[80:83], v[188:191], v[72:75], v[80:83]\n"
    "v_pk_mul_f16 v63, v50, v55 op_sel_hi:[0,1]\n"
    "s_waitcnt lgkmcnt(0)\n"
    "v_readfirstlane_b32 s45, v22\n"
    "s_cmp_lt_i32 s45, 1\n"
    "s_cbranch_scc1 .Lxd_ck36\n"
    ".Lxd_ck36r:\n"
    "v_mfma_f32_16x16x32_f16 v[80:83], v[176:179], v[68:71], v[80:83]\n"
    "v_pk_mul_f16 v64, v52, v54 op_sel_hi:[0,1]\n"
    "ds_read_b64_tr_b16 v[44:45], v5 offset:13440\n"
    "v_mfma_f32_16x16x32_f16 v[84:87], v[180:183], v[68:71], v[84:87]\n"
    "v_pk_mul_f16 v65, v52, v55 op_sel_hi:[0,1]\n"
    "ds_read_b64_tr_b16 v[46:47], v6 offset:13440\n"
    "v_mfma_f32_16x16x32_f16 v[84:87], v[192:195], v[72:75], v[84:87]\n"
    "v_pk_mul_f16 v66, v52, v52 op_sel_hi:[0,1]\n"
    "ds_read_b64_tr_b16 v[58:59], v4 offset:16640\n"
    "s_cbranch_vccnz .Lxd_pr35\n"
    "s_setprio 0\n"
    ".Lxd_pr35:\n"
    "v_smfmac_f32_16x16x64_f16 v[88:91], v[196:199], v[68:75], v2\n"
    "v_pk_mul_f16 v67, v52, v49 op_sel_hi:[0,1]\n"
    "ds_read_b64_tr_b16 v[56:57], v4 offset:16000\n"
    "v_mfma_f32_16x16x32_f16 v[76:79], v[200:203], v[60:63], v[76:79]\n"
    "v_pk_mul_f16 v68, v48, v52 op_sel_hi:[0,1]\n"
    "v_pk_mul_f16 v69, v48, v53 op_sel_hi:[0,1]\n"
    "v_mfma_f32_16x16x32_f16 v[76:79], v[212:215], v[64:67], v[76:79]\n"
    "v_pk_mul_f16 v70, v50, v52 op_sel_hi:[0,1]\n"
    "ds_read_b64_tr_b16 v[54:55], v4 offset:15360\n"
    "v_mfma_f32_16x16x32_f16 v[80:83], v[216:219], v[64:67], v[80:83]\n"
    "v_pk_mul_f16 v71, v50, v53 op_sel_hi:[0,1]\n"
    "v_mfma_f32_16x16x32_f16 v[80:83], v[204:207], v[60:63], v[80:83]\n"
    "v_pk_mul_f16 v72, v50, v50 op_sel_hi:[0,1]\n"
    "v_mfma_f32_16x16x32_f16 v[84:87], v[208:211], v[60:63], v[84:87]\n"
    "v_pk_mul_f16 v73, v50, v49 op_sel_hi:[0,1]\n"
    "v_pk_mul_f16 v74, v48, v50 op_sel_hi:[0,1]\n"
    "v_mfma_f32_16x16x32_f16 v[84:87], v[220:223], v[64:67], v[84:87]\n"
    "v_pk_mul_f16 v75, v48, v51 op_sel_hi:[0,1]\n"
    "v_smfmac_f32_16x16x64_f16 v[88:91], v[224:227], v[60:67], v2\n"
    "ds_read_b64_tr_b16 v[48:49], v3 offset:13440\n"
    "ds_read_b64_tr_b16 v[50:51], v4 offset:14080\n"
    "ds_read_b64_tr_b16 v[52:53], v4 offset:14720\n"
    "v_smfmac_f32_16x16x64_f16 v[88:91], v[252:255], v[68:75], v2\n"
    "s_waitcnt lgkmcnt(6)\n"
    "v_pk_mul_f16 v60, v44, s45 op_sel:[0,1]\n"
    "v_pk_mul_f16 v61, v45, s45 op_sel:[0,1]\n"
    "v_mfma_f32_16x16x32_f16 v[76:79], v[228:231], v[68:71], v[76:79]\n"
    "v_pk_mul_f16 v62, v46, s45 op_sel:[0,1]\n"
    "v_pk_mul_f16 v63, v47, s45 op_sel:[0,1]\n"
    "v_mfma_f32_16x16x32_f16 v[76:79], v[240:243], v[72:75], v[76:79]\n"
    "v_pk_mul_f16 v64, v44, v46\n"
    "v_pk_mul_f16 v65, v45, v47\n"
    "v_mfma_f32_16x16x32_f16 v[80:83], v[244:247], v[72:75], v[80:83]\n"
    "v_add_f32 v11, v88, v89\n"
    "v_add_f32 v13, v90, v91\n"
    "v_mfma_f32_16x16x32_f16 v[80:83], v[232:235], v[68:71], v[80:83]\n"
    "v_mov_b64 v[88:89], 0\n"
    "v_mov_b64 v[90:91], 0\n"
    "v_mfma_f32_16x16x32_f16 v[84:87], v[236:239], v[68:71], v[84:87]\n"
    "s_waitcnt lgkmcnt(0)\n"
    "v_pk_mul_f16 v66, v48, v48 op_sel_hi:[0,1]\n"
    "v_pk_mul_f16 v67, v48, v49 op_sel_hi:[0,1]\n"
    "v_mfma_f32_16x16x32_f16 v[84:87], v[248:251], v[72:75], v[84:87]\n"
    "v_mul_f32 v11, v11, v13\n"
    "s_cmp_eq_u32 s45, -1\n"
    "s_cbranch_scc1 .Lxd_ex2\n"
    ".Lxd_pl3:\n"
    "s_cbranch_vccnz .Lxd_pr37\n"
    "s_setprio 2\n"
    ".Lxd_pr37:\n"
    "v_smfmac_f32_16x16x64_f16 v[88:91], v[112:115], v[60:67], v2\n"
    "v_fmac_f32 v11, v76, v80\n"
    "v_fmac_f32 v11, v77, v81\n"
    "v_fmac_f32 v11, v78, v82\n"
    "v_fmac_f32 v11, v79, v83\n"
    "v_mfma_f32_16x16x32_f16 v[80:83], v[92:95], v[60:63], 0\n"
    "v_pk_mul_f16 v68, v58, v58 op_sel_hi:[0,1]\n"
    "v_pk_mul_f16 v69, v58, v49 op_sel_hi:[0,1]\n"
    "v_mfma_f32_16x16x32_f16 v[80:83], v[104:107], v[64:67], v[80:83]\n"
    "v_pk_mul_f16 v70, v48, v58 op_sel_hi:[0,1]\n"
    "v_pk_mul_f16 v71, v48, v59 op_sel_hi:[0,1]\n"
    "v_mfma_f32_16x16x32_f16 v[76:79], v[100:103], v[64:67], 0\n"
    "v_fmac_f32 v11, v84, v85\n"
    "v_fma_f32 v9, v86, v87, v11\n"
    "v_mfma_f32_16x16x32_f16 v[84:87], v[108:111], v[64:67], 0\n"
    "v_pk_mul_f16 v72, v50, v58 op_sel_hi:[0,1]\n"
    "v_pk_mul_f16 v73, v50, v59 op_sel_hi:[0,1]\n"
    "v_mfma_f32_16x16x32_f16 v[84:87], v[96:99], v[60:63], v[84:87]\n"
    "v_pk_mul_f16 v74, v52, v58 op_sel_hi:[0,1]\n"
    "v_pk_mul_f16 v75, v52, v59 op_sel_hi:[0,1]\n"
    "v_mfma_f32_16x16x32_f16 v[76:79], v[116:119], v[68:71], v[76:79]\n"
    "v_pk_mul_f16 v60, v54, v58 op_sel_hi:[0,1]\n"
    "v_pk_mul_f16 v61, v54, v59 op_sel_hi:[0,1]\n"
    "v_mfma_f32_16x16x32_f16 v[76:79], v[128:131], v[72:75], v[76:79]\n"
    "v_pk_mul_f16 v62, v56, v58 op_sel_hi:[0,1]\n"
    "v_add_u32 v3, s46, v3\n"
    "v_mfma_f32_16x16x32_f16 v[80:83], v[132:135], v[72:75], v[80:83]\n"
    "v_pk_mul_f16 v63, v56, v59 op_sel_hi:[0,1]\n"
    "v_add_u32 v4, s46, v4\n"
    "v_mfma_f32_16x16x32_f16 v[80:83], v[120:123], v[68:71], v[80:83]\n"
    "v_pk_mul_f16 v64, v56, v56 op_sel_hi:[0,1]\n"
    "v_add_u32 v5, s46, v5\n"
    "v_mfma_f32_16x16x32_f16 v[84:87], v[124:127], v[68:71], v[84:87]\n"
    "v_pk_mul_f16 v65, v56, v49 op_sel_hi:[0,1]\n"
    "v_add_u32 v6, s46, v6\n"
    "s_sub_u32 s46, 0, s46\n"
    "v_mfma_f32_16x16x32_f16 v[84:87], v[136:139], v[72:75], v[84:87]\n"
    "v_pk_mul_f16 v66, v48, v56 op_sel_hi:[0,1]\n"
    "v_smfmac_f32_16x16x64_f16 v[88:91], v[140:143], v[68:75], v2\n"
    "v_pk_mul_f16 v67, v48, v57 op_sel_hi:[0,1]\n"
    "v_mfma_f32_16x16x32_f16 v[76:79], v[144:147], v[60:63], v[76:79]\n"
    "v_pk_mul_f16 v68, v50, v56 op_sel_hi:[0,1]\n"
    "v_pk_mul_f16 v69, v50, v57 op_sel_hi:[0,1]\n"
    "v_mfma_f32_16x16x32_f16 v[76:79], v[156:159], v[64:67], v[76:79]\n"
    "v_pk_mul_f16 v70, v52, v56 op_sel_hi:[0,1]\n"
    "v_mfma_f32_16x16x32_f16 v[80:83], v[160:163], v[64:67], v[80:83]\n"
    "v_pk_mul_f16 v71, v52, v57 op_sel_hi:[0,1]\n"
    "v_mfma_f32_16x16x32_f16 v[80:83], v[148:151], v[60:63], v[80:83]\n"
    "v_pk_mul_f16 v72, v54, v56 op_sel_hi:[0,1]\n"
    "ds_read_b32 v22, v25 offset:528\n"
    "v_mfma_f32_16x16x32_f16 v[84:87], v[152:155], v[60:63], v[84:87]\n"
    "v_pk_mul_f16 v73, v54, v57 op_sel_hi:[0,1]\n"
    "v_mfma_f32_16x16x32_f16 v[84:87], v[164:167], v[64:67], v[84:87]\n"
    "v_pk_mul_f16 v74, v54, v54 op_sel_hi:[0,1]\n"
    "v_smfmac_f32_16x16x64_f16 v[88:91], v[168:171], v[60:67], v2\n"
    "v_pk_mul_f16 v75, v54, v49 op_sel_hi:[0,1]\n"
    "v_mfma_f32_16x16x32_f16 v[76:79], v[172:175], v[68:71], v[76:79]\n"
    "v_pk_mul_f16 v60, v48, v54 op_sel_hi:[0,1]\n"
    "v_pk_mul_f16 v61, v48, v55 op_sel_hi:[0,1]\n"
    "v_mfma_f32_16x16x32_f16 v[76:79], v[184:187], v[72:75], v[76:79]\n"
    "v_pk_mul_f16 v62, v50, v54 op_sel_hi:[0,1]\n"
    "v_mfma_f32_16x16x32_f16 v[80:83], v[188:191], v[72:75], v[80:83]\n"
    "v_pk_mul_f16 v63, v50, v55 op_sel_hi:[0,1]\n"
    "s_waitcnt lgkmcnt(0)\n"
    "v_readfirstlane_b32 s45, v22\n"
    "s_cmp_lt_i32 s45, 1\n"
    "s_cbranch_scc1 .Lxd_ck39\n"
    ".Lxd_ck39r:\n"
    "v_mfma_f32_16x16x32_f16 v[80:83], v[176:179], v[68:71], v[80:83]\n"
    "v_pk_mul_f16 v64, v52, v54 op_sel_hi:[0,1]\n"
    "ds_read_b64_tr_b16 v[44:45], v5 offset:0\n"
    "v_mfma_f32_16x16x32_f16 v[84:87], v[180:183], v[68:71], v[84:87]\n"
    "v_pk_mul_f16 v65, v52, v55 op_sel_hi:[0,1]\n"
    "ds_read_b64_tr_b16 v[46:47], v6 offset:0\n"
    "v_mfma_f32_16x16x32_f16 v[84:87], v[192:195], v[72:75], v[84:87]\n"
    "v_pk_mul_f16 v66, v52, v52 op_sel_hi:[0,1]\n"
    "ds_read_b64_tr_b16 v[58:59], v4 offset:3200\n"
    "s_cbranch_vccnz .Lxd_pr38\n"
    "s_setprio 0\n"
    ".Lxd_pr38:\n"
    "v_smfmac_f32_16x16x64_f16 v[88:91], v[196:199], v[68:75], v2\n"
    "v_pk_mul_f16 v67, v52, v49 op_sel_hi:[0,1]\n"
    "ds_read_b64_tr_b16 v[56:57], v4 offset:2560\n"
    "v_mfma_f32_16x16x32_f16 v[76:79], v[200:203], v[60:63], v[76:79]\n"
    "v_pk_mul_f16 v68, v48, v52 op_sel_hi:[0,1]\n"
    "v_pk_mul_f16 v69, v48, v53 op_sel_hi:[0,1]\n"
    "v_mfma_f32_16x16x32_f16 v[76:79], v[212:215], v[64:67], v[76:79]\n"
    "v_pk_mul_f16 v70, v50, v52 op_sel_hi:[0,1]\n"
    "ds_read_b64_tr_b16 v[54:55], v4 offset:1920\n"
    "v_mfma_f32_16x16x32_f16 v[80:83], v[216:219], v[64:67], v[80:83]\n"
    "v_pk_mul_f16 v71, v50, v53 op_sel_hi:[0,1]\n"
    "v_mfma_f32_16x16x32_f16 v[80:83], v[204:207], v[60:63], v[80:83]\n"
    "v_pk_mul_f16 v72, v50, v50 op_sel_hi:[0,1]\n"
    "v_mfma_f32_16x16x32_f16 v[84:87], v[208:211], v[60:63], v[84:87]\n"
    "v_pk_mul_f16 v73, v50, v49 op_sel_hi:[0,1]\n"
    "v_pk_mul_f16 v74, v48, v50 op_sel_hi:[0,1]\n"
    "v_mfma_f32_16x16x32_f16 v[84:87], v[220:223], v[64:67], v[84:87]\n"
    "v_pk_mul_f16 v75, v48, v51 op_sel_hi:[0,1]\n"
    "v_smfmac_f32_16x16x64_f16 v[88:91], v[224:227], v[60:67], v2\n"
    "ds_read_b64_tr_b16 v[48:49], v3 offset:0\n"
    "ds_read_b64_tr_b16 v[50:51], v4 offset:640\n"
    "ds_read_b64_tr_b16 v[52:53], v4 offset:1280\n"
    "v_smfmac_f32_16x16x64_f16 v[88:91], v[252:255], v[68:75], v2\n"
    "s_waitcnt lgkmcnt(6)\n"
    "v_pk_mul_f16 v60, v44, s45 op_sel:[0,1]\n"
    "v_pk_mul_f16 v61, v45, s45 op_sel:[0,1]\n"
    "v_mfma_f32_16x16x32_f16 v[76:79], v[228:231], v[68:71], v[76:79]\n"
    "v_pk_mul_f16 v62, v46, s45 op_sel:[0,1]\n"
    "v_pk_mul_f16 v63, v47, s45 op_sel:[0,1]\n"
    "v_mfma_f32_16x16x32_f16 v[76:79], v[240:243], v[72:75], v[76:79]\n"
    "v_pk_mul_f16 v64, v44, v46\n"
    "v_pk_mul_f16 v65, v45, v47\n"
    "v_mfma_f32_16x16x32_f16 v[80:83], v[244:247], v[72:75], v[80:83]\n"
    "v_add_f32 v11, v88, v89\n"
    "v_add_f32 v13, v90, v91\n"
    "v_mfma_f32_16x16x32_f16 v[80:83], v[232:235], v[68:71], v[80:83]\n"
    "v_mov_b64 v[88:89], 0\n"
    "v_mov_b64 v[90:91], 0\n"
    "v_mfma_f32_16x16x32_f16 v[84:87], v[236:239], v[68:71], v[84:87]\n"
    "s_waitcnt lgkmcnt(0)\n"
    "v_pk_mul_f16 v66, v48, v48 op_sel_hi:[0,1]\n"
    "v_pk_mul_f16 v67, v48, v49 op_sel_hi:[0,1]\n"
    "v_mfma_f32_16x16x32_f16 v[84:87], v[248:251], v[72:75], v[84:87]\n"
    "v_mul_f32 v11, v11, v13\n"
    "v_add_u32 v25, 16, v25\n"
    "s_cmp_eq_u32 s45, -1\n"
    "s_cbranch_scc1 .Lxd_ex3\n"
    "s_cmp_lg_u32 s36, 0\n"
    "s_cbranch_scc1 .Lxd_pl0\n"
    "s_branch .Lxd_ph0\n"
    ".Lxd_ex3:\n"
    "v_fmac_f32 v11, v76, v80\n"
    "v_fmac_f32 v11, v77, v81\n"
    "v_fmac_f32 v11, v78, v82\n"
    "v_fmac_f32 v11, v79, v83\n"
    "s_nop 0\n"
    "v_fmac_f32 v11, v84, v85\n"
    "v_fma_f32 v10, v86, v87, v11\n"
    "s_branch .Lxd_fin\n"
    ".Lxd_ex2:\n"
    "v_fmac_f32 v11, v76, v80\n"
    "v_fmac_f32 v11, v77, v81\n"
    "v_fmac_f32 v11, v78, v82\n"
    "v_fmac_f32 v11, v79, v83\n"
    "s_nop 0\n"
    "v_fmac_f32 v11, v84, v85\n"
    "v_fma_f32 v9, v86, v87, v11\n"
    "s_branch .Lxd_fin\n"
    ".Lxd_ex1:\n"
    "v_fmac_f32 v11, v76, v80\n"
    "v_fmac_f32 v11, v77, v81\n"
    "v_fmac_f32 v11, v78, v82\n"
    "v_fmac_f32 v11, v79, v83\n"
    "s_nop 0\n"
    "v_fmac_f32 v11, v84, v85\n"
    "v_fma_f32 v8, v86, v87, v11\n"
    "s_branch .Lxd_fin\n"
    ".Lxd_ex0:\n"
    "v_fmac_f32 v11, v76, v80\n"
    "v_fmac_f32 v11, v77, v81\n"
    "v_fmac_f32 v11, v78, v82\n"
    "v_fmac_f32 v11, v79, v83\n"
    "s_nop 0\n"
    "v_fmac_f32 v11, v84, v85\n"
    "v_fma_f32 v7, v86, v87, v11\n"
    ".Lxd_fin:\n"
    "s_nop 1\n"
    "v_permlane32_swap_b32 v7, v8\n"
    "v_permlane32_swap_b32 v9, v10\n"
    "v_add_f32 v7, v7, v8\n"
    "v_add_f32 v9, v9, v10\n"
    "s_nop 1\n"
    "v_permlane16_swap_b32 v7, v9\n"
    "v_add_f32 v7, v7, v9\n"
    "s_nop 1\n"
    "v_add_f32_dpp v7, v7, v7 quad_perm:[1,0,3,2] row_mask:0xf bank_mask:0xf\n"
    "s_nop 1\n"
    "v_add_f32_dpp v7, v7, v7 quad_perm:[2,3,0,1] row_mask:0xf bank_mask:0xf\n"
    "s_nop 1\n"
    "v_add_f32_dpp v7, v7, v7 row_half_mirror row_mask:0xf bank_mask:0xf\n"
    "s_nop 1\n"
    "v_add_f32_dpp v7, v7, v7 row_mirror row_mask:0xf bank_mask:0xf\n"
    "ds_write_b32 v14, v7\n"
    "v_add_u32 v14, 64, v14\n"
    "s_cmp_lg_u32 s56, 2\n"
    "s_cbranch_scc1 .Lxd_f2\n"
    ".Lxd_f3:\n"
    "v_lshl_add_u32 v12, v18, 6, v20\n"
    "v_lshlrev_b32 v13, 2, v12\n"
    "v_add_u32 v12, 150528, v12\n"
    "v_add_u32 v13, 152128, v13\n"
    "v_mov_b32 v21, 140944\n"
    "s_lshl_b32 s26, s26, 16\n"
    "s_waitcnt vmcnt(0) lgkmcnt(0)\n"
    "s_barrier\n"
    "s_cmp_lg_u32 s20, 0\n"
    "s_cbranch_scc1 .Lxd_done\n"
    "ds_read_b128 v[28:31], v13\n"
    "ds_read_b32 v32, v12\n"
    "ds_read_u16 v33, v12 offset:512\n"
    "ds_read_b32 v34, v12 offset:1024\n"
    "ds_read_b32 v35, v21\n"
    "s_waitcnt lgkmcnt(0)\n"
    "v_add_f32 v28, v28, v29\n"
    "v_add_f32 v30, v30, v31\n"
    "v_cmp_lt_i32 vcc, 0, v32\n"
    "v_add_f32 v28, v28, v30\n"
    "v_lshl_add_u32 v33, v33, 2, v35\n"
    "v_add_f32 v34, s26, v34\n"
    "v_ldexp_f32 v28, v28, v33\n"
    "v_add_f32 v28, v28, v34\n"
    "v_cvt_pk_bf16_f32 v28, v28, v28\n"
    "s_mov_b64 exec, vcc\n"
    "global_store_short v32, v28, s[18:19] offset:-2\n"
    "s_mov_b64 exec, -1\n"
    ".Lxd_done:\n"
    "s_endpgm\n"
    ".Lxd_cin:\n"
    "s_load_dwordx4 s[28:31], s[0:1], 0x40\n"
    "v_lshlrev_b32 v42, 4, v1\n"
    "s_mul_i32 s40, s20, 4480\n"
    "s_min_u32 s40, s40, 30672\n"
    "s_mov_b32 m0, s40\n"
    "s_add_u32 s40, s40, 70304\n"
    "s_mul_i32 s41, s20, 4056\n"
    "s_and_b32 s41, s41, -16\n"
    "s_mul_i32 s35, s20, 6144\n"
    "s_add_u32 s35, s35, 98304\n"
    "s_mul_i32 s47, s2, 86\n"
    "s_lshr_b32 s47, s47, 8\n"
    "s_mul_i32 s3, s47, 3\n"
    "s_sub_u32 s3, s2, s3\n"
    "s_and_b32 s24, s20, 5\n"
    "s_or_b32 s66, s20, s2\n"
    "s_waitcnt lgkmcnt(0)\n"
    "s_add_u32 s38, s28, s40\n"
    "s_addc_u32 s39, s29, 0\n"
    "global_load_lds_dwordx4 v42, s[38:39] offset:0\n"
    "global_load_lds_dwordx4 v42, s[38:39] offset:1024\n"
    "global_load_lds_dwordx4 v42, s[38:39] offset:2048\n"
    "global_load_lds_dwordx4 v42, s[38:39] offset:3072\n"
    "global_load_lds_dwordx4 v42, s[38:39] offset:3456\n"
    "s_cmp_eq_u32 s20, 3\n"
    "s_cbranch_scc1 .Lxd_on40\n"
    ".Lxd_on40r:\n"
    "s_cmp_eq_u32 s20, 5\n"
    "s_cbranch_scc1 .Lxd_on41\n"
    ".Lxd_on41r:\n"
    "s_cmp_eq_u32 s66, 0\n"
    "s_cbranch_scc1 .Lxd_on42\n"
    ".Lxd_on42r:\n"
    "s_cmp_eq_u32 s24, 0\n"
    "s_cbranch_scc1 .Lxd_on43\n"
    ".Lxd_on43r:\n"
    "s_add_u32 s38, s28, s41\n"
    "s_addc_u32 s39, s29, 0\n"
    "s_mov_b32 m0, s35\n"
    "s_load_dword s26, s[16:17], 0\n"
    "global_load_lds_dwordx4 v42, s[38:39] offset:0\n"
    "global_load_lds_dwordx4 v42, s[38:39] offset:1024\n"
    "global_load_lds_dwordx4 v42, s[38:39] offset:2048\n"
    "global_load_lds_dwordx4 v42, s[38:39] offset:3072\n"
    "s_setprio 1\n"
    "s_lshr_b32 s21, s20, 2\n"
    "s_lshl1_add_u32 s22, s21, s20\n"
    "s_and_b32 s22, s22, 3\n"
    "s_lshl_b32 s58, s21, 8\n"
    "s_add_u32 s58, s58, 150528\n"
    "s_lshl_b32 s54, s47, 9\n"
    "s_add_u32 s54, s54, 2\n"
    "s_lshl_b32 s40, s47, 7\n"
    "s_add_u32 s40, s40, 32768\n"
    "s_add_u32 s52, s10, s40\n"
    "s_addc_u32 s53, s11, 0\n"
    "s_mov_b32 s57, 0x7fff7fff\n"
    "s_mov_b32 s56, 0\n"
    "s_mov_b32 s27, 0\n"
    "s_mov_b32 s55, 9\n"
    "s_mov_b32 s44, 0\n"
    "s_movk_i32 s46, 17920\n"
    "v_lshrrev_b32 v17, 3, v1\n"
    "v_mul_u32_u24 v17, 12, v17\n"
    "v_and_b32 v19, 7, v1\n"
    "v_lshlrev_b32 v19, 4, v19\n"
    "v_lshrrev_b32 v18, 5, v1\n"
    "v_lshlrev_b32 v18, 2, v18\n"
    "v_and_b32 v20, 31, v1\n"
    "v_lshlrev_b32 v20, 2, v20\n"
    "s_lshl2_add_u32 s40, s22, s58\n"
    "s_sub_u32 s40, s40, 20\n"
    "v_mov_b32 v25, s40\n"
    "s_lshl1_add_u32 s23, s3, s21\n"
    "s_mul_i32 s23, s23, 6\n"
    "s_add_u32 s23, s23, s22\n"
    "s_add_u32 s23, s23, 78\n"
    "v_mov_b32 v24, s23\n"
    "v_lshrrev_b32 v96, 2, v1\n"
    "v_readfirstlane_b32 s40, v24\n"
    "s_or_b32 s40, s40, s44\n"
    "s_lshl1_add_u32 s41, s40, s54\n"
    "s_cmp_lt_u32 s40, 256\n"
    "s_cselect_b32 s41, s41, 2\n"
    "s_cselect_b64 s[48:49], -1, 0\n"
    "v_mov_b32 v12, s41\n"
    "s_mul_i32 s41, s41, 52\n"
    "s_add_u32 s38, s4, s41\n"
    "s_addc_u32 s39, s5, 0\n"
    "global_load_dwordx3 v[36:38], v17, s[38:39] offset:-104\n"
    "global_load_dword v41, v18, s[38:39] offset:-8\n"
    "v_mul_u32_u24 v96, 52, v96\n"
    "v_and_b32 v97, 48, v42\n"
    "v_add_u32 v96, v96, v97\n"
    "s_mul_i32 s40, s20, 832\n"
    "s_add_u32 s40, s40, 35152\n"
    "s_add_u32 s38, s28, s40\n"
    "s_addc_u32 s39, s29, 0\n"
    "s_lshl_b32 s34, s20, 10\n"
    "s_setprio 0\n"
    "s_add_u32 m0, s34, 49152\n"
    "s_nop 0\n"
    "global_load_lds_dwordx4 v96, s[38:39]\n"
    "v_add_u32 v96, 6656, v96\n"
    "v_mov_b32 v98, 0\n"
    "v_mov_b32 v99, 0\n"
    "v_lshlrev_b32 v13, 3, v0\n"
    "v_lshlrev_b32 v21, 2, v0\n"
    "v_add_u32 v21, 149504, v21\n"
    "ds_write_b64 v13, v[98:99] offset:35840\n"
    "ds_write_b32 v21, v98\n"
    "v_lshrrev_b32 v92, 5, v1\n"
    "v_lshrrev_b32 v90, 4, v1\n"
    "v_and_b32 v91, 15, v1\n"
    "s_add_u32 m0, s34, 57344\n"
    "s_nop 0\n"
    "global_load_lds_dwordx4 v96, s[38:39]\n"
    "v_add_u32 v96, 6656, v96\n"
    "v_lshrrev_b32 v13, 2, v91\n"
    "v_lshl_add_u32 v93, v90, 3, v13\n"
    "v_add_u32 v94, 4, v93\n"
    "v_and_b32 v22, 3, v1\n"
    "s_lshl_b32 s40, s20, 5\n"
    "v_lshl_add_u32 v21, v22, 3, s40\n"
    "s_movk_i32 s41, 1352\n"
    "v_min_u32 v93, 25, v93\n"
    "v_mad_u32_u24 v93, v93, s41, v21\n"
    "v_min_u32 v94, 25, v94\n"
    "s_add_u32 m0, s34, 65536\n"
    "s_nop 0\n"
    "global_load_lds_dwordx4 v96, s[38:39]\n"
    "v_add_u32 v96, 6656, v96\n"
    "v_mad_u32_u24 v94, v94, s41, v21\n"
    "s_lshl_b32 s40, s20, 4\n"
    "v_add_u32 v21, s40, v91\n"
    "v_mul_u32_u24 v60, 2521, v21\n"
    "v_lshrrev_b32 v60, 16, v60\n"
    "v_mad_u32_u24 v60, v60, 6, v21\n"
    "v_lshlrev_b32 v60, 1, v60\n"
    "v_add_u32 v21, 128, v21\n"
    "v_mul_u32_u24 v61, 2521, v21\n"
    "v_lshrrev_b32 v61, 16, v61\n"
    "s_add_u32 m0, s34, 73728\n"
    "s_nop 0\n"
    "global_load_lds_dwordx4 v96, s[38:39]\n"
    "v_add_u32 v96, 6656, v96\n"
    "v_mad_u32_u24 v61, v61, 6, v21\n"
    "v_lshlrev_b32 v61, 1, v61\n"
    "v_add_u32 v21, 128, v21\n"
    "v_mul_u32_u24 v62, 2521, v21\n"
    "v_lshrrev_b32 v62, 16, v62\n"
    "v_mad_u32_u24 v62, v62, 6, v21\n"
    "v_lshlrev_b32 v62, 1, v62\n"
    "v_add_u32 v21, 128, v21\n"
    "v_mul_u32_u24 v63, 2521, v21\n"
    "v_lshrrev_b32 v63, 16, v63\n"
    "s_add_u32 m0, s34, 81920\n"
    "s_nop 0\n"
    "global_load_lds_dwordx4 v96, s[38:39]\n"
    "v_add_u32 v96, 6656, v96\n"
    "v_mad_u32_u24 v63, v63, 6, v21\n"
    "v_lshlrev_b32 v63, 1, v63\n"
    "v_add_u32 v21, 128, v21\n"
    "v_mul_u32_u24 v64, 2521, v21\n"
    "v_lshrrev_b32 v64, 16, v64\n"
    "v_mad_u32_u24 v64, v64, 6, v21\n"
    "v_lshlrev_b32 v64, 1, v64\n"
    "v_add_u32 v21, 128, v21\n"
    "v_mul_u32_u24 v65, 2521, v21\n"
    "v_lshrrev_b32 v65, 16, v65\n"
    "s_add_u32 m0, s34, 90112\n"
    "s_cmp_ge_u32 s20, 3\n"
    "s_cbranch_scc1 .Lxd_b1\n"
    "global_load_lds_dwordx4 v96, s[38:39]\n"
    ".Lxd_b1:\n"
    "v_mad_u32_u24 v65, v65, 6, v21\n"
    "v_lshlrev_b32 v65, 1, v65\n"
    "v_cmp_gt_u32 vcc, 676, v21\n"
    "v_lshlrev_b32 v95, 4, v90\n"
    "v_mov_b32 v21, 0x40000\n"
    "v_cndmask_b32 v65, v21, v65, vcc\n"
    "v_cmp_gt_u32 vcc, 26, v1\n"
    "v_lshlrev_b32 v66, 6, v1\n"
    "v_lshlrev_b32 v67, 1, v1\n"
    "v_cndmask_b32 v66, v21, v66, vcc\n"
    "v_add_u32 v95, 39936, v95\n"
    "v_bfe_u32 v21, v1, 1, 4\n"
    "v_sub_u32 v21, v21, v18\n"
    "v_and_b32 v104, 16, v42\n"
    "s_movk_i32 s40, 0x3f80\n"
    "v_lshlrev_b32_e64 v96, v104, s40\n"
    "v_cmp_eq_u32 s[60:61], 0, v21\n"
    "v_cmp_eq_u32 s[62:63], 1, v21\n"
    "v_cmp_eq_u32 s[64:65], 2, v21\n"
    "v_cmp_eq_u32 s[50:51], 3, v21\n"
    "v_cndmask_b32 v104, 0, v96, s[60:61]\n"
    "v_cndmask_b32 v105, 0, v96, s[62:63]\n"
    "v_cndmask_b32 v106, 0, v96, s[64:65]\n"
    "v_cndmask_b32 v107, 0, v96, s[50:51]\n"
    "v_cmp_eq_u32 s[60:61], 8, v21\n"
    "v_cmp_eq_u32 s[62:63], 9, v21\n"
    "v_cmp_eq_u32 s[64:65], 10, v21\n"
    "v_cmp_eq_u32 s[50:51], 11, v21\n"
    "v_cndmask_b32 v108, 0, v96, s[60:61]\n"
    "v_cndmask_b32 v109, 0, v96, s[62:63]\n"
    "v_cndmask_b32 v110, 0, v96, s[64:65]\n"
    "v_cndmask_b32 v111, 0, v96, s[50:51]\n"
    "v_lshrrev_b32 v21, 2, v20\n"
    "v_min_u32 v21, 25, v21\n"
    "v_mul_u32_u24 v21, 52, v21\n"
    "v_lshl_add_u32 v21, v92, 4, v21\n"
    "s_and_b32 s40, s20, 1\n"
    "s_lshl3_add_u32 s40, s40, s35\n"
    "v_add_u32 v249, s40, v21\n"
    "v_add_u32 v250, 1352, v249\n"
    "v_add_u32 v251, 2704, v249\n"
    "s_lshl2_add_u32 s40, s20, s35\n"
    "s_add_u32 s40, s40, 4096\n"
    "v_add_u32 v252, s40, v21\n"
    "v_add_u32 v253, s35, v42\n"
    "v_lshl_add_u32 v13, v92, 3, v13\n"
    "v_and_b32 v21, 1, v90\n"
    "v_lshl_add_u32 v21, v21, 2, v22\n"
    "v_cmp_eq_u32 vcc, 7, v21\n"
    "v_lshlrev_b32 v21, 3, v21\n"
    "v_lshl_add_u32 v248, v13, 6, v21\n"
    "s_mul_i32 s34, s20, 3\n"
    "s_lshl_b32 s40, s34, 6\n"
    "s_add_u32 s40, s40, 49152\n"
    "v_add_u32 v21, s40, v21\n"
    "s_movk_i32 s41, 1664\n"
    "v_mov_b32 v97, 37888\n"
    "s_bfe_u32 s42, s20, 0x10001\n"
    "s_add_u32 s35, s42, 24\n"
    "s_sub_u32 s42, s35, s34\n"
    "s_lshl_b32 s42, s42, 6\n"
    "v_add_u32 v240, 0, v13\n"
    "v_min_u32 v240, 25, v240\n"
    "v_mad_u32_u24 v240, v240, s41, v21\n"
    "v_cndmask_b32 v240, v240, v97, vcc\n"
    "v_add_u32 v244, s42, v240\n"
    "v_add_u32 v241, 4, v13\n"
    "v_min_u32 v241, 25, v241\n"
    "v_mad_u32_u24 v241, v241, s41, v21\n"
    "v_cndmask_b32 v241, v241, v97, vcc\n"
    "v_add_u32 v245, s42, v241\n"
    "v_add_u32 v242, 16, v13\n"
    "v_min_u32 v242, 25, v242\n"
    "v_mad_u32_u24 v242, v242, s41, v21\n"
    "v_cndmask_b32 v242, v242, v97, vcc\n"
    "v_add_u32 v246, s42, v242\n"
    "v_add_u32 v243, 20, v13\n"
    "v_min_u32 v243, 25, v243\n"
    "v_mad_u32_u24 v243, v243, s41, v21\n"
    "v_cndmask_b32 v243, v243, v97, vcc\n"
    "v_add_u32 v247, s42, v243\n"
    "s_waitcnt vmcnt(5)\n"
    "v_lshl_or_b32 v36, v36, 7, v19\n"
    "v_lshl_or_b32 v37, v37, 7, v19\n"
    "v_lshl_or_b32 v38, v38, 7, v19\n"
    "v_lshl_or_b32 v41, v41, 7, v20\n"
    "global_load_dwordx4 v[28:31], v36, s[8:9]\n"
    "global_load_dwordx4 v[32:35], v37, s[8:9]\n"
    "global_load_dword v40, v41, s[8:9]\n"
    "global_load_dwordx4 v[36:39], v38, s[8:9]\n"
    "ds_read2_b32 v[112:113], v249 offset0:0 offset1:1\n"
    "ds_read2_b32 v[114:115], v249 offset0:2 offset1:3\n"
    "ds_read2_b32 v[116:117], v249 offset0:8 offset1:9\n"
    "ds_read2_b32 v[118:119], v249 offset0:10 offset1:11\n"
    "ds_read2_b32 v[120:121], v250 offset0:0 offset1:1\n"
    "ds_read2_b32 v[122:123], v250 offset0:2 offset1:3\n"
    "ds_read2_b32 v[124:125], v250 offset0:8 offset1:9\n"
    "ds_read2_b32 v[126:127], v250 offset0:10 offset1:11\n"
    "ds_read2_b32 v[128:129], v251 offset0:0 offset1:1\n"
    "ds_read2_b32 v[130:131], v251 offset0:2 offset1:3\n"
    "ds_read2_b32 v[132:133], v251 offset0:8 offset1:9\n"
    "ds_read2_b32 v[134:135], v251 offset0:10 offset1:11\n"
    "ds_read2_b32 v[136:137], v252 offset0:0 offset1:1\n"
    "ds_read2_b32 v[138:139], v252 offset0:2 offset1:3\n"
    "ds_read2_b32 v[140:141], v252 offset0:8 offset1:9\n"
    "ds_read2_b32 v[142:143], v252 offset0:10 offset1:11\n"
    "v_lshrrev_b32 v13, 2, v20\n"
    "v_cmp_eq_u32 vcc, 60, v1\n"
    "v_sub_u32 v13, v13, v18\n"
    "v_mov_b64 v[96:97], 0\n"
    "v_cndmask_b32 v13, v13, v1, vcc\n"
    "v_cmp_eq_u32 s[60:61], 0, v13\n"
    "v_cmp_eq_u32 s[62:63], 1, v13\n"
    "v_cmp_eq_u32 s[64:65], 2, v13\n"
    "v_cmp_eq_u32 s[50:51], 3, v13\n"
    "v_cndmask_b32 v76, 1.0, 0.5, s[60:61]\n"
    "v_cndmask_b32 v77, 1.0, 0.5, s[62:63]\n"
    "v_cndmask_b32 v78, 1.0, 0.5, s[64:65]\n"
    "v_cndmask_b32 v79, 1.0, 0.5, s[50:51]\n"
    "v_cmp_eq_u32 s[60:61], 8, v13\n"
    "v_cmp_eq_u32 s[62:63], 9, v13\n"
    "v_cmp_eq_u32 s[64:65], 10, v13\n"
    "v_cmp_eq_u32 s[50:51], 11, v13\n"
    "v_cndmask_b32 v80, 1.0, 0.5, s[60:61]\n"
    "v_cndmask_b32 v81, 1.0, 0.5, s[62:63]\n"
    "v_cndmask_b32 v82, 1.0, 0.5, s[64:65]\n"
    "v_cndmask_b32 v83, 1.0, 0.5, s[50:51]\n"
    "v_cmp_eq_u32 s[60:61], 16, v13\n"
    "v_cmp_eq_u32 s[62:63], 17, v13\n"
    "v_cmp_eq_u32 s[64:65], 18, v13\n"
    "v_cmp_eq_u32 s[50:51], 19, v13\n"
    "v_cndmask_b32 v84, 1.0, 0.5, s[60:61]\n"
    "v_cndmask_b32 v85, 1.0, 0.5, s[62:63]\n"
    "v_cndmask_b32 v86, 1.0, 0.5, s[64:65]\n"
    "v_cndmask_b32 v87, 1.0, 0.5, s[50:51]\n"
    "v_cmp_eq_u32 s[60:61], 24, v13\n"
    "v_cmp_eq_u32 s[62:63], 25, v13\n"
    "s_nop 0\n"
    "v_cndmask_b32 v88, 1.0, 0.5, s[60:61]\n"
    "v_cndmask_b32 v89, 1.0, 0.5, s[62:63]\n"
    "ds_write_b128 v253, v[96:99] offset:0\n"
    "ds_write_b128 v253, v[96:99] offset:1024\n"
    "ds_write_b128 v253, v[96:99] offset:2048\n"
    "ds_write_b128 v253, v[96:99] offset:3072\n"
    "ds_write_b128 v253, v[96:99] offset:4096\n"
    "ds_write_b128 v253, v[96:99] offset:5120\n"
    "s_waitcnt lgkmcnt(0)\n"
    "s_barrier\n"
    "ds_read2_b64 v[96:99], v95 offset0:13 offset1:14\n"
    "ds_read_u16 v68, v67 offset:39988\n"
    "ds_read_u16 v95, v67 offset:39936\n"
    "ds_read_b64_tr_b16 v[192:193], v93 offset:0\n"
    "ds_read_b64_tr_b16 v[194:195], v94 offset:0\n"
    "ds_read_b64_tr_b16 v[196:197], v93 offset:256\n"
    "ds_read_b64_tr_b16 v[198:199], v94 offset:256\n"
    "ds_read_b64_tr_b16 v[200:201], v93 offset:512\n"
    "ds_read_b64_tr_b16 v[202:203], v94 offset:512\n"
    "ds_read_b64_tr_b16 v[204:205], v93 offset:768\n"
    "ds_read_b64_tr_b16 v[206:207], v94 offset:768\n"
    "ds_read_b64_tr_b16 v[208:209], v93 offset:1024\n"
    "ds_read_b64_tr_b16 v[210:211], v94 offset:1024\n"
    "ds_read_b64_tr_b16 v[212:213], v93 offset:1280\n"
    "ds_read_b64_tr_b16 v[214:215], v94 offset:1280\n"
    "v_mfma_f32_32x32x16_bf16 v[144:159], v[104:107], v[112:115], 0\n"
    "v_mfma_f32_32x32x16_bf16 v[144:159], v[112:115], v[104:107], v[144:159]\n"
    "v_mfma_f32_32x32x16_bf16 v[144:159], v[108:111], v[116:119], v[144:159]\n"
    "v_mfma_f32_32x32x16_bf16 v[144:159], v[116:119], v[108:111], v[144:159]\n"
    "ds_write_b32 v25, v12 offset:20\n"
    "v_cmp_gt_u32 vcc, 3, v90\n"
    "v_mov_b32 v90, 39920\n"
    "s_waitcnt lgkmcnt(13)\n"
    "v_lshlrev_b32 v68, 16, v68\n"
    "v_cndmask_b32 v97, 0, v97, vcc\n"
    "v_cndmask_b32 v98, 0, v98, vcc\n"
    "v_cndmask_b32 v99, 0, v99, vcc\n"
    "v_mul_f32 v68, 0x41800000, v68\n"
    "v_lshlrev_b32 v95, 16, v95\n"
    "s_waitcnt lgkmcnt(1)\n"
    "v_mfma_f32_16x16x32_bf16 v[216:219], v[96:99], v[192:195], 0\n"
    "v_mfma_f32_16x16x32_bf16 v[220:223], v[96:99], v[196:199], 0\n"
    "v_mfma_f32_16x16x32_bf16 v[224:227], v[96:99], v[200:203], 0\n"
    "v_mfma_f32_16x16x32_bf16 v[228:231], v[96:99], v[204:207], 0\n"
    "v_mfma_f32_16x16x32_bf16 v[232:235], v[96:99], v[208:211], 0\n"
    "v_mfma_f32_16x16x32_bf16 v[236:239], v[96:99], v[212:215], 0\n"
    "ds_write_b16_d16_hi v66, v68 offset:35896\n"
    "ds_read_b128 v[192:195], v42 offset:40960\n"
    "ds_read_b128 v[196:199], v42 offset:41984\n"
    "ds_read_b128 v[200:203], v42 offset:43008\n"
    "ds_read_b128 v[204:207], v42 offset:44032\n"
    "v_mul_f32 v95, 0x43800000, v95\n"
    "v_mfma_f32_32x32x16_bf16 v[160:175], v[104:107], v[120:123], 0\n"
    "v_cvt_pk_bf16_f32 v216, v216, v216\n"
    "ds_write_b16 v60, v216 offset:35840\n"
    "v_cvt_pk_bf16_f32 v220, v220, v220\n"
    "v_mfma_f32_32x32x16_bf16 v[160:175], v[120:123], v[104:107], v[160:175]\n"
    "ds_write_b16 v61, v220 offset:35840\n"
    "v_cvt_pk_bf16_f32 v224, v224, v224\n"
    "ds_write_b16 v62, v224 offset:35840\n"
    "v_mfma_f32_32x32x16_bf16 v[160:175], v[108:111], v[124:127], v[160:175]\n"
    "v_cvt_pk_bf16_f32 v228, v228, v228\n"
    "ds_write_b16 v63, v228 offset:35840\n"
    "v_cvt_pk_bf16_f32 v232, v232, v232\n"
    "v_mfma_f32_32x32x16_bf16 v[160:175], v[124:127], v[108:111], v[160:175]\n"
    "ds_write_b16 v64, v232 offset:35840\n"
    "v_cvt_pk_bf16_f32 v236, v236, v236\n"
    "ds_write_b16 v65, v236 offset:35840\n"
    "s_lshl_b32 s30, s20, 5\n"
    "s_add_u32 s30, s30, 97264\n"
    "s_waitcnt lgkmcnt(6)\n"
    "v_mfma_f32_32x32x16_bf16 v[176:191], v[104:107], v[128:131], 0\n"
    "v_max3_f32 v11, |v144|, |v145|, 0\n"
    "v_max3_f32 v11, |v146|, |v147|, v11\n"
    "v_max3_f32 v11, |v148|, |v149|, v11\n"
    "v_max3_f32 v11, |v150|, |v151|, v11\n"
    "v_mfma_f32_32x32x16_bf16 v[176:191], v[128:131], v[104:107], v[176:191]\n"
    "v_max3_f32 v11, |v152|, |v153|, v11\n"
    "v_max3_f32 v11, |v154|, |v155|, v11\n"
    "v_max3_f32 v11, |v156|, |v157|, v11\n"
    "v_mad_u32_u16 v44, v192, 2, s30 op_sel:[0,0,0,0]\n"
    "v_mfma_f32_32x32x16_bf16 v[176:191], v[108:111], v[132:135], v[176:191]\n"
    "v_mad_u32_u16 v45, v192, 2, s30 op_sel:[1,0,0,0]\n"
    "v_mad_u32_u16 v46, v193, 2, s30 op_sel:[0,0,0,0]\n"
    "v_mad_u32_u16 v47, v193, 2, s30 op_sel:[1,0,0,0]\n"
    "v_mad_u32_u16 v48, v194, 2, s30 op_sel:[0,0,0,0]\n"
    "v_mfma_f32_32x32x16_bf16 v[176:191], v[132:135], v[108:111], v[176:191]\n"
    "v_mad_u32_u16 v49, v194, 2, s30 op_sel:[1,0,0,0]\n"
    "v_mad_u32_u16 v50, v195, 2, s30 op_sel:[0,0,0,0]\n"
    "v_mad_u32_u16 v51, v195, 2, s30 op_sel:[1,0,0,0]\n"
    "v_mad_u32_u16 v52, v196, 2, s30 op_sel:[0,0,0,0]\n"
    "s_cmp_eq_u32 s24, 0\n"
    "s_cbranch_scc1 .Lxd_on44\n"
    ".Lxd_on44r:\n"
    "s_cmp_eq_u32 s66, 0\n"
    "s_cbranch_scc1 .Lxd_on45\n"
    ".Lxd_on45r:\n"
    "v_max3_f32 v11, |v160|, |v161|, v11\n"
    "v_max3_f32 v11, |v162|, |v163|, v11\n"
    "v_max3_f32 v11, |v164|, |v165|, v11\n"
    "v_max3_f32 v11, |v166|, |v167|, v11\n"
    "v_max3_f32 v11, |v168|, |v169|, v11\n"
    "v_max3_f32 v11, |v170|, |v171|, v11\n"
    "v_max3_f32 v11, |v172|, |v173|, v11\n"
    "v_mad_u32_u16 v53, v196, 2, s30 op_sel:[1,0,0,0]\n"
    "v_mad_u32_u16 v54, v197, 2, s30 op_sel:[0,0,0,0]\n"
    "v_mad_u32_u16 v55, v197, 2, s30 op_sel:[1,0,0,0]\n"
    "v_mad_u32_u16 v56, v198, 2, s30 op_sel:[0,0,0,0]\n"
    "v_mad_u32_u16 v57, v198, 2, s30 op_sel:[1,0,0,0]\n"
    "v_mad_u32_u16 v58, v199, 2, s30 op_sel:[0,0,0,0]\n"
    "v_mad_u32_u16 v60, v200, 2, s30 op_sel:[0,0,0,0]\n"
    "v_mad_u32_u16 v61, v200, 2, s30 op_sel:[1,0,0,0]\n"
    "v_mad_u32_u16 v62, v201, 2, s30 op_sel:[0,0,0,0]\n"
    "v_max3_f32 v11, |v176|, |v177|, v11\n"
    "v_max3_f32 v11, |v178|, |v179|, v11\n"
    "v_max3_f32 v11, |v180|, |v181|, v11\n"
    "v_max3_f32 v11, |v182|, |v183|, v11\n"
    "v_max3_f32 v11, |v184|, |v185|, v11\n"
    "v_max3_f32 v11, |v186|, |v187|, v11\n"
    "v_max3_f32 v11, |v188|, |v189|, v11\n"
    "s_cmp_eq_u32 s24, 0\n"
    "s_cbranch_scc1 .Lxd_on46\n"
    ".Lxd_on46r:\n"
    "v_mad_u32_u16 v63, v201, 2, s30 op_sel:[1,0,0,0]\n"
    "v_mad_u32_u16 v64, v202, 2, s30 op_sel:[0,0,0,0]\n"
    "v_max_u32_dpp v11, v11, v11 quad_perm:[1,0,3,2] row_mask:0xf bank_mask:0xf\n"
    "v_mad_u32_u16 v65, v202, 2, s30 op_sel:[1,0,0,0]\n"
    "v_mad_u32_u16 v66, v203, 2, s30 op_sel:[0,0,0,0]\n"
    "v_max_u32_dpp v11, v11, v11 quad_perm:[2,3,0,1] row_mask:0xf bank_mask:0xf\n"
    "v_mad_u32_u16 v67, v203, 2, s30 op_sel:[1,0,0,0]\n"
    "v_mad_u32_u16 v68, v204, 2, s30 op_sel:[0,0,0,0]\n"
    "v_max_u32_dpp v11, v11, v11 row_half_mirror row_mask:0xf bank_mask:0xf\n"
    "v_mad_u32_u16 v69, v204, 2, s30 op_sel:[1,0,0,0]\n"
    "v_mad_u32_u16 v70, v205, 2, s30 op_sel:[0,0,0,0]\n"
    "v_max_u32_dpp v11, v11, v11 row_mirror row_mask:0xf bank_mask:0xf\n"
    "v_mad_u32_u16 v71, v205, 2, s30 op_sel:[1,0,0,0]\n"
    "v_mad_u32_u16 v72, v206, 2, s30 op_sel:[0,0,0,0]\n"
    "v_max_u32_dpp v11, v11, v11 row_bcast:15 row_mask:0xa bank_mask:0xf\n"
    "v_mad_u32_u16 v73, v206, 2, s30 op_sel:[1,0,0,0]\n"
    "v_mad_u32_u16 v74, v207, 2, s30 op_sel:[0,0,0,0]\n"
    "v_max_u32_dpp v11, v11, v11 row_bcast:31 row_mask:0xc bank_mask:0xf\n"
    "s_brev_b64 exec, 1\n"
    "ds_max_u32 v90, v11 offset:0\n"
    "s_mov_b64 exec, -1\n"
    "s_waitcnt vmcnt(4) lgkmcnt(0)\n"
    "s_barrier\n"
    "ds_read_b32 v98, v90\n"
    "ds_read_b64_tr_b16 v[104:105], v248 offset:35840\n"
    "ds_read_b64_tr_b16 v[106:107], v248 offset:36096\n"
    "ds_read_b64_tr_b16 v[108:109], v248 offset:36864\n"
    "ds_read_b64_tr_b16 v[110:111], v248 offset:37120\n"
    "ds_read_b64_tr_b16 v[128:129], v240 offset:128\n"
    "ds_read_b64_tr_b16 v[130:131], v241 offset:128\n"
    "ds_read_b64_tr_b16 v[132:133], v242 offset:128\n"
    "ds_read_b64_tr_b16 v[134:135], v243 offset:128\n"
    "ds_read_b64_tr_b16 v[136:137], v244 offset:0\n"
    "ds_read_b64_tr_b16 v[138:139], v245 offset:0\n"
    "ds_read_b64_tr_b16 v[140:141], v246 offset:0\n"
    "ds_read_b64_tr_b16 v[142:143], v247 offset:0\n"
    "s_waitcnt lgkmcnt(12)\n"
    "v_lshrrev_b32 v98, 23, v98\n"
    "v_max_u32 v98, 40, v98\n"
    "v_min_u32 v98, 250, v98\n"
    "v_sub_u32 v96, 141, v98\n"
    "v_ldexp_f32 v76, v76, v96\n"
    "v_ldexp_f32 v77, v77, v96\n"
    "v_ldexp_f32 v78, v78, v96\n"
    "v_ldexp_f32 v79, v79, v96\n"
    "v_ldexp_f32 v80, v80, v96\n"
    "v_ldexp_f32 v81, v81, v96\n"
    "v_ldexp_f32 v82, v82, v96\n"
    "v_ldexp_f32 v83, v83, v96\n"
    "v_ldexp_f32 v84, v84, v96\n"
    "v_ldexp_f32 v85, v85, v96\n"
    "v_ldexp_f32 v86, v86, v96\n"
    "v_ldexp_f32 v87, v87, v96\n"
    "v_ldexp_f32 v88, v88, v96\n"
    "v_ldexp_f32 v89, v89, v96\n"
    "s_cmp_eq_u32 s24, 0\n"
    "s_cbranch_scc1 .Lxd_on47\n"
    ".Lxd_on47r:\n"
    "ds_read_b64_tr_b16 v[112:113], v240 offset:0\n"
    "ds_read_b64_tr_b16 v[114:115], v241 offset:0\n"
    "ds_read_b64_tr_b16 v[116:117], v242 offset:0\n"
    "ds_read_b64_tr_b16 v[118:119], v243 offset:0\n"
    "ds_read_b64_tr_b16 v[120:121], v240 offset:64\n"
    "ds_read_b64_tr_b16 v[122:123], v241 offset:64\n"
    "ds_read_b64_tr_b16 v[124:125], v242 offset:64\n"
    "ds_read_b64_tr_b16 v[126:127], v243 offset:64\n"
    "s_waitcnt lgkmcnt(8)\n"
    "v_mfma_f32_32x32x16_bf16 v[224:239], v[104:107], v[128:131], 0\n"
    "v_pk_mul_f32 v[144:145], v[144:145], v[76:77]\n"
    "v_cvt_pk_f16_f32 v144, v144, v145\n"
    "ds_write_b16 v44, v144 offset:0\n"
    "ds_write_b16_d16_hi v45, v144 offset:0\n"
    "v_pk_mul_f32 v[146:147], v[146:147], v[78:79]\n"
    "v_cvt_pk_f16_f32 v146, v146, v147\n"
    "ds_write_b16 v46, v146 offset:0\n"
    "v_mfma_f32_32x32x16_bf16 v[224:239], v[128:131], v[104:107], v[224:239]\n"
    "ds_write_b16_d16_hi v47, v146 offset:0\n"
    "v_pk_mul_f32 v[148:149], v[148:149], v[80:81]\n"
    "v_cvt_pk_f16_f32 v148, v148, v149\n"
    "ds_write_b16 v48, v148 offset:0\n"
    "ds_write_b16_d16_hi v49, v148 offset:0\n"
    "v_pk_mul_f32 v[150:151], v[150:151], v[82:83]\n"
    "v_cvt_pk_f16_f32 v150, v150, v151\n"
    "v_mfma_f32_32x32x16_bf16 v[224:239], v[108:111], v[132:135], v[224:239]\n"
    "ds_write_b16 v50, v150 offset:0\n"
    "ds_write_b16_d16_hi v51, v150 offset:0\n"
    "v_pk_mul_f32 v[152:153], v[152:153], v[84:85]\n"
    "v_cvt_pk_f16_f32 v152, v152, v153\n"
    "ds_write_b16 v52, v152 offset:0\n"
    "ds_write_b16_d16_hi v53, v152 offset:0\n"
    "v_pk_mul_f32 v[154:155], v[154:155], v[86:87]\n"
    "v_mfma_f32_32x32x16_bf16 v[224:239], v[132:135], v[108:111], v[224:239]\n"
    "v_cvt_pk_f16_f32 v154, v154, v155\n"
    "ds_write_b16 v54, v154 offset:0\n"
    "ds_write_b16_d16_hi v55, v154 offset:0\n"
    "v_pk_mul_f32 v[156:157], v[156:157], v[88:89]\n"
    "v_cvt_pk_f16_f32 v156, v156, v157\n"
    "ds_write_b16 v58, v156 offset:0\n"
    "ds_write_b16_d16_hi v57, v156 offset:0\n"
    "s_waitcnt lgkmcnt(14)\n"
    "v_mfma_f32_32x32x16_bf16 v[192:207], v[104:107], v[112:115], 0\n"
    "v_pk_mul_f32 v[160:161], v[160:161], v[76:77]\n"
    "v_cvt_pk_f16_f32 v160, v160, v161\n"
    "ds_write_b16 v44, v160 offset:16\n"
    "v_mfma_f32_32x32x16_bf16 v[192:207], v[112:115], v[104:107], v[192:207]\n"
    "ds_write_b16_d16_hi v45, v160 offset:16\n"
    "v_pk_mul_f32 v[162:163], v[162:163], v[78:79]\n"
    "v_cvt_pk_f16_f32 v162, v162, v163\n"
    "ds_write_b16 v46, v162 offset:16\n"
    "v_mfma_f32_32x32x16_bf16 v[192:207], v[108:111], v[116:119], v[192:207]\n"
    "ds_write_b16_d16_hi v47, v162 offset:16\n"
    "v_pk_mul_f32 v[164:165], v[164:165], v[80:81]\n"
    "v_cvt_pk_f16_f32 v164, v164, v165\n"
    "v_mfma_f32_32x32x16_bf16 v[192:207], v[116:119], v[108:111], v[192:207]\n"
    "ds_write_b16 v48, v164 offset:16\n"
    "ds_write_b16_d16_hi v49, v164 offset:16\n"
    "v_pk_mul_f32 v[166:167], v[166:167], v[82:83]\n"
    "v_cvt_pk_f16_f32 v166, v166, v167\n"
    "v_mfma_f32_32x32x16_bf16 v[208:223], v[104:107], v[120:123], 0\n"
    "ds_write_b16 v50, v166 offset:16\n"
    "ds_write_b16_d16_hi v51, v166 offset:16\n"
    "v_pk_mul_f32 v[168:169], v[168:169], v[84:85]\n"
    "v_mfma_f32_32x32x16_bf16 v[208:223], v[120:123], v[104:107], v[208:223]\n"
    "v_cvt_pk_f16_f32 v168, v168, v169\n"
    "ds_write_b16 v52, v168 offset:16\n"
    "ds_write_b16_d16_hi v53, v168 offset:16\n"
    "v_pk_mul_f32 v[170:171], v[170:171], v[86:87]\n"
    "v_mfma_f32_32x32x16_bf16 v[208:223], v[108:111], v[124:127], v[208:223]\n"
    "v_cvt_pk_f16_f32 v170, v170, v171\n"
    "ds_write_b16 v54, v170 offset:16\n"
    "ds_write_b16_d16_hi v55, v170 offset:16\n"
    "v_mfma_f32_32x32x16_bf16 v[208:223], v[124:127], v[108:111], v[208:223]\n"
    "v_pk_mul_f32 v[172:173], v[172:173], v[88:89]\n"
    "v_cvt_pk_f16_f32 v172, v172, v173\n"
    "ds_write_b16 v58, v172 offset:16\n"
    "ds_write_b16_d16_hi v57, v172 offset:16\n"
    "s_cmp_eq_u32 s24, 1\n"
    "s_cbranch_scc1 .Lxd_on48\n"
    ".Lxd_on48r:\n"
    "v_pk_mul_f32 v[176:177], v[176:177], v[76:77]\n"
    "v_cvt_pk_f16_f32 v176, v176, v177\n"
    "s_add_u32 s40, s34, 2\n"
    "v_readlane_b32 s41, v95, s40\n"
    "s_nop 3\n"
    "v_writelane_b32 v236, s41, 60\n"
    "ds_write_b16 v44, v176 offset:2080\n"
    "v_max3_f32 v11, |v224|, |v225|, 0\n"
    "ds_write_b16_d16_hi v45, v176 offset:2080\n"
    "v_max3_f32 v11, |v226|, |v227|, v11\n"
    "v_pk_mul_f32 v[178:179], v[178:179], v[78:79]\n"
    "v_max3_f32 v11, |v228|, |v229|, v11\n"
    "v_cvt_pk_f16_f32 v178, v178, v179\n"
    "v_max3_f32 v11, |v230|, |v231|, v11\n"
    "ds_write_b16 v46, v178 offset:2080\n"
    "v_max3_f32 v11, |v232|, |v233|, v11\n"
    "ds_write_b16_d16_hi v47, v178 offset:2080\n"
    "v_pk_mul_f32 v[180:181], v[180:181], v[80:81]\n"
    "v_max3_f32 v11, |v234|, |v235|, v11\n"
    "v_cvt_pk_f16_f32 v180, v180, v181\n"
    "v_max3_f32 v11, |v236|, |v237|, v11\n"
    "ds_write_b16 v48, v180 offset:2080\n"
    "s_add_u32 s40, s34, 0\n"
    "v_readlane_b32 s41, v95, s40\n"
    "s_nop 3\n"
    "v_writelane_b32 v204, s41, 60\n"
    "ds_write_b16_d16_hi v49, v180 offset:2080\n"
    "v_max3_f32 v11, |v192|, |v193|, v11\n"
    "v_pk_mul_f32 v[182:183], v[182:183], v[82:83]\n"
    "v_max3_f32 v11, |v194|, |v195|, v11\n"
    "v_cvt_pk_f16_f32 v182, v182, v183\n"
    "v_max3_f32 v11, |v196|, |v197|, v11\n"
    "ds_write_b16 v50, v182 offset:2080\n"
    "ds_write_b16_d16_hi v51, v182 offset:2080\n"
    "v_max3_f32 v11, |v198|, |v199|, v11\n"
    "v_pk_mul_f32 v[184:185], v[184:185], v[84:85]\n"
    "v_max3_f32 v11, |v200|, |v201|, v11\n"
    "v_cvt_pk_f16_f32 v184, v184, v185\n"
    "v_max3_f32 v11, |v202|, |v203|, v11\n"
    "ds_write_b16 v52, v184 offset:2080\n"
    "v_max3_f32 v11, |v204|, |v205|, v11\n"
    "ds_write_b16_d16_hi v53, v184 offset:2080\n"
    "s_add_u32 s40, s34, 1\n"
    "v_readlane_b32 s41, v95, s40\n"
    "s_nop 3\n"
    "v_writelane_b32 v220, s41, 60\n"
    "v_pk_mul_f32 v[186:187], v[186:187], v[86:87]\n"
    "v_max3_f32 v11, |v208|, |v209|, v11\n"
    "v_cvt_pk_f16_f32 v186, v186, v187\n"
    "ds_write_b16 v54, v186 offset:2080\n"
    "v_max3_f32 v11, |v210|, |v211|, v11\n"
    "ds_write_b16_d16_hi v55, v186 offset:2080\n"
    "v_max3_f32 v11, |v212|, |v213|, v11\n"
    "v_pk_mul_f32 v[188:189], v[188:189], v[88:89]\n"
    "v_max3_f32 v11, |v214|, |v215|, v11\n"
    "v_cvt_pk_f16_f32 v188, v188, v189\n"
    "v_max3_f32 v11, |v216|, |v217|, v11\n"
    "ds_write_b16 v58, v188 offset:2080\n"
    "v_max3_f32 v11, |v218|, |v219|, v11\n"
    "ds_write_b16_d16_hi v57, v188 offset:2080\n"
    "v_max3_f32 v11, |v220|, |v221|, v11\n"
    "s_cmp_eq_u32 s24, 1\n"
    "s_cbranch_scc1 .Lxd_on49\n"
    ".Lxd_on49r:\n"
    "s_waitcnt vmcnt(0)\n"
    "s_movk_i32 s43, 15744\n"
    "v_pk_maximum3_f16 v21, v28, v28, s43 op_sel_hi:[1,1,0] neg_lo:[0,1,0] neg_hi:[0,1,0]\n"
    "v_pk_maximum3_f16 v21, v29, v29, v21 neg_lo:[0,1,0] neg_hi:[0,1,0]\n"
    "v_max_u32_dpp v11, v11, v11 quad_perm:[1,0,3,2] row_mask:0xf bank_mask:0xf\n"
    "v_pk_maximum3_f16 v21, v30, v30, v21 neg_lo:[0,1,0] neg_hi:[0,1,0]\n"
    "v_pk_maximum3_f16 v21, v31, v31, v21 neg_lo:[0,1,0] neg_hi:[0,1,0]\n"
    "v_max_u32_dpp v11, v11, v11 quad_perm:[2,3,0,1] row_mask:0xf bank_mask:0xf\n"
    "v_pk_maximum3_f16 v21, v32, v32, v21 neg_lo:[0,1,0] neg_hi:[0,1,0]\n"
    "v_pk_maximum3_f16 v21, v33, v33, v21 neg_lo:[0,1,0] neg_hi:[0,1,0]\n"
    "v_max_u32_dpp v11, v11, v11 row_half_mirror row_mask:0xf bank_mask:0xf\n"
    "v_pk_maximum3_f16 v21, v34, v34, v21 neg_lo:[0,1,0] neg_hi:[0,1,0]\n"
    "v_pk_maximum3_f16 v21, v35, v35, v21 neg_lo:[0,1,0] neg_hi:[0,1,0]\n"
    "v_max_u32_dpp v11, v11, v11 row_mirror row_mask:0xf bank_mask:0xf\n"
    "v_pk_maximum3_f16 v21, v36, v36, v21 neg_lo:[0,1,0] neg_hi:[0,1,0]\n"
    "v_pk_maximum3_f16 v21, v37, v37, v21 neg_lo:[0,1,0] neg_hi:[0,1,0]\n"
    "v_max_u32_dpp v11, v11, v11 row_bcast:15 row_mask:0xa bank_mask:0xf\n"
    "v_pk_maximum3_f16 v21, v38, v38, v21 neg_lo:[0,1,0] neg_hi:[0,1,0]\n"
    "v_pk_maximum3_f16 v21, v39, v39, v21 neg_lo:[0,1,0] neg_hi:[0,1,0]\n"
    "v_max_u32_dpp v11, v11, v11 row_bcast:31 row_mask:0xc bank_mask:0xf\n"
    "s_brev_b64 exec, 1\n"
    "ds_max_u32 v90, v11 offset:4\n"
    "s_mov_b64 exec, -1\n"
    "v_pk_maximum3_f16 v21, v40, v40, v21 neg_lo:[0,1,0] neg_hi:[0,1,0]\n"
    "v_pk_max_u16 v21, v21, v21 op_sel:[0,1] op_sel_hi:[1,0]\n"
    "s_cmp_eq_u32 s20, 7\n"
    "s_cbranch_scc1 .Lxd_on50\n"
    ".Lxd_on50r:\n"
    "s_nop 1\n"
    "v_max_u32_dpp v21, v21, v21 quad_perm:[1,0,3,2] row_mask:0xf bank_mask:0xf\n"
    "s_nop 1\n"
    "v_max_u32_dpp v21, v21, v21 quad_perm:[2,3,0,1] row_mask:0xf bank_mask:0xf\n"
    "s_nop 1\n"
    "v_max_u32_dpp v21, v21, v21 row_half_mirror row_mask:0xf bank_mask:0xf\n"
    "s_nop 1\n"
    "v_max_u32_dpp v21, v21, v21 row_mirror row_mask:0xf bank_mask:0xf\n"
    "s_nop 1\n"
    "v_max_u32_dpp v21, v21, v21 row_bcast:15 row_mask:0xa bank_mask:0xf\n"
    "s_nop 1\n"
    "v_max_u32_dpp v21, v21, v21 row_bcast:31 row_mask:0xc bank_mask:0xf\n"
    "s_nop 1\n"
    "v_readlane_b32 s47, v21, 63\n"
    "s_and_b32 s50, s47, 0x7f80\n"
    "s_lshr_b32 s47, s50, 6\n"
    "s_sub_u32 s50, s50, 2816\n"
    "s_mul_i32 s51, s50, -8\n"
    "s_waitcnt lgkmcnt(0)\n"
    "s_barrier\n"
    "ds_read_b32 v99, v90 offset:4\n"
    "ds_read_b32 v41, v25 offset:24\n"
    "v_pk_maximum3_f16 v21, v28, v28, s50 op_sel_hi:[1,1,0] neg_lo:[0,1,0] neg_hi:[0,1,0]\n"
    "v_pk_mad_u16 v21, v21, 8, s51 op_sel_hi:[1,0,0]\n"
    "v_bfi_b32 v28, s57, v21, v28\n"
    "v_pk_maximum3_f16 v12, v29, v29, s50 op_sel_hi:[1,1,0] neg_lo:[0,1,0] neg_hi:[0,1,0]\n"
    "v_pk_mad_u16 v12, v12, 8, s51 op_sel_hi:[1,0,0]\n"
    "v_bfi_b32 v29, s57, v12, v29\n"
    "v_pk_maximum3_f16 v21, v30, v30, s50 op_sel_hi:[1,1,0] neg_lo:[0,1,0] neg_hi:[0,1,0]\n"
    "v_pk_mad_u16 v21, v21, 8, s51 op_sel_hi:[1,0,0]\n"
    "v_bfi_b32 v30, s57, v21, v30\n"
    "v_pk_maximum3_f16 v12, v31, v31, s50 op_sel_hi:[1,1,0] neg_lo:[0,1,0] neg_hi:[0,1,0]\n"
    "v_pk_mad_u16 v12, v12, 8, s51 op_sel_hi:[1,0,0]\n"
    "v_bfi_b32 v31, s57, v12, v31\n"
    "s_waitcnt lgkmcnt(1)\n"
    "v_lshrrev_b32 v99, 23, v99\n"
    "v_max_u32 v99, 40, v99\n"
    "v_min_u32 v99, 250, v99\n"
    "v_sub_u32 v96, v98, v99\n"
    "v_ldexp_f32 v76, v76, v96\n"
    "v_ldexp_f32 v77, v77, v96\n"
    "v_ldexp_f32 v78, v78, v96\n"
    "v_ldexp_f32 v79, v79, v96\n"
    "v_ldexp_f32 v80, v80, v96\n"
    "v_ldexp_f32 v81, v81, v96\n"
    "v_ldexp_f32 v82, v82, v96\n"
    "v_ldexp_f32 v83, v83, v96\n"
    "v_ldexp_f32 v84, v84, v96\n"
    "v_ldexp_f32 v85, v85, v96\n"
    "v_ldexp_f32 v86, v86, v96\n"
    "v_ldexp_f32 v87, v87, v96\n"
    "v_ldexp_f32 v88, v88, v96\n"
    "v_ldexp_f32 v89, v89, v96\n"
    "v_pk_mul_f32 v[192:193], v[192:193], v[76:77]\n"
    "v_cvt_pk_f16_f32 v192, v192, v193\n"
    "v_pk_maximum3_f16 v21, v32, v32, s50 op_sel_hi:[1,1,0] neg_lo:[0,1,0] neg_hi:[0,1,0]\n"
    "ds_write_b16 v44, v192 offset:1040\n"
    "v_pk_mad_u16 v21, v21, 8, s51 op_sel_hi:[1,0,0]\n"
    "ds_write_b16_d16_hi v45, v192 offset:1040\n"
    "v_bfi_b32 v32, s57, v21, v32\n"
    "v_pk_mul_f32 v[194:195], v[194:195], v[78:79]\n"
    "v_pk_maximum3_f16 v12, v33, v33, s50 op_sel_hi:[1,1,0] neg_lo:[0,1,0] neg_hi:[0,1,0]\n"
    "v_cvt_pk_f16_f32 v194, v194, v195\n"
    "v_pk_mad_u16 v12, v12, 8, s51 op_sel_hi:[1,0,0]\n"
    "ds_write_b16 v46, v194 offset:1040\n"
    "v_bfi_b32 v33, s57, v12, v33\n"
    "ds_write_b16_d16_hi v47, v194 offset:1040\n"
    "v_pk_maximum3_f16 v21, v34, v34, s50 op_sel_hi:[1,1,0] neg_lo:[0,1,0] neg_hi:[0,1,0]\n"
    "v_pk_mul_f32 v[196:197], v[196:197], v[80:81]\n"
    "v_pk_mad_u16 v21, v21, 8, s51 op_sel_hi:[1,0,0]\n"
    "v_cvt_pk_f16_f32 v196, v196, v197\n"
    "v_bfi_b32 v34, s57, v21, v34\n"
    "ds_write_b16 v48, v196 offset:1040\n"
    "v_pk_maximum3_f16 v12, v35, v35, s50 op_sel_hi:[1,1,0] neg_lo:[0,1,0] neg_hi:[0,1,0]\n"
    "ds_write_b16_d16_hi v49, v196 offset:1040\n"
    "v_pk_mad_u16 v12, v12, 8, s51 op_sel_hi:[1,0,0]\n"
    "v_pk_mul_f32 v[198:199], v[198:199], v[82:83]\n"
    "v_bfi_b32 v35, s57, v12, v35\n"
    "v_cvt_pk_f16_f32 v198, v198, v199\n"
    "v_pk_maximum3_f16 v21, v36, v36, s50 op_sel_hi:[1,1,0] neg_lo:[0,1,0] neg_hi:[0,1,0]\n"
    "ds_write_b16 v50, v198 offset:1040\n"
    "v_pk_mad_u16 v21, v21, 8, s51 op_sel_hi:[1,0,0]\n"
    "ds_write_b16_d16_hi v51, v198 offset:1040\n"
    "v_bfi_b32 v36, s57, v21, v36\n"
    "v_pk_mul_f32 v[200:201], v[200:201], v[84:85]\n"
    "v_pk_maximum3_f16 v12, v37, v37, s50 op_sel_hi:[1,1,0] neg_lo:[0,1,0] neg_hi:[0,1,0]\n"
    "v_cvt_pk_f16_f32 v200, v200, v201\n"
    "v_pk_mad_u16 v12, v12, 8, s51 op_sel_hi:[1,0,0]\n"
    "ds_write_b16 v52, v200 offset:1040\n"
    "v_bfi_b32 v37, s57, v12, v37\n"
    "ds_write_b16_d16_hi v53, v200 offset:1040\n"
    "v_pk_maximum3_f16 v21, v38, v38, s50 op_sel_hi:[1,1,0] neg_lo:[0,1,0] neg_hi:[0,1,0]\n"
    "v_pk_mul_f32 v[202:203], v[202:203], v[86:87]\n"
    "v_pk_mad_u16 v21, v21, 8, s51 op_sel_hi:[1,0,0]\n"
    "v_cvt_pk_f16_f32 v202, v202, v203\n"
    "v_bfi_b32 v38, s57, v21, v38\n"
    "ds_write_b16 v54, v202 offset:1040\n"
    "v_pk_maximum3_f16 v12, v39, v39, s50 op_sel_hi:[1,1,0] neg_lo:[0,1,0] neg_hi:[0,1,0]\n"
    "ds_write_b16_d16_hi v55, v202 offset:1040\n"
    "v_pk_mad_u16 v12, v12, 8, s51 op_sel_hi:[1,0,0]\n"
    "v_pk_mul_f32 v[204:205], v[204:205], v[88:89]\n"
    "v_bfi_b32 v39, s57, v12, v39\n"
    "v_cvt_pk_f16_f32 v204, v204, v205\n"
    "v_pk_maximum3_f16 v21, v40, v40, s50 op_sel_hi:[1,1,0] neg_lo:[0,1,0] neg_hi:[0,1,0]\n"
    "ds_write_b16 v56, v204 offset:1040\n"
    "v_pk_mad_u16 v21, v21, 8, s51 op_sel_hi:[1,0,0]\n"
    "ds_write_b16_d16_hi v57, v204 offset:1040\n"
    "v_bfi_b32 v40, s57, v21, v40\n"
    "v_pk_mul_f32 v[208:209], v[208:209], v[76:77]\n"
    "s_mul_i32 s43, s21, 49152\n"
    "v_cvt_pk_f16_f32 v208, v208, v209\n"
    "v_lshrrev_b32 v104, 4, v1\n"
    "ds_write_b16 v44, v208 offset:1056\n"
    "v_bfe_u32 v105, v1, 2, 2\n"
    "ds_write_b16_d16_hi v45, v208 offset:1056\n"
    "v_and_b32 v106, 3, v1\n"
    "s_lshl_b32 s40, s22, 5\n"
    "v_pk_mul_f32 v[210:211], v[210:211], v[78:79]\n"
    "s_add_u32 s40, s40, s43\n"
    "v_cvt_pk_f16_f32 v210, v210, v211\n"
    "v_lshl_add_u32 v106, v106, 3, s40\n"
    "ds_write_b16 v46, v210 offset:1056\n"
    "s_movk_i32 s41, 160\n"
    "v_add_u32 v107, v104, v105\n"
    "ds_write_b16_d16_hi v47, v210 offset:1056\n"
    "v_and_b32 v107, 3, v107\n"
    "v_pk_mul_f32 v[212:213], v[212:213], v[80:81]\n"
    "v_mad_u32_u24 v4, v107, s41, v106\n"
    "v_cvt_pk_f16_f32 v212, v212, v213\n"
    "v_cmp_gt_u32 vcc, 2, v105\n"
    "ds_write_b16 v48, v212 offset:1056\n"
    "v_add_u32 v108, 22, v105\n"
    "v_lshlrev_b32 v105, 3, v105\n"
    "ds_write_b16_d16_hi v49, v212 offset:1056\n"
    "v_cndmask_b32 v107, v108, v107, vcc\n"
    "v_pk_mul_f32 v[214:215], v[214:215], v[82:83]\n"
    "v_mad_u32_u24 v3, v107, s41, v106\n"
    "v_cvt_pk_f16_f32 v214, v214, v215\n"
    "v_cmp_eq_u32 vcc, 3, v104\n"
    "v_xor_b32 v107, 0, v104\n"
    "ds_write_b16 v50, v214 offset:1056\n"
    "v_and_b32 v107, 1, v107\n"
    "ds_write_b16_d16_hi v51, v214 offset:1056\n"
    "v_lshlrev_b32 v107, 1, v107\n"
    "v_pk_mul_f32 v[216:217], v[216:217], v[84:85]\n"
    "v_lshl_add_u32 v107, v104, 3, v107\n"
    "v_cvt_pk_f16_f32 v216, v216, v217\n"
    "s_mov_b32 s40, 0x05040100\n"
    "v_bfe_u32 v108, s40, v105, 8\n"
    "ds_write_b16 v52, v216 offset:1056\n"
    "v_add_u32 v107, v107, v108\n"
    "ds_write_b16_d16_hi v53, v216 offset:1056\n"
    "s_mov_b32 s40, 0x1a191818\n"
    "v_pk_mul_f32 v[218:219], v[218:219], v[86:87]\n"
    "v_bfe_u32 v108, s40, v105, 8\n"
    "v_cndmask_b32 v107, v107, v108, vcc\n"
    "v_cvt_pk_f16_f32 v218, v218, v219\n"
    "v_mad_u32_u24 v5, v107, s41, v106\n"
    "ds_write_b16 v54, v218 offset:1056\n"
    "v_xor_b32 v107, 1, v104\n"
    "ds_write_b16_d16_hi v55, v218 offset:1056\n"
    "v_and_b32 v107, 1, v107\n"
    "v_pk_mul_f32 v[220:221], v[220:221], v[88:89]\n"
    "v_lshlrev_b32 v107, 1, v107\n"
    "v_lshl_add_u32 v107, v104, 3, v107\n"
    "v_cvt_pk_f16_f32 v220, v220, v221\n"
    "s_mov_b32 s40, 0x05040100\n"
    "ds_write_b16 v56, v220 offset:1056\n"
    "v_bfe_u32 v108, s40, v105, 8\n"
    "ds_write_b16_d16_hi v57, v220 offset:1056\n"
    "v_add_u32 v107, v107, v108\n"
    "s_mov_b32 s40, 0x1a191918\n"
    "v_pk_mul_f32 v[224:225], v[224:225], v[76:77]\n"
    "v_bfe_u32 v108, s40, v105, 8\n"
    "v_cvt_pk_f16_f32 v224, v224, v225\n"
    "v_cndmask_b32 v107, v107, v108, vcc\n"
    "ds_write_b16 v44, v224 offset:2096\n"
    "v_mad_u32_u24 v6, v107, s41, v106\n"
    "ds_write_b16_d16_hi v45, v224 offset:2096\n"
    "v_bfrev_b32 v14, v104\n"
    "v_lshrrev_b32 v14, 26, v14\n"
    "v_pk_mul_f32 v[226:227], v[226:227], v[78:79]\n"
    "s_add_u32 s40, s22, 3\n"
    "v_cvt_pk_f16_f32 v226, v226, v227\n"
    "s_and_b32 s40, s40, 3\n"
    "ds_write_b16 v46, v226 offset:2096\n"
    "s_lshl2_add_u32 s40, s40, s22\n"
    "s_add_u32 s40, s40, s58\n"
    "ds_write_b16_d16_hi v47, v226 offset:2096\n"
    "s_lshl_b32 s40, s40, 2\n"
    "v_pk_mul_f32 v[228:229], v[228:229], v[80:81]\n"
    "s_add_u32 s40, s40, -450048\n"
    "v_cvt_pk_f16_f32 v228, v228, v229\n"
    "v_add_u32 v14, s40, v14\n"
    "ds_write_b16 v48, v228 offset:2096\n"
    "v_and_b32 v2, 1, v1\n"
    "v_mul_u32_u24 v2, 0xaaaa, v2\n"
    "ds_write_b16_d16_hi v49, v228 offset:2096\n"
    "v_add_u32 v2, 0x4444, v2\n"
    "v_pk_mul_f32 v[230:231], v[230:231], v[82:83]\n"
    "v_mad_u32_u24 v15, v17, 40, v19\n"
    "v_cvt_pk_f16_f32 v230, v230, v231\n"
    "v_mad_u32_u24 v16, v18, 40, v20\n"
    "s_movk_i32 s41, 4480\n"
    "ds_write_b16 v50, v230 offset:2096\n"
    "v_lshrrev_b32 v104, 5, v19\n"
    "ds_write_b16_d16_hi v51, v230 offset:2096\n"
    "v_sub_u32 v104, 1, v104\n"
    "v_pk_mul_f32 v[232:233], v[232:233], v[84:85]\n"
    "v_and_b32 v104, 3, v104\n"
    "v_cvt_pk_f16_f32 v232, v232, v233\n"
    "v_add_u32 v104, s22, v104\n"
    "v_add_u32 v15, s43, v15\n"
    "ds_write_b16 v52, v232 offset:2096\n"
    "v_xor_b32 v105, 4, v104\n"
    "ds_write_b16_d16_hi v53, v232 offset:2096\n"
    "v_mad_u32_u24 v0, v105, s41, v15\n"
    "v_pk_mul_f32 v[234:235], v[234:235], v[86:87]\n"
    "v_mad_u32_u24 v15, v104, s41, v15\n"
    "v_add_u32 v0, v0, v15\n"
    "v_cvt_pk_f16_f32 v234, v234, v235\n"
    "v_lshrrev_b32 v104, 5, v20\n"
    "ds_write_b16 v54, v234 offset:2096\n"
    "v_sub_u32 v104, 1, v104\n"
    "ds_write_b16_d16_hi v55, v234 offset:2096\n"
    "v_and_b32 v104, 3, v104\n"
    "v_pk_mul_f32 v[236:237], v[236:237], v[88:89]\n"
    "v_add_u32 v104, s22, v104\n"
    "v_add_u32 v16, s43, v16\n"
    "v_cvt_pk_f16_f32 v236, v236, v237\n"
    "v_xor_b32 v105, 4, v104\n"
    "ds_write_b16 v56, v236 offset:2096\n"
    "v_mad_u32_u24 v23, v105, s41, v16\n"
    "ds_write_b16_d16_hi v57, v236 offset:2096\n"
    "v_mad_u32_u24 v16, v104, s41, v16\n"
    "v_add_u32 v23, v23, v16\n"
    "s_cmp_eq_u32 s24, 1\n"
    "s_cbranch_scc1 .Lxd_on51\n"
    ".Lxd_on51r:\n"
    "s_sub_u32 s43, 514, s47\n"
    "s_lshl_b32 s43, s43, 22\n"
    "v_cvt_pkrtz_f16_f32 v12, s43, s43\n"
    "v_lshl_or_b32 v21, v12, 17, s47\n"
    "s_waitcnt lgkmcnt(15)\n"
    "v_alignbit_b32 v21, v41, v21, 1\n"
    "s_mov_b64 exec, s[48:49]\n"
    "ds_write_b128 v15, v[28:31] offset:0\n"
    "ds_write_b128 v15, v[32:35] offset:160\n"
    "ds_write_b128 v15, v[36:39] offset:320\n"
    "ds_write_b32 v16, v40 offset:3840\n"
    "ds_write_b32 v16, v12 offset:4160\n"
    "ds_write_b32 v25, v21 offset:532\n"
    "s_mov_b64 exec, -1\n"
    "v_add_u32 v98, v98, v99\n"
    "v_mov_b32 v97, 140944\n"
    "v_subrev_u32 v98, 818, v98\n"
    "ds_write_b32 v97, v98\n"
    "s_cmp_eq_u32 s20, 7\n"
    "s_cbranch_scc1 .Lxd_on53\n"
    ".Lxd_on53r:\n"
    "s_cmp_lt_u32 s22, 2\n"
    "s_cselect_b32 s40, 16, 0\n"
    "v_add_u32 v25, s40, v25\n"
    "s_mov_b64 s[48:49], 0\n"
    "s_add_u32 s23, s23, 4\n"
    "v_mov_b32 v24, s23\n"
    "v_add_u32 v42, 98304, v42\n"
    "v_mov_b32 v13, s58\n"
    "s_mov_b32 s36, 0\n"
    "s_sub_u32 s40, 1, s22\n"
    "s_and_b32 s40, s40, 3\n"
    "s_mul_i32 s40, s40, 4480\n"
    "v_add_u32 v7, s40, v3\n"
    "v_add_u32 v8, s40, v4\n"
    "v_add_u32 v9, s40, v5\n"
    "v_add_u32 v10, s40, v6\n"
    "s_lshr_b32 s66, s22, 1\n"
    "s_cmp_eq_u32 s22, 0\n"
    "s_cbranch_scc1 .Lxd_on54\n"
    ".Lxd_on54r:\n"
    "s_waitcnt lgkmcnt(0)\n"
    "s_barrier\n"
    "s_cmp_eq_u32 s66, 1\n"
    "s_cbranch_scc1 .Lxd_on55\n"
    ".Lxd_on55r:\n"
    "s_cmp_eq_u32 s2, 0\n"
    "s_cbranch_scc1 .Lxd_on56\n"
    ".Lxd_on56r:\n"
    "ds_read_b32 v22, v13 offset:512\n"
    "ds_read_b64_tr_b16 v[44:45], v9 offset:0\n"
    "ds_read_b64_tr_b16 v[46:47], v10 offset:0\n"
    "ds_read_b64_tr_b16 v[48:49], v7 offset:0\n"
    "ds_read_b64_tr_b16 v[50:51], v8 offset:640\n"
    "ds_read_b64_tr_b16 v[52:53], v8 offset:1280\n"
    "ds_read_b64_tr_b16 v[54:55], v8 offset:1920\n"
    "ds_read_b64_tr_b16 v[56:57], v8 offset:2560\n"
    "ds_read_b64_tr_b16 v[58:59], v8 offset:3200\n"
    "ds_read_b128 v[92:95], v42 offset:0\n"
    "ds_read_b128 v[96:99], v42 offset:1040\n"
    "ds_read_b128 v[100:103], v42 offset:2080\n"
    "ds_read_b128 v[104:107], v42 offset:3120\n"
    "ds_read_b128 v[108:111], v42 offset:4160\n"
    "ds_read_b128 v[112:115], v42 offset:5200\n"
    "ds_read_b128 v[116:119], v42 offset:6240\n"
    "ds_read_b128 v[120:123], v42 offset:7280\n"
    "ds_read_b128 v[124:127], v42 offset:8320\n"
    "ds_read_b128 v[128:131], v42 offset:9360\n"
    "ds_read_b128 v[132:135], v42 offset:10400\n"
    "ds_read_b128 v[136:139], v42 offset:11440\n"
    "ds_read_b128 v[140:143], v42 offset:12480\n"
    "ds_read_b128 v[144:147], v42 offset:13520\n"
    "ds_read_b128 v[148:151], v42 offset:14560\n"
    "ds_read_b128 v[152:155], v42 offset:15600\n"
    "ds_read_b128 v[156:159], v42 offset:16640\n"
    "ds_read_b128 v[160:163], v42 offset:17680\n"
    "ds_read_b128 v[164:167], v42 offset:18720\n"
    "ds_read_b128 v[168:171], v42 offset:19760\n"
    "ds_read_b128 v[172:175], v42 offset:20800\n"
    "ds_read_b128 v[176:179], v42 offset:21840\n"
    "ds_read_b128 v[180:183], v42 offset:22880\n"
    "ds_read_b128 v[184:187], v42 offset:23920\n"
    "ds_read_b128 v[188:191], v42 offset:24960\n"
    "ds_read_b128 v[192:195], v42 offset:26000\n"
    "ds_read_b128 v[196:199], v42 offset:27040\n"
    "ds_read_b128 v[200:203], v42 offset:28080\n"
    "ds_read_b128 v[204:207], v42 offset:29120\n"
    "ds_read_b128 v[208:211], v42 offset:30160\n"
    "ds_read_b128 v[212:215], v42 offset:31200\n"
    "ds_read_b128 v[216:219], v42 offset:32240\n"
    "ds_read_b128 v[220:223], v42 offset:33280\n"
    "ds_read_b128 v[224:227], v42 offset:34320\n"
    "ds_read_b128 v[228:231], v42 offset:35360\n"
    "ds_read_b128 v[232:235], v42 offset:36400\n"
    "ds_read_b128 v[236:239], v42 offset:37440\n"
    "ds_read_b128 v[240:243], v42 offset:38480\n"
    "ds_read_b128 v[244:247], v42 offset:39520\n"
    "ds_read_b128 v[248:251], v42 offset:40560\n"
    "ds_read_b128 v[252:255], v42 offset:41600\n"
    "v_mov_b64 v[88:89], 0\n"
    "v_mov_b64 v[90:91], 0\n"
    "s_waitcnt lgkmcnt(15)\n"
    "v_readfirstlane_b32 s45, v22\n"
    "v_pk_mul_f16 v64, v44, v46\n"
    "v_pk_mul_f16 v65, v45, v47\n"
    "v_pk_mul_f16 v66, v48, v48 op_sel_hi:[0,1]\n"
    "v_pk_mul_f16 v67, v48, v49 op_sel_hi:[0,1]\n"
    "v_pk_mul_f16 v60, v44, s45 op_sel:[0,1]\n"
    "v_pk_mul_f16 v61, v45, s45 op_sel:[0,1]\n"
    "v_pk_mul_f16 v62, v46, s45 op_sel:[0,1]\n"
    "v_pk_mul_f16 v63, v47, s45 op_sel:[0,1]\n"
    "s_waitcnt lgkmcnt(0)\n"
    "s_branch .Lxd_enter\n"
    ".Lxd_slow:\n"
    "s_mov_b32 s59, 0x100000\n"
    "s_cmp_lg_u32 s45, 0\n"
    "s_cbranch_scc1 .Lxd_sl2\n"
    ".Lxd_sl1:\n"
    "s_sleep 1\n"
    "ds_read_b32 v22, v13 offset:512\n"
    "s_waitcnt lgkmcnt(0)\n"
    "v_readfirstlane_b32 s45, v22\n"
    "s_sub_u32 s59, s59, 1\n"
    "s_cbranch_scc1 .Lxd_sl5\n"
    "s_cmp_eq_u32 s45, 0\n"
    "s_cbranch_scc1 .Lxd_sl1\n"
    "s_cmp_gt_i32 s45, 0\n"
    "s_cbranch_scc1 .Lxd_sl9\n"
    ".Lxd_sl2:\n"
    "s_cmp_eq_u32 s45, -1\n"
    "s_cbranch_scc1 .Lxd_sl3\n"
    "s_bitset0_b32 s45, 31\n"
    "s_branch .Lxd_sl6\n"
    ".Lxd_sl3:\n"
    "v_lshl_add_u32 v13, v1, 2, v13\n"
    "s_mov_b64 exec, 7\n"
    ".Lxd_sl4:\n"
    "ds_read_b32 v22, v13 offset:4\n"
    "s_waitcnt lgkmcnt(0)\n"
    "v_cmp_eq_u32 s[40:41], 0, v22\n"
    "v_cmp_ne_u32 s[42:43], -1, v22\n"
    "s_sub_u32 s59, s59, 1\n"
    "s_cbranch_scc1 .Lxd_sl5\n"
    "s_cmp_lg_u32 s40, 0\n"
    "s_cbranch_scc1 .Lxd_sl4\n"
    "s_cmp_eq_u32 s42, 0\n"
    "s_cbranch_scc1 .Lxd_sl5\n"
    "s_mov_b64 exec, -1\n"
    "s_mov_b32 s45, 1\n"
    "s_mov_b32 s56, 0\n"
    "s_setpc_b64 s[62:63]\n"
    ".Lxd_sl5:\n"
    "s_mov_b64 exec, -1\n"
    "s_mov_b32 s45, -1\n"
    "s_cmp_eq_u32 s56, 0\n"
    "s_cbranch_scc1 .Lxd_sl6\n"
    "s_call_b64 s[60:61], .Lxd_dsum\n"
    ".Lxd_sl6:\n"
    "s_cmp_lg_u32 s56, 0\n"
    "s_cbranch_scc1 .Lxd_sl9\n"
    "s_call_b64 s[60:61], .Lxd_gran\n"
    ".Lxd_sl9:\n"
    "s_setpc_b64 s[62:63]\n"
    ".Lxd_gran:\n"
    "s_waitcnt vmcnt(0)\n"
    "s_mov_b32 s56, 1\n"
    "s_lshl4_add_u32 s40, s22, s58\n"
    "v_lshrrev_b32 v42, 4, v1\n"
    "v_lshl_add_u32 v42, v42, 2, s40\n"
    "ds_read2_b32 v[26:27], v42 offset1:16\n"
    "v_and_b32 v42, 15, v1\n"
    "v_min_u32 v42, 12, v42\n"
    "v_lshlrev_b32 v42, 15, v42\n"
    "s_add_u32 s40, s10, 65528\n"
    "s_addc_u32 s41, s11, 0\n"
    "s_waitcnt lgkmcnt(0)\n"
    "v_max_i32 v26, 2, v26\n"
    "v_max_i32 v27, 2, v27\n"
    "v_lshl_add_u32 v43, v26, 2, v42\n"
    "v_lshl_add_u32 v26, v27, 2, v42\n"
    "global_load_dwordx2 v[42:43], v43, s[40:41] sc1\n"
    "global_load_dwordx2 v[26:27], v26, s[40:41] sc1\n"
    "s_setpc_b64 s[60:61]\n"
    ".Lxd_dsum:\n"
    "s_waitcnt vmcnt(0)\n"
    "v_mov_b32 v28, 154112\n"
    "ds_read_b64 v[30:31], v28\n"
    "v_and_b32 v32, 15, v1\n"
    "v_cmp_gt_u32 s[42:43], 13, v32\n"
    "s_waitcnt lgkmcnt(0)\n"
    "v_xor_b32 v28, v42, v30\n"
    "v_xor_b32 v29, v26, v30\n"
    "v_or_b32 v28, v28, v29\n"
    "v_and_b32 v28, 0xff, v28\n"
    "v_xor_b32 v29, v43, v31\n"
    "v_xor_b32 v32, v27, v31\n"
    "v_or3_b32 v28, v28, v29, v32\n"
    "v_cmp_ne_u32 s[40:41], 0, v28\n"
    "v_and_b32 v28, 0xffffff00, v42\n"
    "v_and_b32 v29, 0xffffff00, v26\n"
    "s_cmp_eq_u64 s[40:41], 0\n"
    "s_cbranch_scc0 .Lxd_ds1\n"
    "v_cndmask_b32 v28, 0, v28, s[42:43]\n"
    "v_cndmask_b32 v29, 0, v29, s[42:43]\n"
    "s_lshl4_add_u32 s40, s22, s58\n"
    "v_lshrrev_b32 v32, 4, v1\n"
    "v_add_f32_dpp v28, v28, v28 quad_perm:[1,0,3,2] row_mask:0xf bank_mask:0xf\n"
    "v_add_f32_dpp v29, v29, v29 quad_perm:[1,0,3,2] row_mask:0xf bank_mask:0xf\n"
    "v_lshl_add_u32 v32, v32, 2, s40\n"
    "v_add_f32_dpp v28, v28, v28 quad_perm:[2,3,0,1] row_mask:0xf bank_mask:0xf\n"
    "v_add_f32_dpp v29, v29, v29 quad_perm:[2,3,0,1] row_mask:0xf bank_mask:0xf\n"
    "s_mov_b32 s56, 2\n"
    "v_add_f32_dpp v28, v28, v28 row_half_mirror row_mask:0xf bank_mask:0xf\n"
    "v_add_f32_dpp v29, v29, v29 row_half_mirror row_mask:0xf bank_mask:0xf\n"
    "s_nop 0\n"
    "v_add_f32_dpp v28, v28, v28 row_mirror row_mask:0xf bank_mask:0xf\n"
    "v_add_f32_dpp v29, v29, v29 row_mirror row_mask:0xf bank_mask:0xf\n"
    "s_nop 0\n"
    "ds_write_b32 v32, v28 offset:1024\n"
    "ds_write_b32 v32, v29 offset:1088\n"
    ".Lxd_ds1:\n"
    "s_setpc_b64 s[60:61]\n"
    ".Lxd_mark:\n"
    "v_lshl_add_u32 v21, v1, 4, v25\n"
    "s_add_u32 s40, s58, 232\n"
    "v_mov_b32 v12, -1\n"
    "v_cmp_gt_u32 s[42:43], s40, v21\n"
    "s_mov_b32 s36, 1\n"
    "s_mov_b64 exec, s[42:43]\n"
    "ds_write2_b32 v21, v12, v12 offset0:5 offset1:133\n"
    "s_mov_b64 exec, -1\n"
    "s_setpc_b64 s[60:61]\n"
    ".Lxd_on1:\n"
    "s_and_b32 s40, s3, 0xf8\n"
    "s_mul_i32 s40, s40, 11\n"
    "s_lshr_b32 s40, s40, 3\n"
    "v_min_u32 v7, 10, v1\n"
    "v_add_lshl_u32 v7, v7, s40, 7\n"
    "s_getpc_b64 s[60:61]\n"
    ".Lxd_pc0:\n"
    "s_sub_u32 s60, s60, .Lxd_pc0-.Lxd_g0\n"
    "s_subb_u32 s61, s61, 0\n"
    "v_min_u32 v7, .Lxd_end-.Lxd_g0-4, v7\n"
    "global_load_dword v7, v7, s[60:61]\n"
    "s_branch .Lxd_on1r\n"
    ".Lxd_on2:\n"
    "s_mov_b64 exec, 7\n"
    "global_load_dwordx2 v[26:27], v11, s[10:11] sc1\n"
    "s_mov_b64 exec, -1\n"
    "s_branch .Lxd_on2r\n"
    ".Lxd_pv3:\n"
    "s_mov_b32 s59, 0x20000\n"
    ".Lxd_pv3l:\n"
    "s_sleep 2\n"
    "s_mov_b64 exec, 7\n"
    "global_load_dwordx2 v[26:27], v11, s[10:11] sc1\n"
    "s_mov_b64 exec, -1\n"
    "s_waitcnt vmcnt(0)\n"
    "v_readlane_b32 s25, v26, 2\n"
    "v_readlane_b32 s40, v26, 0\n"
    "v_readlane_b32 s41, v27, 0\n"
    "v_readlane_b32 s66, v26, 1\n"
    "v_readlane_b32 s67, v27, 1\n"
    "s_mul_i32 s42, s25, 0x9e3779b1\n"
    "s_lshr_b32 s42, s42, 24\n"
    "s_xor_b32 s42, s42, s40\n"
    "s_and_b32 s42, s42, 0xff\n"
    "s_xor_b32 s41, s41, s25\n"
    "s_xor_b32 s41, s41, 0xa5c30f96\n"
    "s_or_b32 s42, s41, s42\n"
    "s_lshr_b32 s41, s40, 8\n"
    "s_mul_i32 s40, s41, 0x9e3779b1\n"
    "s_lshr_b32 s40, s40, 24\n"
    "s_xor_b32 s41, s41, 0xa5c30f96\n"
    "s_cmp_eq_u64 s[66:67], s[40:41]\n"
    "s_cselect_b32 s42, s42, 1\n"
    "s_cmp_eq_u32 s42, 0\n"
    "s_cbranch_scc1 .Lxd_pv3r\n"
    "s_sub_u32 s59, s59, 1\n"
    "s_cbranch_scc0 .Lxd_pv3l\n"
    "s_branch .Lxd_pv3r\n"
    ".Lxd_on4:\n"
    "ds_write_b64 v13, v[26:27]\n"
    "s_mov_b64 exec, 4\n"
    "global_store_dword v11, v43, s[10:11] sc1\n"
    "s_mov_b64 exec, -1\n"
    "s_branch .Lxd_on4r\n"
    ".Lxd_on5:\n"
    "v_mov_b32 v24, 1\n"
    "s_mov_b64 exec, 1\n"
    "global_atomic_add v24, v17, v24, s[52:53] sc0 sc1\n"
    "s_mov_b64 exec, -1\n"
    "s_branch .Lxd_on5r\n"
    ".Lxd_tf6:\n"
    "s_call_b64 s[60:61], .Lxd_mark\n"
    "s_branch .Lxd_tf6r\n"
    ".Lxd_on7:\n"
    "s_waitcnt vmcnt(0)\n"
    "v_readfirstlane_b32 s40, v24\n"
    "s_or_b32 s40, s40, s44\n"
    "s_lshl1_add_u32 s41, s40, s54\n"
    "s_cmp_lt_u32 s40, 256\n"
    "s_cselect_b32 s41, s41, 2\n"
    "s_cselect_b64 s[48:49], -1, 0\n"
    "v_mov_b32 v12, s41\n"
    "s_cbranch_scc0 .Lxd_tf6\n"
    ".Lxd_tf6r:\n"
    "s_mul_i32 s41, s41, 52\n"
    "s_add_u32 s38, s4, s41\n"
    "s_addc_u32 s39, s5, 0\n"
    "ds_write_b32 v25, v12 offset:20\n"
    "global_load_dwordx3 v[36:38], v17, s[38:39] offset:-104\n"
    "global_load_dword v41, v18, s[38:39] offset:-8\n"
    "s_branch .Lxd_on7r\n"
    ".Lxd_on8:\n"
    "v_mov_b32 v24, 1\n"
    "s_mov_b64 exec, 1\n"
    "global_atomic_add v24, v17, v24, s[52:53] sc0 sc1\n"
    "s_mov_b64 exec, -1\n"
    "s_branch .Lxd_on8r\n"
    ".Lxd_ck9:\n"
    "v_mov_b32 v13, s58\n"
    "s_call_b64 s[62:63], .Lxd_slow\n"
    "s_branch .Lxd_ck9r\n"
    ".Lxd_on10:\n"
    "s_waitcnt vmcnt(0)\n"
    "v_lshl_or_b32 v36, v36, 7, v19\n"
    "v_lshl_or_b32 v37, v37, 7, v19\n"
    "v_lshl_or_b32 v38, v38, 7, v19\n"
    "v_lshl_or_b32 v41, v41, 7, v20\n"
    "global_load_dwordx4 v[28:31], v36, s[8:9]\n"
    "global_load_dwordx4 v[32:35], v37, s[8:9]\n"
    "global_load_dword v40, v41, s[8:9]\n"
    "global_load_dwordx4 v[36:39], v38, s[8:9]\n"
    "s_branch .Lxd_on10r\n"
    ".Lxd_tf11:\n"
    "s_call_b64 s[60:61], .Lxd_mark\n"
    "s_branch .Lxd_tf11r\n"
    ".Lxd_on12:\n"
    "s_waitcnt vmcnt(0)\n"
    "v_readfirstlane_b32 s40, v24\n"
    "s_or_b32 s40, s40, s44\n"
    "s_lshl1_add_u32 s41, s40, s54\n"
    "s_cmp_lt_u32 s40, 256\n"
    "s_cselect_b32 s41, s41, 2\n"
    "s_cselect_b64 s[48:49], -1, 0\n"
    "v_mov_b32 v12, s41\n"
    "s_cbranch_scc0 .Lxd_tf11\n"
    ".Lxd_tf11r:\n"
    "s_mul_i32 s41, s41, 52\n"
    "s_add_u32 s38, s4, s41\n"
    "s_addc_u32 s39, s5, 0\n"
    "ds_write_b32 v25, v12 offset:20\n"
    "v_sub_u32 v15, v0, v15\n"
    "v_sub_u32 v16, v23, v16\n"
    "global_load_dwordx3 v[36:38], v17, s[38:39] offset:-104\n"
    "global_load_dword v41, v18, s[38:39] offset:-8\n"
    "s_branch .Lxd_on12r\n"
    ".Lxd_on13:\n"
    "v_mov_b32 v24, 1\n"
    "s_mov_b64 exec, 1\n"
    "global_atomic_add v24, v17, v24, s[52:53] sc0 sc1\n"
    "s_mov_b64 exec, -1\n"
    "s_branch .Lxd_on13r\n"
    ".Lxd_on14:\n"
    "v_mov_b32 v24, 1\n"
    "s_mov_b64 exec, 1\n"
    "global_atomic_add v24, v17, v24, s[52:53] sc0 sc1\n"
    "s_mov_b64 exec, -1\n"
    "s_branch .Lxd_on14r\n"
    ".Lxd_tf15:\n"
    "s_call_b64 s[60:61], .Lxd_mark\n"
    "s_branch .Lxd_tf15r\n"
    ".Lxd_ck18:\n"
    "v_add_u32 v13, 4, v25\n"
    "s_call_b64 s[62:63], .Lxd_slow\n"
    "s_branch .Lxd_ck18r\n"
    ".Lxd_ck21:\n"
    "v_add_u32 v13, 8, v25\n"
    "s_call_b64 s[62:63], .Lxd_slow\n"
    "s_branch .Lxd_ck21r\n"
    ".Lxd_ck24:\n"
    "v_add_u32 v13, 12, v25\n"
    "s_call_b64 s[62:63], .Lxd_slow\n"
    "s_branch .Lxd_ck24r\n"
    ".Lxd_ck27:\n"
    "v_add_u32 v13, 16, v25\n"
    "s_call_b64 s[62:63], .Lxd_slow\n"
    "s_branch .Lxd_ck27r\n"
    ".Lxd_ck30:\n"
    "v_add_u32 v13, 4, v25\n"
    "s_call_b64 s[62:63], .Lxd_slow\n"
    "s_branch .Lxd_ck30r\n"
    ".Lxd_ck33:\n"
    "v_add_u32 v13, 8, v25\n"
    "s_call_b64 s[62:63], .Lxd_slow\n"
    "s_branch .Lxd_ck33r\n"
    ".Lxd_ck36:\n"
    "v_add_u32 v13, 12, v25\n"
    "s_call_b64 s[62:63], .Lxd_slow\n"
    "s_branch .Lxd_ck36r\n"
    ".Lxd_ck39:\n"
    "v_add_u32 v13, 16, v25\n"
    "s_call_b64 s[62:63], .Lxd_slow\n"
    "s_branch .Lxd_ck39r\n"
    ".Lxd_f2:\n"
    "s_mov_b32 s59, 0x20000\n"
    ".Lxd_f4:\n"
    "s_call_b64 s[60:61], .Lxd_dsum\n"
    "s_cmp_eq_u32 s56, 2\n"
    "s_cbranch_scc1 .Lxd_f3\n"
    "s_sleep 2\n"
    "s_call_b64 s[60:61], .Lxd_gran\n"
    "s_sub_u32 s59, s59, 1\n"
    "s_cbranch_scc0 .Lxd_f4\n"
    "s_waitcnt vmcnt(0)\n"
    "s_branch .Lxd_f3\n"
    ".Lxd_on40:\n"
    "v_min_u32 v12, 38, v1\n"
    "s_mov_b32 m0, 39936\n"
    "v_lshlrev_b32 v12, 2, v12\n"
    "global_load_lds_dword v12, s[30:31]\n"
    "s_branch .Lxd_on40r\n"
    ".Lxd_on41:\n"
    "s_getpc_b64 s[38:39]\n"
    "s_add_u32 s38, s38, xd_tab@rel32@lo+4\n"
    "s_addc_u32 s39, s39, xd_tab@rel32@hi+12\n"
    "s_mov_b32 m0, 40960\n"
    "s_nop 0\n"
    "global_load_lds_dwordx4 v42, s[38:39] offset:0\n"
    "global_load_lds_dwordx4 v42, s[38:39] offset:1024\n"
    "global_load_lds_dwordx4 v42, s[38:39] offset:2048\n"
    "global_load_lds_dwordx4 v42, s[38:39] offset:3072\n"
    "s_branch .Lxd_on41r\n"
    ".Lxd_on42:\n"
    "s_load_dword s25, s[10:11], 0\n"
    "v_and_b32 v12, 15, v0\n"
    "v_lshlrev_b32 v12, 7, v12\n"
    "v_add_u32 v12, 32768, v12\n"
    "v_mov_b32 v26, 114\n"
    "global_store_dword v12, v26, s[10:11] sc1\n"
    "global_load_dwordx4 v[100:103], v42, s[10:11] sc1\n"
    "s_branch .Lxd_on42r\n"
    ".Lxd_on43:\n"
    "s_mul_i32 s40, s20, 672\n"
    "s_add_u32 s40, s40, 32448\n"
    "s_add_u32 s38, s28, s40\n"
    "s_addc_u32 s39, s29, 0\n"
    "s_add_u32 m0, s35, 4096\n"
    "s_nop 0\n"
    "global_load_lds_dwordx4 v42, s[38:39] offset:0\n"
    "global_load_lds_dwordx4 v42, s[38:39] offset:1024\n"
    "s_branch .Lxd_on43r\n"
    ".Lxd_on44:\n"
    "v_mfma_f32_32x32x16_bf16 v[112:127], v[104:107], v[136:139], 0\n"
    "v_mfma_f32_32x32x16_bf16 v[112:127], v[136:139], v[104:107], v[112:127]\n"
    "v_mfma_f32_32x32x16_bf16 v[112:127], v[108:111], v[140:143], v[112:127]\n"
    "v_mfma_f32_32x32x16_bf16 v[112:127], v[140:143], v[108:111], v[112:127]\n"
    "s_branch .Lxd_on44r\n"
    ".Lxd_on45:\n"
    "s_mov_b32 s40, 0x9e3779b1\n"
    "v_mul_lo_u32 v216, v100, s40\n"
    "v_xor_b32 v217, 0xa5c30f96, v100\n"
    "v_alignbit_b32 v216, s25, v216, 24\n"
    "v_mul_lo_u32 v218, v101, s40\n"
    "v_xor_b32 v219, 0xa5c30f96, v101\n"
    "v_alignbit_b32 v218, s25, v218, 24\n"
    "v_mul_lo_u32 v220, v102, s40\n"
    "v_xor_b32 v221, 0xa5c30f96, v102\n"
    "v_alignbit_b32 v220, s25, v220, 24\n"
    "v_mul_lo_u32 v222, v103, s40\n"
    "v_xor_b32 v223, 0xa5c30f96, v103\n"
    "v_alignbit_b32 v222, s25, v222, 24\n"
    "v_lshlrev_b32 v100, 1, v42\n"
    "s_add_u32 s40, s10, 4096\n"
    "s_addc_u32 s41, s11, 0\n"
    "global_store_dwordx4 v100, v[216:219], s[40:41] sc1\n"
    "global_store_dwordx4 v100, v[220:223], s[40:41] offset:16 sc1\n"
    "s_waitcnt vmcnt(0)\n"
    "s_branch .Lxd_on45r\n"
    ".Lxd_on46:\n"
    "v_max3_f32 v11, |v112|, |v113|, v11\n"
    "v_max3_f32 v11, |v114|, |v115|, v11\n"
    "v_max3_f32 v11, |v116|, |v117|, v11\n"
    "v_max3_f32 v11, |v118|, |v119|, v11\n"
    "v_max3_f32 v11, |v120|, |v121|, v11\n"
    "v_max3_f32 v11, |v122|, |v123|, v11\n"
    "v_max3_f32 v11, |v124|, |v125|, v11\n"
    "s_branch .Lxd_on46r\n"
    ".Lxd_on47:\n"
    "v_pk_mul_f32 v[112:113], v[112:113], v[76:77]\n"
    "v_cvt_pk_f16_f32 v112, v112, v113\n"
    "ds_write_b16 v60, v112 offset:0\n"
    "ds_write_b16_d16_hi v61, v112 offset:0\n"
    "v_pk_mul_f32 v[114:115], v[114:115], v[78:79]\n"
    "v_cvt_pk_f16_f32 v114, v114, v115\n"
    "ds_write_b16 v62, v114 offset:0\n"
    "ds_write_b16_d16_hi v63, v114 offset:0\n"
    "v_pk_mul_f32 v[116:117], v[116:117], v[80:81]\n"
    "v_cvt_pk_f16_f32 v116, v116, v117\n"
    "ds_write_b16 v64, v116 offset:0\n"
    "ds_write_b16_d16_hi v65, v116 offset:0\n"
    "v_pk_mul_f32 v[118:119], v[118:119], v[82:83]\n"
    "v_cvt_pk_f16_f32 v118, v118, v119\n"
    "ds_write_b16 v66, v118 offset:0\n"
    "ds_write_b16_d16_hi v67, v118 offset:0\n"
    "v_pk_mul_f32 v[120:121], v[120:121], v[84:85]\n"
    "v_cvt_pk_f16_f32 v120, v120, v121\n"
    "ds_write_b16 v68, v120 offset:0\n"
    "ds_write_b16_d16_hi v69, v120 offset:0\n"
    "v_pk_mul_f32 v[122:123], v[122:123], v[86:87]\n"
    "v_cvt_pk_f16_f32 v122, v122, v123\n"
    "ds_write_b16 v70, v122 offset:0\n"
    "ds_write_b16_d16_hi v71, v122 offset:0\n"
    "v_pk_mul_f32 v[124:125], v[124:125], v[88:89]\n"
    "v_cvt_pk_f16_f32 v124, v124, v125\n"
    "ds_write_b16 v74, v124 offset:0\n"
    "ds_write_b16_d16_hi v73, v124 offset:0\n"
    "s_branch .Lxd_on47r\n"
    ".Lxd_on48:\n"
    "v_mfma_f32_32x32x16_bf16 v[112:127], v[104:107], v[136:139], 0\n"
    "v_mfma_f32_32x32x16_bf16 v[112:127], v[136:139], v[104:107], v[112:127]\n"
    "v_mfma_f32_32x32x16_bf16 v[112:127], v[108:111], v[140:143], v[112:127]\n"
    "v_mfma_f32_32x32x16_bf16 v[112:127], v[140:143], v[108:111], v[112:127]\n"
    "s_branch .Lxd_on48r\n"
    ".Lxd_on49:\n"
    "v_readlane_b32 s41, v95, s35\n"
    "s_nop 3\n"
    "v_writelane_b32 v124, s41, 60\n"
    "v_max3_f32 v11, |v112|, |v113|, v11\n"
    "v_max3_f32 v11, |v114|, |v115|, v11\n"
    "v_max3_f32 v11, |v116|, |v117|, v11\n"
    "v_max3_f32 v11, |v118|, |v119|, v11\n"
    "v_max3_f32 v11, |v120|, |v121|, v11\n"
    "v_max3_f32 v11, |v122|, |v123|, v11\n"
    "v_max3_f32 v11, |v124|, |v125|, v11\n"
    "s_branch .Lxd_on49r\n"
    ".Lxd_on50:\n"
    "s_lshl3_add_u32 s40, s2, 4096\n"
    "s_lshl_b32 s41, s2, 2\n"
    "v_mov_b32 v11, 40960\n"
    "v_writelane_b32 v11, s40, 0\n"
    "v_writelane_b32 v11, s41, 2\n"
    "s_mov_b64 exec, 7\n"
    "global_load_dwordx2 v[26:27], v11, s[10:11] sc1\n"
    "s_mov_b64 exec, -1\n"
    "s_branch .Lxd_on50r\n"
    ".Lxd_on51:\n"
    "v_pk_mul_f32 v[112:113], v[112:113], v[76:77]\n"
    "v_cvt_pk_f16_f32 v112, v112, v113\n"
    "ds_write_b16 v60, v112 offset:0\n"
    "ds_write_b16_d16_hi v61, v112 offset:0\n"
    "v_pk_mul_f32 v[114:115], v[114:115], v[78:79]\n"
    "v_cvt_pk_f16_f32 v114, v114, v115\n"
    "ds_write_b16 v62, v114 offset:0\n"
    "ds_write_b16_d16_hi v63, v114 offset:0\n"
    "v_pk_mul_f32 v[116:117], v[116:117], v[80:81]\n"
    "v_cvt_pk_f16_f32 v116, v116, v117\n"
    "ds_write_b16 v64, v116 offset:0\n"
    "ds_write_b16_d16_hi v65, v116 offset:0\n"
    "v_pk_mul_f32 v[118:119], v[118:119], v[82:83]\n"
    "v_cvt_pk_f16_f32 v118, v118, v119\n"
    "ds_write_b16 v66, v118 offset:0\n"
    "ds_write_b16_d16_hi v67, v118 offset:0\n"
    "v_pk_mul_f32 v[120:121], v[120:121], v[84:85]\n"
    "v_cvt_pk_f16_f32 v120, v120, v121\n"
    "ds_write_b16 v68, v120 offset:0\n"
    "ds_write_b16_d16_hi v69, v120 offset:0\n"
    "v_pk_mul_f32 v[122:123], v[122:123], v[86:87]\n"
    "v_cvt_pk_f16_f32 v122, v122, v123\n"
    "ds_write_b16 v70, v122 offset:0\n"
    "ds_write_b16_d16_hi v71, v122 offset:0\n"
    "v_pk_mul_f32 v[124:125], v[124:125], v[88:89]\n"
    "v_cvt_pk_f16_f32 v124, v124, v125\n"
    "ds_write_b16 v72, v124 offset:0\n"
    "ds_write_b16_d16_hi v73, v124 offset:0\n"
    "s_branch .Lxd_on51r\n"
    ".Lxd_pv52:\n"
    "s_mov_b32 s59, 0x20000\n"
    ".Lxd_pv52l:\n"
    "s_sleep 2\n"
    "s_mov_b64 exec, 7\n"
    "global_load_dwordx2 v[26:27], v11, s[10:11] sc1\n"
    "s_mov_b64 exec, -1\n"
    "s_waitcnt vmcnt(0)\n"
    "v_readlane_b32 s25, v26, 2\n"
    "v_readlane_b32 s40, v26, 0\n"
    "v_readlane_b32 s41, v27, 0\n"
    "s_mul_i32 s42, s25, 0x9e3779b1\n"
    "s_lshr_b32 s42, s42, 24\n"
    "s_xor_b32 s42, s42, s40\n"
    "s_and_b32 s42, s42, 0xff\n"
    "s_xor_b32 s41, s41, s25\n"
    "s_xor_b32 s41, s41, 0xa5c30f96\n"
    "s_or_b32 s42, s41, s42\n"
    "s_lshr_b32 s41, s40, 8\n"
    "s_mul_i32 s40, s41, 0x9e3779b1\n"
    "s_lshr_b32 s40, s40, 24\n"
    "s_xor_b32 s41, s41, 0xa5c30f96\n"
    "s_cmp_eq_u32 s42, 0\n"
    "s_cbranch_scc1 .Lxd_pv52r\n"
    "s_sub_u32 s59, s59, 1\n"
    "s_cbranch_scc0 .Lxd_pv52l\n"
    "s_branch .Lxd_pv52r\n"
    ".Lxd_on53:\n"
    "s_waitcnt vmcnt(0)\n"
    "v_readlane_b32 s25, v26, 2\n"
    "v_readlane_b32 s40, v26, 0\n"
    "v_readlane_b32 s41, v27, 0\n"
    "s_mul_i32 s42, s25, 0x9e3779b1\n"
    "s_lshr_b32 s42, s42, 24\n"
    "s_xor_b32 s42, s42, s40\n"
    "s_and_b32 s42, s42, 0xff\n"
    "s_xor_b32 s41, s41, s25\n"
    "s_xor_b32 s41, s41, 0xa5c30f96\n"
    "s_or_b32 s42, s41, s42\n"
    "s_lshr_b32 s41, s40, 8\n"
    "s_mul_i32 s40, s41, 0x9e3779b1\n"
    "s_lshr_b32 s40, s40, 24\n"
    "s_xor_b32 s41, s41, 0xa5c30f96\n"
    "s_cmp_eq_u32 s20, 7\n"
    "s_cselect_b32 s42, s42, 0\n"
    "s_cmp_lg_u32 s42, 0\n"
    "s_cbranch_scc1 .Lxd_pv52\n"
    ".Lxd_pv52r:\n"
    "v_mov_b32 v26, s40\n"
    "v_mov_b32 v27, s41\n"
    "v_mov_b32 v13, 154112\n"
    "v_add_u32_e64 v43, s25, 1\n"
    "ds_write_b64 v13, v[26:27]\n"
    "s_mov_b64 exec, 4\n"
    "global_store_dword v11, v43, s[10:11] sc1\n"
    "s_mov_b64 exec, -1\n"
    "s_branch .Lxd_on53r\n"
    ".Lxd_on54:\n"
    "v_readfirstlane_b32 s40, v24\n"
    "s_or_b32 s40, s40, s44\n"
    "s_lshl1_add_u32 s41, s40, s54\n"
    "s_cmp_lt_u32 s40, 256\n"
    "s_cselect_b32 s41, s41, 2\n"
    "s_cselect_b64 s[48:49], -1, 0\n"
    "v_mov_b32 v12, s41\n"
    "s_mul_i32 s41, s41, 52\n"
    "s_add_u32 s38, s4, s41\n"
    "s_addc_u32 s39, s5, 0\n"
    "ds_write_b32 v25, v12 offset:20\n"
    "v_sub_u32 v15, v0, v15\n"
    "v_sub_u32 v16, v23, v16\n"
    "global_load_dwordx3 v[36:38], v17, s[38:39] offset:-104\n"
    "global_load_dword v41, v18, s[38:39] offset:-8\n"
    "s_branch .Lxd_on54r\n"
    ".Lxd_on55:\n"
    "v_mov_b32 v24, 1\n"
    "s_mov_b64 exec, 1\n"
    "global_atomic_add v24, v17, v24, s[52:53] sc0 sc1\n"
    "s_mov_b64 exec, -1\n"
    "s_branch .Lxd_on55r\n"
    ".Lxd_on56:\n"
    "s_mul_i32 s40, s20, 5376\n"
    "v_add_u32 v12, s40, v42\n"
    "ds_read_b128 v[92:95], v12 offset:0\n"
    "ds_read_b128 v[96:99], v12 offset:1024\n"
    "ds_read_b128 v[100:103], v12 offset:2048\n"
    "ds_read_b128 v[104:107], v12 offset:3072\n"
    "ds_read_b128 v[108:111], v12 offset:4096\n"
    "ds_read_b128 v[112:115], v12 offset:4352\n"
    "v_mov_b32 v21, 154112\n"
    "ds_read_b64 v[116:117], v21\n"
    "s_add_u32 s40, s10, 951296\n"
    "s_addc_u32 s41, s11, 0\n"
    "v_mov_b32 v21, 40960\n"
    "s_waitcnt lgkmcnt(0)\n"
    "global_store_dwordx4 v12, v[92:95], s[40:41] offset:-1024 sc1\n"
    "global_store_dwordx4 v12, v[96:99], s[40:41] offset:0 sc1\n"
    "global_store_dwordx4 v12, v[100:103], s[40:41] offset:1024 sc1\n"
    "global_store_dwordx4 v12, v[104:107], s[40:41] offset:2048 sc1\n"
    "global_store_dwordx4 v12, v[108:111], s[40:41] offset:3072 sc1\n"
    "global_store_dwordx4 v12, v[112:115], s[40:41] offset:3328 sc1\n"
    "s_waitcnt vmcnt(0)\n"
    "s_barrier\n"
    "s_cmp_lg_u32 s20, 0\n"
    "s_cbranch_scc1 .Lxd_b2\n"
    "s_mov_b64 exec, 1\n"
    "global_store_dwordx2 v21, v[116:117], s[10:11] sc1\n"
    "s_mov_b64 exec, -1\n"
    "s_waitcnt vmcnt(0)\n"
    ".Lxd_b2:\n"
    "s_branch .Lxd_on56r\n"
    ".Lxd_end:\n"
    : "+{v0}"(tid) : "{s[0:1]}"(__builtin_amdgcn_kernarg_segment_ptr()), "{s2}"(blockIdx.x)
    : "memory", "vcc", "scc", "v1", "v2", "v3", "v4", "v5", "v6", "v7", "v8", "v9", "v10", "v11", "v12", "v13", "v14", "v15", "v16", "v17", "v18", "v19", "v20", "v21", "v22", "v23", "v24", "v25", "v26", "v27", "v28", "v29", "v30", "v31", "v32", "v33", "v34", "v35", "v36", "v37", "v38", "v39", "v40", "v41", "v42", "v43", "v44", "v45", "v46", "v47", "v48", "v49", "v50", "v51", "v52", "v53", "v54", "v55", "v56", "v57", "v58", "v59", "v60", "v61", "v62", "v63", "v64", "v65", "v66", "v67", "v68", "v69", "v70", "v71", "v72", "v73", "v74", "v75", "v76", "v77", "v78", "v79", "v80", "v81", "v82", "v83", "v84", "v85", "v86", "v87", "v88", "v89", "v90", "v91", "v92", "v93", "v94", "v95", "v96", "v97", "v98", "v99", "v100", "v101", "v102", "v103", "v104", "v105", "v106", "v107", "v108", "v109", "v110", "v111", "v112", "v113", "v114", "v115", "v116", "v117", "v118", "v119", "v120", "v121", "v122", "v123", "v124", "v125", "v126", "v127", "v128", "v129", "v130", "v131", "v132", "v133", "v134", "v135", "v136", "v137", "v138", "v139", "v140", "v141", "v142", "v143", "v144", "v145", "v146", "v147", "v148", "v149", "v150", "v151", "v152", "v153", "v154", "v155", "v156", "v157", "v158", "v159", "v160", "v161", "v162", "v163", "v164", "v165", "v166", "v167", "v168", "v169", "v170", "v171", "v172", "v173", "v174", "v175", "v176", "v177", "v178", "v179", "v180", "v181", "v182", "v183", "v184", "v185", "v186", "v187", "v188", "v189", "v190", "v191", "v192", "v193", "v194", "v195", "v196", "v197", "v198", "v199", "v200", "v201", "v202", "v203", "v204", "v205", "v206", "v207", "v208", "v209", "v210", "v211", "v212", "v213", "v214", "v215", "v216", "v217", "v218", "v219", "v220", "v221", "v222", "v223", "v224", "v225", "v226", "v227", "v228", "v229", "v230", "v231", "v232", "v233", "v234", "v235", "v236", "v237", "v238", "v239", "v240", "v241", "v242", "v243", "v244", "v245", "v246", "v247", "v248", "v249", "v250", "v251", "v252", "v253", "v254", "v255", "s3", "s4", "s5", "s6", "s7", "s8", "s9", "s10", "s11", "s12", "s13", "s14", "s15", "s16", "s17", "s18", "s19", "s20", "s21", "s22", "s23", "s24", "s25", "s26", "s27", "s28", "s29", "s30", "s31", "s34", "s35", "s36", "s37", "s38", "s39", "s40", "s41", "s42", "s43", "s44", "s45", "s46", "s47", "s48", "s49", "s50", "s51", "s52", "s53", "s54", "s55", "s56", "s57", "s58", "s59", "s60", "s61", "s62", "s63", "s64", "s65", "s66", "s67");
}

extern "C" void kernel_launch(void* const* d_in, const int* in_sizes, int n_in, void* d_out, int out_size,
                              void* d_ws, size_t ws_size, hipStream_t stream) {
  xd_kernel<<<256, 512, 0, stream>>>((const int*)d_in[0], (const u16*)d_in[4], (const u16*)d_in[1], (char*)d_ws, (const u16*)d_in[5], (const u16*)d_in[6],
                                     (const u16*)d_in[7], (u16*)d_out, (const u16*)d_in[2], (const u16*)d_in[3]);
}
```

```cpp
#include <hip/hip_runtime.h>
typedef unsigned short u16;

__device__ const __attribute__((aligned(16), used, visibility("hidden"))) u16 xd_tab[5120] asm("xd_tab") = {924, 925, 928, 929, 926, 927, 930, 931, 934, 935, 938, 939, 932, 933, 936, 937, 940, 941, 944, 945, 942, 943, 946, 947, 948, 0, 949, 0, 0, 0, 0, 0, 2, 36, 138, 172, 0, 1, 24, 25, 338, 372, 474, 508, 34, 35, 57, 58, 546, 580, 682, 716, 68, 69, 90, 91, 816, 817, 850, 952, 102, 99, 123, 124, 680, 681, 684, 685, 20, 21, 22, 23, 714, 715, 717, 718, 54, 55, 56, 53, 748, 749, 750, 751, 88, 89, 86, 87, 782, 779, 783, 784, 122, 119, 120, 121, 152, 153, 154, 155, 284, 285, 286, 287, 186, 187, 188, 185, 318, 319, 320, 317, 220, 221, 218, 219, 352, 353, 350, 351, 254, 251, 252, 253, 386, 383, 384, 385, 416, 417, 418, 419, 548, 549, 550, 551, 450, 451, 452, 449, 582, 583, 584, 581, 484, 485, 482, 483, 616, 617, 614, 615, 518, 515, 516, 517, 650, 647, 648, 649, 544, 545, 552, 553, 16, 17, 18, 19, 578, 579, 585, 586, 50, 51, 52, 49, 612, 613, 618, 619, 84, 85, 82, 83, 646, 643, 651, 652, 118, 115, 116, 117, 148, 149, 150, 151, 280, 281, 282, 283, 182, 183, 184, 181, 314, 315, 316, 313, 216, 217, 214, 215, 348, 349, 346, 347, 250, 247, 248, 249, 382, 379, 380, 381, 412, 413, 414, 415, 408, 409, 420, 421, 446, 447, 448, 445, 442, 443, 453, 454, 480, 481, 478, 479, 476, 477, 486, 487, 514, 511, 512, 513, 510, 507, 519, 520, 12, 13, 14, 15, 144, 145, 146, 147, 46, 47, 48, 45, 178, 179, 180, 177, 80, 81, 78, 79, 212, 213, 210, 211, 114, 111, 112, 113, 246, 243, 244, 245, 276, 277, 278, 279, 272, 273, 288, 289, 310, 311, 312, 309, 306, 307, 321, 322, 344, 345, 342, 343, 340, 341, 354, 355, 378, 375, 376, 377, 374, 371, 387, 388, 8, 9, 10, 11, 140, 141, 142, 143, 42, 43, 44, 41, 174, 175, 176, 173, 76, 77, 74, 75, 208, 209, 206, 207, 110, 107, 108, 109, 242, 239, 240, 241, 136, 137, 156, 157, 4, 5, 6, 7, 170, 171, 189, 190, 38, 39, 40, 37, 204, 205, 222, 223, 72, 73, 70, 71, 238, 235, 255, 256, 106, 103, 104, 105, 0, 0, 0, 0, 0, 0, 0, 0, 0, 0, 0, 0, 0, 0, 0, 0, 0, 0, 0, 0, 0, 0, 0, 0, 0, 0, 0, 0, 0, 0, 0, 0, 0, 0, 0, 0, 0, 0, 0, 0, 0, 0, 0, 0, 0, 0, 0, 0, 0, 0, 0, 0, 0, 0, 0, 0, 0, 0, 0, 0, 0, 0, 0, 0, 0, 0, 0, 0, 0, 0, 0, 0, 0, 0, 0, 0, 0, 0, 0, 0, 0, 0, 0, 0, 0, 0, 0, 0, 0, 0, 0, 0, 0, 0, 0, 0, 0, 0, 0, 0, 0, 0, 0, 0, 0, 0, 0, 0, 0, 0, 0, 0, 0, 0, 0, 0, 0, 0, 0, 0, 0, 0, 0, 0, 0, 0, 0, 0, 924, 925, 926, 927, 934, 935, 932, 933, 940, 941, 942, 943, 948, 0, 0, 0, 2, 36, 0, 1, 338, 372, 34, 35, 546, 580, 68, 69, 816, 817, 102, 99, 928, 929, 930, 931, 938, 939, 936, 937, 944, 945, 946, 947, 949, 0, 0, 0, 138, 172, 24, 25, 474, 508, 57, 58, 682, 716, 90, 91, 850, 952, 123, 124, 680, 681, 20, 21, 714, 715, 54, 55, 748, 749, 88, 89, 782, 779, 122, 119, 152, 153, 284, 285, 186, 187, 318, 319, 220, 221, 352, 353, 254, 251, 386, 383, 684, 685, 22, 23, 717, 718, 56, 53, 750, 751, 86, 87, 783, 784, 120, 121, 154, 155, 286, 287, 188, 185, 320, 317, 218, 219, 350, 351, 252, 253, 384, 385, 416, 417, 548, 549, 450, 451, 582, 583, 484, 485, 616, 617, 518, 515, 650, 647, 544, 545, 16, 17, 578, 579, 50, 51, 612, 613, 84, 85, 646, 643, 118, 115, 418, 419, 550, 551, 452, 449, 584, 581, 482, 483, 614, 615, 516, 517, 648, 649, 552, 553, 18, 19, 585, 586, 52, 49, 618, 619, 82, 83, 651, 652, 116, 117, 148, 149, 280, 281, 182, 183, 314, 315, 216, 217, 348, 349, 250, 247, 382, 379, 412, 413, 408, 409, 446, 447, 442, 443, 480, 481, 476, 477, 514, 511, 510, 507, 150, 151, 282, 283, 184, 181, 316, 313, 214, 215, 346, 347, 248, 249, 380, 381, 414, 415, 420, 421, 448, 445, 453, 454, 478, 479, 486, 487, 512, 513, 519, 520, 12, 13, 144, 145, 46, 47, 178, 179, 80, 81, 212, 213, 114, 111, 246, 243, 276, 277, 272, 273, 310, 311, 306, 307, 344, 345, 340, 341, 378, 375, 374, 371, 14, 15, 146, 147, 48, 45, 180, 177, 78, 79, 210, 211, 112, 113, 244, 245, 278, 279, 288, 289, 312, 309, 321, 322, 342, 343, 354, 355, 376, 377, 387, 388, 8, 9, 140, 141, 42, 43, 174, 175, 76, 77, 208, 209, 110, 107, 242, 239, 136, 137, 4, 5, 170, 171, 38, 39, 204, 205, 72, 73, 238, 235, 106, 103, 10, 11, 142, 143, 44, 41, 176, 173, 74, 75, 206, 207, 108, 109, 240, 241, 156, 157, 6, 7, 189, 190, 40, 37, 222, 223, 70, 71, 255, 256, 104, 105, 0, 0, 0, 0, 0, 0, 0, 0, 0, 0, 0, 0, 0, 0, 0, 0, 0, 0, 0, 0, 0, 0, 0, 0, 0, 0, 0, 0, 0, 0, 0, 0, 0, 0, 0, 0, 0, 0, 0, 0, 0, 0, 0, 0, 0, 0, 0, 0, 0, 0, 0, 0, 0, 0, 0, 0, 0, 0, 0, 0, 0, 0, 0, 0, 0, 0, 0, 0, 0, 0, 0, 0, 0, 0, 0, 0, 0, 0, 0, 0, 0, 0, 0, 0, 0, 0, 0, 0, 0, 0, 0, 0, 0, 0, 0, 0, 0, 0, 0, 0, 0, 0, 0, 0, 0, 0, 0, 0, 0, 0, 0, 0, 0, 0, 0, 0, 0, 0, 0, 0, 0, 0, 0, 0, 0, 0, 0, 0, 0, 0, 0, 0, 0, 0, 0, 0, 0, 0, 0, 0, 0, 0, 0, 0, 0, 0, 0, 0, 0, 0, 0, 0, 0, 0, 0, 0, 0, 0, 0, 0, 0, 16256, 0, 16256, 0, 16256, 0, 16256, 0, 16256, 0, 16256, 0, 16256, 0, 16256, 0, 16256, 0, 16256, 0, 16256, 0, 16256, 0, 16128, 0, 16256, 0, 16128, 0, 0, 0, 16128, 0, 16256, 0, 16256, 0, 16256, 0, 16128, 0, 16256, 0, 16256, 0, 16256, 0, 16128, 0, 16256, 0, 16256, 0, 16256, 0, 16128, 0, 16256, 0, 16256, 0, 16256, 0, 16256, 0, 16256, 0, 16256, 0, 16256, 0, 16256, 0, 16256, 0, 16256, 0, 16256, 0, 16256, 0, 16256, 0, 16256, 0, 16256, 0, 16256, 0, 16256, 0, 16256, 0, 16256, 0, 16256, 0, 16256, 0, 16256, 0, 16256, 0, 16256, 0, 16256, 0, 16256, 0, 16256, 0, 16256, 0, 16256, 0, 16256, 0, 16256, 0, 16256, 0, 16256, 0, 16256, 0, 16256, 0, 16128, 0, 16256, 0, 16256, 0, 16256, 0, 16128, 0, 16256, 0, 16256, 0, 16256, 0, 16128, 0, 16256, 0, 16256, 0, 16256, 0, 16128, 0, 16256, 0, 16256, 0, 16256, 0, 16256, 0, 16256, 0, 16256, 0, 16256, 0, 16256, 0, 16256, 0, 16256, 0, 16256, 0, 16256, 0, 16256, 0, 16256, 0, 16256, 0, 16256, 0, 16256, 0, 16256, 0, 16256, 0, 16256, 0, 16256, 0, 16256, 0, 16256, 0, 16256, 0, 16256, 0, 16256, 0, 16256, 0, 16256, 0, 16256, 0, 16256, 0, 16256, 0, 16256, 0, 16256, 0, 16256, 0, 16256, 0, 16256, 0, 16256, 0, 16256, 0, 16256, 0, 16256, 0, 16256, 0, 16256, 0, 16256, 0, 16256, 0, 16256, 0, 16256, 0, 16256, 0, 16256, 0, 16256, 0, 16256, 0, 16256, 0, 16256, 0, 16256, 0, 16256, 0, 16256, 0, 16256, 0, 16256, 0, 16256, 0, 16256, 0, 16256, 0, 16256, 0, 16256, 0, 16256, 0, 16256, 0, 16256, 0, 16256, 0, 16256, 0, 16256, 0, 16256, 0, 16256, 0, 16256, 0, 16256, 0, 16256, 0, 16256, 0, 16256, 0, 16256, 0, 16256, 0, 16256, 0, 16256, 0, 16256, 0, 16256, 0, 16256, 0, 16256, 0, 16128, 0, 16256, 0, 16256, 0, 16256, 0, 16128, 0, 16256, 0, 16256, 0, 16256, 0, 16128, 0, 16256, 0, 16256, 0, 16256, 0, 16128, 0, 16256, 0, 16256, 0, 16256, 0, 0, 0, 0, 0, 0, 0, 0, 0, 0, 0, 0, 0, 0, 0, 0, 0, 0, 0, 0, 0, 0, 0, 0, 0, 0, 0, 0, 0, 0, 0, 0, 0, 0, 0, 0, 0, 0, 0, 0, 0, 0, 0, 0, 0, 0, 0, 0, 0, 0, 0, 0, 0, 0, 0, 0, 0, 0, 0, 0, 0, 0, 0, 0, 0, 0, 0, 0, 0, 0, 0, 0, 0, 0, 0, 0, 0, 0, 0, 0, 0, 0, 0, 0, 0, 0, 0, 0, 0, 0, 0, 0, 0, 0, 0, 0, 0, 0, 0, 0, 0, 0, 0, 0, 0, 0, 0, 0, 0, 0, 0, 0, 0, 0, 0, 0, 0, 0, 0, 0, 0, 0, 0, 0, 0, 0, 0, 0, 0, 0, 0, 0, 0, 0, 0, 0, 0, 0, 0, 0, 0, 0, 0, 0, 0, 0, 0, 0, 0, 0, 0, 0, 0, 0, 0, 0, 0, 0, 0, 0, 0, 16128, 0, 16256, 0, 16256, 0, 16256, 0, 16128, 0, 16256, 0, 16256, 0, 16256, 0, 16128, 0, 16256, 0, 16256, 0, 16256, 0, 16128, 0, 16256, 0, 16256, 0, 16256, 0, 16256, 0, 16256, 0, 16256, 0, 16256, 0, 16256, 0, 16256, 0, 16256, 0, 16256, 0, 16256, 0, 16256, 0, 16256, 0, 16256, 0, 16256, 0, 16256, 0, 16256, 0, 16256, 0, 16256, 0, 16256, 0, 16256, 0, 16256, 0, 16256, 0, 16256, 0, 16256, 0, 16256, 0, 16256, 0, 16256, 0, 16256, 0, 16256, 0, 16256, 0, 16256, 0, 16256, 0, 16256, 0, 16256, 0, 16256, 0, 16256, 0, 16256, 0, 16256, 0, 16256, 0, 16256, 0, 16256, 0, 16256, 0, 16256, 0, 16256, 0, 16256, 0, 16256, 0, 16256, 0, 16256, 0, 16256, 0, 16256, 0, 16256, 0, 16256, 0, 16256, 0, 16256, 0, 16256, 0, 16256, 0, 16256, 0, 16256, 0, 16256, 0, 16256, 0, 16256, 0, 16256, 0, 16256, 0, 16256, 0, 16256, 0, 16256, 0, 16256, 0, 16256, 0, 16256, 0, 16256, 0, 16256, 0, 16256, 0, 16256, 0, 16256, 0, 16256, 0, 16256, 0, 16256, 0, 16256, 0, 16256, 0, 16256, 0, 16256, 0, 16128, 0, 16256, 0, 16256, 0, 16256, 0, 16128, 0, 16256, 0, 16256, 0, 16256, 0, 16128, 0, 16256, 0, 16256, 0, 16256, 0, 16128, 0, 16256, 0, 16256, 0, 16256, 0, 16256, 0, 16256, 0, 16256, 0, 16256, 0, 16256, 0, 16256, 0, 16256, 0, 16256, 0, 16256, 0, 16256, 0, 16256, 0, 16256, 0, 16256, 0, 16256, 0, 16256, 0, 16256, 0, 16128, 0, 16256, 0, 16256, 0, 16256, 0, 16128, 0, 16256, 0, 16256, 0, 16256, 0, 16128, 0, 16256, 0, 16256, 0, 16256, 0, 16128, 0, 16256, 0, 16256, 0, 16256, 0, 16256, 0, 16256, 0, 16256, 0, 16256, 0, 16256, 0, 16256, 0, 16256, 0, 16256, 0, 16256, 0, 16256, 0, 16256, 0, 16256, 0, 16256, 0, 16256, 0, 16256, 0, 16256, 0, 16256, 0, 16256, 0, 16256, 0, 16256, 0, 16256, 0, 16256, 0, 16256, 0, 16256, 0, 16256, 0, 16256, 0, 16256, 0, 16256, 0, 16256, 0, 16256, 0, 16256, 0, 16256, 0, 0, 0, 0, 0, 0, 0, 0, 0, 0, 0, 0, 0, 0, 0, 0, 0, 0, 0, 0, 0, 0, 0, 0, 0, 0, 0, 0, 0, 0, 0, 0, 0, 0, 0, 0, 0, 0, 0, 0, 0, 0, 0, 0, 0, 0, 0, 0, 0, 0, 0, 0, 0, 0, 0, 0, 0, 0, 0, 0, 0, 0, 0, 0, 0, 0, 0, 0, 0, 0, 0, 0, 0, 0, 0, 0, 0, 0, 0, 0, 0, 0, 0, 0, 0, 0, 0, 0, 0, 0, 0, 0, 0, 0, 0, 0, 0, 0, 0, 0, 0, 0, 0, 0, 0, 0, 0, 0, 0, 0, 0, 0, 0, 0, 0, 0, 0, 0, 0, 0, 0, 0, 0, 0, 0, 0, 0, 0, 0, 16768, 0, 16768, 0, 16768, 0, 16768, 0, 16768, 0, 16768, 0, 16768, 0, 16768, 0, 16768, 0, 16768, 0, 16768, 0, 16768, 0, 16768, 0, 0, 0, 16768, 0, 0, 0, 16256, 0, 16256, 0, 16256, 0, 16256, 0, 16256, 0, 16256, 0, 16256, 0, 16256, 0, 16256, 0, 16256, 0, 16256, 0, 16256, 0, 16128, 0, 16256, 0, 16128, 0, 17280, 0, 16128, 0, 16256, 0, 16256, 0, 16256, 0, 16128, 0, 16256, 0, 16256, 0, 16256, 0, 16128, 0, 16256, 0, 16256, 0, 16256, 0, 16128, 0, 16256, 0, 16256, 0, 16256, 0, 16256, 0, 16256, 0, 16256, 0, 16256, 0, 16256, 0, 16256, 0, 16256, 0, 16256, 0, 16256, 0, 16256, 0, 16256, 0, 16256, 0, 16256, 0, 16256, 0, 16256, 0, 16256, 0, 16256, 0, 16256, 0, 16256, 0, 16256, 0, 16256, 0, 16256, 0, 16256, 0, 16256, 0, 16256, 0, 16256, 0, 16256, 0, 16256, 0, 16256, 0, 16256, 0, 16256, 0, 16256, 0, 16128, 0, 16256, 0, 16256, 0, 16256, 0, 16128, 0, 16256, 0, 16256, 0, 16256, 0, 16128, 0, 16256, 0, 16256, 0, 16256, 0, 16128, 0, 16256, 0, 16256, 0, 16256, 0, 16256, 0, 16256, 0, 16256, 0, 16256, 0, 16256, 0, 16256, 0, 16256, 0, 16256, 0, 16256, 0, 16256, 0, 16256, 0, 16256, 0, 16256, 0, 16256, 0, 16256, 0, 16256, 0, 16256, 0, 16256, 0, 16256, 0, 16256, 0, 16256, 0, 16256, 0, 16256, 0, 16256, 0, 16256, 0, 16256, 0, 16256, 0, 16256, 0, 16256, 0, 16256, 0, 16256, 0, 16256, 0, 16256, 0, 16256, 0, 16256, 0, 16256, 0, 16256, 0, 16256, 0, 16256, 0, 16256, 0, 16256, 0, 16256, 0, 16256, 0, 16256, 0, 16256, 0, 16256, 0, 16256, 0, 16256, 0, 16256, 0, 16256, 0, 16256, 0, 16256, 0, 16256, 0, 16256, 0, 16256, 0, 16256, 0, 16256, 0, 16256, 0, 16256, 0, 16256, 0, 16256, 0, 16256, 0, 16256, 0, 16256, 0, 16256, 0, 16256, 0, 16256, 0, 16256, 0, 16256, 0, 16256, 0, 16256, 0, 16256, 0, 16256, 0, 16256, 0, 16256, 0, 16256, 0, 16256, 0, 16256, 0, 16256, 0, 16256, 0, 16128, 0, 16256, 0, 16256, 0, 16256, 0, 16128, 0, 16256, 0, 16256, 0, 16256, 0, 16128, 0, 16256, 0, 16256, 0, 16256, 0, 16128, 0, 16256, 0, 16256, 0, 16256, 0, 0, 0, 0, 0, 0, 0, 0, 0, 0, 0, 0, 0, 0, 0, 0, 0, 0, 0, 0, 0, 0, 0, 0, 0, 0, 0, 0, 0, 0, 0, 0, 0, 0, 0, 0, 0, 0, 0, 0, 0, 0, 0, 0, 0, 0, 0, 0, 0, 0, 0, 0, 0, 0, 0, 0, 0, 0, 0, 0, 0, 0, 0, 0, 0, 0, 0, 0, 0, 0, 0, 0, 0, 0, 0, 0, 0, 0, 0, 0, 0, 0, 0, 0, 0, 0, 0, 0, 0, 0, 0, 0, 0, 0, 0, 0, 0, 0, 0, 0, 0, 0, 0, 0, 0, 0, 0, 0, 0, 0, 0, 0, 0, 0, 0, 0, 0, 0, 0, 0, 0, 0, 0, 0, 0, 0, 0, 0, 0, 16768, 0, 16768, 0, 16768, 0, 16768, 0, 16768, 0, 16768, 0, 16768, 0, 16768, 0, 16768, 0, 16768, 0, 16768, 0, 16768, 0, 0, 0, 0, 0, 0, 0, 0, 0, 16128, 0, 16256, 0, 16256, 0, 16256, 0, 16128, 0, 16256, 0, 16256, 0, 16256, 0, 16128, 0, 16256, 0, 16256, 0, 16256, 0, 16128, 0, 16256, 0, 16256, 0, 16256, 0, 16256, 0, 16256, 0, 16256, 0, 16256, 0, 16256, 0, 16256, 0, 16256, 0, 16256, 0, 16256, 0, 16256, 0, 16256, 0, 16256, 0, 16256, 0, 16256, 0, 16256, 0, 16256, 0, 16256, 0, 16256, 0, 16256, 0, 16256, 0, 16256, 0, 16256, 0, 16256, 0, 16256, 0, 16256, 0, 16256, 0, 16256, 0, 16256, 0, 16256, 0, 16256, 0, 16256, 0, 16256, 0, 16256, 0, 16256, 0, 16256, 0, 16256, 0, 16256, 0, 16256, 0, 16256, 0, 16256, 0, 16256, 0, 16256, 0, 16256, 0, 16256, 0, 16256, 0, 16256, 0, 16256, 0, 16256, 0, 16256, 0, 16256, 0, 16256, 0, 16256, 0, 16256, 0, 16256, 0, 16256, 0, 16256, 0, 16256, 0, 16256, 0, 16256, 0, 16256, 0, 16256, 0, 16256, 0, 16256, 0, 16256, 0, 16256, 0, 16256, 0, 16256, 0, 16256, 0, 16256, 0, 16256, 0, 16256, 0, 16256, 0, 16256, 0, 16256, 0, 16256, 0, 16256, 0, 16256, 0, 16256, 0, 16256, 0, 16256, 0, 16128, 0, 16256, 0, 16256, 0, 16256, 0, 16128, 0, 16256, 0, 16256, 0, 16256, 0, 16128, 0, 16256, 0, 16256, 0, 16256, 0, 16128, 0, 16256, 0, 16256, 0, 16256, 0, 16256, 0, 16256, 0, 16256, 0, 16256, 0, 16256, 0, 16256, 0, 16256, 0, 16256, 0, 16256, 0, 16256, 0, 16256, 0, 16256, 0, 16256, 0, 16256, 0, 16256, 0, 16256, 0, 16128, 0, 16256, 0, 16256, 0, 16256, 0, 16128, 0, 16256, 0, 16256, 0, 16256, 0, 16128, 0, 16256, 0, 16256, 0, 16256, 0, 16128, 0, 16256, 0, 16256, 0, 16256, 0, 16256, 0, 16256, 0, 16256, 0, 16256, 0, 16256, 0, 16256, 0, 16256, 0, 16256, 0, 16256, 0, 16256, 0, 16256, 0, 16256, 0, 16256, 0, 16256, 0, 16256, 0, 16256, 0, 16256, 0, 16256, 0, 16256, 0, 16256, 0, 16256, 0, 16256, 0, 16256, 0, 16256, 0, 16256, 0, 16256, 0, 16256, 0, 16256, 0, 16256, 0, 16256, 0, 16256, 0, 16256, 0, 0, 0, 0, 0, 0, 0, 0, 0, 0, 0, 0, 0, 0, 0, 0, 0, 0, 0, 0, 0, 0, 0, 0, 0, 0, 0, 0, 0, 0, 0, 0, 0, 0, 0, 0, 0, 0, 0, 0, 0, 0, 0, 0, 0, 0, 0, 0, 0, 0, 0, 0, 0, 0, 0, 0, 0, 0, 0, 0, 0, 0, 0, 0, 0, 0, 0, 0, 0, 0, 0, 0, 0, 0, 0, 0, 0, 0, 0, 0, 0, 0, 0, 0, 0, 0, 0, 0, 0, 0, 0, 0, 0, 0, 0, 0, 0, 0, 0, 0, 0, 0, 0, 0, 0, 0, 0, 0, 0, 0, 0, 0, 0, 0, 0, 0, 0, 0, 0, 0, 0, 0, 0, 0, 0, 0, 0, 0, 0, 0, 0, 0, 0, 0, 0, 0, 0, 0, 0, 0, 0, 0, 0, 0, 0, 16256, 0, 16256, 0, 16128, 0, 16256, 0, 16256, 0, 16256, 0, 16128, 0, 16256, 0, 0, 0, 0, 0, 0, 0, 0, 0, 0, 0, 0, 0, 0, 0, 0, 0, 16256, 0, 16256, 0, 16256, 0, 16256, 0, 16256, 0, 16256, 0, 16256, 0, 16256, 0, 16128, 0, 16256, 0, 16256, 0, 16256, 0, 16128, 0, 16256, 0, 16256, 0, 16256, 0, 16256, 0, 16256, 0, 16256, 0, 16256, 0, 16256, 0, 16256, 0, 16256, 0, 16256, 0, 16256, 0, 16256, 0, 16256, 0, 16256, 0, 16256, 0, 16256, 0, 16256, 0, 16256, 0, 16256, 0, 16256, 0, 16256, 0, 16256, 0, 16256, 0, 16256, 0, 16256, 0, 16256, 0, 16256, 0, 16256, 0, 16256, 0, 16256, 0, 16256, 0, 16256, 0, 16256, 0, 16256, 0, 16128, 0, 16256, 0, 16256, 0, 16256, 0, 16128, 0, 16256, 0, 16256, 0, 16256, 0, 16256, 0, 16256, 0, 16256, 0, 16256, 0, 16256, 0, 16256, 0, 16256, 0, 16256, 0, 16256, 0, 16256, 0, 16256, 0, 16256, 0, 16256, 0, 16256, 0, 16256, 0, 16256, 0, 16256, 0, 16256, 0, 16256, 0, 16256, 0, 16256, 0, 16256, 0, 16256, 0, 16256, 0, 16256, 0, 16256, 0, 16128, 0, 16256, 0, 16256, 0, 16256, 0, 16128, 0, 16256, 0, 16256, 0, 16256, 0, 16256, 0, 16256, 0, 16256, 0, 16256, 0, 16256, 0, 16256, 0, 16256, 0, 16256, 0, 16256, 0, 16256, 0, 16256, 0, 16256, 0, 16256, 0, 16256, 0, 16256, 0, 16256, 0, 16256, 0, 16256, 0, 16256, 0, 16256, 0, 16256, 0, 16256, 0, 16256, 0, 16256, 0, 16128, 0, 16256, 0, 16256, 0, 16256, 0, 16128, 0, 16256, 0, 16256, 0, 16256, 0, 16256, 0, 16256, 0, 16256, 0, 16256, 0, 16256, 0, 16256, 0, 16256, 0, 16256, 0, 16256, 0, 16256, 0, 16256, 0, 16256, 0, 16256, 0, 16256, 0, 16256, 0, 16256, 0, 16256, 0, 16256, 0, 16256, 0, 16256, 0, 16256, 0, 16256, 0, 16128, 0, 16256, 0, 16256, 0, 16256, 0, 16128, 0, 16256, 0, 16256, 0, 16256, 0, 16256, 0, 16256, 0, 16256, 0, 16256, 0, 16256, 0, 16256, 0, 16256, 0, 16256, 0, 16256, 0, 16256, 0, 16256, 0, 16256, 0, 16256, 0, 16256, 0, 16256, 0, 16256, 0, 0, 0, 0, 0, 0, 0, 0, 0, 0, 0, 0, 0, 0, 0, 0, 0, 0, 0, 0, 0, 0, 0, 0, 0, 0, 0, 0, 0, 0, 0, 0, 0, 0, 0, 0, 0, 0, 0, 0, 0, 0, 0, 0, 0, 0, 0, 0, 0, 0, 0, 0, 0, 0, 0, 0, 0, 0, 0, 0, 0, 0, 0, 0, 0, 0, 0, 0, 0, 0, 0, 0, 0, 0, 0, 0, 0, 0, 0, 0, 0, 0, 0, 0, 0, 0, 0, 0, 0, 0, 0, 0, 0, 0, 0, 0, 0, 0, 0, 0, 0, 0, 0, 0, 0, 0, 0, 0, 0, 0, 0, 0, 0, 0, 0, 0, 0, 0, 0, 0, 0, 0, 0, 0, 0, 0, 0, 0, 0, 0, 0, 0, 0, 0, 0, 0, 0, 0, 0, 0, 0, 0, 0, 0, 0, 16256, 0, 16256, 0, 16128, 0, 16256, 0, 16128, 0, 16256, 0, 16128, 0, 16256, 0, 0, 0, 0, 0, 0, 0, 0, 0, 0, 0, 0, 0, 0, 0, 0, 0, 16256, 0, 16256, 0, 16256, 0, 16256, 0, 16128, 0, 0, 0, 16256, 0, 16256, 0, 16128, 0, 16256, 0, 16256, 0, 16256, 0, 16128, 0, 16256, 0, 16256, 0, 16256, 0, 16256, 0, 16256, 0, 16256, 0, 16256, 0, 16256, 0, 16256, 0, 16256, 0, 16256, 0, 16256, 0, 16256, 0, 16256, 0, 16256, 0, 16256, 0, 16256, 0, 16256, 0, 16256, 0, 16256, 0, 16256, 0, 16256, 0, 16256, 0, 16256, 0, 16256, 0, 16256, 0, 16256, 0, 16256, 0, 16256, 0, 16256, 0, 16256, 0, 16256, 0, 16256, 0, 16256, 0, 16256, 0, 16128, 0, 16256, 0, 16256, 0, 16256, 0, 16128, 0, 16256, 0, 16256, 0, 16256, 0, 16256, 0, 16256, 0, 16256, 0, 16256, 0, 16256, 0, 16256, 0, 16256, 0, 16256, 0, 16256, 0, 16256, 0, 16256, 0, 16256, 0, 16256, 0, 16256, 0, 16256, 0, 16256, 0, 16256, 0, 16256, 0, 16256, 0, 16256, 0, 16256, 0, 16256, 0, 16256, 0, 16256, 0, 16256, 0, 16256, 0, 16128, 0, 16256, 0, 16256, 0, 16256, 0, 16128, 0, 16256, 0, 16256, 0, 16256, 0, 16256, 0, 16256, 0, 16256, 0, 16256, 0, 16256, 0, 16256, 0, 16256, 0, 16256, 0, 16256, 0, 16256, 0, 16256, 0, 16256, 0, 16256, 0, 16256, 0, 16256, 0, 16256, 0, 16256, 0, 16256, 0, 16256, 0, 16256, 0, 16256, 0, 16256, 0, 16256, 0, 16256, 0, 16128, 0, 16256, 0, 16256, 0, 16256, 0, 16128, 0, 16256, 0, 16256, 0, 16256, 0, 16256, 0, 16256, 0, 16256, 0, 16256, 0, 16256, 0, 16256, 0, 16256, 0, 16256, 0, 16256, 0, 16256, 0, 16256, 0, 16256, 0, 16256, 0, 16256, 0, 16256, 0, 16256, 0, 16256, 0, 16256, 0, 16256, 0, 16256, 0, 16256, 0, 16256, 0, 16128, 0, 16256, 0, 16256, 0, 16256, 0, 16128, 0, 16256, 0, 16256, 0, 16256, 0, 16256, 0, 16256, 0, 16256, 0, 16256, 0, 16256, 0, 16256, 0, 16256, 0, 16256, 0, 16256, 0, 16256, 0, 16256, 0, 16256, 0, 16256, 0, 16256, 0, 16256, 0, 16256, 0, 0, 0, 0, 0, 0, 0, 0, 0, 0, 0, 0, 0, 0, 0, 0, 0, 0, 0, 0, 0, 0, 0, 0, 0, 0, 0, 0, 0, 0, 0, 0, 0, 0, 0, 0, 0, 0, 0, 0, 0, 0, 0, 0, 0, 0, 0, 0, 0, 0, 0, 0, 0, 0, 0, 0, 0, 0, 0, 0, 0, 0, 0, 0, 0, 0, 0, 0, 0, 0, 0, 0, 0, 0, 0, 0, 0, 0, 0, 0, 0, 0, 0, 0, 0, 0, 0, 0, 0, 0, 0, 0, 0, 0, 0, 0, 0, 0, 0, 0, 0, 0, 0, 0, 0, 0, 0, 0, 0, 0, 0, 0, 0, 0, 0, 0, 0, 0, 0, 0, 0, 0, 0, 0, 0, 0, 0, 0, 0, 16768, 0, 16768, 0, 16768, 0, 16768, 0, 16768, 0, 16768, 0, 16768, 0, 16768, 0, 16256, 0, 16256, 0, 16128, 0, 16256, 0, 16256, 0, 16256, 0, 16128, 0, 16256, 0, 16768, 0, 16768, 0, 16768, 0, 16768, 0, 16768, 0, 16768, 0, 16768, 0, 16768, 0, 16256, 0, 16256, 0, 16256, 0, 16256, 0, 16256, 0, 16256, 0, 16256, 0, 16256, 0, 16128, 0, 16256, 0, 16256, 0, 16256, 0, 16128, 0, 16256, 0, 16256, 0, 16256, 0, 16256, 0, 16256, 0, 16256, 0, 16256, 0, 16256, 0, 16256, 0, 16256, 0, 16256, 0, 16256, 0, 16256, 0, 16256, 0, 16256, 0, 16256, 0, 16256, 0, 16256, 0, 16256, 0, 16256, 0, 16256, 0, 16256, 0, 16256, 0, 16256, 0, 16256, 0, 16256, 0, 16256, 0, 16256, 0, 16256, 0, 16256, 0, 16256, 0, 16256, 0, 16256, 0, 16256, 0, 16256, 0, 16128, 0, 16256, 0, 16256, 0, 16256, 0, 16128, 0, 16256, 0, 16256, 0, 16256, 0, 16256, 0, 16256, 0, 16256, 0, 16256, 0, 16256, 0, 16256, 0, 16256, 0, 16256, 0, 16256, 0, 16256, 0, 16256, 0, 16256, 0, 16256, 0, 16256, 0, 16256, 0, 16256, 0, 16256, 0, 16256, 0, 16256, 0, 16256, 0, 16256, 0, 16256, 0, 16256, 0, 16256, 0, 16256, 0, 16256, 0, 16128, 0, 16256, 0, 16256, 0, 16256, 0, 16128, 0, 16256, 0, 16256, 0, 16256, 0, 16256, 0, 16256, 0, 16256, 0, 16256, 0, 16256, 0, 16256, 0, 16256, 0, 16256, 0, 16256, 0, 16256, 0, 16256, 0, 16256, 0, 16256, 0, 16256, 0, 16256, 0, 16256, 0, 16256, 0, 16256, 0, 16256, 0, 16256, 0, 16256, 0, 16256, 0, 16256, 0, 16256, 0, 16128, 0, 16256, 0, 16256, 0, 16256, 0, 16128, 0, 16256, 0, 16256, 0, 16256, 0, 16256, 0, 16256, 0, 16256, 0, 16256, 0, 16256, 0, 16256, 0, 16256, 0, 16256, 0, 16256, 0, 16256, 0, 16256, 0, 16256, 0, 16256, 0, 16256, 0, 16256, 0, 16256, 0, 16256, 0, 16256, 0, 16256, 0, 16256, 0, 16256, 0, 16256, 0, 16128, 0, 16256, 0, 16256, 0, 16256, 0, 16128, 0, 16256, 0, 16256, 0, 16256, 0, 16256, 0, 16256, 0, 16256, 0, 16256, 0, 16256, 0, 16256, 0, 16256, 0, 16256, 0, 16256, 0, 16256, 0, 16256, 0, 16256, 0, 16256, 0, 16256, 0, 16256, 0, 16256, 0, 0, 0, 0, 0, 0, 0, 0, 0, 0, 0, 0, 0, 0, 0, 0, 0, 0, 0, 0, 0, 0, 0, 0, 0, 0, 0, 0, 0, 0, 0, 0, 0, 0, 0, 0, 0, 0, 0, 0, 0, 0, 0, 0, 0, 0, 0, 0, 0, 0, 0, 0, 0, 0, 0, 0, 0, 0, 0, 0, 0, 0, 0, 0, 0, 0, 0, 0, 0, 0, 0, 0, 0, 0, 0, 0, 0, 0, 0, 0, 0, 0, 0, 0, 0, 0, 0, 0, 0, 0, 0, 0, 0, 0, 0, 0, 0, 0, 0, 0, 0, 0, 0, 0, 0, 0, 0, 0, 0, 0, 0, 0, 0, 0, 0, 0, 0, 0, 0, 0, 0, 0, 0, 0, 0, 0, 0, 0, 0, 16768, 0, 16768, 0, 16768, 0, 16768, 0, 16768, 0, 0, 0, 0, 0, 0, 0, 16256, 0, 16256, 0, 16128, 0, 16256, 0, 16128, 0, 16256, 0, 16128, 0, 16256, 0, 16768, 0, 16768, 0, 16768, 0, 16768, 0, 16768, 0, 0, 0, 0, 0, 0, 0, 16256, 0, 16256, 0, 16256, 0, 16256, 0, 16128, 0, 17280, 0, 16256, 0, 16256, 0, 16128, 0, 16256, 0, 16256, 0, 16256, 0, 16128, 0, 16256, 0, 16256, 0, 16256, 0, 16256, 0, 16256, 0, 16256, 0, 16256, 0, 16256, 0, 16256, 0, 16256, 0, 16256, 0, 16256, 0, 16256, 0, 16256, 0, 16256, 0, 16256, 0, 16256, 0, 16256, 0, 16256, 0, 16256, 0, 16256, 0, 16256, 0, 16256, 0, 16256, 0, 16256, 0, 16256, 0, 16256, 0, 16256, 0, 16256, 0, 16256, 0, 16256, 0, 16256, 0, 16256, 0, 16256, 0, 16256, 0, 16128, 0, 16256, 0, 16256, 0, 16256, 0, 16128, 0, 16256, 0, 16256, 0, 16256, 0, 16256, 0, 16256, 0, 16256, 0, 16256, 0, 16256, 0, 16256, 0, 16256, 0, 16256, 0, 16256, 0, 16256, 0, 16256, 0, 16256, 0, 16256, 0, 16256, 0, 16256, 0, 16256, 0, 16256, 0, 16256, 0, 16256, 0, 16256, 0, 16256, 0, 16256, 0, 16256, 0, 16256, 0, 16256, 0, 16256, 0, 16128, 0, 16256, 0, 16256, 0, 16256, 0, 16128, 0, 16256, 0, 16256, 0, 16256, 0, 16256, 0, 16256, 0, 16256, 0, 16256, 0, 16256, 0, 16256, 0, 16256, 0, 16256, 0, 16256, 0, 16256, 0, 16256, 0, 16256, 0, 16256, 0, 16256, 0, 16256, 0, 16256, 0, 16256, 0, 16256, 0, 16256, 0, 16256, 0, 16256, 0, 16256, 0, 16256, 0, 16256, 0, 16128, 0, 16256, 0, 16256, 0, 16256, 0, 16128, 0, 16256, 0, 16256, 0, 16256, 0, 16256, 0, 16256, 0, 16256, 0, 16256, 0, 16256, 0, 16256, 0, 16256, 0, 16256, 0, 16256, 0, 16256, 0, 16256, 0, 16256, 0, 16256, 0, 16256, 0, 16256, 0, 16256, 0, 16256, 0, 16256, 0, 16256, 0, 16256, 0, 16256, 0, 16256, 0, 16128, 0, 16256, 0, 16256, 0, 16256, 0, 16128, 0, 16256, 0, 16256, 0, 16256, 0, 16256, 0, 16256, 0, 16256, 0, 16256, 0, 16256, 0, 16256, 0, 16256, 0, 16256, 0, 16256, 0, 16256, 0, 16256, 0, 16256, 0, 16256, 0, 16256, 0, 16256, 0, 16256, 0, 0, 0, 0, 0, 0, 0, 0, 0, 0, 0, 0, 0, 0, 0, 0, 0, 0, 0, 0, 0, 0, 0, 0, 0, 0, 0, 0, 0, 0, 0, 0, 0, 0, 0, 0, 0, 0, 0, 0, 0, 0, 0, 0, 0, 0, 0, 0, 0, 0, 0, 0, 0, 0, 0, 0, 0, 0, 0, 0, 0, 0, 0, 0, 0, 0, 0, 0, 0, 0, 0, 0, 0, 0, 0, 0, 0, 0, 0, 0, 0, 0, 0, 0, 0, 0, 0, 0, 0, 0, 0, 0, 0, 0, 0, 0, 0, 0, 0, 0, 0, 0, 0, 0, 0, 0, 0, 0, 0, 0, 0, 0, 0, 0, 0, 0, 0, 0, 0, 0, 0, 0, 0, 0, 0, 0, 0, 0};

__global__ __launch_bounds__(512) void xd_kernel(const int* idx, const u16* w1, const u16* table, const u16* b1, const u16* ws_cin, const u16* wout, char* ws, const u16* w2, const u16* b_out, u16* out) {
  __shared__ __attribute__((aligned(1024))) char shm[163840];
  unsigned tid = threadIdx.x;
  asm volatile(
    "s_load_dwordx16 s[4:19], s[0:1], 0\n"
    ".Lxd_top:\n"
    "v_readfirstlane_b32 s43, v0\n"
    "s_setprio 3\n"
    "s_cmp_lt_u32 s2, 48\n"
    "s_cbranch_scc1 .Lxd_cin\n"
    "s_and_b32 s24, s2, 15\n"
    "s_mul_i32 s40, s43, 36\n"
    "s_mul_i32 s41, s24, 26624\n"
    "s_add_u32 s38, s40, s41\n"
    "v_lshlrev_b32 v86, 4, v0\n"
    "s_lshl_b32 s34, s43, 6\n"
    "s_add_u32 m0, s34, 98304\n"
    "s_waitcnt lgkmcnt(0)\n"
    "s_add_u32 s38, s4, s38\n"
    "s_addc_u32 s39, s5, 0\n"
    "global_load_lds_dwordx4 v86, s[38:39] offset:0\n"
    "global_load_lds_dwordx4 v86, s[38:39] offset:1024\n"
    "global_load_lds_dwordx4 v86, s[38:39] offset:2048\n"
    "global_load_lds_dwordx4 v86, s[38:39] offset:2304\n"
    "s_setprio 2\n"
    "s_sub_u32 s3, s2, 48\n"
    "s_lshl_b32 s40, s2, 2\n"
    "s_lshr_b32 s23, s3, 4\n"
    "s_lshr_b32 s40, 0xa8864220, s40\n"
    "s_and_b32 s42, s40, 14\n"
    "v_bfe_u32 v85, v0, 4, 2\n"
    "v_lshlrev_b32 v87, 1, v0\n"
    "v_and_b32 v87, 0xe0, v87\n"
    "v_and_b32 v88, 0xf0, v86\n"
    "v_xor_b32 v87, v87, v88\n"
    "s_movk_i32 s41, 3328\n"
    "v_mad_u32_u24 v46, v85, s41, v87\n"
    "v_add_u32 v47, 12288, v46\n"
    "s_mul_i32 s41, s43, 416\n"
    "s_lshl_b32 s40, s23, 8\n"
    "s_add_u32 s41, s41, s40\n"
    "s_mul_i32 s30, s42, 425984\n"
    "s_lshl_b32 s35, s43, 5\n"
    "s_bitset1_b32 s35, 15\n"
    "s_add_u32 s6, s6, s41\n"
    "s_addc_u32 s7, s7, 0\n"
    "s_add_u32 s30, s6, s30\n"
    "s_addc_u32 s31, s7, 0\n"
    "s_add_u32 m0, s35, 0\n"
    "s_nop 0\n"
    "global_load_lds_dwordx4 v46, s[30:31] offset:0\n"
    "global_load_lds_dwordx4 v47, s[30:31] offset:1024\n"
    "s_add_u32 s30, s30, 212992\n"
    "s_addc_u32 s31, s31, 0\n"
    "s_setprio 1\n"
    "v_and_b32 v84, 3, v0\n"
    "v_bfe_u32 v44, v0, 3, 3\n"
    "v_lshl_add_u32 v44, v84, 3, v44\n"
    "s_add_u32 s40, s34, 98304\n"
    "v_mul_u32_u24 v44, 104, v44\n"
    "v_add_u32 v44, s40, v44\n"
    "v_lshl_add_u32 v45, s42, 3, v44\n"
    "v_xor_b32 v51, v0, v86\n"
    "v_and_b32 v86, 0x70, v86\n"
    "v_bfi_b32 v86, 48, v51, v86\n"
    "v_xor_b32 v51, 64, v86\n"
    "v_add_u32 v48, 3072, v86\n"
    "v_add_u32 v49, 2048, v51\n"
    "v_add_u32 v50, 1024, v86\n"
    "s_sub_u32 s36, s8, 3072\n"
    "s_subb_u32 s37, s9, 0\n"
    "s_setprio 3\n"
    "s_waitcnt vmcnt(2)\n"
    "ds_read2_b64 v[96:99], v45 offset1:1\n"
    "s_lshr_b32 s20, s43, 6\n"
    "s_lshr_b32 s21, s43, 8\n"
    "v_and_b32 v1, 63, v0\n"
    "s_waitcnt lgkmcnt(0)\n"
    "v_lshlrev_b32 v96, 7, v96\n"
    "v_lshlrev_b32 v97, 7, v97\n"
    "v_lshlrev_b32 v98, 7, v98\n"
    "v_add_u32_dpp v52, v96, v48 quad_perm:[0,0,0,0] row_mask:0xf bank_mask:0xf\n"
    "v_add_u32_dpp v53, v96, v49 quad_perm:[1,1,1,1] row_mask:0xf bank_mask:0xf\n"
    "v_add_u32_dpp v54, v96, v50 quad_perm:[2,2,2,2] row_mask:0xf bank_mask:0xf\n"
    "v_add_u32_dpp v55, v96, v51 quad_perm:[3,3,3,3] row_mask:0xf bank_mask:0xf\n"
    "s_add_u32 m0, s34, 0\n"
    "s_nop 0\n"
    "global_load_lds_dwordx4 v52, s[36:37] offset:0\n"
    "global_load_lds_dwordx4 v53, s[36:37] offset:1024\n"
    "global_load_lds_dwordx4 v54, s[36:37] offset:2048\n"
    "global_load_lds_dwordx4 v55, s[36:37] offset:3072\n"
    "s_setprio 1\n"
    "v_add_u32_dpp v52, v97, v48 quad_perm:[0,0,0,0] row_mask:0xf bank_mask:0xf\n"
    "v_add_u32_dpp v53, v97, v49 quad_perm:[1,1,1,1] row_mask:0xf bank_mask:0xf\n"
    "v_add_u32_dpp v54, v97, v50 quad_perm:[2,2,2,2] row_mask:0xf bank_mask:0xf\n"
    "v_add_u32_dpp v55, v97, v51 quad_perm:[3,3,3,3] row_mask:0xf bank_mask:0xf\n"
    "s_load_dwordx4 s[68:71], s[0:1], 64\n"
    "s_add_u32 m0, s35, 49152\n"
    "s_nop 0\n"
    "global_load_lds_dwordx4 v46, s[30:31] offset:0\n"
    "s_lshl2_add_u32 s41, s42, 8\n"
    "s_mov_b32 s28, 0x30201000\n"
    "s_movk_i32 s29, 0x5040\n"
    "s_lshr_b64 s[42:43], s[28:29], s41\n"
    "s_sub_u32 s41, 48, s41\n"
    "s_lshl_b64 s[28:29], s[28:29], s41\n"
    "s_or_b64 s[28:29], s[28:29], s[42:43]\n"
    "s_and_b32 s29, s29, 0xff\n"
    "s_or_b32 s29, s29, 0x6000\n"
    "global_load_lds_dwordx4 v47, s[30:31] offset:1024\n"
    "s_add_u32 s30, s30, 212992\n"
    "s_addc_u32 s31, s31, 0\n"
    "s_bfe_u32 s40, s28, 0x80000\n"
    "v_add_u32 v45, s40, v44\n"
    "ds_read2_b64 v[100:103], v45 offset1:1\n"
    "s_bfe_u32 s40, s28, 0x80008\n"
    "v_add_u32 v45, s40, v44\n"
    "ds_read2_b64 v[104:107], v45 offset1:1\n"
    "s_bfe_u32 s40, s28, 0x80010\n"
    "v_add_u32 v45, s40, v44\n"
    "ds_read2_b64 v[108:111], v45 offset1:1\n"
    "s_add_u32 m0, s34, 49152\n"
    "s_nop 0\n"
    "global_load_lds_dwordx4 v52, s[36:37] offset:0\n"
    "s_bfe_u32 s40, s28, 0x80018\n"
    "v_add_u32 v45, s40, v44\n"
    "ds_read2_b64 v[112:115], v45 offset1:1\n"
    "s_bfe_u32 s40, s29, 0x80000\n"
    "v_add_u32 v45, s40, v44\n"
    "ds_read2_b64 v[116:119], v45 offset1:1\n"
    "s_bfe_u32 s40, s29, 0x80008\n"
    "v_add_u32 v45, s40, v44\n"
    "ds_read_b64 v[120:121], v45\n"
    "global_load_lds_dwordx4 v53, s[36:37] offset:1024\n"
    "v_lshrrev_b32 v86, 5, v88\n"
    "v_xor_b32 v86, v86, v85\n"
    "v_lshlrev_b32 v86, 4, v86\n"
    "v_lshl_add_u32 v86, v88, 3, v86\n"
    "s_and_b32 s41, s20, 1\n"
    "s_lshl1_add_u32 s40, s21, s41\n"
    "v_lshl_add_u32 v56, s40, 13, v86\n"
    "v_xor_b32 v57, 64, v56\n"
    "global_load_lds_dwordx4 v54, s[36:37] offset:2048\n"
    "v_lshrrev_b32 v86, 6, v88\n"
    "v_lshl_add_u32 v88, v85, 3, v86\n"
    "v_bfe_u32 v86, v0, 2, 3\n"
    "s_and_b32 s40, s20, 2\n"
    "s_lshl1_add_u32 s40, s40, 1024\n"
    "v_xor_b32 v86, s40, v86\n"
    "v_lshlrev_b32 v86, 5, v86\n"
    "v_lshl_add_u32 v86, v88, 8, v86\n"
    "v_lshl_add_u32 v76, v84, 3, v86\n"
    "global_load_lds_dwordx4 v55, s[36:37] offset:3072\n"
    "v_xor_b32 v77, 32, v76\n"
    "v_xor_b32 v78, 64, v76\n"
    "v_xor_b32 v79, 96, v76\n"
    "s_setprio 0\n"
    "v_add_u32_dpp v52, v98, v48 quad_perm:[0,0,0,0] row_mask:0xf bank_mask:0xf\n"
    "v_add_u32_dpp v53, v98, v49 quad_perm:[1,1,1,1] row_mask:0xf bank_mask:0xf\n"
    "v_add_u32_dpp v54, v98, v50 quad_perm:[2,2,2,2] row_mask:0xf bank_mask:0xf\n"
    "v_add_u32_dpp v55, v98, v51 quad_perm:[3,3,3,3] row_mask:0xf bank_mask:0xf\n"
    "v_lshlrev_b32 v99, 7, v99\n"
    "s_lshr_b32 s41, s3, 3\n"
    "s_waitcnt vmcnt(10)\n"
    "s_cmp_eq_u32 s21, 0\n"
    "s_cbranch_scc1 .Lxd_g0\n"
    "s_barrier\n"
    ".Lxd_g0:\n"
    "s_cmp_lg_u32 s20, 7\n"
    "s_cbranch_scc1 .Lxd_on2\n"
    "s_mul_i32 s40, s41, 12\n"
    "v_min_u32 v7, 11, v1\n"
    "v_add_lshl_u32 v7, v7, s40, 7\n"
    "s_getpc_b64 s[60:61]\n"
    ".Lxd_pc1:\n"
    "s_sub_u32 s60, s60, .Lxd_pc1-.Lxd_top\n"
    "s_subb_u32 s61, s61, 0\n"
    "v_min_u32 v7, .Lxd_end-.Lxd_top-4, v7\n"
    "global_load_dword v7, v7, s[60:61]\n"
    ".Lxd_on2:\n"
    "s_waitcnt vmcnt(6) lgkmcnt(0)\n"
    "s_barrier\n"
    "s_add_u32 m0, s35, 98304\n"
    "s_nop 0\n"
    "global_load_lds_dwordx4 v46, s[30:31] offset:0\n"
    "ds_read_b128 v[128:131], v56 offset:0\n"
    "ds_read_b128 v[132:135], v56 offset:2048\n"
    "ds_read_b128 v[136:139], v56 offset:4096\n"
    "ds_read_b128 v[140:143], v56 offset:6144\n"
    "global_load_lds_dwordx4 v47, s[30:31] offset:1024\n"
    "s_add_u32 s30, s30, 212992\n"
    "s_addc_u32 s31, s31, 0\n"
    "ds_read_b64_tr_b16 v[144:145], v76 offset:0\n"
    "ds_read_b64_tr_b16 v[146:147], v76 offset:1024\n"
    "ds_read_b64_tr_b16 v[148:149], v77 offset:0\n"
    "ds_read_b64_tr_b16 v[150:151], v77 offset:1024\n"
    "s_add_u32 m0, s34, 98304\n"
    "s_nop 0\n"
    "global_load_lds_dwordx4 v52, s[36:37] offset:0\n"
    "ds_read_b64_tr_b16 v[152:153], v78 offset:0\n"
    "ds_read_b64_tr_b16 v[154:155], v78 offset:1024\n"
    "ds_read_b64_tr_b16 v[156:157], v79 offset:0\n"
    "ds_read_b64_tr_b16 v[158:159], v79 offset:1024\n"
    "global_load_lds_dwordx4 v53, s[36:37] offset:1024\n"
    "ds_read_b128 v[160:163], v57 offset:0\n"
    "ds_read_b128 v[164:167], v57 offset:2048\n"
    "ds_read_b128 v[168:171], v57 offset:4096\n"
    "ds_read_b128 v[172:175], v57 offset:6144\n"
    "global_load_lds_dwordx4 v54, s[36:37] offset:2048\n"
    "ds_read_b64_tr_b16 v[176:177], v76 offset:8192\n"
    "ds_read_b64_tr_b16 v[178:179], v76 offset:9216\n"
    "ds_read_b64_tr_b16 v[180:181], v77 offset:8192\n"
    "ds_read_b64_tr_b16 v[182:183], v77 offset:9216\n"
    "global_load_lds_dwordx4 v55, s[36:37] offset:3072\n"
    "ds_read_b64_tr_b16 v[184:185], v78 offset:8192\n"
    "ds_read_b64_tr_b16 v[186:187], v78 offset:9216\n"
    "ds_read_b64_tr_b16 v[188:189], v79 offset:8192\n"
    "ds_read_b64_tr_b16 v[190:191], v79 offset:9216\n"
    "s_waitcnt vmcnt(10) lgkmcnt(0)\n"
    "s_barrier\n"
    "v_mfma_f32_16x16x32_bf16 v[192:195], v[144:147], v[128:131], 0\n"
    "v_lshlrev_b32 v100, 7, v100\n"
    "v_mfma_f32_16x16x32_bf16 v[192:195], v[176:179], v[160:163], v[192:195]\n"
    "v_add_u32_dpp v52, v99, v48 quad_perm:[0,0,0,0] row_mask:0xf bank_mask:0xf\n"
    "v_mfma_f32_16x16x32_bf16 v[196:199], v[176:179], v[164:167], 0\n"
    "v_mfma_f32_16x16x32_bf16 v[196:199], v[144:147], v[132:135], v[196:199]\n"
    "v_add_u32_dpp v53, v99, v49 quad_perm:[1,1,1,1] row_mask:0xf bank_mask:0xf\n"
    "v_mfma_f32_16x16x32_bf16 v[200:203], v[144:147], v[136:139], 0\n"
    "v_add_u32_dpp v54, v99, v50 quad_perm:[2,2,2,2] row_mask:0xf bank_mask:0xf\n"
    "v_mfma_f32_16x16x32_bf16 v[200:203], v[176:179], v[168:171], v[200:203]\n"
    "v_mfma_f32_16x16x32_bf16 v[204:207], v[176:179], v[172:175], 0\n"
    "v_add_u32_dpp v55, v99, v51 quad_perm:[3,3,3,3] row_mask:0xf bank_mask:0xf\n"
    "v_mfma_f32_16x16x32_bf16 v[204:207], v[144:147], v[140:143], v[204:207]\n"
    "s_and_b32 s40, s20, 2\n"
    "v_mfma_f32_16x16x32_bf16 v[220:223], v[148:151], v[140:143], 0\n"
    "v_mfma_f32_16x16x32_bf16 v[220:223], v[180:183], v[172:175], v[220:223]\n"
    "s_lshl2_add_u32 s40, s23, s40\n"
    "v_mfma_f32_16x16x32_bf16 v[216:219], v[180:183], v[168:171], 0\n"
    "s_lshl_b32 s40, s40, 6\n"
    "v_mfma_f32_16x16x32_bf16 v[216:219], v[148:151], v[136:139], v[216:219]\n"
    "v_mfma_f32_16x16x32_bf16 v[212:215], v[148:151], v[132:135], 0\n"
    "v_and_b32 v43, 31, v0\n"
    "v_mfma_f32_16x16x32_bf16 v[212:215], v[180:183], v[164:167], v[212:215]\n"
    "s_lshl_b32 s64, s20, 8\n"
    "v_mfma_f32_16x16x32_bf16 v[208:211], v[180:183], v[160:163], 0\n"
    "v_mfma_f32_16x16x32_bf16 v[208:211], v[148:151], v[128:131], v[208:211]\n"
    "s_add_u32 s64, s64, 147456\n"
    "v_mfma_f32_16x16x32_bf16 v[224:227], v[152:155], v[128:131], 0\n"
    "v_mfma_f32_16x16x32_bf16 v[224:227], v[184:187], v[160:163], v[224:227]\n"
    "v_lshl_add_u32 v43, v43, 2, s40\n"
    "v_mfma_f32_16x16x32_bf16 v[228:231], v[184:187], v[164:167], 0\n"
    "v_lshl_add_u32 v42, v85, 3, s64\n"
    "v_mfma_f32_16x16x32_bf16 v[228:231], v[152:155], v[132:135], v[228:231]\n"
    "v_mfma_f32_16x16x32_bf16 v[232:235], v[152:155], v[136:139], 0\n"
    "s_lshl_b32 s24, s24, 8\n"
    "v_mfma_f32_16x16x32_bf16 v[232:235], v[184:187], v[168:171], v[232:235]\n"
    "v_add_u32 v58, 98304, v56\n"
    "v_mfma_f32_16x16x32_bf16 v[236:239], v[184:187], v[172:175], 0\n"
    "v_mfma_f32_16x16x32_bf16 v[236:239], v[152:155], v[140:143], v[236:239]\n"
    "v_add_u32 v59, 98304, v57\n"
    "v_mfma_f32_16x16x32_bf16 v[252:255], v[156:159], v[140:143], 0\n"
    "v_add_u32 v80, 98304, v76\n"
    "v_mfma_f32_16x16x32_bf16 v[252:255], v[188:191], v[172:175], v[252:255]\n"
    "v_mfma_f32_16x16x32_bf16 v[248:251], v[188:191], v[168:171], 0\n"
    "v_add_u32 v81, 98304, v77\n"
    "v_mfma_f32_16x16x32_bf16 v[248:251], v[156:159], v[136:139], v[248:251]\n"
    "v_add_u32 v82, 98304, v78\n"
    "v_mfma_f32_16x16x32_bf16 v[244:247], v[156:159], v[132:135], 0\n"
    "v_mfma_f32_16x16x32_bf16 v[244:247], v[188:191], v[164:167], v[244:247]\n"
    "v_add_u32 v83, 98304, v79\n"
    "v_mfma_f32_16x16x32_bf16 v[240:243], v[188:191], v[160:163], 0\n"
    "v_mfma_f32_16x16x32_bf16 v[240:243], v[156:159], v[128:131], v[240:243]\n"
    "s_waitcnt vmcnt(6)\n"
    "s_barrier\n"
    "s_add_u32 m0, s35, 0\n"
    "s_nop 0\n"
    "global_load_lds_dwordx4 v46, s[30:31] offset:0\n"
    "ds_read_b128 v[128:131], v56 offset:49152\n"
    "ds_read_b128 v[132:135], v56 offset:51200\n"
    "ds_read_b128 v[136:139], v56 offset:53248\n"
    "ds_read_b128 v[140:143], v56 offset:55296\n"
    "global_load_lds_dwordx4 v47, s[30:31] offset:1024\n"
    "s_bfe_u32 s66, s28, 0x80000\n"
    "s_mul_i32 s66, s66, 53248\n"
    "s_add_u32 s30, s6, s66\n"
    "s_addc_u32 s31, s7, 0\n"
    "ds_read_b64_tr_b16 v[144:145], v76 offset:49152\n"
    "ds_read_b64_tr_b16 v[146:147], v76 offset:50176\n"
    "ds_read_b64_tr_b16 v[148:149], v77 offset:49152\n"
    "ds_read_b64_tr_b16 v[150:151], v77 offset:50176\n"
    "s_add_u32 m0, s34, 0\n"
    "s_nop 0\n"
    "global_load_lds_dwordx4 v52, s[36:37] offset:0\n"
    "ds_read_b64_tr_b16 v[152:153], v78 offset:49152\n"
    "ds_read_b64_tr_b16 v[154:155], v78 offset:50176\n"
    "ds_read_b64_tr_b16 v[156:157], v79 offset:49152\n"
    "ds_read_b64_tr_b16 v[158:159], v79 offset:50176\n"
    "global_load_lds_dwordx4 v53, s[36:37] offset:1024\n"
    "ds_read_b128 v[160:163], v57 offset:49152\n"
    "ds_read_b128 v[164:167], v57 offset:51200\n"
    "ds_read_b128 v[168:171], v57 offset:53248\n"
    "ds_read_b128 v[172:175], v57 offset:55296\n"
    "global_load_lds_dwordx4 v54, s[36:37] offset:2048\n"
    "ds_read_b64_tr_b16 v[176:177], v76 offset:57344\n"
    "ds_read_b64_tr_b16 v[178:179], v76 offset:58368\n"
    "ds_read_b64_tr_b16 v[180:181], v77 offset:57344\n"
    "ds_read_b64_tr_b16 v[182:183], v77 offset:58368\n"
    "global_load_lds_dwordx4 v55, s[36:37] offset:3072\n"
    "ds_read_b64_tr_b16 v[184:185], v78 offset:57344\n"
    "ds_read_b64_tr_b16 v[186:187], v78 offset:58368\n"
    "ds_read_b64_tr_b16 v[188:189], v79 offset:57344\n"
    "ds_read_b64_tr_b16 v[190:191], v79 offset:58368\n"
    "s_waitcnt vmcnt(10) lgkmcnt(0)\n"
    "s_barrier\n"
    "v_mfma_f32_16x16x32_bf16 v[192:195], v[144:147], v[128:131], v[192:195]\n"
    "v_lshlrev_b32 v101, 7, v101\n"
    "v_mfma_f32_16x16x32_bf16 v[192:195], v[176:179], v[160:163], v[192:195]\n"
    "v_mfma_f32_16x16x32_bf16 v[196:199], v[176:179], v[164:167], v[196:199]\n"
    "v_add_u32_dpp v52, v100, v48 quad_perm:[0,0,0,0] row_mask:0xf bank_mask:0xf\n"
    "v_mfma_f32_16x16x32_bf16 v[196:199], v[144:147], v[132:135], v[196:199]\n"
    "v_mfma_f32_16x16x32_bf16 v[200:203], v[144:147], v[136:139], v[200:203]\n"
    "v_add_u32_dpp v53, v100, v49 quad_perm:[1,1,1,1] row_mask:0xf bank_mask:0xf\n"
    "v_mfma_f32_16x16x32_bf16 v[200:203], v[176:179], v[168:171], v[200:203]\n"
    "v_mfma_f32_16x16x32_bf16 v[204:207], v[176:179], v[172:175], v[204:207]\n"
    "v_add_u32_dpp v54, v100, v50 quad_perm:[2,2,2,2] row_mask:0xf bank_mask:0xf\n"
    "v_mfma_f32_16x16x32_bf16 v[204:207], v[144:147], v[140:143], v[204:207]\n"
    "v_mfma_f32_16x16x32_bf16 v[220:223], v[148:151], v[140:143], v[220:223]\n"
    "v_add_u32_dpp v55, v100, v51 quad_perm:[3,3,3,3] row_mask:0xf bank_mask:0xf\n"
    "v_mfma_f32_16x16x32_bf16 v[220:223], v[180:183], v[172:175], v[220:223]\n"
    "v_mfma_f32_16x16x32_bf16 v[216:219], v[180:183], v[168:171], v[216:219]\n"
    "s_cmp_lt_u32 s20, 4\n"
    "s_cselect_b32 s40, -1, 0\n"
    "v_mfma_f32_16x16x32_bf16 v[216:219], v[148:151], v[136:139], v[216:219]\n"
    "v_mfma_f32_16x16x32_bf16 v[212:215], v[148:151], v[132:135], v[212:215]\n"
    "v_lshlrev_b32 v28, 2, v0\n"
    "v_mfma_f32_16x16x32_bf16 v[212:215], v[180:183], v[164:167], v[212:215]\n"
    "v_mfma_f32_16x16x32_bf16 v[208:211], v[180:183], v[160:163], v[208:211]\n"
    "v_mov_b32 v29, s40\n"
    "v_mfma_f32_16x16x32_bf16 v[208:211], v[148:151], v[128:131], v[208:211]\n"
    "v_mfma_f32_16x16x32_bf16 v[224:227], v[152:155], v[128:131], v[224:227]\n"
    "v_add_u32 v28, 149504, v28\n"
    "v_mfma_f32_16x16x32_bf16 v[224:227], v[184:187], v[160:163], v[224:227]\n"
    "v_mfma_f32_16x16x32_bf16 v[228:231], v[184:187], v[164:167], v[228:231]\n"
    "s_mul_i32 s47, s3, 5042\n"
    "v_mfma_f32_16x16x32_bf16 v[228:231], v[152:155], v[132:135], v[228:231]\n"
    "v_mfma_f32_16x16x32_bf16 v[232:235], v[152:155], v[136:139], v[232:235]\n"
    "s_lshr_b32 s47, s47, 16\n"
    "v_mfma_f32_16x16x32_bf16 v[232:235], v[184:187], v[168:171], v[232:235]\n"
    "v_mfma_f32_16x16x32_bf16 v[236:239], v[184:187], v[172:175], v[236:239]\n"
    "s_lshr_b32 s21, s20, 2\n"
    "v_mfma_f32_16x16x32_bf16 v[236:239], v[152:155], v[140:143], v[236:239]\n"
    "v_mfma_f32_16x16x32_bf16 v[252:255], v[156:159], v[140:143], v[252:255]\n"
    "s_lshl1_add_u32 s22, s21, s20\n"
    "v_mfma_f32_16x16x32_bf16 v[252:255], v[188:191], v[172:175], v[252:255]\n"
    "v_mfma_f32_16x16x32_bf16 v[248:251], v[188:191], v[168:171], v[248:251]\n"
    "s_and_b32 s22, s22, 3\n"
    "v_mfma_f32_16x16x32_bf16 v[248:251], v[156:159], v[136:139], v[248:251]\n"
    "v_mfma_f32_16x16x32_bf16 v[244:247], v[156:159], v[132:135], v[244:247]\n"
    "s_lshl_b32 s58, s21, 8\n"
    "v_mfma_f32_16x16x32_bf16 v[244:247], v[188:191], v[164:167], v[244:247]\n"
    "v_mfma_f32_16x16x32_bf16 v[240:243], v[188:191], v[160:163], v[240:243]\n"
    "v_mfma_f32_16x16x32_bf16 v[240:243], v[156:159], v[128:131], v[240:243]\n"
    "s_waitcnt vmcnt(6)\n"
    "s_barrier\n"
    "s_add_u32 m0, s35, 49152\n"
    "s_nop 0\n"
    "global_load_lds_dwordx4 v46, s[30:31] offset:0\n"
    "ds_read_b128 v[128:131], v58 offset:0\n"
    "ds_read_b128 v[132:135], v58 offset:2048\n"
    "ds_read_b128 v[136:139], v58 offset:4096\n"
    "ds_read_b128 v[140:143], v58 offset:6144\n"
    "global_load_lds_dwordx4 v47, s[30:31] offset:1024\n"
    "s_add_u32 s30, s30, 212992\n"
    "s_addc_u32 s31, s31, 0\n"
    "ds_read_b64_tr_b16 v[144:145], v80 offset:0\n"
    "ds_read_b64_tr_b16 v[146:147], v80 offset:1024\n"
    "ds_read_b64_tr_b16 v[148:149], v81 offset:0\n"
    "ds_read_b64_tr_b16 v[150:151], v81 offset:1024\n"
    "s_add_u32 m0, s34, 49152\n"
    "s_nop 0\n"
    "global_load_lds_dwordx4 v52, s[36:37] offset:0\n"
    "ds_read_b64_tr_b16 v[152:153], v82 offset:0\n"
    "ds_read_b64_tr_b16 v[154:155], v82 offset:1024\n"
    "ds_read_b64_tr_b16 v[156:157], v83 offset:0\n"
    "ds_read_b64_tr_b16 v[158:159], v83 offset:1024\n"
    "global_load_lds_dwordx4 v53, s[36:37] offset:1024\n"
    "ds_read_b128 v[160:163], v59 offset:0\n"
    "ds_read_b128 v[164:167], v59 offset:2048\n"
    "ds_read_b128 v[168:171], v59 offset:4096\n"
    "ds_read_b128 v[172:175], v59 offset:6144\n"
    "global_load_lds_dwordx4 v54, s[36:37] offset:2048\n"
    "ds_read_b64_tr_b16 v[176:177], v80 offset:8192\n"
    "ds_read_b64_tr_b16 v[178:179], v80 offset:9216\n"
    "ds_read_b64_tr_b16 v[180:181], v81 offset:8192\n"
    "ds_read_b64_tr_b16 v[182:183], v81 offset:9216\n"
    "global_load_lds_dwordx4 v55, s[36:37] offset:3072\n"
    "ds_read_b64_tr_b16 v[184:185], v82 offset:8192\n"
    "ds_read_b64_tr_b16 v[186:187], v82 offset:9216\n"
    "ds_read_b64_tr_b16 v[188:189], v83 offset:8192\n"
    "ds_read_b64_tr_b16 v[190:191], v83 offset:9216\n"
    "s_waitcnt vmcnt(10) lgkmcnt(0)\n"
    "s_barrier\n"
    "v_mfma_f32_16x16x32_bf16 v[192:195], v[144:147], v[128:131], v[192:195]\n"
    "v_lshlrev_b32 v102, 7, v102\n"
    "v_mfma_f32_16x16x32_bf16 v[192:195], v[176:179], v[160:163], v[192:195]\n"
    "v_mfma_f32_16x16x32_bf16 v[196:199], v[176:179], v[164:167], v[196:199]\n"
    "v_add_u32_dpp v52, v101, v48 quad_perm:[0,0,0,0] row_mask:0xf bank_mask:0xf\n"
    "v_mfma_f32_16x16x32_bf16 v[196:199], v[144:147], v[132:135], v[196:199]\n"
    "v_mfma_f32_16x16x32_bf16 v[200:203], v[144:147], v[136:139], v[200:203]\n"
    "v_add_u32_dpp v53, v101, v49 quad_perm:[1,1,1,1] row_mask:0xf bank_mask:0xf\n"
    "v_mfma_f32_16x16x32_bf16 v[200:203], v[176:179], v[168:171], v[200:203]\n"
    "v_mfma_f32_16x16x32_bf16 v[204:207], v[176:179], v[172:175], v[204:207]\n"
    "v_add_u32_dpp v54, v101, v50 quad_perm:[2,2,2,2] row_mask:0xf bank_mask:0xf\n"
    "v_mfma_f32_16x16x32_bf16 v[204:207], v[144:147], v[140:143], v[204:207]\n"
    "v_mfma_f32_16x16x32_bf16 v[220:223], v[148:151], v[140:143], v[220:223]\n"
    "v_add_u32_dpp v55, v101, v51 quad_perm:[3,3,3,3] row_mask:0xf bank_mask:0xf\n"
    "v_mfma_f32_16x16x32_bf16 v[220:223], v[180:183], v[172:175], v[220:223]\n"
    "v_mfma_f32_16x16x32_bf16 v[216:219], v[180:183], v[168:171], v[216:219]\n"
    "s_add_u32 s58, s58, 150528\n"
    "v_mfma_f32_16x16x32_bf16 v[216:219], v[148:151], v[136:139], v[216:219]\n"
    "v_mfma_f32_16x16x32_bf16 v[212:215], v[148:151], v[132:135], v[212:215]\n"
    "s_mov_b32 s40, s47\n"
    "v_mfma_f32_16x16x32_bf16 v[212:215], v[180:183], v[164:167], v[212:215]\n"
    "v_mfma_f32_16x16x32_bf16 v[208:211], v[180:183], v[160:163], v[208:211]\n"
    "s_mul_i32 s54, s40, 512\n"
    "v_mfma_f32_16x16x32_bf16 v[208:211], v[148:151], v[128:131], v[208:211]\n"
    "v_mfma_f32_16x16x32_bf16 v[224:227], v[152:155], v[128:131], v[224:227]\n"
    "s_add_u32 s54, s54, 2\n"
    "v_mfma_f32_16x16x32_bf16 v[224:227], v[184:187], v[160:163], v[224:227]\n"
    "v_mfma_f32_16x16x32_bf16 v[228:231], v[184:187], v[164:167], v[228:231]\n"
    "s_lshl_b32 s40, s40, 7\n"
    "v_mfma_f32_16x16x32_bf16 v[228:231], v[152:155], v[132:135], v[228:231]\n"
    "v_mfma_f32_16x16x32_bf16 v[232:235], v[152:155], v[136:139], v[232:235]\n"
    "s_add_u32 s40, s40, 32768\n"
    "v_mfma_f32_16x16x32_bf16 v[232:235], v[184:187], v[168:171], v[232:235]\n"
    "v_mfma_f32_16x16x32_bf16 v[236:239], v[184:187], v[172:175], v[236:239]\n"
    "s_add_u32 s52, s16, s40\n"
    "s_addc_u32 s53, s17, 0\n"
    "v_mfma_f32_16x16x32_bf16 v[236:239], v[152:155], v[140:143], v[236:239]\n"
    "v_mfma_f32_16x16x32_bf16 v[252:255], v[156:159], v[140:143], v[252:255]\n"
    "s_mov_b32 s57, 0x7fff7fff\n"
    "v_mfma_f32_16x16x32_bf16 v[252:255], v[188:191], v[172:175], v[252:255]\n"
    "v_mfma_f32_16x16x32_bf16 v[248:251], v[188:191], v[168:171], v[248:251]\n"
    "s_mov_b32 s56, 0\n"
    "v_mfma_f32_16x16x32_bf16 v[248:251], v[156:159], v[136:139], v[248:251]\n"
    "v_mfma_f32_16x16x32_bf16 v[244:247], v[156:159], v[132:135], v[244:247]\n"
    "s_mov_b32 s27, 0\n"
    "v_mfma_f32_16x16x32_bf16 v[244:247], v[188:191], v[164:167], v[244:247]\n"
    "v_mfma_f32_16x16x32_bf16 v[240:243], v[188:191], v[160:163], v[240:243]\n"
    "v_mfma_f32_16x16x32_bf16 v[240:243], v[156:159], v[128:131], v[240:243]\n"
    "s_waitcnt vmcnt(6)\n"
    "s_barrier\n"
    "ds_write_b32 v28, v29\n"
    "s_add_u32 m0, s35, 98304\n"
    "s_nop 0\n"
    "global_load_lds_dwordx4 v46, s[30:31] offset:0\n"
    "ds_read_b128 v[128:131], v56 offset:0\n"
    "ds_read_b128 v[132:135], v56 offset:2048\n"
    "ds_read_b128 v[136:139], v56 offset:4096\n"
    "ds_read_b128 v[140:143], v56 offset:6144\n"
    "global_load_lds_dwordx4 v47, s[30:31] offset:1024\n"
    "s_add_u32 s30, s30, 212992\n"
    "s_addc_u32 s31, s31, 0\n"
    "ds_read_b64_tr_b16 v[144:145], v76 offset:0\n"
    "ds_read_b64_tr_b16 v[146:147], v76 offset:1024\n"
    "ds_read_b64_tr_b16 v[148:149], v77 offset:0\n"
    "ds_read_b64_tr_b16 v[150:151], v77 offset:1024\n"
    "s_add_u32 m0, s34, 98304\n"
    "s_nop 0\n"
    "global_load_lds_dwordx4 v52, s[36:37] offset:0\n"
    "ds_read_b64_tr_b16 v[152:153], v78 offset:0\n"
    "ds_read_b64_tr_b16 v[154:155], v78 offset:1024\n"
    "ds_read_b64_tr_b16 v[156:157], v79 offset:0\n"
    "ds_read_b64_tr_b16 v[158:159], v79 offset:1024\n"
    "global_load_lds_dwordx4 v53, s[36:37] offset:1024\n"
    "ds_read_b128 v[160:163], v57 offset:0\n"
    "ds_read_b128 v[164:167], v57 offset:2048\n"
    "ds_read_b128 v[168:171], v57 offset:4096\n"
    "ds_read_b128 v[172:175], v57 offset:6144\n"
    "global_load_lds_dwordx4 v54, s[36:37] offset:2048\n"
    "ds_read_b64_tr_b16 v[176:177], v76 offset:8192\n"
    "ds_read_b64_tr_b16 v[178:179], v76 offset:9216\n"
    "ds_read_b64_tr_b16 v[180:181], v77 offset:8192\n"
    "ds_read_b64_tr_b16 v[182:183], v77 offset:9216\n"
    "global_load_lds_dwordx4 v55, s[36:37] offset:3072\n"
    "ds_read_b64_tr_b16 v[184:185], v78 offset:8192\n"
    "ds_read_b64_tr_b16 v[186:187], v78 offset:9216\n"
    "ds_read_b64_tr_b16 v[188:189], v79 offset:8192\n"
    "ds_read_b64_tr_b16 v[190:191], v79 offset:9216\n"
    "s_waitcnt vmcnt(10) lgkmcnt(0)\n"
    "s_barrier\n"
    "v_mfma_f32_16x16x32_bf16 v[192:195], v[144:147], v[128:131], v[192:195]\n"
    "v_lshlrev_b32 v103, 7, v103\n"
    "v_mfma_f32_16x16x32_bf16 v[192:195], v[176:179], v[160:163], v[192:195]\n"
    "v_mfma_f32_16x16x32_bf16 v[196:199], v[176:179], v[164:167], v[196:199]\n"
    "v_add_u32_dpp v52, v102, v48 quad_perm:[0,0,0,0] row_mask:0xf bank_mask:0xf\n"
    "v_mfma_f32_16x16x32_bf16 v[196:199], v[144:147], v[132:135], v[196:199]\n"
    "v_mfma_f32_16x16x32_bf16 v[200:203], v[144:147], v[136:139], v[200:203]\n"
    "v_add_u32_dpp v53, v102, v49 quad_perm:[1,1,1,1] row_mask:0xf bank_mask:0xf\n"
    "v_mfma_f32_16x16x32_bf16 v[200:203], v[176:179], v[168:171], v[200:203]\n"
    "v_mfma_f32_16x16x32_bf16 v[204:207], v[176:179], v[172:175], v[204:207]\n"
    "v_add_u32_dpp v54, v102, v50 quad_perm:[2,2,2,2] row_mask:0xf bank_mask:0xf\n"
    "v_mfma_f32_16x16x32_bf16 v[204:207], v[144:147], v[140:143], v[204:207]\n"
    "v_mfma_f32_16x16x32_bf16 v[220:223], v[148:151], v[140:143], v[220:223]\n"
    "v_add_u32_dpp v55, v102, v51 quad_perm:[3,3,3,3] row_mask:0xf bank_mask:0xf\n"
    "v_mfma_f32_16x16x32_bf16 v[220:223], v[180:183], v[172:175], v[220:223]\n"
    "v_mfma_f32_16x16x32_bf16 v[216:219], v[180:183], v[168:171], v[216:219]\n"
    "s_mov_b32 s55, 9\n"
    "v_mfma_f32_16x16x32_bf16 v[216:219], v[148:151], v[136:139], v[216:219]\n"
    "v_mfma_f32_16x16x32_bf16 v[212:215], v[148:151], v[132:135], v[212:215]\n"
    "s_mov_b32 s44, 0\n"
    "v_mfma_f32_16x16x32_bf16 v[212:215], v[180:183], v[164:167], v[212:215]\n"
    "v_mfma_f32_16x16x32_bf16 v[208:211], v[180:183], v[160:163], v[208:211]\n"
    "v_lshrrev_b32 v17, 3, v1\n"
    "v_mfma_f32_16x16x32_bf16 v[208:211], v[148:151], v[128:131], v[208:211]\n"
    "v_mfma_f32_16x16x32_bf16 v[224:227], v[152:155], v[128:131], v[224:227]\n"
    "v_mul_u32_u24 v17, 12, v17\n"
    "v_mfma_f32_16x16x32_bf16 v[224:227], v[184:187], v[160:163], v[224:227]\n"
    "v_mfma_f32_16x16x32_bf16 v[228:231], v[184:187], v[164:167], v[228:231]\n"
    "v_and_b32 v19, 7, v1\n"
    "v_mfma_f32_16x16x32_bf16 v[228:231], v[152:155], v[132:135], v[228:231]\n"
    "v_mfma_f32_16x16x32_bf16 v[232:235], v[152:155], v[136:139], v[232:235]\n"
    "v_lshlrev_b32 v19, 4, v19\n"
    "v_mfma_f32_16x16x32_bf16 v[232:235], v[184:187], v[168:171], v[232:235]\n"
    "v_mfma_f32_16x16x32_bf16 v[236:239], v[184:187], v[172:175], v[236:239]\n"
    "v_lshrrev_b32 v18, 5, v1\n"
    "v_mfma_f32_16x16x32_bf16 v[236:239], v[152:155], v[140:143], v[236:239]\n"
    "v_mfma_f32_16x16x32_bf16 v[252:255], v[156:159], v[140:143], v[252:255]\n"
    "v_lshlrev_b32 v18, 2, v18\n"
    "v_mfma_f32_16x16x32_bf16 v[252:255], v[188:191], v[172:175], v[252:255]\n"
    "v_mfma_f32_16x16x32_bf16 v[248:251], v[188:191], v[168:171], v[248:251]\n"
    "v_and_b32 v20, 31, v1\n"
    "v_mfma_f32_16x16x32_bf16 v[248:251], v[156:159], v[136:139], v[248:251]\n"
    "v_mfma_f32_16x16x32_bf16 v[244:247], v[156:159], v[132:135], v[244:247]\n"
    "v_lshlrev_b32 v20, 2, v20\n"
    "v_mfma_f32_16x16x32_bf16 v[244:247], v[188:191], v[164:167], v[244:247]\n"
    "v_mfma_f32_16x16x32_bf16 v[240:243], v[188:191], v[160:163], v[240:243]\n"
    "v_mfma_f32_16x16x32_bf16 v[240:243], v[156:159], v[128:131], v[240:243]\n"
    "s_waitcnt vmcnt(6)\n"
    "s_barrier\n"
    "s_add_u32 m0, s35, 0\n"
    "s_nop 0\n"
    "global_load_lds_dwordx4 v46, s[30:31] offset:0\n"
    "ds_read_b128 v[128:131], v56 offset:49152\n"
    "ds_read_b128 v[132:135], v56 offset:51200\n"
    "ds_read_b128 v[136:139], v56 offset:53248\n"
    "ds_read_b128 v[140:143], v56 offset:55296\n"
    "global_load_lds_dwordx4 v47, s[30:31] offset:1024\n"
    "s_add_u32 s30, s30, 212992\n"
    "s_addc_u32 s31, s31, 0\n"
    "ds_read_b64_tr_b16 v[144:145], v76 offset:49152\n"
    "ds_read_b64_tr_b16 v[146:147], v76 offset:50176\n"
    "ds_read_b64_tr_b16 v[148:149], v77 offset:49152\n"
    "ds_read_b64_tr_b16 v[150:151], v77 offset:50176\n"
    "s_add_u32 m0, s34, 0\n"
    "s_nop 0\n"
    "global_load_lds_dwordx4 v52, s[36:37] offset:0\n"
    "ds_read_b64_tr_b16 v[152:153], v78 offset:49152\n"
    "ds_read_b64_tr_b16 v[154:155], v78 offset:50176\n"
    "ds_read_b64_tr_b16 v[156:157], v79 offset:49152\n"
    "ds_read_b64_tr_b16 v[158:159], v79 offset:50176\n"
    "global_load_lds_dwordx4 v53, s[36:37] offset:1024\n"
    "ds_read_b128 v[160:163], v57 offset:49152\n"
    "ds_read_b128 v[164:167], v57 offset:51200\n"
    "ds_read_b128 v[168:171], v57 offset:53248\n"
    "ds_read_b128 v[172:175], v57 offset:55296\n"
    "global_load_lds_dwordx4 v54, s[36:37] offset:2048\n"
    "ds_read_b64_tr_b16 v[176:177], v76 offset:57344\n"
    "ds_read_b64_tr_b16 v[178:179], v76 offset:58368\n"
    "ds_read_b64_tr_b16 v[180:181], v77 offset:57344\n"
    "ds_read_b64_tr_b16 v[182:183], v77 offset:58368\n"
    "global_load_lds_dwordx4 v55, s[36:37] offset:3072\n"
    "ds_read_b64_tr_b16 v[184:185], v78 offset:57344\n"
    "ds_read_b64_tr_b16 v[186:187], v78 offset:58368\n"
    "ds_read_b64_tr_b16 v[188:189], v79 offset:57344\n"
    "ds_read_b64_tr_b16 v[190:191], v79 offset:58368\n"
    "s_waitcnt vmcnt(10) lgkmcnt(0)\n"
    "s_barrier\n"
    "v_mfma_f32_16x16x32_bf16 v[192:195], v[144:147], v[128:131], v[192:195]\n"
    "v_lshlrev_b32 v104, 7, v104\n"
    "v_mfma_f32_16x16x32_bf16 v[192:195], v[176:179], v[160:163], v[192:195]\n"
    "v_add_u32_dpp v52, v103, v48 quad_perm:[0,0,0,0] row_mask:0xf bank_mask:0xf\n"
    "v_mfma_f32_16x16x32_bf16 v[196:199], v[176:179], v[164:167], v[196:199]\n"
    "v_mfma_f32_16x16x32_bf16 v[196:199], v[144:147], v[132:135], v[196:199]\n"
    "v_add_u32_dpp v53, v103, v49 quad_perm:[1,1,1,1] row_mask:0xf bank_mask:0xf\n"
    "v_mfma_f32_16x16x32_bf16 v[200:203], v[144:147], v[136:139], v[200:203]\n"
    "v_mfma_f32_16x16x32_bf16 v[200:203], v[176:179], v[168:171], v[200:203]\n"
    "v_add_u32_dpp v54, v103, v50 quad_perm:[2,2,2,2] row_mask:0xf bank_mask:0xf\n"
    "v_mfma_f32_16x16x32_bf16 v[204:207], v[176:179], v[172:175], v[204:207]\n"
    "v_mfma_f32_16x16x32_bf16 v[204:207], v[144:147], v[140:143], v[204:207]\n"
    "v_add_u32_dpp v55, v103, v51 quad_perm:[3,3,3,3] row_mask:0xf bank_mask:0xf\n"
    "v_mfma_f32_16x16x32_bf16 v[220:223], v[148:151], v[140:143], v[220:223]\n"
    "v_mfma_f32_16x16x32_bf16 v[220:223], v[180:183], v[172:175], v[220:223]\n"
    "s_lshl2_add_u32 s40, s22, s58\n"
    "v_mfma_f32_16x16x32_bf16 v[216:219], v[180:183], v[168:171], v[216:219]\n"
    "v_mfma_f32_16x16x32_bf16 v[216:219], v[148:151], v[136:139], v[216:219]\n"
    "s_sub_u32 s40, s40, 20\n"
    "v_mfma_f32_16x16x32_bf16 v[212:215], v[148:151], v[132:135], v[212:215]\n"
    "v_mfma_f32_16x16x32_bf16 v[212:215], v[180:183], v[164:167], v[212:215]\n"
    "v_mov_b32 v25, s40\n"
    "v_mfma_f32_16x16x32_bf16 v[208:211], v[180:183], v[160:163], v[208:211]\n"
    "v_mfma_f32_16x16x32_bf16 v[208:211], v[148:151], v[128:131], v[208:211]\n"
    "s_lshl_b32 s43, s21, 16\n"
    "v_mfma_f32_16x16x32_bf16 v[224:227], v[152:155], v[128:131], v[224:227]\n"
    "v_mfma_f32_16x16x32_bf16 v[224:227], v[184:187], v[160:163], v[224:227]\n"
    "v_lshrrev_b32 v12, 4, v1\n"
    "v_mfma_f32_16x16x32_bf16 v[228:231], v[184:187], v[164:167], v[228:231]\n"
    "v_mfma_f32_16x16x32_bf16 v[228:231], v[152:155], v[132:135], v[228:231]\n"
    "v_bfe_u32 v13, v1, 2, 2\n"
    "v_mfma_f32_16x16x32_bf16 v[232:235], v[152:155], v[136:139], v[232:235]\n"
    "v_mfma_f32_16x16x32_bf16 v[232:235], v[184:187], v[168:171], v[232:235]\n"
    "v_and_b32 v21, 3, v1\n"
    "v_mfma_f32_16x16x32_bf16 v[236:239], v[184:187], v[172:175], v[236:239]\n"
    "v_mfma_f32_16x16x32_bf16 v[236:239], v[152:155], v[140:143], v[236:239]\n"
    "s_lshl_b32 s40, s22, 5\n"
    "v_mfma_f32_16x16x32_bf16 v[252:255], v[156:159], v[140:143], v[252:255]\n"
    "v_mfma_f32_16x16x32_bf16 v[252:255], v[188:191], v[172:175], v[252:255]\n"
    "s_add_u32 s40, s40, s43\n"
    "v_mfma_f32_16x16x32_bf16 v[248:251], v[188:191], v[168:171], v[248:251]\n"
    "v_mfma_f32_16x16x32_bf16 v[248:251], v[156:159], v[136:139], v[248:251]\n"
    "v_lshl_add_u32 v21, v21, 3, s40\n"
    "v_mfma_f32_16x16x32_bf16 v[244:247], v[156:159], v[132:135], v[244:247]\n"
    "v_mfma_f32_16x16x32_bf16 v[244:247], v[188:191], v[164:167], v[244:247]\n"
    "s_movk_i32 s41, 144\n"
    "v_mfma_f32_16x16x32_bf16 v[240:243], v[188:191], v[160:163], v[240:243]\n"
    "v_mfma_f32_16x16x32_bf16 v[240:243], v[156:159], v[128:131], v[240:243]\n"
    "s_waitcnt vmcnt(6)\n"
    "s_barrier\n"
    "s_add_u32 m0, s35, 49152\n"
    "s_nop 0\n"
    "global_load_lds_dwordx4 v46, s[30:31] offset:0\n"
    "ds_read_b128 v[128:131], v58 offset:0\n"
    "ds_read_b128 v[132:135], v58 offset:2048\n"
    "ds_read_b128 v[136:139], v58 offset:4096\n"
    "ds_read_b128 v[140:143], v58 offset:6144\n"
    "global_load_lds_dwordx4 v47, s[30:31] offset:1024\n"
    "s_bfe_u32 s66, s28, 0x80008\n"
    "s_mul_i32 s66, s66, 53248\n"
    "s_add_u32 s30, s6, s66\n"
    "s_addc_u32 s31, s7, 0\n"
    "ds_read_b64_tr_b16 v[144:145], v80 offset:0\n"
    "ds_read_b64_tr_b16 v[146:147], v80 offset:1024\n"
    "ds_read_b64_tr_b16 v[148:149], v81 offset:0\n"
    "ds_read_b64_tr_b16 v[150:151], v81 offset:1024\n"
    "s_add_u32 m0, s34, 49152\n"
    "s_nop 0\n"
    "global_load_lds_dwordx4 v52, s[36:37] offset:0\n"
    "ds_read_b64_tr_b16 v[152:153], v82 offset:0\n"
    "ds_read_b64_tr_b16 v[154:155], v82 offset:1024\n"
    "ds_read_b64_tr_b16 v[156:157], v83 offset:0\n"
    "ds_read_b64_tr_b16 v[158:159], v83 offset:1024\n"
    "global_load_lds_dwordx4 v53, s[36:37] offset:1024\n"
    "ds_read_b128 v[160:163], v59 offset:0\n"
    "ds_read_b128 v[164:167], v59 offset:2048\n"
    "ds_read_b128 v[168:171], v59 offset:4096\n"
    "ds_read_b128 v[172:175], v59 offset:6144\n"
    "global_load_lds_dwordx4 v54, s[36:37] offset:2048\n"
    "ds_read_b64_tr_b16 v[176:177], v80 offset:8192\n"
    "ds_read_b64_tr_b16 v[178:179], v80 offset:9216\n"
    "ds_read_b64_tr_b16 v[180:181], v81 offset:8192\n"
    "ds_read_b64_tr_b16 v[182:183], v81 offset:9216\n"
    "global_load_lds_dwordx4 v55, s[36:37] offset:3072\n"
    "ds_read_b64_tr_b16 v[184:185], v82 offset:8192\n"
    "ds_read_b64_tr_b16 v[186:187], v82 offset:9216\n"
    "ds_read_b64_tr_b16 v[188:189], v83 offset:8192\n"
    "ds_read_b64_tr_b16 v[190:191], v83 offset:9216\n"
    "s_waitcnt vmcnt(10) lgkmcnt(0)\n"
    "s_barrier\n"
    "v_mfma_f32_16x16x32_bf16 v[192:195], v[144:147], v[128:131], v[192:195]\n"
    "v_lshlrev_b32 v105, 7, v105\n"
    "v_mfma_f32_16x16x32_bf16 v[192:195], v[176:179], v[160:163], v[192:195]\n"
    "v_mfma_f32_16x16x32_bf16 v[196:199], v[176:179], v[164:167], v[196:199]\n"
    "v_add_u32_dpp v52, v104, v48 quad_perm:[0,0,0,0] row_mask:0xf bank_mask:0xf\n"
    "v_mfma_f32_16x16x32_bf16 v[196:199], v[144:147], v[132:135], v[196:199]\n"
    "v_mfma_f32_16x16x32_bf16 v[200:203], v[144:147], v[136:139], v[200:203]\n"
    "v_add_u32_dpp v53, v104, v49 quad_perm:[1,1,1,1] row_mask:0xf bank_mask:0xf\n"
    "v_mfma_f32_16x16x32_bf16 v[200:203], v[176:179], v[168:171], v[200:203]\n"
    "v_mfma_f32_16x16x32_bf16 v[204:207], v[176:179], v[172:175], v[204:207]\n"
    "v_add_u32_dpp v54, v104, v50 quad_perm:[2,2,2,2] row_mask:0xf bank_mask:0xf\n"
    "v_mfma_f32_16x16x32_bf16 v[204:207], v[144:147], v[140:143], v[204:207]\n"
    "v_mfma_f32_16x16x32_bf16 v[220:223], v[148:151], v[140:143], v[220:223]\n"
    "v_add_u32_dpp v55, v104, v51 quad_perm:[3,3,3,3] row_mask:0xf bank_mask:0xf\n"
    "v_mfma_f32_16x16x32_bf16 v[220:223], v[180:183], v[172:175], v[220:223]\n"
    "v_mfma_f32_16x16x32_bf16 v[216:219], v[180:183], v[168:171], v[216:219]\n"
    "v_add_u32 v22, v12, v13\n"
    "v_mfma_f32_16x16x32_bf16 v[216:219], v[148:151], v[136:139], v[216:219]\n"
    "v_mfma_f32_16x16x32_bf16 v[212:215], v[148:151], v[132:135], v[212:215]\n"
    "v_and_b32 v22, 3, v22\n"
    "v_mfma_f32_16x16x32_bf16 v[212:215], v[180:183], v[164:167], v[212:215]\n"
    "v_mfma_f32_16x16x32_bf16 v[208:211], v[180:183], v[160:163], v[208:211]\n"
    "v_mad_u32_u24 v4, v22, s41, v21\n"
    "v_mfma_f32_16x16x32_bf16 v[208:211], v[148:151], v[128:131], v[208:211]\n"
    "v_mfma_f32_16x16x32_bf16 v[224:227], v[152:155], v[128:131], v[224:227]\n"
    "v_cmp_gt_u32 vcc, 2, v13\n"
    "v_mfma_f32_16x16x32_bf16 v[224:227], v[184:187], v[160:163], v[224:227]\n"
    "v_mfma_f32_16x16x32_bf16 v[228:231], v[184:187], v[164:167], v[228:231]\n"
    "v_add_u32 v24, 22, v13\n"
    "v_mfma_f32_16x16x32_bf16 v[228:231], v[152:155], v[132:135], v[228:231]\n"
    "v_mfma_f32_16x16x32_bf16 v[232:235], v[152:155], v[136:139], v[232:235]\n"
    "v_lshlrev_b32 v13, 3, v13\n"
    "v_mfma_f32_16x16x32_bf16 v[232:235], v[184:187], v[168:171], v[232:235]\n"
    "v_mfma_f32_16x16x32_bf16 v[236:239], v[184:187], v[172:175], v[236:239]\n"
    "v_cndmask_b32 v22, v24, v22, vcc\n"
    "v_mfma_f32_16x16x32_bf16 v[236:239], v[152:155], v[140:143], v[236:239]\n"
    "v_mfma_f32_16x16x32_bf16 v[252:255], v[156:159], v[140:143], v[252:255]\n"
    "v_mad_u32_u24 v3, v22, s41, v21\n"
    "v_mfma_f32_16x16x32_bf16 v[252:255], v[188:191], v[172:175], v[252:255]\n"
    "v_mfma_f32_16x16x32_bf16 v[248:251], v[188:191], v[168:171], v[248:251]\n"
    "v_cmp_eq_u32 vcc, 3, v12\n"
    "v_mfma_f32_16x16x32_bf16 v[248:251], v[156:159], v[136:139], v[248:251]\n"
    "v_mfma_f32_16x16x32_bf16 v[244:247], v[156:159], v[132:135], v[244:247]\n"
    "v_xor_b32 v22, 0, v12\n"
    "v_mfma_f32_16x16x32_bf16 v[244:247], v[188:191], v[164:167], v[244:247]\n"
    "v_mfma_f32_16x16x32_bf16 v[240:243], v[188:191], v[160:163], v[240:243]\n"
    "v_mfma_f32_16x16x32_bf16 v[240:243], v[156:159], v[128:131], v[240:243]\n"
    "s_waitcnt vmcnt(6)\n"
    "s_barrier\n"
    "s_add_u32 m0, s35, 98304\n"
    "s_nop 0\n"
    "global_load_lds_dwordx4 v46, s[30:31] offset:0\n"
    "ds_read_b128 v[128:131], v56 offset:0\n"
    "ds_read_b128 v[132:135], v56 offset:2048\n"
    "ds_read_b128 v[136:139], v56 offset:4096\n"
    "global_load_lds_dwordx4 v47, s[30:31] offset:1024\n"
    "s_add_u32 s30, s30, 212992\n"
    "s_addc_u32 s31, s31, 0\n"
    "ds_read_b128 v[140:143], v56 offset:6144\n"
    "ds_read_b64_tr_b16 v[144:145], v76 offset:0\n"
    "ds_read_b64_tr_b16 v[146:147], v76 offset:1024\n"
    "s_add_u32 m0, s34, 98304\n"
    "s_nop 0\n"
    "global_load_lds_dwordx4 v52, s[36:37] offset:0\n"
    "ds_read_b64_tr_b16 v[148:149], v77 offset:0\n"
    "ds_read_b64_tr_b16 v[150:151], v77 offset:1024\n"
    "ds_read_b64_tr_b16 v[152:153], v78 offset:0\n"
    "global_load_lds_dwordx4 v53, s[36:37] offset:1024\n"
    "ds_read_b64_tr_b16 v[154:155], v78 offset:1024\n"
    "ds_read_b64_tr_b16 v[156:157], v79 offset:0\n"
    "ds_read_b64_tr_b16 v[158:159], v79 offset:1024\n"
    "global_load_lds_dwordx4 v54, s[36:37] offset:2048\n"
    "ds_read_b128 v[160:163], v57 offset:0\n"
    "ds_read_b128 v[164:167], v57 offset:2048\n"
    "ds_read_b128 v[168:171], v57 offset:4096\n"
    "global_load_lds_dwordx4 v55, s[36:37] offset:3072\n"
    "ds_read_b128 v[172:175], v57 offset:6144\n"
    "ds_read_b64_tr_b16 v[176:177], v76 offset:8192\n"
    "ds_read_b64_tr_b16 v[178:179], v76 offset:9216\n"
    "s_mov_b32 m0, s64\n"
    "s_nop 0\n"
    "global_load_lds_dword v43, s[10:11]\n"
    "ds_read_b64_tr_b16 v[180:181], v77 offset:8192\n"
    "ds_read_b64_tr_b16 v[182:183], v77 offset:9216\n"
    "ds_read_b64_tr_b16 v[184:185], v78 offset:8192\n"
    "ds_read_b64_tr_b16 v[186:187], v78 offset:9216\n"
    "ds_read_b64_tr_b16 v[188:189], v79 offset:8192\n"
    "ds_read_b64_tr_b16 v[190:191], v79 offset:9216\n"
    "s_waitcnt vmcnt(11) lgkmcnt(0)\n"
    "s_barrier\n"
    "v_mfma_f32_16x16x32_bf16 v[192:195], v[144:147], v[128:131], v[192:195]\n"
    "v_lshlrev_b32 v106, 7, v106\n"
    "v_mfma_f32_16x16x32_bf16 v[192:195], v[176:179], v[160:163], v[192:195]\n"
    "v_mfma_f32_16x16x32_bf16 v[196:199], v[176:179], v[164:167], v[196:199]\n"
    "v_add_u32_dpp v52, v105, v48 quad_perm:[0,0,0,0] row_mask:0xf bank_mask:0xf\n"
    "v_mfma_f32_16x16x32_bf16 v[196:199], v[144:147], v[132:135], v[196:199]\n"
    "v_mfma_f32_16x16x32_bf16 v[200:203], v[144:147], v[136:139], v[200:203]\n"
    "v_add_u32_dpp v53, v105, v49 quad_perm:[1,1,1,1] row_mask:0xf bank_mask:0xf\n"
    "v_mfma_f32_16x16x32_bf16 v[200:203], v[176:179], v[168:171], v[200:203]\n"
    "v_mfma_f32_16x16x32_bf16 v[204:207], v[176:179], v[172:175], v[204:207]\n"
    "v_add_u32_dpp v54, v105, v50 quad_perm:[2,2,2,2] row_mask:0xf bank_mask:0xf\n"
    "v_mfma_f32_16x16x32_bf16 v[204:207], v[144:147], v[140:143], v[204:207]\n"
    "v_mfma_f32_16x16x32_bf16 v[220:223], v[148:151], v[140:143], v[220:223]\n"
    "v_add_u32_dpp v55, v105, v51 quad_perm:[3,3,3,3] row_mask:0xf bank_mask:0xf\n"
    "v_mfma_f32_16x16x32_bf16 v[220:223], v[180:183], v[172:175], v[220:223]\n"
    "v_mfma_f32_16x16x32_bf16 v[216:219], v[180:183], v[168:171], v[216:219]\n"
    "v_and_b32 v22, 1, v22\n"
    "v_mfma_f32_16x16x32_bf16 v[216:219], v[148:151], v[136:139], v[216:219]\n"
    "v_mfma_f32_16x16x32_bf16 v[212:215], v[148:151], v[132:135], v[212:215]\n"
    "v_lshlrev_b32 v22, 1, v22\n"
    "v_mfma_f32_16x16x32_bf16 v[212:215], v[180:183], v[164:167], v[212:215]\n"
    "v_mfma_f32_16x16x32_bf16 v[208:211], v[180:183], v[160:163], v[208:211]\n"
    "v_lshl_add_u32 v22, v12, 3, v22\n"
    "v_mfma_f32_16x16x32_bf16 v[208:211], v[148:151], v[128:131], v[208:211]\n"
    "v_mfma_f32_16x16x32_bf16 v[224:227], v[152:155], v[128:131], v[224:227]\n"
    "s_mov_b32 s40, 0x05040100\n"
    "v_mfma_f32_16x16x32_bf16 v[224:227], v[184:187], v[160:163], v[224:227]\n"
    "v_mfma_f32_16x16x32_bf16 v[228:231], v[184:187], v[164:167], v[228:231]\n"
    "v_bfe_u32 v24, s40, v13, 8\n"
    "v_mfma_f32_16x16x32_bf16 v[228:231], v[152:155], v[132:135], v[228:231]\n"
    "v_mfma_f32_16x16x32_bf16 v[232:235], v[152:155], v[136:139], v[232:235]\n"
    "v_add_u32 v22, v22, v24\n"
    "v_mfma_f32_16x16x32_bf16 v[232:235], v[184:187], v[168:171], v[232:235]\n"
    "v_mfma_f32_16x16x32_bf16 v[236:239], v[184:187], v[172:175], v[236:239]\n"
    "s_mov_b32 s40, 0x1a191818\n"
    "v_mfma_f32_16x16x32_bf16 v[236:239], v[152:155], v[140:143], v[236:239]\n"
    "v_mfma_f32_16x16x32_bf16 v[252:255], v[156:159], v[140:143], v[252:255]\n"
    "v_bfe_u32 v24, s40, v13, 8\n"
    "v_mfma_f32_16x16x32_bf16 v[252:255], v[188:191], v[172:175], v[252:255]\n"
    "v_mfma_f32_16x16x32_bf16 v[248:251], v[188:191], v[168:171], v[248:251]\n"
    "v_cndmask_b32 v22, v22, v24, vcc\n"
    "v_mfma_f32_16x16x32_bf16 v[248:251], v[156:159], v[136:139], v[248:251]\n"
    "v_mfma_f32_16x16x32_bf16 v[244:247], v[156:159], v[132:135], v[244:247]\n"
    "v_mad_u32_u24 v5, v22, s41, v21\n"
    "v_mfma_f32_16x16x32_bf16 v[244:247], v[188:191], v[164:167], v[244:247]\n"
    "v_mfma_f32_16x16x32_bf16 v[240:243], v[188:191], v[160:163], v[240:243]\n"
    "v_mfma_f32_16x16x32_bf16 v[240:243], v[156:159], v[128:131], v[240:243]\n"
    "s_waitcnt vmcnt(7)\n"
    "s_barrier\n"
    "s_add_u32 m0, s35, 0\n"
    "s_nop 0\n"
    "global_load_lds_dwordx4 v46, s[30:31] offset:0\n"
    "ds_read_b128 v[128:131], v56 offset:49152\n"
    "ds_read_b128 v[132:135], v56 offset:51200\n"
    "ds_read_b128 v[136:139], v56 offset:53248\n"
    "ds_read_b128 v[140:143], v56 offset:55296\n"
    "global_load_lds_dwordx4 v47, s[30:31] offset:1024\n"
    "s_add_u32 s30, s30, 212992\n"
    "s_addc_u32 s31, s31, 0\n"
    "ds_read_b64_tr_b16 v[144:145], v76 offset:49152\n"
    "ds_read_b64_tr_b16 v[146:147], v76 offset:50176\n"
    "ds_read_b64_tr_b16 v[148:149], v77 offset:49152\n"
    "ds_read_b64_tr_b16 v[150:151], v77 offset:50176\n"
    "s_add_u32 m0, s34, 0\n"
    "s_nop 0\n"
    "global_load_lds_dwordx4 v52, s[36:37] offset:0\n"
    "ds_read_b64_tr_b16 v[152:153], v78 offset:49152\n"
    "ds_read_b64_tr_b16 v[154:155], v78 offset:50176\n"
    "ds_read_b64_tr_b16 v[156:157], v79 offset:49152\n"
    "ds_read_b64_tr_b16 v[158:159], v79 offset:50176\n"
    "global_load_lds_dwordx4 v53, s[36:37] offset:1024\n"
    "ds_read_b128 v[160:163], v57 offset:49152\n"
    "ds_read_b128 v[164:167], v57 offset:51200\n"
    "ds_read_b128 v[168:171], v57 offset:53248\n"
    "ds_read_b128 v[172:175], v57 offset:55296\n"
    "global_load_lds_dwordx4 v54, s[36:37] offset:2048\n"
    "ds_read_b64_tr_b16 v[176:177], v76 offset:57344\n"
    "ds_read_b64_tr_b16 v[178:179], v76 offset:58368\n"
    "ds_read_b64_tr_b16 v[180:181], v77 offset:57344\n"
    "ds_read_b64_tr_b16 v[182:183], v77 offset:58368\n"
    "global_load_lds_dwordx4 v55, s[36:37] offset:3072\n"
    "ds_read_b64_tr_b16 v[184:185], v78 offset:57344\n"
    "ds_read_b64_tr_b16 v[186:187], v78 offset:58368\n"
    "ds_read_b64_tr_b16 v[188:189], v79 offset:57344\n"
    "ds_read_b64_tr_b16 v[190:191], v79 offset:58368\n"
    "s_waitcnt vmcnt(11) lgkmcnt(0)\n"
    "s_barrier\n"
    "v_mfma_f32_16x16x32_bf16 v[192:195], v[144:147], v[128:131], v[192:195]\n"
    "v_lshlrev_b32 v107, 7, v107\n"
    "v_mfma_f32_16x16x32_bf16 v[192:195], v[176:179], v[160:163], v[192:195]\n"
    "v_add_u32_dpp v52, v106, v48 quad_perm:[0,0,0,0] row_mask:0xf bank_mask:0xf\n"
    "v_mfma_f32_16x16x32_bf16 v[196:199], v[176:179], v[164:167], v[196:199]\n"
    "v_mfma_f32_16x16x32_bf16 v[196:199], v[144:147], v[132:135], v[196:199]\n"
    "v_add_u32_dpp v53, v106, v49 quad_perm:[1,1,1,1] row_mask:0xf bank_mask:0xf\n"
    "v_mfma_f32_16x16x32_bf16 v[200:203], v[144:147], v[136:139], v[200:203]\n"
    "v_mfma_f32_16x16x32_bf16 v[200:203], v[176:179], v[168:171], v[200:203]\n"
    "v_add_u32_dpp v54, v106, v50 quad_perm:[2,2,2,2] row_mask:0xf bank_mask:0xf\n"
    "v_mfma_f32_16x16x32_bf16 v[204:207], v[176:179], v[172:175], v[204:207]\n"
    "v_mfma_f32_16x16x32_bf16 v[204:207], v[144:147], v[140:143], v[204:207]\n"
    "v_add_u32_dpp v55, v106, v51 quad_perm:[3,3,3,3] row_mask:0xf bank_mask:0xf\n"
    "v_mfma_f32_16x16x32_bf16 v[220:223], v[148:151], v[140:143], v[220:223]\n"
    "v_mfma_f32_16x16x32_bf16 v[220:223], v[180:183], v[172:175], v[220:223]\n"
    "v_xor_b32 v22, 1, v12\n"
    "v_mfma_f32_16x16x32_bf16 v[216:219], v[180:183], v[168:171], v[216:219]\n"
    "v_mfma_f32_16x16x32_bf16 v[216:219], v[148:151], v[136:139], v[216:219]\n"
    "v_and_b32 v22, 1, v22\n"
    "v_mfma_f32_16x16x32_bf16 v[212:215], v[148:151], v[132:135], v[212:215]\n"
    "v_mfma_f32_16x16x32_bf16 v[212:215], v[180:183], v[164:167], v[212:215]\n"
    "v_lshlrev_b32 v22, 1, v22\n"
    "v_mfma_f32_16x16x32_bf16 v[208:211], v[180:183], v[160:163], v[208:211]\n"
    "v_mfma_f32_16x16x32_bf16 v[208:211], v[148:151], v[128:131], v[208:211]\n"
    "v_lshl_add_u32 v22, v12, 3, v22\n"
    "v_mfma_f32_16x16x32_bf16 v[224:227], v[152:155], v[128:131], v[224:227]\n"
    "v_mfma_f32_16x16x32_bf16 v[224:227], v[184:187], v[160:163], v[224:227]\n"
    "s_mov_b32 s40, 0x05040100\n"
    "v_mfma_f32_16x16x32_bf16 v[228:231], v[184:187], v[164:167], v[228:231]\n"
    "v_mfma_f32_16x16x32_bf16 v[228:231], v[152:155], v[132:135], v[228:231]\n"
    "v_bfe_u32 v24, s40, v13, 8\n"
    "v_mfma_f32_16x16x32_bf16 v[232:235], v[152:155], v[136:139], v[232:235]\n"
    "v_mfma_f32_16x16x32_bf16 v[232:235], v[184:187], v[168:171], v[232:235]\n"
    "v_add_u32 v22, v22, v24\n"
    "v_mfma_f32_16x16x32_bf16 v[236:239], v[184:187], v[172:175], v[236:239]\n"
    "v_mfma_f32_16x16x32_bf16 v[236:239], v[152:155], v[140:143], v[236:239]\n"
    "s_mov_b32 s40, 0x1a191918\n"
    "v_mfma_f32_16x16x32_bf16 v[252:255], v[156:159], v[140:143], v[252:255]\n"
    "v_mfma_f32_16x16x32_bf16 v[252:255], v[188:191], v[172:175], v[252:255]\n"
    "v_bfe_u32 v24, s40, v13, 8\n"
    "v_mfma_f32_16x16x32_bf16 v[248:251], v[188:191], v[168:171], v[248:251]\n"
    "v_mfma_f32_16x16x32_bf16 v[248:251], v[156:159], v[136:139], v[248:251]\n"
    "v_cndmask_b32 v22, v22, v24, vcc\n"
    "v_mfma_f32_16x16x32_bf16 v[244:247], v[156:159], v[132:135], v[244:247]\n"
    "v_mfma_f32_16x16x32_bf16 v[244:247], v[188:191], v[164:167], v[244:247]\n"
    "v_mad_u32_u24 v6, v22, s41, v21\n"
    "v_mfma_f32_16x16x32_bf16 v[240:243], v[188:191], v[160:163], v[240:243]\n"
    "v_mfma_f32_16x16x32_bf16 v[240:243], v[156:159], v[128:131], v[240:243]\n"
    "s_waitcnt vmcnt(7)\n"
    "s_barrier\n"
    "s_add_u32 m0, s35, 49152\n"
    "s_nop 0\n"
    "global_load_lds_dwordx4 v46, s[30:31] offset:0\n"
    "ds_read_b128 v[128:131], v58 offset:0\n"
    "ds_read_b128 v[132:135], v58 offset:2048\n"
    "ds_read_b128 v[136:139], v58 offset:4096\n"
    "ds_read_b128 v[140:143], v58 offset:6144\n"
    "global_load_lds_dwordx4 v47, s[30:31] offset:1024\n"
    "s_add_u32 s30, s30, 212992\n"
    "s_addc_u32 s31, s31, 0\n"
    "ds_read_b64_tr_b16 v[144:145], v80 offset:0\n"
    "ds_read_b64_tr_b16 v[146:147], v80 offset:1024\n"
    "ds_read_b64_tr_b16 v[148:149], v81 offset:0\n"
    "ds_read_b64_tr_b16 v[150:151], v81 offset:1024\n"
    "s_add_u32 m0, s34, 49152\n"
    "s_nop 0\n"
    "global_load_lds_dwordx4 v52, s[36:37] offset:0\n"
    "ds_read_b64_tr_b16 v[152:153], v82 offset:0\n"
    "ds_read_b64_tr_b16 v[154:155], v82 offset:1024\n"
    "ds_read_b64_tr_b16 v[156:157], v83 offset:0\n"
    "ds_read_b64_tr_b16 v[158:159], v83 offset:1024\n"
    "global_load_lds_dwordx4 v53, s[36:37] offset:1024\n"
    "ds_read_b128 v[160:163], v59 offset:0\n"
    "ds_read_b128 v[164:167], v59 offset:2048\n"
    "ds_read_b128 v[168:171], v59 offset:4096\n"
    "ds_read_b128 v[172:175], v59 offset:6144\n"
    "global_load_lds_dwordx4 v54, s[36:37] offset:2048\n"
    "ds_read_b64_tr_b16 v[176:177], v80 offset:8192\n"
    "ds_read_b64_tr_b16 v[178:179], v80 offset:9216\n"
    "ds_read_b64_tr_b16 v[180:181], v81 offset:8192\n"
    "ds_read_b64_tr_b16 v[182:183], v81 offset:9216\n"
    "global_load_lds_dwordx4 v55, s[36:37] offset:3072\n"
    "ds_read_b64_tr_b16 v[184:185], v82 offset:8192\n"
    "ds_read_b64_tr_b16 v[186:187], v82 offset:9216\n"
    "ds_read_b64_tr_b16 v[188:189], v83 offset:8192\n"
    "ds_read_b64_tr_b16 v[190:191], v83 offset:9216\n"
    "s_waitcnt vmcnt(10) lgkmcnt(0)\n"
    "s_barrier\n"
    "v_mfma_f32_16x16x32_bf16 v[192:195], v[144:147], v[128:131], v[192:195]\n"
    "v_lshlrev_b32 v108, 7, v108\n"
    "v_mfma_f32_16x16x32_bf16 v[192:195], v[176:179], v[160:163], v[192:195]\n"
    "v_mfma_f32_16x16x32_bf16 v[196:199], v[176:179], v[164:167], v[196:199]\n"
    "v_add_u32_dpp v52, v107, v48 quad_perm:[0,0,0,0] row_mask:0xf bank_mask:0xf\n"
    "v_mfma_f32_16x16x32_bf16 v[196:199], v[144:147], v[132:135], v[196:199]\n"
    "v_mfma_f32_16x16x32_bf16 v[200:203], v[144:147], v[136:139], v[200:203]\n"
    "v_add_u32_dpp v53, v107, v49 quad_perm:[1,1,1,1] row_mask:0xf bank_mask:0xf\n"
    "v_mfma_f32_16x16x32_bf16 v[200:203], v[176:179], v[168:171], v[200:203]\n"
    "v_mfma_f32_16x16x32_bf16 v[204:207], v[176:179], v[172:175], v[204:207]\n"
    "v_add_u32_dpp v54, v107, v50 quad_perm:[2,2,2,2] row_mask:0xf bank_mask:0xf\n"
    "v_mfma_f32_16x16x32_bf16 v[204:207], v[144:147], v[140:143], v[204:207]\n"
    "v_mfma_f32_16x16x32_bf16 v[220:223], v[148:151], v[140:143], v[220:223]\n"
    "v_add_u32_dpp v55, v107, v51 quad_perm:[3,3,3,3] row_mask:0xf bank_mask:0xf\n"
    "v_mfma_f32_16x16x32_bf16 v[220:223], v[180:183], v[172:175], v[220:223]\n"
    "v_mfma_f32_16x16x32_bf16 v[216:219], v[180:183], v[168:171], v[216:219]\n"
    "v_bfrev_b32 v14, v12\n"
    "v_mfma_f32_16x16x32_bf16 v[216:219], v[148:151], v[136:139], v[216:219]\n"
    "v_mfma_f32_16x16x32_bf16 v[212:215], v[148:151], v[132:135], v[212:215]\n"
    "v_lshrrev_b32 v14, 26, v14\n"
    "v_mfma_f32_16x16x32_bf16 v[212:215], v[180:183], v[164:167], v[212:215]\n"
    "v_mfma_f32_16x16x32_bf16 v[208:211], v[180:183], v[160:163], v[208:211]\n"
    "s_add_u32 s40, s22, 3\n"
    "v_mfma_f32_16x16x32_bf16 v[208:211], v[148:151], v[128:131], v[208:211]\n"
    "v_mfma_f32_16x16x32_bf16 v[224:227], v[152:155], v[128:131], v[224:227]\n"
    "s_and_b32 s40, s40, 3\n"
    "v_mfma_f32_16x16x32_bf16 v[224:227], v[184:187], v[160:163], v[224:227]\n"
    "v_mfma_f32_16x16x32_bf16 v[228:231], v[184:187], v[164:167], v[228:231]\n"
    "s_lshl2_add_u32 s40, s40, s22\n"
    "v_mfma_f32_16x16x32_bf16 v[228:231], v[152:155], v[132:135], v[228:231]\n"
    "v_mfma_f32_16x16x32_bf16 v[232:235], v[152:155], v[136:139], v[232:235]\n"
    "s_add_u32 s40, s40, s58\n"
    "v_mfma_f32_16x16x32_bf16 v[232:235], v[184:187], v[168:171], v[232:235]\n"
    "v_mfma_f32_16x16x32_bf16 v[236:239], v[184:187], v[172:175], v[236:239]\n"
    "s_lshl_b32 s40, s40, 2\n"
    "v_mfma_f32_16x16x32_bf16 v[236:239], v[152:155], v[140:143], v[236:239]\n"
    "v_mfma_f32_16x16x32_bf16 v[252:255], v[156:159], v[140:143], v[252:255]\n"
    "s_add_u32 s40, s40, -450048\n"
    "v_mfma_f32_16x16x32_bf16 v[252:255], v[188:191], v[172:175], v[252:255]\n"
    "v_mfma_f32_16x16x32_bf16 v[248:251], v[188:191], v[168:171], v[248:251]\n"
    "v_add_u32 v14, s40, v14\n"
    "v_mfma_f32_16x16x32_bf16 v[248:251], v[156:159], v[136:139], v[248:251]\n"
    "v_mfma_f32_16x16x32_bf16 v[244:247], v[156:159], v[132:135], v[244:247]\n"
    "v_and_b32 v2, 1, v1\n"
    "v_mfma_f32_16x16x32_bf16 v[244:247], v[188:191], v[164:167], v[244:247]\n"
    "v_mfma_f32_16x16x32_bf16 v[240:243], v[188:191], v[160:163], v[240:243]\n"
    "v_mfma_f32_16x16x32_bf16 v[240:243], v[156:159], v[128:131], v[240:243]\n"
    "s_waitcnt vmcnt(6)\n"
    "s_barrier\n"
    "s_add_u32 m0, s35, 98304\n"
    "s_nop 0\n"
    "global_load_lds_dwordx4 v46, s[30:31] offset:0\n"
    "ds_read_b128 v[128:131], v56 offset:0\n"
    "ds_read_b128 v[132:135], v56 offset:2048\n"
    "ds_read_b128 v[136:139], v56 offset:4096\n"
    "ds_read_b128 v[140:143], v56 offset:6144\n"
    "global_load_lds_dwordx4 v47, s[30:31] offset:1024\n"
    "s_bfe_u32 s66, s28, 0x80010\n"
    "s_mul_i32 s66, s66, 53248\n"
    "s_add_u32 s30, s6, s66\n"
    "s_addc_u32 s31, s7, 0\n"
    "ds_read_b64_tr_b16 v[144:145], v76 offset:0\n"
    "ds_read_b64_tr_b16 v[146:147], v76 offset:1024\n"
    "ds_read_b64_tr_b16 v[148:149], v77 offset:0\n"
    "ds_read_b64_tr_b16 v[150:151], v77 offset:1024\n"
    "s_add_u32 m0, s34, 98304\n"
    "s_nop 0\n"
    "global_load_lds_dwordx4 v52, s[36:37] offset:0\n"
    "ds_read_b64_tr_b16 v[152:153], v78 offset:0\n"
    "ds_read_b64_tr_b16 v[154:155], v78 offset:1024\n"
    "ds_read_b64_tr_b16 v[156:157], v79 offset:0\n"
    "ds_read_b64_tr_b16 v[158:159], v79 offset:1024\n"
    "global_load_lds_dwordx4 v53, s[36:37] offset:1024\n"
    "ds_read_b128 v[160:163], v57 offset:0\n"
    "ds_read_b128 v[164:167], v57 offset:2048\n"
    "ds_read_b128 v[168:171], v57 offset:4096\n"
    "ds_read_b128 v[172:175], v57 offset:6144\n"
    "global_load_lds_dwordx4 v54, s[36:37] offset:2048\n"
    "ds_read_b64_tr_b16 v[176:177], v76 offset:8192\n"
    "ds_read_b64_tr_b16 v[178:179], v76 offset:9216\n"
    "ds_read_b64_tr_b16 v[180:181], v77 offset:8192\n"
    "ds_read_b64_tr_b16 v[182:183], v77 offset:9216\n"
    "global_load_lds_dwordx4 v55, s[36:37] offset:3072\n"
    "ds_read_b64_tr_b16 v[184:185], v78 offset:8192\n"
    "ds_read_b64_tr_b16 v[186:187], v78 offset:9216\n"
    "ds_read_b64_tr_b16 v[188:189], v79 offset:8192\n"
    "ds_read_b64_tr_b16 v[190:191], v79 offset:9216\n"
    "s_waitcnt vmcnt(10) lgkmcnt(0)\n"
    "s_barrier\n"
    "v_mfma_f32_16x16x32_bf16 v[192:195], v[144:147], v[128:131], v[192:195]\n"
    "v_lshlrev_b32 v109, 7, v109\n"
    "v_mfma_f32_16x16x32_bf16 v[192:195], v[176:179], v[160:163], v[192:195]\n"
    "v_mfma_f32_16x16x32_bf16 v[196:199], v[176:179], v[164:167], v[196:199]\n"
    "v_add_u32_dpp v52, v108, v48 quad_perm:[0,0,0,0] row_mask:0xf bank_mask:0xf\n"
    "v_mfma_f32_16x16x32_bf16 v[196:199], v[144:147], v[132:135], v[196:199]\n"
    "v_mfma_f32_16x16x32_bf16 v[200:203], v[144:147], v[136:139], v[200:203]\n"
    "v_add_u32_dpp v53, v108, v49 quad_perm:[1,1,1,1] row_mask:0xf bank_mask:0xf\n"
    "v_mfma_f32_16x16x32_bf16 v[200:203], v[176:179], v[168:171], v[200:203]\n"
    "v_mfma_f32_16x16x32_bf16 v[204:207], v[176:179], v[172:175], v[204:207]\n"
    "v_add_u32_dpp v54, v108, v50 quad_perm:[2,2,2,2] row_mask:0xf bank_mask:0xf\n"
    "v_mfma_f32_16x16x32_bf16 v[204:207], v[144:147], v[140:143], v[204:207]\n"
    "v_mfma_f32_16x16x32_bf16 v[220:223], v[148:151], v[140:143], v[220:223]\n"
    "v_add_u32_dpp v55, v108, v51 quad_perm:[3,3,3,3] row_mask:0xf bank_mask:0xf\n"
    "v_mfma_f32_16x16x32_bf16 v[220:223], v[180:183], v[172:175], v[220:223]\n"
    "v_mfma_f32_16x16x32_bf16 v[216:219], v[180:183], v[168:171], v[216:219]\n"
    "v_mul_u32_u24 v2, 0xaaaa, v2\n"
    "v_mfma_f32_16x16x32_bf16 v[216:219], v[148:151], v[136:139], v[216:219]\n"
    "v_mfma_f32_16x16x32_bf16 v[212:215], v[148:151], v[132:135], v[212:215]\n"
    "v_add_u32 v2, 0x4444, v2\n"
    "v_mfma_f32_16x16x32_bf16 v[212:215], v[180:183], v[164:167], v[212:215]\n"
    "v_mfma_f32_16x16x32_bf16 v[208:211], v[180:183], v[160:163], v[208:211]\n"
    "v_mad_u32_u24 v15, v17, 36, v19\n"
    "v_mfma_f32_16x16x32_bf16 v[208:211], v[148:151], v[128:131], v[208:211]\n"
    "v_mfma_f32_16x16x32_bf16 v[224:227], v[152:155], v[128:131], v[224:227]\n"
    "v_mad_u32_u24 v16, v18, 36, v20\n"
    "v_mfma_f32_16x16x32_bf16 v[224:227], v[184:187], v[160:163], v[224:227]\n"
    "v_mfma_f32_16x16x32_bf16 v[228:231], v[184:187], v[164:167], v[228:231]\n"
    "s_lshl4_add_u32 s41, s21, s22\n"
    "v_mfma_f32_16x16x32_bf16 v[228:231], v[152:155], v[132:135], v[228:231]\n"
    "v_mfma_f32_16x16x32_bf16 v[232:235], v[152:155], v[136:139], v[232:235]\n"
    "v_lshrrev_b32 v12, 5, v19\n"
    "v_mfma_f32_16x16x32_bf16 v[232:235], v[184:187], v[168:171], v[232:235]\n"
    "v_mfma_f32_16x16x32_bf16 v[236:239], v[184:187], v[172:175], v[236:239]\n"
    "v_sub_u32 v12, 1, v12\n"
    "v_mfma_f32_16x16x32_bf16 v[236:239], v[152:155], v[140:143], v[236:239]\n"
    "v_mfma_f32_16x16x32_bf16 v[252:255], v[156:159], v[140:143], v[252:255]\n"
    "v_and_b32 v12, 3, v12\n"
    "v_mfma_f32_16x16x32_bf16 v[252:255], v[188:191], v[172:175], v[252:255]\n"
    "v_mfma_f32_16x16x32_bf16 v[248:251], v[188:191], v[168:171], v[248:251]\n"
    "v_add_lshl_u32 v12, v12, s41, 12\n"
    "v_mfma_f32_16x16x32_bf16 v[248:251], v[156:159], v[136:139], v[248:251]\n"
    "v_mfma_f32_16x16x32_bf16 v[244:247], v[156:159], v[132:135], v[244:247]\n"
    "v_add_u32 v15, v12, v15\n"
    "v_mfma_f32_16x16x32_bf16 v[244:247], v[188:191], v[164:167], v[244:247]\n"
    "v_mfma_f32_16x16x32_bf16 v[240:243], v[188:191], v[160:163], v[240:243]\n"
    "v_mfma_f32_16x16x32_bf16 v[240:243], v[156:159], v[128:131], v[240:243]\n"
    "s_waitcnt vmcnt(6)\n"
    "s_barrier\n"
    "s_add_u32 m0, s35, 0\n"
    "s_nop 0\n"
    "global_load_lds_dwordx4 v46, s[30:31] offset:0\n"
    "ds_read_b128 v[128:131], v56 offset:49152\n"
    "ds_read_b128 v[132:135], v56 offset:51200\n"
    "ds_read_b128 v[136:139], v56 offset:53248\n"
    "ds_read_b128 v[140:143], v56 offset:55296\n"
    "global_load_lds_dwordx4 v47, s[30:31] offset:1024\n"
    "s_add_u32 s30, s30, 212992\n"
    "s_addc_u32 s31, s31, 0\n"
    "ds_read_b64_tr_b16 v[144:145], v76 offset:49152\n"
    "ds_read_b64_tr_b16 v[146:147], v76 offset:50176\n"
    "ds_read_b64_tr_b16 v[148:149], v77 offset:49152\n"
    "ds_read_b64_tr_b16 v[150:151], v77 offset:50176\n"
    "s_add_u32 m0, s34, 0\n"
    "s_nop 0\n"
    "global_load_lds_dwordx4 v52, s[36:37] offset:0\n"
    "ds_read_b64_tr_b16 v[152:153], v78 offset:49152\n"
    "ds_read_b64_tr_b16 v[154:155], v78 offset:50176\n"
    "ds_read_b64_tr_b16 v[156:157], v79 offset:49152\n"
    "ds_read_b64_tr_b16 v[158:159], v79 offset:50176\n"
    "global_load_lds_dwordx4 v53, s[36:37] offset:1024\n"
    "ds_read_b128 v[160:163], v57 offset:49152\n"
    "ds_read_b128 v[164:167], v57 offset:51200\n"
    "ds_read_b128 v[168:171], v57 offset:53248\n"
    "ds_read_b128 v[172:175], v57 offset:55296\n"
    "global_load_lds_dwordx4 v54, s[36:37] offset:2048\n"
    "ds_read_b64_tr_b16 v[176:177], v76 offset:57344\n"
    "ds_read_b64_tr_b16 v[178:179], v76 offset:58368\n"
    "ds_read_b64_tr_b16 v[180:181], v77 offset:57344\n"
    "ds_read_b64_tr_b16 v[182:183], v77 offset:58368\n"
    "global_load_lds_dwordx4 v55, s[36:37] offset:3072\n"
    "ds_read_b64_tr_b16 v[184:185], v78 offset:57344\n"
    "ds_read_b64_tr_b16 v[186:187], v78 offset:58368\n"
    "ds_read_b64_tr_b16 v[188:189], v79 offset:57344\n"
    "ds_read_b64_tr_b16 v[190:191], v79 offset:58368\n"
    "s_waitcnt vmcnt(10) lgkmcnt(0)\n"
    "s_barrier\n"
    "v_mfma_f32_16x16x32_bf16 v[192:195], v[144:147], v[128:131], v[192:195]\n"
    "v_lshlrev_b32 v110, 7, v110\n"
    "v_mfma_f32_16x16x32_bf16 v[192:195], v[176:179], v[160:163], v[192:195]\n"
    "v_add_u32_dpp v52, v109, v48 quad_perm:[0,0,0,0] row_mask:0xf bank_mask:0xf\n"
    "v_mfma_f32_16x16x32_bf16 v[196:199], v[176:179], v[164:167], v[196:199]\n"
    "v_mfma_f32_16x16x32_bf16 v[196:199], v[144:147], v[132:135], v[196:199]\n"
    "v_add_u32_dpp v53, v109, v49 quad_perm:[1,1,1,1] row_mask:0xf bank_mask:0xf\n"
    "v_mfma_f32_16x16x32_bf16 v[200:203], v[144:147], v[136:139], v[200:203]\n"
    "v_mfma_f32_16x16x32_bf16 v[200:203], v[176:179], v[168:171], v[200:203]\n"
    "v_add_u32_dpp v54, v109, v50 quad_perm:[2,2,2,2] row_mask:0xf bank_mask:0xf\n"
    "v_mfma_f32_16x16x32_bf16 v[204:207], v[176:179], v[172:175], v[204:207]\n"
    "v_mfma_f32_16x16x32_bf16 v[204:207], v[144:147], v[140:143], v[204:207]\n"
    "v_add_u32_dpp v55, v109, v51 quad_perm:[3,3,3,3] row_mask:0xf bank_mask:0xf\n"
    "v_mfma_f32_16x16x32_bf16 v[220:223], v[148:151], v[140:143], v[220:223]\n"
    "v_mfma_f32_16x16x32_bf16 v[220:223], v[180:183], v[172:175], v[220:223]\n"
    "v_lshrrev_b32 v12, 5, v20\n"
    "v_mfma_f32_16x16x32_bf16 v[216:219], v[180:183], v[168:171], v[216:219]\n"
    "v_mfma_f32_16x16x32_bf16 v[216:219], v[148:151], v[136:139], v[216:219]\n"
    "v_sub_u32 v12, 1, v12\n"
    "v_mfma_f32_16x16x32_bf16 v[212:215], v[148:151], v[132:135], v[212:215]\n"
    "v_mfma_f32_16x16x32_bf16 v[212:215], v[180:183], v[164:167], v[212:215]\n"
    "v_and_b32 v12, 3, v12\n"
    "v_mfma_f32_16x16x32_bf16 v[208:211], v[180:183], v[160:163], v[208:211]\n"
    "v_mfma_f32_16x16x32_bf16 v[208:211], v[148:151], v[128:131], v[208:211]\n"
    "v_add_lshl_u32 v12, v12, s41, 12\n"
    "v_mfma_f32_16x16x32_bf16 v[224:227], v[152:155], v[128:131], v[224:227]\n"
    "v_mfma_f32_16x16x32_bf16 v[224:227], v[184:187], v[160:163], v[224:227]\n"
    "v_add_u32 v16, v12, v16\n"
    "v_mfma_f32_16x16x32_bf16 v[228:231], v[184:187], v[164:167], v[228:231]\n"
    "v_mfma_f32_16x16x32_bf16 v[228:231], v[152:155], v[132:135], v[228:231]\n"
    "s_mul_i32 s41, s47, 13\n"
    "v_mfma_f32_16x16x32_bf16 v[232:235], v[152:155], v[136:139], v[232:235]\n"
    "v_mfma_f32_16x16x32_bf16 v[232:235], v[184:187], v[168:171], v[232:235]\n"
    "s_sub_u32 s41, s3, s41\n"
    "v_mfma_f32_16x16x32_bf16 v[236:239], v[184:187], v[172:175], v[236:239]\n"
    "v_mfma_f32_16x16x32_bf16 v[236:239], v[152:155], v[140:143], v[236:239]\n"
    "s_lshl1_add_u32 s41, s41, s21\n"
    "v_mfma_f32_16x16x32_bf16 v[252:255], v[156:159], v[140:143], v[252:255]\n"
    "v_mfma_f32_16x16x32_bf16 v[252:255], v[188:191], v[172:175], v[252:255]\n"
    "s_mul_i32 s41, s41, 4\n"
    "v_mfma_f32_16x16x32_bf16 v[248:251], v[188:191], v[168:171], v[248:251]\n"
    "v_mfma_f32_16x16x32_bf16 v[248:251], v[156:159], v[136:139], v[248:251]\n"
    "s_add_u32 s41, s41, s22\n"
    "v_mfma_f32_16x16x32_bf16 v[244:247], v[156:159], v[132:135], v[244:247]\n"
    "v_mfma_f32_16x16x32_bf16 v[244:247], v[188:191], v[164:167], v[244:247]\n"
    "v_mov_b32 v24, s41\n"
    "v_mfma_f32_16x16x32_bf16 v[240:243], v[188:191], v[160:163], v[240:243]\n"
    "v_mfma_f32_16x16x32_bf16 v[240:243], v[156:159], v[128:131], v[240:243]\n"
    "s_waitcnt vmcnt(6)\n"
    "s_barrier\n"
    "s_add_u32 m0, s35, 49152\n"
    "s_nop 0\n"
    "global_load_lds_dwordx4 v46, s[30:31] offset:0\n"
    "ds_read_b128 v[128:131], v58 offset:0\n"
    "ds_read_b128 v[132:135], v58 offset:2048\n"
    "ds_read_b128 v[136:139], v58 offset:4096\n"
    "ds_read_b128 v[140:143], v58 offset:6144\n"
    "global_load_lds_dwordx4 v47, s[30:31] offset:1024\n"
    "s_add_u32 s30, s30, 212992\n"
    "s_addc_u32 s31, s31, 0\n"
    "ds_read_b64_tr_b16 v[144:145], v80 offset:0\n"
    "ds_read_b64_tr_b16 v[146:147], v80 offset:1024\n"
    "ds_read_b64_tr_b16 v[148:149], v81 offset:0\n"
    "ds_read_b64_tr_b16 v[150:151], v81 offset:1024\n"
    "s_add_u32 m0, s34, 49152\n"
    "s_nop 0\n"
    "global_load_lds_dwordx4 v52, s[36:37] offset:0\n"
    "ds_read_b64_tr_b16 v[152:153], v82 offset:0\n"
    "ds_read_b64_tr_b16 v[154:155], v82 offset:1024\n"
    "ds_read_b64_tr_b16 v[156:157], v83 offset:0\n"
    "ds_read_b64_tr_b16 v[158:159], v83 offset:1024\n"
    "global_load_lds_dwordx4 v53, s[36:37] offset:1024\n"
    "ds_read_b128 v[160:163], v59 offset:0\n"
    "ds_read_b128 v[164:167], v59 offset:2048\n"
    "ds_read_b128 v[168:171], v59 offset:4096\n"
    "ds_read_b128 v[172:175], v59 offset:6144\n"
    "global_load_lds_dwordx4 v54, s[36:37] offset:2048\n"
    "ds_read_b64_tr_b16 v[176:177], v80 offset:8192\n"
    "ds_read_b64_tr_b16 v[178:179], v80 offset:9216\n"
    "ds_read_b64_tr_b16 v[180:181], v81 offset:8192\n"
    "ds_read_b64_tr_b16 v[182:183], v81 offset:9216\n"
    "global_load_lds_dwordx4 v55, s[36:37] offset:3072\n"
    "ds_read_b64_tr_b16 v[184:185], v82 offset:8192\n"
    "ds_read_b64_tr_b16 v[186:187], v82 offset:9216\n"
    "ds_read_b64_tr_b16 v[188:189], v83 offset:8192\n"
    "ds_read_b64_tr_b16 v[190:191], v83 offset:9216\n"
    "s_waitcnt vmcnt(10) lgkmcnt(0)\n"
    "s_barrier\n"
    "v_mfma_f32_16x16x32_bf16 v[192:195], v[144:147], v[128:131], v[192:195]\n"
    "v_lshlrev_b32 v111, 7, v111\n"
    "v_mfma_f32_16x16x32_bf16 v[192:195], v[176:179], v[160:163], v[192:195]\n"
    "v_mfma_f32_16x16x32_bf16 v[196:199], v[176:179], v[164:167], v[196:199]\n"
    "v_add_u32_dpp v52, v110, v48 quad_perm:[0,0,0,0] row_mask:0xf bank_mask:0xf\n"
    "v_mfma_f32_16x16x32_bf16 v[196:199], v[144:147], v[132:135], v[196:199]\n"
    "v_mfma_f32_16x16x32_bf16 v[200:203], v[144:147], v[136:139], v[200:203]\n"
    "v_add_u32_dpp v53, v110, v49 quad_perm:[1,1,1,1] row_mask:0xf bank_mask:0xf\n"
    "v_mfma_f32_16x16x32_bf16 v[200:203], v[176:179], v[168:171], v[200:203]\n"
    "v_mfma_f32_16x16x32_bf16 v[204:207], v[176:179], v[172:175], v[204:207]\n"
    "v_add_u32_dpp v54, v110, v50 quad_perm:[2,2,2,2] row_mask:0xf bank_mask:0xf\n"
    "v_mfma_f32_16x16x32_bf16 v[204:207], v[144:147], v[140:143], v[204:207]\n"
    "v_mfma_f32_16x16x32_bf16 v[220:223], v[148:151], v[140:143], v[220:223]\n"
    "v_add_u32_dpp v55, v110, v51 quad_perm:[3,3,3,3] row_mask:0xf bank_mask:0xf\n"
    "v_mfma_f32_16x16x32_bf16 v[220:223], v[180:183], v[172:175], v[220:223]\n"
    "v_mfma_f32_16x16x32_bf16 v[216:219], v[180:183], v[168:171], v[216:219]\n"
    "v_mfma_f32_16x16x32_bf16 v[216:219], v[148:151], v[136:139], v[216:219]\n"
    "v_readfirstlane_b32 s40, v24\n"
    "v_mfma_f32_16x16x32_bf16 v[212:215], v[148:151], v[132:135], v[212:215]\n"
    "v_mfma_f32_16x16x32_bf16 v[212:215], v[180:183], v[164:167], v[212:215]\n"
    "s_or_b32 s40, s40, s44\n"
    "v_mfma_f32_16x16x32_bf16 v[208:211], v[180:183], v[160:163], v[208:211]\n"
    "v_mfma_f32_16x16x32_bf16 v[208:211], v[148:151], v[128:131], v[208:211]\n"
    "s_lshl1_add_u32 s41, s40, s54\n"
    "v_mfma_f32_16x16x32_bf16 v[224:227], v[152:155], v[128:131], v[224:227]\n"
    "v_mfma_f32_16x16x32_bf16 v[224:227], v[184:187], v[160:163], v[224:227]\n"
    "s_cmpk_lt_u32 s40, 256\n"
    "v_mfma_f32_16x16x32_bf16 v[228:231], v[184:187], v[164:167], v[228:231]\n"
    "v_mfma_f32_16x16x32_bf16 v[228:231], v[152:155], v[132:135], v[228:231]\n"
    "s_cselect_b32 s41, s41, 2\n"
    "v_mfma_f32_16x16x32_bf16 v[232:235], v[152:155], v[136:139], v[232:235]\n"
    "v_mfma_f32_16x16x32_bf16 v[232:235], v[184:187], v[168:171], v[232:235]\n"
    "v_mfma_f32_16x16x32_bf16 v[236:239], v[184:187], v[172:175], v[236:239]\n"
    "v_mov_b32 v12, s41\n"
    "v_mfma_f32_16x16x32_bf16 v[236:239], v[152:155], v[140:143], v[236:239]\n"
    "v_mfma_f32_16x16x32_bf16 v[252:255], v[156:159], v[140:143], v[252:255]\n"
    "s_mul_i32 s41, s41, 52\n"
    "v_mfma_f32_16x16x32_bf16 v[252:255], v[188:191], v[172:175], v[252:255]\n"
    "v_mfma_f32_16x16x32_bf16 v[248:251], v[188:191], v[168:171], v[248:251]\n"
    "s_add_u32 s38, s4, s41\n"
    "v_mfma_f32_16x16x32_bf16 v[248:251], v[156:159], v[136:139], v[248:251]\n"
    "v_mfma_f32_16x16x32_bf16 v[244:247], v[156:159], v[132:135], v[244:247]\n"
    "s_addc_u32 s39, s5, 0\n"
    "v_mfma_f32_16x16x32_bf16 v[244:247], v[188:191], v[164:167], v[244:247]\n"
    "v_mfma_f32_16x16x32_bf16 v[240:243], v[188:191], v[160:163], v[240:243]\n"
    "v_mfma_f32_16x16x32_bf16 v[240:243], v[156:159], v[128:131], v[240:243]\n"
    "s_waitcnt vmcnt(6)\n"
    "s_barrier\n"
    "ds_read_b64 v[84:85], v42 offset:0\n"
    "ds_read_b64 v[86:87], v42 offset:32\n"
    "ds_read_b64 v[88:89], v42 offset:64\n"
    "ds_read_b64 v[90:91], v42 offset:96\n"
    "s_add_u32 m0, s35, 98304\n"
    "s_nop 0\n"
    "global_load_lds_dwordx4 v46, s[30:31] offset:0\n"
    "ds_read_b128 v[128:131], v56 offset:0\n"
    "ds_read_b128 v[132:135], v56 offset:2048\n"
    "ds_read_b128 v[136:139], v56 offset:4096\n"
    "global_load_lds_dwordx4 v47, s[30:31] offset:1024\n"
    "s_add_u32 s30, s30, 212992\n"
    "s_addc_u32 s31, s31, 0\n"
    "ds_read_b128 v[140:143], v56 offset:6144\n"
    "ds_read_b64_tr_b16 v[144:145], v76 offset:0\n"
    "ds_read_b64_tr_b16 v[146:147], v76 offset:1024\n"
    "s_add_u32 m0, s34, 98304\n"
    "s_nop 0\n"
    "global_load_lds_dwordx4 v52, s[36:37] offset:0\n"
    "ds_read_b64_tr_b16 v[148:149], v77 offset:0\n"
    "ds_read_b64_tr_b16 v[150:151], v77 offset:1024\n"
    "ds_read_b64_tr_b16 v[152:153], v78 offset:0\n"
    "global_load_lds_dwordx4 v53, s[36:37] offset:1024\n"
    "ds_read_b64_tr_b16 v[154:155], v78 offset:1024\n"
    "ds_read_b64_tr_b16 v[156:157], v79 offset:0\n"
    "ds_read_b64_tr_b16 v[158:159], v79 offset:1024\n"
    "global_load_lds_dwordx4 v54, s[36:37] offset:2048\n"
    "ds_read_b128 v[160:163], v57 offset:0\n"
    "ds_read_b128 v[164:167], v57 offset:2048\n"
    "ds_read_b128 v[168:171], v57 offset:4096\n"
    "global_load_lds_dwordx4 v55, s[36:37] offset:3072\n"
    "ds_read_b128 v[172:175], v57 offset:6144\n"
    "ds_read_b64_tr_b16 v[176:177], v76 offset:8192\n"
    "ds_read_b64_tr_b16 v[178:179], v76 offset:9216\n"
    "global_load_dwordx3 v[36:38], v17, s[38:39] offset:-104\n"
    "ds_read_b64_tr_b16 v[180:181], v77 offset:8192\n"
    "ds_read_b64_tr_b16 v[182:183], v77 offset:9216\n"
    "ds_read_b64_tr_b16 v[184:185], v78 offset:8192\n"
    "ds_read_b64_tr_b16 v[186:187], v78 offset:9216\n"
    "ds_read_b64_tr_b16 v[188:189], v79 offset:8192\n"
    "ds_read_b64_tr_b16 v[190:191], v79 offset:9216\n"
    "s_waitcnt vmcnt(11) lgkmcnt(0)\n"
    "s_barrier\n"
    "v_mfma_f32_16x16x32_bf16 v[192:195], v[144:147], v[128:131], v[192:195]\n"
    "v_lshlrev_b32 v112, 7, v112\n"
    "v_mfma_f32_16x16x32_bf16 v[192:195], v[176:179], v[160:163], v[192:195]\n"
    "v_add_u32_dpp v52, v111, v48 quad_perm:[0,0,0,0] row_mask:0xf bank_mask:0xf\n"
    "v_mfma_f32_16x16x32_bf16 v[196:199], v[176:179], v[164:167], v[196:199]\n"
    "v_add_u32_dpp v53, v111, v49 quad_perm:[1,1,1,1] row_mask:0xf bank_mask:0xf\n"
    "v_mfma_f32_16x16x32_bf16 v[196:199], v[144:147], v[132:135], v[196:199]\n"
    "v_add_u32_dpp v54, v111, v50 quad_perm:[2,2,2,2] row_mask:0xf bank_mask:0xf\n"
    "v_mfma_f32_16x16x32_bf16 v[200:203], v[144:147], v[136:139], v[200:203]\n"
    "v_add_u32_dpp v55, v111, v51 quad_perm:[3,3,3,3] row_mask:0xf bank_mask:0xf\n"
    "v_mfma_f32_16x16x32_bf16 v[200:203], v[176:179], v[168:171], v[200:203]\n"
    "v_lshlrev_b32 v60, 16, v84\n"
    "v_mfma_f32_16x16x32_bf16 v[204:207], v[176:179], v[172:175], v[204:207]\n"
    "v_and_b32 v61, 0xffff0000, v84\n"
    "v_mfma_f32_16x16x32_bf16 v[204:207], v[144:147], v[140:143], v[204:207]\n"
    "v_mfma_f32_16x16x32_bf16 v[220:223], v[148:151], v[140:143], v[220:223]\n"
    "v_lshlrev_b32 v62, 16, v85\n"
    "v_mfma_f32_16x16x32_bf16 v[220:223], v[180:183], v[172:175], v[220:223]\n"
    "v_and_b32 v63, 0xffff0000, v85\n"
    "v_mfma_f32_16x16x32_bf16 v[216:219], v[180:183], v[168:171], v[216:219]\n"
    "v_lshlrev_b32 v64, 16, v86\n"
    "v_mfma_f32_16x16x32_bf16 v[216:219], v[148:151], v[136:139], v[216:219]\n"
    "v_and_b32 v65, 0xffff0000, v86\n"
    "v_mfma_f32_16x16x32_bf16 v[212:215], v[148:151], v[132:135], v[212:215]\n"
    "v_lshlrev_b32 v66, 16, v87\n"
    "v_mfma_f32_16x16x32_bf16 v[212:215], v[180:183], v[164:167], v[212:215]\n"
    "v_and_b32 v67, 0xffff0000, v87\n"
    "v_mfma_f32_16x16x32_bf16 v[208:211], v[180:183], v[160:163], v[208:211]\n"
    "v_lshlrev_b32 v68, 16, v88\n"
    "v_mfma_f32_16x16x32_bf16 v[208:211], v[148:151], v[128:131], v[208:211]\n"
    "v_mfma_f32_16x16x32_bf16 v[224:227], v[152:155], v[128:131], v[224:227]\n"
    "v_and_b32 v69, 0xffff0000, v88\n"
    "v_mfma_f32_16x16x32_bf16 v[224:227], v[184:187], v[160:163], v[224:227]\n"
    "v_lshlrev_b32 v70, 16, v89\n"
    "v_mfma_f32_16x16x32_bf16 v[228:231], v[184:187], v[164:167], v[228:231]\n"
    "v_and_b32 v71, 0xffff0000, v89\n"
    "v_mfma_f32_16x16x32_bf16 v[228:231], v[152:155], v[132:135], v[228:231]\n"
    "v_lshlrev_b32 v72, 16, v90\n"
    "v_mfma_f32_16x16x32_bf16 v[232:235], v[152:155], v[136:139], v[232:235]\n"
    "v_and_b32 v73, 0xffff0000, v90\n"
    "v_mfma_f32_16x16x32_bf16 v[232:235], v[184:187], v[168:171], v[232:235]\n"
    "v_lshlrev_b32 v74, 16, v91\n"
    "v_mfma_f32_16x16x32_bf16 v[236:239], v[184:187], v[172:175], v[236:239]\n"
    "v_and_b32 v75, 0xffff0000, v91\n"
    "v_mfma_f32_16x16x32_bf16 v[236:239], v[152:155], v[140:143], v[236:239]\n"
    "v_mfma_f32_16x16x32_bf16 v[252:255], v[156:159], v[140:143], v[252:255]\n"
    "s_lshl3_add_u32 s40, s2, 4096\n"
    "v_mfma_f32_16x16x32_bf16 v[252:255], v[188:191], v[172:175], v[252:255]\n"
    "s_lshl_b32 s41, s2, 2\n"
    "v_mfma_f32_16x16x32_bf16 v[248:251], v[188:191], v[168:171], v[248:251]\n"
    "v_lshlrev_b32 v11, 3, v1\n"
    "v_mfma_f32_16x16x32_bf16 v[248:251], v[156:159], v[136:139], v[248:251]\n"
    "v_add_u32 v11, 40960, v11\n"
    "v_mfma_f32_16x16x32_bf16 v[244:247], v[156:159], v[132:135], v[244:247]\n"
    "v_writelane_b32 v11, s40, 8\n"
    "v_mfma_f32_16x16x32_bf16 v[244:247], v[188:191], v[164:167], v[244:247]\n"
    "v_writelane_b32 v11, s41, 9\n"
    "v_mfma_f32_16x16x32_bf16 v[240:243], v[188:191], v[160:163], v[240:243]\n"
    "v_mfma_f32_16x16x32_bf16 v[240:243], v[156:159], v[128:131], v[240:243]\n"
    "s_waitcnt vmcnt(7)\n"
    "s_barrier\n"
    "ds_write_b32 v25, v12 offset:20\n"
    "s_add_u32 m0, s35, 0\n"
    "s_nop 0\n"
    "global_load_lds_dwordx4 v46, s[30:31] offset:0\n"
    "ds_read_b128 v[128:131], v56 offset:49152\n"
    "ds_read_b128 v[132:135], v56 offset:51200\n"
    "ds_read_b128 v[136:139], v56 offset:53248\n"
    "global_load_lds_dwordx4 v47, s[30:31] offset:1024\n"
    "s_bfe_u32 s66, s28, 0x80018\n"
    "s_mul_i32 s66, s66, 53248\n"
    "s_add_u32 s30, s6, s66\n"
    "s_addc_u32 s31, s7, 0\n"
    "ds_read_b128 v[140:143], v56 offset:55296\n"
    "ds_read_b64_tr_b16 v[144:145], v76 offset:49152\n"
    "ds_read_b64_tr_b16 v[146:147], v76 offset:50176\n"
    "s_add_u32 m0, s34, 0\n"
    "s_nop 0\n"
    "global_load_lds_dwordx4 v52, s[36:37] offset:0\n"
    "ds_read_b64_tr_b16 v[148:149], v77 offset:49152\n"
    "ds_read_b64_tr_b16 v[150:151], v77 offset:50176\n"
    "ds_read_b64_tr_b16 v[152:153], v78 offset:49152\n"
    "global_load_lds_dwordx4 v53, s[36:37] offset:1024\n"
    "ds_read_b64_tr_b16 v[154:155], v78 offset:50176\n"
    "ds_read_b64_tr_b16 v[156:157], v79 offset:49152\n"
    "ds_read_b64_tr_b16 v[158:159], v79 offset:50176\n"
    "global_load_lds_dwordx4 v54, s[36:37] offset:2048\n"
    "ds_read_b128 v[160:163], v57 offset:49152\n"
    "ds_read_b128 v[164:167], v57 offset:51200\n"
    "ds_read_b128 v[168:171], v57 offset:53248\n"
    "global_load_lds_dwordx4 v55, s[36:37] offset:3072\n"
    "ds_read_b128 v[172:175], v57 offset:55296\n"
    "ds_read_b64_tr_b16 v[176:177], v76 offset:57344\n"
    "ds_read_b64_tr_b16 v[178:179], v76 offset:58368\n"
    "global_load_dword v41, v18, s[38:39] offset:-8\n"
    "ds_read_b64_tr_b16 v[180:181], v77 offset:57344\n"
    "ds_read_b64_tr_b16 v[182:183], v77 offset:58368\n"
    "ds_read_b64_tr_b16 v[184:185], v78 offset:57344\n"
    "ds_read_b64_tr_b16 v[186:187], v78 offset:58368\n"
    "ds_read_b64_tr_b16 v[188:189], v79 offset:57344\n"
    "ds_read_b64_tr_b16 v[190:191], v79 offset:58368\n"
    "s_waitcnt vmcnt(12) lgkmcnt(0)\n"
    "s_barrier\n"
    "v_mfma_f32_16x16x32_bf16 v[192:195], v[144:147], v[128:131], v[192:195]\n"
    "v_mfma_f32_16x16x32_bf16 v[192:195], v[176:179], v[160:163], v[192:195]\n"
    "v_lshlrev_b32 v113, 7, v113\n"
    "v_mfma_f32_16x16x32_bf16 v[196:199], v[176:179], v[164:167], v[196:199]\n"
    "v_mfma_f32_16x16x32_bf16 v[196:199], v[144:147], v[132:135], v[196:199]\n"
    "v_add_u32_dpp v52, v112, v48 quad_perm:[0,0,0,0] row_mask:0xf bank_mask:0xf\n"
    "v_mfma_f32_16x16x32_bf16 v[200:203], v[144:147], v[136:139], v[200:203]\n"
    "v_mfma_f32_16x16x32_bf16 v[200:203], v[176:179], v[168:171], v[200:203]\n"
    "v_add_u32_dpp v53, v112, v49 quad_perm:[1,1,1,1] row_mask:0xf bank_mask:0xf\n"
    "v_mfma_f32_16x16x32_bf16 v[204:207], v[176:179], v[172:175], v[204:207]\n"
    "v_mfma_f32_16x16x32_bf16 v[204:207], v[144:147], v[140:143], v[204:207]\n"
    "v_add_u32_dpp v54, v112, v50 quad_perm:[2,2,2,2] row_mask:0xf bank_mask:0xf\n"
    "v_mfma_f32_16x16x32_bf16 v[220:223], v[148:151], v[140:143], v[220:223]\n"
    "v_mfma_f32_16x16x32_bf16 v[220:223], v[180:183], v[172:175], v[220:223]\n"
    "v_add_u32_dpp v55, v112, v51 quad_perm:[3,3,3,3] row_mask:0xf bank_mask:0xf\n"
    "v_mfma_f32_16x16x32_bf16 v[216:219], v[180:183], v[168:171], v[216:219]\n"
    "v_mfma_f32_16x16x32_bf16 v[216:219], v[148:151], v[136:139], v[216:219]\n"
    "v_mfma_f32_16x16x32_bf16 v[212:215], v[148:151], v[132:135], v[212:215]\n"
    "v_mfma_f32_16x16x32_bf16 v[212:215], v[180:183], v[164:167], v[212:215]\n"
    "v_mfma_f32_16x16x32_bf16 v[208:211], v[180:183], v[160:163], v[208:211]\n"
    "v_add_f32 v192, v192, v60\n"
    "v_mfma_f32_16x16x32_bf16 v[208:211], v[148:151], v[128:131], v[208:211]\n"
    "v_add_f32 v193, v193, v61\n"
    "v_mfma_f32_16x16x32_bf16 v[224:227], v[152:155], v[128:131], v[224:227]\n"
    "v_add_f32 v194, v194, v62\n"
    "v_mfma_f32_16x16x32_bf16 v[224:227], v[184:187], v[160:163], v[224:227]\n"
    "v_add_f32 v195, v195, v63\n"
    "v_mfma_f32_16x16x32_bf16 v[228:231], v[184:187], v[164:167], v[228:231]\n"
    "v_add_f32 v196, v196, v60\n"
    "v_mfma_f32_16x16x32_bf16 v[228:231], v[152:155], v[132:135], v[228:231]\n"
    "v_add_f32 v197, v197, v61\n"
    "v_mfma_f32_16x16x32_bf16 v[232:235], v[152:155], v[136:139], v[232:235]\n"
    "v_add_f32 v198, v198, v62\n"
    "v_mfma_f32_16x16x32_bf16 v[232:235], v[184:187], v[168:171], v[232:235]\n"
    "v_add_f32 v199, v199, v63\n"
    "v_mfma_f32_16x16x32_bf16 v[236:239], v[184:187], v[172:175], v[236:239]\n"
    "v_add_f32 v200, v200, v60\n"
    "v_mfma_f32_16x16x32_bf16 v[236:239], v[152:155], v[140:143], v[236:239]\n"
    "v_add_f32 v201, v201, v61\n"
    "v_mfma_f32_16x16x32_bf16 v[252:255], v[156:159], v[140:143], v[252:255]\n"
    "v_add_f32 v202, v202, v62\n"
    "v_mfma_f32_16x16x32_bf16 v[252:255], v[188:191], v[172:175], v[252:255]\n"
    "v_add_f32 v203, v203, v63\n"
    "v_mfma_f32_16x16x32_bf16 v[248:251], v[188:191], v[168:171], v[248:251]\n"
    "v_add_f32 v204, v204, v60\n"
    "v_mfma_f32_16x16x32_bf16 v[248:251], v[156:159], v[136:139], v[248:251]\n"
    "v_add_f32 v205, v205, v61\n"
    "v_mfma_f32_16x16x32_bf16 v[244:247], v[156:159], v[132:135], v[244:247]\n"
    "v_add_f32 v206, v206, v62\n"
    "v_mfma_f32_16x16x32_bf16 v[244:247], v[188:191], v[164:167], v[244:247]\n"
    "v_add_f32 v207, v207, v63\n"
    "v_mfma_f32_16x16x32_bf16 v[240:243], v[188:191], v[160:163], v[240:243]\n"
    "v_mfma_f32_16x16x32_bf16 v[240:243], v[156:159], v[128:131], v[240:243]\n"
    "s_waitcnt vmcnt(8)\n"
    "s_barrier\n"
    "s_add_u32 m0, s35, 49152\n"
    "s_nop 0\n"
    "global_load_lds_dwordx4 v46, s[30:31] offset:0\n"
    "ds_read_b128 v[128:131], v58 offset:0\n"
    "ds_read_b128 v[132:135], v58 offset:2048\n"
    "ds_read_b128 v[136:139], v58 offset:4096\n"
    "global_load_lds_dwordx4 v47, s[30:31] offset:1024\n"
    "s_add_u32 s30, s30, 212992\n"
    "s_addc_u32 s31, s31, 0\n"
    "ds_read_b128 v[140:143], v58 offset:6144\n"
    "ds_read_b64_tr_b16 v[144:145], v80 offset:0\n"
    "ds_read_b64_tr_b16 v[146:147], v80 offset:1024\n"
    "s_add_u32 m0, s34, 49152\n"
    "s_nop 0\n"
    "global_load_lds_dwordx4 v52, s[36:37] offset:0\n"
    "ds_read_b64_tr_b16 v[148:149], v81 offset:0\n"
    "ds_read_b64_tr_b16 v[150:151], v81 offset:1024\n"
    "ds_read_b64_tr_b16 v[152:153], v82 offset:0\n"
    "global_load_lds_dwordx4 v53, s[36:37] offset:1024\n"
    "ds_read_b64_tr_b16 v[154:155], v82 offset:1024\n"
    "ds_read_b64_tr_b16 v[156:157], v83 offset:0\n"
    "ds_read_b64_tr_b16 v[158:159], v83 offset:1024\n"
    "global_load_lds_dwordx4 v54, s[36:37] offset:2048\n"
    "ds_read_b128 v[160:163], v59 offset:0\n"
    "ds_read_b128 v[164:167], v59 offset:2048\n"
    "ds_read_b128 v[168:171], v59 offset:4096\n"
    "global_load_lds_dwordx4 v55, s[36:37] offset:3072\n"
    "ds_read_b128 v[172:175], v59 offset:6144\n"
    "ds_read_b64_tr_b16 v[176:177], v80 offset:8192\n"
    "ds_read_b64_tr_b16 v[178:179], v80 offset:9216\n"
    "s_mov_b32 m0, s64\n"
    "s_nop 0\n"
    "global_load_lds_dword v43, s[18:19]\n"
    "ds_read_b64_tr_b16 v[180:181], v81 offset:8192\n"
    "ds_read_b64_tr_b16 v[182:183], v81 offset:9216\n"
    "ds_read_b64_tr_b16 v[184:185], v82 offset:8192\n"
    "ds_read_b64_tr_b16 v[186:187], v82 offset:9216\n"
    "ds_read_b64_tr_b16 v[188:189], v83 offset:8192\n"
    "ds_read_b64_tr_b16 v[190:191], v83 offset:9216\n"
    "s_waitcnt vmcnt(12) lgkmcnt(0)\n"
    "s_barrier\n"
    "v_mfma_f32_16x16x32_bf16 v[192:195], v[144:147], v[128:131], v[192:195]\n"
    "v_mfma_f32_16x16x32_bf16 v[192:195], v[176:179], v[160:163], v[192:195]\n"
    "v_lshlrev_b32 v114, 7, v114\n"
    "v_mfma_f32_16x16x32_bf16 v[196:199], v[176:179], v[164:167], v[196:199]\n"
    "v_mfma_f32_16x16x32_bf16 v[196:199], v[144:147], v[132:135], v[196:199]\n"
    "v_add_u32_dpp v52, v113, v48 quad_perm:[0,0,0,0] row_mask:0xf bank_mask:0xf\n"
    "v_mfma_f32_16x16x32_bf16 v[200:203], v[144:147], v[136:139], v[200:203]\n"
    "v_mfma_f32_16x16x32_bf16 v[200:203], v[176:179], v[168:171], v[200:203]\n"
    "v_add_u32_dpp v53, v113, v49 quad_perm:[1,1,1,1] row_mask:0xf bank_mask:0xf\n"
    "v_mfma_f32_16x16x32_bf16 v[204:207], v[176:179], v[172:175], v[204:207]\n"
    "v_mfma_f32_16x16x32_bf16 v[204:207], v[144:147], v[140:143], v[204:207]\n"
    "v_add_u32_dpp v54, v113, v50 quad_perm:[2,2,2,2] row_mask:0xf bank_mask:0xf\n"
    "v_mfma_f32_16x16x32_bf16 v[220:223], v[148:151], v[140:143], v[220:223]\n"
    "v_mfma_f32_16x16x32_bf16 v[220:223], v[180:183], v[172:175], v[220:223]\n"
    "v_add_u32_dpp v55, v113, v51 quad_perm:[3,3,3,3] row_mask:0xf bank_mask:0xf\n"
    "v_mfma_f32_16x16x32_bf16 v[216:219], v[180:183], v[168:171], v[216:219]\n"
    "v_mfma_f32_16x16x32_bf16 v[216:219], v[148:151], v[136:139], v[216:219]\n"
    "v_mfma_f32_16x16x32_bf16 v[212:215], v[148:151], v[132:135], v[212:215]\n"
    "v_mfma_f32_16x16x32_bf16 v[212:215], v[180:183], v[164:167], v[212:215]\n"
    "v_mfma_f32_16x16x32_bf16 v[208:211], v[180:183], v[160:163], v[208:211]\n"
    "v_mfma_f32_16x16x32_bf16 v[208:211], v[148:151], v[128:131], v[208:211]\n"
    "v_mfma_f32_16x16x32_bf16 v[224:227], v[152:155], v[128:131], v[224:227]\n"
    "v_mfma_f32_16x16x32_bf16 v[224:227], v[184:187], v[160:163], v[224:227]\n"
    "v_mfma_f32_16x16x32_bf16 v[228:231], v[184:187], v[164:167], v[228:231]\n"
    "v_add_f32 v222, v222, v66\n"
    "v_add_f32 v223, v223, v67\n"
    "v_mfma_f32_16x16x32_bf16 v[228:231], v[152:155], v[132:135], v[228:231]\n"
    "v_add_f32 v221, v221, v65\n"
    "v_mfma_f32_16x16x32_bf16 v[232:235], v[152:155], v[136:139], v[232:235]\n"
    "v_add_f32 v220, v220, v64\n"
    "v_mfma_f32_16x16x32_bf16 v[232:235], v[184:187], v[168:171], v[232:235]\n"
    "v_add_f32 v219, v219, v67\n"
    "v_mfma_f32_16x16x32_bf16 v[236:239], v[184:187], v[172:175], v[236:239]\n"
    "v_add_f32 v217, v217, v65\n"
    "v_add_f32 v218, v218, v66\n"
    "v_mfma_f32_16x16x32_bf16 v[236:239], v[152:155], v[140:143], v[236:239]\n"
    "v_add_f32 v216, v216, v64\n"
    "v_mfma_f32_16x16x32_bf16 v[252:255], v[156:159], v[140:143], v[252:255]\n"
    "v_add_f32 v215, v215, v67\n"
    "v_mfma_f32_16x16x32_bf16 v[252:255], v[188:191], v[172:175], v[252:255]\n"
    "v_add_f32 v214, v214, v66\n"
    "v_mfma_f32_16x16x32_bf16 v[248:251], v[188:191], v[168:171], v[248:251]\n"
    "v_add_f32 v212, v212, v64\n"
    "v_add_f32 v213, v213, v65\n"
    "v_mfma_f32_16x16x32_bf16 v[248:251], v[156:159], v[136:139], v[248:251]\n"
    "v_add_f32 v211, v211, v67\n"
    "v_mfma_f32_16x16x32_bf16 v[244:247], v[156:159], v[132:135], v[244:247]\n"
    "v_add_f32 v210, v210, v66\n"
    "v_mfma_f32_16x16x32_bf16 v[244:247], v[188:191], v[164:167], v[244:247]\n"
    "v_add_f32 v209, v209, v65\n"
    "v_mfma_f32_16x16x32_bf16 v[240:243], v[188:191], v[160:163], v[240:243]\n"
    "v_add_f32 v208, v208, v64\n"
    "v_mfma_f32_16x16x32_bf16 v[240:243], v[156:159], v[128:131], v[240:243]\n"
    "s_waitcnt vmcnt(8)\n"
    "s_barrier\n"
    "s_add_u32 m0, s35, 98304\n"
    "s_nop 0\n"
    "global_load_lds_dwordx4 v46, s[30:31] offset:0\n"
    "ds_read_b128 v[128:131], v56 offset:0\n"
    "ds_read_b128 v[132:135], v56 offset:2048\n"
    "ds_read_b128 v[136:139], v56 offset:4096\n"
    "ds_read_b128 v[140:143], v56 offset:6144\n"
    "global_load_lds_dwordx4 v47, s[30:31] offset:1024\n"
    "s_add_u32 s30, s30, 212992\n"
    "s_addc_u32 s31, s31, 0\n"
    "ds_read_b64_tr_b16 v[144:145], v76 offset:0\n"
    "ds_read_b64_tr_b16 v[146:147], v76 offset:1024\n"
    "ds_read_b64_tr_b16 v[148:149], v77 offset:0\n"
    "ds_read_b64_tr_b16 v[150:151], v77 offset:1024\n"
    "s_add_u32 m0, s34, 98304\n"
    "s_nop 0\n"
    "global_load_lds_dwordx4 v52, s[36:37] offset:0\n"
    "ds_read_b64_tr_b16 v[152:153], v78 offset:0\n"
    "ds_read_b64_tr_b16 v[154:155], v78 offset:1024\n"
    "ds_read_b64_tr_b16 v[156:157], v79 offset:0\n"
    "ds_read_b64_tr_b16 v[158:159], v79 offset:1024\n"
    "global_load_lds_dwordx4 v53, s[36:37] offset:1024\n"
    "ds_read_b128 v[160:163], v57 offset:0\n"
    "ds_read_b128 v[164:167], v57 offset:2048\n"
    "ds_read_b128 v[168:171], v57 offset:4096\n"
    "ds_read_b128 v[172:175], v57 offset:6144\n"
    "global_load_lds_dwordx4 v54, s[36:37] offset:2048\n"
    "ds_read_b64_tr_b16 v[176:177], v76 offset:8192\n"
    "ds_read_b64_tr_b16 v[178:179], v76 offset:9216\n"
    "ds_read_b64_tr_b16 v[180:181], v77 offset:8192\n"
    "ds_read_b64_tr_b16 v[182:183], v77 offset:9216\n"
    "global_load_lds_dwordx4 v55, s[36:37] offset:3072\n"
    "ds_read_b64_tr_b16 v[184:185], v78 offset:8192\n"
    "ds_read_b64_tr_b16 v[186:187], v78 offset:9216\n"
    "ds_read_b64_tr_b16 v[188:189], v79 offset:8192\n"
    "ds_read_b64_tr_b16 v[190:191], v79 offset:9216\n"
    "s_waitcnt vmcnt(11) lgkmcnt(0)\n"
    "s_barrier\n"
    "v_mfma_f32_16x16x32_bf16 v[192:195], v[144:147], v[128:131], v[192:195]\n"
    "v_lshlrev_b32 v115, 7, v115\n"
    "v_mfma_f32_16x16x32_bf16 v[192:195], v[176:179], v[160:163], v[192:195]\n"
    "v_mfma_f32_16x16x32_bf16 v[196:199], v[176:179], v[164:167], v[196:199]\n"
    "v_mfma_f32_16x16x32_bf16 v[196:199], v[144:147], v[132:135], v[196:199]\n"
    "v_add_u32_dpp v52, v114, v48 quad_perm:[0,0,0,0] row_mask:0xf bank_mask:0xf\n"
    "v_mfma_f32_16x16x32_bf16 v[200:203], v[144:147], v[136:139], v[200:203]\n"
    "v_mfma_f32_16x16x32_bf16 v[200:203], v[176:179], v[168:171], v[200:203]\n"
    "v_mfma_f32_16x16x32_bf16 v[204:207], v[176:179], v[172:175], v[204:207]\n"
    "v_add_u32_dpp v53, v114, v49 quad_perm:[1,1,1,1] row_mask:0xf bank_mask:0xf\n"
    "v_mfma_f32_16x16x32_bf16 v[204:207], v[144:147], v[140:143], v[204:207]\n"
    "v_mfma_f32_16x16x32_bf16 v[220:223], v[148:151], v[140:143], v[220:223]\n"
    "v_mfma_f32_16x16x32_bf16 v[220:223], v[180:183], v[172:175], v[220:223]\n"
    "v_mfma_f32_16x16x32_bf16 v[216:219], v[180:183], v[168:171], v[216:219]\n"
    "v_add_u32_dpp v54, v114, v50 quad_perm:[2,2,2,2] row_mask:0xf bank_mask:0xf\n"
    "v_mfma_f32_16x16x32_bf16 v[216:219], v[148:151], v[136:139], v[216:219]\n"
    "v_mfma_f32_16x16x32_bf16 v[212:215], v[148:151], v[132:135], v[212:215]\n"
    "v_mfma_f32_16x16x32_bf16 v[212:215], v[180:183], v[164:167], v[212:215]\n"
    "v_add_u32_dpp v55, v114, v51 quad_perm:[3,3,3,3] row_mask:0xf bank_mask:0xf\n"
    "v_mfma_f32_16x16x32_bf16 v[208:211], v[180:183], v[160:163], v[208:211]\n"
    "v_mfma_f32_16x16x32_bf16 v[208:211], v[148:151], v[128:131], v[208:211]\n"
    "v_mfma_f32_16x16x32_bf16 v[224:227], v[152:155], v[128:131], v[224:227]\n"
    "v_mfma_f32_16x16x32_bf16 v[224:227], v[184:187], v[160:163], v[224:227]\n"
    "v_lshl_or_b32 v36, v36, 7, v19\n"
    "v_mfma_f32_16x16x32_bf16 v[228:231], v[184:187], v[164:167], v[228:231]\n"
    "v_mfma_f32_16x16x32_bf16 v[228:231], v[152:155], v[132:135], v[228:231]\n"
    "v_mfma_f32_16x16x32_bf16 v[232:235], v[152:155], v[136:139], v[232:235]\n"
    "v_lshl_or_b32 v37, v37, 7, v19\n"
    "v_mfma_f32_16x16x32_bf16 v[232:235], v[184:187], v[168:171], v[232:235]\n"
    "v_mfma_f32_16x16x32_bf16 v[236:239], v[184:187], v[172:175], v[236:239]\n"
    "v_mfma_f32_16x16x32_bf16 v[236:239], v[152:155], v[140:143], v[236:239]\n"
    "v_mfma_f32_16x16x32_bf16 v[252:255], v[156:159], v[140:143], v[252:255]\n"
    "v_lshl_or_b32 v38, v38, 7, v19\n"
    "v_mfma_f32_16x16x32_bf16 v[252:255], v[188:191], v[172:175], v[252:255]\n"
    "v_mfma_f32_16x16x32_bf16 v[248:251], v[188:191], v[168:171], v[248:251]\n"
    "v_mfma_f32_16x16x32_bf16 v[248:251], v[156:159], v[136:139], v[248:251]\n"
    "v_lshl_or_b32 v41, v41, 7, v20\n"
    "v_mfma_f32_16x16x32_bf16 v[244:247], v[156:159], v[132:135], v[244:247]\n"
    "v_mfma_f32_16x16x32_bf16 v[244:247], v[188:191], v[164:167], v[244:247]\n"
    "v_mfma_f32_16x16x32_bf16 v[240:243], v[188:191], v[160:163], v[240:243]\n"
    "v_mfma_f32_16x16x32_bf16 v[240:243], v[156:159], v[128:131], v[240:243]\n"
    "s_waitcnt vmcnt(7)\n"
    "s_barrier\n"
    "s_cmp_eq_u32 s20, 3\n"
    "s_cbranch_scc1 .Lxd_on3\n"
    ".Lxd_on3r:\n"
    "s_add_u32 m0, s35, 0\n"
    "s_nop 0\n"
    "global_load_lds_dwordx4 v46, s[30:31] offset:0\n"
    "ds_read_b128 v[128:131], v56 offset:49152\n"
    "ds_read_b128 v[132:135], v56 offset:51200\n"
    "ds_read_b128 v[136:139], v56 offset:53248\n"
    "global_load_lds_dwordx4 v47, s[30:31] offset:1024\n"
    "s_add_u32 s30, s30, 212992\n"
    "s_addc_u32 s31, s31, 0\n"
    "ds_read_b128 v[140:143], v56 offset:55296\n"
    "ds_read_b64_tr_b16 v[144:145], v76 offset:49152\n"
    "ds_read_b64_tr_b16 v[146:147], v76 offset:50176\n"
    "s_add_u32 m0, s34, 0\n"
    "s_nop 0\n"
    "global_load_lds_dwordx4 v52, s[36:37] offset:0\n"
    "ds_read_b64_tr_b16 v[148:149], v77 offset:49152\n"
    "ds_read_b64_tr_b16 v[150:151], v77 offset:50176\n"
    "ds_read_b64_tr_b16 v[152:153], v78 offset:49152\n"
    "global_load_lds_dwordx4 v53, s[36:37] offset:1024\n"
    "ds_read_b64_tr_b16 v[154:155], v78 offset:50176\n"
    "ds_read_b64_tr_b16 v[156:157], v79 offset:49152\n"
    "ds_read_b64_tr_b16 v[158:159], v79 offset:50176\n"
    "global_load_lds_dwordx4 v54, s[36:37] offset:2048\n"
    "ds_read_b128 v[160:163], v57 offset:49152\n"
    "ds_read_b128 v[164:167], v57 offset:51200\n"
    "ds_read_b128 v[168:171], v57 offset:53248\n"
    "global_load_lds_dwordx4 v55, s[36:37] offset:3072\n"
    "ds_read_b128 v[172:175], v57 offset:55296\n"
    "ds_read_b64_tr_b16 v[176:177], v76 offset:57344\n"
    "ds_read_b64_tr_b16 v[178:179], v76 offset:58368\n"
    "global_load_dwordx4 v[28:31], v36, s[8:9]\n"
    "ds_read_b64_tr_b16 v[180:181], v77 offset:57344\n"
    "ds_read_b64_tr_b16 v[182:183], v77 offset:58368\n"
    "ds_read_b64_tr_b16 v[184:185], v78 offset:57344\n"
    "ds_read_b64_tr_b16 v[186:187], v78 offset:58368\n"
    "ds_read_b64_tr_b16 v[188:189], v79 offset:57344\n"
    "ds_read_b64_tr_b16 v[190:191], v79 offset:58368\n"
    "s_waitcnt vmcnt(11) lgkmcnt(0)\n"
    "s_barrier\n"
    "v_mfma_f32_16x16x32_bf16 v[192:195], v[144:147], v[128:131], v[192:195]\n"
    "v_mfma_f32_16x16x32_bf16 v[192:195], v[176:179], v[160:163], v[192:195]\n"
    "v_add_f32 v224, v224, v68\n"
    "v_add_f32 v225, v225, v69\n"
    "v_mfma_f32_16x16x32_bf16 v[196:199], v[176:179], v[164:167], v[196:199]\n"
    "v_add_f32 v226, v226, v70\n"
    "v_mfma_f32_16x16x32_bf16 v[196:199], v[144:147], v[132:135], v[196:199]\n"
    "v_add_f32 v227, v227, v71\n"
    "v_mfma_f32_16x16x32_bf16 v[200:203], v[144:147], v[136:139], v[200:203]\n"
    "v_add_f32 v228, v228, v68\n"
    "v_mfma_f32_16x16x32_bf16 v[200:203], v[176:179], v[168:171], v[200:203]\n"
    "v_add_f32 v229, v229, v69\n"
    "v_add_f32 v230, v230, v70\n"
    "v_mfma_f32_16x16x32_bf16 v[204:207], v[176:179], v[172:175], v[204:207]\n"
    "v_add_f32 v231, v231, v71\n"
    "v_mfma_f32_16x16x32_bf16 v[204:207], v[144:147], v[140:143], v[204:207]\n"
    "v_add_f32 v232, v232, v68\n"
    "v_mfma_f32_16x16x32_bf16 v[220:223], v[148:151], v[140:143], v[220:223]\n"
    "v_add_f32 v233, v233, v69\n"
    "v_mfma_f32_16x16x32_bf16 v[220:223], v[180:183], v[172:175], v[220:223]\n"
    "v_add_f32 v234, v234, v70\n"
    "v_add_f32 v235, v235, v71\n"
    "v_mfma_f32_16x16x32_bf16 v[216:219], v[180:183], v[168:171], v[216:219]\n"
    "v_add_f32 v236, v236, v68\n"
    "v_mfma_f32_16x16x32_bf16 v[216:219], v[148:151], v[136:139], v[216:219]\n"
    "v_add_f32 v237, v237, v69\n"
    "v_mfma_f32_16x16x32_bf16 v[212:215], v[148:151], v[132:135], v[212:215]\n"
    "v_add_f32 v238, v238, v70\n"
    "v_mfma_f32_16x16x32_bf16 v[212:215], v[180:183], v[164:167], v[212:215]\n"
    "v_add_f32 v239, v239, v71\n"
    "v_mfma_f32_16x16x32_bf16 v[208:211], v[180:183], v[160:163], v[208:211]\n"
    "v_mfma_f32_16x16x32_bf16 v[208:211], v[148:151], v[128:131], v[208:211]\n"
    "v_mfma_f32_16x16x32_bf16 v[224:227], v[152:155], v[128:131], v[224:227]\n"
    "v_mfma_f32_16x16x32_bf16 v[224:227], v[184:187], v[160:163], v[224:227]\n"
    "v_mfma_f32_16x16x32_bf16 v[228:231], v[184:187], v[164:167], v[228:231]\n"
    "v_lshlrev_b32 v116, 7, v116\n"
    "v_mfma_f32_16x16x32_bf16 v[228:231], v[152:155], v[132:135], v[228:231]\n"
    "v_mfma_f32_16x16x32_bf16 v[232:235], v[152:155], v[136:139], v[232:235]\n"
    "v_add_u32_dpp v52, v115, v48 quad_perm:[0,0,0,0] row_mask:0xf bank_mask:0xf\n"
    "v_mfma_f32_16x16x32_bf16 v[232:235], v[184:187], v[168:171], v[232:235]\n"
    "v_mfma_f32_16x16x32_bf16 v[236:239], v[184:187], v[172:175], v[236:239]\n"
    "v_mfma_f32_16x16x32_bf16 v[236:239], v[152:155], v[140:143], v[236:239]\n"
    "v_add_u32_dpp v53, v115, v49 quad_perm:[1,1,1,1] row_mask:0xf bank_mask:0xf\n"
    "v_mfma_f32_16x16x32_bf16 v[252:255], v[156:159], v[140:143], v[252:255]\n"
    "v_mfma_f32_16x16x32_bf16 v[252:255], v[188:191], v[172:175], v[252:255]\n"
    "v_add_u32_dpp v54, v115, v50 quad_perm:[2,2,2,2] row_mask:0xf bank_mask:0xf\n"
    "v_mfma_f32_16x16x32_bf16 v[248:251], v[188:191], v[168:171], v[248:251]\n"
    "v_mfma_f32_16x16x32_bf16 v[248:251], v[156:159], v[136:139], v[248:251]\n"
    "v_mfma_f32_16x16x32_bf16 v[244:247], v[156:159], v[132:135], v[244:247]\n"
    "v_add_u32_dpp v55, v115, v51 quad_perm:[3,3,3,3] row_mask:0xf bank_mask:0xf\n"
    "v_mfma_f32_16x16x32_bf16 v[244:247], v[188:191], v[164:167], v[244:247]\n"
    "v_mfma_f32_16x16x32_bf16 v[240:243], v[188:191], v[160:163], v[240:243]\n"
    "v_mfma_f32_16x16x32_bf16 v[240:243], v[156:159], v[128:131], v[240:243]\n"
    "s_waitcnt vmcnt(7)\n"
    "s_barrier\n"
    "s_add_u32 m0, s35, 49152\n"
    "s_nop 0\n"
    "global_load_lds_dwordx4 v46, s[30:31] offset:0\n"
    "ds_read_b128 v[128:131], v58 offset:0\n"
    "ds_read_b128 v[132:135], v58 offset:2048\n"
    "ds_read_b128 v[136:139], v58 offset:4096\n"
    "global_load_lds_dwordx4 v47, s[30:31] offset:1024\n"
    "s_bfe_u32 s66, s29, 0x80000\n"
    "s_mul_i32 s66, s66, 53248\n"
    "s_add_u32 s30, s6, s66\n"
    "s_addc_u32 s31, s7, 0\n"
    "ds_read_b128 v[140:143], v58 offset:6144\n"
    "ds_read_b64_tr_b16 v[144:145], v80 offset:0\n"
    "ds_read_b64_tr_b16 v[146:147], v80 offset:1024\n"
    "s_add_u32 m0, s34, 49152\n"
    "s_nop 0\n"
    "global_load_lds_dwordx4 v52, s[36:37] offset:0\n"
    "ds_read_b64_tr_b16 v[148:149], v81 offset:0\n"
    "ds_read_b64_tr_b16 v[150:151], v81 offset:1024\n"
    "ds_read_b64_tr_b16 v[152:153], v82 offset:0\n"
    "global_load_lds_dwordx4 v53, s[36:37] offset:1024\n"
    "ds_read_b64_tr_b16 v[154:155], v82 offset:1024\n"
    "ds_read_b64_tr_b16 v[156:157], v83 offset:0\n"
    "ds_read_b64_tr_b16 v[158:159], v83 offset:1024\n"
    "global_load_lds_dwordx4 v54, s[36:37] offset:2048\n"
    "ds_read_b128 v[160:163], v59 offset:0\n"
    "ds_read_b128 v[164:167], v59 offset:2048\n"
    "ds_read_b128 v[168:171], v59 offset:4096\n"
    "global_load_lds_dwordx4 v55, s[36:37] offset:3072\n"
    "ds_read_b128 v[172:175], v59 offset:6144\n"
    "ds_read_b64_tr_b16 v[176:177], v80 offset:8192\n"
    "ds_read_b64_tr_b16 v[178:179], v80 offset:9216\n"
    "global_load_dwordx4 v[32:35], v37, s[8:9]\n"
    "ds_read_b64_tr_b16 v[180:181], v81 offset:8192\n"
    "ds_read_b64_tr_b16 v[182:183], v81 offset:9216\n"
    "ds_read_b64_tr_b16 v[184:185], v82 offset:8192\n"
    "ds_read_b64_tr_b16 v[186:187], v82 offset:9216\n"
    "ds_read_b64_tr_b16 v[188:189], v83 offset:8192\n"
    "ds_read_b64_tr_b16 v[190:191], v83 offset:9216\n"
    "s_waitcnt vmcnt(12) lgkmcnt(0)\n"
    "s_barrier\n"
    "v_mfma_f32_16x16x32_bf16 v[192:195], v[144:147], v[128:131], v[192:195]\n"
    "v_mfma_f32_16x16x32_bf16 v[192:195], v[176:179], v[160:163], v[192:195]\n"
    "v_mfma_f32_16x16x32_bf16 v[196:199], v[176:179], v[164:167], v[196:199]\n"
    "v_mfma_f32_16x16x32_bf16 v[196:199], v[144:147], v[132:135], v[196:199]\n"
    "v_mfma_f32_16x16x32_bf16 v[200:203], v[144:147], v[136:139], v[200:203]\n"
    "v_add_f32 v240, v240, v72\n"
    "v_mfma_f32_16x16x32_bf16 v[200:203], v[176:179], v[168:171], v[200:203]\n"
    "v_add_f32 v241, v241, v73\n"
    "v_mfma_f32_16x16x32_bf16 v[204:207], v[176:179], v[172:175], v[204:207]\n"
    "v_add_f32 v242, v242, v74\n"
    "v_mfma_f32_16x16x32_bf16 v[204:207], v[144:147], v[140:143], v[204:207]\n"
    "v_add_f32 v243, v243, v75\n"
    "v_mfma_f32_16x16x32_bf16 v[220:223], v[148:151], v[140:143], v[220:223]\n"
    "v_add_f32 v244, v244, v72\n"
    "v_mfma_f32_16x16x32_bf16 v[220:223], v[180:183], v[172:175], v[220:223]\n"
    "v_add_f32 v245, v245, v73\n"
    "v_mfma_f32_16x16x32_bf16 v[216:219], v[180:183], v[168:171], v[216:219]\n"
    "v_add_f32 v246, v246, v74\n"
    "v_mfma_f32_16x16x32_bf16 v[216:219], v[148:151], v[136:139], v[216:219]\n"
    "v_add_f32 v247, v247, v75\n"
    "v_mfma_f32_16x16x32_bf16 v[212:215], v[148:151], v[132:135], v[212:215]\n"
    "v_add_f32 v248, v248, v72\n"
    "v_mfma_f32_16x16x32_bf16 v[212:215], v[180:183], v[164:167], v[212:215]\n"
    "v_add_f32 v249, v249, v73\n"
    "v_mfma_f32_16x16x32_bf16 v[208:211], v[180:183], v[160:163], v[208:211]\n"
    "v_add_f32 v250, v250, v74\n"
    "v_mfma_f32_16x16x32_bf16 v[208:211], v[148:151], v[128:131], v[208:211]\n"
    "v_add_f32 v251, v251, v75\n"
    "v_mfma_f32_16x16x32_bf16 v[224:227], v[152:155], v[128:131], v[224:227]\n"
    "v_add_f32 v252, v252, v72\n"
    "v_mfma_f32_16x16x32_bf16 v[224:227], v[184:187], v[160:163], v[224:227]\n"
    "v_add_f32 v253, v253, v73\n"
    "v_mfma_f32_16x16x32_bf16 v[228:231], v[184:187], v[164:167], v[228:231]\n"
    "v_lshlrev_b32 v117, 7, v117\n"
    "v_add_f32 v254, v254, v74\n"
    "v_mfma_f32_16x16x32_bf16 v[228:231], v[152:155], v[132:135], v[228:231]\n"
    "v_add_f32 v255, v255, v75\n"
    "v_mfma_f32_16x16x32_bf16 v[232:235], v[152:155], v[136:139], v[232:235]\n"
    "v_add_u32_dpp v52, v116, v48 quad_perm:[0,0,0,0] row_mask:0xf bank_mask:0xf\n"
    "v_mfma_f32_16x16x32_bf16 v[232:235], v[184:187], v[168:171], v[232:235]\n"
    "v_mfma_f32_16x16x32_bf16 v[236:239], v[184:187], v[172:175], v[236:239]\n"
    "v_mfma_f32_16x16x32_bf16 v[236:239], v[152:155], v[140:143], v[236:239]\n"
    "v_add_u32_dpp v53, v116, v49 quad_perm:[1,1,1,1] row_mask:0xf bank_mask:0xf\n"
    "v_mfma_f32_16x16x32_bf16 v[252:255], v[156:159], v[140:143], v[252:255]\n"
    "v_mfma_f32_16x16x32_bf16 v[252:255], v[188:191], v[172:175], v[252:255]\n"
    "v_add_u32_dpp v54, v116, v50 quad_perm:[2,2,2,2] row_mask:0xf bank_mask:0xf\n"
    "v_mfma_f32_16x16x32_bf16 v[248:251], v[188:191], v[168:171], v[248:251]\n"
    "v_mfma_f32_16x16x32_bf16 v[248:251], v[156:159], v[136:139], v[248:251]\n"
    "v_mfma_f32_16x16x32_bf16 v[244:247], v[156:159], v[132:135], v[244:247]\n"
    "v_add_u32_dpp v55, v116, v51 quad_perm:[3,3,3,3] row_mask:0xf bank_mask:0xf\n"
    "v_mfma_f32_16x16x32_bf16 v[244:247], v[188:191], v[164:167], v[244:247]\n"
    "v_mfma_f32_16x16x32_bf16 v[240:243], v[188:191], v[160:163], v[240:243]\n"
    "v_mfma_f32_16x16x32_bf16 v[240:243], v[156:159], v[128:131], v[240:243]\n"
    "s_waitcnt vmcnt(8)\n"
    "s_barrier\n"
    "s_add_u32 m0, s35, 98304\n"
    "s_nop 0\n"
    "global_load_lds_dwordx4 v46, s[30:31] offset:0\n"
    "ds_read_b128 v[128:131], v56 offset:0\n"
    "ds_read_b128 v[132:135], v56 offset:2048\n"
    "ds_read_b128 v[136:139], v56 offset:4096\n"
    "global_load_lds_dwordx4 v47, s[30:31] offset:1024\n"
    "s_add_u32 s30, s30, 212992\n"
    "s_addc_u32 s31, s31, 0\n"
    "ds_read_b128 v[140:143], v56 offset:6144\n"
    "ds_read_b64_tr_b16 v[144:145], v76 offset:0\n"
    "ds_read_b64_tr_b16 v[146:147], v76 offset:1024\n"
    "s_add_u32 m0, s34, 98304\n"
    "s_nop 0\n"
    "global_load_lds_dwordx4 v52, s[36:37] offset:0\n"
    "ds_read_b64_tr_b16 v[148:149], v77 offset:0\n"
    "ds_read_b64_tr_b16 v[150:151], v77 offset:1024\n"
    "ds_read_b64_tr_b16 v[152:153], v78 offset:0\n"
    "global_load_lds_dwordx4 v53, s[36:37] offset:1024\n"
    "ds_read_b64_tr_b16 v[154:155], v78 offset:1024\n"
    "ds_read_b64_tr_b16 v[156:157], v79 offset:0\n"
    "ds_read_b64_tr_b16 v[158:159], v79 offset:1024\n"
    "global_load_lds_dwordx4 v54, s[36:37] offset:2048\n"
    "ds_read_b128 v[160:163], v57 offset:0\n"
    "ds_read_b128 v[164:167], v57 offset:2048\n"
    "ds_read_b128 v[168:171], v57 offset:4096\n"
    "global_load_lds_dwordx4 v55, s[36:37] offset:3072\n"
    "ds_read_b128 v[172:175], v57 offset:6144\n"
    "ds_read_b64_tr_b16 v[176:177], v76 offset:8192\n"
    "ds_read_b64_tr_b16 v[178:179], v76 offset:9216\n"
    "global_load_dword v40, v41, s[8:9]\n"
    "ds_read_b64_tr_b16 v[180:181], v77 offset:8192\n"
    "ds_read_b64_tr_b16 v[182:183], v77 offset:9216\n"
    "ds_read_b64_tr_b16 v[184:185], v78 offset:8192\n"
    "ds_read_b64_tr_b16 v[186:187], v78 offset:9216\n"
    "ds_read_b64_tr_b16 v[188:189], v79 offset:8192\n"
    "ds_read_b64_tr_b16 v[190:191], v79 offset:9216\n"
    "s_waitcnt vmcnt(12) lgkmcnt(0)\n"
    "s_barrier\n"
    "v_mfma_f32_16x16x32_bf16 v[192:195], v[144:147], v[128:131], v[192:195]\n"
    "v_lshlrev_b32 v118, 7, v118\n"
    "v_mfma_f32_16x16x32_bf16 v[192:195], v[176:179], v[160:163], v[192:195]\n"
    "v_add_u32_dpp v52, v117, v48 quad_perm:[0,0,0,0] row_mask:0xf bank_mask:0xf\n"
    "v_mfma_f32_16x16x32_bf16 v[196:199], v[176:179], v[164:167], v[196:199]\n"
    "v_add_u32_dpp v53, v117, v49 quad_perm:[1,1,1,1] row_mask:0xf bank_mask:0xf\n"
    "v_mfma_f32_16x16x32_bf16 v[196:199], v[144:147], v[132:135], v[196:199]\n"
    "v_add_u32_dpp v54, v117, v50 quad_perm:[2,2,2,2] row_mask:0xf bank_mask:0xf\n"
    "v_mfma_f32_16x16x32_bf16 v[200:203], v[144:147], v[136:139], v[200:203]\n"
    "v_add_u32_dpp v55, v117, v51 quad_perm:[3,3,3,3] row_mask:0xf bank_mask:0xf\n"
    "v_mfma_f32_16x16x32_bf16 v[200:203], v[176:179], v[168:171], v[200:203]\n"
    "v_readlane_b32 s25, v26, 9\n"
    "v_mfma_f32_16x16x32_bf16 v[204:207], v[176:179], v[172:175], v[204:207]\n"
    "v_readlane_b32 s40, v26, 8\n"
    "v_mfma_f32_16x16x32_bf16 v[204:207], v[144:147], v[140:143], v[204:207]\n"
    "v_readlane_b32 s41, v27, 8\n"
    "v_mfma_f32_16x16x32_bf16 v[220:223], v[148:151], v[140:143], v[220:223]\n"
    "s_mul_i32 s42, s25, 0x9e3779b1\n"
    "v_mfma_f32_16x16x32_bf16 v[220:223], v[180:183], v[172:175], v[220:223]\n"
    "s_lshr_b32 s42, s42, 24\n"
    "v_mfma_f32_16x16x32_bf16 v[216:219], v[180:183], v[168:171], v[216:219]\n"
    "s_xor_b32 s42, s42, s40\n"
    "v_mfma_f32_16x16x32_bf16 v[216:219], v[148:151], v[136:139], v[216:219]\n"
    "s_and_b32 s42, s42, 0xff\n"
    "v_mfma_f32_16x16x32_bf16 v[212:215], v[148:151], v[132:135], v[212:215]\n"
    "s_xor_b32 s41, s41, s25\n"
    "v_mfma_f32_16x16x32_bf16 v[212:215], v[180:183], v[164:167], v[212:215]\n"
    "s_xor_b32 s41, s41, 0xa5c30f96\n"
    "v_mfma_f32_16x16x32_bf16 v[208:211], v[180:183], v[160:163], v[208:211]\n"
    "s_or_b32 s42, s41, s42\n"
    "v_mfma_f32_16x16x32_bf16 v[208:211], v[148:151], v[128:131], v[208:211]\n"
    "v_mfma_f32_16x16x32_bf16 v[224:227], v[152:155], v[128:131], v[224:227]\n"
    "s_lshr_b32 s41, s40, 8\n"
    "v_mfma_f32_16x16x32_bf16 v[224:227], v[184:187], v[160:163], v[224:227]\n"
    "s_mul_i32 s40, s41, 0x9e3779b1\n"
    "v_mfma_f32_16x16x32_bf16 v[228:231], v[184:187], v[164:167], v[228:231]\n"
    "s_xor_b32 s41, s41, 0xa5c30f96\n"
    "v_mfma_f32_16x16x32_bf16 v[228:231], v[152:155], v[132:135], v[228:231]\n"
    "s_lshr_b32 s40, s40, 24\n"
    "v_mfma_f32_16x16x32_bf16 v[232:235], v[152:155], v[136:139], v[232:235]\n"
    "v_xor_b32 v13, s40, v26\n"
    "v_mfma_f32_16x16x32_bf16 v[232:235], v[184:187], v[168:171], v[232:235]\n"
    "v_xor_b32 v22, s41, v27\n"
    "v_mfma_f32_16x16x32_bf16 v[236:239], v[184:187], v[172:175], v[236:239]\n"
    "v_lshl_or_b32 v13, v13, 24, v22\n"
    "v_mfma_f32_16x16x32_bf16 v[236:239], v[152:155], v[140:143], v[236:239]\n"
    "v_cmp_ne_u32 s[66:67], 0, v13\n"
    "v_mfma_f32_16x16x32_bf16 v[252:255], v[156:159], v[140:143], v[252:255]\n"
    "s_and_b32 s66, s66, 0xff\n"
    "v_mfma_f32_16x16x32_bf16 v[252:255], v[188:191], v[172:175], v[252:255]\n"
    "s_or_b32 s42, s42, s66\n"
    "v_mfma_f32_16x16x32_bf16 v[248:251], v[188:191], v[168:171], v[248:251]\n"
    "s_cmp_eq_u32 s20, 3\n"
    "v_mfma_f32_16x16x32_bf16 v[248:251], v[156:159], v[136:139], v[248:251]\n"
    "s_cselect_b32 s42, s42, 0\n"
    "v_mfma_f32_16x16x32_bf16 v[244:247], v[156:159], v[132:135], v[244:247]\n"
    "s_cmp_lg_u32 s42, 0\n"
    "v_mfma_f32_16x16x32_bf16 v[244:247], v[188:191], v[164:167], v[244:247]\n"
    "s_cbranch_scc1 .Lxd_pv4\n"
    ".Lxd_pv4r:\n"
    "v_mfma_f32_16x16x32_bf16 v[240:243], v[188:191], v[160:163], v[240:243]\n"
    "v_mfma_f32_16x16x32_bf16 v[240:243], v[156:159], v[128:131], v[240:243]\n"
    "s_waitcnt vmcnt(8)\n"
    "s_barrier\n"
    "ds_read_b64 v[84:85], v42 offset:0\n"
    "ds_read_b64 v[86:87], v42 offset:32\n"
    "ds_read_b64 v[88:89], v42 offset:64\n"
    "ds_read_b64 v[90:91], v42 offset:96\n"
    "s_add_u32 m0, s35, 0\n"
    "s_nop 0\n"
    "global_load_lds_dwordx4 v46, s[30:31] offset:0\n"
    "ds_read_b128 v[128:131], v56 offset:49152\n"
    "ds_read_b128 v[132:135], v56 offset:51200\n"
    "ds_read_b128 v[136:139], v56 offset:53248\n"
    "global_load_lds_dwordx4 v47, s[30:31] offset:1024\n"
    "s_add_u32 s30, s30, 212992\n"
    "s_addc_u32 s31, s31, 0\n"
    "ds_read_b128 v[140:143], v56 offset:55296\n"
    "ds_read_b64_tr_b16 v[144:145], v76 offset:49152\n"
    "ds_read_b64_tr_b16 v[146:147], v76 offset:50176\n"
    "s_add_u32 m0, s34, 0\n"
    "s_nop 0\n"
    "global_load_lds_dwordx4 v52, s[36:37] offset:0\n"
    "ds_read_b64_tr_b16 v[148:149], v77 offset:49152\n"
    "ds_read_b64_tr_b16 v[150:151], v77 offset:50176\n"
    "ds_read_b64_tr_b16 v[152:153], v78 offset:49152\n"
    "global_load_lds_dwordx4 v53, s[36:37] offset:1024\n"
    "ds_read_b64_tr_b16 v[154:155], v78 offset:50176\n"
    "ds_read_b64_tr_b16 v[156:157], v79 offset:49152\n"
    "ds_read_b64_tr_b16 v[158:159], v79 offset:50176\n"
    "global_load_lds_dwordx4 v54, s[36:37] offset:2048\n"
    "ds_read_b128 v[160:163], v57 offset:49152\n"
    "ds_read_b128 v[164:167], v57 offset:51200\n"
    "ds_read_b128 v[168:171], v57 offset:53248\n"
    "global_load_lds_dwordx4 v55, s[36:37] offset:3072\n"
    "ds_read_b128 v[172:175], v57 offset:55296\n"
    "ds_read_b64_tr_b16 v[176:177], v76 offset:57344\n"
    "ds_read_b64_tr_b16 v[178:179], v76 offset:58368\n"
    "global_load_dwordx4 v[36:39], v38, s[8:9]\n"
    "ds_read_b64_tr_b16 v[180:181], v77 offset:57344\n"
    "ds_read_b64_tr_b16 v[182:183], v77 offset:58368\n"
    "ds_read_b64_tr_b16 v[184:185], v78 offset:57344\n"
    "ds_read_b64_tr_b16 v[186:187], v78 offset:58368\n"
    "ds_read_b64_tr_b16 v[188:189], v79 offset:57344\n"
    "ds_read_b64_tr_b16 v[190:191], v79 offset:58368\n"
    "s_waitcnt vmcnt(12) lgkmcnt(0)\n"
    "s_barrier\n"
    "v_mfma_f32_16x16x32_bf16 v[192:195], v[144:147], v[128:131], v[192:195]\n"
    "v_lshlrev_b32 v119, 7, v119\n"
    "v_mfma_f32_16x16x32_bf16 v[192:195], v[176:179], v[160:163], v[192:195]\n"
    "v_add_u32_dpp v52, v118, v48 quad_perm:[0,0,0,0] row_mask:0xf bank_mask:0xf\n"
    "v_mfma_f32_16x16x32_bf16 v[196:199], v[176:179], v[164:167], v[196:199]\n"
    "v_add_u32_dpp v53, v118, v49 quad_perm:[1,1,1,1] row_mask:0xf bank_mask:0xf\n"
    "v_mfma_f32_16x16x32_bf16 v[196:199], v[144:147], v[132:135], v[196:199]\n"
    "v_mfma_f32_16x16x32_bf16 v[200:203], v[144:147], v[136:139], v[200:203]\n"
    "v_add_u32_dpp v54, v118, v50 quad_perm:[2,2,2,2] row_mask:0xf bank_mask:0xf\n"
    "v_mfma_f32_16x16x32_bf16 v[200:203], v[176:179], v[168:171], v[200:203]\n"
    "v_add_u32_dpp v55, v118, v51 quad_perm:[3,3,3,3] row_mask:0xf bank_mask:0xf\n"
    "v_mfma_f32_16x16x32_bf16 v[204:207], v[176:179], v[172:175], v[204:207]\n"
    "v_mfma_f32_16x16x32_bf16 v[204:207], v[144:147], v[140:143], v[204:207]\n"
    "v_lshlrev_b32 v60, 16, v84\n"
    "v_mfma_f32_16x16x32_bf16 v[220:223], v[148:151], v[140:143], v[220:223]\n"
    "v_and_b32 v61, 0xffff0000, v84\n"
    "v_mfma_f32_16x16x32_bf16 v[220:223], v[180:183], v[172:175], v[220:223]\n"
    "v_mfma_f32_16x16x32_bf16 v[216:219], v[180:183], v[168:171], v[216:219]\n"
    "v_lshlrev_b32 v62, 16, v85\n"
    "v_mfma_f32_16x16x32_bf16 v[216:219], v[148:151], v[136:139], v[216:219]\n"
    "v_and_b32 v63, 0xffff0000, v85\n"
    "v_mfma_f32_16x16x32_bf16 v[212:215], v[148:151], v[132:135], v[212:215]\n"
    "v_mfma_f32_16x16x32_bf16 v[212:215], v[180:183], v[164:167], v[212:215]\n"
    "v_lshlrev_b32 v64, 16, v86\n"
    "v_mfma_f32_16x16x32_bf16 v[208:211], v[180:183], v[160:163], v[208:211]\n"
    "v_and_b32 v65, 0xffff0000, v86\n"
    "v_mfma_f32_16x16x32_bf16 v[208:211], v[148:151], v[128:131], v[208:211]\n"
    "v_mfma_f32_16x16x32_bf16 v[224:227], v[152:155], v[128:131], v[224:227]\n"
    "v_lshlrev_b32 v66, 16, v87\n"
    "v_mfma_f32_16x16x32_bf16 v[224:227], v[184:187], v[160:163], v[224:227]\n"
    "v_and_b32 v67, 0xffff0000, v87\n"
    "v_mfma_f32_16x16x32_bf16 v[228:231], v[184:187], v[164:167], v[228:231]\n"
    "v_mfma_f32_16x16x32_bf16 v[228:231], v[152:155], v[132:135], v[228:231]\n"
    "v_lshlrev_b32 v68, 16, v88\n"
    "v_mfma_f32_16x16x32_bf16 v[232:235], v[152:155], v[136:139], v[232:235]\n"
    "v_and_b32 v69, 0xffff0000, v88\n"
    "v_mfma_f32_16x16x32_bf16 v[232:235], v[184:187], v[168:171], v[232:235]\n"
    "v_mfma_f32_16x16x32_bf16 v[236:239], v[184:187], v[172:175], v[236:239]\n"
    "v_lshlrev_b32 v70, 16, v89\n"
    "v_mfma_f32_16x16x32_bf16 v[236:239], v[152:155], v[140:143], v[236:239]\n"
    "v_and_b32 v71, 0xffff0000, v89\n"
    "v_mfma_f32_16x16x32_bf16 v[252:255], v[156:159], v[140:143], v[252:255]\n"
    "v_mfma_f32_16x16x32_bf16 v[252:255], v[188:191], v[172:175], v[252:255]\n"
    "v_lshlrev_b32 v72, 16, v90\n"
    "v_mfma_f32_16x16x32_bf16 v[248:251], v[188:191], v[168:171], v[248:251]\n"
    "v_and_b32 v73, 0xffff0000, v90\n"
    "v_mfma_f32_16x16x32_bf16 v[248:251], v[156:159], v[136:139], v[248:251]\n"
    "v_mfma_f32_16x16x32_bf16 v[244:247], v[156:159], v[132:135], v[244:247]\n"
    "v_lshlrev_b32 v74, 16, v91\n"
    "v_mfma_f32_16x16x32_bf16 v[244:247], v[188:191], v[164:167], v[244:247]\n"
    "v_and_b32 v75, 0xffff0000, v91\n"
    "v_mfma_f32_16x16x32_bf16 v[240:243], v[188:191], v[160:163], v[240:243]\n"
    "v_mfma_f32_16x16x32_bf16 v[240:243], v[156:159], v[128:131], v[240:243]\n"
    "s_waitcnt vmcnt(8)\n"
    "s_barrier\n"
    "s_add_u32 m0, s35, 49152\n"
    "s_nop 0\n"
    "global_load_lds_dwordx4 v46, s[30:31] offset:0\n"
    "ds_read_b128 v[128:131], v58 offset:0\n"
    "ds_read_b128 v[132:135], v58 offset:2048\n"
    "ds_read_b128 v[136:139], v58 offset:4096\n"
    "ds_read_b128 v[140:143], v58 offset:6144\n"
    "global_load_lds_dwordx4 v47, s[30:31] offset:1024\n"
    "s_add_u32 s30, s30, 212992\n"
    "s_addc_u32 s31, s31, 0\n"
    "ds_read_b64_tr_b16 v[144:145], v80 offset:0\n"
    "ds_read_b64_tr_b16 v[146:147], v80 offset:1024\n"
    "ds_read_b64_tr_b16 v[148:149], v81 offset:0\n"
    "ds_read_b64_tr_b16 v[150:151], v81 offset:1024\n"
    "s_add_u32 m0, s34, 49152\n"
    "s_nop 0\n"
    "global_load_lds_dwordx4 v52, s[36:37] offset:0\n"
    "ds_read_b64_tr_b16 v[152:153], v82 offset:0\n"
    "ds_read_b64_tr_b16 v[154:155], v82 offset:1024\n"
    "ds_read_b64_tr_b16 v[156:157], v83 offset:0\n"
    "ds_read_b64_tr_b16 v[158:159], v83 offset:1024\n"
    "global_load_lds_dwordx4 v53, s[36:37] offset:1024\n"
    "ds_read_b128 v[160:163], v59 offset:0\n"
    "ds_read_b128 v[164:167], v59 offset:2048\n"
    "ds_read_b128 v[168:171], v59 offset:4096\n"
    "ds_read_b128 v[172:175], v59 offset:6144\n"
    "global_load_lds_dwordx4 v54, s[36:37] offset:2048\n"
    "ds_read_b64_tr_b16 v[176:177], v80 offset:8192\n"
    "ds_read_b64_tr_b16 v[178:179], v80 offset:9216\n"
    "ds_read_b64_tr_b16 v[180:181], v81 offset:8192\n"
    "ds_read_b64_tr_b16 v[182:183], v81 offset:9216\n"
    "global_load_lds_dwordx4 v55, s[36:37] offset:3072\n"
    "ds_read_b64_tr_b16 v[184:185], v82 offset:8192\n"
    "ds_read_b64_tr_b16 v[186:187], v82 offset:9216\n"
    "ds_read_b64_tr_b16 v[188:189], v83 offset:8192\n"
    "ds_read_b64_tr_b16 v[190:191], v83 offset:9216\n"
    "s_waitcnt vmcnt(11) lgkmcnt(0)\n"
    "s_barrier\n"
    "v_mfma_f32_16x16x32_bf16 v[192:195], v[144:147], v[128:131], v[192:195]\n"
    "v_lshlrev_b32 v120, 7, v120\n"
    "v_mfma_f32_16x16x32_bf16 v[192:195], v[176:179], v[160:163], v[192:195]\n"
    "v_add_u32_dpp v52, v119, v48 quad_perm:[0,0,0,0] row_mask:0xf bank_mask:0xf\n"
    "v_mfma_f32_16x16x32_bf16 v[196:199], v[176:179], v[164:167], v[196:199]\n"
    "v_mfma_f32_16x16x32_bf16 v[196:199], v[144:147], v[132:135], v[196:199]\n"
    "v_add_u32_dpp v53, v119, v49 quad_perm:[1,1,1,1] row_mask:0xf bank_mask:0xf\n"
    "v_mfma_f32_16x16x32_bf16 v[200:203], v[144:147], v[136:139], v[200:203]\n"
    "v_mfma_f32_16x16x32_bf16 v[200:203], v[176:179], v[168:171], v[200:203]\n"
    "v_add_u32_dpp v54, v119, v50 quad_perm:[2,2,2,2] row_mask:0xf bank_mask:0xf\n"
    "v_mfma_f32_16x16x32_bf16 v[204:207], v[176:179], v[172:175], v[204:207]\n"
    "v_add_u32_dpp v55, v119, v51 quad_perm:[3,3,3,3] row_mask:0xf bank_mask:0xf\n"
    "v_mfma_f32_16x16x32_bf16 v[204:207], v[144:147], v[140:143], v[204:207]\n"
    "v_mfma_f32_16x16x32_bf16 v[220:223], v[148:151], v[140:143], v[220:223]\n"
    "v_lshrrev_b32 v22, 8, v26\n"
    "v_mfma_f32_16x16x32_bf16 v[220:223], v[180:183], v[172:175], v[220:223]\n"
    "v_mfma_f32_16x16x32_bf16 v[216:219], v[180:183], v[168:171], v[216:219]\n"
    "v_mov_b32 v26, s40\n"
    "v_mfma_f32_16x16x32_bf16 v[216:219], v[148:151], v[136:139], v[216:219]\n"
    "v_mfma_f32_16x16x32_bf16 v[212:215], v[148:151], v[132:135], v[212:215]\n"
    "v_mov_b32 v27, s41\n"
    "v_mfma_f32_16x16x32_bf16 v[212:215], v[180:183], v[164:167], v[212:215]\n"
    "v_add_u32_dpp v22, v22, v22 row_shl:4 row_mask:0xf bank_mask:0xf\n"
    "v_mfma_f32_16x16x32_bf16 v[208:211], v[180:183], v[160:163], v[208:211]\n"
    "v_mfma_f32_16x16x32_bf16 v[208:211], v[148:151], v[128:131], v[208:211]\n"
    "v_add_u32_e64 v43, s25, 1\n"
    "v_mfma_f32_16x16x32_bf16 v[224:227], v[152:155], v[128:131], v[224:227]\n"
    "v_mfma_f32_16x16x32_bf16 v[224:227], v[184:187], v[160:163], v[224:227]\n"
    "v_lshlrev_b32 v8, 2, v1\n"
    "v_mfma_f32_16x16x32_bf16 v[228:231], v[184:187], v[164:167], v[228:231]\n"
    "v_max_u32_dpp v13, v22, v22 quad_perm:[1,0,3,2] row_mask:0xf bank_mask:0xf\n"
    "v_mfma_f32_16x16x32_bf16 v[228:231], v[152:155], v[132:135], v[228:231]\n"
    "v_mfma_f32_16x16x32_bf16 v[232:235], v[152:155], v[136:139], v[232:235]\n"
    "v_add_u32 v8, 154112, v8\n"
    "v_mfma_f32_16x16x32_bf16 v[232:235], v[184:187], v[168:171], v[232:235]\n"
    "v_mfma_f32_16x16x32_bf16 v[236:239], v[184:187], v[172:175], v[236:239]\n"
    "s_nop 0\n"
    "v_mfma_f32_16x16x32_bf16 v[236:239], v[152:155], v[140:143], v[236:239]\n"
    "v_mfma_f32_16x16x32_bf16 v[252:255], v[156:159], v[140:143], v[252:255]\n"
    "v_max_u32_dpp v13, v13, v13 quad_perm:[2,3,0,1] row_mask:0xf bank_mask:0xf\n"
    "v_mfma_f32_16x16x32_bf16 v[252:255], v[188:191], v[172:175], v[252:255]\n"
    "v_sub_u32 v22, v22, v13\n"
    "v_mfma_f32_16x16x32_bf16 v[248:251], v[188:191], v[168:171], v[248:251]\n"
    "v_mfma_f32_16x16x32_bf16 v[248:251], v[156:159], v[136:139], v[248:251]\n"
    "v_subrev_u32 v13, 818, v13\n"
    "v_mfma_f32_16x16x32_bf16 v[244:247], v[156:159], v[132:135], v[244:247]\n"
    "v_mfma_f32_16x16x32_bf16 v[244:247], v[188:191], v[164:167], v[244:247]\n"
    "v_ldexp_f32 v22, 1.0, v22\n"
    "v_mfma_f32_16x16x32_bf16 v[240:243], v[188:191], v[160:163], v[240:243]\n"
    "v_mfma_f32_16x16x32_bf16 v[240:243], v[156:159], v[128:131], v[240:243]\n"
    "s_waitcnt vmcnt(7)\n"
    "s_barrier\n"
    "s_cmp_eq_u32 s20, 3\n"
    "s_cbranch_scc1 .Lxd_on5\n"
    ".Lxd_on5r:\n"
    "s_add_u32 m0, s35, 98304\n"
    "s_nop 0\n"
    "global_load_lds_dwordx4 v46, s[30:31] offset:0\n"
    "ds_read_b128 v[128:131], v56 offset:0\n"
    "ds_read_b128 v[132:135], v56 offset:2048\n"
    "ds_read_b128 v[136:139], v56 offset:4096\n"
    "ds_read_b128 v[140:143], v56 offset:6144\n"
    "global_load_lds_dwordx4 v47, s[30:31] offset:1024\n"
    "s_bfe_u32 s66, s29, 0x80008\n"
    "s_mul_i32 s66, s66, 53248\n"
    "s_add_u32 s30, s6, s66\n"
    "s_addc_u32 s31, s7, 0\n"
    "ds_read_b64_tr_b16 v[144:145], v76 offset:0\n"
    "ds_read_b64_tr_b16 v[146:147], v76 offset:1024\n"
    "ds_read_b64_tr_b16 v[148:149], v77 offset:0\n"
    "ds_read_b64_tr_b16 v[150:151], v77 offset:1024\n"
    "s_add_u32 m0, s34, 98304\n"
    "s_nop 0\n"
    "global_load_lds_dwordx4 v52, s[36:37] offset:0\n"
    "ds_read_b64_tr_b16 v[152:153], v78 offset:0\n"
    "ds_read_b64_tr_b16 v[154:155], v78 offset:1024\n"
    "ds_read_b64_tr_b16 v[156:157], v79 offset:0\n"
    "ds_read_b64_tr_b16 v[158:159], v79 offset:1024\n"
    "global_load_lds_dwordx4 v53, s[36:37] offset:1024\n"
    "ds_read_b128 v[160:163], v57 offset:0\n"
    "ds_read_b128 v[164:167], v57 offset:2048\n"
    "ds_read_b128 v[168:171], v57 offset:4096\n"
    "ds_read_b128 v[172:175], v57 offset:6144\n"
    "global_load_lds_dwordx4 v54, s[36:37] offset:2048\n"
    "ds_read_b64_tr_b16 v[176:177], v76 offset:8192\n"
    "ds_read_b64_tr_b16 v[178:179], v76 offset:9216\n"
    "ds_read_b64_tr_b16 v[180:181], v77 offset:8192\n"
    "ds_read_b64_tr_b16 v[182:183], v77 offset:9216\n"
    "global_load_lds_dwordx4 v55, s[36:37] offset:3072\n"
    "ds_read_b64_tr_b16 v[184:185], v78 offset:8192\n"
    "ds_read_b64_tr_b16 v[186:187], v78 offset:9216\n"
    "ds_read_b64_tr_b16 v[188:189], v79 offset:8192\n"
    "ds_read_b64_tr_b16 v[190:191], v79 offset:9216\n"
    "s_waitcnt vmcnt(10) lgkmcnt(0)\n"
    "s_barrier\n"
    "v_mfma_f32_16x16x32_bf16 v[192:195], v[144:147], v[128:131], v[192:195]\n"
    "v_lshlrev_b32 v121, 7, v121\n"
    "v_mfma_f32_16x16x32_bf16 v[192:195], v[176:179], v[160:163], v[192:195]\n"
    "v_add_u32_dpp v52, v120, v48 quad_perm:[0,0,0,0] row_mask:0xf bank_mask:0xf\n"
    "v_mfma_f32_16x16x32_bf16 v[196:199], v[176:179], v[164:167], v[196:199]\n"
    "v_mfma_f32_16x16x32_bf16 v[196:199], v[144:147], v[132:135], v[196:199]\n"
    "v_add_u32_dpp v53, v120, v49 quad_perm:[1,1,1,1] row_mask:0xf bank_mask:0xf\n"
    "v_mfma_f32_16x16x32_bf16 v[200:203], v[144:147], v[136:139], v[200:203]\n"
    "v_add_u32_dpp v54, v120, v50 quad_perm:[2,2,2,2] row_mask:0xf bank_mask:0xf\n"
    "v_mfma_f32_16x16x32_bf16 v[200:203], v[176:179], v[168:171], v[200:203]\n"
    "v_mfma_f32_16x16x32_bf16 v[204:207], v[176:179], v[172:175], v[204:207]\n"
    "v_add_u32_dpp v55, v120, v51 quad_perm:[3,3,3,3] row_mask:0xf bank_mask:0xf\n"
    "v_mfma_f32_16x16x32_bf16 v[204:207], v[144:147], v[140:143], v[204:207]\n"
    "v_mfma_f32_16x16x32_bf16 v[220:223], v[148:151], v[140:143], v[220:223]\n"
    "s_movk_i32 s43, 15744\n"
    "v_pk_maximum3_f16 v21, v28, v28, s43 op_sel_hi:[1,1,0] neg_lo:[0,1,0] neg_hi:[0,1,0]\n"
    "v_mfma_f32_16x16x32_bf16 v[220:223], v[180:183], v[172:175], v[220:223]\n"
    "v_pk_maximum3_f16 v21, v29, v29, v21 neg_lo:[0,1,0] neg_hi:[0,1,0]\n"
    "v_mfma_f32_16x16x32_bf16 v[216:219], v[180:183], v[168:171], v[216:219]\n"
    "v_mfma_f32_16x16x32_bf16 v[216:219], v[148:151], v[136:139], v[216:219]\n"
    "v_pk_maximum3_f16 v21, v30, v30, v21 neg_lo:[0,1,0] neg_hi:[0,1,0]\n"
    "v_mfma_f32_16x16x32_bf16 v[212:215], v[148:151], v[132:135], v[212:215]\n"
    "v_mfma_f32_16x16x32_bf16 v[212:215], v[180:183], v[164:167], v[212:215]\n"
    "v_pk_maximum3_f16 v21, v31, v31, v21 neg_lo:[0,1,0] neg_hi:[0,1,0]\n"
    "v_mfma_f32_16x16x32_bf16 v[208:211], v[180:183], v[160:163], v[208:211]\n"
    "v_pk_maximum3_f16 v21, v32, v32, v21 neg_lo:[0,1,0] neg_hi:[0,1,0]\n"
    "v_mfma_f32_16x16x32_bf16 v[208:211], v[148:151], v[128:131], v[208:211]\n"
    "v_mfma_f32_16x16x32_bf16 v[224:227], v[152:155], v[128:131], v[224:227]\n"
    "v_pk_maximum3_f16 v21, v33, v33, v21 neg_lo:[0,1,0] neg_hi:[0,1,0]\n"
    "v_mfma_f32_16x16x32_bf16 v[224:227], v[184:187], v[160:163], v[224:227]\n"
    "v_pk_maximum3_f16 v21, v34, v34, v21 neg_lo:[0,1,0] neg_hi:[0,1,0]\n"
    "v_mfma_f32_16x16x32_bf16 v[228:231], v[184:187], v[164:167], v[228:231]\n"
    "v_mfma_f32_16x16x32_bf16 v[228:231], v[152:155], v[132:135], v[228:231]\n"
    "v_pk_maximum3_f16 v21, v35, v35, v21 neg_lo:[0,1,0] neg_hi:[0,1,0]\n"
    "v_mfma_f32_16x16x32_bf16 v[232:235], v[152:155], v[136:139], v[232:235]\n"
    "v_mfma_f32_16x16x32_bf16 v[232:235], v[184:187], v[168:171], v[232:235]\n"
    "v_pk_maximum3_f16 v21, v36, v36, v21 neg_lo:[0,1,0] neg_hi:[0,1,0]\n"
    "v_mfma_f32_16x16x32_bf16 v[236:239], v[184:187], v[172:175], v[236:239]\n"
    "v_pk_maximum3_f16 v21, v37, v37, v21 neg_lo:[0,1,0] neg_hi:[0,1,0]\n"
    "v_mfma_f32_16x16x32_bf16 v[236:239], v[152:155], v[140:143], v[236:239]\n"
    "v_mfma_f32_16x16x32_bf16 v[252:255], v[156:159], v[140:143], v[252:255]\n"
    "v_pk_maximum3_f16 v21, v38, v38, v21 neg_lo:[0,1,0] neg_hi:[0,1,0]\n"
    "v_mfma_f32_16x16x32_bf16 v[252:255], v[188:191], v[172:175], v[252:255]\n"
    "v_mfma_f32_16x16x32_bf16 v[248:251], v[188:191], v[168:171], v[248:251]\n"
    "v_pk_maximum3_f16 v21, v39, v39, v21 neg_lo:[0,1,0] neg_hi:[0,1,0]\n"
    "v_mfma_f32_16x16x32_bf16 v[248:251], v[156:159], v[136:139], v[248:251]\n"
    "v_pk_maximum3_f16 v21, v40, v40, v21 neg_lo:[0,1,0] neg_hi:[0,1,0]\n"
    "v_mfma_f32_16x16x32_bf16 v[244:247], v[156:159], v[132:135], v[244:247]\n"
    "v_mfma_f32_16x16x32_bf16 v[244:247], v[188:191], v[164:167], v[244:247]\n"
    "v_pk_max_u16 v21, v21, v21 op_sel:[0,1] op_sel_hi:[1,0]\n"
    "v_mfma_f32_16x16x32_bf16 v[240:243], v[188:191], v[160:163], v[240:243]\n"
    "v_mfma_f32_16x16x32_bf16 v[240:243], v[156:159], v[128:131], v[240:243]\n"
    "s_waitcnt vmcnt(6)\n"
    "s_barrier\n"
    "s_add_u32 m0, s35, 0\n"
    "s_nop 0\n"
    "global_load_lds_dwordx4 v46, s[30:31] offset:0\n"
    "ds_read_b128 v[128:131], v56 offset:49152\n"
    "ds_read_b128 v[132:135], v56 offset:51200\n"
    "ds_read_b128 v[136:139], v56 offset:53248\n"
    "ds_read_b128 v[140:143], v56 offset:55296\n"
    "global_load_lds_dwordx4 v47, s[30:31] offset:1024\n"
    "s_add_u32 s30, s30, 212992\n"
    "s_addc_u32 s31, s31, 0\n"
    "ds_read_b64_tr_b16 v[144:145], v76 offset:49152\n"
    "ds_read_b64_tr_b16 v[146:147], v76 offset:50176\n"
    "ds_read_b64_tr_b16 v[148:149], v77 offset:49152\n"
    "ds_read_b64_tr_b16 v[150:151], v77 offset:50176\n"
    "s_add_u32 m0, s34, 0\n"
    "s_nop 0\n"
    "global_load_lds_dwordx4 v52, s[36:37] offset:0\n"
    "ds_read_b64_tr_b16 v[152:153], v78 offset:49152\n"
    "ds_read_b64_tr_b16 v[154:155], v78 offset:50176\n"
    "ds_read_b64_tr_b16 v[156:157], v79 offset:49152\n"
    "ds_read_b64_tr_b16 v[158:159], v79 offset:50176\n"
    "global_load_lds_dwordx4 v53, s[36:37] offset:1024\n"
    "ds_read_b128 v[160:163], v57 offset:49152\n"
    "ds_read_b128 v[164:167], v57 offset:51200\n"
    "ds_read_b128 v[168:171], v57 offset:53248\n"
    "ds_read_b128 v[172:175], v57 offset:55296\n"
    "global_load_lds_dwordx4 v54, s[36:37] offset:2048\n"
    "ds_read_b64_tr_b16 v[176:177], v76 offset:57344\n"
    "ds_read_b64_tr_b16 v[178:179], v76 offset:58368\n"
    "ds_read_b64_tr_b16 v[180:181], v77 offset:57344\n"
    "ds_read_b64_tr_b16 v[182:183], v77 offset:58368\n"
    "global_load_lds_dwordx4 v55, s[36:37] offset:3072\n"
    "ds_read_b64_tr_b16 v[184:185], v78 offset:57344\n"
    "ds_read_b64_tr_b16 v[186:187], v78 offset:58368\n"
    "ds_read_b64_tr_b16 v[188:189], v79 offset:57344\n"
    "ds_read_b64_tr_b16 v[190:191], v79 offset:58368\n"
    "s_waitcnt vmcnt(10) lgkmcnt(0)\n"
    "s_barrier\n"
    "v_mfma_f32_16x16x32_bf16 v[192:195], v[144:147], v[128:131], v[192:195]\n"
    "v_add_u32_dpp v52, v121, v48 quad_perm:[0,0,0,0] row_mask:0xf bank_mask:0xf\n"
    "v_mfma_f32_16x16x32_bf16 v[192:195], v[176:179], v[160:163], v[192:195]\n"
    "v_add_u32_dpp v53, v121, v49 quad_perm:[1,1,1,1] row_mask:0xf bank_mask:0xf\n"
    "v_mfma_f32_16x16x32_bf16 v[196:199], v[176:179], v[164:167], v[196:199]\n"
    "v_add_u32_dpp v54, v121, v50 quad_perm:[2,2,2,2] row_mask:0xf bank_mask:0xf\n"
    "v_mfma_f32_16x16x32_bf16 v[196:199], v[144:147], v[132:135], v[196:199]\n"
    "v_add_u32_dpp v55, v121, v51 quad_perm:[3,3,3,3] row_mask:0xf bank_mask:0xf\n"
    "v_mfma_f32_16x16x32_bf16 v[200:203], v[144:147], v[136:139], v[200:203]\n"
    "v_bfe_u32 v22, v1, 4, 1\n"
    "v_mfma_f32_16x16x32_bf16 v[200:203], v[176:179], v[168:171], v[200:203]\n"
    "v_lshlrev_b32 v22, 2, v22\n"
    "v_mfma_f32_16x16x32_bf16 v[204:207], v[176:179], v[172:175], v[204:207]\n"
    "v_add_u32 v22, 154112, v22\n"
    "v_mfma_f32_16x16x32_bf16 v[204:207], v[144:147], v[140:143], v[204:207]\n"
    "v_mov_b32 v13, 154112\n"
    "v_mfma_f32_16x16x32_bf16 v[220:223], v[148:151], v[140:143], v[220:223]\n"
    "v_mfma_f32_16x16x32_bf16 v[220:223], v[180:183], v[172:175], v[220:223]\n"
    "v_max_u32_dpp v21, v21, v21 quad_perm:[1,0,3,2] row_mask:0xf bank_mask:0xf\n"
    "v_mfma_f32_16x16x32_bf16 v[216:219], v[180:183], v[168:171], v[216:219]\n"
    "v_mfma_f32_16x16x32_bf16 v[216:219], v[148:151], v[136:139], v[216:219]\n"
    "v_max_u32_dpp v21, v21, v21 quad_perm:[2,3,0,1] row_mask:0xf bank_mask:0xf\n"
    "v_mfma_f32_16x16x32_bf16 v[212:215], v[148:151], v[132:135], v[212:215]\n"
    "v_mfma_f32_16x16x32_bf16 v[212:215], v[180:183], v[164:167], v[212:215]\n"
    "v_max_u32_dpp v21, v21, v21 row_half_mirror row_mask:0xf bank_mask:0xf\n"
    "v_mfma_f32_16x16x32_bf16 v[208:211], v[180:183], v[160:163], v[208:211]\n"
    "v_mfma_f32_16x16x32_bf16 v[208:211], v[148:151], v[128:131], v[208:211]\n"
    "v_max_u32_dpp v21, v21, v21 row_mirror row_mask:0xf bank_mask:0xf\n"
    "v_mfma_f32_16x16x32_bf16 v[224:227], v[152:155], v[128:131], v[224:227]\n"
    "v_mfma_f32_16x16x32_bf16 v[224:227], v[184:187], v[160:163], v[224:227]\n"
    "v_max_u32_dpp v21, v21, v21 row_bcast:15 row_mask:0xa bank_mask:0xf\n"
    "v_mfma_f32_16x16x32_bf16 v[228:231], v[184:187], v[164:167], v[228:231]\n"
    "v_mfma_f32_16x16x32_bf16 v[228:231], v[152:155], v[132:135], v[228:231]\n"
    "v_max_u32_dpp v21, v21, v21 row_bcast:31 row_mask:0xc bank_mask:0xf\n"
    "v_mfma_f32_16x16x32_bf16 v[232:235], v[152:155], v[136:139], v[232:235]\n"
    "v_mfma_f32_16x16x32_bf16 v[232:235], v[184:187], v[168:171], v[232:235]\n"
    "v_readlane_b32 s47, v21, 63\n"
    "v_mfma_f32_16x16x32_bf16 v[236:239], v[184:187], v[172:175], v[236:239]\n"
    "v_mfma_f32_16x16x32_bf16 v[236:239], v[152:155], v[140:143], v[236:239]\n"
    "s_and_b32 s50, s47, 0x7f80\n"
    "v_mfma_f32_16x16x32_bf16 v[252:255], v[156:159], v[140:143], v[252:255]\n"
    "s_lshr_b32 s47, s50, 3\n"
    "v_mfma_f32_16x16x32_bf16 v[252:255], v[188:191], v[172:175], v[252:255]\n"
    "s_sub_u32 s50, s50, 2816\n"
    "v_mfma_f32_16x16x32_bf16 v[248:251], v[188:191], v[168:171], v[248:251]\n"
    "s_mul_i32 s51, s50, -8\n"
    "v_mfma_f32_16x16x32_bf16 v[248:251], v[156:159], v[136:139], v[248:251]\n"
    "v_mfma_f32_16x16x32_bf16 v[244:247], v[156:159], v[132:135], v[244:247]\n"
    "v_mfma_f32_16x16x32_bf16 v[244:247], v[188:191], v[164:167], v[244:247]\n"
    "v_mfma_f32_16x16x32_bf16 v[240:243], v[188:191], v[160:163], v[240:243]\n"
    "v_mfma_f32_16x16x32_bf16 v[240:243], v[156:159], v[128:131], v[240:243]\n"
    "s_waitcnt vmcnt(6)\n"
    "s_barrier\n"
    "s_add_u32 m0, s35, 49152\n"
    "s_nop 0\n"
    "global_load_lds_dwordx4 v46, s[30:31] offset:0\n"
    "ds_read_b128 v[128:131], v58 offset:0\n"
    "ds_read_b128 v[132:135], v58 offset:2048\n"
    "ds_read_b128 v[136:139], v58 offset:4096\n"
    "ds_read_b128 v[140:143], v58 offset:6144\n"
    "global_load_lds_dwordx4 v47, s[30:31] offset:1024\n"
    "ds_read_b64_tr_b16 v[144:145], v80 offset:0\n"
    "ds_read_b64_tr_b16 v[146:147], v80 offset:1024\n"
    "ds_read_b64_tr_b16 v[148:149], v81 offset:0\n"
    "ds_read_b64_tr_b16 v[150:151], v81 offset:1024\n"
    "s_add_u32 m0, s34, 49152\n"
    "s_nop 0\n"
    "global_load_lds_dwordx4 v52, s[36:37] offset:0\n"
    "ds_read_b64_tr_b16 v[152:153], v82 offset:0\n"
    "ds_read_b64_tr_b16 v[154:155], v82 offset:1024\n"
    "ds_read_b64_tr_b16 v[156:157], v83 offset:0\n"
    "ds_read_b64_tr_b16 v[158:159], v83 offset:1024\n"
    "global_load_lds_dwordx4 v53, s[36:37] offset:1024\n"
    "ds_read_b128 v[160:163], v59 offset:0\n"
    "ds_read_b128 v[164:167], v59 offset:2048\n"
    "ds_read_b128 v[168:171], v59 offset:4096\n"
    "ds_read_b128 v[172:175], v59 offset:6144\n"
    "global_load_lds_dwordx4 v54, s[36:37] offset:2048\n"
    "ds_read_b64_tr_b16 v[176:177], v80 offset:8192\n"
    "ds_read_b64_tr_b16 v[178:179], v80 offset:9216\n"
    "ds_read_b64_tr_b16 v[180:181], v81 offset:8192\n"
    "ds_read_b64_tr_b16 v[182:183], v81 offset:9216\n"
    "global_load_lds_dwordx4 v55, s[36:37] offset:3072\n"
    "ds_read_b64_tr_b16 v[184:185], v82 offset:8192\n"
    "ds_read_b64_tr_b16 v[186:187], v82 offset:9216\n"
    "ds_read_b64_tr_b16 v[188:189], v83 offset:8192\n"
    "ds_read_b64_tr_b16 v[190:191], v83 offset:9216\n"
    "s_waitcnt vmcnt(10) lgkmcnt(0)\n"
    "s_barrier\n"
    "v_mfma_f32_16x16x32_bf16 v[192:195], v[144:147], v[128:131], v[192:195]\n"
    "v_pk_maximum3_f16 v21, v28, v28, s50 op_sel_hi:[1,1,0] neg_lo:[0,1,0] neg_hi:[0,1,0]\n"
    "v_mfma_f32_16x16x32_bf16 v[192:195], v[176:179], v[160:163], v[192:195]\n"
    "v_pk_mad_u16 v21, v21, 8, s51 op_sel_hi:[1,0,0]\n"
    "v_mfma_f32_16x16x32_bf16 v[196:199], v[176:179], v[164:167], v[196:199]\n"
    "v_bfi_b32 v28, s57, v21, v28\n"
    "v_mfma_f32_16x16x32_bf16 v[196:199], v[144:147], v[132:135], v[196:199]\n"
    "v_mfma_f32_16x16x32_bf16 v[200:203], v[144:147], v[136:139], v[200:203]\n"
    "v_pk_maximum3_f16 v12, v29, v29, s50 op_sel_hi:[1,1,0] neg_lo:[0,1,0] neg_hi:[0,1,0]\n"
    "v_mfma_f32_16x16x32_bf16 v[200:203], v[176:179], v[168:171], v[200:203]\n"
    "v_pk_mad_u16 v12, v12, 8, s51 op_sel_hi:[1,0,0]\n"
    "v_mfma_f32_16x16x32_bf16 v[204:207], v[176:179], v[172:175], v[204:207]\n"
    "v_bfi_b32 v29, s57, v12, v29\n"
    "v_mfma_f32_16x16x32_bf16 v[204:207], v[144:147], v[140:143], v[204:207]\n"
    "v_mfma_f32_16x16x32_bf16 v[220:223], v[148:151], v[140:143], v[220:223]\n"
    "v_pk_maximum3_f16 v21, v30, v30, s50 op_sel_hi:[1,1,0] neg_lo:[0,1,0] neg_hi:[0,1,0]\n"
    "v_mfma_f32_16x16x32_bf16 v[220:223], v[180:183], v[172:175], v[220:223]\n"
    "v_pk_mad_u16 v21, v21, 8, s51 op_sel_hi:[1,0,0]\n"
    "v_mfma_f32_16x16x32_bf16 v[216:219], v[180:183], v[168:171], v[216:219]\n"
    "v_bfi_b32 v30, s57, v21, v30\n"
    "v_mfma_f32_16x16x32_bf16 v[216:219], v[148:151], v[136:139], v[216:219]\n"
    "v_mfma_f32_16x16x32_bf16 v[212:215], v[148:151], v[132:135], v[212:215]\n"
    "v_pk_maximum3_f16 v12, v31, v31, s50 op_sel_hi:[1,1,0] neg_lo:[0,1,0] neg_hi:[0,1,0]\n"
    "v_mfma_f32_16x16x32_bf16 v[212:215], v[180:183], v[164:167], v[212:215]\n"
    "v_pk_mad_u16 v12, v12, 8, s51 op_sel_hi:[1,0,0]\n"
    "v_mfma_f32_16x16x32_bf16 v[208:211], v[180:183], v[160:163], v[208:211]\n"
    "v_bfi_b32 v31, s57, v12, v31\n"
    "v_mfma_f32_16x16x32_bf16 v[208:211], v[148:151], v[128:131], v[208:211]\n"
    "v_mfma_f32_16x16x32_bf16 v[224:227], v[152:155], v[128:131], v[224:227]\n"
    "v_pk_maximum3_f16 v21, v32, v32, s50 op_sel_hi:[1,1,0] neg_lo:[0,1,0] neg_hi:[0,1,0]\n"
    "v_mfma_f32_16x16x32_bf16 v[224:227], v[184:187], v[160:163], v[224:227]\n"
    "v_pk_mad_u16 v21, v21, 8, s51 op_sel_hi:[1,0,0]\n"
    "v_mfma_f32_16x16x32_bf16 v[228:231], v[184:187], v[164:167], v[228:231]\n"
    "v_bfi_b32 v32, s57, v21, v32\n"
    "v_mfma_f32_16x16x32_bf16 v[228:231], v[152:155], v[132:135], v[228:231]\n"
    "v_mfma_f32_16x16x32_bf16 v[232:235], v[152:155], v[136:139], v[232:235]\n"
    "v_pk_maximum3_f16 v12, v33, v33, s50 op_sel_hi:[1,1,0] neg_lo:[0,1,0] neg_hi:[0,1,0]\n"
    "v_mfma_f32_16x16x32_bf16 v[232:235], v[184:187], v[168:171], v[232:235]\n"
    "v_pk_mad_u16 v12, v12, 8, s51 op_sel_hi:[1,0,0]\n"
    "v_mfma_f32_16x16x32_bf16 v[236:239], v[184:187], v[172:175], v[236:239]\n"
    "v_bfi_b32 v33, s57, v12, v33\n"
    "v_mfma_f32_16x16x32_bf16 v[236:239], v[152:155], v[140:143], v[236:239]\n"
    "v_mfma_f32_16x16x32_bf16 v[252:255], v[156:159], v[140:143], v[252:255]\n"
    "v_pk_maximum3_f16 v21, v34, v34, s50 op_sel_hi:[1,1,0] neg_lo:[0,1,0] neg_hi:[0,1,0]\n"
    "v_mfma_f32_16x16x32_bf16 v[252:255], v[188:191], v[172:175], v[252:255]\n"
    "v_pk_mad_u16 v21, v21, 8, s51 op_sel_hi:[1,0,0]\n"
    "v_mfma_f32_16x16x32_bf16 v[248:251], v[188:191], v[168:171], v[248:251]\n"
    "v_bfi_b32 v34, s57, v21, v34\n"
    "v_mfma_f32_16x16x32_bf16 v[248:251], v[156:159], v[136:139], v[248:251]\n"
    "v_mfma_f32_16x16x32_bf16 v[244:247], v[156:159], v[132:135], v[244:247]\n"
    "v_lshlrev_b32 v52, 4, v1\n"
    "v_mfma_f32_16x16x32_bf16 v[244:247], v[188:191], v[164:167], v[244:247]\n"
    "v_add_u32 v53, 4096, v52\n"
    "v_mfma_f32_16x16x32_bf16 v[240:243], v[188:191], v[160:163], v[240:243]\n"
    "v_mfma_f32_16x16x32_bf16 v[240:243], v[156:159], v[128:131], v[240:243]\n"
    "s_waitcnt vmcnt(6)\n"
    "s_barrier\n"
    "ds_read_b64 v[26:27], v13\n"
    "ds_read_b32 v0, v22 offset:16\n"
    "ds_read_b32 v23, v22 offset:24\n"
    "s_mul_i32 s42, s20, 5376\n"
    "s_add_u32 s40, s42, 1048576\n"
    "s_add_u32 s38, s16, s40\n"
    "s_addc_u32 s39, s17, 0\n"
    "s_add_u32 m0, s42, 98304\n"
    "s_nop 0\n"
    "global_load_lds_dwordx4 v52, s[38:39] offset:0 sc1\n"
    "ds_read_b128 v[128:131], v56 offset:0\n"
    "ds_read_b128 v[132:135], v56 offset:2048\n"
    "ds_read_b128 v[136:139], v56 offset:4096\n"
    "ds_read_b128 v[140:143], v56 offset:6144\n"
    "global_load_lds_dwordx4 v52, s[38:39] offset:1024 sc1\n"
    "ds_read_b64_tr_b16 v[144:145], v76 offset:0\n"
    "ds_read_b64_tr_b16 v[146:147], v76 offset:1024\n"
    "ds_read_b64_tr_b16 v[148:149], v77 offset:0\n"
    "ds_read_b64_tr_b16 v[150:151], v77 offset:1024\n"
    "global_load_lds_dwordx4 v52, s[38:39] offset:2048 sc1\n"
    "ds_read_b64_tr_b16 v[152:153], v78 offset:0\n"
    "ds_read_b64_tr_b16 v[154:155], v78 offset:1024\n"
    "ds_read_b64_tr_b16 v[156:157], v79 offset:0\n"
    "ds_read_b64_tr_b16 v[158:159], v79 offset:1024\n"
    "global_load_lds_dwordx4 v52, s[38:39] offset:3072 sc1\n"
    "ds_read_b128 v[160:163], v57 offset:0\n"
    "ds_read_b128 v[164:167], v57 offset:2048\n"
    "ds_read_b128 v[168:171], v57 offset:4096\n"
    "ds_read_b128 v[172:175], v57 offset:6144\n"
    "s_add_u32 m0, s42, 102400\n"
    "s_nop 0\n"
    "global_load_lds_dwordx4 v53, s[38:39] offset:0 sc1\n"
    "ds_read_b64_tr_b16 v[176:177], v76 offset:8192\n"
    "ds_read_b64_tr_b16 v[178:179], v76 offset:9216\n"
    "ds_read_b64_tr_b16 v[180:181], v77 offset:8192\n"
    "ds_read_b64_tr_b16 v[182:183], v77 offset:9216\n"
    "global_load_lds_dwordx4 v53, s[38:39] offset:256 sc1\n"
    "ds_read_b64_tr_b16 v[184:185], v78 offset:8192\n"
    "ds_read_b64_tr_b16 v[186:187], v78 offset:9216\n"
    "ds_read_b64_tr_b16 v[188:189], v79 offset:8192\n"
    "ds_read_b64_tr_b16 v[190:191], v79 offset:9216\n"
    "s_waitcnt vmcnt(10) lgkmcnt(0)\n"
    "s_barrier\n"
    "v_mfma_f32_16x16x32_bf16 v[192:195], v[144:147], v[128:131], v[192:195]\n"
    "v_pk_maximum3_f16 v12, v35, v35, s50 op_sel_hi:[1,1,0] neg_lo:[0,1,0] neg_hi:[0,1,0]\n"
    "v_mfma_f32_16x16x32_bf16 v[192:195], v[176:179], v[160:163], v[192:195]\n"
    "v_pk_mad_u16 v12, v12, 8, s51 op_sel_hi:[1,0,0]\n"
    "v_mfma_f32_16x16x32_bf16 v[196:199], v[176:179], v[164:167], v[196:199]\n"
    "v_bfi_b32 v35, s57, v12, v35\n"
    "v_mfma_f32_16x16x32_bf16 v[196:199], v[144:147], v[132:135], v[196:199]\n"
    "v_pk_maximum3_f16 v21, v36, v36, s50 op_sel_hi:[1,1,0] neg_lo:[0,1,0] neg_hi:[0,1,0]\n"
    "v_mfma_f32_16x16x32_bf16 v[200:203], v[144:147], v[136:139], v[200:203]\n"
    "v_pk_mad_u16 v21, v21, 8, s51 op_sel_hi:[1,0,0]\n"
    "v_mfma_f32_16x16x32_bf16 v[200:203], v[176:179], v[168:171], v[200:203]\n"
    "v_bfi_b32 v36, s57, v21, v36\n"
    "v_mfma_f32_16x16x32_bf16 v[204:207], v[176:179], v[172:175], v[204:207]\n"
    "v_pk_maximum3_f16 v12, v37, v37, s50 op_sel_hi:[1,1,0] neg_lo:[0,1,0] neg_hi:[0,1,0]\n"
    "v_mfma_f32_16x16x32_bf16 v[204:207], v[144:147], v[140:143], v[204:207]\n"
    "v_mfma_f32_16x16x32_bf16 v[220:223], v[148:151], v[140:143], v[220:223]\n"
    "v_pk_mad_u16 v12, v12, 8, s51 op_sel_hi:[1,0,0]\n"
    "v_mfma_f32_16x16x32_bf16 v[220:223], v[180:183], v[172:175], v[220:223]\n"
    "v_bfi_b32 v37, s57, v12, v37\n"
    "v_mfma_f32_16x16x32_bf16 v[216:219], v[180:183], v[168:171], v[216:219]\n"
    "v_pk_maximum3_f16 v21, v38, v38, s50 op_sel_hi:[1,1,0] neg_lo:[0,1,0] neg_hi:[0,1,0]\n"
    "v_mfma_f32_16x16x32_bf16 v[216:219], v[148:151], v[136:139], v[216:219]\n"
    "v_pk_mad_u16 v21, v21, 8, s51 op_sel_hi:[1,0,0]\n"
    "v_mfma_f32_16x16x32_bf16 v[212:215], v[148:151], v[132:135], v[212:215]\n"
    "v_bfi_b32 v38, s57, v21, v38\n"
    "v_mfma_f32_16x16x32_bf16 v[212:215], v[180:183], v[164:167], v[212:215]\n"
    "v_pk_maximum3_f16 v12, v39, v39, s50 op_sel_hi:[1,1,0] neg_lo:[0,1,0] neg_hi:[0,1,0]\n"
    "v_mfma_f32_16x16x32_bf16 v[208:211], v[180:183], v[160:163], v[208:211]\n"
    "v_pk_mad_u16 v12, v12, 8, s51 op_sel_hi:[1,0,0]\n"
    "v_mfma_f32_16x16x32_bf16 v[208:211], v[148:151], v[128:131], v[208:211]\n"
    "v_mfma_f32_16x16x32_bf16 v[224:227], v[152:155], v[128:131], v[224:227]\n"
    "v_bfi_b32 v39, s57, v12, v39\n"
    "v_mfma_f32_16x16x32_bf16 v[224:227], v[184:187], v[160:163], v[224:227]\n"
    "v_pk_maximum3_f16 v21, v40, v40, s50 op_sel_hi:[1,1,0] neg_lo:[0,1,0] neg_hi:[0,1,0]\n"
    "v_mfma_f32_16x16x32_bf16 v[228:231], v[184:187], v[164:167], v[228:231]\n"
    "v_pk_mad_u16 v21, v21, 8, s51 op_sel_hi:[1,0,0]\n"
    "v_mfma_f32_16x16x32_bf16 v[228:231], v[152:155], v[132:135], v[228:231]\n"
    "v_bfi_b32 v40, s57, v21, v40\n"
    "v_mfma_f32_16x16x32_bf16 v[232:235], v[152:155], v[136:139], v[232:235]\n"
    "s_sub_u32 s43, 4112, s47\n"
    "s_lshl_b32 s43, s43, 19\n"
    "v_cvt_pkrtz_f16_f32 v12, s43, s43\n"
    "v_mfma_f32_16x16x32_bf16 v[232:235], v[184:187], v[168:171], v[232:235]\n"
    "v_lshl_or_b32 v21, v12, 16, s47\n"
    "v_mfma_f32_16x16x32_bf16 v[236:239], v[184:187], v[172:175], v[236:239]\n"
    "s_getpc_b64 s[66:67]\n"
    ".Lxd_en6:\n"
    "v_mfma_f32_16x16x32_bf16 v[236:239], v[152:155], v[140:143], v[236:239]\n"
    "v_mfma_f32_16x16x32_bf16 v[252:255], v[156:159], v[140:143], v[252:255]\n"
    "s_mov_b32 s40, .Lxd_ph1-.Lxd_en6\n"
    "v_mfma_f32_16x16x32_bf16 v[252:255], v[188:191], v[172:175], v[252:255]\n"
    "s_cmp_eq_u32 s22, 1\n"
    "s_cselect_b32 s40, .Lxd_ph0-.Lxd_en6, s40\n"
    "v_mfma_f32_16x16x32_bf16 v[248:251], v[188:191], v[168:171], v[248:251]\n"
    "s_cmp_eq_u32 s22, 2\n"
    "s_cselect_b32 s40, .Lxd_pl3-.Lxd_en6, s40\n"
    "v_mfma_f32_16x16x32_bf16 v[248:251], v[156:159], v[136:139], v[248:251]\n"
    "s_cmp_eq_u32 s22, 3\n"
    "s_cselect_b32 s40, .Lxd_pl2-.Lxd_en6, s40\n"
    "v_mfma_f32_16x16x32_bf16 v[244:247], v[156:159], v[132:135], v[244:247]\n"
    "s_ashr_i32 s41, s40, 31\n"
    "v_mfma_f32_16x16x32_bf16 v[244:247], v[188:191], v[164:167], v[244:247]\n"
    "s_add_u32 s66, s66, s40\n"
    "s_addc_u32 s67, s67, s41\n"
    "v_mfma_f32_16x16x32_bf16 v[240:243], v[188:191], v[160:163], v[240:243]\n"
    "v_mfma_f32_16x16x32_bf16 v[240:243], v[156:159], v[128:131], v[240:243]\n"
    "s_waitcnt vmcnt(6)\n"
    "s_barrier\n"
    "ds_read_b128 v[128:131], v56 offset:49152\n"
    "ds_read_b128 v[132:135], v56 offset:51200\n"
    "ds_read_b128 v[136:139], v56 offset:53248\n"
    "ds_read_b128 v[140:143], v56 offset:55296\n"
    "ds_read_b64_tr_b16 v[144:145], v76 offset:49152\n"
    "ds_read_b64_tr_b16 v[146:147], v76 offset:50176\n"
    "ds_read_b64_tr_b16 v[148:149], v77 offset:49152\n"
    "ds_read_b64_tr_b16 v[150:151], v77 offset:50176\n"
    "ds_read_b64_tr_b16 v[152:153], v78 offset:49152\n"
    "ds_read_b64_tr_b16 v[154:155], v78 offset:50176\n"
    "ds_read_b64_tr_b16 v[156:157], v79 offset:49152\n"
    "ds_read_b64_tr_b16 v[158:159], v79 offset:50176\n"
    "ds_read_b128 v[160:163], v57 offset:49152\n"
    "ds_read_b128 v[164:167], v57 offset:51200\n"
    "ds_read_b128 v[168:171], v57 offset:53248\n"
    "ds_read_b128 v[172:175], v57 offset:55296\n"
    "ds_read_b64_tr_b16 v[176:177], v76 offset:57344\n"
    "ds_read_b64_tr_b16 v[178:179], v76 offset:58368\n"
    "ds_read_b64_tr_b16 v[180:181], v77 offset:57344\n"
    "ds_read_b64_tr_b16 v[182:183], v77 offset:58368\n"
    "ds_read_b64_tr_b16 v[184:185], v78 offset:57344\n"
    "ds_read_b64_tr_b16 v[186:187], v78 offset:58368\n"
    "ds_read_b64_tr_b16 v[188:189], v79 offset:57344\n"
    "ds_read_b64_tr_b16 v[190:191], v79 offset:58368\n"
    "s_waitcnt vmcnt(0) lgkmcnt(0)\n"
    "s_barrier\n"
    "s_setprio 3\n"
    "v_mfma_f32_16x16x32_bf16 v[192:195], v[144:147], v[128:131], v[192:195]\n"
    "v_add_u32 v42, 98304, v52\n"
    "v_mfma_f32_16x16x32_bf16 v[192:195], v[176:179], v[160:163], v[192:195]\n"
    "s_mov_b32 s64, 0xbdd2d3e8\n"
    "v_mfma_f32_16x16x32_bf16 v[196:199], v[176:179], v[164:167], v[196:199]\n"
    "s_mov_b32 s65, 0xc0135761\n"
    "v_mfma_f32_16x16x32_bf16 v[196:199], v[144:147], v[132:135], v[196:199]\n"
    "s_cmp_lt_u32 s22, 2\n"
    "v_mfma_f32_16x16x32_bf16 v[200:203], v[144:147], v[136:139], v[200:203]\n"
    "s_cselect_b32 s43, 16, 0\n"
    "v_mfma_f32_16x16x32_bf16 v[200:203], v[176:179], v[168:171], v[200:203]\n"
    "s_and_b32 s41, s20, 1\n"
    "v_mfma_f32_16x16x32_bf16 v[204:207], v[176:179], v[172:175], v[204:207]\n"
    "s_lshl1_add_u32 s40, s21, s41\n"
    "v_mfma_f32_16x16x32_bf16 v[204:207], v[144:147], v[140:143], v[204:207]\n"
    "v_mfma_f32_16x16x32_bf16 v[220:223], v[148:151], v[140:143], v[220:223]\n"
    "s_lshl_b32 s41, s40, 8\n"
    "v_mfma_f32_16x16x32_bf16 v[220:223], v[180:183], v[172:175], v[220:223]\n"
    "s_add_u32 s41, s41, 149504\n"
    "v_mfma_f32_16x16x32_bf16 v[216:219], v[180:183], v[168:171], v[216:219]\n"
    "v_lshl_add_u32 v11, v1, 2, s41\n"
    "v_mfma_f32_16x16x32_bf16 v[216:219], v[148:151], v[136:139], v[216:219]\n"
    "s_lshl_b32 s40, s40, 6\n"
    "v_mfma_f32_16x16x32_bf16 v[212:215], v[148:151], v[132:135], v[212:215]\n"
    "s_add_u32 s40, s40, s24\n"
    "v_mfma_f32_16x16x32_bf16 v[212:215], v[180:183], v[164:167], v[212:215]\n"
    "s_lshl_b32 s41, s23, 12\n"
    "v_mfma_f32_16x16x32_bf16 v[208:211], v[180:183], v[160:163], v[208:211]\n"
    "s_add_u32 s40, s40, s41\n"
    "v_mfma_f32_16x16x32_bf16 v[208:211], v[148:151], v[128:131], v[208:211]\n"
    "v_mfma_f32_16x16x32_bf16 v[224:227], v[152:155], v[128:131], v[224:227]\n"
    "s_lshl3_add_u32 s40, s40, 65536\n"
    "v_mfma_f32_16x16x32_bf16 v[224:227], v[184:187], v[160:163], v[224:227]\n"
    "s_add_u32 s34, s40, s16\n"
    "v_mfma_f32_16x16x32_bf16 v[228:231], v[184:187], v[164:167], v[228:231]\n"
    "s_addc_u32 s35, s17, 0\n"
    "v_mfma_f32_16x16x32_bf16 v[228:231], v[152:155], v[132:135], v[228:231]\n"
    "v_mov_b32 v13, s58\n"
    "v_mfma_f32_16x16x32_bf16 v[232:235], v[152:155], v[136:139], v[232:235]\n"
    "s_mov_b32 s36, 0\n"
    "v_mfma_f32_16x16x32_bf16 v[232:235], v[184:187], v[168:171], v[232:235]\n"
    "v_readfirstlane_b32 s28, v26\n"
    "v_mfma_f32_16x16x32_bf16 v[236:239], v[184:187], v[172:175], v[236:239]\n"
    "v_readfirstlane_b32 s29, v27\n"
    "v_mfma_f32_16x16x32_bf16 v[236:239], v[152:155], v[140:143], v[236:239]\n"
    "v_mfma_f32_16x16x32_bf16 v[252:255], v[156:159], v[140:143], v[252:255]\n"
    "s_sub_u32 s40, 1, s22\n"
    "v_mfma_f32_16x16x32_bf16 v[252:255], v[188:191], v[172:175], v[252:255]\n"
    "s_and_b32 s40, s40, 3\n"
    "v_mfma_f32_16x16x32_bf16 v[248:251], v[188:191], v[168:171], v[248:251]\n"
    "v_lshl_add_u32 v7, s40, 12, v3\n"
    "v_mfma_f32_16x16x32_bf16 v[248:251], v[156:159], v[136:139], v[248:251]\n"
    "v_lshl_add_u32 v8, s40, 12, v4\n"
    "v_mfma_f32_16x16x32_bf16 v[244:247], v[156:159], v[132:135], v[244:247]\n"
    "v_lshl_add_u32 v9, s40, 12, v5\n"
    "v_mfma_f32_16x16x32_bf16 v[244:247], v[188:191], v[164:167], v[244:247]\n"
    "v_lshl_add_u32 v10, s40, 12, v6\n"
    "v_mfma_f32_16x16x32_bf16 v[240:243], v[188:191], v[160:163], v[240:243]\n"
    "v_mfma_f32_16x16x32_bf16 v[240:243], v[156:159], v[128:131], v[240:243]\n"
    "s_cmp_lg_u32 s21, 0\n"
    "s_cbranch_scc1 .Lxd_g1\n"
    "s_barrier\n"
    ".Lxd_g1:\n"
    "s_setprio 2\n"
    "ds_write_b128 v15, v[28:31] offset:0\n"
    "ds_write_b128 v15, v[32:35] offset:144\n"
    "ds_write_b128 v15, v[36:39] offset:288\n"
    "ds_write_b32 v16, v40 offset:3456\n"
    "ds_write_b32 v16, v12 offset:3744\n"
    "s_mov_b64 exec, 1\n"
    "ds_or_b32 v25, v21 offset:532\n"
    "s_mov_b64 exec, -1\n"
    "s_load_dword s26, s[68:69], 0\n"
    "v_add_u32 v25, s43, v25\n"
    "ds_read_b128 v[92:95], v42 offset:0\n"
    "ds_read_b128 v[96:99], v42 offset:1024\n"
    "ds_read_b128 v[100:103], v42 offset:2048\n"
    "ds_read_b128 v[104:107], v42 offset:3072\n"
    "ds_read_b128 v[108:111], v42 offset:4096\n"
    "ds_read_b128 v[112:115], v42 offset:5120\n"
    "ds_read_b128 v[116:119], v42 offset:6144\n"
    "ds_read_b128 v[120:123], v42 offset:7168\n"
    "ds_read_b128 v[124:127], v42 offset:8192\n"
    "v_pk_mul_f32 v[76:77], v[192:193], v[192:193]\n"
    "v_pk_mul_f32 v[78:79], v[196:197], v[196:197]\n"
    "v_pk_mul_f32 v[80:81], v[200:201], v[200:201]\n"
    "v_pk_mul_f32 v[82:83], v[204:205], v[204:205]\n"
    "v_pk_fma_f32 v[76:77], v[76:77], s[64:65], s[64:65] op_sel:[0,0,1] op_sel_hi:[1,0,1]\n"
    "v_pk_fma_f32 v[78:79], v[78:79], s[64:65], s[64:65] op_sel:[0,0,1] op_sel_hi:[1,0,1]\n"
    "v_pk_fma_f32 v[80:81], v[80:81], s[64:65], s[64:65] op_sel:[0,0,1] op_sel_hi:[1,0,1]\n"
    "v_pk_fma_f32 v[82:83], v[82:83], s[64:65], s[64:65] op_sel:[0,0,1] op_sel_hi:[1,0,1]\n"
    "v_pk_mul_f32 v[76:77], v[76:77], v[192:193]\n"
    "v_pk_mul_f32 v[78:79], v[78:79], v[196:197]\n"
    "v_pk_mul_f32 v[80:81], v[80:81], v[200:201]\n"
    "v_pk_mul_f32 v[82:83], v[82:83], v[204:205]\n"
    "v_exp_f32 v76, v76\n"
    "v_exp_f32 v77, v77\n"
    "ds_read_b128 v[128:131], v42 offset:9216\n"
    "v_exp_f32 v78, v78\n"
    "v_exp_f32 v79, v79\n"
    "v_exp_f32 v80, v80\n"
    "ds_read_b128 v[132:135], v42 offset:10240\n"
    "v_exp_f32 v81, v81\n"
    "v_exp_f32 v82, v82\n"
    "v_exp_f32 v83, v83\n"
    "ds_read_b128 v[136:139], v42 offset:11264\n"
    "v_pk_mul_f32 v[192:193], v[192:193], v[60:61]\n"
    "v_pk_mul_f32 v[196:197], v[196:197], v[60:61]\n"
    "v_pk_mul_f32 v[200:201], v[200:201], v[60:61]\n"
    "v_pk_mul_f32 v[204:205], v[204:205], v[60:61]\n"
    "v_pk_add_f32 v[76:77], v[76:77], 1.0 op_sel_hi:[1,0]\n"
    "v_pk_add_f32 v[78:79], v[78:79], 1.0 op_sel_hi:[1,0]\n"
    "v_pk_add_f32 v[80:81], v[80:81], 1.0 op_sel_hi:[1,0]\n"
    "v_pk_add_f32 v[82:83], v[82:83], 1.0 op_sel_hi:[1,0]\n"
    "v_rcp_f32 v76, v76\n"
    "v_rcp_f32 v77, v77\n"
    "ds_read_b128 v[140:143], v42 offset:12288\n"
    "v_rcp_f32 v78, v78\n"
    "v_rcp_f32 v79, v79\n"
    "v_rcp_f32 v80, v80\n"
    "v_rcp_f32 v81, v81\n"
    "ds_read_b128 v[144:147], v42 offset:13312\n"
    "v_rcp_f32 v82, v82\n"
    "v_rcp_f32 v83, v83\n"
    "v_pk_mul_f32 v[84:85], v[192:193], v[76:77]\n"
    "v_pk_mul_f32 v[86:87], v[196:197], v[78:79]\n"
    "v_pk_mul_f32 v[88:89], v[200:201], v[80:81]\n"
    "v_pk_mul_f32 v[90:91], v[204:205], v[82:83]\n"
    "v_pk_mul_f32 v[76:77], v[194:195], v[194:195]\n"
    "v_pk_mul_f32 v[78:79], v[198:199], v[198:199]\n"
    "v_pk_mul_f32 v[80:81], v[202:203], v[202:203]\n"
    "v_pk_mul_f32 v[82:83], v[206:207], v[206:207]\n"
    "v_pk_fma_f32 v[76:77], v[76:77], s[64:65], s[64:65] op_sel:[0,0,1] op_sel_hi:[1,0,1]\n"
    "v_pk_fma_f32 v[78:79], v[78:79], s[64:65], s[64:65] op_sel:[0,0,1] op_sel_hi:[1,0,1]\n"
    "v_pk_fma_f32 v[80:81], v[80:81], s[64:65], s[64:65] op_sel:[0,0,1] op_sel_hi:[1,0,1]\n"
    "v_pk_fma_f32 v[82:83], v[82:83], s[64:65], s[64:65] op_sel:[0,0,1] op_sel_hi:[1,0,1]\n"
    "v_pk_mul_f32 v[76:77], v[76:77], v[194:195]\n"
    "v_pk_mul_f32 v[78:79], v[78:79], v[198:199]\n"
    "v_pk_mul_f32 v[80:81], v[80:81], v[202:203]\n"
    "v_pk_mul_f32 v[82:83], v[82:83], v[206:207]\n"
    "v_exp_f32 v76, v76\n"
    "v_exp_f32 v77, v77\n"
    "ds_read_b128 v[148:151], v42 offset:14336\n"
    "v_exp_f32 v78, v78\n"
    "v_exp_f32 v79, v79\n"
    "v_exp_f32 v80, v80\n"
    "ds_read_b128 v[152:155], v42 offset:15360\n"
    "v_exp_f32 v81, v81\n"
    "v_exp_f32 v82, v82\n"
    "v_exp_f32 v83, v83\n"
    "ds_read_b128 v[156:159], v42 offset:16384\n"
    "v_pk_mul_f32 v[194:195], v[194:195], v[62:63]\n"
    "v_pk_mul_f32 v[198:199], v[198:199], v[62:63]\n"
    "v_pk_mul_f32 v[202:203], v[202:203], v[62:63]\n"
    "v_pk_mul_f32 v[206:207], v[206:207], v[62:63]\n"
    "v_pk_add_f32 v[76:77], v[76:77], 1.0 op_sel_hi:[1,0]\n"
    "v_pk_add_f32 v[78:79], v[78:79], 1.0 op_sel_hi:[1,0]\n"
    "v_pk_add_f32 v[80:81], v[80:81], 1.0 op_sel_hi:[1,0]\n"
    "v_pk_add_f32 v[82:83], v[82:83], 1.0 op_sel_hi:[1,0]\n"
    "v_rcp_f32 v76, v76\n"
    "v_rcp_f32 v77, v77\n"
    "ds_read_b128 v[160:163], v42 offset:17408\n"
    "v_rcp_f32 v78, v78\n"
    "v_rcp_f32 v79, v79\n"
    "v_rcp_f32 v80, v80\n"
    "v_rcp_f32 v81, v81\n"
    "ds_read_b128 v[164:167], v42 offset:18432\n"
    "v_rcp_f32 v82, v82\n"
    "v_rcp_f32 v83, v83\n"
    "v_pk_fma_f32 v[84:85], v[194:195], v[76:77], v[84:85]\n"
    "v_pk_fma_f32 v[86:87], v[198:199], v[78:79], v[86:87]\n"
    "v_pk_fma_f32 v[88:89], v[202:203], v[80:81], v[88:89]\n"
    "v_pk_fma_f32 v[90:91], v[206:207], v[82:83], v[90:91]\n"
    "ds_read_b32 v22, v13 offset:512\n"
    "v_pk_mul_f32 v[76:77], v[208:209], v[208:209]\n"
    "v_pk_mul_f32 v[78:79], v[212:213], v[212:213]\n"
    "v_pk_mul_f32 v[80:81], v[216:217], v[216:217]\n"
    "v_pk_mul_f32 v[82:83], v[220:221], v[220:221]\n"
    "v_pk_fma_f32 v[76:77], v[76:77], s[64:65], s[64:65] op_sel:[0,0,1] op_sel_hi:[1,0,1]\n"
    "v_pk_fma_f32 v[78:79], v[78:79], s[64:65], s[64:65] op_sel:[0,0,1] op_sel_hi:[1,0,1]\n"
    "v_pk_fma_f32 v[80:81], v[80:81], s[64:65], s[64:65] op_sel:[0,0,1] op_sel_hi:[1,0,1]\n"
    "v_pk_fma_f32 v[82:83], v[82:83], s[64:65], s[64:65] op_sel:[0,0,1] op_sel_hi:[1,0,1]\n"
    "v_pk_mul_f32 v[76:77], v[76:77], v[208:209]\n"
    "v_pk_mul_f32 v[78:79], v[78:79], v[212:213]\n"
    "v_pk_mul_f32 v[80:81], v[80:81], v[216:217]\n"
    "v_pk_mul_f32 v[82:83], v[82:83], v[220:221]\n"
    "v_exp_f32 v76, v76\n"
    "v_exp_f32 v77, v77\n"
    "ds_read_b128 v[168:171], v42 offset:19456\n"
    "v_exp_f32 v78, v78\n"
    "v_exp_f32 v79, v79\n"
    "v_exp_f32 v80, v80\n"
    "ds_read_b128 v[172:175], v42 offset:20480\n"
    "v_exp_f32 v81, v81\n"
    "v_exp_f32 v82, v82\n"
    "v_exp_f32 v83, v83\n"
    "ds_read_b128 v[176:179], v42 offset:21504\n"
    "v_pk_mul_f32 v[208:209], v[208:209], v[64:65]\n"
    "v_pk_mul_f32 v[212:213], v[212:213], v[64:65]\n"
    "v_pk_mul_f32 v[216:217], v[216:217], v[64:65]\n"
    "v_pk_mul_f32 v[220:221], v[220:221], v[64:65]\n"
    "v_pk_add_f32 v[76:77], v[76:77], 1.0 op_sel_hi:[1,0]\n"
    "v_pk_add_f32 v[78:79], v[78:79], 1.0 op_sel_hi:[1,0]\n"
    "v_pk_add_f32 v[80:81], v[80:81], 1.0 op_sel_hi:[1,0]\n"
    "v_pk_add_f32 v[82:83], v[82:83], 1.0 op_sel_hi:[1,0]\n"
    "v_rcp_f32 v76, v76\n"
    "v_rcp_f32 v77, v77\n"
    "ds_read_b128 v[180:183], v42 offset:22528\n"
    "v_rcp_f32 v78, v78\n"
    "v_rcp_f32 v79, v79\n"
    "v_rcp_f32 v80, v80\n"
    "v_rcp_f32 v81, v81\n"
    "ds_read_b128 v[184:187], v42 offset:23552\n"
    "v_rcp_f32 v82, v82\n"
    "v_rcp_f32 v83, v83\n"
    "v_pk_fma_f32 v[84:85], v[208:209], v[76:77], v[84:85]\n"
    "v_pk_fma_f32 v[86:87], v[212:213], v[78:79], v[86:87]\n"
    "v_pk_fma_f32 v[88:89], v[216:217], v[80:81], v[88:89]\n"
    "v_pk_fma_f32 v[90:91], v[220:221], v[82:83], v[90:91]\n"
    "s_waitcnt lgkmcnt(5)\n"
    "v_readfirstlane_b32 s45, v22\n"
    "s_cmp_lt_i32 s45, 16\n"
    "s_cbranch_scc1 .Lxd_ck7\n"
    ".Lxd_ck7r:\n"
    "ds_read_b64_tr_b16 v[44:45], v9 offset:0\n"
    "ds_read_b64_tr_b16 v[46:47], v10 offset:0\n"
    "ds_read_b64_tr_b16 v[48:49], v7 offset:0\n"
    "ds_read_b64_tr_b16 v[50:51], v8 offset:576\n"
    "ds_read_b64_tr_b16 v[52:53], v8 offset:1152\n"
    "ds_read_b64_tr_b16 v[54:55], v8 offset:1728\n"
    "ds_read_b64_tr_b16 v[56:57], v8 offset:2304\n"
    "ds_read_b64_tr_b16 v[58:59], v8 offset:2880\n"
    "s_cmp_eq_u32 s22, 0\n"
    "s_cbranch_scc1 .Lxd_on8\n"
    ".Lxd_on8r:\n"
    "s_setprio 1\n"
    "v_pk_mul_f32 v[76:77], v[210:211], v[210:211]\n"
    "v_pk_mul_f32 v[78:79], v[214:215], v[214:215]\n"
    "v_pk_mul_f32 v[80:81], v[218:219], v[218:219]\n"
    "v_pk_mul_f32 v[82:83], v[222:223], v[222:223]\n"
    "v_pk_fma_f32 v[76:77], v[76:77], s[64:65], s[64:65] op_sel:[0,0,1] op_sel_hi:[1,0,1]\n"
    "v_pk_fma_f32 v[78:79], v[78:79], s[64:65], s[64:65] op_sel:[0,0,1] op_sel_hi:[1,0,1]\n"
    "v_pk_fma_f32 v[80:81], v[80:81], s[64:65], s[64:65] op_sel:[0,0,1] op_sel_hi:[1,0,1]\n"
    "v_pk_fma_f32 v[82:83], v[82:83], s[64:65], s[64:65] op_sel:[0,0,1] op_sel_hi:[1,0,1]\n"
    "v_pk_mul_f32 v[76:77], v[76:77], v[210:211]\n"
    "v_pk_mul_f32 v[78:79], v[78:79], v[214:215]\n"
    "v_pk_mul_f32 v[80:81], v[80:81], v[218:219]\n"
    "v_pk_mul_f32 v[82:83], v[82:83], v[222:223]\n"
    "v_exp_f32 v76, v76\n"
    "v_exp_f32 v77, v77\n"
    "ds_read_b128 v[188:191], v42 offset:24576\n"
    "v_exp_f32 v78, v78\n"
    "v_exp_f32 v79, v79\n"
    "v_exp_f32 v80, v80\n"
    "ds_read_b128 v[192:195], v42 offset:25600\n"
    "v_exp_f32 v81, v81\n"
    "v_exp_f32 v82, v82\n"
    "v_exp_f32 v83, v83\n"
    "ds_read_b128 v[196:199], v42 offset:26624\n"
    "v_pk_mul_f32 v[210:211], v[210:211], v[66:67]\n"
    "v_pk_mul_f32 v[214:215], v[214:215], v[66:67]\n"
    "v_pk_mul_f32 v[218:219], v[218:219], v[66:67]\n"
    "v_pk_mul_f32 v[222:223], v[222:223], v[66:67]\n"
    "v_pk_add_f32 v[76:77], v[76:77], 1.0 op_sel_hi:[1,0]\n"
    "v_pk_add_f32 v[78:79], v[78:79], 1.0 op_sel_hi:[1,0]\n"
    "v_pk_add_f32 v[80:81], v[80:81], 1.0 op_sel_hi:[1,0]\n"
    "v_pk_add_f32 v[82:83], v[82:83], 1.0 op_sel_hi:[1,0]\n"
    "v_rcp_f32 v76, v76\n"
    "v_rcp_f32 v77, v77\n"
    "ds_read_b128 v[200:203], v42 offset:27648\n"
    "v_rcp_f32 v78, v78\n"
    "v_rcp_f32 v79, v79\n"
    "v_rcp_f32 v80, v80\n"
    "v_rcp_f32 v81, v81\n"
    "ds_read_b128 v[204:207], v42 offset:28672\n"
    "v_rcp_f32 v82, v82\n"
    "v_rcp_f32 v83, v83\n"
    "v_pk_fma_f32 v[84:85], v[210:211], v[76:77], v[84:85]\n"
    "v_pk_fma_f32 v[86:87], v[214:215], v[78:79], v[86:87]\n"
    "v_pk_fma_f32 v[88:89], v[218:219], v[80:81], v[88:89]\n"
    "v_pk_fma_f32 v[90:91], v[222:223], v[82:83], v[90:91]\n"
    "v_pk_mul_f32 v[76:77], v[224:225], v[224:225]\n"
    "v_pk_mul_f32 v[78:79], v[228:229], v[228:229]\n"
    "v_pk_mul_f32 v[80:81], v[232:233], v[232:233]\n"
    "v_pk_mul_f32 v[82:83], v[236:237], v[236:237]\n"
    "v_pk_fma_f32 v[76:77], v[76:77], s[64:65], s[64:65] op_sel:[0,0,1] op_sel_hi:[1,0,1]\n"
    "v_pk_fma_f32 v[78:79], v[78:79], s[64:65], s[64:65] op_sel:[0,0,1] op_sel_hi:[1,0,1]\n"
    "v_pk_fma_f32 v[80:81], v[80:81], s[64:65], s[64:65] op_sel:[0,0,1] op_sel_hi:[1,0,1]\n"
    "v_pk_fma_f32 v[82:83], v[82:83], s[64:65], s[64:65] op_sel:[0,0,1] op_sel_hi:[1,0,1]\n"
    "v_pk_mul_f32 v[76:77], v[76:77], v[224:225]\n"
    "v_pk_mul_f32 v[78:79], v[78:79], v[228:229]\n"
    "v_pk_mul_f32 v[80:81], v[80:81], v[232:233]\n"
    "v_pk_mul_f32 v[82:83], v[82:83], v[236:237]\n"
    "v_exp_f32 v76, v76\n"
    "v_exp_f32 v77, v77\n"
    "ds_read_b128 v[208:211], v42 offset:29696\n"
    "v_exp_f32 v78, v78\n"
    "v_exp_f32 v79, v79\n"
    "v_exp_f32 v80, v80\n"
    "ds_read_b128 v[212:215], v42 offset:30720\n"
    "v_exp_f32 v81, v81\n"
    "v_exp_f32 v82, v82\n"
    "v_exp_f32 v83, v83\n"
    "ds_read_b128 v[216:219], v42 offset:31744\n"
    "v_pk_mul_f32 v[224:225], v[224:225], v[68:69]\n"
    "v_pk_mul_f32 v[228:229], v[228:229], v[68:69]\n"
    "v_pk_mul_f32 v[232:233], v[232:233], v[68:69]\n"
    "v_pk_mul_f32 v[236:237], v[236:237], v[68:69]\n"
    "v_pk_add_f32 v[76:77], v[76:77], 1.0 op_sel_hi:[1,0]\n"
    "v_pk_add_f32 v[78:79], v[78:79], 1.0 op_sel_hi:[1,0]\n"
    "v_pk_add_f32 v[80:81], v[80:81], 1.0 op_sel_hi:[1,0]\n"
    "v_pk_add_f32 v[82:83], v[82:83], 1.0 op_sel_hi:[1,0]\n"
    "v_rcp_f32 v76, v76\n"
    "v_rcp_f32 v77, v77\n"
    "ds_read_b128 v[220:223], v42 offset:32768\n"
    "v_rcp_f32 v78, v78\n"
    "v_rcp_f32 v79, v79\n"
    "v_rcp_f32 v80, v80\n"
    "v_rcp_f32 v81, v81\n"
    "v_rcp_f32 v82, v82\n"
    "v_rcp_f32 v83, v83\n"
    "v_pk_fma_f32 v[84:85], v[224:225], v[76:77], v[84:85]\n"
    "v_pk_fma_f32 v[86:87], v[228:229], v[78:79], v[86:87]\n"
    "v_pk_fma_f32 v[88:89], v[232:233], v[80:81], v[88:89]\n"
    "v_pk_fma_f32 v[90:91], v[236:237], v[82:83], v[90:91]\n"
    "v_pk_mul_f32 v[76:77], v[226:227], v[226:227]\n"
    "v_pk_mul_f32 v[78:79], v[230:231], v[230:231]\n"
    "v_pk_mul_f32 v[80:81], v[234:235], v[234:235]\n"
    "v_pk_mul_f32 v[82:83], v[238:239], v[238:239]\n"
    "v_pk_fma_f32 v[76:77], v[76:77], s[64:65], s[64:65] op_sel:[0,0,1] op_sel_hi:[1,0,1]\n"
    "v_pk_fma_f32 v[78:79], v[78:79], s[64:65], s[64:65] op_sel:[0,0,1] op_sel_hi:[1,0,1]\n"
    "v_pk_fma_f32 v[80:81], v[80:81], s[64:65], s[64:65] op_sel:[0,0,1] op_sel_hi:[1,0,1]\n"
    "v_pk_fma_f32 v[82:83], v[82:83], s[64:65], s[64:65] op_sel:[0,0,1] op_sel_hi:[1,0,1]\n"
    "v_pk_mul_f32 v[76:77], v[76:77], v[226:227]\n"
    "v_pk_mul_f32 v[78:79], v[78:79], v[230:231]\n"
    "v_pk_mul_f32 v[80:81], v[80:81], v[234:235]\n"
    "v_pk_mul_f32 v[82:83], v[82:83], v[238:239]\n"
    "v_exp_f32 v76, v76\n"
    "v_exp_f32 v77, v77\n"
    "v_exp_f32 v78, v78\n"
    "v_exp_f32 v79, v79\n"
    "v_exp_f32 v80, v80\n"
    "v_exp_f32 v81, v81\n"
    "v_exp_f32 v82, v82\n"
    "v_exp_f32 v83, v83\n"
    "v_pk_mul_f32 v[226:227], v[226:227], v[70:71]\n"
    "v_pk_mul_f32 v[230:231], v[230:231], v[70:71]\n"
    "v_pk_mul_f32 v[234:235], v[234:235], v[70:71]\n"
    "v_pk_mul_f32 v[238:239], v[238:239], v[70:71]\n"
    "v_pk_add_f32 v[76:77], v[76:77], 1.0 op_sel_hi:[1,0]\n"
    "v_pk_add_f32 v[78:79], v[78:79], 1.0 op_sel_hi:[1,0]\n"
    "v_pk_add_f32 v[80:81], v[80:81], 1.0 op_sel_hi:[1,0]\n"
    "v_pk_add_f32 v[82:83], v[82:83], 1.0 op_sel_hi:[1,0]\n"
    "v_rcp_f32 v76, v76\n"
    "v_rcp_f32 v77, v77\n"
    "v_rcp_f32 v78, v78\n"
    "v_rcp_f32 v79, v79\n"
    "v_rcp_f32 v80, v80\n"
    "v_rcp_f32 v81, v81\n"
    "v_rcp_f32 v82, v82\n"
    "v_rcp_f32 v83, v83\n"
    "v_pk_fma_f32 v[84:85], v[226:227], v[76:77], v[84:85]\n"
    "v_pk_fma_f32 v[86:87], v[230:231], v[78:79], v[86:87]\n"
    "v_pk_fma_f32 v[88:89], v[234:235], v[80:81], v[88:89]\n"
    "v_pk_fma_f32 v[90:91], v[238:239], v[82:83], v[90:91]\n"
    "s_cmp_eq_u32 s22, 0\n"
    "s_cbranch_scc1 .Lxd_on10\n"
    ".Lxd_on10r:\n"
    "s_cmp_eq_u32 s22, 1\n"
    "s_cbranch_scc1 .Lxd_on11\n"
    ".Lxd_on11r:\n"
    "s_setprio 0\n"
    "v_pk_mul_f32 v[76:77], v[240:241], v[240:241]\n"
    "v_pk_mul_f32 v[78:79], v[244:245], v[244:245]\n"
    "v_pk_mul_f32 v[80:81], v[248:249], v[248:249]\n"
    "v_pk_mul_f32 v[82:83], v[252:253], v[252:253]\n"
    "v_pk_fma_f32 v[76:77], v[76:77], s[64:65], s[64:65] op_sel:[0,0,1] op_sel_hi:[1,0,1]\n"
    "v_pk_fma_f32 v[78:79], v[78:79], s[64:65], s[64:65] op_sel:[0,0,1] op_sel_hi:[1,0,1]\n"
    "v_pk_fma_f32 v[80:81], v[80:81], s[64:65], s[64:65] op_sel:[0,0,1] op_sel_hi:[1,0,1]\n"
    "v_pk_fma_f32 v[82:83], v[82:83], s[64:65], s[64:65] op_sel:[0,0,1] op_sel_hi:[1,0,1]\n"
    "v_pk_mul_f32 v[76:77], v[76:77], v[240:241]\n"
    "v_pk_mul_f32 v[78:79], v[78:79], v[244:245]\n"
    "v_pk_mul_f32 v[80:81], v[80:81], v[248:249]\n"
    "v_pk_mul_f32 v[82:83], v[82:83], v[252:253]\n"
    "v_exp_f32 v76, v76\n"
    "v_exp_f32 v77, v77\n"
    "ds_read_b128 v[224:227], v42 offset:33792\n"
    "v_exp_f32 v78, v78\n"
    "v_exp_f32 v79, v79\n"
    "v_exp_f32 v80, v80\n"
    "ds_read_b128 v[228:231], v42 offset:34816\n"
    "v_exp_f32 v81, v81\n"
    "v_exp_f32 v82, v82\n"
    "v_exp_f32 v83, v83\n"
    "ds_read_b128 v[232:235], v42 offset:35840\n"
    "v_pk_mul_f32 v[240:241], v[240:241], v[72:73]\n"
    "v_pk_mul_f32 v[244:245], v[244:245], v[72:73]\n"
    "v_pk_mul_f32 v[248:249], v[248:249], v[72:73]\n"
    "v_pk_mul_f32 v[252:253], v[252:253], v[72:73]\n"
    "v_pk_add_f32 v[76:77], v[76:77], 1.0 op_sel_hi:[1,0]\n"
    "v_pk_add_f32 v[78:79], v[78:79], 1.0 op_sel_hi:[1,0]\n"
    "v_pk_add_f32 v[80:81], v[80:81], 1.0 op_sel_hi:[1,0]\n"
    "v_pk_add_f32 v[82:83], v[82:83], 1.0 op_sel_hi:[1,0]\n"
    "v_rcp_f32 v76, v76\n"
    "v_rcp_f32 v77, v77\n"
    "ds_read_b128 v[236:239], v42 offset:36864\n"
    "v_rcp_f32 v78, v78\n"
    "v_rcp_f32 v79, v79\n"
    "v_rcp_f32 v80, v80\n"
    "v_rcp_f32 v81, v81\n"
    "v_rcp_f32 v82, v82\n"
    "v_rcp_f32 v83, v83\n"
    "v_pk_fma_f32 v[84:85], v[240:241], v[76:77], v[84:85]\n"
    "v_pk_fma_f32 v[86:87], v[244:245], v[78:79], v[86:87]\n"
    "v_pk_fma_f32 v[88:89], v[248:249], v[80:81], v[88:89]\n"
    "v_pk_fma_f32 v[90:91], v[252:253], v[82:83], v[90:91]\n"
    "v_pk_mul_f32 v[76:77], v[242:243], v[242:243]\n"
    "v_pk_mul_f32 v[78:79], v[246:247], v[246:247]\n"
    "v_pk_mul_f32 v[80:81], v[250:251], v[250:251]\n"
    "v_pk_mul_f32 v[82:83], v[254:255], v[254:255]\n"
    "v_pk_fma_f32 v[76:77], v[76:77], s[64:65], s[64:65] op_sel:[0,0,1] op_sel_hi:[1,0,1]\n"
    "v_pk_fma_f32 v[78:79], v[78:79], s[64:65], s[64:65] op_sel:[0,0,1] op_sel_hi:[1,0,1]\n"
    "v_pk_fma_f32 v[80:81], v[80:81], s[64:65], s[64:65] op_sel:[0,0,1] op_sel_hi:[1,0,1]\n"
    "v_pk_fma_f32 v[82:83], v[82:83], s[64:65], s[64:65] op_sel:[0,0,1] op_sel_hi:[1,0,1]\n"
    "v_pk_mul_f32 v[76:77], v[76:77], v[242:243]\n"
    "v_pk_mul_f32 v[78:79], v[78:79], v[246:247]\n"
    "v_pk_mul_f32 v[80:81], v[80:81], v[250:251]\n"
    "v_pk_mul_f32 v[82:83], v[82:83], v[254:255]\n"
    "v_exp_f32 v76, v76\n"
    "v_exp_f32 v77, v77\n"
    "v_exp_f32 v78, v78\n"
    "v_exp_f32 v79, v79\n"
    "v_exp_f32 v80, v80\n"
    "v_exp_f32 v81, v81\n"
    "v_exp_f32 v82, v82\n"
    "v_exp_f32 v83, v83\n"
    "v_pk_mul_f32 v[242:243], v[242:243], v[74:75]\n"
    "v_pk_mul_f32 v[246:247], v[246:247], v[74:75]\n"
    "v_pk_mul_f32 v[250:251], v[250:251], v[74:75]\n"
    "v_pk_mul_f32 v[254:255], v[254:255], v[74:75]\n"
    "v_pk_add_f32 v[76:77], v[76:77], 1.0 op_sel_hi:[1,0]\n"
    "v_pk_add_f32 v[78:79], v[78:79], 1.0 op_sel_hi:[1,0]\n"
    "v_pk_add_f32 v[80:81], v[80:81], 1.0 op_sel_hi:[1,0]\n"
    "v_pk_add_f32 v[82:83], v[82:83], 1.0 op_sel_hi:[1,0]\n"
    "v_rcp_f32 v76, v76\n"
    "v_rcp_f32 v77, v77\n"
    "v_rcp_f32 v78, v78\n"
    "v_rcp_f32 v79, v79\n"
    "v_rcp_f32 v80, v80\n"
    "v_rcp_f32 v81, v81\n"
    "v_rcp_f32 v82, v82\n"
    "v_rcp_f32 v83, v83\n"
    "v_pk_fma_f32 v[84:85], v[242:243], v[76:77], v[84:85]\n"
    "v_pk_fma_f32 v[86:87], v[246:247], v[78:79], v[86:87]\n"
    "v_pk_fma_f32 v[88:89], v[250:251], v[80:81], v[88:89]\n"
    "v_pk_fma_f32 v[90:91], v[254:255], v[82:83], v[90:91]\n"
    "ds_read_b128 v[240:243], v42 offset:37888\n"
    "ds_read_b128 v[244:247], v42 offset:38912\n"
    "ds_read_b128 v[248:251], v42 offset:39936\n"
    "ds_read_b128 v[252:255], v42 offset:40960\n"
    "s_cmp_eq_u32 s21, 0\n"
    "s_cbranch_scc1 .Lxd_gb0\n"
    "s_barrier\n"
    "s_setprio 3\n"
    ".Lxd_gb0:\n"
    "v_add_f32 v84, v84, v85\n"
    "v_add_f32 v86, v86, v87\n"
    "v_add_f32 v88, v88, v89\n"
    "v_add_f32 v90, v90, v91\n"
    "s_waitcnt lgkmcnt(15)\n"
    "v_pk_mul_f16 v60, v44, s45 op_sel:[0,1]\n"
    "v_pk_mul_f16 v61, v45, s45 op_sel:[0,1]\n"
    "v_permlane32_swap_b32 v84, v88\n"
    "v_permlane32_swap_b32 v86, v90\n"
    "v_add_f32 v84, v84, v88\n"
    "v_add_f32 v86, v86, v90\n"
    "v_pk_mul_f16 v62, v46, s45 op_sel:[0,1]\n"
    "v_pk_mul_f16 v63, v47, s45 op_sel:[0,1]\n"
    "v_permlane16_swap_b32 v84, v86\n"
    "v_add_f32 v84, v84, v86\n"
    "v_and_b32 v84, -2, v84\n"
    "ds_wrxchg_rtn_b32 v85, v11, v84\n"
    "v_pk_mul_f16 v64, v44, v46\n"
    "v_pk_mul_f16 v65, v45, v47\n"
    "v_pk_mul_f16 v66, v48, v48 op_sel_hi:[0,1]\n"
    "v_pk_mul_f16 v67, v48, v49 op_sel_hi:[0,1]\n"
    "v_lshlrev_b32 v86, 3, v1\n"
    "v_mov_b64 v[88:89], 0\n"
    "v_mov_b64 v[90:91], 0\n"
    "s_waitcnt lgkmcnt(0)\n"
    "v_cmp_ne_u32 vcc, -1, v85\n"
    "v_add_f32 v84, v84, v85\n"
    "v_add_u32 v84, 0x80, v84\n"
    "v_and_b32 v84, 0xffffff00, v84\n"
    "v_or_b32 v26, v84, v26\n"
    "s_nop 0\n"
    "s_cbranch_vccz .Lxd_nost\n"
    "s_mov_b64 exec, vcc\n"
    "global_store_dwordx2 v86, v[26:27], s[34:35] sc1\n"
    "s_mov_b64 exec, -1\n"
    ".Lxd_nost:\n"
    "s_cmp_lg_u32 s21, 0\n"
    "s_cbranch_scc1 .Lxd_gb1\n"
    "s_barrier\n"
    ".Lxd_gb1:\n"
    ".Lxd_enter:\n"
    "s_cmp_eq_u32 s21, 0\n"
    "s_cselect_b64 vcc, 0, -1\n"
    "s_cbranch_scc1 .Lxd_pr\n"
    "s_setprio 1\n"
    ".Lxd_pr:\n"
    "s_cmp_lg_u32 s36, 0\n"
    "s_cbranch_scc1 .Lxd_pl1\n"
    "s_setpc_b64 s[66:67]\n"
    ".Lxd_ph0:\n"
    "s_cbranch_vccnz .Lxd_pr13\n"
    "s_setprio 2\n"
    ".Lxd_pr13:\n"
    "v_smfmac_f32_16x16x64_f16 v[88:91], v[112:115], v[60:67], v2\n"
    "v_fmac_f32 v11, v76, v80\n"
    "v_fmac_f32 v11, v77, v81\n"
    "v_fmac_f32 v11, v78, v82\n"
    "v_fmac_f32 v11, v79, v83\n"
    "v_mfma_f32_16x16x32_f16 v[80:83], v[92:95], v[60:63], 0\n"
    "v_pk_mul_f16 v68, v58, v58 op_sel_hi:[0,1]\n"
    "v_pk_mul_f16 v69, v58, v49 op_sel_hi:[0,1]\n"
    "v_mfma_f32_16x16x32_f16 v[80:83], v[104:107], v[64:67], v[80:83]\n"
    "v_pk_mul_f16 v70, v48, v58 op_sel_hi:[0,1]\n"
    "v_pk_mul_f16 v71, v48, v59 op_sel_hi:[0,1]\n"
    "v_mfma_f32_16x16x32_f16 v[76:79], v[100:103], v[64:67], 0\n"
    "v_fmac_f32 v11, v84, v85\n"
    "v_fma_f32 v10, v86, v87, v11\n"
    "v_mfma_f32_16x16x32_f16 v[84:87], v[108:111], v[64:67], 0\n"
    "v_pk_mul_f16 v72, v50, v58 op_sel_hi:[0,1]\n"
    "v_pk_mul_f16 v73, v50, v59 op_sel_hi:[0,1]\n"
    "v_mfma_f32_16x16x32_f16 v[84:87], v[96:99], v[60:63], v[84:87]\n"
    "v_pk_mul_f16 v74, v52, v58 op_sel_hi:[0,1]\n"
    "v_pk_mul_f16 v75, v52, v59 op_sel_hi:[0,1]\n"
    "s_waitcnt vmcnt(0)\n"
    "v_mfma_f32_16x16x32_f16 v[76:79], v[116:119], v[68:71], v[76:79]\n"
    "v_pk_mul_f16 v60, v54, v58 op_sel_hi:[0,1]\n"
    "v_pk_mul_f16 v61, v54, v59 op_sel_hi:[0,1]\n"
    "v_mfma_f32_16x16x32_f16 v[76:79], v[128:131], v[72:75], v[76:79]\n"
    "v_pk_mul_f16 v62, v56, v58 op_sel_hi:[0,1]\n"
    "v_readfirstlane_b32 s40, v24\n"
    "s_or_b32 s40, s40, s44\n"
    "s_lshl1_add_u32 s41, s40, s54\n"
    "s_cmpk_lt_u32 s40, 256\n"
    "s_cselect_b32 s41, s41, 2\n"
    "v_mov_b32 v12, s41\n"
    "s_cbranch_scc0 .Lxd_tf12\n"
    ".Lxd_tf12r:\n"
    "s_mul_i32 s41, s41, 52\n"
    "s_add_u32 s38, s4, s41\n"
    "s_addc_u32 s39, s5, 0\n"
    "v_mfma_f32_16x16x32_f16 v[80:83], v[132:135], v[72:75], v[80:83]\n"
    "v_pk_mul_f16 v63, v56, v59 op_sel_hi:[0,1]\n"
    "ds_write_b32 v25, v12 offset:20\n"
    "global_load_dwordx3 v[36:38], v17, s[38:39] offset:-104\n"
    "global_load_dword v41, v18, s[38:39] offset:-8\n"
    "v_mfma_f32_16x16x32_f16 v[80:83], v[120:123], v[68:71], v[80:83]\n"
    "v_pk_mul_f16 v64, v56, v56 op_sel_hi:[0,1]\n"
    "v_xor_b32 v15, 0x4000, v15\n"
    "v_mfma_f32_16x16x32_f16 v[84:87], v[124:127], v[68:71], v[84:87]\n"
    "v_pk_mul_f16 v65, v56, v49 op_sel_hi:[0,1]\n"
    "v_xor_b32 v16, 0x4000, v16\n"
    "v_mfma_f32_16x16x32_f16 v[84:87], v[136:139], v[72:75], v[84:87]\n"
    "v_pk_mul_f16 v66, v48, v56 op_sel_hi:[0,1]\n"
    "v_permlane32_swap_b32 v7, v8\n"
    "v_permlane32_swap_b32 v9, v10\n"
    "v_smfmac_f32_16x16x64_f16 v[88:91], v[140:143], v[68:75], v2\n"
    "v_pk_mul_f16 v67, v48, v57 op_sel_hi:[0,1]\n"
    "v_mul_f32 v7, v0, v7\n"
    "v_mul_f32 v9, v0, v9\n"
    "v_mfma_f32_16x16x32_f16 v[76:79], v[144:147], v[60:63], v[76:79]\n"
    "v_pk_mul_f16 v68, v50, v56 op_sel_hi:[0,1]\n"
    "v_pk_mul_f16 v69, v50, v57 op_sel_hi:[0,1]\n"
    "v_mfma_f32_16x16x32_f16 v[76:79], v[156:159], v[64:67], v[76:79]\n"
    "v_pk_mul_f16 v70, v52, v56 op_sel_hi:[0,1]\n"
    "v_fmac_f32 v7, v23, v8\n"
    "v_fmac_f32 v9, v23, v10\n"
    "v_mfma_f32_16x16x32_f16 v[80:83], v[160:163], v[64:67], v[80:83]\n"
    "v_pk_mul_f16 v71, v52, v57 op_sel_hi:[0,1]\n"
    "v_permlane16_swap_b32 v7, v9\n"
    "v_mfma_f32_16x16x32_f16 v[80:83], v[148:151], v[60:63], v[80:83]\n"
    "v_pk_mul_f16 v72, v54, v56 op_sel_hi:[0,1]\n"
    "ds_read_b32 v22, v25 offset:516\n"
    "v_mfma_f32_16x16x32_f16 v[84:87], v[152:155], v[60:63], v[84:87]\n"
    "v_pk_mul_f16 v73, v54, v57 op_sel_hi:[0,1]\n"
    "v_add_f32 v7, v7, v9\n"
    "v_mfma_f32_16x16x32_f16 v[84:87], v[164:167], v[64:67], v[84:87]\n"
    "v_pk_mul_f16 v74, v54, v54 op_sel_hi:[0,1]\n"
    "v_add_f32_dpp v7, v7, v7 quad_perm:[1,0,3,2] row_mask:0xf bank_mask:0xf\n"
    "v_smfmac_f32_16x16x64_f16 v[88:91], v[168:171], v[60:67], v2\n"
    "v_pk_mul_f16 v75, v54, v49 op_sel_hi:[0,1]\n"
    "v_add_f32_dpp v7, v7, v7 quad_perm:[2,3,0,1] row_mask:0xf bank_mask:0xf\n"
    "v_mfma_f32_16x16x32_f16 v[76:79], v[172:175], v[68:71], v[76:79]\n"
    "v_pk_mul_f16 v60, v48, v54 op_sel_hi:[0,1]\n"
    "v_pk_mul_f16 v61, v48, v55 op_sel_hi:[0,1]\n"
    "v_mfma_f32_16x16x32_f16 v[76:79], v[184:187], v[72:75], v[76:79]\n"
    "v_pk_mul_f16 v62, v50, v54 op_sel_hi:[0,1]\n"
    "v_add_f32_dpp v7, v7, v7 row_half_mirror row_mask:0xf bank_mask:0xf\n"
    "v_mfma_f32_16x16x32_f16 v[80:83], v[188:191], v[72:75], v[80:83]\n"
    "v_pk_mul_f16 v63, v50, v55 op_sel_hi:[0,1]\n"
    "s_waitcnt lgkmcnt(0)\n"
    "v_readfirstlane_b32 s45, v22\n"
    "s_cmp_lt_i32 s45, 16\n"
    "s_cbranch_scc1 .Lxd_ck15\n"
    ".Lxd_ck15r:\n"
    "v_mfma_f32_16x16x32_f16 v[80:83], v[176:179], v[68:71], v[80:83]\n"
    "v_pk_mul_f16 v64, v52, v54 op_sel_hi:[0,1]\n"
    "ds_read_b64_tr_b16 v[44:45], v5 offset:4096\n"
    "v_mfma_f32_16x16x32_f16 v[84:87], v[180:183], v[68:71], v[84:87]\n"
    "v_pk_mul_f16 v65, v52, v55 op_sel_hi:[0,1]\n"
    "ds_read_b64_tr_b16 v[46:47], v6 offset:4096\n"
    "v_mfma_f32_16x16x32_f16 v[84:87], v[192:195], v[72:75], v[84:87]\n"
    "v_pk_mul_f16 v66, v52, v52 op_sel_hi:[0,1]\n"
    "ds_read_b64_tr_b16 v[58:59], v4 offset:6976\n"
    "s_cbranch_vccnz .Lxd_pr14\n"
    "s_setprio 0\n"
    ".Lxd_pr14:\n"
    "v_smfmac_f32_16x16x64_f16 v[88:91], v[196:199], v[68:75], v2\n"
    "v_pk_mul_f16 v67, v52, v49 op_sel_hi:[0,1]\n"
    "ds_read_b64_tr_b16 v[56:57], v4 offset:6400\n"
    "v_mfma_f32_16x16x32_f16 v[76:79], v[200:203], v[60:63], v[76:79]\n"
    "v_pk_mul_f16 v68, v48, v52 op_sel_hi:[0,1]\n"
    "v_pk_mul_f16 v69, v48, v53 op_sel_hi:[0,1]\n"
    "v_mfma_f32_16x16x32_f16 v[76:79], v[212:215], v[64:67], v[76:79]\n"
    "v_pk_mul_f16 v70, v50, v52 op_sel_hi:[0,1]\n"
    "ds_read_b64_tr_b16 v[54:55], v4 offset:5824\n"
    "v_mfma_f32_16x16x32_f16 v[80:83], v[216:219], v[64:67], v[80:83]\n"
    "v_pk_mul_f16 v71, v50, v53 op_sel_hi:[0,1]\n"
    "v_add_f32_dpp v7, v7, v7 row_mirror row_mask:0xf bank_mask:0xf\n"
    "v_mfma_f32_16x16x32_f16 v[80:83], v[204:207], v[60:63], v[80:83]\n"
    "v_pk_mul_f16 v72, v50, v50 op_sel_hi:[0,1]\n"
    "ds_write_b32 v14, v7\n"
    "v_add_u32 v14, 64, v14\n"
    "v_mfma_f32_16x16x32_f16 v[84:87], v[208:211], v[60:63], v[84:87]\n"
    "v_pk_mul_f16 v73, v50, v49 op_sel_hi:[0,1]\n"
    "v_pk_mul_f16 v74, v48, v50 op_sel_hi:[0,1]\n"
    "v_mfma_f32_16x16x32_f16 v[84:87], v[220:223], v[64:67], v[84:87]\n"
    "v_pk_mul_f16 v75, v48, v51 op_sel_hi:[0,1]\n"
    "v_smfmac_f32_16x16x64_f16 v[88:91], v[224:227], v[60:67], v2\n"
    "ds_read_b64_tr_b16 v[48:49], v3 offset:4096\n"
    "ds_read_b64_tr_b16 v[50:51], v4 offset:4672\n"
    "ds_read_b64_tr_b16 v[52:53], v4 offset:5248\n"
    "v_smfmac_f32_16x16x64_f16 v[88:91], v[252:255], v[68:75], v2\n"
    "s_waitcnt lgkmcnt(7)\n"
    "v_pk_mul_f16 v60, v44, s45 op_sel:[0,1]\n"
    "v_pk_mul_f16 v61, v45, s45 op_sel:[0,1]\n"
    "v_mfma_f32_16x16x32_f16 v[76:79], v[228:231], v[68:71], v[76:79]\n"
    "v_pk_mul_f16 v62, v46, s45 op_sel:[0,1]\n"
    "v_pk_mul_f16 v63, v47, s45 op_sel:[0,1]\n"
    "v_mfma_f32_16x16x32_f16 v[76:79], v[240:243], v[72:75], v[76:79]\n"
    "v_pk_mul_f16 v64, v44, v46\n"
    "v_pk_mul_f16 v65, v45, v47\n"
    "v_mfma_f32_16x16x32_f16 v[80:83], v[244:247], v[72:75], v[80:83]\n"
    "v_add_f32 v11, v88, v89\n"
    "v_add_f32 v13, v90, v91\n"
    "v_mfma_f32_16x16x32_f16 v[80:83], v[232:235], v[68:71], v[80:83]\n"
    "v_mov_b64 v[88:89], 0\n"
    "v_mov_b64 v[90:91], 0\n"
    "v_mfma_f32_16x16x32_f16 v[84:87], v[236:239], v[68:71], v[84:87]\n"
    "s_waitcnt lgkmcnt(0)\n"
    "v_pk_mul_f16 v66, v48, v48 op_sel_hi:[0,1]\n"
    "v_pk_mul_f16 v67, v48, v49 op_sel_hi:[0,1]\n"
    "v_mfma_f32_16x16x32_f16 v[84:87], v[248:251], v[72:75], v[84:87]\n"
    "v_mul_f32 v11, v11, v13\n"
    "s_cmp_eq_u32 s45, -1\n"
    "s_cbranch_scc1 .Lxd_ex0\n"
    "s_cmp_lg_u32 s36, 0\n"
    "s_cbranch_scc1 .Lxd_pl1\n"
    ".Lxd_ph1:\n"
    "s_cbranch_vccnz .Lxd_pr16\n"
    "s_setprio 2\n"
    ".Lxd_pr16:\n"
    "v_smfmac_f32_16x16x64_f16 v[88:91], v[112:115], v[60:67], v2\n"
    "v_fmac_f32 v11, v76, v80\n"
    "v_fmac_f32 v11, v77, v81\n"
    "v_fmac_f32 v11, v78, v82\n"
    "v_fmac_f32 v11, v79, v83\n"
    "v_mfma_f32_16x16x32_f16 v[80:83], v[92:95], v[60:63], 0\n"
    "v_pk_mul_f16 v68, v58, v58 op_sel_hi:[0,1]\n"
    "v_pk_mul_f16 v69, v58, v49 op_sel_hi:[0,1]\n"
    "v_mfma_f32_16x16x32_f16 v[80:83], v[104:107], v[64:67], v[80:83]\n"
    "v_pk_mul_f16 v70, v48, v58 op_sel_hi:[0,1]\n"
    "v_pk_mul_f16 v71, v48, v59 op_sel_hi:[0,1]\n"
    "v_mfma_f32_16x16x32_f16 v[76:79], v[100:103], v[64:67], 0\n"
    "v_fmac_f32 v11, v84, v85\n"
    "v_fma_f32 v7, v86, v87, v11\n"
    "v_mfma_f32_16x16x32_f16 v[84:87], v[108:111], v[64:67], 0\n"
    "v_pk_mul_f16 v72, v50, v58 op_sel_hi:[0,1]\n"
    "v_pk_mul_f16 v73, v50, v59 op_sel_hi:[0,1]\n"
    "v_mfma_f32_16x16x32_f16 v[84:87], v[96:99], v[60:63], v[84:87]\n"
    "v_pk_mul_f16 v74, v52, v58 op_sel_hi:[0,1]\n"
    "v_pk_mul_f16 v75, v52, v59 op_sel_hi:[0,1]\n"
    "s_waitcnt vmcnt(0)\n"
    "v_mfma_f32_16x16x32_f16 v[76:79], v[116:119], v[68:71], v[76:79]\n"
    "v_pk_mul_f16 v60, v54, v58 op_sel_hi:[0,1]\n"
    "v_pk_mul_f16 v61, v54, v59 op_sel_hi:[0,1]\n"
    "v_mfma_f32_16x16x32_f16 v[76:79], v[128:131], v[72:75], v[76:79]\n"
    "v_pk_mul_f16 v62, v56, v58 op_sel_hi:[0,1]\n"
    "v_lshl_or_b32 v36, v36, 7, v19\n"
    "global_load_dwordx4 v[28:31], v36, s[8:9]\n"
    "v_mfma_f32_16x16x32_f16 v[80:83], v[132:135], v[72:75], v[80:83]\n"
    "v_pk_mul_f16 v63, v56, v59 op_sel_hi:[0,1]\n"
    "v_lshl_or_b32 v37, v37, 7, v19\n"
    "global_load_dwordx4 v[32:35], v37, s[8:9]\n"
    "v_mfma_f32_16x16x32_f16 v[80:83], v[120:123], v[68:71], v[80:83]\n"
    "v_pk_mul_f16 v64, v56, v56 op_sel_hi:[0,1]\n"
    "v_lshl_or_b32 v41, v41, 7, v20\n"
    "global_load_dword v40, v41, s[8:9]\n"
    "v_mfma_f32_16x16x32_f16 v[84:87], v[124:127], v[68:71], v[84:87]\n"
    "v_pk_mul_f16 v65, v56, v49 op_sel_hi:[0,1]\n"
    "v_lshl_or_b32 v38, v38, 7, v19\n"
    "global_load_dwordx4 v[36:39], v38, s[8:9]\n"
    "v_mfma_f32_16x16x32_f16 v[84:87], v[136:139], v[72:75], v[84:87]\n"
    "v_pk_mul_f16 v66, v48, v56 op_sel_hi:[0,1]\n"
    "v_smfmac_f32_16x16x64_f16 v[88:91], v[140:143], v[68:75], v2\n"
    "v_pk_mul_f16 v67, v48, v57 op_sel_hi:[0,1]\n"
    "v_mfma_f32_16x16x32_f16 v[76:79], v[144:147], v[60:63], v[76:79]\n"
    "v_pk_mul_f16 v68, v50, v56 op_sel_hi:[0,1]\n"
    "v_pk_mul_f16 v69, v50, v57 op_sel_hi:[0,1]\n"
    "v_mfma_f32_16x16x32_f16 v[76:79], v[156:159], v[64:67], v[76:79]\n"
    "v_pk_mul_f16 v70, v52, v56 op_sel_hi:[0,1]\n"
    "v_mfma_f32_16x16x32_f16 v[80:83], v[160:163], v[64:67], v[80:83]\n"
    "v_pk_mul_f16 v71, v52, v57 op_sel_hi:[0,1]\n"
    "v_mfma_f32_16x16x32_f16 v[80:83], v[148:151], v[60:63], v[80:83]\n"
    "v_pk_mul_f16 v72, v54, v56 op_sel_hi:[0,1]\n"
    "ds_read_b32 v22, v25 offset:520\n"
    "v_mfma_f32_16x16x32_f16 v[84:87], v[152:155], v[60:63], v[84:87]\n"
    "v_pk_mul_f16 v73, v54, v57 op_sel_hi:[0,1]\n"
    "v_mfma_f32_16x16x32_f16 v[84:87], v[164:167], v[64:67], v[84:87]\n"
    "v_pk_mul_f16 v74, v54, v54 op_sel_hi:[0,1]\n"
    "v_smfmac_f32_16x16x64_f16 v[88:91], v[168:171], v[60:67], v2\n"
    "v_pk_mul_f16 v75, v54, v49 op_sel_hi:[0,1]\n"
    "v_mfma_f32_16x16x32_f16 v[76:79], v[172:175], v[68:71], v[76:79]\n"
    "v_pk_mul_f16 v60, v48, v54 op_sel_hi:[0,1]\n"
    "v_pk_mul_f16 v61, v48, v55 op_sel_hi:[0,1]\n"
    "v_mfma_f32_16x16x32_f16 v[76:79], v[184:187], v[72:75], v[76:79]\n"
    "v_pk_mul_f16 v62, v50, v54 op_sel_hi:[0,1]\n"
    "v_mfma_f32_16x16x32_f16 v[80:83], v[188:191], v[72:75], v[80:83]\n"
    "v_pk_mul_f16 v63, v50, v55 op_sel_hi:[0,1]\n"
    "s_waitcnt lgkmcnt(0)\n"
    "v_readfirstlane_b32 s45, v22\n"
    "s_cmp_lt_i32 s45, 16\n"
    "s_cbranch_scc1 .Lxd_ck18\n"
    ".Lxd_ck18r:\n"
    "v_mfma_f32_16x16x32_f16 v[80:83], v[176:179], v[68:71], v[80:83]\n"
    "v_pk_mul_f16 v64, v52, v54 op_sel_hi:[0,1]\n"
    "ds_read_b64_tr_b16 v[44:45], v5 offset:8192\n"
    "v_mfma_f32_16x16x32_f16 v[84:87], v[180:183], v[68:71], v[84:87]\n"
    "v_pk_mul_f16 v65, v52, v55 op_sel_hi:[0,1]\n"
    "ds_read_b64_tr_b16 v[46:47], v6 offset:8192\n"
    "v_mfma_f32_16x16x32_f16 v[84:87], v[192:195], v[72:75], v[84:87]\n"
    "v_pk_mul_f16 v66, v52, v52 op_sel_hi:[0,1]\n"
    "ds_read_b64_tr_b16 v[58:59], v4 offset:11072\n"
    "s_cbranch_vccnz .Lxd_pr17\n"
    "s_setprio 0\n"
    ".Lxd_pr17:\n"
    "v_smfmac_f32_16x16x64_f16 v[88:91], v[196:199], v[68:75], v2\n"
    "v_pk_mul_f16 v67, v52, v49 op_sel_hi:[0,1]\n"
    "ds_read_b64_tr_b16 v[56:57], v4 offset:10496\n"
    "v_mfma_f32_16x16x32_f16 v[76:79], v[200:203], v[60:63], v[76:79]\n"
    "v_pk_mul_f16 v68, v48, v52 op_sel_hi:[0,1]\n"
    "v_pk_mul_f16 v69, v48, v53 op_sel_hi:[0,1]\n"
    "v_mfma_f32_16x16x32_f16 v[76:79], v[212:215], v[64:67], v[76:79]\n"
    "v_pk_mul_f16 v70, v50, v52 op_sel_hi:[0,1]\n"
    "ds_read_b64_tr_b16 v[54:55], v4 offset:9920\n"
    "v_mfma_f32_16x16x32_f16 v[80:83], v[216:219], v[64:67], v[80:83]\n"
    "v_pk_mul_f16 v71, v50, v53 op_sel_hi:[0,1]\n"
    "v_mfma_f32_16x16x32_f16 v[80:83], v[204:207], v[60:63], v[80:83]\n"
    "v_pk_mul_f16 v72, v50, v50 op_sel_hi:[0,1]\n"
    "v_mfma_f32_16x16x32_f16 v[84:87], v[208:211], v[60:63], v[84:87]\n"
    "v_pk_mul_f16 v73, v50, v49 op_sel_hi:[0,1]\n"
    "v_pk_mul_f16 v74, v48, v50 op_sel_hi:[0,1]\n"
    "v_mfma_f32_16x16x32_f16 v[84:87], v[220:223], v[64:67], v[84:87]\n"
    "v_pk_mul_f16 v75, v48, v51 op_sel_hi:[0,1]\n"
    "v_smfmac_f32_16x16x64_f16 v[88:91], v[224:227], v[60:67], v2\n"
    "ds_read_b64_tr_b16 v[48:49], v3 offset:8192\n"
    "ds_read_b64_tr_b16 v[50:51], v4 offset:8768\n"
    "ds_read_b64_tr_b16 v[52:53], v4 offset:9344\n"
    "v_smfmac_f32_16x16x64_f16 v[88:91], v[252:255], v[68:75], v2\n"
    "s_waitcnt lgkmcnt(6)\n"
    "v_pk_mul_f16 v60, v44, s45 op_sel:[0,1]\n"
    "v_pk_mul_f16 v61, v45, s45 op_sel:[0,1]\n"
    "v_mfma_f32_16x16x32_f16 v[76:79], v[228:231], v[68:71], v[76:79]\n"
    "v_pk_mul_f16 v62, v46, s45 op_sel:[0,1]\n"
    "v_pk_mul_f16 v63, v47, s45 op_sel:[0,1]\n"
    "v_mfma_f32_16x16x32_f16 v[76:79], v[240:243], v[72:75], v[76:79]\n"
    "v_pk_mul_f16 v64, v44, v46\n"
    "v_pk_mul_f16 v65, v45, v47\n"
    "v_mfma_f32_16x16x32_f16 v[80:83], v[244:247], v[72:75], v[80:83]\n"
    "v_add_f32 v11, v88, v89\n"
    "v_add_f32 v13, v90, v91\n"
    "v_mfma_f32_16x16x32_f16 v[80:83], v[232:235], v[68:71], v[80:83]\n"
    "v_mov_b64 v[88:89], 0\n"
    "v_mov_b64 v[90:91], 0\n"
    "v_mfma_f32_16x16x32_f16 v[84:87], v[236:239], v[68:71], v[84:87]\n"
    "s_waitcnt lgkmcnt(0)\n"
    "v_pk_mul_f16 v66, v48, v48 op_sel_hi:[0,1]\n"
    "v_pk_mul_f16 v67, v48, v49 op_sel_hi:[0,1]\n"
    "v_mfma_f32_16x16x32_f16 v[84:87], v[248:251], v[72:75], v[84:87]\n"
    "v_mul_f32 v11, v11, v13\n"
    "s_cmp_eq_u32 s45, -1\n"
    "s_cbranch_scc1 .Lxd_ex1\n"
    ".Lxd_ph2:\n"
    "s_cbranch_vccnz .Lxd_pr19\n"
    "s_setprio 2\n"
    ".Lxd_pr19:\n"
    "v_smfmac_f32_16x16x64_f16 v[88:91], v[112:115], v[60:67], v2\n"
    "v_fmac_f32 v11, v76, v80\n"
    "v_fmac_f32 v11, v77, v81\n"
    "v_fmac_f32 v11, v78, v82\n"
    "v_fmac_f32 v11, v79, v83\n"
    "v_mfma_f32_16x16x32_f16 v[80:83], v[92:95], v[60:63], 0\n"
    "v_pk_mul_f16 v68, v58, v58 op_sel_hi:[0,1]\n"
    "v_pk_mul_f16 v69, v58, v49 op_sel_hi:[0,1]\n"
    "v_mfma_f32_16x16x32_f16 v[80:83], v[104:107], v[64:67], v[80:83]\n"
    "v_pk_mul_f16 v70, v48, v58 op_sel_hi:[0,1]\n"
    "v_pk_mul_f16 v71, v48, v59 op_sel_hi:[0,1]\n"
    "v_mfma_f32_16x16x32_f16 v[76:79], v[100:103], v[64:67], 0\n"
    "v_fmac_f32 v11, v84, v85\n"
    "v_fma_f32 v8, v86, v87, v11\n"
    "v_mfma_f32_16x16x32_f16 v[84:87], v[108:111], v[64:67], 0\n"
    "v_pk_mul_f16 v72, v50, v58 op_sel_hi:[0,1]\n"
    "v_pk_mul_f16 v73, v50, v59 op_sel_hi:[0,1]\n"
    "v_mfma_f32_16x16x32_f16 v[84:87], v[96:99], v[60:63], v[84:87]\n"
    "v_pk_mul_f16 v74, v52, v58 op_sel_hi:[0,1]\n"
    "v_pk_mul_f16 v75, v52, v59 op_sel_hi:[0,1]\n"
    "s_waitcnt vmcnt(0)\n"
    "v_mfma_f32_16x16x32_f16 v[76:79], v[116:119], v[68:71], v[76:79]\n"
    "v_pk_mul_f16 v60, v54, v58 op_sel_hi:[0,1]\n"
    "v_pk_mul_f16 v61, v54, v59 op_sel_hi:[0,1]\n"
    "s_movk_i32 s43, 15744\n"
    "v_pk_maximum3_f16 v21, v28, v28, s43 op_sel_hi:[1,1,0] neg_lo:[0,1,0] neg_hi:[0,1,0]\n"
    "v_mfma_f32_16x16x32_f16 v[76:79], v[128:131], v[72:75], v[76:79]\n"
    "v_pk_mul_f16 v62, v56, v58 op_sel_hi:[0,1]\n"
    "v_pk_maximum3_f16 v21, v29, v29, v21 neg_lo:[0,1,0] neg_hi:[0,1,0]\n"
    "v_mfma_f32_16x16x32_f16 v[80:83], v[132:135], v[72:75], v[80:83]\n"
    "v_pk_mul_f16 v63, v56, v59 op_sel_hi:[0,1]\n"
    "v_pk_maximum3_f16 v21, v30, v30, v21 neg_lo:[0,1,0] neg_hi:[0,1,0]\n"
    "v_mfma_f32_16x16x32_f16 v[80:83], v[120:123], v[68:71], v[80:83]\n"
    "v_pk_mul_f16 v64, v56, v56 op_sel_hi:[0,1]\n"
    "v_pk_maximum3_f16 v21, v31, v31, v21 neg_lo:[0,1,0] neg_hi:[0,1,0]\n"
    "v_mfma_f32_16x16x32_f16 v[84:87], v[124:127], v[68:71], v[84:87]\n"
    "v_pk_mul_f16 v65, v56, v49 op_sel_hi:[0,1]\n"
    "v_pk_maximum3_f16 v21, v32, v32, v21 neg_lo:[0,1,0] neg_hi:[0,1,0]\n"
    "v_mfma_f32_16x16x32_f16 v[84:87], v[136:139], v[72:75], v[84:87]\n"
    "v_pk_mul_f16 v66, v48, v56 op_sel_hi:[0,1]\n"
    "v_pk_maximum3_f16 v21, v33, v33, v21 neg_lo:[0,1,0] neg_hi:[0,1,0]\n"
    "v_smfmac_f32_16x16x64_f16 v[88:91], v[140:143], v[68:75], v2\n"
    "v_pk_mul_f16 v67, v48, v57 op_sel_hi:[0,1]\n"
    "v_pk_maximum3_f16 v21, v34, v34, v21 neg_lo:[0,1,0] neg_hi:[0,1,0]\n"
    "v_mfma_f32_16x16x32_f16 v[76:79], v[144:147], v[60:63], v[76:79]\n"
    "v_pk_mul_f16 v68, v50, v56 op_sel_hi:[0,1]\n"
    "v_pk_mul_f16 v69, v50, v57 op_sel_hi:[0,1]\n"
    "v_pk_maximum3_f16 v21, v35, v35, v21 neg_lo:[0,1,0] neg_hi:[0,1,0]\n"
    "v_mfma_f32_16x16x32_f16 v[76:79], v[156:159], v[64:67], v[76:79]\n"
    "v_pk_mul_f16 v70, v52, v56 op_sel_hi:[0,1]\n"
    "v_pk_maximum3_f16 v21, v36, v36, v21 neg_lo:[0,1,0] neg_hi:[0,1,0]\n"
    "v_mfma_f32_16x16x32_f16 v[80:83], v[160:163], v[64:67], v[80:83]\n"
    "v_pk_mul_f16 v71, v52, v57 op_sel_hi:[0,1]\n"
    "v_pk_maximum3_f16 v21, v37, v37, v21 neg_lo:[0,1,0] neg_hi:[0,1,0]\n"
    "v_mfma_f32_16x16x32_f16 v[80:83], v[148:151], v[60:63], v[80:83]\n"
    "v_pk_mul_f16 v72, v54, v56 op_sel_hi:[0,1]\n"
    "ds_read_b32 v22, v25 offset:524\n"
    "v_pk_maximum3_f16 v21, v38, v38, v21 neg_lo:[0,1,0] neg_hi:[0,1,0]\n"
    "v_mfma_f32_16x16x32_f16 v[84:87], v[152:155], v[60:63], v[84:87]\n"
    "v_pk_mul_f16 v73, v54, v57 op_sel_hi:[0,1]\n"
    "v_pk_maximum3_f16 v21, v39, v39, v21 neg_lo:[0,1,0] neg_hi:[0,1,0]\n"
    "v_mfma_f32_16x16x32_f16 v[84:87], v[164:167], v[64:67], v[84:87]\n"
    "v_pk_mul_f16 v74, v54, v54 op_sel_hi:[0,1]\n"
    "v_pk_maximum3_f16 v21, v40, v40, v21 neg_lo:[0,1,0] neg_hi:[0,1,0]\n"
    "v_smfmac_f32_16x16x64_f16 v[88:91], v[168:171], v[60:67], v2\n"
    "v_pk_mul_f16 v75, v54, v49 op_sel_hi:[0,1]\n"
    "v_pk_max_u16 v21, v21, v21 op_sel:[0,1] op_sel_hi:[1,0]\n"
    "v_mfma_f32_16x16x32_f16 v[76:79], v[172:175], v[68:71], v[76:79]\n"
    "v_pk_mul_f16 v60, v48, v54 op_sel_hi:[0,1]\n"
    "v_pk_mul_f16 v61, v48, v55 op_sel_hi:[0,1]\n"
    "v_max_u32_dpp v21, v21, v21 quad_perm:[1,0,3,2] row_mask:0xf bank_mask:0xf\n"
    "v_mfma_f32_16x16x32_f16 v[76:79], v[184:187], v[72:75], v[76:79]\n"
    "v_pk_mul_f16 v62, v50, v54 op_sel_hi:[0,1]\n"
    "v_max_u32_dpp v21, v21, v21 quad_perm:[2,3,0,1] row_mask:0xf bank_mask:0xf\n"
    "v_mfma_f32_16x16x32_f16 v[80:83], v[188:191], v[72:75], v[80:83]\n"
    "v_pk_mul_f16 v63, v50, v55 op_sel_hi:[0,1]\n"
    "s_waitcnt lgkmcnt(0)\n"
    "v_readfirstlane_b32 s45, v22\n"
    "s_cmp_lt_i32 s45, 16\n"
    "s_cbranch_scc1 .Lxd_ck21\n"
    ".Lxd_ck21r:\n"
    "v_max_u32_dpp v21, v21, v21 row_half_mirror row_mask:0xf bank_mask:0xf\n"
    "v_mfma_f32_16x16x32_f16 v[80:83], v[176:179], v[68:71], v[80:83]\n"
    "v_pk_mul_f16 v64, v52, v54 op_sel_hi:[0,1]\n"
    "ds_read_b64_tr_b16 v[44:45], v5 offset:12288\n"
    "v_max_u32_dpp v21, v21, v21 row_mirror row_mask:0xf bank_mask:0xf\n"
    "v_mfma_f32_16x16x32_f16 v[84:87], v[180:183], v[68:71], v[84:87]\n"
    "v_pk_mul_f16 v65, v52, v55 op_sel_hi:[0,1]\n"
    "ds_read_b64_tr_b16 v[46:47], v6 offset:12288\n"
    "v_max_u32_dpp v21, v21, v21 row_bcast:15 row_mask:0xa bank_mask:0xf\n"
    "v_mfma_f32_16x16x32_f16 v[84:87], v[192:195], v[72:75], v[84:87]\n"
    "v_pk_mul_f16 v66, v52, v52 op_sel_hi:[0,1]\n"
    "ds_read_b64_tr_b16 v[58:59], v4 offset:15168\n"
    "v_max_u32_dpp v21, v21, v21 row_bcast:31 row_mask:0xc bank_mask:0xf\n"
    "s_cbranch_vccnz .Lxd_pr20\n"
    "s_setprio 0\n"
    ".Lxd_pr20:\n"
    "v_smfmac_f32_16x16x64_f16 v[88:91], v[196:199], v[68:75], v2\n"
    "v_pk_mul_f16 v67, v52, v49 op_sel_hi:[0,1]\n"
    "ds_read_b64_tr_b16 v[56:57], v4 offset:14592\n"
    "v_readlane_b32 s47, v21, 63\n"
    "v_mfma_f32_16x16x32_f16 v[76:79], v[200:203], v[60:63], v[76:79]\n"
    "v_pk_mul_f16 v68, v48, v52 op_sel_hi:[0,1]\n"
    "v_pk_mul_f16 v69, v48, v53 op_sel_hi:[0,1]\n"
    "s_and_b32 s50, s47, 0x7f80\n"
    "s_lshr_b32 s47, s50, 3\n"
    "s_sub_u32 s50, s50, 2816\n"
    "s_mul_i32 s51, s50, -8\n"
    "v_mfma_f32_16x16x32_f16 v[76:79], v[212:215], v[64:67], v[76:79]\n"
    "v_pk_mul_f16 v70, v50, v52 op_sel_hi:[0,1]\n"
    "ds_read_b64_tr_b16 v[54:55], v4 offset:14016\n"
    "v_pk_maximum3_f16 v21, v28, v28, s50 op_sel_hi:[1,1,0] neg_lo:[0,1,0] neg_hi:[0,1,0]\n"
    "v_mfma_f32_16x16x32_f16 v[80:83], v[216:219], v[64:67], v[80:83]\n"
    "v_pk_mul_f16 v71, v50, v53 op_sel_hi:[0,1]\n"
    "v_pk_mad_u16 v21, v21, 8, s51 op_sel_hi:[1,0,0]\n"
    "v_mfma_f32_16x16x32_f16 v[80:83], v[204:207], v[60:63], v[80:83]\n"
    "v_pk_mul_f16 v72, v50, v50 op_sel_hi:[0,1]\n"
    "v_bfi_b32 v28, s57, v21, v28\n"
    "v_mfma_f32_16x16x32_f16 v[84:87], v[208:211], v[60:63], v[84:87]\n"
    "v_pk_mul_f16 v73, v50, v49 op_sel_hi:[0,1]\n"
    "v_pk_mul_f16 v74, v48, v50 op_sel_hi:[0,1]\n"
    "v_pk_maximum3_f16 v12, v29, v29, s50 op_sel_hi:[1,1,0] neg_lo:[0,1,0] neg_hi:[0,1,0]\n"
    "v_mfma_f32_16x16x32_f16 v[84:87], v[220:223], v[64:67], v[84:87]\n"
    "v_pk_mul_f16 v75, v48, v51 op_sel_hi:[0,1]\n"
    "v_pk_mad_u16 v12, v12, 8, s51 op_sel_hi:[1,0,0]\n"
    "v_smfmac_f32_16x16x64_f16 v[88:91], v[224:227], v[60:67], v2\n"
    "ds_read_b64_tr_b16 v[48:49], v3 offset:12288\n"
    "ds_read_b64_tr_b16 v[50:51], v4 offset:12864\n"
    "ds_read_b64_tr_b16 v[52:53], v4 offset:13440\n"
    "v_bfi_b32 v29, s57, v12, v29\n"
    "v_smfmac_f32_16x16x64_f16 v[88:91], v[252:255], v[68:75], v2\n"
    "s_waitcnt lgkmcnt(6)\n"
    "v_pk_mul_f16 v60, v44, s45 op_sel:[0,1]\n"
    "v_pk_mul_f16 v61, v45, s45 op_sel:[0,1]\n"
    "v_mfma_f32_16x16x32_f16 v[76:79], v[228:231], v[68:71], v[76:79]\n"
    "v_pk_mul_f16 v62, v46, s45 op_sel:[0,1]\n"
    "v_pk_mul_f16 v63, v47, s45 op_sel:[0,1]\n"
    "v_mfma_f32_16x16x32_f16 v[76:79], v[240:243], v[72:75], v[76:79]\n"
    "v_pk_mul_f16 v64, v44, v46\n"
    "v_pk_mul_f16 v65, v45, v47\n"
    "v_mfma_f32_16x16x32_f16 v[80:83], v[244:247], v[72:75], v[80:83]\n"
    "v_add_f32 v11, v88, v89\n"
    "v_add_f32 v13, v90, v91\n"
    "v_mfma_f32_16x16x32_f16 v[80:83], v[232:235], v[68:71], v[80:83]\n"
    "v_mov_b64 v[88:89], 0\n"
    "v_mov_b64 v[90:91], 0\n"
    "v_mfma_f32_16x16x32_f16 v[84:87], v[236:239], v[68:71], v[84:87]\n"
    "s_waitcnt lgkmcnt(0)\n"
    "v_pk_mul_f16 v66, v48, v48 op_sel_hi:[0,1]\n"
    "v_pk_mul_f16 v67, v48, v49 op_sel_hi:[0,1]\n"
    "v_mfma_f32_16x16x32_f16 v[84:87], v[248:251], v[72:75], v[84:87]\n"
    "v_mul_f32 v11, v11, v13\n"
    "s_cmp_eq_u32 s45, -1\n"
    "s_cbranch_scc1 .Lxd_ex2\n"
    ".Lxd_ph3:\n"
    "s_cbranch_vccnz .Lxd_pr22\n"
    "s_setprio 2\n"
    ".Lxd_pr22:\n"
    "v_smfmac_f32_16x16x64_f16 v[88:91], v[112:115], v[60:67], v2\n"
    "v_fmac_f32 v11, v76, v80\n"
    "v_fmac_f32 v11, v77, v81\n"
    "v_fmac_f32 v11, v78, v82\n"
    "v_fmac_f32 v11, v79, v83\n"
    "v_mfma_f32_16x16x32_f16 v[80:83], v[92:95], v[60:63], 0\n"
    "v_pk_mul_f16 v68, v58, v58 op_sel_hi:[0,1]\n"
    "v_pk_mul_f16 v69, v58, v49 op_sel_hi:[0,1]\n"
    "v_xor_b32 v3, 0x4000, v3\n"
    "v_xor_b32 v4, 0x4000, v4\n"
    "v_mfma_f32_16x16x32_f16 v[80:83], v[104:107], v[64:67], v[80:83]\n"
    "v_pk_mul_f16 v70, v48, v58 op_sel_hi:[0,1]\n"
    "v_pk_mul_f16 v71, v48, v59 op_sel_hi:[0,1]\n"
    "v_xor_b32 v5, 0x4000, v5\n"
    "v_mfma_f32_16x16x32_f16 v[76:79], v[100:103], v[64:67], 0\n"
    "v_fmac_f32 v11, v84, v85\n"
    "v_fma_f32 v9, v86, v87, v11\n"
    "v_xor_b32 v6, 0x4000, v6\n"
    "v_mfma_f32_16x16x32_f16 v[84:87], v[108:111], v[64:67], 0\n"
    "v_pk_mul_f16 v72, v50, v58 op_sel_hi:[0,1]\n"
    "v_pk_mul_f16 v73, v50, v59 op_sel_hi:[0,1]\n"
    "v_pk_maximum3_f16 v21, v30, v30, s50 op_sel_hi:[1,1,0] neg_lo:[0,1,0] neg_hi:[0,1,0]\n"
    "v_pk_mad_u16 v21, v21, 8, s51 op_sel_hi:[1,0,0]\n"
    "v_mfma_f32_16x16x32_f16 v[84:87], v[96:99], v[60:63], v[84:87]\n"
    "v_pk_mul_f16 v74, v52, v58 op_sel_hi:[0,1]\n"
    "v_pk_mul_f16 v75, v52, v59 op_sel_hi:[0,1]\n"
    "v_bfi_b32 v30, s57, v21, v30\n"
    "v_mfma_f32_16x16x32_f16 v[76:79], v[116:119], v[68:71], v[76:79]\n"
    "v_pk_mul_f16 v60, v54, v58 op_sel_hi:[0,1]\n"
    "v_pk_mul_f16 v61, v54, v59 op_sel_hi:[0,1]\n"
    "v_pk_maximum3_f16 v12, v31, v31, s50 op_sel_hi:[1,1,0] neg_lo:[0,1,0] neg_hi:[0,1,0]\n"
    "v_mfma_f32_16x16x32_f16 v[76:79], v[128:131], v[72:75], v[76:79]\n"
    "v_pk_mul_f16 v62, v56, v58 op_sel_hi:[0,1]\n"
    "v_pk_mad_u16 v12, v12, 8, s51 op_sel_hi:[1,0,0]\n"
    "v_mfma_f32_16x16x32_f16 v[80:83], v[132:135], v[72:75], v[80:83]\n"
    "v_pk_mul_f16 v63, v56, v59 op_sel_hi:[0,1]\n"
    "v_bfi_b32 v31, s57, v12, v31\n"
    "v_pk_maximum3_f16 v21, v32, v32, s50 op_sel_hi:[1,1,0] neg_lo:[0,1,0] neg_hi:[0,1,0]\n"
    "v_mfma_f32_16x16x32_f16 v[80:83], v[120:123], v[68:71], v[80:83]\n"
    "v_pk_mul_f16 v64, v56, v56 op_sel_hi:[0,1]\n"
    "v_pk_mad_u16 v21, v21, 8, s51 op_sel_hi:[1,0,0]\n"
    "v_mfma_f32_16x16x32_f16 v[84:87], v[124:127], v[68:71], v[84:87]\n"
    "v_pk_mul_f16 v65, v56, v49 op_sel_hi:[0,1]\n"
    "v_bfi_b32 v32, s57, v21, v32\n"
    "v_mfma_f32_16x16x32_f16 v[84:87], v[136:139], v[72:75], v[84:87]\n"
    "v_pk_mul_f16 v66, v48, v56 op_sel_hi:[0,1]\n"
    "v_pk_maximum3_f16 v12, v33, v33, s50 op_sel_hi:[1,1,0] neg_lo:[0,1,0] neg_hi:[0,1,0]\n"
    "v_pk_mad_u16 v12, v12, 8, s51 op_sel_hi:[1,0,0]\n"
    "v_smfmac_f32_16x16x64_f16 v[88:91], v[140:143], v[68:75], v2\n"
    "v_pk_mul_f16 v67, v48, v57 op_sel_hi:[0,1]\n"
    "v_bfi_b32 v33, s57, v12, v33\n"
    "v_mfma_f32_16x16x32_f16 v[76:79], v[144:147], v[60:63], v[76:79]\n"
    "v_pk_mul_f16 v68, v50, v56 op_sel_hi:[0,1]\n"
    "v_pk_mul_f16 v69, v50, v57 op_sel_hi:[0,1]\n"
    "v_pk_maximum3_f16 v21, v34, v34, s50 op_sel_hi:[1,1,0] neg_lo:[0,1,0] neg_hi:[0,1,0]\n"
    "v_mfma_f32_16x16x32_f16 v[76:79], v[156:159], v[64:67], v[76:79]\n"
    "v_pk_mul_f16 v70, v52, v56 op_sel_hi:[0,1]\n"
    "v_pk_mad_u16 v21, v21, 8, s51 op_sel_hi:[1,0,0]\n"
    "v_mfma_f32_16x16x32_f16 v[80:83], v[160:163], v[64:67], v[80:83]\n"
    "v_pk_mul_f16 v71, v52, v57 op_sel_hi:[0,1]\n"
    "v_bfi_b32 v34, s57, v21, v34\n"
    "v_pk_maximum3_f16 v12, v35, v35, s50 op_sel_hi:[1,1,0] neg_lo:[0,1,0] neg_hi:[0,1,0]\n"
    "v_mfma_f32_16x16x32_f16 v[80:83], v[148:151], v[60:63], v[80:83]\n"
    "v_pk_mul_f16 v72, v54, v56 op_sel_hi:[0,1]\n"
    "ds_read_b32 v22, v25 offset:528\n"
    "v_pk_mad_u16 v12, v12, 8, s51 op_sel_hi:[1,0,0]\n"
    "v_mfma_f32_16x16x32_f16 v[84:87], v[152:155], v[60:63], v[84:87]\n"
    "v_pk_mul_f16 v73, v54, v57 op_sel_hi:[0,1]\n"
    "v_bfi_b32 v35, s57, v12, v35\n"
    "v_mfma_f32_16x16x32_f16 v[84:87], v[164:167], v[64:67], v[84:87]\n"
    "v_pk_mul_f16 v74, v54, v54 op_sel_hi:[0,1]\n"
    "v_pk_maximum3_f16 v21, v36, v36, s50 op_sel_hi:[1,1,0] neg_lo:[0,1,0] neg_hi:[0,1,0]\n"
    "v_pk_mad_u16 v21, v21, 8, s51 op_sel_hi:[1,0,0]\n"
    "v_smfmac_f32_16x16x64_f16 v[88:91], v[168:171], v[60:67], v2\n"
    "v_pk_mul_f16 v75, v54, v49 op_sel_hi:[0,1]\n"
    "v_bfi_b32 v36, s57, v21, v36\n"
    "v_mfma_f32_16x16x32_f16 v[76:79], v[172:175], v[68:71], v[76:79]\n"
    "v_pk_mul_f16 v60, v48, v54 op_sel_hi:[0,1]\n"
    "v_pk_mul_f16 v61, v48, v55 op_sel_hi:[0,1]\n"
    "v_pk_maximum3_f16 v12, v37, v37, s50 op_sel_hi:[1,1,0] neg_lo:[0,1,0] neg_hi:[0,1,0]\n"
    "v_mfma_f32_16x16x32_f16 v[76:79], v[184:187], v[72:75], v[76:79]\n"
    "v_pk_mul_f16 v62, v50, v54 op_sel_hi:[0,1]\n"
    "v_pk_mad_u16 v12, v12, 8, s51 op_sel_hi:[1,0,0]\n"
    "v_mfma_f32_16x16x32_f16 v[80:83], v[188:191], v[72:75], v[80:83]\n"
    "v_pk_mul_f16 v63, v50, v55 op_sel_hi:[0,1]\n"
    "s_waitcnt lgkmcnt(0)\n"
    "v_readfirstlane_b32 s45, v22\n"
    "s_cmp_lt_i32 s45, 16\n"
    "s_cbranch_scc1 .Lxd_ck24\n"
    ".Lxd_ck24r:\n"
    "v_bfi_b32 v37, s57, v12, v37\n"
    "v_pk_maximum3_f16 v21, v38, v38, s50 op_sel_hi:[1,1,0] neg_lo:[0,1,0] neg_hi:[0,1,0]\n"
    "v_mfma_f32_16x16x32_f16 v[80:83], v[176:179], v[68:71], v[80:83]\n"
    "v_pk_mul_f16 v64, v52, v54 op_sel_hi:[0,1]\n"
    "ds_read_b64_tr_b16 v[44:45], v5 offset:0\n"
    "s_and_b32 s40, s45, 15\n"
    "s_cbranch_scc1 .Lxd_dr25\n"
    "s_cmp_lt_u32 s55, 32\n"
    "s_cselect_b32 s44, 0, -1\n"
    "s_add_u32 s55, s55, 4\n"
    "v_add_u32_e64 v24, s44, 1\n"
    "s_mov_b64 exec, 1\n"
    "global_atomic_add v24, v17, v24, s[52:53] sc0 sc1\n"
    "s_mov_b64 exec, -1\n"
    ".Lxd_dr25r:\n"
    "v_pk_mad_u16 v21, v21, 8, s51 op_sel_hi:[1,0,0]\n"
    "v_mfma_f32_16x16x32_f16 v[84:87], v[180:183], v[68:71], v[84:87]\n"
    "v_pk_mul_f16 v65, v52, v55 op_sel_hi:[0,1]\n"
    "ds_read_b64_tr_b16 v[46:47], v6 offset:0\n"
    "v_bfi_b32 v38, s57, v21, v38\n"
    "v_mfma_f32_16x16x32_f16 v[84:87], v[192:195], v[72:75], v[84:87]\n"
    "v_pk_mul_f16 v66, v52, v52 op_sel_hi:[0,1]\n"
    "ds_read_b64_tr_b16 v[58:59], v4 offset:2880\n"
    "v_pk_maximum3_f16 v12, v39, v39, s50 op_sel_hi:[1,1,0] neg_lo:[0,1,0] neg_hi:[0,1,0]\n"
    "v_pk_mad_u16 v12, v12, 8, s51 op_sel_hi:[1,0,0]\n"
    "s_cbranch_vccnz .Lxd_pr23\n"
    "s_setprio 0\n"
    ".Lxd_pr23:\n"
    "v_smfmac_f32_16x16x64_f16 v[88:91], v[196:199], v[68:75], v2\n"
    "v_pk_mul_f16 v67, v52, v49 op_sel_hi:[0,1]\n"
    "ds_read_b64_tr_b16 v[56:57], v4 offset:2304\n"
    "v_bfi_b32 v39, s57, v12, v39\n"
    "v_mfma_f32_16x16x32_f16 v[76:79], v[200:203], v[60:63], v[76:79]\n"
    "v_pk_mul_f16 v68, v48, v52 op_sel_hi:[0,1]\n"
    "v_pk_mul_f16 v69, v48, v53 op_sel_hi:[0,1]\n"
    "v_pk_maximum3_f16 v21, v40, v40, s50 op_sel_hi:[1,1,0] neg_lo:[0,1,0] neg_hi:[0,1,0]\n"
    "v_mfma_f32_16x16x32_f16 v[76:79], v[212:215], v[64:67], v[76:79]\n"
    "v_pk_mul_f16 v70, v50, v52 op_sel_hi:[0,1]\n"
    "ds_read_b64_tr_b16 v[54:55], v4 offset:1728\n"
    "v_pk_mad_u16 v21, v21, 8, s51 op_sel_hi:[1,0,0]\n"
    "v_mfma_f32_16x16x32_f16 v[80:83], v[216:219], v[64:67], v[80:83]\n"
    "v_pk_mul_f16 v71, v50, v53 op_sel_hi:[0,1]\n"
    "v_bfi_b32 v40, s57, v21, v40\n"
    "s_sub_u32 s43, 4112, s47\n"
    "s_lshl_b32 s43, s43, 19\n"
    "v_cvt_pkrtz_f16_f32 v12, s43, s43\n"
    "v_mfma_f32_16x16x32_f16 v[80:83], v[204:207], v[60:63], v[80:83]\n"
    "v_pk_mul_f16 v72, v50, v50 op_sel_hi:[0,1]\n"
    "v_lshl_or_b32 v21, v12, 16, s47\n"
    "v_mfma_f32_16x16x32_f16 v[84:87], v[208:211], v[60:63], v[84:87]\n"
    "v_pk_mul_f16 v73, v50, v49 op_sel_hi:[0,1]\n"
    "v_pk_mul_f16 v74, v48, v50 op_sel_hi:[0,1]\n"
    "ds_write_b128 v15, v[28:31] offset:0\n"
    "ds_write_b128 v15, v[32:35] offset:144\n"
    "ds_write_b128 v15, v[36:39] offset:288\n"
    "v_mfma_f32_16x16x32_f16 v[84:87], v[220:223], v[64:67], v[84:87]\n"
    "v_pk_mul_f16 v75, v48, v51 op_sel_hi:[0,1]\n"
    "ds_write_b32 v16, v40 offset:3456\n"
    "ds_write_b32 v16, v12 offset:3744\n"
    "s_mov_b64 exec, 1\n"
    "ds_or_b32 v25, v21 offset:532\n"
    "s_mov_b64 exec, -1\n"
    "v_smfmac_f32_16x16x64_f16 v[88:91], v[224:227], v[60:67], v2\n"
    "ds_read_b64_tr_b16 v[48:49], v3 offset:0\n"
    "ds_read_b64_tr_b16 v[50:51], v4 offset:576\n"
    "ds_read_b64_tr_b16 v[52:53], v4 offset:1152\n"
    "v_smfmac_f32_16x16x64_f16 v[88:91], v[252:255], v[68:75], v2\n"
    "s_waitcnt lgkmcnt(12)\n"
    "v_pk_mul_f16 v60, v44, s45 op_sel:[0,1]\n"
    "v_pk_mul_f16 v61, v45, s45 op_sel:[0,1]\n"
    "v_mfma_f32_16x16x32_f16 v[76:79], v[228:231], v[68:71], v[76:79]\n"
    "v_pk_mul_f16 v62, v46, s45 op_sel:[0,1]\n"
    "v_pk_mul_f16 v63, v47, s45 op_sel:[0,1]\n"
    "v_mfma_f32_16x16x32_f16 v[76:79], v[240:243], v[72:75], v[76:79]\n"
    "v_pk_mul_f16 v64, v44, v46\n"
    "v_pk_mul_f16 v65, v45, v47\n"
    "v_mfma_f32_16x16x32_f16 v[80:83], v[244:247], v[72:75], v[80:83]\n"
    "v_add_f32 v11, v88, v89\n"
    "v_add_f32 v13, v90, v91\n"
    "v_mfma_f32_16x16x32_f16 v[80:83], v[232:235], v[68:71], v[80:83]\n"
    "v_mov_b64 v[88:89], 0\n"
    "v_mov_b64 v[90:91], 0\n"
    "v_mfma_f32_16x16x32_f16 v[84:87], v[236:239], v[68:71], v[84:87]\n"
    "s_waitcnt lgkmcnt(0)\n"
    "v_pk_mul_f16 v66, v48, v48 op_sel_hi:[0,1]\n"
    "v_pk_mul_f16 v67, v48, v49 op_sel_hi:[0,1]\n"
    "v_mfma_f32_16x16x32_f16 v[84:87], v[248:251], v[72:75], v[84:87]\n"
    "v_mul_f32 v11, v11, v13\n"
    "v_add_u32 v25, 16, v25\n"
    "s_cmp_eq_u32 s45, -1\n"
    "s_cbranch_scc1 .Lxd_ex3\n"
    "s_cmp_eq_u32 s36, 0\n"
    "s_cbranch_scc1 .Lxd_ph0\n"
    ".Lxd_pl0:\n"
    "s_cbranch_vccnz .Lxd_pr26\n"
    "s_setprio 2\n"
    ".Lxd_pr26:\n"
    "v_smfmac_f32_16x16x64_f16 v[88:91], v[112:115], v[60:67], v2\n"
    "v_fmac_f32 v11, v76, v80\n"
    "v_fmac_f32 v11, v77, v81\n"
    "v_fmac_f32 v11, v78, v82\n"
    "v_fmac_f32 v11, v79, v83\n"
    "v_mfma_f32_16x16x32_f16 v[80:83], v[92:95], v[60:63], 0\n"
    "v_pk_mul_f16 v68, v58, v58 op_sel_hi:[0,1]\n"
    "v_pk_mul_f16 v69, v58, v49 op_sel_hi:[0,1]\n"
    "v_mfma_f32_16x16x32_f16 v[80:83], v[104:107], v[64:67], v[80:83]\n"
    "v_pk_mul_f16 v70, v48, v58 op_sel_hi:[0,1]\n"
    "v_pk_mul_f16 v71, v48, v59 op_sel_hi:[0,1]\n"
    "v_mfma_f32_16x16x32_f16 v[76:79], v[100:103], v[64:67], 0\n"
    "v_fmac_f32 v11, v84, v85\n"
    "v_fma_f32 v10, v86, v87, v11\n"
    "v_mfma_f32_16x16x32_f16 v[84:87], v[108:111], v[64:67], 0\n"
    "v_pk_mul_f16 v72, v50, v58 op_sel_hi:[0,1]\n"
    "v_pk_mul_f16 v73, v50, v59 op_sel_hi:[0,1]\n"
    "v_mfma_f32_16x16x32_f16 v[84:87], v[96:99], v[60:63], v[84:87]\n"
    "v_pk_mul_f16 v74, v52, v58 op_sel_hi:[0,1]\n"
    "v_pk_mul_f16 v75, v52, v59 op_sel_hi:[0,1]\n"
    "v_mfma_f32_16x16x32_f16 v[76:79], v[116:119], v[68:71], v[76:79]\n"
    "v_pk_mul_f16 v60, v54, v58 op_sel_hi:[0,1]\n"
    "v_pk_mul_f16 v61, v54, v59 op_sel_hi:[0,1]\n"
    "v_mfma_f32_16x16x32_f16 v[76:79], v[128:131], v[72:75], v[76:79]\n"
    "v_pk_mul_f16 v62, v56, v58 op_sel_hi:[0,1]\n"
    "v_mfma_f32_16x16x32_f16 v[80:83], v[132:135], v[72:75], v[80:83]\n"
    "v_pk_mul_f16 v63, v56, v59 op_sel_hi:[0,1]\n"
    "v_mfma_f32_16x16x32_f16 v[80:83], v[120:123], v[68:71], v[80:83]\n"
    "v_pk_mul_f16 v64, v56, v56 op_sel_hi:[0,1]\n"
    "v_mfma_f32_16x16x32_f16 v[84:87], v[124:127], v[68:71], v[84:87]\n"
    "v_pk_mul_f16 v65, v56, v49 op_sel_hi:[0,1]\n"
    "v_mfma_f32_16x16x32_f16 v[84:87], v[136:139], v[72:75], v[84:87]\n"
    "v_pk_mul_f16 v66, v48, v56 op_sel_hi:[0,1]\n"
    "v_permlane32_swap_b32 v7, v8\n"
    "v_permlane32_swap_b32 v9, v10\n"
    "v_smfmac_f32_16x16x64_f16 v[88:91], v[140:143], v[68:75], v2\n"
    "v_pk_mul_f16 v67, v48, v57 op_sel_hi:[0,1]\n"
    "v_mul_f32 v7, v0, v7\n"
    "v_mul_f32 v9, v0, v9\n"
    "v_mfma_f32_16x16x32_f16 v[76:79], v[144:147], v[60:63], v[76:79]\n"
    "v_pk_mul_f16 v68, v50, v56 op_sel_hi:[0,1]\n"
    "v_pk_mul_f16 v69, v50, v57 op_sel_hi:[0,1]\n"
    "v_mfma_f32_16x16x32_f16 v[76:79], v[156:159], v[64:67], v[76:79]\n"
    "v_pk_mul_f16 v70, v52, v56 op_sel_hi:[0,1]\n"
    "v_fmac_f32 v7, v23, v8\n"
    "v_fmac_f32 v9, v23, v10\n"
    "v_mfma_f32_16x16x32_f16 v[80:83], v[160:163], v[64:67], v[80:83]\n"
    "v_pk_mul_f16 v71, v52, v57 op_sel_hi:[0,1]\n"
    "v_permlane16_swap_b32 v7, v9\n"
    "v_mfma_f32_16x16x32_f16 v[80:83], v[148:151], v[60:63], v[80:83]\n"
    "v_pk_mul_f16 v72, v54, v56 op_sel_hi:[0,1]\n"
    "ds_read_b32 v22, v25 offset:516\n"
    "v_mfma_f32_16x16x32_f16 v[84:87], v[152:155], v[60:63], v[84:87]\n"
    "v_pk_mul_f16 v73, v54, v57 op_sel_hi:[0,1]\n"
    "v_add_f32 v7, v7, v9\n"
    "v_mfma_f32_16x16x32_f16 v[84:87], v[164:167], v[64:67], v[84:87]\n"
    "v_pk_mul_f16 v74, v54, v54 op_sel_hi:[0,1]\n"
    "v_add_f32_dpp v7, v7, v7 quad_perm:[1,0,3,2] row_mask:0xf bank_mask:0xf\n"
    "v_smfmac_f32_16x16x64_f16 v[88:91], v[168:171], v[60:67], v2\n"
    "v_pk_mul_f16 v75, v54, v49 op_sel_hi:[0,1]\n"
    "v_add_f32_dpp v7, v7, v7 quad_perm:[2,3,0,1] row_mask:0xf bank_mask:0xf\n"
    "v_mfma_f32_16x16x32_f16 v[76:79], v[172:175], v[68:71], v[76:79]\n"
    "v_pk_mul_f16 v60, v48, v54 op_sel_hi:[0,1]\n"
    "v_pk_mul_f16 v61, v48, v55 op_sel_hi:[0,1]\n"
    "v_mfma_f32_16x16x32_f16 v[76:79], v[184:187], v[72:75], v[76:79]\n"
    "v_pk_mul_f16 v62, v50, v54 op_sel_hi:[0,1]\n"
    "v_add_f32_dpp v7, v7, v7 row_half_mirror row_mask:0xf bank_mask:0xf\n"
    "v_mfma_f32_16x16x32_f16 v[80:83], v[188:191], v[72:75], v[80:83]\n"
    "v_pk_mul_f16 v63, v50, v55 op_sel_hi:[0,1]\n"
    "s_waitcnt lgkmcnt(0)\n"
    "v_readfirstlane_b32 s45, v22\n"
    "s_cmp_lt_i32 s45, 16\n"
    "s_cbranch_scc1 .Lxd_ck28\n"
    ".Lxd_ck28r:\n"
    "v_mfma_f32_16x16x32_f16 v[80:83], v[176:179], v[68:71], v[80:83]\n"
    "v_pk_mul_f16 v64, v52, v54 op_sel_hi:[0,1]\n"
    "ds_read_b64_tr_b16 v[44:45], v5 offset:4096\n"
    "v_mfma_f32_16x16x32_f16 v[84:87], v[180:183], v[68:71], v[84:87]\n"
    "v_pk_mul_f16 v65, v52, v55 op_sel_hi:[0,1]\n"
    "ds_read_b64_tr_b16 v[46:47], v6 offset:4096\n"
    "v_mfma_f32_16x16x32_f16 v[84:87], v[192:195], v[72:75], v[84:87]\n"
    "v_pk_mul_f16 v66, v52, v52 op_sel_hi:[0,1]\n"
    "ds_read_b64_tr_b16 v[58:59], v4 offset:6976\n"
    "s_cbranch_vccnz .Lxd_pr27\n"
    "s_setprio 0\n"
    ".Lxd_pr27:\n"
    "v_smfmac_f32_16x16x64_f16 v[88:91], v[196:199], v[68:75], v2\n"
    "v_pk_mul_f16 v67, v52, v49 op_sel_hi:[0,1]\n"
    "ds_read_b64_tr_b16 v[56:57], v4 offset:6400\n"
    "v_mfma_f32_16x16x32_f16 v[76:79], v[200:203], v[60:63], v[76:79]\n"
    "v_pk_mul_f16 v68, v48, v52 op_sel_hi:[0,1]\n"
    "v_pk_mul_f16 v69, v48, v53 op_sel_hi:[0,1]\n"
    "v_mfma_f32_16x16x32_f16 v[76:79], v[212:215], v[64:67], v[76:79]\n"
    "v_pk_mul_f16 v70, v50, v52 op_sel_hi:[0,1]\n"
    "ds_read_b64_tr_b16 v[54:55], v4 offset:5824\n"
    "v_mfma_f32_16x16x32_f16 v[80:83], v[216:219], v[64:67], v[80:83]\n"
    "v_pk_mul_f16 v71, v50, v53 op_sel_hi:[0,1]\n"
    "v_add_f32_dpp v7, v7, v7 row_mirror row_mask:0xf bank_mask:0xf\n"
    "v_mfma_f32_16x16x32_f16 v[80:83], v[204:207], v[60:63], v[80:83]\n"
    "v_pk_mul_f16 v72, v50, v50 op_sel_hi:[0,1]\n"
    "ds_write_b32 v14, v7\n"
    "v_add_u32 v14, 64, v14\n"
    "v_mfma_f32_16x16x32_f16 v[84:87], v[208:211], v[60:63], v[84:87]\n"
    "v_pk_mul_f16 v73, v50, v49 op_sel_hi:[0,1]\n"
    "v_pk_mul_f16 v74, v48, v50 op_sel_hi:[0,1]\n"
    "v_mfma_f32_16x16x32_f16 v[84:87], v[220:223], v[64:67], v[84:87]\n"
    "v_pk_mul_f16 v75, v48, v51 op_sel_hi:[0,1]\n"
    "v_smfmac_f32_16x16x64_f16 v[88:91], v[224:227], v[60:67], v2\n"
    "ds_read_b64_tr_b16 v[48:49], v3 offset:4096\n"
    "ds_read_b64_tr_b16 v[50:51], v4 offset:4672\n"
    "ds_read_b64_tr_b16 v[52:53], v4 offset:5248\n"
    "v_smfmac_f32_16x16x64_f16 v[88:91], v[252:255], v[68:75], v2\n"
    "s_waitcnt lgkmcnt(7)\n"
    "v_pk_mul_f16 v60, v44, s45 op_sel:[0,1]\n"
    "v_pk_mul_f16 v61, v45, s45 op_sel:[0,1]\n"
    "v_mfma_f32_16x16x32_f16 v[76:79], v[228:231], v[68:71], v[76:79]\n"
    "v_pk_mul_f16 v62, v46, s45 op_sel:[0,1]\n"
    "v_pk_mul_f16 v63, v47, s45 op_sel:[0,1]\n"
    "v_mfma_f32_16x16x32_f16 v[76:79], v[240:243], v[72:75], v[76:79]\n"
    "v_pk_mul_f16 v64, v44, v46\n"
    "v_pk_mul_f16 v65, v45, v47\n"
    "v_mfma_f32_16x16x32_f16 v[80:83], v[244:247], v[72:75], v[80:83]\n"
    "v_add_f32 v11, v88, v89\n"
    "v_add_f32 v13, v90, v91\n"
    "v_mfma_f32_16x16x32_f16 v[80:83], v[232:235], v[68:71], v[80:83]\n"
    "v_mov_b64 v[88:89], 0\n"
    "v_mov_b64 v[90:91], 0\n"
    "v_mfma_f32_16x16x32_f16 v[84:87], v[236:239], v[68:71], v[84:87]\n"
    "s_waitcnt lgkmcnt(0)\n"
    "v_pk_mul_f16 v66, v48, v48 op_sel_hi:[0,1]\n"
    "v_pk_mul_f16 v67, v48, v49 op_sel_hi:[0,1]\n"
    "v_mfma_f32_16x16x32_f16 v[84:87], v[248:251], v[72:75], v[84:87]\n"
    "v_mul_f32 v11, v11, v13\n"
    "s_cmp_eq_u32 s45, -1\n"
    "s_cbranch_scc1 .Lxd_ex0\n"
    ".Lxd_pl1:\n"
    "s_cbranch_vccnz .Lxd_pr29\n"
    "s_setprio 2\n"
    ".Lxd_pr29:\n"
    "v_smfmac_f32_16x16x64_f16 v[88:91], v[112:115], v[60:67], v2\n"
    "v_fmac_f32 v11, v76, v80\n"
    "v_fmac_f32 v11, v77, v81\n"
    "v_fmac_f32 v11, v78, v82\n"
    "v_fmac_f32 v11, v79, v83\n"
    "v_mfma_f32_16x16x32_f16 v[80:83], v[92:95], v[60:63], 0\n"
    "v_pk_mul_f16 v68, v58, v58 op_sel_hi:[0,1]\n"
    "v_pk_mul_f16 v69, v58, v49 op_sel_hi:[0,1]\n"
    "v_mfma_f32_16x16x32_f16 v[80:83], v[104:107], v[64:67], v[80:83]\n"
    "v_pk_mul_f16 v70, v48, v58 op_sel_hi:[0,1]\n"
    "v_pk_mul_f16 v71, v48, v59 op_sel_hi:[0,1]\n"
    "v_mfma_f32_16x16x32_f16 v[76:79], v[100:103], v[64:67], 0\n"
    "v_fmac_f32 v11, v84, v85\n"
    "v_fma_f32 v7, v86, v87, v11\n"
    "v_mfma_f32_16x16x32_f16 v[84:87], v[108:111], v[64:67], 0\n"
    "v_pk_mul_f16 v72, v50, v58 op_sel_hi:[0,1]\n"
    "v_pk_mul_f16 v73, v50, v59 op_sel_hi:[0,1]\n"
    "v_mfma_f32_16x16x32_f16 v[84:87], v[96:99], v[60:63], v[84:87]\n"
    "v_pk_mul_f16 v74, v52, v58 op_sel_hi:[0,1]\n"
    "v_pk_mul_f16 v75, v52, v59 op_sel_hi:[0,1]\n"
    "v_mfma_f32_16x16x32_f16 v[76:79], v[116:119], v[68:71], v[76:79]\n"
    "v_pk_mul_f16 v60, v54, v58 op_sel_hi:[0,1]\n"
    "v_pk_mul_f16 v61, v54, v59 op_sel_hi:[0,1]\n"
    "v_mfma_f32_16x16x32_f16 v[76:79], v[128:131], v[72:75], v[76:79]\n"
    "v_pk_mul_f16 v62, v56, v58 op_sel_hi:[0,1]\n"
    "v_mfma_f32_16x16x32_f16 v[80:83], v[132:135], v[72:75], v[80:83]\n"
    "v_pk_mul_f16 v63, v56, v59 op_sel_hi:[0,1]\n"
    "v_mfma_f32_16x16x32_f16 v[80:83], v[120:123], v[68:71], v[80:83]\n"
    "v_pk_mul_f16 v64, v56, v56 op_sel_hi:[0,1]\n"
    "v_mfma_f32_16x16x32_f16 v[84:87], v[124:127], v[68:71], v[84:87]\n"
    "v_pk_mul_f16 v65, v56, v49 op_sel_hi:[0,1]\n"
    "v_mfma_f32_16x16x32_f16 v[84:87], v[136:139], v[72:75], v[84:87]\n"
    "v_pk_mul_f16 v66, v48, v56 op_sel_hi:[0,1]\n"
    "v_smfmac_f32_16x16x64_f16 v[88:91], v[140:143], v[68:75], v2\n"
    "v_pk_mul_f16 v67, v48, v57 op_sel_hi:[0,1]\n"
    "v_mfma_f32_16x16x32_f16 v[76:79], v[144:147], v[60:63], v[76:79]\n"
    "v_pk_mul_f16 v68, v50, v56 op_sel_hi:[0,1]\n"
    "v_pk_mul_f16 v69, v50, v57 op_sel_hi:[0,1]\n"
    "v_mfma_f32_16x16x32_f16 v[76:79], v[156:159], v[64:67], v[76:79]\n"
    "v_pk_mul_f16 v70, v52, v56 op_sel_hi:[0,1]\n"
    "v_mfma_f32_16x16x32_f16 v[80:83], v[160:163], v[64:67], v[80:83]\n"
    "v_pk_mul_f16 v71, v52, v57 op_sel_hi:[0,1]\n"
    "v_mfma_f32_16x16x32_f16 v[80:83], v[148:151], v[60:63], v[80:83]\n"
    "v_pk_mul_f16 v72, v54, v56 op_sel_hi:[0,1]\n"
    "ds_read_b32 v22, v25 offset:520\n"
    "v_mfma_f32_16x16x32_f16 v[84:87], v[152:155], v[60:63], v[84:87]\n"
    "v_pk_mul_f16 v73, v54, v57 op_sel_hi:[0,1]\n"
    "v_mfma_f32_16x16x32_f16 v[84:87], v[164:167], v[64:67], v[84:87]\n"
    "v_pk_mul_f16 v74, v54, v54 op_sel_hi:[0,1]\n"
    "v_smfmac_f32_16x16x64_f16 v[88:91], v[168:171], v[60:67], v2\n"
    "v_pk_mul_f16 v75, v54, v49 op_sel_hi:[0,1]\n"
    "v_mfma_f32_16x16x32_f16 v[76:79], v[172:175], v[68:71], v[76:79]\n"
    "v_pk_mul_f16 v60, v48, v54 op_sel_hi:[0,1]\n"
    "v_pk_mul_f16 v61, v48, v55 op_sel_hi:[0,1]\n"
    "v_mfma_f32_16x16x32_f16 v[76:79], v[184:187], v[72:75], v[76:79]\n"
    "v_pk_mul_f16 v62, v50, v54 op_sel_hi:[0,1]\n"
    "v_mfma_f32_16x16x32_f16 v[80:83], v[188:191], v[72:75], v[80:83]\n"
    "v_pk_mul_f16 v63, v50, v55 op_sel_hi:[0,1]\n"
    "s_waitcnt lgkmcnt(0)\n"
    "v_readfirstlane_b32 s45, v22\n"
    "s_cmp_lt_i32 s45, 16\n"
    "s_cbranch_scc1 .Lxd_ck31\n"
    ".Lxd_ck31r:\n"
    "v_mfma_f32_16x16x32_f16 v[80:83], v[176:179], v[68:71], v[80:83]\n"
    "v_pk_mul_f16 v64, v52, v54 op_sel_hi:[0,1]\n"
    "ds_read_b64_tr_b16 v[44:45], v5 offset:8192\n"
    "v_mfma_f32_16x16x32_f16 v[84:87], v[180:183], v[68:71], v[84:87]\n"
    "v_pk_mul_f16 v65, v52, v55 op_sel_hi:[0,1]\n"
    "ds_read_b64_tr_b16 v[46:47], v6 offset:8192\n"
    "v_mfma_f32_16x16x32_f16 v[84:87], v[192:195], v[72:75], v[84:87]\n"
    "v_pk_mul_f16 v66, v52, v52 op_sel_hi:[0,1]\n"
    "ds_read_b64_tr_b16 v[58:59], v4 offset:11072\n"
    "s_cbranch_vccnz .Lxd_pr30\n"
    "s_setprio 0\n"
    ".Lxd_pr30:\n"
    "v_smfmac_f32_16x16x64_f16 v[88:91], v[196:199], v[68:75], v2\n"
    "v_pk_mul_f16 v67, v52, v49 op_sel_hi:[0,1]\n"
    "ds_read_b64_tr_b16 v[56:57], v4 offset:10496\n"
    "v_mfma_f32_16x16x32_f16 v[76:79], v[200:203], v[60:63], v[76:79]\n"
    "v_pk_mul_f16 v68, v48, v52 op_sel_hi:[0,1]\n"
    "v_pk_mul_f16 v69, v48, v53 op_sel_hi:[0,1]\n"
    "v_mfma_f32_16x16x32_f16 v[76:79], v[212:215], v[64:67], v[76:79]\n"
    "v_pk_mul_f16 v70, v50, v52 op_sel_hi:[0,1]\n"
    "ds_read_b64_tr_b16 v[54:55], v4 offset:9920\n"
    "v_mfma_f32_16x16x32_f16 v[80:83], v[216:219], v[64:67], v[80:83]\n"
    "v_pk_mul_f16 v71, v50, v53 op_sel_hi:[0,1]\n"
    "v_mfma_f32_16x16x32_f16 v[80:83], v[204:207], v[60:63], v[80:83]\n"
    "v_pk_mul_f16 v72, v50, v50 op_sel_hi:[0,1]\n"
    "v_mfma_f32_16x16x32_f16 v[84:87], v[208:211], v[60:63], v[84:87]\n"
    "v_pk_mul_f16 v73, v50, v49 op_sel_hi:[0,1]\n"
    "v_pk_mul_f16 v74, v48, v50 op_sel_hi:[0,1]\n"
    "v_mfma_f32_16x16x32_f16 v[84:87], v[220:223], v[64:67], v[84:87]\n"
    "v_pk_mul_f16 v75, v48, v51 op_sel_hi:[0,1]\n"
    "v_smfmac_f32_16x16x64_f16 v[88:91], v[224:227], v[60:67], v2\n"
    "ds_read_b64_tr_b16 v[48:49], v3 offset:8192\n"
    "ds_read_b64_tr_b16 v[50:51], v4 offset:8768\n"
    "ds_read_b64_tr_b16 v[52:53], v4 offset:9344\n"
    "v_smfmac_f32_16x16x64_f16 v[88:91], v[252:255], v[68:75], v2\n"
    "s_waitcnt lgkmcnt(6)\n"
    "v_pk_mul_f16 v60, v44, s45 op_sel:[0,1]\n"
    "v_pk_mul_f16 v61, v45, s45 op_sel:[0,1]\n"
    "v_mfma_f32_16x16x32_f16 v[76:79], v[228:231], v[68:71], v[76:79]\n"
    "v_pk_mul_f16 v62, v46, s45 op_sel:[0,1]\n"
    "v_pk_mul_f16 v63, v47, s45 op_sel:[0,1]\n"
    "v_mfma_f32_16x16x32_f16 v[76:79], v[240:243], v[72:75], v[76:79]\n"
    "v_pk_mul_f16 v64, v44, v46\n"
    "v_pk_mul_f16 v65, v45, v47\n"
    "v_mfma_f32_16x16x32_f16 v[80:83], v[244:247], v[72:75], v[80:83]\n"
    "v_add_f32 v11, v88, v89\n"
    "v_add_f32 v13, v90, v91\n"
    "v_mfma_f32_16x16x32_f16 v[80:83], v[232:235], v[68:71], v[80:83]\n"
    "v_mov_b64 v[88:89], 0\n"
    "v_mov_b64 v[90:91], 0\n"
    "v_mfma_f32_16x16x32_f16 v[84:87], v[236:239], v[68:71], v[84:87]\n"
    "s_waitcnt lgkmcnt(0)\n"
    "v_pk_mul_f16 v66, v48, v48 op_sel_hi:[0,1]\n"
    "v_pk_mul_f16 v67, v48, v49 op_sel_hi:[0,1]\n"
    "v_mfma_f32_16x16x32_f16 v[84:87], v[248:251], v[72:75], v[84:87]\n"
    "v_mul_f32 v11, v11, v13\n"
    "s_cmp_eq_u32 s45, -1\n"
    "s_cbranch_scc1 .Lxd_ex1\n"
    ".Lxd_pl2:\n"
    "s_cbranch_vccnz .Lxd_pr32\n"
    "s_setprio 2\n"
    ".Lxd_pr32:\n"
    "v_smfmac_f32_16x16x64_f16 v[88:91], v[112:115], v[60:67], v2\n"
    "v_fmac_f32 v11, v76, v80\n"
    "v_fmac_f32 v11, v77, v81\n"
    "v_fmac_f32 v11, v78, v82\n"
    "v_fmac_f32 v11, v79, v83\n"
    "v_mfma_f32_16x16x32_f16 v[80:83], v[92:95], v[60:63], 0\n"
    "v_pk_mul_f16 v68, v58, v58 op_sel_hi:[0,1]\n"
    "v_pk_mul_f16 v69, v58, v49 op_sel_hi:[0,1]\n"
    "v_mfma_f32_16x16x32_f16 v[80:83], v[104:107], v[64:67], v[80:83]\n"
    "v_pk_mul_f16 v70, v48, v58 op_sel_hi:[0,1]\n"
    "v_pk_mul_f16 v71, v48, v59 op_sel_hi:[0,1]\n"
    "v_mfma_f32_16x16x32_f16 v[76:79], v[100:103], v[64:67], 0\n"
    "v_fmac_f32 v11, v84, v85\n"
    "v_fma_f32 v8, v86, v87, v11\n"
    "v_mfma_f32_16x16x32_f16 v[84:87], v[108:111], v[64:67], 0\n"
    "v_pk_mul_f16 v72, v50, v58 op_sel_hi:[0,1]\n"
    "v_pk_mul_f16 v73, v50, v59 op_sel_hi:[0,1]\n"
    "v_mfma_f32_16x16x32_f16 v[84:87], v[96:99], v[60:63], v[84:87]\n"
    "v_pk_mul_f16 v74, v52, v58 op_sel_hi:[0,1]\n"
    "v_pk_mul_f16 v75, v52, v59 op_sel_hi:[0,1]\n"
    "v_mfma_f32_16x16x32_f16 v[76:79], v[116:119], v[68:71], v[76:79]\n"
    "v_pk_mul_f16 v60, v54, v58 op_sel_hi:[0,1]\n"
    "v_pk_mul_f16 v61, v54, v59 op_sel_hi:[0,1]\n"
    "v_mfma_f32_16x16x32_f16 v[76:79], v[128:131], v[72:75], v[76:79]\n"
    "v_pk_mul_f16 v62, v56, v58 op_sel_hi:[0,1]\n"
    "v_mfma_f32_16x16x32_f16 v[80:83], v[132:135], v[72:75], v[80:83]\n"
    "v_pk_mul_f16 v63, v56, v59 op_sel_hi:[0,1]\n"
    "v_mfma_f32_16x16x32_f16 v[80:83], v[120:123], v[68:71], v[80:83]\n"
    "v_pk_mul_f16 v64, v56, v56 op_sel_hi:[0,1]\n"
    "v_mfma_f32_16x16x32_f16 v[84:87], v[124:127], v[68:71], v[84:87]\n"
    "v_pk_mul_f16 v65, v56, v49 op_sel_hi:[0,1]\n"
    "v_mfma_f32_16x16x32_f16 v[84:87], v[136:139], v[72:75], v[84:87]\n"
    "v_pk_mul_f16 v66, v48, v56 op_sel_hi:[0,1]\n"
    "v_smfmac_f32_16x16x64_f16 v[88:91], v[140:143], v[68:75], v2\n"
    "v_pk_mul_f16 v67, v48, v57 op_sel_hi:[0,1]\n"
    "v_mfma_f32_16x16x32_f16 v[76:79], v[144:147], v[60:63], v[76:79]\n"
    "v_pk_mul_f16 v68, v50, v56 op_sel_hi:[0,1]\n"
    "v_pk_mul_f16 v69, v50, v57 op_sel_hi:[0,1]\n"
    "v_mfma_f32_16x16x32_f16 v[76:79], v[156:159], v[64:67], v[76:79]\n"
    "v_pk_mul_f16 v70, v52, v56 op_sel_hi:[0,1]\n"
    "v_mfma_f32_16x16x32_f16 v[80:83], v[160:163], v[64:67], v[80:83]\n"
    "v_pk_mul_f16 v71, v52, v57 op_sel_hi:[0,1]\n"
    "v_mfma_f32_16x16x32_f16 v[80:83], v[148:151], v[60:63], v[80:83]\n"
    "v_pk_mul_f16 v72, v54, v56 op_sel_hi:[0,1]\n"
    "ds_read_b32 v22, v25 offset:524\n"
    "v_mfma_f32_16x16x32_f16 v[84:87], v[152:155], v[60:63], v[84:87]\n"
    "v_pk_mul_f16 v73, v54, v57 op_sel_hi:[0,1]\n"
    "v_mfma_f32_16x16x32_f16 v[84:87], v[164:167], v[64:67], v[84:87]\n"
    "v_pk_mul_f16 v74, v54, v54 op_sel_hi:[0,1]\n"
    "v_smfmac_f32_16x16x64_f16 v[88:91], v[168:171], v[60:67], v2\n"
    "v_pk_mul_f16 v75, v54, v49 op_sel_hi:[0,1]\n"
    "v_mfma_f32_16x16x32_f16 v[76:79], v[172:175], v[68:71], v[76:79]\n"
    "v_pk_mul_f16 v60, v48, v54 op_sel_hi:[0,1]\n"
    "v_pk_mul_f16 v61, v48, v55 op_sel_hi:[0,1]\n"
    "v_mfma_f32_16x16x32_f16 v[76:79], v[184:187], v[72:75], v[76:79]\n"
    "v_pk_mul_f16 v62, v50, v54 op_sel_hi:[0,1]\n"
    "v_mfma_f32_16x16x32_f16 v[80:83], v[188:191], v[72:75], v[80:83]\n"
    "v_pk_mul_f16 v63, v50, v55 op_sel_hi:[0,1]\n"
    "s_waitcnt lgkmcnt(0)\n"
    "v_readfirstlane_b32 s45, v22\n"
    "s_cmp_lt_i32 s45, 16\n"
    "s_cbranch_scc1 .Lxd_ck34\n"
    ".Lxd_ck34r:\n"
    "v_mfma_f32_16x16x32_f16 v[80:83], v[176:179], v[68:71], v[80:83]\n"
    "v_pk_mul_f16 v64, v52, v54 op_sel_hi:[0,1]\n"
    "ds_read_b64_tr_b16 v[44:45], v5 offset:12288\n"
    "v_mfma_f32_16x16x32_f16 v[84:87], v[180:183], v[68:71], v[84:87]\n"
    "v_pk_mul_f16 v65, v52, v55 op_sel_hi:[0,1]\n"
    "ds_read_b64_tr_b16 v[46:47], v6 offset:12288\n"
    "v_mfma_f32_16x16x32_f16 v[84:87], v[192:195], v[72:75], v[84:87]\n"
    "v_pk_mul_f16 v66, v52, v52 op_sel_hi:[0,1]\n"
    "ds_read_b64_tr_b16 v[58:59], v4 offset:15168\n"
    "s_cbranch_vccnz .Lxd_pr33\n"
    "s_setprio 0\n"
    ".Lxd_pr33:\n"
    "v_smfmac_f32_16x16x64_f16 v[88:91], v[196:199], v[68:75], v2\n"
    "v_pk_mul_f16 v67, v52, v49 op_sel_hi:[0,1]\n"
    "ds_read_b64_tr_b16 v[56:57], v4 offset:14592\n"
    "v_mfma_f32_16x16x32_f16 v[76:79], v[200:203], v[60:63], v[76:79]\n"
    "v_pk_mul_f16 v68, v48, v52 op_sel_hi:[0,1]\n"
    "v_pk_mul_f16 v69, v48, v53 op_sel_hi:[0,1]\n"
    "v_mfma_f32_16x16x32_f16 v[76:79], v[212:215], v[64:67], v[76:79]\n"
    "v_pk_mul_f16 v70, v50, v52 op_sel_hi:[0,1]\n"
    "ds_read_b64_tr_b16 v[54:55], v4 offset:14016\n"
    "v_mfma_f32_16x16x32_f16 v[80:83], v[216:219], v[64:67], v[80:83]\n"
    "v_pk_mul_f16 v71, v50, v53 op_sel_hi:[0,1]\n"
    "v_mfma_f32_16x16x32_f16 v[80:83], v[204:207], v[60:63], v[80:83]\n"
    "v_pk_mul_f16 v72, v50, v50 op_sel_hi:[0,1]\n"
    "v_mfma_f32_16x16x32_f16 v[84:87], v[208:211], v[60:63], v[84:87]\n"
    "v_pk_mul_f16 v73, v50, v49 op_sel_hi:[0,1]\n"
    "v_pk_mul_f16 v74, v48, v50 op_sel_hi:[0,1]\n"
    "v_mfma_f32_16x16x32_f16 v[84:87], v[220:223], v[64:67], v[84:87]\n"
    "v_pk_mul_f16 v75, v48, v51 op_sel_hi:[0,1]\n"
    "v_smfmac_f32_16x16x64_f16 v[88:91], v[224:227], v[60:67], v2\n"
    "ds_read_b64_tr_b16 v[48:49], v3 offset:12288\n"
    "ds_read_b64_tr_b16 v[50:51], v4 offset:12864\n"
    "ds_read_b64_tr_b16 v[52:53], v4 offset:13440\n"
    "v_smfmac_f32_16x16x64_f16 v[88:91], v[252:255], v[68:75], v2\n"
    "s_waitcnt lgkmcnt(6)\n"
    "v_pk_mul_f16 v60, v44, s45 op_sel:[0,1]\n"
    "v_pk_mul_f16 v61, v45, s45 op_sel:[0,1]\n"
    "v_mfma_f32_16x16x32_f16 v[76:79], v[228:231], v[68:71], v[76:79]\n"
    "v_pk_mul_f16 v62, v46, s45 op_sel:[0,1]\n"
    "v_pk_mul_f16 v63, v47, s45 op_sel:[0,1]\n"
    "v_mfma_f32_16x16x32_f16 v[76:79], v[240:243], v[72:75], v[76:79]\n"
    "v_pk_mul_f16 v64, v44, v46\n"
    "v_pk_mul_f16 v65, v45, v47\n"
    "v_mfma_f32_16x16x32_f16 v[80:83], v[244:247], v[72:75], v[80:83]\n"
    "v_add_f32 v11, v88, v89\n"
    "v_add_f32 v13, v90, v91\n"
    "v_mfma_f32_16x16x32_f16 v[80:83], v[232:235], v[68:71], v[80:83]\n"
    "v_mov_b64 v[88:89], 0\n"
    "v_mov_b64 v[90:91], 0\n"
    "v_mfma_f32_16x16x32_f16 v[84:87], v[236:239], v[68:71], v[84:87]\n"
    "s_waitcnt lgkmcnt(0)\n"
    "v_pk_mul_f16 v66, v48, v48 op_sel_hi:[0,1]\n"
    "v_pk_mul_f16 v67, v48, v49 op_sel_hi:[0,1]\n"
    "v_mfma_f32_16x16x32_f16 v[84:87], v[248:251], v[72:75], v[84:87]\n"
    "v_mul_f32 v11, v11, v13\n"
    "s_cmp_eq_u32 s45, -1\n"
    "s_cbranch_scc1 .Lxd_ex2\n"
    ".Lxd_pl3:\n"
    "s_cbranch_vccnz .Lxd_pr35\n"
    "s_setprio 2\n"
    ".Lxd_pr35:\n"
    "v_smfmac_f32_16x16x64_f16 v[88:91], v[112:115], v[60:67], v2\n"
    "v_fmac_f32 v11, v76, v80\n"
    "v_fmac_f32 v11, v77, v81\n"
    "v_fmac_f32 v11, v78, v82\n"
    "v_fmac_f32 v11, v79, v83\n"
    "v_mfma_f32_16x16x32_f16 v[80:83], v[92:95], v[60:63], 0\n"
    "v_pk_mul_f16 v68, v58, v58 op_sel_hi:[0,1]\n"
    "v_pk_mul_f16 v69, v58, v49 op_sel_hi:[0,1]\n"
    "v_mfma_f32_16x16x32_f16 v[80:83], v[104:107], v[64:67], v[80:83]\n"
    "v_pk_mul_f16 v70, v48, v58 op_sel_hi:[0,1]\n"
    "v_pk_mul_f16 v71, v48, v59 op_sel_hi:[0,1]\n"
    "v_mfma_f32_16x16x32_f16 v[76:79], v[100:103], v[64:67], 0\n"
    "v_fmac_f32 v11, v84, v85\n"
    "v_fma_f32 v9, v86, v87, v11\n"
    "v_mfma_f32_16x16x32_f16 v[84:87], v[108:111], v[64:67], 0\n"
    "v_pk_mul_f16 v72, v50, v58 op_sel_hi:[0,1]\n"
    "v_pk_mul_f16 v73, v50, v59 op_sel_hi:[0,1]\n"
    "v_mfma_f32_16x16x32_f16 v[84:87], v[96:99], v[60:63], v[84:87]\n"
    "v_pk_mul_f16 v74, v52, v58 op_sel_hi:[0,1]\n"
    "v_pk_mul_f16 v75, v52, v59 op_sel_hi:[0,1]\n"
    "v_mfma_f32_16x16x32_f16 v[76:79], v[116:119], v[68:71], v[76:79]\n"
    "v_pk_mul_f16 v60, v54, v58 op_sel_hi:[0,1]\n"
    "v_pk_mul_f16 v61, v54, v59 op_sel_hi:[0,1]\n"
    "v_mfma_f32_16x16x32_f16 v[76:79], v[128:131], v[72:75], v[76:79]\n"
    "v_pk_mul_f16 v62, v56, v58 op_sel_hi:[0,1]\n"
    "v_mfma_f32_16x16x32_f16 v[80:83], v[132:135], v[72:75], v[80:83]\n"
    "v_pk_mul_f16 v63, v56, v59 op_sel_hi:[0,1]\n"
    "v_xor_b32 v3, 0x4000, v3\n"
    "v_mfma_f32_16x16x32_f16 v[80:83], v[120:123], v[68:71], v[80:83]\n"
    "v_pk_mul_f16 v64, v56, v56 op_sel_hi:[0,1]\n"
    "v_xor_b32 v4, 0x4000, v4\n"
    "v_mfma_f32_16x16x32_f16 v[84:87], v[124:127], v[68:71], v[84:87]\n"
    "v_pk_mul_f16 v65, v56, v49 op_sel_hi:[0,1]\n"
    "v_xor_b32 v5, 0x4000, v5\n"
    "v_mfma_f32_16x16x32_f16 v[84:87], v[136:139], v[72:75], v[84:87]\n"
    "v_pk_mul_f16 v66, v48, v56 op_sel_hi:[0,1]\n"
    "v_xor_b32 v6, 0x4000, v6\n"
    "v_smfmac_f32_16x16x64_f16 v[88:91], v[140:143], v[68:75], v2\n"
    "v_pk_mul_f16 v67, v48, v57 op_sel_hi:[0,1]\n"
    "v_mfma_f32_16x16x32_f16 v[76:79], v[144:147], v[60:63], v[76:79]\n"
    "v_pk_mul_f16 v68, v50, v56 op_sel_hi:[0,1]\n"
    "v_pk_mul_f16 v69, v50, v57 op_sel_hi:[0,1]\n"
    "v_mfma_f32_16x16x32_f16 v[76:79], v[156:159], v[64:67], v[76:79]\n"
    "v_pk_mul_f16 v70, v52, v56 op_sel_hi:[0,1]\n"
    "v_mfma_f32_16x16x32_f16 v[80:83], v[160:163], v[64:67], v[80:83]\n"
    "v_pk_mul_f16 v71, v52, v57 op_sel_hi:[0,1]\n"
    "v_mfma_f32_16x16x32_f16 v[80:83], v[148:151], v[60:63], v[80:83]\n"
    "v_pk_mul_f16 v72, v54, v56 op_sel_hi:[0,1]\n"
    "ds_read_b32 v22, v25 offset:528\n"
    "v_mfma_f32_16x16x32_f16 v[84:87], v[152:155], v[60:63], v[84:87]\n"
    "v_pk_mul_f16 v73, v54, v57 op_sel_hi:[0,1]\n"
    "v_mfma_f32_16x16x32_f16 v[84:87], v[164:167], v[64:67], v[84:87]\n"
    "v_pk_mul_f16 v74, v54, v54 op_sel_hi:[0,1]\n"
    "v_smfmac_f32_16x16x64_f16 v[88:91], v[168:171], v[60:67], v2\n"
    "v_pk_mul_f16 v75, v54, v49 op_sel_hi:[0,1]\n"
    "v_mfma_f32_16x16x32_f16 v[76:79], v[172:175], v[68:71], v[76:79]\n"
    "v_pk_mul_f16 v60, v48, v54 op_sel_hi:[0,1]\n"
    "v_pk_mul_f16 v61, v48, v55 op_sel_hi:[0,1]\n"
    "v_mfma_f32_16x16x32_f16 v[76:79], v[184:187], v[72:75], v[76:79]\n"
    "v_pk_mul_f16 v62, v50, v54 op_sel_hi:[0,1]\n"
    "v_mfma_f32_16x16x32_f16 v[80:83], v[188:191], v[72:75], v[80:83]\n"
    "v_pk_mul_f16 v63, v50, v55 op_sel_hi:[0,1]\n"
    "s_waitcnt lgkmcnt(0)\n"
    "v_readfirstlane_b32 s45, v22\n"
    "s_cmp_lt_i32 s45, 16\n"
    "s_cbranch_scc1 .Lxd_ck37\n"
    ".Lxd_ck37r:\n"
    "v_mfma_f32_16x16x32_f16 v[80:83], v[176:179], v[68:71], v[80:83]\n"
    "v_pk_mul_f16 v64, v52, v54 op_sel_hi:[0,1]\n"
    "ds_read_b64_tr_b16 v[44:45], v5 offset:0\n"
    "s_cmp_eq_u32 s36, 0\n"
    "s_cbranch_scc1 .Lxd_on38\n"
    ".Lxd_on38r:\n"
    "v_mfma_f32_16x16x32_f16 v[84:87], v[180:183], v[68:71], v[84:87]\n"
    "v_pk_mul_f16 v65, v52, v55 op_sel_hi:[0,1]\n"
    "ds_read_b64_tr_b16 v[46:47], v6 offset:0\n"
    "v_mfma_f32_16x16x32_f16 v[84:87], v[192:195], v[72:75], v[84:87]\n"
    "v_pk_mul_f16 v66, v52, v52 op_sel_hi:[0,1]\n"
    "ds_read_b64_tr_b16 v[58:59], v4 offset:2880\n"
    "s_cbranch_vccnz .Lxd_pr36\n"
    "s_setprio 0\n"
    ".Lxd_pr36:\n"
    "v_smfmac_f32_16x16x64_f16 v[88:91], v[196:199], v[68:75], v2\n"
    "v_pk_mul_f16 v67, v52, v49 op_sel_hi:[0,1]\n"
    "ds_read_b64_tr_b16 v[56:57], v4 offset:2304\n"
    "v_mfma_f32_16x16x32_f16 v[76:79], v[200:203], v[60:63], v[76:79]\n"
    "v_pk_mul_f16 v68, v48, v52 op_sel_hi:[0,1]\n"
    "v_pk_mul_f16 v69, v48, v53 op_sel_hi:[0,1]\n"
    "v_mfma_f32_16x16x32_f16 v[76:79], v[212:215], v[64:67], v[76:79]\n"
    "v_pk_mul_f16 v70, v50, v52 op_sel_hi:[0,1]\n"
    "ds_read_b64_tr_b16 v[54:55], v4 offset:1728\n"
    "v_mfma_f32_16x16x32_f16 v[80:83], v[216:219], v[64:67], v[80:83]\n"
    "v_pk_mul_f16 v71, v50, v53 op_sel_hi:[0,1]\n"
    "v_mfma_f32_16x16x32_f16 v[80:83], v[204:207], v[60:63], v[80:83]\n"
    "v_pk_mul_f16 v72, v50, v50 op_sel_hi:[0,1]\n"
    "v_mfma_f32_16x16x32_f16 v[84:87], v[208:211], v[60:63], v[84:87]\n"
    "v_pk_mul_f16 v73, v50, v49 op_sel_hi:[0,1]\n"
    "v_pk_mul_f16 v74, v48, v50 op_sel_hi:[0,1]\n"
    "v_mfma_f32_16x16x32_f16 v[84:87], v[220:223], v[64:67], v[84:87]\n"
    "v_pk_mul_f16 v75, v48, v51 op_sel_hi:[0,1]\n"
    "v_smfmac_f32_16x16x64_f16 v[88:91], v[224:227], v[60:67], v2\n"
    "ds_read_b64_tr_b16 v[48:49], v3 offset:0\n"
    "ds_read_b64_tr_b16 v[50:51], v4 offset:576\n"
    "ds_read_b64_tr_b16 v[52:53], v4 offset:1152\n"
    "v_smfmac_f32_16x16x64_f16 v[88:91], v[252:255], v[68:75], v2\n"
    "s_waitcnt lgkmcnt(6)\n"
    "v_pk_mul_f16 v60, v44, s45 op_sel:[0,1]\n"
    "v_pk_mul_f16 v61, v45, s45 op_sel:[0,1]\n"
    "v_mfma_f32_16x16x32_f16 v[76:79], v[228:231], v[68:71], v[76:79]\n"
    "v_pk_mul_f16 v62, v46, s45 op_sel:[0,1]\n"
    "v_pk_mul_f16 v63, v47, s45 op_sel:[0,1]\n"
    "v_mfma_f32_16x16x32_f16 v[76:79], v[240:243], v[72:75], v[76:79]\n"
    "v_pk_mul_f16 v64, v44, v46\n"
    "v_pk_mul_f16 v65, v45, v47\n"
    "v_mfma_f32_16x16x32_f16 v[80:83], v[244:247], v[72:75], v[80:83]\n"
    "v_add_f32 v11, v88, v89\n"
    "v_add_f32 v13, v90, v91\n"
    "v_mfma_f32_16x16x32_f16 v[80:83], v[232:235], v[68:71], v[80:83]\n"
    "v_mov_b64 v[88:89], 0\n"
    "v_mov_b64 v[90:91], 0\n"
    "v_mfma_f32_16x16x32_f16 v[84:87], v[236:239], v[68:71], v[84:87]\n"
    "s_waitcnt lgkmcnt(0)\n"
    "v_pk_mul_f16 v66, v48, v48 op_sel_hi:[0,1]\n"
    "v_pk_mul_f16 v67, v48, v49 op_sel_hi:[0,1]\n"
    "v_mfma_f32_16x16x32_f16 v[84:87], v[248:251], v[72:75], v[84:87]\n"
    "v_mul_f32 v11, v11, v13\n"
    "v_add_u32 v25, 16, v25\n"
    "s_cmp_eq_u32 s45, -1\n"
    "s_cbranch_scc1 .Lxd_ex3\n"
    "s_cmp_eq_u32 s36, 0\n"
    "s_cbranch_scc1 .Lxd_ph0\n"
    "s_branch .Lxd_pl0\n"
    ".Lxd_ex3:\n"
    "v_fmac_f32 v11, v76, v80\n"
    "v_fmac_f32 v11, v77, v81\n"
    "v_fmac_f32 v11, v78, v82\n"
    "v_fmac_f32 v11, v79, v83\n"
    "s_nop 0\n"
    "v_fmac_f32 v11, v84, v85\n"
    "v_fma_f32 v10, v86, v87, v11\n"
    "s_branch .Lxd_fin\n"
    ".Lxd_ex2:\n"
    "v_fmac_f32 v11, v76, v80\n"
    "v_fmac_f32 v11, v77, v81\n"
    "v_fmac_f32 v11, v78, v82\n"
    "v_fmac_f32 v11, v79, v83\n"
    "s_nop 0\n"
    "v_fmac_f32 v11, v84, v85\n"
    "v_fma_f32 v9, v86, v87, v11\n"
    "s_branch .Lxd_fin\n"
    ".Lxd_ex1:\n"
    "v_fmac_f32 v11, v76, v80\n"
    "v_fmac_f32 v11, v77, v81\n"
    "v_fmac_f32 v11, v78, v82\n"
    "v_fmac_f32 v11, v79, v83\n"
    "s_nop 0\n"
    "v_fmac_f32 v11, v84, v85\n"
    "v_fma_f32 v8, v86, v87, v11\n"
    "s_branch .Lxd_fin\n"
    ".Lxd_ex0:\n"
    "v_fmac_f32 v11, v76, v80\n"
    "v_fmac_f32 v11, v77, v81\n"
    "v_fmac_f32 v11, v78, v82\n"
    "v_fmac_f32 v11, v79, v83\n"
    "s_nop 0\n"
    "v_fmac_f32 v11, v84, v85\n"
    "v_fma_f32 v7, v86, v87, v11\n"
    ".Lxd_fin:\n"
    "s_nop 1\n"
    "v_permlane32_swap_b32 v7, v8\n"
    "v_permlane32_swap_b32 v9, v10\n"
    "v_mul_f32 v7, v0, v7\n"
    "v_mul_f32 v9, v0, v9\n"
    "v_fmac_f32 v7, v23, v8\n"
    "v_fmac_f32 v9, v23, v10\n"
    "s_nop 1\n"
    "v_permlane16_swap_b32 v7, v9\n"
    "v_add_f32 v7, v7, v9\n"
    "s_nop 1\n"
    "v_add_f32_dpp v7, v7, v7 quad_perm:[1,0,3,2] row_mask:0xf bank_mask:0xf\n"
    "s_nop 1\n"
    "v_add_f32_dpp v7, v7, v7 quad_perm:[2,3,0,1] row_mask:0xf bank_mask:0xf\n"
    "s_nop 1\n"
    "v_add_f32_dpp v7, v7, v7 row_half_mirror row_mask:0xf bank_mask:0xf\n"
    "s_nop 1\n"
    "v_add_f32_dpp v7, v7, v7 row_mirror row_mask:0xf bank_mask:0xf\n"
    "ds_write_b32 v14, v7\n"
    "v_add_u32 v14, 64, v14\n"
    "s_cmp_lg_u32 s56, 2\n"
    "s_cbranch_scc1 .Lxd_f2\n"
    ".Lxd_f3:\n"
    "v_lshl_add_u32 v12, v18, 6, v20\n"
    "v_lshlrev_b32 v13, 2, v12\n"
    "v_add_u32 v12, 150528, v12\n"
    "v_add_u32 v13, 152128, v13\n"
    "v_mov_b32 v21, 154144\n"
    "s_lshl_b32 s26, s26, 16\n"
    "s_waitcnt vmcnt(0) lgkmcnt(0)\n"
    "s_barrier\n"
    "s_cmp_lg_u32 s20, 0\n"
    "s_cbranch_scc1 .Lxd_done\n"
    "ds_read_b128 v[28:31], v13\n"
    "ds_read_b32 v32, v12\n"
    "ds_read_u16 v33, v12 offset:512\n"
    "ds_read_b32 v34, v12 offset:1024\n"
    "ds_read_b32 v35, v21\n"
    "s_waitcnt lgkmcnt(0)\n"
    "v_add_f32 v28, v28, v29\n"
    "v_add_f32 v30, v30, v31\n"
    "v_cmp_lt_i32 vcc, 0, v32\n"
    "v_lshrrev_b32 v33, 4, v33\n"
    "v_add_f32 v28, v28, v30\n"
    "v_lshl_add_u32 v33, v33, 2, v35\n"
    "v_add_f32 v34, s26, v34\n"
    "v_ldexp_f32 v28, v28, v33\n"
    "v_add_f32 v28, v28, v34\n"
    "v_cvt_pk_bf16_f32 v28, v28, v28\n"
    "s_mov_b64 exec, vcc\n"
    "global_store_short v32, v28, s[70:71] offset:-2\n"
    "s_mov_b64 exec, -1\n"
    ".Lxd_done:\n"
    "s_endpgm\n"
    ".Lxd_cin:\n"
    "s_lshr_b32 s20, s43, 6\n"
    "v_and_b32 v1, 63, v0\n"
    "v_lshlrev_b32 v42, 4, v1\n"
    "s_cmp_ge_u32 s2, 8\n"
    "s_cbranch_scc1 .Lxd_help\n"
    "s_and_b32 s40, s2, 3\n"
    "s_lshl2_add_u32 s23, s40, s20\n"
    "s_lshl1_add_u32 s41, s40, s20\n"
    "s_add_u32 s41, s41, 12\n"
    "s_cmp_ge_u32 s20, 4\n"
    "s_cselect_b32 s23, s41, s23\n"
    "s_add_u32 s41, s40, 24\n"
    "s_cmp_ge_u32 s20, 6\n"
    "s_cselect_b32 s23, s41, s23\n"
    "s_cselect_b32 s24, 1, 0\n"
    "s_min_u32 s34, s23, 25\n"
    "s_lshr_b32 s35, s2, 2\n"
    "s_cmp_ge_u32 s2, 4\n"
    "s_cbranch_scc1 .Lxd_bR\n"
    "v_and_b32 v190, 31, v0\n"
    "v_min_u32 v190, 25, v190\n"
    "v_mul_u32_u24 v190, 52, v190\n"
    "v_bfe_u32 v191, v0, 5, 1\n"
    "v_lshl_add_u32 v190, v191, 4, v190\n"
    "s_mul_i32 s40, s34, 1352\n"
    "s_xor_b32 s66, s20, 7\n"
    "s_or_b32 s66, s66, s2\n"
    "s_waitcnt lgkmcnt(0)\n"
    "s_load_dwordx4 s[68:71], s[0:1], 64\n"
    "s_add_u32 s38, s12, s40\n"
    "s_addc_u32 s39, s13, 0\n"
    "s_cmp_lg_u32 s66, 0\n"
    "s_cbranch_scc1 .Lxd_on40\n"
    "v_and_b32 v188, 31, v0\n"
    "v_lshlrev_b32 v188, 7, v188\n"
    "v_add_u32 v188, 32768, v188\n"
    "v_mov_b32 v176, 140\n"
    "global_store_dword v188, v176, s[16:17] sc1\n"
    "global_load_dwordx4 v[180:183], v42, s[16:17] sc1\n"
    ".Lxd_on40:\n"
    "global_load_dwordx4 v[80:83], v190, s[38:39] offset:0\n"
    "global_load_dwordx4 v[84:87], v190, s[38:39] offset:32\n"
    "s_getpc_b64 s[60:61]\n"
    "s_add_u32 s60, s60, xd_tab@rel32@lo+4\n"
    "s_addc_u32 s61, s61, xd_tab@rel32@hi+12\n"
    "v_lshl_add_u32 v188, s24, 10, v42\n"
    "s_lshl1_add_u32 s40, s24, s35\n"
    "v_lshl_add_u32 v189, s40, 11, v42\n"
    "s_load_dword s25, s[16:17], 0\n"
    "global_load_dwordx4 v[44:47], v188, s[60:61]\n"
    "global_load_dwordx4 v[48:51], v189, s[60:61] offset:2048\n"
    "global_load_dwordx4 v[52:55], v189, s[60:61] offset:3072\n"
    "s_setprio 0\n"
    "v_mov_b64 v[188:189], 0\n"
    "v_lshlrev_b32 v190, 3, v0\n"
    "v_lshrrev_b32 v186, 5, v1\n"
    "ds_write_b64 v190, v[188:189] offset:36864\n"
    "v_bfe_u32 v190, v0, 1, 4\n"
    "v_lshlrev_b32 v72, 2, v186\n"
    "v_sub_u32 v190, v190, v72\n"
    "v_and_b32 v72, 16, v42\n"
    "s_movk_i32 s40, 0x3f80\n"
    "v_lshlrev_b32_e64 v191, v72, s40\n"
    "v_cmp_eq_u32 s[60:61], 0, v190\n"
    "v_cmp_eq_u32 s[62:63], 1, v190\n"
    "v_cmp_eq_u32 s[64:65], 2, v190\n"
    "v_cmp_eq_u32 s[50:51], 3, v190\n"
    "v_cndmask_b32 v72, 0, v191, s[60:61]\n"
    "v_cndmask_b32 v73, 0, v191, s[62:63]\n"
    "v_cndmask_b32 v74, 0, v191, s[64:65]\n"
    "v_cndmask_b32 v75, 0, v191, s[50:51]\n"
    "v_cmp_eq_u32 s[60:61], 8, v190\n"
    "v_cmp_eq_u32 s[62:63], 9, v190\n"
    "v_cmp_eq_u32 s[64:65], 10, v190\n"
    "v_cmp_eq_u32 s[50:51], 11, v190\n"
    "v_cndmask_b32 v76, 0, v191, s[60:61]\n"
    "v_cndmask_b32 v77, 0, v191, s[62:63]\n"
    "v_cndmask_b32 v78, 0, v191, s[64:65]\n"
    "v_cndmask_b32 v79, 0, v191, s[50:51]\n"
    "s_lshl_b32 s30, s20, 12\n"
    "s_add_u32 s30, s30, 65536\n"
    "v_and_b32 v188, 31, v0\n"
    "v_mul_u32_u24 v192, 528, v186\n"
    "v_lshl_add_u32 v192, v188, 2, v192\n"
    "v_add_u32 v192, s30, v192\n"
    "s_lshl_b32 s40, s35, 6\n"
    "s_add_u32 s40, s40, s20\n"
    "s_lshl1_add_u32 s41, s20, s35\n"
    "s_add_u32 s41, s41, 120\n"
    "s_cmp_ge_u32 s20, 4\n"
    "s_cselect_b32 s40, s41, s40\n"
    "s_lshl1_add_u32 s41, s35, 384\n"
    "s_cmp_ge_u32 s20, 6\n"
    "s_cselect_b32 s40, s41, s40\n"
    "s_cselect_b32 s42, 1, 192\n"
    "s_and_b32 s41, s2, 3\n"
    "s_lshl2_add_u32 s40, s41, s40\n"
    "v_lshrrev_b32 v188, 3, v1\n"
    "v_mul_u32_u24 v188, 448, v188\n"
    "v_bfe_u32 v189, v0, 2, 1\n"
    "v_mad_u32_u24 v188, v189, s42, v188\n"
    "v_and_b32 v189, 48, v42\n"
    "v_add3_u32 v188, v188, v189, s40\n"
    "v_lshlrev_b32 v193, 4, v188\n"
    "v_mov_b32 v177, 0\n"
    "s_waitcnt vmcnt(0) lgkmcnt(0)\n"
    "s_cmp_lg_u32 s66, 0\n"
    "s_cbranch_scc1 .Lxd_on41\n"
    "s_mov_b32 s40, 0x9e3779b1\n"
    "v_mul_lo_u32 v144, v180, s40\n"
    "v_xor_b32 v145, 0xa5c30f96, v180\n"
    "v_alignbit_b32 v144, s25, v144, 24\n"
    "v_mul_lo_u32 v146, v181, s40\n"
    "v_xor_b32 v147, 0xa5c30f96, v181\n"
    "v_alignbit_b32 v146, s25, v146, 24\n"
    "v_mul_lo_u32 v148, v182, s40\n"
    "v_xor_b32 v149, 0xa5c30f96, v182\n"
    "v_alignbit_b32 v148, s25, v148, 24\n"
    "v_mul_lo_u32 v150, v183, s40\n"
    "v_xor_b32 v151, 0xa5c30f96, v183\n"
    "v_alignbit_b32 v150, s25, v150, 24\n"
    "v_lshlrev_b32 v180, 1, v42\n"
    "s_add_u32 s40, s16, 4096\n"
    "s_addc_u32 s41, s17, 0\n"
    "global_store_dwordx4 v180, v[144:147], s[40:41] sc1\n"
    "global_store_dwordx4 v180, v[148:151], s[40:41] offset:16 sc1\n"
    ".Lxd_on41:\n"
    "s_barrier\n"
    "s_branch .Lxd_bD\n"
    ".Lxd_bR:\n"
    "s_mul_i32 s40, s20, 4480\n"
    "s_min_u32 s40, s40, 30672\n"
    "s_mov_b32 m0, s40\n"
    "s_add_u32 s40, s40, 70304\n"
    "v_lshrrev_b32 v190, 2, v1\n"
    "v_and_b32 v188, 48, v42\n"
    "s_movk_i32 s42, 1352\n"
    "v_min_u32 v191, 9, v190\n"
    "v_mad_u32_u24 v190, v190, s42, v188\n"
    "v_mad_u32_u24 v191, v191, s42, v188\n"
    "v_add_u32 v191, 20608, v191\n"
    "s_mul_i32 s41, s34, 52\n"
    "s_add_u32 s41, s41, 35152\n"
    "s_waitcnt lgkmcnt(0)\n"
    "s_load_dwordx4 s[68:71], s[0:1], 64\n"
    "s_add_u32 s38, s12, s40\n"
    "s_addc_u32 s39, s13, 0\n"
    "global_load_lds_dwordx4 v42, s[38:39] offset:0\n"
    "global_load_lds_dwordx4 v42, s[38:39] offset:1024\n"
    "global_load_lds_dwordx4 v42, s[38:39] offset:2048\n"
    "global_load_lds_dwordx4 v42, s[38:39] offset:3072\n"
    "global_load_lds_dwordx4 v42, s[38:39] offset:3456\n"
    "s_cmp_lg_u32 s20, 7\n"
    "s_cbranch_scc1 .Lxd_on42\n"
    "v_min_u32 v188, 38, v1\n"
    "s_mov_b32 m0, 40960\n"
    "v_lshlrev_b32 v188, 2, v188\n"
    "global_load_lds_dword v188, s[14:15]\n"
    ".Lxd_on42:\n"
    "s_lshl_b32 s28, s20, 11\n"
    "s_add_u32 s28, s28, 49152\n"
    "s_mov_b32 m0, s28\n"
    "s_add_u32 s38, s12, s41\n"
    "s_addc_u32 s39, s13, 0\n"
    "global_load_lds_dwordx4 v190, s[38:39] offset:0\n"
    "global_load_lds_dwordx4 v191, s[38:39] offset:1024\n"
    "s_getpc_b64 s[60:61]\n"
    "s_add_u32 s60, s60, xd_tab@rel32@lo+4\n"
    "s_addc_u32 s61, s61, xd_tab@rel32@hi+12\n"
    "v_lshl_add_u32 v188, s24, 10, v42\n"
    "s_lshl1_add_u32 s40, s24, s35\n"
    "v_lshl_add_u32 v189, s40, 11, v42\n"
    "s_load_dword s25, s[16:17], 0\n"
    "global_load_dwordx4 v[44:47], v188, s[60:61]\n"
    "global_load_dwordx4 v[48:51], v189, s[60:61] offset:2048\n"
    "global_load_dwordx4 v[52:55], v189, s[60:61] offset:3072\n"
    "s_setprio 0\n"
    "v_mov_b64 v[188:189], 0\n"
    "v_lshlrev_b32 v190, 3, v0\n"
    "v_lshrrev_b32 v186, 5, v1\n"
    "ds_write_b64 v190, v[188:189] offset:36864\n"
    "v_lshrrev_b32 v184, 4, v1\n"
    "v_and_b32 v185, 15, v0\n"
    "v_lshrrev_b32 v190, 2, v185\n"
    "v_lshl_add_u32 v64, v184, 3, v190\n"
    "v_add_u32 v65, 4, v64\n"
    "s_lshr_b32 s21, s20, 2\n"
    "s_and_b32 s43, s20, 3\n"
    "s_mul_i32 s40, s21, 13\n"
    "s_lshl2_add_u32 s40, s43, s40\n"
    "v_and_b32 v191, 3, v0\n"
    "v_add_lshl_u32 v188, v191, s40, 3\n"
    "v_min_u32 v64, 25, v64\n"
    "v_mad_u32_u24 v64, v64, s42, v188\n"
    "v_min_u32 v65, 25, v65\n"
    "v_mad_u32_u24 v65, v65, s42, v188\n"
    "v_lshl_add_u32 v188, s43, 4, v185\n"
    "v_cmp_lt_u32 vcc, 25, v188\n"
    "v_cmp_lt_u32 s[60:61], 51, v188\n"
    "v_lshl_add_u32 v66, s21, 6, v188\n"
    "v_cndmask_b32_e64 v189, 0, 6, vcc\n"
    "v_add_lshl_u32 v66, v66, v189, 1\n"
    "v_mov_b32 v189, 0x40000\n"
    "v_cmp_gt_u32 vcc, 26, v1\n"
    "v_cndmask_b32 v66, v66, v189, s[60:61]\n"
    "v_lshlrev_b32 v67, 6, v1\n"
    "v_cndmask_b32 v67, v189, v67, vcc\n"
    "v_lshlrev_b32 v68, 4, v184\n"
    "v_add_u32 v68, 40960, v68\n"
    "v_lshlrev_b32 v69, 1, v1\n"
    "v_lshl_add_u32 v190, v186, 3, v190\n"
    "v_and_b32 v188, 1, v184\n"
    "v_lshl_add_u32 v188, v188, 2, v191\n"
    "v_cmp_eq_u32 vcc, 7, v188\n"
    "v_lshlrev_b32 v188, 3, v188\n"
    "v_lshl_add_u32 v70, v190, 6, v188\n"
    "v_add_u32 v71, s28, v70\n"
    "v_mov_b32 v188, 38912\n"
    "v_cndmask_b32 v71, v71, v188, vcc\n"
    "v_cmp_gt_u32 vcc, 3, v184\n"
    "s_waitcnt vmcnt(5) lgkmcnt(0)\n"
    "s_barrier\n"
    "ds_read2_b64 v[176:179], v68 offset0:13 offset1:14\n"
    "ds_read_u16 v68, v69 offset:41012\n"
    "ds_read_u16 v69, v69 offset:40960\n"
    "ds_read_b64_tr_b16 v[112:113], v64 offset:0\n"
    "ds_read_b64_tr_b16 v[114:115], v65 offset:0\n"
    "ds_read_b64_tr_b16 v[116:117], v64 offset:208\n"
    "ds_read_b64_tr_b16 v[118:119], v65 offset:208\n"
    "ds_read_b64_tr_b16 v[120:121], v64 offset:416\n"
    "ds_read_b64_tr_b16 v[122:123], v65 offset:416\n"
    "ds_read_b64_tr_b16 v[124:125], v64 offset:624\n"
    "ds_read_b64_tr_b16 v[126:127], v65 offset:624\n"
    "ds_read_b64_tr_b16 v[128:129], v64 offset:832\n"
    "ds_read_b64_tr_b16 v[130:131], v65 offset:832\n"
    "ds_read_b64_tr_b16 v[132:133], v64 offset:1040\n"
    "ds_read_b64_tr_b16 v[134:135], v65 offset:1040\n"
    "ds_read_b64_tr_b16 v[136:137], v64 offset:1248\n"
    "ds_read_b64_tr_b16 v[138:139], v65 offset:1248\n"
    "s_waitcnt lgkmcnt(14)\n"
    "v_cndmask_b32 v177, 0, v177, vcc\n"
    "v_cndmask_b32 v178, 0, v178, vcc\n"
    "v_cndmask_b32 v179, 0, v179, vcc\n"
    "ds_write_b16 v67, v68 offset:36920\n"
    "s_waitcnt lgkmcnt(13)\n"
    "v_mfma_f32_16x16x32_bf16 v[144:147], v[176:179], v[112:115], 0\n"
    "s_waitcnt lgkmcnt(11)\n"
    "v_mfma_f32_16x16x32_bf16 v[148:151], v[176:179], v[116:119], 0\n"
    "s_waitcnt lgkmcnt(9)\n"
    "v_mfma_f32_16x16x32_bf16 v[152:155], v[176:179], v[120:123], 0\n"
    "s_waitcnt lgkmcnt(7)\n"
    "v_mfma_f32_16x16x32_bf16 v[156:159], v[176:179], v[124:127], 0\n"
    "s_waitcnt lgkmcnt(5)\n"
    "v_mfma_f32_16x16x32_bf16 v[160:163], v[176:179], v[128:131], 0\n"
    "s_waitcnt lgkmcnt(3)\n"
    "v_mfma_f32_16x16x32_bf16 v[164:167], v[176:179], v[132:135], 0\n"
    "s_waitcnt lgkmcnt(1)\n"
    "v_mfma_f32_16x16x32_bf16 v[168:171], v[176:179], v[136:139], 0\n"
    "s_waitcnt vmcnt(3)\n"
    "ds_read_b64_tr_b16 v[80:81], v71 offset:0\n"
    "ds_read_b64_tr_b16 v[82:83], v71 offset:256\n"
    "ds_read_b64_tr_b16 v[84:85], v71 offset:1024\n"
    "ds_read_b64_tr_b16 v[86:87], v71 offset:1280\n"
    "v_cvt_pk_bf16_f32 v144, v144, v144\n"
    "ds_write_b16 v66, v144 offset:36864\n"
    "v_cvt_pk_bf16_f32 v148, v148, v148\n"
    "ds_write_b16 v66, v148 offset:37120\n"
    "v_cvt_pk_bf16_f32 v152, v152, v152\n"
    "ds_write_b16 v66, v152 offset:37376\n"
    "v_cvt_pk_bf16_f32 v156, v156, v156\n"
    "ds_write_b16 v66, v156 offset:37632\n"
    "v_cvt_pk_bf16_f32 v160, v160, v160\n"
    "ds_write_b16 v66, v160 offset:37888\n"
    "v_cvt_pk_bf16_f32 v164, v164, v164\n"
    "ds_write_b16 v66, v164 offset:38144\n"
    "s_cmp_lg_u32 s21, 0\n"
    "s_cbranch_scc1 .Lxd_on43\n"
    "v_cvt_pk_bf16_f32 v168, v168, v168\n"
    "ds_write_b16 v66, v168 offset:38400\n"
    ".Lxd_on43:\n"
    "v_lshlrev_b32 v177, 16, v69\n"
    "s_waitcnt lgkmcnt(0)\n"
    "s_barrier\n"
    "ds_read_b64_tr_b16 v[72:73], v70 offset:36864\n"
    "ds_read_b64_tr_b16 v[74:75], v70 offset:37120\n"
    "ds_read_b64_tr_b16 v[76:77], v70 offset:37888\n"
    "ds_read_b64_tr_b16 v[78:79], v70 offset:38144\n"
    "s_lshl_b32 s30, s20, 12\n"
    "s_add_u32 s30, s30, 65536\n"
    "v_and_b32 v188, 31, v0\n"
    "v_mul_u32_u24 v192, 528, v186\n"
    "v_lshl_add_u32 v192, v188, 2, v192\n"
    "v_add_u32 v192, s30, v192\n"
    "s_lshl_b32 s40, s35, 6\n"
    "s_add_u32 s40, s40, s20\n"
    "s_lshl1_add_u32 s41, s20, s35\n"
    "s_add_u32 s41, s41, 120\n"
    "s_cmp_ge_u32 s20, 4\n"
    "s_cselect_b32 s40, s41, s40\n"
    "s_lshl1_add_u32 s41, s35, 384\n"
    "s_cmp_ge_u32 s20, 6\n"
    "s_cselect_b32 s40, s41, s40\n"
    "s_cselect_b32 s42, 1, 192\n"
    "s_and_b32 s41, s2, 3\n"
    "s_lshl2_add_u32 s40, s41, s40\n"
    "v_lshrrev_b32 v188, 3, v1\n"
    "v_mul_u32_u24 v188, 448, v188\n"
    "v_bfe_u32 v189, v0, 2, 1\n"
    "v_mad_u32_u24 v188, v189, s42, v188\n"
    "v_and_b32 v189, 48, v42\n"
    "v_add3_u32 v188, v188, v189, s40\n"
    "v_lshlrev_b32 v193, 4, v188\n"
    "s_waitcnt vmcnt(0) lgkmcnt(0)\n"
    ".Lxd_bD:\n"
    "v_mfma_f32_32x32x16_bf16 v[96:111], v[72:75], v[80:83], 0\n"
    "v_mfma_f32_32x32x16_bf16 v[96:111], v[80:83], v[72:75], v[96:111]\n"
    "v_mfma_f32_32x32x16_bf16 v[96:111], v[76:79], v[84:87], v[96:111]\n"
    "v_mfma_f32_32x32x16_bf16 v[96:111], v[84:87], v[76:79], v[96:111]\n"
    "v_readlane_b32 s43, v177, s34\n"
    "v_mad_u32_u16 v64, v44, 4, s30 op_sel:[0,0,0,0]\n"
    "v_mad_u32_u16 v65, v44, 4, s30 op_sel:[1,0,0,0]\n"
    "v_mad_u32_u16 v66, v45, 4, s30 op_sel:[0,0,0,0]\n"
    "v_mad_u32_u16 v67, v45, 4, s30 op_sel:[1,0,0,0]\n"
    "v_mad_u32_u16 v68, v46, 4, s30 op_sel:[0,0,0,0]\n"
    "v_mad_u32_u16 v69, v46, 4, s30 op_sel:[1,0,0,0]\n"
    "v_mad_u32_u16 v70, v47, 4, s30 op_sel:[0,0,0,0]\n"
    "v_mad_u32_u16 v71, v47, 4, s30 op_sel:[1,0,0,0]\n"
    "v_mov_b32 v184, 40944\n"
    "s_and_b32 s25, s25, 0xffffff\n"
    "s_mul_i32 s40, s25, 0x9e3779b1\n"
    "s_xor_b32 s41, s25, 0xa5c30f96\n"
    "s_lshr_b32 s40, s40, 24\n"
    "v_writelane_b32 v108, s43, 60\n"
    "ds_write2_b32 v192, v96, v97 offset0:0 offset1:33\n"
    "ds_write2_b32 v192, v98, v99 offset0:66 offset1:99\n"
    "v_add_u32 v192, 1056, v192\n"
    "ds_write2_b32 v192, v100, v101 offset0:0 offset1:33\n"
    "ds_write2_b32 v192, v102, v103 offset0:66 offset1:99\n"
    "v_add_u32 v192, 1056, v192\n"
    "ds_write2_b32 v192, v104, v105 offset0:0 offset1:33\n"
    "ds_write2_b32 v192, v106, v107 offset0:66 offset1:99\n"
    "v_add_u32 v192, 1056, v192\n"
    "ds_write2_b32 v192, v108, v109 offset0:0 offset1:33\n"
    "ds_read_b32 v56, v64\n"
    "ds_read_b32 v57, v65\n"
    "ds_read_b32 v58, v66\n"
    "ds_read_b32 v59, v67\n"
    "ds_read_b32 v60, v68\n"
    "ds_read_b32 v61, v69\n"
    "ds_read_b32 v62, v70\n"
    "ds_read_b32 v63, v71\n"
    "s_waitcnt lgkmcnt(6)\n"
    "v_pk_mul_f32 v[56:57], v[56:57], v[48:49]\n"
    "v_max3_f32 v188, |v56|, |v57|, 0\n"
    "s_waitcnt lgkmcnt(4)\n"
    "v_pk_mul_f32 v[58:59], v[58:59], v[50:51]\n"
    "v_max3_f32 v188, |v58|, |v59|, v188\n"
    "s_waitcnt lgkmcnt(2)\n"
    "v_pk_mul_f32 v[60:61], v[60:61], v[52:53]\n"
    "v_max3_f32 v188, |v60|, |v61|, v188\n"
    "s_waitcnt lgkmcnt(0)\n"
    "v_pk_mul_f32 v[62:63], v[62:63], v[54:55]\n"
    "v_max3_f32 v188, |v62|, |v63|, v188\n"
    "s_nop 1\n"
    "v_max_u32_dpp v188, v188, v188 quad_perm:[1,0,3,2] row_mask:0xf bank_mask:0xf\n"
    "s_nop 1\n"
    "v_max_u32_dpp v188, v188, v188 quad_perm:[2,3,0,1] row_mask:0xf bank_mask:0xf\n"
    "s_nop 1\n"
    "v_max_u32_dpp v188, v188, v188 row_half_mirror row_mask:0xf bank_mask:0xf\n"
    "s_nop 1\n"
    "v_max_u32_dpp v188, v188, v188 row_mirror row_mask:0xf bank_mask:0xf\n"
    "s_mov_b32 exec_lo, 0x10001\n"
    "s_mov_b32 exec_hi, 1\n"
    "ds_max_u32 v184, v188\n"
    "s_mov_b64 exec, -1\n"
    "s_add_u32 s38, s16, 1047552\n"
    "s_addc_u32 s39, s17, 0\n"
    "s_cmp_lt_u32 s23, 26\n"
    "s_cselect_b32 s43, 0xfff8fff8, 0\n"
    "s_waitcnt lgkmcnt(0)\n"
    "s_barrier\n"
    "ds_read_b32 v188, v184\n"
    "v_cmp_le_u32 vcc, 0x400, v193\n"
    "s_cmp_eq_u32 s20, 7\n"
    "s_cselect_b32 s42, 0, 48\n"
    "s_bfm_b64 s[60:61], s42, 0\n"
    "s_and_b64 s[60:61], s[60:61], vcc\n"
    "s_waitcnt lgkmcnt(0)\n"
    "v_lshrrev_b32 v188, 23, v188\n"
    "v_max_u32 v188, 40, v188\n"
    "v_min_u32 v188, 250, v188\n"
    "v_sub_u32 v190, 268, v188\n"
    "v_lshlrev_b32 v190, 23, v190\n"
    "v_pk_mul_f32 v[56:57], v[56:57], v[190:191] op_sel_hi:[1,0]\n"
    "v_cvt_pkrtz_f16_f32 v56, v56, v57\n"
    "v_add_u32 v56, 0x40004, v56\n"
    "v_and_b32 v56, s43, v56\n"
    "v_pk_mul_f32 v[58:59], v[58:59], v[190:191] op_sel_hi:[1,0]\n"
    "v_cvt_pkrtz_f16_f32 v57, v58, v59\n"
    "v_add_u32 v57, 0x40004, v57\n"
    "v_and_b32 v57, s43, v57\n"
    "v_pk_mul_f32 v[60:61], v[60:61], v[190:191] op_sel_hi:[1,0]\n"
    "v_cvt_pkrtz_f16_f32 v58, v60, v61\n"
    "v_add_u32 v58, 0x40004, v58\n"
    "v_and_b32 v58, s43, v58\n"
    "v_pk_mul_f32 v[62:63], v[62:63], v[190:191] op_sel_hi:[1,0]\n"
    "v_cvt_pkrtz_f16_f32 v59, v62, v63\n"
    "v_add_u32 v59, 0x40004, v59\n"
    "v_and_b32 v59, s43, v59\n"
    "v_lshl_or_b32 v188, v188, 8, s40\n"
    "v_mov_b32 v189, s41\n"
    "s_lshl3_add_u32 s40, s2, 40960\n"
    "v_mov_b32 v190, s40\n"
    "s_cmp_eq_u32 s20, 7\n"
    "s_cbranch_scc1 .Lxd_b3\n"
    "s_mov_b64 exec, s[60:61]\n"
    "global_store_dwordx4 v193, v[56:59], s[38:39] sc1\n"
    "s_mov_b64 exec, -1\n"
    ".Lxd_b3:\n"
    "s_waitcnt vmcnt(0)\n"
    "s_barrier\n"
    "s_cmp_lg_u32 s20, 0\n"
    "s_cbranch_scc1 .Lxd_on44\n"
    "s_mov_b64 exec, 1\n"
    "global_store_dwordx2 v190, v[188:189], s[16:17] sc1\n"
    "s_mov_b64 exec, -1\n"
    ".Lxd_on44:\n"
    "s_branch .Lxd_h1\n"
    ".Lxd_help:\n"
    "s_lshr_b32 s41, s2, 3\n"
    "s_cmp_lg_u32 s20, 7\n"
    "s_cbranch_scc1 .Lxd_on46\n"
    "s_mul_i32 s40, s41, 28\n"
    "v_min_u32 v7, 27, v1\n"
    "v_add_lshl_u32 v7, v7, s40, 7\n"
    "s_getpc_b64 s[60:61]\n"
    ".Lxd_pc45:\n"
    "s_sub_u32 s60, s60, .Lxd_pc45-.Lxd_enter\n"
    "s_subb_u32 s61, s61, 0\n"
    "v_min_u32 v7, .Lxd_end-.Lxd_enter-4, v7\n"
    "global_load_dword v7, v7, s[60:61]\n"
    ".Lxd_on46:\n"
    "s_waitcnt lgkmcnt(0)\n"
    "s_load_dwordx4 s[68:71], s[0:1], 64\n"
    ".Lxd_h1:\n"
    "s_lshr_b32 s3, s2, 4\n"
    "s_mul_i32 s40, s3, 3\n"
    "s_add_u32 s47, s2, s40\n"
    "s_and_b32 s47, s47, 15\n"
    "s_lshr_b32 s21, s20, 2\n"
    "s_lshl1_add_u32 s22, s21, s20\n"
    "s_and_b32 s22, s22, 3\n"
    "s_lshl_b32 s58, s21, 8\n"
    "s_add_u32 s58, s58, 150528\n"
    "s_mov_b32 s40, s47\n"
    "s_mul_i32 s54, s40, 512\n"
    "s_add_u32 s54, s54, 2\n"
    "s_lshl_b32 s40, s40, 7\n"
    "s_add_u32 s40, s40, 32768\n"
    "s_add_u32 s52, s16, s40\n"
    "s_addc_u32 s53, s17, 0\n"
    "s_mov_b32 s57, 0x7fff7fff\n"
    "s_mov_b32 s56, 0\n"
    "s_mov_b32 s27, 0\n"
    "s_mov_b32 s55, 9\n"
    "s_mov_b32 s44, 0\n"
    "v_lshrrev_b32 v17, 3, v1\n"
    "v_mul_u32_u24 v17, 12, v17\n"
    "v_and_b32 v19, 7, v1\n"
    "v_lshlrev_b32 v19, 4, v19\n"
    "v_lshrrev_b32 v18, 5, v1\n"
    "v_lshlrev_b32 v18, 2, v18\n"
    "v_and_b32 v20, 31, v1\n"
    "v_lshlrev_b32 v20, 2, v20\n"
    "s_lshl2_add_u32 s40, s22, s58\n"
    "s_sub_u32 s40, s40, 20\n"
    "v_mov_b32 v25, s40\n"
    "s_lshl1_add_u32 s23, s3, s21\n"
    "s_mul_i32 s23, s23, 6\n"
    "s_add_u32 s23, s23, s22\n"
    "s_add_u32 s23, s23, 104\n"
    "v_mov_b32 v24, s23\n"
    "v_lshlrev_b32 v13, 2, v0\n"
    "v_readfirstlane_b32 s40, v24\n"
    "s_or_b32 s40, s40, s44\n"
    "s_lshl1_add_u32 s41, s40, s54\n"
    "s_cmpk_lt_u32 s40, 256\n"
    "s_cselect_b32 s41, s41, 2\n"
    "v_mov_b32 v12, s41\n"
    "s_mul_i32 s41, s41, 52\n"
    "s_add_u32 s38, s4, s41\n"
    "s_addc_u32 s39, s5, 0\n"
    "global_load_dwordx3 v[36:38], v17, s[38:39] offset:-104\n"
    "global_load_dword v41, v18, s[38:39] offset:-8\n"
    "s_setprio 0\n"
    "s_cmp_lt_u32 s2, 8\n"
    "s_cbranch_scc1 .Lxd_hp47\n"
    "v_mul_u32_u24 v68, 52, v1\n"
    "v_lshrrev_b32 v68, 8, v68\n"
    "v_mad_i32_i24 v69, v68, -5, v1\n"
    "s_and_b32 s40, s2, 0xf8\n"
    "s_add_u32 s40, s40, s20\n"
    "s_mul_i32 s40, s40, 13\n"
    "s_sub_u32 s40, s40, 104\n"
    "v_add_u32 v68, s40, v68\n"
    "v_min_u32 v68, 0x1ff, v68\n"
    "v_lshrrev_b32 v70, 8, v68\n"
    "s_movk_i32 s40, 1792\n"
    "v_mad_u32_u24 v68, v70, s40, v68\n"
    "s_and_b32 s40, s2, 7\n"
    "v_lshl_add_u32 v68, s40, 8, v68\n"
    "v_mul_u32_u24 v68, 104, v68\n"
    "s_lshl_b32 s40, s2, 2\n"
    "s_lshr_b32 s40, 0xa8864220, s40\n"
    "s_and_b32 s40, s40, 14\n"
    "s_lshl_b32 s40, s40, 1\n"
    "v_add3_u32 v70, v69, s40, 4\n"
    "v_subrev_u32 v71, 24, v70\n"
    "v_min_u32 v70, v70, v71\n"
    "v_lshl_add_u32 v70, v70, 2, v68\n"
    "global_load_dword v60, v70, s[4:5]\n"
    "v_add3_u32 v70, v69, s40, 9\n"
    "v_subrev_u32 v71, 24, v70\n"
    "v_min_u32 v70, v70, v71\n"
    "v_lshl_add_u32 v70, v70, 2, v68\n"
    "global_load_dword v61, v70, s[4:5]\n"
    "v_add3_u32 v70, v69, s40, 14\n"
    "v_subrev_u32 v71, 24, v70\n"
    "v_min_u32 v70, v70, v71\n"
    "v_lshl_add_u32 v70, v70, 2, v68\n"
    "global_load_dword v62, v70, s[4:5]\n"
    "v_add3_u32 v70, v69, s40, 19\n"
    "v_subrev_u32 v71, 24, v70\n"
    "v_min_u32 v70, v70, v71\n"
    "v_lshl_add_u32 v70, v70, 2, v68\n"
    "global_load_dword v63, v70, s[4:5]\n"
    ".Lxd_hp47:\n"
    "v_mov_b32 v21, 0\n"
    "v_add_u32 v13, 149504, v13\n"
    "ds_write_b32 v13, v21\n"
    "s_waitcnt vmcnt(0)\n"
    "v_lshl_or_b32 v36, v36, 7, v19\n"
    "v_lshl_or_b32 v37, v37, 7, v19\n"
    "v_lshl_or_b32 v38, v38, 7, v19\n"
    "v_lshl_or_b32 v41, v41, 7, v20\n"
    "global_load_dwordx4 v[28:31], v36, s[8:9]\n"
    "global_load_dwordx4 v[32:35], v37, s[8:9]\n"
    "global_load_dword v40, v41, s[8:9]\n"
    "global_load_dwordx4 v[36:39], v38, s[8:9]\n"
    "s_waitcnt lgkmcnt(0)\n"
    "s_barrier\n"
    "ds_write_b32 v25, v12 offset:20\n"
    "s_load_dword s26, s[68:69], 0\n"
    "s_lshl_b32 s43, s21, 16\n"
    "v_lshrrev_b32 v12, 4, v1\n"
    "v_bfe_u32 v13, v1, 2, 2\n"
    "v_and_b32 v21, 3, v1\n"
    "s_lshl_b32 s40, s22, 5\n"
    "s_add_u32 s40, s40, s43\n"
    "v_lshl_add_u32 v21, v21, 3, s40\n"
    "s_movk_i32 s41, 144\n"
    "v_add_u32 v22, v12, v13\n"
    "v_and_b32 v22, 3, v22\n"
    "v_mad_u32_u24 v4, v22, s41, v21\n"
    "v_cmp_gt_u32 vcc, 2, v13\n"
    "v_add_u32 v24, 22, v13\n"
    "v_lshlrev_b32 v13, 3, v13\n"
    "v_cndmask_b32 v22, v24, v22, vcc\n"
    "v_mad_u32_u24 v3, v22, s41, v21\n"
    "v_cmp_eq_u32 vcc, 3, v12\n"
    "v_xor_b32 v22, 0, v12\n"
    "v_and_b32 v22, 1, v22\n"
    "v_lshlrev_b32 v22, 1, v22\n"
    "v_lshl_add_u32 v22, v12, 3, v22\n"
    "s_mov_b32 s40, 0x05040100\n"
    "v_bfe_u32 v24, s40, v13, 8\n"
    "v_add_u32 v22, v22, v24\n"
    "s_mov_b32 s40, 0x1a191818\n"
    "v_bfe_u32 v24, s40, v13, 8\n"
    "v_cndmask_b32 v22, v22, v24, vcc\n"
    "v_mad_u32_u24 v5, v22, s41, v21\n"
    "v_xor_b32 v22, 1, v12\n"
    "v_and_b32 v22, 1, v22\n"
    "s_cmp_lt_u32 s2, 8\n"
    "s_cbranch_scc1 .Lxd_hp48\n"
    "s_mov_b32 m0, 159744\n"
    "v_lshlrev_b32 v60, 7, v60\n"
    "global_load_lds_dword v60, s[8:9]\n"
    "s_mov_b32 m0, 159744\n"
    "v_lshlrev_b32 v61, 7, v61\n"
    "global_load_lds_dword v61, s[8:9]\n"
    "s_mov_b32 m0, 159744\n"
    "v_lshlrev_b32 v62, 7, v62\n"
    "global_load_lds_dword v62, s[8:9]\n"
    ".Lxd_hp48:\n"
    "v_lshlrev_b32 v22, 1, v22\n"
    "v_lshl_add_u32 v22, v12, 3, v22\n"
    "s_mov_b32 s40, 0x05040100\n"
    "v_bfe_u32 v24, s40, v13, 8\n"
    "v_add_u32 v22, v22, v24\n"
    "s_mov_b32 s40, 0x1a191918\n"
    "v_bfe_u32 v24, s40, v13, 8\n"
    "v_cndmask_b32 v22, v22, v24, vcc\n"
    "v_mad_u32_u24 v6, v22, s41, v21\n"
    "v_bfrev_b32 v14, v12\n"
    "v_lshrrev_b32 v14, 26, v14\n"
    "s_add_u32 s40, s22, 3\n"
    "s_and_b32 s40, s40, 3\n"
    "s_lshl2_add_u32 s40, s40, s22\n"
    "s_add_u32 s40, s40, s58\n"
    "s_lshl_b32 s40, s40, 2\n"
    "s_add_u32 s40, s40, -450048\n"
    "v_add_u32 v14, s40, v14\n"
    "v_and_b32 v2, 1, v1\n"
    "v_mul_u32_u24 v2, 0xaaaa, v2\n"
    "v_add_u32 v2, 0x4444, v2\n"
    "v_mad_u32_u24 v15, v17, 36, v19\n"
    "v_mad_u32_u24 v16, v18, 36, v20\n"
    "s_lshl4_add_u32 s41, s21, s22\n"
    "v_lshrrev_b32 v12, 5, v19\n"
    "v_sub_u32 v12, 1, v12\n"
    "v_and_b32 v12, 3, v12\n"
    "v_add_lshl_u32 v12, v12, s41, 12\n"
    "v_add_u32 v15, v12, v15\n"
    "v_lshrrev_b32 v12, 5, v20\n"
    "v_sub_u32 v12, 1, v12\n"
    "v_and_b32 v12, 3, v12\n"
    "v_add_lshl_u32 v12, v12, s41, 12\n"
    "v_add_u32 v16, v12, v16\n"
    "s_lshl3_add_u32 s40, s2, 4096\n"
    "s_lshl_b32 s41, s2, 2\n"
    "v_lshlrev_b32 v11, 3, v1\n"
    "v_add_u32 v11, 40960, v11\n"
    "v_writelane_b32 v11, s40, 8\n"
    "v_writelane_b32 v11, s41, 9\n"
    "v_add_u32 v43, 4096, v42\n"
    "s_cmp_lt_u32 s2, 8\n"
    "s_cbranch_scc1 .Lxd_hp49\n"
    "s_waitcnt vmcnt(3)\n"
    "s_branch .Lxd_h2\n"
    ".Lxd_hp49:\n"
    "s_waitcnt vmcnt(0)\n"
    ".Lxd_h2:\n"
    "s_cmp_lg_u32 s20, 7\n"
    "s_cbranch_scc1 .Lxd_on50\n"
    "s_mov_b64 exec, 0x3ff\n"
    "global_load_dwordx2 v[26:27], v11, s[16:17] sc1\n"
    "s_mov_b64 exec, -1\n"
    ".Lxd_on50:\n"
    "s_movk_i32 s43, 15744\n"
    "v_pk_maximum3_f16 v21, v28, v28, s43 op_sel_hi:[1,1,0] neg_lo:[0,1,0] neg_hi:[0,1,0]\n"
    "v_pk_maximum3_f16 v21, v29, v29, v21 neg_lo:[0,1,0] neg_hi:[0,1,0]\n"
    "v_pk_maximum3_f16 v21, v30, v30, v21 neg_lo:[0,1,0] neg_hi:[0,1,0]\n"
    "v_pk_maximum3_f16 v21, v31, v31, v21 neg_lo:[0,1,0] neg_hi:[0,1,0]\n"
    "v_pk_maximum3_f16 v21, v32, v32, v21 neg_lo:[0,1,0] neg_hi:[0,1,0]\n"
    "v_pk_maximum3_f16 v21, v33, v33, v21 neg_lo:[0,1,0] neg_hi:[0,1,0]\n"
    "v_pk_maximum3_f16 v21, v34, v34, v21 neg_lo:[0,1,0] neg_hi:[0,1,0]\n"
    "v_pk_maximum3_f16 v21, v35, v35, v21 neg_lo:[0,1,0] neg_hi:[0,1,0]\n"
    "v_pk_maximum3_f16 v21, v36, v36, v21 neg_lo:[0,1,0] neg_hi:[0,1,0]\n"
    "v_pk_maximum3_f16 v21, v37, v37, v21 neg_lo:[0,1,0] neg_hi:[0,1,0]\n"
    "v_pk_maximum3_f16 v21, v38, v38, v21 neg_lo:[0,1,0] neg_hi:[0,1,0]\n"
    "v_pk_maximum3_f16 v21, v39, v39, v21 neg_lo:[0,1,0] neg_hi:[0,1,0]\n"
    "v_pk_maximum3_f16 v21, v40, v40, v21 neg_lo:[0,1,0] neg_hi:[0,1,0]\n"
    "v_pk_max_u16 v21, v21, v21 op_sel:[0,1] op_sel_hi:[1,0]\n"
    "s_nop 1\n"
    "v_max_u32_dpp v21, v21, v21 quad_perm:[1,0,3,2] row_mask:0xf bank_mask:0xf\n"
    "s_nop 1\n"
    "v_max_u32_dpp v21, v21, v21 quad_perm:[2,3,0,1] row_mask:0xf bank_mask:0xf\n"
    "s_nop 1\n"
    "v_max_u32_dpp v21, v21, v21 row_half_mirror row_mask:0xf bank_mask:0xf\n"
    "s_nop 1\n"
    "v_max_u32_dpp v21, v21, v21 row_mirror row_mask:0xf bank_mask:0xf\n"
    "s_nop 1\n"
    "v_max_u32_dpp v21, v21, v21 row_bcast:15 row_mask:0xa bank_mask:0xf\n"
    "s_nop 1\n"
    "v_max_u32_dpp v21, v21, v21 row_bcast:31 row_mask:0xc bank_mask:0xf\n"
    "s_nop 1\n"
    "v_readlane_b32 s47, v21, 63\n"
    "s_and_b32 s50, s47, 0x7f80\n"
    "s_lshr_b32 s47, s50, 3\n"
    "s_sub_u32 s50, s50, 2816\n"
    "s_mul_i32 s51, s50, -8\n"
    "v_pk_maximum3_f16 v21, v28, v28, s50 op_sel_hi:[1,1,0] neg_lo:[0,1,0] neg_hi:[0,1,0]\n"
    "v_pk_mad_u16 v21, v21, 8, s51 op_sel_hi:[1,0,0]\n"
    "v_bfi_b32 v28, s57, v21, v28\n"
    "v_pk_maximum3_f16 v12, v29, v29, s50 op_sel_hi:[1,1,0] neg_lo:[0,1,0] neg_hi:[0,1,0]\n"
    "v_pk_mad_u16 v12, v12, 8, s51 op_sel_hi:[1,0,0]\n"
    "v_bfi_b32 v29, s57, v12, v29\n"
    "v_pk_maximum3_f16 v21, v30, v30, s50 op_sel_hi:[1,1,0] neg_lo:[0,1,0] neg_hi:[0,1,0]\n"
    "v_pk_mad_u16 v21, v21, 8, s51 op_sel_hi:[1,0,0]\n"
    "v_bfi_b32 v30, s57, v21, v30\n"
    "v_pk_maximum3_f16 v12, v31, v31, s50 op_sel_hi:[1,1,0] neg_lo:[0,1,0] neg_hi:[0,1,0]\n"
    "v_pk_mad_u16 v12, v12, 8, s51 op_sel_hi:[1,0,0]\n"
    "v_bfi_b32 v31, s57, v12, v31\n"
    "v_pk_maximum3_f16 v21, v32, v32, s50 op_sel_hi:[1,1,0] neg_lo:[0,1,0] neg_hi:[0,1,0]\n"
    "v_pk_mad_u16 v21, v21, 8, s51 op_sel_hi:[1,0,0]\n"
    "v_bfi_b32 v32, s57, v21, v32\n"
    "v_pk_maximum3_f16 v12, v33, v33, s50 op_sel_hi:[1,1,0] neg_lo:[0,1,0] neg_hi:[0,1,0]\n"
    "v_pk_mad_u16 v12, v12, 8, s51 op_sel_hi:[1,0,0]\n"
    "v_bfi_b32 v33, s57, v12, v33\n"
    "v_pk_maximum3_f16 v21, v34, v34, s50 op_sel_hi:[1,1,0] neg_lo:[0,1,0] neg_hi:[0,1,0]\n"
    "v_pk_mad_u16 v21, v21, 8, s51 op_sel_hi:[1,0,0]\n"
    "v_bfi_b32 v34, s57, v21, v34\n"
    "v_pk_maximum3_f16 v12, v35, v35, s50 op_sel_hi:[1,1,0] neg_lo:[0,1,0] neg_hi:[0,1,0]\n"
    "v_pk_mad_u16 v12, v12, 8, s51 op_sel_hi:[1,0,0]\n"
    "v_bfi_b32 v35, s57, v12, v35\n"
    "v_pk_maximum3_f16 v21, v36, v36, s50 op_sel_hi:[1,1,0] neg_lo:[0,1,0] neg_hi:[0,1,0]\n"
    "v_pk_mad_u16 v21, v21, 8, s51 op_sel_hi:[1,0,0]\n"
    "v_bfi_b32 v36, s57, v21, v36\n"
    "v_pk_maximum3_f16 v12, v37, v37, s50 op_sel_hi:[1,1,0] neg_lo:[0,1,0] neg_hi:[0,1,0]\n"
    "v_pk_mad_u16 v12, v12, 8, s51 op_sel_hi:[1,0,0]\n"
    "v_bfi_b32 v37, s57, v12, v37\n"
    "v_pk_maximum3_f16 v21, v38, v38, s50 op_sel_hi:[1,1,0] neg_lo:[0,1,0] neg_hi:[0,1,0]\n"
    "v_pk_mad_u16 v21, v21, 8, s51 op_sel_hi:[1,0,0]\n"
    "v_bfi_b32 v38, s57, v21, v38\n"
    "v_pk_maximum3_f16 v12, v39, v39, s50 op_sel_hi:[1,1,0] neg_lo:[0,1,0] neg_hi:[0,1,0]\n"
    "v_pk_mad_u16 v12, v12, 8, s51 op_sel_hi:[1,0,0]\n"
    "v_bfi_b32 v39, s57, v12, v39\n"
    "v_pk_maximum3_f16 v21, v40, v40, s50 op_sel_hi:[1,1,0] neg_lo:[0,1,0] neg_hi:[0,1,0]\n"
    "v_pk_mad_u16 v21, v21, 8, s51 op_sel_hi:[1,0,0]\n"
    "v_bfi_b32 v40, s57, v21, v40\n"
    "s_sub_u32 s43, 4112, s47\n"
    "s_lshl_b32 s43, s43, 19\n"
    "v_cvt_pkrtz_f16_f32 v12, s43, s43\n"
    "v_lshl_or_b32 v21, v12, 16, s47\n"
    "ds_write_b128 v15, v[28:31] offset:0\n"
    "ds_write_b128 v15, v[32:35] offset:144\n"
    "ds_write_b128 v15, v[36:39] offset:288\n"
    "ds_write_b32 v16, v40 offset:3456\n"
    "ds_write_b32 v16, v12 offset:3744\n"
    "s_mov_b64 exec, 1\n"
    "ds_or_b32 v25, v21 offset:532\n"
    "s_mov_b64 exec, -1\n"
    "s_cmp_lt_u32 s22, 2\n"
    "s_cselect_b32 s40, 16, 0\n"
    "v_add_u32 v25, s40, v25\n"
    "s_add_u32 s23, s23, 4\n"
    "v_mov_b32 v24, s23\n"
    "s_mov_b32 s36, 0\n"
    "v_mov_b32 v69, v43\n"
    "v_mov_b32 v68, v42\n"
    "s_cmp_lg_u32 s22, 0\n"
    "s_cbranch_scc1 .Lxd_on51\n"
    "v_readfirstlane_b32 s40, v24\n"
    "s_or_b32 s40, s40, s44\n"
    "s_lshl1_add_u32 s41, s40, s54\n"
    "s_cmpk_lt_u32 s40, 256\n"
    "s_cselect_b32 s41, s41, 2\n"
    "v_mov_b32 v12, s41\n"
    "s_mul_i32 s41, s41, 52\n"
    "s_add_u32 s38, s4, s41\n"
    "s_addc_u32 s39, s5, 0\n"
    "ds_write_b32 v25, v12 offset:20\n"
    "v_xor_b32 v15, 0x4000, v15\n"
    "v_xor_b32 v16, 0x4000, v16\n"
    "global_load_dwordx3 v[36:38], v17, s[38:39] offset:-104\n"
    "global_load_dword v41, v18, s[38:39] offset:-8\n"
    ".Lxd_on51:\n"
    "s_cmp_lg_u32 s20, 7\n"
    "s_cbranch_scc1 .Lxd_on53\n"
    "s_waitcnt vmcnt(0)\n"
    "v_readlane_b32 s25, v26, 9\n"
    "v_readlane_b32 s40, v26, 8\n"
    "v_readlane_b32 s41, v27, 8\n"
    "s_mul_i32 s42, s25, 0x9e3779b1\n"
    "s_lshr_b32 s42, s42, 24\n"
    "s_xor_b32 s42, s42, s40\n"
    "s_and_b32 s42, s42, 0xff\n"
    "s_xor_b32 s41, s41, s25\n"
    "s_xor_b32 s41, s41, 0xa5c30f96\n"
    "s_or_b32 s42, s41, s42\n"
    "s_lshr_b32 s41, s40, 8\n"
    "s_mul_i32 s40, s41, 0x9e3779b1\n"
    "s_xor_b32 s41, s41, 0xa5c30f96\n"
    "s_lshr_b32 s40, s40, 24\n"
    "v_xor_b32 v13, s40, v26\n"
    "v_xor_b32 v22, s41, v27\n"
    "v_lshl_or_b32 v13, v13, 24, v22\n"
    "v_cmp_ne_u32 s[66:67], 0, v13\n"
    "s_and_b32 s66, s66, 0xff\n"
    "s_or_b32 s42, s42, s66\n"
    "s_cmp_eq_u32 s20, 7\n"
    "s_cselect_b32 s42, s42, 0\n"
    "s_cmp_lg_u32 s42, 0\n"
    "s_cbranch_scc1 .Lxd_pv52\n"
    ".Lxd_pv52r:\n"
    "v_lshrrev_b32 v22, 8, v26\n"
    "v_mov_b32 v26, s40\n"
    "v_mov_b32 v27, s41\n"
    "v_add_u32_dpp v22, v22, v22 row_shl:4 row_mask:0xf bank_mask:0xf\n"
    "v_add_u32_e64 v43, s25, 1\n"
    "v_lshlrev_b32 v8, 2, v1\n"
    "v_max_u32_dpp v13, v22, v22 quad_perm:[1,0,3,2] row_mask:0xf bank_mask:0xf\n"
    "v_add_u32 v8, 154112, v8\n"
    "s_nop 0\n"
    "v_max_u32_dpp v13, v13, v13 quad_perm:[2,3,0,1] row_mask:0xf bank_mask:0xf\n"
    "v_sub_u32 v22, v22, v13\n"
    "v_subrev_u32 v13, 818, v13\n"
    "v_ldexp_f32 v22, 1.0, v22\n"
    "s_mov_b64 exec, 1\n"
    "ds_write_b64 v8, v[26:27]\n"
    "s_mov_b64 exec, 15\n"
    "ds_write2_b32 v8, v22, v13 offset0:4 offset1:8\n"
    "s_mov_b64 exec, 0x200\n"
    "global_store_dword v11, v43, s[16:17] sc1\n"
    "s_mov_b64 exec, -1\n"
    ".Lxd_on53:\n"
    "s_waitcnt lgkmcnt(0)\n"
    "s_barrier\n"
    "s_mul_i32 s42, s20, 5376\n"
    "s_add_u32 s40, s42, 1048576\n"
    "s_add_u32 s38, s16, s40\n"
    "s_addc_u32 s39, s17, 0\n"
    "s_add_u32 m0, s42, 98304\n"
    "s_nop 0\n"
    "global_load_lds_dwordx4 v68, s[38:39] offset:0 sc1\n"
    "global_load_lds_dwordx4 v68, s[38:39] offset:1024 sc1\n"
    "global_load_lds_dwordx4 v68, s[38:39] offset:2048 sc1\n"
    "global_load_lds_dwordx4 v68, s[38:39] offset:3072 sc1\n"
    "s_add_u32 m0, s42, 102400\n"
    "s_nop 0\n"
    "global_load_lds_dwordx4 v69, s[38:39] offset:0 sc1\n"
    "global_load_lds_dwordx4 v69, s[38:39] offset:256 sc1\n"
    "s_getpc_b64 s[66:67]\n"
    ".Lxd_en54:\n"
    "s_mov_b32 s40, .Lxd_ph1-.Lxd_en54\n"
    "s_cmp_eq_u32 s22, 1\n"
    "s_cselect_b32 s40, .Lxd_ph0-.Lxd_en54, s40\n"
    "s_cmp_eq_u32 s22, 2\n"
    "s_cselect_b32 s40, .Lxd_pl3-.Lxd_en54, s40\n"
    "s_cmp_eq_u32 s22, 3\n"
    "s_cselect_b32 s40, .Lxd_pl2-.Lxd_en54, s40\n"
    "s_ashr_i32 s41, s40, 31\n"
    "s_add_u32 s66, s66, s40\n"
    "s_addc_u32 s67, s67, s41\n"
    "v_mov_b32 v13, 154112\n"
    "ds_read_b64 v[26:27], v13\n"
    "v_add_u32 v42, 98304, v42\n"
    "v_mov_b32 v13, s58\n"
    "s_sub_u32 s40, 1, s22\n"
    "s_and_b32 s40, s40, 3\n"
    "v_lshl_add_u32 v7, s40, 12, v3\n"
    "v_lshl_add_u32 v8, s40, 12, v4\n"
    "v_lshl_add_u32 v9, s40, 12, v5\n"
    "v_lshl_add_u32 v10, s40, 12, v6\n"
    "v_bfe_u32 v21, v1, 4, 1\n"
    "v_lshlrev_b32 v21, 2, v21\n"
    "v_add_u32 v21, 154112, v21\n"
    "ds_read_b32 v0, v21 offset:16\n"
    "ds_read_b32 v23, v21 offset:24\n"
    "ds_read_b32 v22, v13 offset:512\n"
    "ds_read_b64_tr_b16 v[44:45], v9 offset:0\n"
    "ds_read_b64_tr_b16 v[46:47], v10 offset:0\n"
    "ds_read_b64_tr_b16 v[48:49], v7 offset:0\n"
    "ds_read_b64_tr_b16 v[50:51], v8 offset:576\n"
    "ds_read_b64_tr_b16 v[52:53], v8 offset:1152\n"
    "ds_read_b64_tr_b16 v[54:55], v8 offset:1728\n"
    "ds_read_b64_tr_b16 v[56:57], v8 offset:2304\n"
    "ds_read_b64_tr_b16 v[58:59], v8 offset:2880\n"
    "v_mov_b64 v[88:89], 0\n"
    "v_mov_b64 v[90:91], 0\n"
    "s_waitcnt vmcnt(0)\n"
    "s_barrier\n"
    "ds_read_b128 v[92:95], v42 offset:0\n"
    "ds_read_b128 v[96:99], v42 offset:1024\n"
    "ds_read_b128 v[100:103], v42 offset:2048\n"
    "ds_read_b128 v[104:107], v42 offset:3072\n"
    "ds_read_b128 v[108:111], v42 offset:4096\n"
    "ds_read_b128 v[112:115], v42 offset:5120\n"
    "ds_read_b128 v[116:119], v42 offset:6144\n"
    "ds_read_b128 v[120:123], v42 offset:7168\n"
    "ds_read_b128 v[124:127], v42 offset:8192\n"
    "ds_read_b128 v[128:131], v42 offset:9216\n"
    "ds_read_b128 v[132:135], v42 offset:10240\n"
    "ds_read_b128 v[136:139], v42 offset:11264\n"
    "ds_read_b128 v[140:143], v42 offset:12288\n"
    "ds_read_b128 v[144:147], v42 offset:13312\n"
    "s_cmp_lt_u32 s2, 8\n"
    "s_cbranch_scc1 .Lxd_hp55\n"
    "s_mov_b32 m0, 159744\n"
    "v_lshlrev_b32 v63, 7, v63\n"
    "global_load_lds_dword v63, s[8:9]\n"
    ".Lxd_hp55:\n"
    "ds_read_b128 v[148:151], v42 offset:14336\n"
    "ds_read_b128 v[152:155], v42 offset:15360\n"
    "ds_read_b128 v[156:159], v42 offset:16384\n"
    "ds_read_b128 v[160:163], v42 offset:17408\n"
    "ds_read_b128 v[164:167], v42 offset:18432\n"
    "ds_read_b128 v[168:171], v42 offset:19456\n"
    "ds_read_b128 v[172:175], v42 offset:20480\n"
    "ds_read_b128 v[176:179], v42 offset:21504\n"
    "ds_read_b128 v[180:183], v42 offset:22528\n"
    "ds_read_b128 v[184:187], v42 offset:23552\n"
    "ds_read_b128 v[188:191], v42 offset:24576\n"
    "ds_read_b128 v[192:195], v42 offset:25600\n"
    "ds_read_b128 v[196:199], v42 offset:26624\n"
    "ds_read_b128 v[200:203], v42 offset:27648\n"
    "ds_read_b128 v[204:207], v42 offset:28672\n"
    "ds_read_b128 v[208:211], v42 offset:29696\n"
    "ds_read_b128 v[212:215], v42 offset:30720\n"
    "ds_read_b128 v[216:219], v42 offset:31744\n"
    "ds_read_b128 v[220:223], v42 offset:32768\n"
    "ds_read_b128 v[224:227], v42 offset:33792\n"
    "ds_read_b128 v[228:231], v42 offset:34816\n"
    "ds_read_b128 v[232:235], v42 offset:35840\n"
    "ds_read_b128 v[236:239], v42 offset:36864\n"
    "ds_read_b128 v[240:243], v42 offset:37888\n"
    "ds_read_b128 v[244:247], v42 offset:38912\n"
    "ds_read_b128 v[248:251], v42 offset:39936\n"
    "ds_read_b128 v[252:255], v42 offset:40960\n"
    "s_waitcnt lgkmcnt(15)\n"
    "v_readfirstlane_b32 s45, v22\n"
    "v_readfirstlane_b32 s28, v26\n"
    "v_readfirstlane_b32 s29, v27\n"
    "v_pk_mul_f16 v64, v44, v46\n"
    "v_pk_mul_f16 v65, v45, v47\n"
    "v_pk_mul_f16 v66, v48, v48 op_sel_hi:[0,1]\n"
    "v_pk_mul_f16 v67, v48, v49 op_sel_hi:[0,1]\n"
    "v_pk_mul_f16 v60, v44, s45 op_sel:[0,1]\n"
    "v_pk_mul_f16 v61, v45, s45 op_sel:[0,1]\n"
    "v_pk_mul_f16 v62, v46, s45 op_sel:[0,1]\n"
    "v_pk_mul_f16 v63, v47, s45 op_sel:[0,1]\n"
    "s_waitcnt lgkmcnt(0)\n"
    "s_branch .Lxd_enter\n"
    ".Lxd_slow:\n"
    "s_cmp_eq_u32 s45, -1\n"
    "s_cbranch_scc1 .Lxd_sl7\n"
    "s_mov_b32 s59, 0x100000\n"
    "s_and_b32 s40, s45, 0xff0\n"
    "s_cbranch_scc0 .Lxd_sl1\n"
    ".Lxd_sl2:\n"
    "s_cmp_ge_i32 s45, -16\n"
    "s_cbranch_scc1 .Lxd_sl3\n"
    "s_bitset0_b32 s45, 31\n"
    ".Lxd_sl6:\n"
    "s_cmp_lg_u32 s56, 0\n"
    "s_cbranch_scc1 .Lxd_sl9\n"
    "v_readfirstlane_b32 s40, v13\n"
    "s_lshl4_add_u32 s41, s22, s58\n"
    "s_mov_b32 s56, 2\n"
    "s_cmp_lt_u32 s40, s41\n"
    "s_cbranch_scc0 .Lxd_gran\n"
    ".Lxd_sl9:\n"
    "s_setpc_b64 s[60:61]\n"
    ".Lxd_sl1:\n"
    "s_sleep 1\n"
    "ds_read_b32 v22, v13 offset:512\n"
    "s_waitcnt lgkmcnt(0)\n"
    "v_readfirstlane_b32 s45, v22\n"
    "s_sub_u32 s59, s59, 1\n"
    "s_cbranch_scc1 .Lxd_sl5\n"
    "s_and_b32 s40, s45, 0xff0\n"
    "s_cbranch_scc0 .Lxd_sl1\n"
    "s_cmp_gt_i32 s45, 0\n"
    "s_cbranch_scc0 .Lxd_sl2\n"
    "s_setpc_b64 s[60:61]\n"
    ".Lxd_sl3:\n"
    "v_lshl_add_u32 v13, v1, 2, v13\n"
    "s_mov_b64 exec, 7\n"
    ".Lxd_sl4:\n"
    "ds_read_b32 v22, v13 offset:4\n"
    "s_waitcnt lgkmcnt(0)\n"
    "v_cmp_eq_u32 s[40:41], 0, v22\n"
    "v_cmp_ne_u32 s[42:43], -1, v22\n"
    "s_sub_u32 s59, s59, 1\n"
    "s_cbranch_scc1 .Lxd_sl5\n"
    "s_cmp_lg_u32 s40, 0\n"
    "s_cbranch_scc1 .Lxd_sl4\n"
    "s_cmp_eq_u32 s42, 0\n"
    "s_cbranch_scc1 .Lxd_sl5\n"
    "s_mov_b64 exec, -1\n"
    "s_mov_b32 s45, 1\n"
    "s_mov_b32 s56, 0\n"
    "s_setpc_b64 s[60:61]\n"
    ".Lxd_sl5:\n"
    "s_mov_b64 exec, -1\n"
    "s_mov_b32 s45, -1\n"
    ".Lxd_sl7:\n"
    "s_cmp_lg_u32 s56, 1\n"
    "s_cbranch_scc1 .Lxd_sl6\n"
    "s_branch .Lxd_dsum\n"
    ".Lxd_gran:\n"
    "s_lshl4_add_u32 s40, s22, s58\n"
    "v_lshrrev_b32 v13, 4, v1\n"
    "v_lshl_add_u32 v13, v13, 2, s40\n"
    "s_waitcnt vmcnt(0)\n"
    "ds_read2_b32 v[26:27], v13 offset1:16\n"
    "v_and_b32 v13, 15, v1\n"
    "v_lshlrev_b32 v13, 15, v13\n"
    "s_mov_b32 s42, 0x1fff1fff\n"
    "s_mov_b32 s43, s42\n"
    "s_add_u32 s40, s16, 65528\n"
    "s_addc_u32 s41, s17, 0\n"
    "v_mov_b32 v42, 0\n"
    "s_mov_b32 s56, 2\n"
    "s_waitcnt lgkmcnt(0)\n"
    "v_cmp_lt_i32 s[30:31], 0, v26\n"
    "v_cmp_lt_i32 s[64:65], 0, v27\n"
    "v_lshl_add_u32 v43, v26, 2, v13\n"
    "v_lshl_add_u32 v27, v27, 2, v13\n"
    "v_mov_b32 v26, 0\n"
    "s_and_b64 s[30:31], s[30:31], s[42:43]\n"
    "s_cbranch_scc0 .Lxd_gr30\n"
    "s_mov_b64 exec, s[30:31]\n"
    "global_load_dwordx2 v[42:43], v43, s[40:41] sc1\n"
    "s_mov_b64 exec, -1\n"
    "s_mov_b32 s56, 1\n"
    ".Lxd_gr30:\n"
    "s_and_b64 s[64:65], s[64:65], s[42:43]\n"
    "s_cbranch_scc0 .Lxd_gr64\n"
    "s_mov_b64 exec, s[64:65]\n"
    "global_load_dwordx2 v[26:27], v27, s[40:41] sc1\n"
    "s_mov_b64 exec, -1\n"
    "s_mov_b32 s56, 1\n"
    ".Lxd_gr64:\n"
    "s_setpc_b64 s[60:61]\n"
    ".Lxd_dsum:\n"
    "s_lshl4_add_u32 s42, s22, s58\n"
    "s_waitcnt vmcnt(0)\n"
    "s_cmp_eq_u64 s[30:31], 0\n"
    "s_cbranch_scc1 .Lxd_ds30\n"
    "v_xor_b32 v13, s28, v42\n"
    "v_xor_b32 v22, s29, v43\n"
    "v_lshl_or_b32 v13, v13, 24, v22\n"
    "v_and_b32 v22, 0xffffff00, v42\n"
    "v_cmp_ne_u32 s[40:41], 0, v13\n"
    "v_lshrrev_b32 v13, 4, v1\n"
    "v_add_f32_dpp v22, v22, v22 quad_perm:[1,0,3,2] row_mask:0xf bank_mask:0xf\n"
    "s_and_b64 s[40:41], s[40:41], s[30:31]\n"
    "s_cbranch_scc1 .Lxd_ds9\n"
    "v_add_f32_dpp v22, v22, v22 quad_perm:[2,3,0,1] row_mask:0xf bank_mask:0xf\n"
    "v_lshl_add_u32 v13, v13, 2, s42\n"
    "s_nop 0\n"
    "v_add_f32_dpp v22, v22, v22 row_half_mirror row_mask:0xf bank_mask:0xf\n"
    "s_nop 1\n"
    "v_add_f32_dpp v22, v22, v22 row_mirror row_mask:0xf bank_mask:0xf\n"
    "ds_write_b32 v13, v22 offset:1024\n"
    ".Lxd_ds30:\n"
    "s_cmp_eq_u64 s[64:65], 0\n"
    "s_cbranch_scc1 .Lxd_ds64\n"
    "v_xor_b32 v13, s28, v26\n"
    "v_xor_b32 v22, s29, v27\n"
    "v_lshl_or_b32 v13, v13, 24, v22\n"
    "v_and_b32 v22, 0xffffff00, v26\n"
    "v_cmp_ne_u32 s[40:41], 0, v13\n"
    "v_lshrrev_b32 v13, 4, v1\n"
    "v_add_f32_dpp v22, v22, v22 quad_perm:[1,0,3,2] row_mask:0xf bank_mask:0xf\n"
    "s_and_b64 s[40:41], s[40:41], s[64:65]\n"
    "s_cbranch_scc1 .Lxd_ds9\n"
    "v_add_f32_dpp v22, v22, v22 quad_perm:[2,3,0,1] row_mask:0xf bank_mask:0xf\n"
    "v_lshl_add_u32 v13, v13, 2, s42\n"
    "s_nop 0\n"
    "v_add_f32_dpp v22, v22, v22 row_half_mirror row_mask:0xf bank_mask:0xf\n"
    "s_nop 1\n"
    "v_add_f32_dpp v22, v22, v22 row_mirror row_mask:0xf bank_mask:0xf\n"
    "ds_write_b32 v13, v22 offset:1088\n"
    ".Lxd_ds64:\n"
    "s_mov_b32 s56, 2\n"
    ".Lxd_ds9:\n"
    "s_setpc_b64 s[60:61]\n"
    ".Lxd_mark:\n"
    "v_lshl_add_u32 v13, v1, 2, v13\n"
    "s_add_u32 s40, s58, 248\n"
    "v_mov_b32 v24, -1\n"
    "v_cmp_gt_u32 s[42:43], s40, v13\n"
    "s_mov_b32 s62, 0x88888888\n"
    "s_mov_b32 s63, s62\n"
    "s_and_b64 exec, s[42:43], s[62:63]\n"
    "ds_write_b32 v13, v24 offset:8\n"
    "s_mov_b64 exec, s[42:43]\n"
    "v_cndmask_b32 v24, 0, -16, s[62:63]\n"
    "s_brev_b32 s40, 1\n"
    "v_writelane_b32 v24, s40, 2\n"
    "s_lshl_b32 s40, 1, s22\n"
    "v_or_b32 v24, s40, v24\n"
    "s_mov_b32 s36, 1\n"
    "ds_or_b32 v13, v24 offset:520\n"
    "s_mov_b64 exec, -1\n"
    "s_setpc_b64 s[60:61]\n"
    ".Lxd_tf12:\n"
    "v_mov_b32 v12, -1\n"
    "v_mov_b32 v13, v25\n"
    "s_call_b64 s[60:61], .Lxd_mark\n"
    "s_branch .Lxd_tf12r\n"
    ".Lxd_ck15:\n"
    "v_add_u32 v13, 4, v25\n"
    "s_call_b64 s[60:61], .Lxd_slow\n"
    "s_branch .Lxd_ck15r\n"
    ".Lxd_ck18:\n"
    "v_add_u32 v13, 8, v25\n"
    "s_call_b64 s[60:61], .Lxd_slow\n"
    "s_branch .Lxd_ck18r\n"
    ".Lxd_ck21:\n"
    "v_add_u32 v13, 12, v25\n"
    "s_call_b64 s[60:61], .Lxd_slow\n"
    "s_branch .Lxd_ck21r\n"
    ".Lxd_ck24:\n"
    "v_add_u32 v13, 16, v25\n"
    "s_call_b64 s[60:61], .Lxd_slow\n"
    "s_branch .Lxd_ck24r\n"
    ".Lxd_dr25:\n"
    "v_add_u32 v13, 16, v25\n"
    "s_call_b64 s[60:61], .Lxd_mark\n"
    "s_branch .Lxd_dr25r\n"
    ".Lxd_ck28:\n"
    "v_add_u32 v13, 4, v25\n"
    "s_call_b64 s[60:61], .Lxd_slow\n"
    "s_branch .Lxd_ck28r\n"
    ".Lxd_ck31:\n"
    "v_add_u32 v13, 8, v25\n"
    "s_call_b64 s[60:61], .Lxd_slow\n"
    "s_branch .Lxd_ck31r\n"
    ".Lxd_ck34:\n"
    "v_add_u32 v13, 12, v25\n"
    "s_call_b64 s[60:61], .Lxd_slow\n"
    "s_branch .Lxd_ck34r\n"
    ".Lxd_ck37:\n"
    "v_add_u32 v13, 16, v25\n"
    "s_call_b64 s[60:61], .Lxd_slow\n"
    "s_branch .Lxd_ck37r\n"
    ".Lxd_dr39:\n"
    "v_add_u32 v13, 16, v25\n"
    "s_call_b64 s[60:61], .Lxd_mark\n"
    "s_branch .Lxd_dr39r\n"
    ".Lxd_on38:\n"
    "s_and_b32 s40, s45, 15\n"
    "s_cbranch_scc1 .Lxd_dr39\n"
    "v_mov_b32 v24, 1\n"
    "s_mov_b64 exec, 1\n"
    "global_atomic_add v24, v17, v24, s[52:53] sc0 sc1\n"
    "s_mov_b64 exec, -1\n"
    ".Lxd_dr39r:\n"
    "s_branch .Lxd_on38r\n"
    ".Lxd_f2:\n"
    "s_mov_b32 s59, 0x20000\n"
    ".Lxd_f4:\n"
    "s_call_b64 s[60:61], .Lxd_dsum\n"
    "s_cmp_eq_u32 s56, 2\n"
    "s_cbranch_scc1 .Lxd_f3\n"
    "s_sleep 2\n"
    "s_call_b64 s[60:61], .Lxd_gran\n"
    "s_sub_u32 s59, s59, 1\n"
    "s_cbranch_scc0 .Lxd_f4\n"
    "s_waitcnt vmcnt(0)\n"
    "s_branch .Lxd_f3\n"
    ".Lxd_on3:\n"
    "s_mov_b64 exec, 0x3ff\n"
    "global_load_dwordx2 v[26:27], v11, s[16:17] sc1\n"
    "s_mov_b64 exec, -1\n"
    "s_branch .Lxd_on3r\n"
    ".Lxd_pv4:\n"
    "s_mov_b32 s59, 0x20000\n"
    ".Lxd_pv4l:\n"
    "s_sleep 2\n"
    "s_mov_b64 exec, 0x3ff\n"
    "global_load_dwordx2 v[26:27], v11, s[16:17] sc1\n"
    "s_mov_b64 exec, -1\n"
    "s_waitcnt vmcnt(0)\n"
    "v_readlane_b32 s25, v26, 9\n"
    "v_readlane_b32 s40, v26, 8\n"
    "v_readlane_b32 s41, v27, 8\n"
    "s_mul_i32 s42, s25, 0x9e3779b1\n"
    "s_lshr_b32 s42, s42, 24\n"
    "s_xor_b32 s42, s42, s40\n"
    "s_and_b32 s42, s42, 0xff\n"
    "s_xor_b32 s41, s41, s25\n"
    "s_xor_b32 s41, s41, 0xa5c30f96\n"
    "s_or_b32 s42, s41, s42\n"
    "s_lshr_b32 s41, s40, 8\n"
    "s_mul_i32 s40, s41, 0x9e3779b1\n"
    "s_xor_b32 s41, s41, 0xa5c30f96\n"
    "s_lshr_b32 s40, s40, 24\n"
    "v_xor_b32 v13, s40, v26\n"
    "v_xor_b32 v22, s41, v27\n"
    "v_lshl_or_b32 v13, v13, 24, v22\n"
    "v_cmp_ne_u32 s[66:67], 0, v13\n"
    "s_and_b32 s66, s66, 0xff\n"
    "s_or_b32 s42, s42, s66\n"
    "s_cmp_eq_u32 s42, 0\n"
    "s_cbranch_scc1 .Lxd_pv4r\n"
    "s_sub_u32 s59, s59, 1\n"
    "s_cbranch_scc0 .Lxd_pv4l\n"
    "s_branch .Lxd_pv4r\n"
    ".Lxd_on5:\n"
    "s_mov_b64 exec, 1\n"
    "ds_write_b64 v8, v[26:27]\n"
    "s_mov_b64 exec, 15\n"
    "ds_write2_b32 v8, v22, v13 offset0:4 offset1:8\n"
    "s_mov_b64 exec, 0x200\n"
    "global_store_dword v11, v43, s[16:17] sc1\n"
    "s_mov_b64 exec, -1\n"
    "s_branch .Lxd_on5r\n"
    ".Lxd_ck7:\n"
    "v_mov_b32 v13, s58\n"
    "s_call_b64 s[60:61], .Lxd_slow\n"
    "s_branch .Lxd_ck7r\n"
    ".Lxd_on8:\n"
    "v_mov_b32 v24, 1\n"
    "s_mov_b64 exec, 1\n"
    "global_atomic_add v24, v17, v24, s[52:53] sc0 sc1\n"
    "s_mov_b64 exec, -1\n"
    "s_branch .Lxd_on8r\n"
    ".Lxd_tf9:\n"
    "v_mov_b32 v12, -1\n"
    "v_mov_b32 v13, v25\n"
    "s_call_b64 s[60:61], .Lxd_mark\n"
    "s_branch .Lxd_tf9r\n"
    ".Lxd_on10:\n"
    "s_waitcnt vmcnt(0)\n"
    "v_readfirstlane_b32 s40, v24\n"
    "s_or_b32 s40, s40, s44\n"
    "s_lshl1_add_u32 s41, s40, s54\n"
    "s_cmpk_lt_u32 s40, 256\n"
    "s_cselect_b32 s41, s41, 2\n"
    "v_mov_b32 v12, s41\n"
    "s_cbranch_scc0 .Lxd_tf9\n"
    ".Lxd_tf9r:\n"
    "s_mul_i32 s41, s41, 52\n"
    "s_add_u32 s38, s4, s41\n"
    "s_addc_u32 s39, s5, 0\n"
    "ds_write_b32 v25, v12 offset:20\n"
    "v_xor_b32 v15, 0x4000, v15\n"
    "v_xor_b32 v16, 0x4000, v16\n"
    "global_load_dwordx3 v[36:38], v17, s[38:39] offset:-104\n"
    "global_load_dword v41, v18, s[38:39] offset:-8\n"
    "s_branch .Lxd_on10r\n"
    ".Lxd_on11:\n"
    "v_mov_b32 v24, 1\n"
    "s_mov_b64 exec, 1\n"
    "global_atomic_add v24, v17, v24, s[52:53] sc0 sc1\n"
    "s_mov_b64 exec, -1\n"
    "s_branch .Lxd_on11r\n"
    ".Lxd_pv52:\n"
    "s_mov_b32 s59, 0x20000\n"
    ".Lxd_pv52l:\n"
    "s_sleep 2\n"
    "s_mov_b64 exec, 0x3ff\n"
    "global_load_dwordx2 v[26:27], v11, s[16:17] sc1\n"
    "s_mov_b64 exec, -1\n"
    "s_waitcnt vmcnt(0)\n"
    "v_readlane_b32 s25, v26, 9\n"
    "v_readlane_b32 s40, v26, 8\n"
    "v_readlane_b32 s41, v27, 8\n"
    "s_mul_i32 s42, s25, 0x9e3779b1\n"
    "s_lshr_b32 s42, s42, 24\n"
    "s_xor_b32 s42, s42, s40\n"
    "s_and_b32 s42, s42, 0xff\n"
    "s_xor_b32 s41, s41, s25\n"
    "s_xor_b32 s41, s41, 0xa5c30f96\n"
    "s_or_b32 s42, s41, s42\n"
    "s_lshr_b32 s41, s40, 8\n"
    "s_mul_i32 s40, s41, 0x9e3779b1\n"
    "s_xor_b32 s41, s41, 0xa5c30f96\n"
    "s_lshr_b32 s40, s40, 24\n"
    "v_xor_b32 v13, s40, v26\n"
    "v_xor_b32 v22, s41, v27\n"
    "v_lshl_or_b32 v13, v13, 24, v22\n"
    "v_cmp_ne_u32 s[66:67], 0, v13\n"
    "s_and_b32 s66, s66, 0xff\n"
    "s_or_b32 s42, s42, s66\n"
    "s_cmp_eq_u32 s42, 0\n"
    "s_cbranch_scc1 .Lxd_pv52r\n"
    "s_sub_u32 s59, s59, 1\n"
    "s_cbranch_scc0 .Lxd_pv52l\n"
    "s_branch .Lxd_pv52r\n"
    ".Lxd_end:\n"
    : "+{v0}"(tid) : "{s[0:1]}"(__builtin_amdgcn_kernarg_segment_ptr()), "{s2}"(blockIdx.x)
    : "memory", "vcc", "scc", "v1", "v2", "v3", "v4", "v5", "v6", "v7", "v8", "v9", "v10", "v11", "v12", "v13", "v14", "v15", "v16", "v17", "v18", "v19", "v20", "v21", "v22", "v23", "v24", "v25", "v26", "v27", "v28", "v29", "v30", "v31", "v32", "v33", "v34", "v35", "v36", "v37", "v38", "v39", "v40", "v41", "v42", "v43", "v44", "v45", "v46", "v47", "v48", "v49", "v50", "v51", "v52", "v53", "v54", "v55", "v56", "v57", "v58", "v59", "v60", "v61", "v62", "v63", "v64", "v65", "v66", "v67", "v68", "v69", "v70", "v71", "v72", "v73", "v74", "v75", "v76", "v77", "v78", "v79", "v80", "v81", "v82", "v83", "v84", "v85", "v86", "v87", "v88", "v89", "v90", "v91", "v92", "v93", "v94", "v95", "v96", "v97", "v98", "v99", "v100", "v101", "v102", "v103", "v104", "v105", "v106", "v107", "v108", "v109", "v110", "v111", "v112", "v113", "v114", "v115", "v116", "v117", "v118", "v119", "v120", "v121", "v122", "v123", "v124", "v125", "v126", "v127", "v128", "v129", "v130", "v131", "v132", "v133", "v134", "v135", "v136", "v137", "v138", "v139", "v140", "v141", "v142", "v143", "v144", "v145", "v146", "v147", "v148", "v149", "v150", "v151", "v152", "v153", "v154", "v155", "v156", "v157", "v158", "v159", "v160", "v161", "v162", "v163", "v164", "v165", "v166", "v167", "v168", "v169", "v170", "v171", "v172", "v173", "v174", "v175", "v176", "v177", "v178", "v179", "v180", "v181", "v182", "v183", "v184", "v185", "v186", "v187", "v188", "v189", "v190", "v191", "v192", "v193", "v194", "v195", "v196", "v197", "v198", "v199", "v200", "v201", "v202", "v203", "v204", "v205", "v206", "v207", "v208", "v209", "v210", "v211", "v212", "v213", "v214", "v215", "v216", "v217", "v218", "v219", "v220", "v221", "v222", "v223", "v224", "v225", "v226", "v227", "v228", "v229", "v230", "v231", "v232", "v233", "v234", "v235", "v236", "v237", "v238", "v239", "v240", "v241", "v242", "v243", "v244", "v245", "v246", "v247", "v248", "v249", "v250", "v251", "v252", "v253", "v254", "v255", "s3", "s4", "s5", "s6", "s7", "s8", "s9", "s10", "s11", "s12", "s13", "s14", "s15", "s16", "s17", "s18", "s19", "s20", "s21", "s22", "s23", "s24", "s25", "s26", "s27", "s28", "s29", "s30", "s31", "s34", "s35", "s36", "s37", "s38", "s39", "s40", "s41", "s42", "s43", "s44", "s45", "s46", "s47", "s48", "s49", "s50", "s51", "s52", "s53", "s54", "s55", "s56", "s57", "s58", "s59", "s60", "s61", "s62", "s63", "s64", "s65", "s66", "s67", "s68", "s69", "s70", "s71");
  asm volatile("" :: "v"((__attribute__((address_space(3))) char*)shm));
}

extern "C" void kernel_launch(void* const* d_in, const int* in_sizes, int n_in, void* d_out, int out_size,
                              void* d_ws, size_t ws_size, hipStream_t stream) {
  xd_kernel<<<256, 512, 0, stream>>>((const int*)d_in[0], (const u16*)d_in[4], (const u16*)d_in[1], (const u16*)d_in[5], (const u16*)d_in[2], (const u16*)d_in[3], (char*)d_ws,
                                     (const u16*)d_in[6], (const u16*)d_in[7], (u16*)d_out);
}
```

```cpp
#include <hip/hip_runtime.h>
typedef unsigned short u16;

__device__ const __attribute__((aligned(16), used, visibility("hidden"))) u16 xd_tab[5120] asm("xd_tab") = {924, 925, 928, 929, 926, 927, 930, 931, 934, 935, 938, 939, 932, 933, 936, 937, 940, 941, 944, 945, 942, 943, 946, 947, 948, 0, 949, 0, 0, 0, 0, 0, 2, 36, 138, 172, 0, 1, 24, 25, 338, 372, 474, 508, 34, 35, 57, 58, 546, 580, 682, 716, 68, 69, 90, 91, 816, 817, 850, 952, 102, 99, 123, 124, 680, 681, 684, 685, 20, 21, 22, 23, 714, 715, 717, 718, 54, 55, 56, 53, 748, 749, 750, 751, 88, 89, 86, 87, 782, 779, 783, 784, 122, 119, 120, 121, 152, 153, 154, 155, 284, 285, 286, 287, 186, 187, 188, 185, 318, 319, 320, 317, 220, 221, 218, 219, 352, 353, 350, 351, 254, 251, 252, 253, 386, 383, 384, 385, 416, 417, 418, 419, 548, 549, 550, 551, 450, 451, 452, 449, 582, 583, 584, 581, 484, 485, 482, 483, 616, 617, 614, 615, 518, 515, 516, 517, 650, 647, 648, 649, 544, 545, 552, 553, 16, 17, 18, 19, 578, 579, 585, 586, 50, 51, 52, 49, 612, 613, 618, 619, 84, 85, 82, 83, 646, 643, 651, 652, 118, 115, 116, 117, 148, 149, 150, 151, 280, 281, 282, 283, 182, 183, 184, 181, 314, 315, 316, 313, 216, 217, 214, 215, 348, 349, 346, 347, 250, 247, 248, 249, 382, 379, 380, 381, 412, 413, 414, 415, 408, 409, 420, 421, 446, 447, 448, 445, 442, 443, 453, 454, 480, 481, 478, 479, 476, 477, 486, 487, 514, 511, 512, 513, 510, 507, 519, 520, 12, 13, 14, 15, 144, 145, 146, 147, 46, 47, 48, 45, 178, 179, 180, 177, 80, 81, 78, 79, 212, 213, 210, 211, 114, 111, 112, 113, 246, 243, 244, 245, 276, 277, 278, 279, 272, 273, 288, 289, 310, 311, 312, 309, 306, 307, 321, 322, 344, 345, 342, 343, 340, 341, 354, 355, 378, 375, 376, 377, 374, 371, 387, 388, 8, 9, 10, 11, 140, 141, 142, 143, 42, 43, 44, 41, 174, 175, 176, 173, 76, 77, 74, 75, 208, 209, 206, 207, 110, 107, 108, 109, 242, 239, 240, 241, 136, 137, 156, 157, 4, 5, 6, 7, 170, 171, 189, 190, 38, 39, 40, 37, 204, 205, 222, 223, 72, 73, 70, 71, 238, 235, 255, 256, 106, 103, 104, 105, 0, 0, 0, 0, 0, 0, 0, 0, 0, 0, 0, 0, 0, 0, 0, 0, 0, 0, 0, 0, 0, 0, 0, 0, 0, 0, 0, 0, 0, 0, 0, 0, 0, 0, 0, 0, 0, 0, 0, 0, 0, 0, 0, 0, 0, 0, 0, 0, 0, 0, 0, 0, 0, 0, 0, 0, 0, 0, 0, 0, 0, 0, 0, 0, 0, 0, 0, 0, 0, 0, 0, 0, 0, 0, 0, 0, 0, 0, 0, 0, 0, 0, 0, 0, 0, 0, 0, 0, 0, 0, 0, 0, 0, 0, 0, 0, 0, 0, 0, 0, 0, 0, 0, 0, 0, 0, 0, 0, 0, 0, 0, 0, 0, 0, 0, 0, 0, 0, 0, 0, 0, 0, 0, 0, 0, 0, 0, 0, 924, 925, 926, 927, 934, 935, 932, 933, 940, 941, 942, 943, 948, 0, 0, 0, 2, 36, 0, 1, 338, 372, 34, 35, 546, 580, 68, 69, 816, 817, 102, 99, 928, 929, 930, 931, 938, 939, 936, 937, 944, 945, 946, 947, 949, 0, 0, 0, 138, 172, 24, 25, 474, 508, 57, 58, 682, 716, 90, 91, 850, 952, 123, 124, 680, 681, 20, 21, 714, 715, 54, 55, 748, 749, 88, 89, 782, 779, 122, 119, 152, 153, 284, 285, 186, 187, 318, 319, 220, 221, 352, 353, 254, 251, 386, 383, 684, 685, 22, 23, 717, 718, 56, 53, 750, 751, 86, 87, 783, 784, 120, 121, 154, 155, 286, 287, 188, 185, 320, 317, 218, 219, 350, 351, 252, 253, 384, 385, 416, 417, 548, 549, 450, 451, 582, 583, 484, 485, 616, 617, 518, 515, 650, 647, 544, 545, 16, 17, 578, 579, 50, 51, 612, 613, 84, 85, 646, 643, 118, 115, 418, 419, 550, 551, 452, 449, 584, 581, 482, 483, 614, 615, 516, 517, 648, 649, 552, 553, 18, 19, 585, 586, 52, 49, 618, 619, 82, 83, 651, 652, 116, 117, 148, 149, 280, 281, 182, 183, 314, 315, 216, 217, 348, 349, 250, 247, 382, 379, 412, 413, 408, 409, 446, 447, 442, 443, 480, 481, 476, 477, 514, 511, 510, 507, 150, 151, 282, 283, 184, 181, 316, 313, 214, 215, 346, 347, 248, 249, 380, 381, 414, 415, 420, 421, 448, 445, 453, 454, 478, 479, 486, 487, 512, 513, 519, 520, 12, 13, 144, 145, 46, 47, 178, 179, 80, 81, 212, 213, 114, 111, 246, 243, 276, 277, 272, 273, 310, 311, 306, 307, 344, 345, 340, 341, 378, 375, 374, 371, 14, 15, 146, 147, 48, 45, 180, 177, 78, 79, 210, 211, 112, 113, 244, 245, 278, 279, 288, 289, 312, 309, 321, 322, 342, 343, 354, 355, 376, 377, 387, 388, 8, 9, 140, 141, 42, 43, 174, 175, 76, 77, 208, 209, 110, 107, 242, 239, 136, 137, 4, 5, 170, 171, 38, 39, 204, 205, 72, 73, 238, 235, 106, 103, 10, 11, 142, 143, 44, 41, 176, 173, 74, 75, 206, 207, 108, 109, 240, 241, 156, 157, 6, 7, 189, 190, 40, 37, 222, 223, 70, 71, 255, 256, 104, 105, 0, 0, 0, 0, 0, 0, 0, 0, 0, 0, 0, 0, 0, 0, 0, 0, 0, 0, 0, 0, 0, 0, 0, 0, 0, 0, 0, 0, 0, 0, 0, 0, 0, 0, 0, 0, 0, 0, 0, 0, 0, 0, 0, 0, 0, 0, 0, 0, 0, 0, 0, 0, 0, 0, 0, 0, 0, 0, 0, 0, 0, 0, 0, 0, 0, 0, 0, 0, 0, 0, 0, 0, 0, 0, 0, 0, 0, 0, 0, 0, 0, 0, 0, 0, 0, 0, 0, 0, 0, 0, 0, 0, 0, 0, 0, 0, 0, 0, 0, 0, 0, 0, 0, 0, 0, 0, 0, 0, 0, 0, 0, 0, 0, 0, 0, 0, 0, 0, 0, 0, 0, 0, 0, 0, 0, 0, 0, 0, 0, 0, 0, 0, 0, 0, 0, 0, 0, 0, 0, 0, 0, 0, 0, 0, 0, 0, 0, 0, 0, 0, 0, 0, 0, 0, 0, 0, 0, 0, 0, 0, 0, 16256, 0, 16256, 0, 16256, 0, 16256, 0, 16256, 0, 16256, 0, 16256, 0, 16256, 0, 16256, 0, 16256, 0, 16256, 0, 16256, 0, 16128, 0, 16256, 0, 16128, 0, 0, 0, 16128, 0, 16256, 0, 16256, 0, 16256, 0, 16128, 0, 16256, 0, 16256, 0, 16256, 0, 16128, 0, 16256, 0, 16256, 0, 16256, 0, 16128, 0, 16256, 0, 16256, 0, 16256, 0, 16256, 0, 16256, 0, 16256, 0, 16256, 0, 16256, 0, 16256, 0, 16256, 0, 16256, 0, 16256, 0, 16256, 0, 16256, 0, 16256, 0, 16256, 0, 16256, 0, 16256, 0, 16256, 0, 16256, 0, 16256, 0, 16256, 0, 16256, 0, 16256, 0, 16256, 0, 16256, 0, 16256, 0, 16256, 0, 16256, 0, 16256, 0, 16256, 0, 16256, 0, 16256, 0, 16256, 0, 16256, 0, 16128, 0, 16256, 0, 16256, 0, 16256, 0, 16128, 0, 16256, 0, 16256, 0, 16256, 0, 16128, 0, 16256, 0, 16256, 0, 16256, 0, 16128, 0, 16256, 0, 16256, 0, 16256, 0, 16256, 0, 16256, 0, 16256, 0, 16256, 0, 16256, 0, 16256, 0, 16256, 0, 16256, 0, 16256, 0, 16256, 0, 16256, 0, 16256, 0, 16256, 0, 16256, 0, 16256, 0, 16256, 0, 16256, 0, 16256, 0, 16256, 0, 16256, 0, 16256, 0, 16256, 0, 16256, 0, 16256, 0, 16256, 0, 16256, 0, 16256, 0, 16256, 0, 16256, 0, 16256, 0, 16256, 0, 16256, 0, 16256, 0, 16256, 0, 16256, 0, 16256, 0, 16256, 0, 16256, 0, 16256, 0, 16256, 0, 16256, 0, 16256, 0, 16256, 0, 16256, 0, 16256, 0, 16256, 0, 16256, 0, 16256, 0, 16256, 0, 16256, 0, 16256, 0, 16256, 0, 16256, 0, 16256, 0, 16256, 0, 16256, 0, 16256, 0, 16256, 0, 16256, 0, 16256, 0, 16256, 0, 16256, 0, 16256, 0, 16256, 0, 16256, 0, 16256, 0, 16256, 0, 16256, 0, 16256, 0, 16256, 0, 16256, 0, 16256, 0, 16256, 0, 16256, 0, 16256, 0, 16256, 0, 16256, 0, 16256, 0, 16256, 0, 16256, 0, 16128, 0, 16256, 0, 16256, 0, 16256, 0, 16128, 0, 16256, 0, 16256, 0, 16256, 0, 16128, 0, 16256, 0, 16256, 0, 16256, 0, 16128, 0, 16256, 0, 16256, 0, 16256, 0, 0, 0, 0, 0, 0, 0, 0, 0, 0, 0, 0, 0, 0, 0, 0, 0, 0, 0, 0, 0, 0, 0, 0, 0, 0, 0, 0, 0, 0, 0, 0, 0, 0, 0, 0, 0, 0, 0, 0, 0, 0, 0, 0, 0, 0, 0, 0, 0, 0, 0, 0, 0, 0, 0, 0, 0, 0, 0, 0, 0, 0, 0, 0, 0, 0, 0, 0, 0, 0, 0, 0, 0, 0, 0, 0, 0, 0, 0, 0, 0, 0, 0, 0, 0, 0, 0, 0, 0, 0, 0, 0, 0, 0, 0, 0, 0, 0, 0, 0, 0, 0, 0, 0, 0, 0, 0, 0, 0, 0, 0, 0, 0, 0, 0, 0, 0, 0, 0, 0, 0, 0, 0, 0, 0, 0, 0, 0, 0, 0, 0, 0, 0, 0, 0, 0, 0, 0, 0, 0, 0, 0, 0, 0, 0, 0, 0, 0, 0, 0, 0, 0, 0, 0, 0, 0, 0, 0, 0, 0, 0, 16128, 0, 16256, 0, 16256, 0, 16256, 0, 16128, 0, 16256, 0, 16256, 0, 16256, 0, 16128, 0, 16256, 0, 16256, 0, 16256, 0, 16128, 0, 16256, 0, 16256, 0, 16256, 0, 16256, 0, 16256, 0, 16256, 0, 16256, 0, 16256, 0, 16256, 0, 16256, 0, 16256, 0, 16256, 0, 16256, 0, 16256, 0, 16256, 0, 16256, 0, 16256, 0, 16256, 0, 16256, 0, 16256, 0, 16256, 0, 16256, 0, 16256, 0, 16256, 0, 16256, 0, 16256, 0, 16256, 0, 16256, 0, 16256, 0, 16256, 0, 16256, 0, 16256, 0, 16256, 0, 16256, 0, 16256, 0, 16256, 0, 16256, 0, 16256, 0, 16256, 0, 16256, 0, 16256, 0, 16256, 0, 16256, 0, 16256, 0, 16256, 0, 16256, 0, 16256, 0, 16256, 0, 16256, 0, 16256, 0, 16256, 0, 16256, 0, 16256, 0, 16256, 0, 16256, 0, 16256, 0, 16256, 0, 16256, 0, 16256, 0, 16256, 0, 16256, 0, 16256, 0, 16256, 0, 16256, 0, 16256, 0, 16256, 0, 16256, 0, 16256, 0, 16256, 0, 16256, 0, 16256, 0, 16256, 0, 16256, 0, 16256, 0, 16256, 0, 16256, 0, 16256, 0, 16256, 0, 16256, 0, 16256, 0, 16256, 0, 16256, 0, 16256, 0, 16128, 0, 16256, 0, 16256, 0, 16256, 0, 16128, 0, 16256, 0, 16256, 0, 16256, 0, 16128, 0, 16256, 0, 16256, 0, 16256, 0, 16128, 0, 16256, 0, 16256, 0, 16256, 0, 16256, 0, 16256, 0, 16256, 0, 16256, 0, 16256, 0, 16256, 0, 16256, 0, 16256, 0, 16256, 0, 16256, 0, 16256, 0, 16256, 0, 16256, 0, 16256, 0, 16256, 0, 16256, 0, 16128, 0, 16256, 0, 16256, 0, 16256, 0, 16128, 0, 16256, 0, 16256, 0, 16256, 0, 16128, 0, 16256, 0, 16256, 0, 16256, 0, 16128, 0, 16256, 0, 16256, 0, 16256, 0, 16256, 0, 16256, 0, 16256, 0, 16256, 0, 16256, 0, 16256, 0, 16256, 0, 16256, 0, 16256, 0, 16256, 0, 16256, 0, 16256, 0, 16256, 0, 16256, 0, 16256, 0, 16256, 0, 16256, 0, 16256, 0, 16256, 0, 16256, 0, 16256, 0, 16256, 0, 16256, 0, 16256, 0, 16256, 0, 16256, 0, 16256, 0, 16256, 0, 16256, 0, 16256, 0, 16256, 0, 16256, 0, 0, 0, 0, 0, 0, 0, 0, 0, 0, 0, 0, 0, 0, 0, 0, 0, 0, 0, 0, 0, 0, 0, 0, 0, 0, 0, 0, 0, 0, 0, 0, 0, 0, 0, 0, 0, 0, 0, 0, 0, 0, 0, 0, 0, 0, 0, 0, 0, 0, 0, 0, 0, 0, 0, 0, 0, 0, 0, 0, 0, 0, 0, 0, 0, 0, 0, 0, 0, 0, 0, 0, 0, 0, 0, 0, 0, 0, 0, 0, 0, 0, 0, 0, 0, 0, 0, 0, 0, 0, 0, 0, 0, 0, 0, 0, 0, 0, 0, 0, 0, 0, 0, 0, 0, 0, 0, 0, 0, 0, 0, 0, 0, 0, 0, 0, 0, 0, 0, 0, 0, 0, 0, 0, 0, 0, 0, 0, 0, 16768, 0, 16768, 0, 16768, 0, 16768, 0, 16768, 0, 16768, 0, 16768, 0, 16768, 0, 16768, 0, 16768, 0, 16768, 0, 16768, 0, 16768, 0, 0, 0, 16768, 0, 0, 0, 16256, 0, 16256, 0, 16256, 0, 16256, 0, 16256, 0, 16256, 0, 16256, 0, 16256, 0, 16256, 0, 16256, 0, 16256, 0, 16256, 0, 16128, 0, 16256, 0, 16128, 0, 17280, 0, 16128, 0, 16256, 0, 16256, 0, 16256, 0, 16128, 0, 16256, 0, 16256, 0, 16256, 0, 16128, 0, 16256, 0, 16256, 0, 16256, 0, 16128, 0, 16256, 0, 16256, 0, 16256, 0, 16256, 0, 16256, 0, 16256, 0, 16256, 0, 16256, 0, 16256, 0, 16256, 0, 16256, 0, 16256, 0, 16256, 0, 16256, 0, 16256, 0, 16256, 0, 16256, 0, 16256, 0, 16256, 0, 16256, 0, 16256, 0, 16256, 0, 16256, 0, 16256, 0, 16256, 0, 16256, 0, 16256, 0, 16256, 0, 16256, 0, 16256, 0, 16256, 0, 16256, 0, 16256, 0, 16256, 0, 16256, 0, 16128, 0, 16256, 0, 16256, 0, 16256, 0, 16128, 0, 16256, 0, 16256, 0, 16256, 0, 16128, 0, 16256, 0, 16256, 0, 16256, 0, 16128, 0, 16256, 0, 16256, 0, 16256, 0, 16256, 0, 16256, 0, 16256, 0, 16256, 0, 16256, 0, 16256, 0, 16256, 0, 16256, 0, 16256, 0, 16256, 0, 16256, 0, 16256, 0, 16256, 0, 16256, 0, 16256, 0, 16256, 0, 16256, 0, 16256, 0, 16256, 0, 16256, 0, 16256, 0, 16256, 0, 16256, 0, 16256, 0, 16256, 0, 16256, 0, 16256, 0, 16256, 0, 16256, 0, 16256, 0, 16256, 0, 16256, 0, 16256, 0, 16256, 0, 16256, 0, 16256, 0, 16256, 0, 16256, 0, 16256, 0, 16256, 0, 16256, 0, 16256, 0, 16256, 0, 16256, 0, 16256, 0, 16256, 0, 16256, 0, 16256, 0, 16256, 0, 16256, 0, 16256, 0, 16256, 0, 16256, 0, 16256, 0, 16256, 0, 16256, 0, 16256, 0, 16256, 0, 16256, 0, 16256, 0, 16256, 0, 16256, 0, 16256, 0, 16256, 0, 16256, 0, 16256, 0, 16256, 0, 16256, 0, 16256, 0, 16256, 0, 16256, 0, 16256, 0, 16256, 0, 16256, 0, 16256, 0, 16256, 0, 16256, 0, 16256, 0, 16256, 0, 16256, 0, 16128, 0, 16256, 0, 16256, 0, 16256, 0, 16128, 0, 16256, 0, 16256, 0, 16256, 0, 16128, 0, 16256, 0, 16256, 0, 16256, 0, 16128, 0, 16256, 0, 16256, 0, 16256, 0, 0, 0, 0, 0, 0, 0, 0, 0, 0, 0, 0, 0, 0, 0, 0, 0, 0, 0, 0, 0, 0, 0, 0, 0, 0, 0, 0, 0, 0, 0, 0, 0, 0, 0, 0, 0, 0, 0, 0, 0, 0, 0, 0, 0, 0, 0, 0, 0, 0, 0, 0, 0, 0, 0, 0, 0, 0, 0, 0, 0, 0, 0, 0, 0, 0, 0, 0, 0, 0, 0, 0, 0, 0, 0, 0, 0, 0, 0, 0, 0, 0, 0, 0, 0, 0, 0, 0, 0, 0, 0, 0, 0, 0, 0, 0, 0, 0, 0, 0, 0, 0, 0, 0, 0, 0, 0, 0, 0, 0, 0, 0, 0, 0, 0, 0, 0, 0, 0, 0, 0, 0, 0, 0, 0, 0, 0, 0, 0, 16768, 0, 16768, 0, 16768, 0, 16768, 0, 16768, 0, 16768, 0, 16768, 0, 16768, 0, 16768, 0, 16768, 0, 16768, 0, 16768, 0, 0, 0, 0, 0, 0, 0, 0, 0, 16128, 0, 16256, 0, 16256, 0, 16256, 0, 16128, 0, 16256, 0, 16256, 0, 16256, 0, 16128, 0, 16256, 0, 16256, 0, 16256, 0, 16128, 0, 16256, 0, 16256, 0, 16256, 0, 16256, 0, 16256, 0, 16256, 0, 16256, 0, 16256, 0, 16256, 0, 16256, 0, 16256, 0, 16256, 0, 16256, 0, 16256, 0, 16256, 0, 16256, 0, 16256, 0, 16256, 0, 16256, 0, 16256, 0, 16256, 0, 16256, 0, 16256, 0, 16256, 0, 16256, 0, 16256, 0, 16256, 0, 16256, 0, 16256, 0, 16256, 0, 16256, 0, 16256, 0, 16256, 0, 16256, 0, 16256, 0, 16256, 0, 16256, 0, 16256, 0, 16256, 0, 16256, 0, 16256, 0, 16256, 0, 16256, 0, 16256, 0, 16256, 0, 16256, 0, 16256, 0, 16256, 0, 16256, 0, 16256, 0, 16256, 0, 16256, 0, 16256, 0, 16256, 0, 16256, 0, 16256, 0, 16256, 0, 16256, 0, 16256, 0, 16256, 0, 16256, 0, 16256, 0, 16256, 0, 16256, 0, 16256, 0, 16256, 0, 16256, 0, 16256, 0, 16256, 0, 16256, 0, 16256, 0, 16256, 0, 16256, 0, 16256, 0, 16256, 0, 16256, 0, 16256, 0, 16256, 0, 16256, 0, 16256, 0, 16256, 0, 16256, 0, 16256, 0, 16128, 0, 16256, 0, 16256, 0, 16256, 0, 16128, 0, 16256, 0, 16256, 0, 16256, 0, 16128, 0, 16256, 0, 16256, 0, 16256, 0, 16128, 0, 16256, 0, 16256, 0, 16256, 0, 16256, 0, 16256, 0, 16256, 0, 16256, 0, 16256, 0, 16256, 0, 16256, 0, 16256, 0, 16256, 0, 16256, 0, 16256, 0, 16256, 0, 16256, 0, 16256, 0, 16256, 0, 16256, 0, 16128, 0, 16256, 0, 16256, 0, 16256, 0, 16128, 0, 16256, 0, 16256, 0, 16256, 0, 16128, 0, 16256, 0, 16256, 0, 16256, 0, 16128, 0, 16256, 0, 16256, 0, 16256, 0, 16256, 0, 16256, 0, 16256, 0, 16256, 0, 16256, 0, 16256, 0, 16256, 0, 16256, 0, 16256, 0, 16256, 0, 16256, 0, 16256, 0, 16256, 0, 16256, 0, 16256, 0, 16256, 0, 16256, 0, 16256, 0, 16256, 0, 16256, 0, 16256, 0, 16256, 0, 16256, 0, 16256, 0, 16256, 0, 16256, 0, 16256, 0, 16256, 0, 16256, 0, 16256, 0, 16256, 0, 16256, 0, 0, 0, 0, 0, 0, 0, 0, 0, 0, 0, 0, 0, 0, 0, 0, 0, 0, 0, 0, 0, 0, 0, 0, 0, 0, 0, 0, 0, 0, 0, 0, 0, 0, 0, 0, 0, 0, 0, 0, 0, 0, 0, 0, 0, 0, 0, 0, 0, 0, 0, 0, 0, 0, 0, 0, 0, 0, 0, 0, 0, 0, 0, 0, 0, 0, 0, 0, 0, 0, 0, 0, 0, 0, 0, 0, 0, 0, 0, 0, 0, 0, 0, 0, 0, 0, 0, 0, 0, 0, 0, 0, 0, 0, 0, 0, 0, 0, 0, 0, 0, 0, 0, 0, 0, 0, 0, 0, 0, 0, 0, 0, 0, 0, 0, 0, 0, 0, 0, 0, 0, 0, 0, 0, 0, 0, 0, 0, 0, 0, 0, 0, 0, 0, 0, 0, 0, 0, 0, 0, 0, 0, 0, 0, 0, 16256, 0, 16256, 0, 16128, 0, 16256, 0, 16256, 0, 16256, 0, 16128, 0, 16256, 0, 0, 0, 0, 0, 0, 0, 0, 0, 0, 0, 0, 0, 0, 0, 0, 0, 16256, 0, 16256, 0, 16256, 0, 16256, 0, 16256, 0, 16256, 0, 16256, 0, 16256, 0, 16128, 0, 16256, 0, 16256, 0, 16256, 0, 16128, 0, 16256, 0, 16256, 0, 16256, 0, 16256, 0, 16256, 0, 16256, 0, 16256, 0, 16256, 0, 16256, 0, 16256, 0, 16256, 0, 16256, 0, 16256, 0, 16256, 0, 16256, 0, 16256, 0, 16256, 0, 16256, 0, 16256, 0, 16256, 0, 16256, 0, 16256, 0, 16256, 0, 16256, 0, 16256, 0, 16256, 0, 16256, 0, 16256, 0, 16256, 0, 16256, 0, 16256, 0, 16256, 0, 16256, 0, 16256, 0, 16256, 0, 16128, 0, 16256, 0, 16256, 0, 16256, 0, 16128, 0, 16256, 0, 16256, 0, 16256, 0, 16256, 0, 16256, 0, 16256, 0, 16256, 0, 16256, 0, 16256, 0, 16256, 0, 16256, 0, 16256, 0, 16256, 0, 16256, 0, 16256, 0, 16256, 0, 16256, 0, 16256, 0, 16256, 0, 16256, 0, 16256, 0, 16256, 0, 16256, 0, 16256, 0, 16256, 0, 16256, 0, 16256, 0, 16256, 0, 16256, 0, 16128, 0, 16256, 0, 16256, 0, 16256, 0, 16128, 0, 16256, 0, 16256, 0, 16256, 0, 16256, 0, 16256, 0, 16256, 0, 16256, 0, 16256, 0, 16256, 0, 16256, 0, 16256, 0, 16256, 0, 16256, 0, 16256, 0, 16256, 0, 16256, 0, 16256, 0, 16256, 0, 16256, 0, 16256, 0, 16256, 0, 16256, 0, 16256, 0, 16256, 0, 16256, 0, 16256, 0, 16256, 0, 16128, 0, 16256, 0, 16256, 0, 16256, 0, 16128, 0, 16256, 0, 16256, 0, 16256, 0, 16256, 0, 16256, 0, 16256, 0, 16256, 0, 16256, 0, 16256, 0, 16256, 0, 16256, 0, 16256, 0, 16256, 0, 16256, 0, 16256, 0, 16256, 0, 16256, 0, 16256, 0, 16256, 0, 16256, 0, 16256, 0, 16256, 0, 16256, 0, 16256, 0, 16256, 0, 16128, 0, 16256, 0, 16256, 0, 16256, 0, 16128, 0, 16256, 0, 16256, 0, 16256, 0, 16256, 0, 16256, 0, 16256, 0, 16256, 0, 16256, 0, 16256, 0, 16256, 0, 16256, 0, 16256, 0, 16256, 0, 16256, 0, 16256, 0, 16256, 0, 16256, 0, 16256, 0, 16256, 0, 0, 0, 0, 0, 0, 0, 0, 0, 0, 0, 0, 0, 0, 0, 0, 0, 0, 0, 0, 0, 0, 0, 0, 0, 0, 0, 0, 0, 0, 0, 0, 0, 0, 0, 0, 0, 0, 0, 0, 0, 0, 0, 0, 0, 0, 0, 0, 0, 0, 0, 0, 0, 0, 0, 0, 0, 0, 0, 0, 0, 0, 0, 0, 0, 0, 0, 0, 0, 0, 0, 0, 0, 0, 0, 0, 0, 0, 0, 0, 0, 0, 0, 0, 0, 0, 0, 0, 0, 0, 0, 0, 0, 0, 0, 0, 0, 0, 0, 0, 0, 0, 0, 0, 0, 0, 0, 0, 0, 0, 0, 0, 0, 0, 0, 0, 0, 0, 0, 0, 0, 0, 0, 0, 0, 0, 0, 0, 0, 0, 0, 0, 0, 0, 0, 0, 0, 0, 0, 0, 0, 0, 0, 0, 0, 16256, 0, 16256, 0, 16128, 0, 16256, 0, 16128, 0, 16256, 0, 16128, 0, 16256, 0, 0, 0, 0, 0, 0, 0, 0, 0, 0, 0, 0, 0, 0, 0, 0, 0, 16256, 0, 16256, 0, 16256, 0, 16256, 0, 16128, 0, 0, 0, 16256, 0, 16256, 0, 16128, 0, 16256, 0, 16256, 0, 16256, 0, 16128, 0, 16256, 0, 16256, 0, 16256, 0, 16256, 0, 16256, 0, 16256, 0, 16256, 0, 16256, 0, 16256, 0, 16256, 0, 16256, 0, 16256, 0, 16256, 0, 16256, 0, 16256, 0, 16256, 0, 16256, 0, 16256, 0, 16256, 0, 16256, 0, 16256, 0, 16256, 0, 16256, 0, 16256, 0, 16256, 0, 16256, 0, 16256, 0, 16256, 0, 16256, 0, 16256, 0, 16256, 0, 16256, 0, 16256, 0, 16256, 0, 16256, 0, 16128, 0, 16256, 0, 16256, 0, 16256, 0, 16128, 0, 16256, 0, 16256, 0, 16256, 0, 16256, 0, 16256, 0, 16256, 0, 16256, 0, 16256, 0, 16256, 0, 16256, 0, 16256, 0, 16256, 0, 16256, 0, 16256, 0, 16256, 0, 16256, 0, 16256, 0, 16256, 0, 16256, 0, 16256, 0, 16256, 0, 16256, 0, 16256, 0, 16256, 0, 16256, 0, 16256, 0, 16256, 0, 16256, 0, 16256, 0, 16128, 0, 16256, 0, 16256, 0, 16256, 0, 16128, 0, 16256, 0, 16256, 0, 16256, 0, 16256, 0, 16256, 0, 16256, 0, 16256, 0, 16256, 0, 16256, 0, 16256, 0, 16256, 0, 16256, 0, 16256, 0, 16256, 0, 16256, 0, 16256, 0, 16256, 0, 16256, 0, 16256, 0, 16256, 0, 16256, 0, 16256, 0, 16256, 0, 16256, 0, 16256, 0, 16256, 0, 16256, 0, 16128, 0, 16256, 0, 16256, 0, 16256, 0, 16128, 0, 16256, 0, 16256, 0, 16256, 0, 16256, 0, 16256, 0, 16256, 0, 16256, 0, 16256, 0, 16256, 0, 16256, 0, 16256, 0, 16256, 0, 16256, 0, 16256, 0, 16256, 0, 16256, 0, 16256, 0, 16256, 0, 16256, 0, 16256, 0, 16256, 0, 16256, 0, 16256, 0, 16256, 0, 16256, 0, 16128, 0, 16256, 0, 16256, 0, 16256, 0, 16128, 0, 16256, 0, 16256, 0, 16256, 0, 16256, 0, 16256, 0, 16256, 0, 16256, 0, 16256, 0, 16256, 0, 16256, 0, 16256, 0, 16256, 0, 16256, 0, 16256, 0, 16256, 0, 16256, 0, 16256, 0, 16256, 0, 16256, 0, 0, 0, 0, 0, 0, 0, 0, 0, 0, 0, 0, 0, 0, 0, 0, 0, 0, 0, 0, 0, 0, 0, 0, 0, 0, 0, 0, 0, 0, 0, 0, 0, 0, 0, 0, 0, 0, 0, 0, 0, 0, 0, 0, 0, 0, 0, 0, 0, 0, 0, 0, 0, 0, 0, 0, 0, 0, 0, 0, 0, 0, 0, 0, 0, 0, 0, 0, 0, 0, 0, 0, 0, 0, 0, 0, 0, 0, 0, 0, 0, 0, 0, 0, 0, 0, 0, 0, 0, 0, 0, 0, 0, 0, 0, 0, 0, 0, 0, 0, 0, 0, 0, 0, 0, 0, 0, 0, 0, 0, 0, 0, 0, 0, 0, 0, 0, 0, 0, 0, 0, 0, 0, 0, 0, 0, 0, 0, 0, 16768, 0, 16768, 0, 16768, 0, 16768, 0, 16768, 0, 16768, 0, 16768, 0, 16768, 0, 16256, 0, 16256, 0, 16128, 0, 16256, 0, 16256, 0, 16256, 0, 16128, 0, 16256, 0, 16768, 0, 16768, 0, 16768, 0, 16768, 0, 16768, 0, 16768, 0, 16768, 0, 16768, 0, 16256, 0, 16256, 0, 16256, 0, 16256, 0, 16256, 0, 16256, 0, 16256, 0, 16256, 0, 16128, 0, 16256, 0, 16256, 0, 16256, 0, 16128, 0, 16256, 0, 16256, 0, 16256, 0, 16256, 0, 16256, 0, 16256, 0, 16256, 0, 16256, 0, 16256, 0, 16256, 0, 16256, 0, 16256, 0, 16256, 0, 16256, 0, 16256, 0, 16256, 0, 16256, 0, 16256, 0, 16256, 0, 16256, 0, 16256, 0, 16256, 0, 16256, 0, 16256, 0, 16256, 0, 16256, 0, 16256, 0, 16256, 0, 16256, 0, 16256, 0, 16256, 0, 16256, 0, 16256, 0, 16256, 0, 16256, 0, 16128, 0, 16256, 0, 16256, 0, 16256, 0, 16128, 0, 16256, 0, 16256, 0, 16256, 0, 16256, 0, 16256, 0, 16256, 0, 16256, 0, 16256, 0, 16256, 0, 16256, 0, 16256, 0, 16256, 0, 16256, 0, 16256, 0, 16256, 0, 16256, 0, 16256, 0, 16256, 0, 16256, 0, 16256, 0, 16256, 0, 16256, 0, 16256, 0, 16256, 0, 16256, 0, 16256, 0, 16256, 0, 16256, 0, 16256, 0, 16128, 0, 16256, 0, 16256, 0, 16256, 0, 16128, 0, 16256, 0, 16256, 0, 16256, 0, 16256, 0, 16256, 0, 16256, 0, 16256, 0, 16256, 0, 16256, 0, 16256, 0, 16256, 0, 16256, 0, 16256, 0, 16256, 0, 16256, 0, 16256, 0, 16256, 0, 16256, 0, 16256, 0, 16256, 0, 16256, 0, 16256, 0, 16256, 0, 16256, 0, 16256, 0, 16256, 0, 16256, 0, 16128, 0, 16256, 0, 16256, 0, 16256, 0, 16128, 0, 16256, 0, 16256, 0, 16256, 0, 16256, 0, 16256, 0, 16256, 0, 16256, 0, 16256, 0, 16256, 0, 16256, 0, 16256, 0, 16256, 0, 16256, 0, 16256, 0, 16256, 0, 16256, 0, 16256, 0, 16256, 0, 16256, 0, 16256, 0, 16256, 0, 16256, 0, 16256, 0, 16256, 0, 16256, 0, 16128, 0, 16256, 0, 16256, 0, 16256, 0, 16128, 0, 16256, 0, 16256, 0, 16256, 0, 16256, 0, 16256, 0, 16256, 0, 16256, 0, 16256, 0, 16256, 0, 16256, 0, 16256, 0, 16256, 0, 16256, 0, 16256, 0, 16256, 0, 16256, 0, 16256, 0, 16256, 0, 16256, 0, 0, 0, 0, 0, 0, 0, 0, 0, 0, 0, 0, 0, 0, 0, 0, 0, 0, 0, 0, 0, 0, 0, 0, 0, 0, 0, 0, 0, 0, 0, 0, 0, 0, 0, 0, 0, 0, 0, 0, 0, 0, 0, 0, 0, 0, 0, 0, 0, 0, 0, 0, 0, 0, 0, 0, 0, 0, 0, 0, 0, 0, 0, 0, 0, 0, 0, 0, 0, 0, 0, 0, 0, 0, 0, 0, 0, 0, 0, 0, 0, 0, 0, 0, 0, 0, 0, 0, 0, 0, 0, 0, 0, 0, 0, 0, 0, 0, 0, 0, 0, 0, 0, 0, 0, 0, 0, 0, 0, 0, 0, 0, 0, 0, 0, 0, 0, 0, 0, 0, 0, 0, 0, 0, 0, 0, 0, 0, 0, 16768, 0, 16768, 0, 16768, 0, 16768, 0, 16768, 0, 0, 0, 0, 0, 0, 0, 16256, 0, 16256, 0, 16128, 0, 16256, 0, 16128, 0, 16256, 0, 16128, 0, 16256, 0, 16768, 0, 16768, 0, 16768, 0, 16768, 0, 16768, 0, 0, 0, 0, 0, 0, 0, 16256, 0, 16256, 0, 16256, 0, 16256, 0, 16128, 0, 17280, 0, 16256, 0, 16256, 0, 16128, 0, 16256, 0, 16256, 0, 16256, 0, 16128, 0, 16256, 0, 16256, 0, 16256, 0, 16256, 0, 16256, 0, 16256, 0, 16256, 0, 16256, 0, 16256, 0, 16256, 0, 16256, 0, 16256, 0, 16256, 0, 16256, 0, 16256, 0, 16256, 0, 16256, 0, 16256, 0, 16256, 0, 16256, 0, 16256, 0, 16256, 0, 16256, 0, 16256, 0, 16256, 0, 16256, 0, 16256, 0, 16256, 0, 16256, 0, 16256, 0, 16256, 0, 16256, 0, 16256, 0, 16256, 0, 16256, 0, 16128, 0, 16256, 0, 16256, 0, 16256, 0, 16128, 0, 16256, 0, 16256, 0, 16256, 0, 16256, 0, 16256, 0, 16256, 0, 16256, 0, 16256, 0, 16256, 0, 16256, 0, 16256, 0, 16256, 0, 16256, 0, 16256, 0, 16256, 0, 16256, 0, 16256, 0, 16256, 0, 16256, 0, 16256, 0, 16256, 0, 16256, 0, 16256, 0, 16256, 0, 16256, 0, 16256, 0, 16256, 0, 16256, 0, 16256, 0, 16128, 0, 16256, 0, 16256, 0, 16256, 0, 16128, 0, 16256, 0, 16256, 0, 16256, 0, 16256, 0, 16256, 0, 16256, 0, 16256, 0, 16256, 0, 16256, 0, 16256, 0, 16256, 0, 16256, 0, 16256, 0, 16256, 0, 16256, 0, 16256, 0, 16256, 0, 16256, 0, 16256, 0, 16256, 0, 16256, 0, 16256, 0, 16256, 0, 16256, 0, 16256, 0, 16256, 0, 16256, 0, 16128, 0, 16256, 0, 16256, 0, 16256, 0, 16128, 0, 16256, 0, 16256, 0, 16256, 0, 16256, 0, 16256, 0, 16256, 0, 16256, 0, 16256, 0, 16256, 0, 16256, 0, 16256, 0, 16256, 0, 16256, 0, 16256, 0, 16256, 0, 16256, 0, 16256, 0, 16256, 0, 16256, 0, 16256, 0, 16256, 0, 16256, 0, 16256, 0, 16256, 0, 16256, 0, 16128, 0, 16256, 0, 16256, 0, 16256, 0, 16128, 0, 16256, 0, 16256, 0, 16256, 0, 16256, 0, 16256, 0, 16256, 0, 16256, 0, 16256, 0, 16256, 0, 16256, 0, 16256, 0, 16256, 0, 16256, 0, 16256, 0, 16256, 0, 16256, 0, 16256, 0, 16256, 0, 16256, 0, 0, 0, 0, 0, 0, 0, 0, 0, 0, 0, 0, 0, 0, 0, 0, 0, 0, 0, 0, 0, 0, 0, 0, 0, 0, 0, 0, 0, 0, 0, 0, 0, 0, 0, 0, 0, 0, 0, 0, 0, 0, 0, 0, 0, 0, 0, 0, 0, 0, 0, 0, 0, 0, 0, 0, 0, 0, 0, 0, 0, 0, 0, 0, 0, 0, 0, 0, 0, 0, 0, 0, 0, 0, 0, 0, 0, 0, 0, 0, 0, 0, 0, 0, 0, 0, 0, 0, 0, 0, 0, 0, 0, 0, 0, 0, 0, 0, 0, 0, 0, 0, 0, 0, 0, 0, 0, 0, 0, 0, 0, 0, 0, 0, 0, 0, 0, 0, 0, 0, 0, 0, 0, 0, 0, 0, 0, 0};

__global__ __launch_bounds__(512) void xd_kernel(const int* idx, const u16* w1, const u16* table, const u16* b1, const u16* ws_cin, const u16* wout, char* ws, const u16* w2, const u16* b_out, u16* out) {
  __shared__ __attribute__((aligned(1024))) char shm[163840];
  unsigned tid = threadIdx.x;
  asm volatile(
    "s_load_dwordx16 s[4:19], s[0:1], 0\n"
    ".Lxd_top:\n"
    "v_readfirstlane_b32 s43, v0\n"
    "s_setprio 3\n"
    "s_cmp_lt_u32 s2, 48\n"
    "s_cbranch_scc1 .Lxd_cin\n"
    "s_and_b32 s24, s2, 15\n"
    "s_mul_i32 s40, s43, 36\n"
    "s_mul_i32 s41, s24, 26624\n"
    "s_add_u32 s38, s40, s41\n"
    "v_lshlrev_b32 v86, 4, v0\n"
    "s_lshl_b32 s34, s43, 6\n"
    "s_add_u32 m0, s34, 98304\n"
    "s_waitcnt lgkmcnt(0)\n"
    "s_add_u32 s38, s4, s38\n"
    "s_addc_u32 s39, s5, 0\n"
    "global_load_lds_dwordx4 v86, s[38:39] offset:0\n"
    "global_load_lds_dwordx4 v86, s[38:39] offset:1024\n"
    "global_load_lds_dwordx4 v86, s[38:39] offset:2048\n"
    "global_load_lds_dwordx4 v86, s[38:39] offset:2304\n"
    "s_sub_u32 s3, s2, 48\n"
    "s_lshl_b32 s40, s2, 2\n"
    "s_lshr_b32 s23, s3, 4\n"
    "s_lshr_b32 s40, 0xa8864220, s40\n"
    "s_and_b32 s42, s40, 14\n"
    "v_bfe_u32 v85, v0, 4, 2\n"
    "v_lshlrev_b32 v87, 1, v0\n"
    "v_and_b32 v87, 0xe0, v87\n"
    "v_and_b32 v88, 0xf0, v86\n"
    "v_xor_b32 v87, v87, v88\n"
    "s_movk_i32 s41, 3328\n"
    "v_mad_u32_u24 v46, v85, s41, v87\n"
    "v_add_u32 v47, 12288, v46\n"
    "s_mul_i32 s41, s43, 416\n"
    "s_lshl_b32 s40, s23, 8\n"
    "s_add_u32 s41, s41, s40\n"
    "s_mul_i32 s30, s42, 425984\n"
    "s_lshl_b32 s35, s43, 5\n"
    "s_bitset1_b32 s35, 15\n"
    "s_add_u32 s6, s6, s41\n"
    "s_addc_u32 s7, s7, 0\n"
    "s_add_u32 s30, s6, s30\n"
    "s_addc_u32 s31, s7, 0\n"
    "s_add_u32 m0, s35, 0\n"
    "s_nop 0\n"
    "global_load_lds_dwordx4 v46, s[30:31] offset:0\n"
    "global_load_lds_dwordx4 v47, s[30:31] offset:1024\n"
    "s_add_u32 s30, s30, 212992\n"
    "s_addc_u32 s31, s31, 0\n"
    "s_setprio 2\n"
    "v_and_b32 v84, 3, v0\n"
    "v_bfe_u32 v44, v0, 3, 3\n"
    "v_lshl_add_u32 v44, v84, 3, v44\n"
    "s_add_u32 s40, s34, 98304\n"
    "v_mul_u32_u24 v44, 104, v44\n"
    "v_add_u32 v44, s40, v44\n"
    "v_lshl_add_u32 v45, s42, 3, v44\n"
    "v_xor_b32 v51, v0, v86\n"
    "v_and_b32 v86, 0x70, v86\n"
    "v_bfi_b32 v86, 48, v51, v86\n"
    "v_xor_b32 v51, 64, v86\n"
    "v_add_u32 v48, 3072, v86\n"
    "v_add_u32 v49, 2048, v51\n"
    "v_add_u32 v50, 1024, v86\n"
    "s_sub_u32 s36, s8, 3072\n"
    "s_subb_u32 s37, s9, 0\n"
    "s_add_u32 m0, s35, 49152\n"
    "s_nop 0\n"
    "global_load_lds_dwordx4 v46, s[30:31] offset:0\n"
    "global_load_lds_dwordx4 v47, s[30:31] offset:1024\n"
    "s_add_u32 s30, s30, 212992\n"
    "s_addc_u32 s31, s31, 0\n"
    "s_add_u32 m0, s35, 98304\n"
    "s_nop 0\n"
    "global_load_lds_dwordx4 v46, s[30:31] offset:0\n"
    "global_load_lds_dwordx4 v47, s[30:31] offset:1024\n"
    "s_add_u32 s30, s30, 212992\n"
    "s_addc_u32 s31, s31, 0\n"
    "s_waitcnt vmcnt(6)\n"
    "ds_read2_b64 v[96:99], v45 offset1:1\n"
    "s_lshr_b32 s20, s43, 6\n"
    "s_lshr_b32 s21, s43, 8\n"
    "v_and_b32 v1, 63, v0\n"
    "s_waitcnt lgkmcnt(0)\n"
    "v_lshlrev_b32 v96, 7, v96\n"
    "v_lshlrev_b32 v97, 7, v97\n"
    "v_lshlrev_b32 v98, 7, v98\n"
    "v_add_u32_dpp v52, v96, v48 quad_perm:[0,0,0,0] row_mask:0xf bank_mask:0xf\n"
    "v_add_u32_dpp v53, v96, v49 quad_perm:[1,1,1,1] row_mask:0xf bank_mask:0xf\n"
    "v_add_u32_dpp v54, v96, v50 quad_perm:[2,2,2,2] row_mask:0xf bank_mask:0xf\n"
    "v_add_u32_dpp v55, v96, v51 quad_perm:[3,3,3,3] row_mask:0xf bank_mask:0xf\n"
    "s_add_u32 m0, s34, 0\n"
    "s_nop 0\n"
    "global_load_lds_dwordx4 v52, s[36:37] offset:0\n"
    "global_load_lds_dwordx4 v53, s[36:37] offset:1024\n"
    "global_load_lds_dwordx4 v54, s[36:37] offset:2048\n"
    "global_load_lds_dwordx4 v55, s[36:37] offset:3072\n"
    "s_setprio 1\n"
    "v_add_u32_dpp v52, v97, v48 quad_perm:[0,0,0,0] row_mask:0xf bank_mask:0xf\n"
    "v_add_u32_dpp v53, v97, v49 quad_perm:[1,1,1,1] row_mask:0xf bank_mask:0xf\n"
    "v_add_u32_dpp v54, v97, v50 quad_perm:[2,2,2,2] row_mask:0xf bank_mask:0xf\n"
    "v_add_u32_dpp v55, v97, v51 quad_perm:[3,3,3,3] row_mask:0xf bank_mask:0xf\n"
    "s_load_dwordx4 s[68:71], s[0:1], 64\n"
    "s_cmp_eq_u32 s21, 0\n"
    "s_cbranch_scc1 .Lxd_g0\n"
    "s_waitcnt vmcnt(8)\n"
    "s_barrier\n"
    ".Lxd_g0:\n"
    "s_add_u32 m0, s34, 49152\n"
    "s_nop 0\n"
    "global_load_lds_dwordx4 v52, s[36:37] offset:0\n"
    "s_lshl2_add_u32 s41, s42, 8\n"
    "s_mov_b32 s28, 0x30201000\n"
    "s_movk_i32 s29, 0x5040\n"
    "s_lshr_b64 s[42:43], s[28:29], s41\n"
    "s_sub_u32 s41, 48, s41\n"
    "s_lshl_b64 s[28:29], s[28:29], s41\n"
    "s_or_b64 s[28:29], s[28:29], s[42:43]\n"
    "s_and_b32 s29, s29, 0xff\n"
    "s_or_b32 s29, s29, 0x6000\n"
    "global_load_lds_dwordx4 v53, s[36:37] offset:1024\n"
    "s_bfe_u32 s40, s28, 0x80000\n"
    "v_add_u32 v45, s40, v44\n"
    "ds_read2_b64 v[100:103], v45 offset1:1\n"
    "s_bfe_u32 s40, s28, 0x80008\n"
    "v_add_u32 v45, s40, v44\n"
    "ds_read2_b64 v[104:107], v45 offset1:1\n"
    "s_bfe_u32 s40, s28, 0x80010\n"
    "v_add_u32 v45, s40, v44\n"
    "ds_read2_b64 v[108:111], v45 offset1:1\n"
    "global_load_lds_dwordx4 v54, s[36:37] offset:2048\n"
    "s_bfe_u32 s40, s28, 0x80018\n"
    "v_add_u32 v45, s40, v44\n"
    "ds_read2_b64 v[112:115], v45 offset1:1\n"
    "s_bfe_u32 s40, s29, 0x80000\n"
    "v_add_u32 v45, s40, v44\n"
    "ds_read2_b64 v[116:119], v45 offset1:1\n"
    "s_bfe_u32 s40, s29, 0x80008\n"
    "v_add_u32 v45, s40, v44\n"
    "ds_read_b64 v[120:121], v45\n"
    "global_load_lds_dwordx4 v55, s[36:37] offset:3072\n"
    "v_lshrrev_b32 v86, 5, v88\n"
    "v_xor_b32 v86, v86, v85\n"
    "v_lshlrev_b32 v86, 4, v86\n"
    "v_lshl_add_u32 v86, v88, 3, v86\n"
    "s_and_b32 s41, s20, 1\n"
    "s_lshl1_add_u32 s40, s21, s41\n"
    "v_lshl_add_u32 v56, s40, 13, v86\n"
    "v_xor_b32 v57, 64, v56\n"
    "v_lshrrev_b32 v86, 6, v88\n"
    "v_lshl_add_u32 v88, v85, 3, v86\n"
    "v_bfe_u32 v86, v0, 2, 3\n"
    "s_and_b32 s40, s20, 2\n"
    "s_lshl1_add_u32 s40, s40, 1024\n"
    "v_xor_b32 v86, s40, v86\n"
    "v_lshlrev_b32 v86, 5, v86\n"
    "v_lshl_add_u32 v86, v88, 8, v86\n"
    "v_lshl_add_u32 v76, v84, 3, v86\n"
    "v_xor_b32 v77, 32, v76\n"
    "v_xor_b32 v78, 64, v76\n"
    "v_xor_b32 v79, 96, v76\n"
    "s_setprio 0\n"
    "v_add_u32_dpp v52, v98, v48 quad_perm:[0,0,0,0] row_mask:0xf bank_mask:0xf\n"
    "v_add_u32_dpp v53, v98, v49 quad_perm:[1,1,1,1] row_mask:0xf bank_mask:0xf\n"
    "v_add_u32_dpp v54, v98, v50 quad_perm:[2,2,2,2] row_mask:0xf bank_mask:0xf\n"
    "v_add_u32_dpp v55, v98, v51 quad_perm:[3,3,3,3] row_mask:0xf bank_mask:0xf\n"
    "v_lshlrev_b32 v99, 7, v99\n"
    "s_lshr_b32 s41, s3, 3\n"
    "s_waitcnt vmcnt(4) lgkmcnt(0)\n"
    "s_barrier\n"
    "s_add_u32 m0, s34, 98304\n"
    "s_nop 0\n"
    "global_load_lds_dwordx4 v52, s[36:37] offset:0\n"
    "ds_read_b128 v[128:131], v56 offset:0\n"
    "ds_read_b128 v[132:135], v56 offset:2048\n"
    "ds_read_b128 v[136:139], v56 offset:4096\n"
    "ds_read_b128 v[140:143], v56 offset:6144\n"
    "global_load_lds_dwordx4 v53, s[36:37] offset:1024\n"
    "ds_read_b64_tr_b16 v[144:145], v76 offset:0\n"
    "ds_read_b64_tr_b16 v[146:147], v76 offset:1024\n"
    "ds_read_b64_tr_b16 v[148:149], v77 offset:0\n"
    "ds_read_b64_tr_b16 v[150:151], v77 offset:1024\n"
    "global_load_lds_dwordx4 v54, s[36:37] offset:2048\n"
    "ds_read_b64_tr_b16 v[152:153], v78 offset:0\n"
    "ds_read_b64_tr_b16 v[154:155], v78 offset:1024\n"
    "ds_read_b64_tr_b16 v[156:157], v79 offset:0\n"
    "ds_read_b64_tr_b16 v[158:159], v79 offset:1024\n"
    "global_load_lds_dwordx4 v55, s[36:37] offset:3072\n"
    "ds_read_b128 v[160:163], v57 offset:0\n"
    "ds_read_b128 v[164:167], v57 offset:2048\n"
    "ds_read_b128 v[168:171], v57 offset:4096\n"
    "ds_read_b128 v[172:175], v57 offset:6144\n"
    "ds_read_b64_tr_b16 v[176:177], v76 offset:8192\n"
    "ds_read_b64_tr_b16 v[178:179], v76 offset:9216\n"
    "ds_read_b64_tr_b16 v[180:181], v77 offset:8192\n"
    "ds_read_b64_tr_b16 v[182:183], v77 offset:9216\n"
    "ds_read_b64_tr_b16 v[184:185], v78 offset:8192\n"
    "ds_read_b64_tr_b16 v[186:187], v78 offset:9216\n"
    "ds_read_b64_tr_b16 v[188:189], v79 offset:8192\n"
    "ds_read_b64_tr_b16 v[190:191], v79 offset:9216\n"
    "s_waitcnt lgkmcnt(0)\n"
    "s_barrier\n"
    "v_mfma_f32_16x16x32_bf16 v[192:195], v[144:147], v[128:131], 0\n"
    "v_lshlrev_b32 v100, 7, v100\n"
    "v_mfma_f32_16x16x32_bf16 v[192:195], v[176:179], v[160:163], v[192:195]\n"
    "v_add_u32_dpp v52, v99, v48 quad_perm:[0,0,0,0] row_mask:0xf bank_mask:0xf\n"
    "v_mfma_f32_16x16x32_bf16 v[196:199], v[176:179], v[164:167], 0\n"
    "v_mfma_f32_16x16x32_bf16 v[196:199], v[144:147], v[132:135], v[196:199]\n"
    "v_add_u32_dpp v53, v99, v49 quad_perm:[1,1,1,1] row_mask:0xf bank_mask:0xf\n"
    "v_mfma_f32_16x16x32_bf16 v[200:203], v[144:147], v[136:139], 0\n"
    "v_add_u32_dpp v54, v99, v50 quad_perm:[2,2,2,2] row_mask:0xf bank_mask:0xf\n"
    "v_mfma_f32_16x16x32_bf16 v[200:203], v[176:179], v[168:171], v[200:203]\n"
    "v_mfma_f32_16x16x32_bf16 v[204:207], v[176:179], v[172:175], 0\n"
    "v_add_u32_dpp v55, v99, v51 quad_perm:[3,3,3,3] row_mask:0xf bank_mask:0xf\n"
    "v_mfma_f32_16x16x32_bf16 v[204:207], v[144:147], v[140:143], v[204:207]\n"
    "s_and_b32 s40, s20, 2\n"
    "v_mfma_f32_16x16x32_bf16 v[220:223], v[148:151], v[140:143], 0\n"
    "v_mfma_f32_16x16x32_bf16 v[220:223], v[180:183], v[172:175], v[220:223]\n"
    "s_lshl2_add_u32 s40, s23, s40\n"
    "v_mfma_f32_16x16x32_bf16 v[216:219], v[180:183], v[168:171], 0\n"
    "s_lshl_b32 s40, s40, 6\n"
    "v_mfma_f32_16x16x32_bf16 v[216:219], v[148:151], v[136:139], v[216:219]\n"
    "v_mfma_f32_16x16x32_bf16 v[212:215], v[148:151], v[132:135], 0\n"
    "v_and_b32 v43, 31, v0\n"
    "v_mfma_f32_16x16x32_bf16 v[212:215], v[180:183], v[164:167], v[212:215]\n"
    "s_lshl_b32 s64, s20, 8\n"
    "v_mfma_f32_16x16x32_bf16 v[208:211], v[180:183], v[160:163], 0\n"
    "v_mfma_f32_16x16x32_bf16 v[208:211], v[148:151], v[128:131], v[208:211]\n"
    "s_add_u32 s64, s64, 147456\n"
    "v_mfma_f32_16x16x32_bf16 v[224:227], v[152:155], v[128:131], 0\n"
    "v_mfma_f32_16x16x32_bf16 v[224:227], v[184:187], v[160:163], v[224:227]\n"
    "v_lshl_add_u32 v43, v43, 2, s40\n"
    "v_mfma_f32_16x16x32_bf16 v[228:231], v[184:187], v[164:167], 0\n"
    "v_lshl_add_u32 v42, v85, 3, s64\n"
    "v_mfma_f32_16x16x32_bf16 v[228:231], v[152:155], v[132:135], v[228:231]\n"
    "v_mfma_f32_16x16x32_bf16 v[232:235], v[152:155], v[136:139], 0\n"
    "s_lshl_b32 s24, s24, 8\n"
    "v_mfma_f32_16x16x32_bf16 v[232:235], v[184:187], v[168:171], v[232:235]\n"
    "v_add_u32 v58, 98304, v56\n"
    "v_mfma_f32_16x16x32_bf16 v[236:239], v[184:187], v[172:175], 0\n"
    "v_mfma_f32_16x16x32_bf16 v[236:239], v[152:155], v[140:143], v[236:239]\n"
    "v_add_u32 v59, 98304, v57\n"
    "v_mfma_f32_16x16x32_bf16 v[252:255], v[156:159], v[140:143], 0\n"
    "v_add_u32 v80, 98304, v76\n"
    "v_mfma_f32_16x16x32_bf16 v[252:255], v[188:191], v[172:175], v[252:255]\n"
    "v_mfma_f32_16x16x32_bf16 v[248:251], v[188:191], v[168:171], 0\n"
    "v_add_u32 v81, 98304, v77\n"
    "v_mfma_f32_16x16x32_bf16 v[248:251], v[156:159], v[136:139], v[248:251]\n"
    "v_add_u32 v82, 98304, v78\n"
    "v_mfma_f32_16x16x32_bf16 v[244:247], v[156:159], v[132:135], 0\n"
    "v_mfma_f32_16x16x32_bf16 v[244:247], v[188:191], v[164:167], v[244:247]\n"
    "v_add_u32 v83, 98304, v79\n"
    "v_mfma_f32_16x16x32_bf16 v[240:243], v[188:191], v[160:163], 0\n"
    "v_mfma_f32_16x16x32_bf16 v[240:243], v[156:159], v[128:131], v[240:243]\n"
    "s_waitcnt vmcnt(4)\n"
    "s_barrier\n"
    "s_add_u32 m0, s35, 0\n"
    "s_nop 0\n"
    "global_load_lds_dwordx4 v46, s[30:31] offset:0\n"
    "ds_read_b128 v[128:131], v56 offset:49152\n"
    "ds_read_b128 v[132:135], v56 offset:51200\n"
    "ds_read_b128 v[136:139], v56 offset:53248\n"
    "ds_read_b128 v[140:143], v56 offset:55296\n"
    "global_load_lds_dwordx4 v47, s[30:31] offset:1024\n"
    "s_bfe_u32 s66, s28, 0x80000\n"
    "s_mul_i32 s66, s66, 53248\n"
    "s_add_u32 s30, s6, s66\n"
    "s_addc_u32 s31, s7, 0\n"
    "ds_read_b64_tr_b16 v[144:145], v76 offset:49152\n"
    "ds_read_b64_tr_b16 v[146:147], v76 offset:50176\n"
    "ds_read_b64_tr_b16 v[148:149], v77 offset:49152\n"
    "ds_read_b64_tr_b16 v[150:151], v77 offset:50176\n"
    "s_add_u32 m0, s34, 0\n"
    "s_nop 0\n"
    "global_load_lds_dwordx4 v52, s[36:37] offset:0\n"
    "ds_read_b64_tr_b16 v[152:153], v78 offset:49152\n"
    "ds_read_b64_tr_b16 v[154:155], v78 offset:50176\n"
    "ds_read_b64_tr_b16 v[156:157], v79 offset:49152\n"
    "ds_read_b64_tr_b16 v[158:159], v79 offset:50176\n"
    "global_load_lds_dwordx4 v53, s[36:37] offset:1024\n"
    "ds_read_b128 v[160:163], v57 offset:49152\n"
    "ds_read_b128 v[164:167], v57 offset:51200\n"
    "ds_read_b128 v[168:171], v57 offset:53248\n"
    "ds_read_b128 v[172:175], v57 offset:55296\n"
    "global_load_lds_dwordx4 v54, s[36:37] offset:2048\n"
    "ds_read_b64_tr_b16 v[176:177], v76 offset:57344\n"
    "ds_read_b64_tr_b16 v[178:179], v76 offset:58368\n"
    "ds_read_b64_tr_b16 v[180:181], v77 offset:57344\n"
    "ds_read_b64_tr_b16 v[182:183], v77 offset:58368\n"
    "global_load_lds_dwordx4 v55, s[36:37] offset:3072\n"
    "ds_read_b64_tr_b16 v[184:185], v78 offset:57344\n"
    "ds_read_b64_tr_b16 v[186:187], v78 offset:58368\n"
    "ds_read_b64_tr_b16 v[188:189], v79 offset:57344\n"
    "ds_read_b64_tr_b16 v[190:191], v79 offset:58368\n"
    "s_waitcnt lgkmcnt(0)\n"
    "s_barrier\n"
    "v_mfma_f32_16x16x32_bf16 v[192:195], v[144:147], v[128:131], v[192:195]\n"
    "v_lshlrev_b32 v101, 7, v101\n"
    "v_mfma_f32_16x16x32_bf16 v[192:195], v[176:179], v[160:163], v[192:195]\n"
    "v_mfma_f32_16x16x32_bf16 v[196:199], v[176:179], v[164:167], v[196:199]\n"
    "v_add_u32_dpp v52, v100, v48 quad_perm:[0,0,0,0] row_mask:0xf bank_mask:0xf\n"
    "v_mfma_f32_16x16x32_bf16 v[196:199], v[144:147], v[132:135], v[196:199]\n"
    "v_mfma_f32_16x16x32_bf16 v[200:203], v[144:147], v[136:139], v[200:203]\n"
    "v_add_u32_dpp v53, v100, v49 quad_perm:[1,1,1,1] row_mask:0xf bank_mask:0xf\n"
    "v_mfma_f32_16x16x32_bf16 v[200:203], v[176:179], v[168:171], v[200:203]\n"
    "v_mfma_f32_16x16x32_bf16 v[204:207], v[176:179], v[172:175], v[204:207]\n"
    "v_add_u32_dpp v54, v100, v50 quad_perm:[2,2,2,2] row_mask:0xf bank_mask:0xf\n"
    "v_mfma_f32_16x16x32_bf16 v[204:207], v[144:147], v[140:143], v[204:207]\n"
    "v_mfma_f32_16x16x32_bf16 v[220:223], v[148:151], v[140:143], v[220:223]\n"
    "v_add_u32_dpp v55, v100, v51 quad_perm:[3,3,3,3] row_mask:0xf bank_mask:0xf\n"
    "v_mfma_f32_16x16x32_bf16 v[220:223], v[180:183], v[172:175], v[220:223]\n"
    "v_mfma_f32_16x16x32_bf16 v[216:219], v[180:183], v[168:171], v[216:219]\n"
    "s_cmp_lt_u32 s20, 4\n"
    "s_cselect_b32 s40, -1, 0\n"
    "v_mfma_f32_16x16x32_bf16 v[216:219], v[148:151], v[136:139], v[216:219]\n"
    "v_mfma_f32_16x16x32_bf16 v[212:215], v[148:151], v[132:135], v[212:215]\n"
    "v_lshlrev_b32 v92, 2, v0\n"
    "v_mfma_f32_16x16x32_bf16 v[212:215], v[180:183], v[164:167], v[212:215]\n"
    "v_mfma_f32_16x16x32_bf16 v[208:211], v[180:183], v[160:163], v[208:211]\n"
    "v_mov_b32 v93, s40\n"
    "v_mfma_f32_16x16x32_bf16 v[208:211], v[148:151], v[128:131], v[208:211]\n"
    "v_mfma_f32_16x16x32_bf16 v[224:227], v[152:155], v[128:131], v[224:227]\n"
    "v_add_u32 v92, 149504, v92\n"
    "v_mfma_f32_16x16x32_bf16 v[224:227], v[184:187], v[160:163], v[224:227]\n"
    "v_mfma_f32_16x16x32_bf16 v[228:231], v[184:187], v[164:167], v[228:231]\n"
    "s_mul_i32 s47, s3, 5042\n"
    "v_mfma_f32_16x16x32_bf16 v[228:231], v[152:155], v[132:135], v[228:231]\n"
    "v_mfma_f32_16x16x32_bf16 v[232:235], v[152:155], v[136:139], v[232:235]\n"
    "s_lshr_b32 s47, s47, 16\n"
    "v_mfma_f32_16x16x32_bf16 v[232:235], v[184:187], v[168:171], v[232:235]\n"
    "v_mfma_f32_16x16x32_bf16 v[236:239], v[184:187], v[172:175], v[236:239]\n"
    "s_lshr_b32 s21, s20, 2\n"
    "v_mfma_f32_16x16x32_bf16 v[236:239], v[152:155], v[140:143], v[236:239]\n"
    "v_mfma_f32_16x16x32_bf16 v[252:255], v[156:159], v[140:143], v[252:255]\n"
    "s_lshl1_add_u32 s22, s21, s20\n"
    "v_mfma_f32_16x16x32_bf16 v[252:255], v[188:191], v[172:175], v[252:255]\n"
    "v_mfma_f32_16x16x32_bf16 v[248:251], v[188:191], v[168:171], v[248:251]\n"
    "s_and_b32 s22, s22, 3\n"
    "v_mfma_f32_16x16x32_bf16 v[248:251], v[156:159], v[136:139], v[248:251]\n"
    "v_mfma_f32_16x16x32_bf16 v[244:247], v[156:159], v[132:135], v[244:247]\n"
    "s_lshl_b32 s58, s21, 8\n"
    "v_mfma_f32_16x16x32_bf16 v[244:247], v[188:191], v[164:167], v[244:247]\n"
    "v_mfma_f32_16x16x32_bf16 v[240:243], v[188:191], v[160:163], v[240:243]\n"
    "v_mfma_f32_16x16x32_bf16 v[240:243], v[156:159], v[128:131], v[240:243]\n"
    "s_waitcnt vmcnt(6)\n"
    "s_barrier\n"
    "s_add_u32 m0, s35, 49152\n"
    "s_nop 0\n"
    "global_load_lds_dwordx4 v46, s[30:31] offset:0\n"
    "ds_read_b128 v[128:131], v58 offset:0\n"
    "ds_read_b128 v[132:135], v58 offset:2048\n"
    "ds_read_b128 v[136:139], v58 offset:4096\n"
    "ds_read_b128 v[140:143], v58 offset:6144\n"
    "global_load_lds_dwordx4 v47, s[30:31] offset:1024\n"
    "s_add_u32 s30, s30, 212992\n"
    "s_addc_u32 s31, s31, 0\n"
    "ds_read_b64_tr_b16 v[144:145], v80 offset:0\n"
    "ds_read_b64_tr_b16 v[146:147], v80 offset:1024\n"
    "ds_read_b64_tr_b16 v[148:149], v81 offset:0\n"
    "ds_read_b64_tr_b16 v[150:151], v81 offset:1024\n"
    "s_add_u32 m0, s34, 49152\n"
    "s_nop 0\n"
    "global_load_lds_dwordx4 v52, s[36:37] offset:0\n"
    "ds_read_b64_tr_b16 v[152:153], v82 offset:0\n"
    "ds_read_b64_tr_b16 v[154:155], v82 offset:1024\n"
    "ds_read_b64_tr_b16 v[156:157], v83 offset:0\n"
    "ds_read_b64_tr_b16 v[158:159], v83 offset:1024\n"
    "global_load_lds_dwordx4 v53, s[36:37] offset:1024\n"
    "ds_read_b128 v[160:163], v59 offset:0\n"
    "ds_read_b128 v[164:167], v59 offset:2048\n"
    "ds_read_b128 v[168:171], v59 offset:4096\n"
    "ds_read_b128 v[172:175], v59 offset:6144\n"
    "global_load_lds_dwordx4 v54, s[36:37] offset:2048\n"
    "ds_read_b64_tr_b16 v[176:177], v80 offset:8192\n"
    "ds_read_b64_tr_b16 v[178:179], v80 offset:9216\n"
    "ds_read_b64_tr_b16 v[180:181], v81 offset:8192\n"
    "ds_read_b64_tr_b16 v[182:183], v81 offset:9216\n"
    "global_load_lds_dwordx4 v55, s[36:37] offset:3072\n"
    "ds_read_b64_tr_b16 v[184:185], v82 offset:8192\n"
    "ds_read_b64_tr_b16 v[186:187], v82 offset:9216\n"
    "ds_read_b64_tr_b16 v[188:189], v83 offset:8192\n"
    "ds_read_b64_tr_b16 v[190:191], v83 offset:9216\n"
    "s_waitcnt vmcnt(10) lgkmcnt(0)\n"
    "s_barrier\n"
    "v_mfma_f32_16x16x32_bf16 v[192:195], v[144:147], v[128:131], v[192:195]\n"
    "v_lshlrev_b32 v102, 7, v102\n"
    "v_mfma_f32_16x16x32_bf16 v[192:195], v[176:179], v[160:163], v[192:195]\n"
    "v_mfma_f32_16x16x32_bf16 v[196:199], v[176:179], v[164:167], v[196:199]\n"
    "v_add_u32_dpp v52, v101, v48 quad_perm:[0,0,0,0] row_mask:0xf bank_mask:0xf\n"
    "v_mfma_f32_16x16x32_bf16 v[196:199], v[144:147], v[132:135], v[196:199]\n"
    "v_mfma_f32_16x16x32_bf16 v[200:203], v[144:147], v[136:139], v[200:203]\n"
    "v_add_u32_dpp v53, v101, v49 quad_perm:[1,1,1,1] row_mask:0xf bank_mask:0xf\n"
    "v_mfma_f32_16x16x32_bf16 v[200:203], v[176:179], v[168:171], v[200:203]\n"
    "v_mfma_f32_16x16x32_bf16 v[204:207], v[176:179], v[172:175], v[204:207]\n"
    "v_add_u32_dpp v54, v101, v50 quad_perm:[2,2,2,2] row_mask:0xf bank_mask:0xf\n"
    "v_mfma_f32_16x16x32_bf16 v[204:207], v[144:147], v[140:143], v[204:207]\n"
    "v_mfma_f32_16x16x32_bf16 v[220:223], v[148:151], v[140:143], v[220:223]\n"
    "v_add_u32_dpp v55, v101, v51 quad_perm:[3,3,3,3] row_mask:0xf bank_mask:0xf\n"
    "v_mfma_f32_16x16x32_bf16 v[220:223], v[180:183], v[172:175], v[220:223]\n"
    "v_mfma_f32_16x16x32_bf16 v[216:219], v[180:183], v[168:171], v[216:219]\n"
    "s_add_u32 s58, s58, 150528\n"
    "v_mfma_f32_16x16x32_bf16 v[216:219], v[148:151], v[136:139], v[216:219]\n"
    "v_mfma_f32_16x16x32_bf16 v[212:215], v[148:151], v[132:135], v[212:215]\n"
    "s_mov_b32 s40, s47\n"
    "v_mfma_f32_16x16x32_bf16 v[212:215], v[180:183], v[164:167], v[212:215]\n"
    "v_mfma_f32_16x16x32_bf16 v[208:211], v[180:183], v[160:163], v[208:211]\n"
    "s_mul_i32 s54, s40, 512\n"
    "v_mfma_f32_16x16x32_bf16 v[208:211], v[148:151], v[128:131], v[208:211]\n"
    "v_mfma_f32_16x16x32_bf16 v[224:227], v[152:155], v[128:131], v[224:227]\n"
    "s_add_u32 s54, s54, 2\n"
    "v_mfma_f32_16x16x32_bf16 v[224:227], v[184:187], v[160:163], v[224:227]\n"
    "v_mfma_f32_16x16x32_bf16 v[228:231], v[184:187], v[164:167], v[228:231]\n"
    "s_lshl_b32 s40, s40, 7\n"
    "v_mfma_f32_16x16x32_bf16 v[228:231], v[152:155], v[132:135], v[228:231]\n"
    "v_mfma_f32_16x16x32_bf16 v[232:235], v[152:155], v[136:139], v[232:235]\n"
    "s_add_u32 s40, s40, 32768\n"
    "v_mfma_f32_16x16x32_bf16 v[232:235], v[184:187], v[168:171], v[232:235]\n"
    "v_mfma_f32_16x16x32_bf16 v[236:239], v[184:187], v[172:175], v[236:239]\n"
    "s_add_u32 s52, s16, s40\n"
    "s_addc_u32 s53, s17, 0\n"
    "v_mfma_f32_16x16x32_bf16 v[236:239], v[152:155], v[140:143], v[236:239]\n"
    "v_mfma_f32_16x16x32_bf16 v[252:255], v[156:159], v[140:143], v[252:255]\n"
    "s_mov_b32 s57, 0x7fff7fff\n"
    "v_mfma_f32_16x16x32_bf16 v[252:255], v[188:191], v[172:175], v[252:255]\n"
    "v_mfma_f32_16x16x32_bf16 v[248:251], v[188:191], v[168:171], v[248:251]\n"
    "s_mov_b32 s56, 0\n"
    "v_mfma_f32_16x16x32_bf16 v[248:251], v[156:159], v[136:139], v[248:251]\n"
    "v_mfma_f32_16x16x32_bf16 v[244:247], v[156:159], v[132:135], v[244:247]\n"
    "s_mov_b32 s27, 0\n"
    "v_mfma_f32_16x16x32_bf16 v[244:247], v[188:191], v[164:167], v[244:247]\n"
    "v_mfma_f32_16x16x32_bf16 v[240:243], v[188:191], v[160:163], v[240:243]\n"
    "v_mfma_f32_16x16x32_bf16 v[240:243], v[156:159], v[128:131], v[240:243]\n"
    "s_waitcnt vmcnt(6)\n"
    "s_barrier\n"
    "s_add_u32 m0, s35, 98304\n"
    "s_nop 0\n"
    "global_load_lds_dwordx4 v46, s[30:31] offset:0\n"
    "ds_read_b128 v[128:131], v56 offset:0\n"
    "ds_read_b128 v[132:135], v56 offset:2048\n"
    "ds_read_b128 v[136:139], v56 offset:4096\n"
    "ds_read_b128 v[140:143], v56 offset:6144\n"
    "global_load_lds_dwordx4 v47, s[30:31] offset:1024\n"
    "s_add_u32 s30, s30, 212992\n"
    "s_addc_u32 s31, s31, 0\n"
    "ds_read_b64_tr_b16 v[144:145], v76 offset:0\n"
    "ds_read_b64_tr_b16 v[146:147], v76 offset:1024\n"
    "ds_read_b64_tr_b16 v[148:149], v77 offset:0\n"
    "ds_read_b64_tr_b16 v[150:151], v77 offset:1024\n"
    "s_add_u32 m0, s34, 98304\n"
    "s_nop 0\n"
    "global_load_lds_dwordx4 v52, s[36:37] offset:0\n"
    "ds_read_b64_tr_b16 v[152:153], v78 offset:0\n"
    "ds_read_b64_tr_b16 v[154:155], v78 offset:1024\n"
    "ds_read_b64_tr_b16 v[156:157], v79 offset:0\n"
    "ds_read_b64_tr_b16 v[158:159], v79 offset:1024\n"
    "global_load_lds_dwordx4 v53, s[36:37] offset:1024\n"
    "ds_read_b128 v[160:163], v57 offset:0\n"
    "ds_read_b128 v[164:167], v57 offset:2048\n"
    "ds_read_b128 v[168:171], v57 offset:4096\n"
    "ds_read_b128 v[172:175], v57 offset:6144\n"
    "global_load_lds_dwordx4 v54, s[36:37] offset:2048\n"
    "ds_read_b64_tr_b16 v[176:177], v76 offset:8192\n"
    "ds_read_b64_tr_b16 v[178:179], v76 offset:9216\n"
    "ds_read_b64_tr_b16 v[180:181], v77 offset:8192\n"
    "ds_read_b64_tr_b16 v[182:183], v77 offset:9216\n"
    "global_load_lds_dwordx4 v55, s[36:37] offset:3072\n"
    "ds_read_b64_tr_b16 v[184:185], v78 offset:8192\n"
    "ds_read_b64_tr_b16 v[186:187], v78 offset:9216\n"
    "ds_read_b64_tr_b16 v[188:189], v79 offset:8192\n"
    "ds_read_b64_tr_b16 v[190:191], v79 offset:9216\n"
    "s_waitcnt vmcnt(10) lgkmcnt(0)\n"
    "s_barrier\n"
    "v_mfma_f32_16x16x32_bf16 v[192:195], v[144:147], v[128:131], v[192:195]\n"
    "v_lshlrev_b32 v103, 7, v103\n"
    "v_mfma_f32_16x16x32_bf16 v[192:195], v[176:179], v[160:163], v[192:195]\n"
    "v_mfma_f32_16x16x32_bf16 v[196:199], v[176:179], v[164:167], v[196:199]\n"
    "v_add_u32_dpp v52, v102, v48 quad_perm:[0,0,0,0] row_mask:0xf bank_mask:0xf\n"
    "v_mfma_f32_16x16x32_bf16 v[196:199], v[144:147], v[132:135], v[196:199]\n"
    "v_mfma_f32_16x16x32_bf16 v[200:203], v[144:147], v[136:139], v[200:203]\n"
    "v_add_u32_dpp v53, v102, v49 quad_perm:[1,1,1,1] row_mask:0xf bank_mask:0xf\n"
    "v_mfma_f32_16x16x32_bf16 v[200:203], v[176:179], v[168:171], v[200:203]\n"
    "v_mfma_f32_16x16x32_bf16 v[204:207], v[176:179], v[172:175], v[204:207]\n"
    "v_add_u32_dpp v54, v102, v50 quad_perm:[2,2,2,2] row_mask:0xf bank_mask:0xf\n"
    "v_mfma_f32_16x16x32_bf16 v[204:207], v[144:147], v[140:143], v[204:207]\n"
    "v_mfma_f32_16x16x32_bf16 v[220:223], v[148:151], v[140:143], v[220:223]\n"
    "v_add_u32_dpp v55, v102, v51 quad_perm:[3,3,3,3] row_mask:0xf bank_mask:0xf\n"
    "v_mfma_f32_16x16x32_bf16 v[220:223], v[180:183], v[172:175], v[220:223]\n"
    "v_mfma_f32_16x16x32_bf16 v[216:219], v[180:183], v[168:171], v[216:219]\n"
    "s_mov_b32 s55, 9\n"
    "v_mfma_f32_16x16x32_bf16 v[216:219], v[148:151], v[136:139], v[216:219]\n"
    "v_mfma_f32_16x16x32_bf16 v[212:215], v[148:151], v[132:135], v[212:215]\n"
    "s_mov_b32 s44, 0\n"
    "v_mfma_f32_16x16x32_bf16 v[212:215], v[180:183], v[164:167], v[212:215]\n"
    "v_mfma_f32_16x16x32_bf16 v[208:211], v[180:183], v[160:163], v[208:211]\n"
    "v_lshrrev_b32 v17, 3, v1\n"
    "v_mfma_f32_16x16x32_bf16 v[208:211], v[148:151], v[128:131], v[208:211]\n"
    "v_mfma_f32_16x16x32_bf16 v[224:227], v[152:155], v[128:131], v[224:227]\n"
    "v_mul_u32_u24 v17, 12, v17\n"
    "v_mfma_f32_16x16x32_bf16 v[224:227], v[184:187], v[160:163], v[224:227]\n"
    "v_mfma_f32_16x16x32_bf16 v[228:231], v[184:187], v[164:167], v[228:231]\n"
    "v_and_b32 v19, 7, v1\n"
    "v_mfma_f32_16x16x32_bf16 v[228:231], v[152:155], v[132:135], v[228:231]\n"
    "v_mfma_f32_16x16x32_bf16 v[232:235], v[152:155], v[136:139], v[232:235]\n"
    "v_lshlrev_b32 v19, 4, v19\n"
    "v_mfma_f32_16x16x32_bf16 v[232:235], v[184:187], v[168:171], v[232:235]\n"
    "v_mfma_f32_16x16x32_bf16 v[236:239], v[184:187], v[172:175], v[236:239]\n"
    "v_lshrrev_b32 v18, 5, v1\n"
    "v_mfma_f32_16x16x32_bf16 v[236:239], v[152:155], v[140:143], v[236:239]\n"
    "v_mfma_f32_16x16x32_bf16 v[252:255], v[156:159], v[140:143], v[252:255]\n"
    "v_lshlrev_b32 v18, 2, v18\n"
    "v_mfma_f32_16x16x32_bf16 v[252:255], v[188:191], v[172:175], v[252:255]\n"
    "v_mfma_f32_16x16x32_bf16 v[248:251], v[188:191], v[168:171], v[248:251]\n"
    "v_and_b32 v20, 31, v1\n"
    "v_mfma_f32_16x16x32_bf16 v[248:251], v[156:159], v[136:139], v[248:251]\n"
    "v_mfma_f32_16x16x32_bf16 v[244:247], v[156:159], v[132:135], v[244:247]\n"
    "v_lshlrev_b32 v20, 2, v20\n"
    "v_mfma_f32_16x16x32_bf16 v[244:247], v[188:191], v[164:167], v[244:247]\n"
    "v_mfma_f32_16x16x32_bf16 v[240:243], v[188:191], v[160:163], v[240:243]\n"
    "v_mfma_f32_16x16x32_bf16 v[240:243], v[156:159], v[128:131], v[240:243]\n"
    "s_waitcnt vmcnt(6)\n"
    "s_barrier\n"
    "s_add_u32 m0, s35, 0\n"
    "s_nop 0\n"
    "global_load_lds_dwordx4 v46, s[30:31] offset:0\n"
    "ds_read_b128 v[128:131], v56 offset:49152\n"
    "ds_read_b128 v[132:135], v56 offset:51200\n"
    "ds_read_b128 v[136:139], v56 offset:53248\n"
    "ds_read_b128 v[140:143], v56 offset:55296\n"
    "global_load_lds_dwordx4 v47, s[30:31] offset:1024\n"
    "s_add_u32 s30, s30, 212992\n"
    "s_addc_u32 s31, s31, 0\n"
    "ds_read_b64_tr_b16 v[144:145], v76 offset:49152\n"
    "ds_read_b64_tr_b16 v[146:147], v76 offset:50176\n"
    "ds_read_b64_tr_b16 v[148:149], v77 offset:49152\n"
    "ds_read_b64_tr_b16 v[150:151], v77 offset:50176\n"
    "s_add_u32 m0, s34, 0\n"
    "s_nop 0\n"
    "global_load_lds_dwordx4 v52, s[36:37] offset:0\n"
    "ds_read_b64_tr_b16 v[152:153], v78 offset:49152\n"
    "ds_read_b64_tr_b16 v[154:155], v78 offset:50176\n"
    "ds_read_b64_tr_b16 v[156:157], v79 offset:49152\n"
    "ds_read_b64_tr_b16 v[158:159], v79 offset:50176\n"
    "global_load_lds_dwordx4 v53, s[36:37] offset:1024\n"
    "ds_read_b128 v[160:163], v57 offset:49152\n"
    "ds_read_b128 v[164:167], v57 offset:51200\n"
    "ds_read_b128 v[168:171], v57 offset:53248\n"
    "ds_read_b128 v[172:175], v57 offset:55296\n"
    "global_load_lds_dwordx4 v54, s[36:37] offset:2048\n"
    "ds_read_b64_tr_b16 v[176:177], v76 offset:57344\n"
    "ds_read_b64_tr_b16 v[178:179], v76 offset:58368\n"
    "ds_read_b64_tr_b16 v[180:181], v77 offset:57344\n"
    "ds_read_b64_tr_b16 v[182:183], v77 offset:58368\n"
    "global_load_lds_dwordx4 v55, s[36:37] offset:3072\n"
    "ds_read_b64_tr_b16 v[184:185], v78 offset:57344\n"
    "ds_read_b64_tr_b16 v[186:187], v78 offset:58368\n"
    "ds_read_b64_tr_b16 v[188:189], v79 offset:57344\n"
    "ds_read_b64_tr_b16 v[190:191], v79 offset:58368\n"
    "s_waitcnt vmcnt(10) lgkmcnt(0)\n"
    "s_barrier\n"
    "v_mfma_f32_16x16x32_bf16 v[192:195], v[144:147], v[128:131], v[192:195]\n"
    "v_lshlrev_b32 v104, 7, v104\n"
    "v_mfma_f32_16x16x32_bf16 v[192:195], v[176:179], v[160:163], v[192:195]\n"
    "v_add_u32_dpp v52, v103, v48 quad_perm:[0,0,0,0] row_mask:0xf bank_mask:0xf\n"
    "v_mfma_f32_16x16x32_bf16 v[196:199], v[176:179], v[164:167], v[196:199]\n"
    "v_mfma_f32_16x16x32_bf16 v[196:199], v[144:147], v[132:135], v[196:199]\n"
    "v_add_u32_dpp v53, v103, v49 quad_perm:[1,1,1,1] row_mask:0xf bank_mask:0xf\n"
    "v_mfma_f32_16x16x32_bf16 v[200:203], v[144:147], v[136:139], v[200:203]\n"
    "v_mfma_f32_16x16x32_bf16 v[200:203], v[176:179], v[168:171], v[200:203]\n"
    "v_add_u32_dpp v54, v103, v50 quad_perm:[2,2,2,2] row_mask:0xf bank_mask:0xf\n"
    "v_mfma_f32_16x16x32_bf16 v[204:207], v[176:179], v[172:175], v[204:207]\n"
    "v_mfma_f32_16x16x32_bf16 v[204:207], v[144:147], v[140:143], v[204:207]\n"
    "v_add_u32_dpp v55, v103, v51 quad_perm:[3,3,3,3] row_mask:0xf bank_mask:0xf\n"
    "v_mfma_f32_16x16x32_bf16 v[220:223], v[148:151], v[140:143], v[220:223]\n"
    "v_mfma_f32_16x16x32_bf16 v[220:223], v[180:183], v[172:175], v[220:223]\n"
    "s_lshl2_add_u32 s40, s22, s58\n"
    "v_mfma_f32_16x16x32_bf16 v[216:219], v[180:183], v[168:171], v[216:219]\n"
    "v_mfma_f32_16x16x32_bf16 v[216:219], v[148:151], v[136:139], v[216:219]\n"
    "s_sub_u32 s40, s40, 20\n"
    "v_mfma_f32_16x16x32_bf16 v[212:215], v[148:151], v[132:135], v[212:215]\n"
    "v_mfma_f32_16x16x32_bf16 v[212:215], v[180:183], v[164:167], v[212:215]\n"
    "v_mov_b32 v25, s40\n"
    "v_mfma_f32_16x16x32_bf16 v[208:211], v[180:183], v[160:163], v[208:211]\n"
    "v_mfma_f32_16x16x32_bf16 v[208:211], v[148:151], v[128:131], v[208:211]\n"
    "s_lshl_b32 s43, s21, 16\n"
    "v_mfma_f32_16x16x32_bf16 v[224:227], v[152:155], v[128:131], v[224:227]\n"
    "v_mfma_f32_16x16x32_bf16 v[224:227], v[184:187], v[160:163], v[224:227]\n"
    "v_lshrrev_b32 v12, 4, v1\n"
    "v_mfma_f32_16x16x32_bf16 v[228:231], v[184:187], v[164:167], v[228:231]\n"
    "v_mfma_f32_16x16x32_bf16 v[228:231], v[152:155], v[132:135], v[228:231]\n"
    "v_bfe_u32 v13, v1, 2, 2\n"
    "v_mfma_f32_16x16x32_bf16 v[232:235], v[152:155], v[136:139], v[232:235]\n"
    "v_mfma_f32_16x16x32_bf16 v[232:235], v[184:187], v[168:171], v[232:235]\n"
    "v_and_b32 v21, 3, v1\n"
    "v_mfma_f32_16x16x32_bf16 v[236:239], v[184:187], v[172:175], v[236:239]\n"
    "v_mfma_f32_16x16x32_bf16 v[236:239], v[152:155], v[140:143], v[236:239]\n"
    "s_lshl_b32 s40, s22, 5\n"
    "v_mfma_f32_16x16x32_bf16 v[252:255], v[156:159], v[140:143], v[252:255]\n"
    "v_mfma_f32_16x16x32_bf16 v[252:255], v[188:191], v[172:175], v[252:255]\n"
    "s_add_u32 s40, s40, s43\n"
    "v_mfma_f32_16x16x32_bf16 v[248:251], v[188:191], v[168:171], v[248:251]\n"
    "v_mfma_f32_16x16x32_bf16 v[248:251], v[156:159], v[136:139], v[248:251]\n"
    "v_lshl_add_u32 v21, v21, 3, s40\n"
    "v_mfma_f32_16x16x32_bf16 v[244:247], v[156:159], v[132:135], v[244:247]\n"
    "v_mfma_f32_16x16x32_bf16 v[244:247], v[188:191], v[164:167], v[244:247]\n"
    "s_movk_i32 s41, 144\n"
    "v_mfma_f32_16x16x32_bf16 v[240:243], v[188:191], v[160:163], v[240:243]\n"
    "v_mfma_f32_16x16x32_bf16 v[240:243], v[156:159], v[128:131], v[240:243]\n"
    "s_waitcnt vmcnt(6)\n"
    "s_barrier\n"
    "s_add_u32 m0, s35, 49152\n"
    "s_nop 0\n"
    "global_load_lds_dwordx4 v46, s[30:31] offset:0\n"
    "ds_read_b128 v[128:131], v58 offset:0\n"
    "ds_read_b128 v[132:135], v58 offset:2048\n"
    "ds_read_b128 v[136:139], v58 offset:4096\n"
    "ds_read_b128 v[140:143], v58 offset:6144\n"
    "global_load_lds_dwordx4 v47, s[30:31] offset:1024\n"
    "s_bfe_u32 s66, s28, 0x80008\n"
    "s_mul_i32 s66, s66, 53248\n"
    "s_add_u32 s30, s6, s66\n"
    "s_addc_u32 s31, s7, 0\n"
    "ds_read_b64_tr_b16 v[144:145], v80 offset:0\n"
    "ds_read_b64_tr_b16 v[146:147], v80 offset:1024\n"
    "ds_read_b64_tr_b16 v[148:149], v81 offset:0\n"
    "ds_read_b64_tr_b16 v[150:151], v81 offset:1024\n"
    "s_add_u32 m0, s34, 49152\n"
    "s_nop 0\n"
    "global_load_lds_dwordx4 v52, s[36:37] offset:0\n"
    "ds_read_b64_tr_b16 v[152:153], v82 offset:0\n"
    "ds_read_b64_tr_b16 v[154:155], v82 offset:1024\n"
    "ds_read_b64_tr_b16 v[156:157], v83 offset:0\n"
    "ds_read_b64_tr_b16 v[158:159], v83 offset:1024\n"
    "global_load_lds_dwordx4 v53, s[36:37] offset:1024\n"
    "ds_read_b128 v[160:163], v59 offset:0\n"
    "ds_read_b128 v[164:167], v59 offset:2048\n"
    "ds_read_b128 v[168:171], v59 offset:4096\n"
    "ds_read_b128 v[172:175], v59 offset:6144\n"
    "global_load_lds_dwordx4 v54, s[36:37] offset:2048\n"
    "ds_read_b64_tr_b16 v[176:177], v80 offset:8192\n"
    "ds_read_b64_tr_b16 v[178:179], v80 offset:9216\n"
    "ds_read_b64_tr_b16 v[180:181], v81 offset:8192\n"
    "ds_read_b64_tr_b16 v[182:183], v81 offset:9216\n"
    "global_load_lds_dwordx4 v55, s[36:37] offset:3072\n"
    "ds_read_b64_tr_b16 v[184:185], v82 offset:8192\n"
    "ds_read_b64_tr_b16 v[186:187], v82 offset:9216\n"
    "ds_read_b64_tr_b16 v[188:189], v83 offset:8192\n"
    "ds_read_b64_tr_b16 v[190:191], v83 offset:9216\n"
    "s_waitcnt vmcnt(10) lgkmcnt(0)\n"
    "s_barrier\n"
    "v_mfma_f32_16x16x32_bf16 v[192:195], v[144:147], v[128:131], v[192:195]\n"
    "v_lshlrev_b32 v105, 7, v105\n"
    "v_mfma_f32_16x16x32_bf16 v[192:195], v[176:179], v[160:163], v[192:195]\n"
    "v_mfma_f32_16x16x32_bf16 v[196:199], v[176:179], v[164:167], v[196:199]\n"
    "v_add_u32_dpp v52, v104, v48 quad_perm:[0,0,0,0] row_mask:0xf bank_mask:0xf\n"
    "v_mfma_f32_16x16x32_bf16 v[196:199], v[144:147], v[132:135], v[196:199]\n"
    "v_mfma_f32_16x16x32_bf16 v[200:203], v[144:147], v[136:139], v[200:203]\n"
    "v_add_u32_dpp v53, v104, v49 quad_perm:[1,1,1,1] row_mask:0xf bank_mask:0xf\n"
    "v_mfma_f32_16x16x32_bf16 v[200:203], v[176:179], v[168:171], v[200:203]\n"
    "v_mfma_f32_16x16x32_bf16 v[204:207], v[176:179], v[172:175], v[204:207]\n"
    "v_add_u32_dpp v54, v104, v50 quad_perm:[2,2,2,2] row_mask:0xf bank_mask:0xf\n"
    "v_mfma_f32_16x16x32_bf16 v[204:207], v[144:147], v[140:143], v[204:207]\n"
    "v_mfma_f32_16x16x32_bf16 v[220:223], v[148:151], v[140:143], v[220:223]\n"
    "v_add_u32_dpp v55, v104, v51 quad_perm:[3,3,3,3] row_mask:0xf bank_mask:0xf\n"
    "v_mfma_f32_16x16x32_bf16 v[220:223], v[180:183], v[172:175], v[220:223]\n"
    "v_mfma_f32_16x16x32_bf16 v[216:219], v[180:183], v[168:171], v[216:219]\n"
    "v_add_u32 v22, v12, v13\n"
    "v_mfma_f32_16x16x32_bf16 v[216:219], v[148:151], v[136:139], v[216:219]\n"
    "v_mfma_f32_16x16x32_bf16 v[212:215], v[148:151], v[132:135], v[212:215]\n"
    "v_and_b32 v22, 3, v22\n"
    "v_mfma_f32_16x16x32_bf16 v[212:215], v[180:183], v[164:167], v[212:215]\n"
    "v_mfma_f32_16x16x32_bf16 v[208:211], v[180:183], v[160:163], v[208:211]\n"
    "v_mad_u32_u24 v4, v22, s41, v21\n"
    "v_mfma_f32_16x16x32_bf16 v[208:211], v[148:151], v[128:131], v[208:211]\n"
    "v_mfma_f32_16x16x32_bf16 v[224:227], v[152:155], v[128:131], v[224:227]\n"
    "v_cmp_gt_u32 vcc, 2, v13\n"
    "v_mfma_f32_16x16x32_bf16 v[224:227], v[184:187], v[160:163], v[224:227]\n"
    "v_mfma_f32_16x16x32_bf16 v[228:231], v[184:187], v[164:167], v[228:231]\n"
    "v_add_u32 v24, 22, v13\n"
    "v_mfma_f32_16x16x32_bf16 v[228:231], v[152:155], v[132:135], v[228:231]\n"
    "v_mfma_f32_16x16x32_bf16 v[232:235], v[152:155], v[136:139], v[232:235]\n"
    "v_lshlrev_b32 v13, 3, v13\n"
    "v_mfma_f32_16x16x32_bf16 v[232:235], v[184:187], v[168:171], v[232:235]\n"
    "v_mfma_f32_16x16x32_bf16 v[236:239], v[184:187], v[172:175], v[236:239]\n"
    "v_cndmask_b32 v22, v24, v22, vcc\n"
    "v_mfma_f32_16x16x32_bf16 v[236:239], v[152:155], v[140:143], v[236:239]\n"
    "v_mfma_f32_16x16x32_bf16 v[252:255], v[156:159], v[140:143], v[252:255]\n"
    "v_mad_u32_u24 v3, v22, s41, v21\n"
    "v_mfma_f32_16x16x32_bf16 v[252:255], v[188:191], v[172:175], v[252:255]\n"
    "v_mfma_f32_16x16x32_bf16 v[248:251], v[188:191], v[168:171], v[248:251]\n"
    "v_cmp_eq_u32 vcc, 3, v12\n"
    "v_mfma_f32_16x16x32_bf16 v[248:251], v[156:159], v[136:139], v[248:251]\n"
    "v_mfma_f32_16x16x32_bf16 v[244:247], v[156:159], v[132:135], v[244:247]\n"
    "v_xor_b32 v22, 0, v12\n"
    "v_mfma_f32_16x16x32_bf16 v[244:247], v[188:191], v[164:167], v[244:247]\n"
    "v_mfma_f32_16x16x32_bf16 v[240:243], v[188:191], v[160:163], v[240:243]\n"
    "v_mfma_f32_16x16x32_bf16 v[240:243], v[156:159], v[128:131], v[240:243]\n"
    "s_waitcnt vmcnt(6)\n"
    "s_barrier\n"
    "s_add_u32 m0, s35, 98304\n"
    "s_nop 0\n"
    "global_load_lds_dwordx4 v46, s[30:31] offset:0\n"
    "ds_read_b128 v[128:131], v56 offset:0\n"
    "ds_read_b128 v[132:135], v56 offset:2048\n"
    "ds_read_b128 v[136:139], v56 offset:4096\n"
    "global_load_lds_dwordx4 v47, s[30:31] offset:1024\n"
    "s_add_u32 s30, s30, 212992\n"
    "s_addc_u32 s31, s31, 0\n"
    "ds_read_b128 v[140:143], v56 offset:6144\n"
    "ds_read_b64_tr_b16 v[144:145], v76 offset:0\n"
    "ds_read_b64_tr_b16 v[146:147], v76 offset:1024\n"
    "s_add_u32 m0, s34, 98304\n"
    "s_nop 0\n"
    "global_load_lds_dwordx4 v52, s[36:37] offset:0\n"
    "ds_read_b64_tr_b16 v[148:149], v77 offset:0\n"
    "ds_read_b64_tr_b16 v[150:151], v77 offset:1024\n"
    "ds_read_b64_tr_b16 v[152:153], v78 offset:0\n"
    "global_load_lds_dwordx4 v53, s[36:37] offset:1024\n"
    "ds_read_b64_tr_b16 v[154:155], v78 offset:1024\n"
    "ds_read_b64_tr_b16 v[156:157], v79 offset:0\n"
    "ds_read_b64_tr_b16 v[158:159], v79 offset:1024\n"
    "global_load_lds_dwordx4 v54, s[36:37] offset:2048\n"
    "ds_read_b128 v[160:163], v57 offset:0\n"
    "ds_read_b128 v[164:167], v57 offset:2048\n"
    "ds_read_b128 v[168:171], v57 offset:4096\n"
    "global_load_lds_dwordx4 v55, s[36:37] offset:3072\n"
    "ds_read_b128 v[172:175], v57 offset:6144\n"
    "ds_read_b64_tr_b16 v[176:177], v76 offset:8192\n"
    "ds_read_b64_tr_b16 v[178:179], v76 offset:9216\n"
    "s_mov_b32 m0, s64\n"
    "s_nop 0\n"
    "global_load_lds_dword v43, s[10:11]\n"
    "ds_read_b64_tr_b16 v[180:181], v77 offset:8192\n"
    "ds_read_b64_tr_b16 v[182:183], v77 offset:9216\n"
    "ds_read_b64_tr_b16 v[184:185], v78 offset:8192\n"
    "ds_read_b64_tr_b16 v[186:187], v78 offset:9216\n"
    "ds_read_b64_tr_b16 v[188:189], v79 offset:8192\n"
    "ds_read_b64_tr_b16 v[190:191], v79 offset:9216\n"
    "s_waitcnt vmcnt(11) lgkmcnt(0)\n"
    "s_barrier\n"
    "v_mfma_f32_16x16x32_bf16 v[192:195], v[144:147], v[128:131], v[192:195]\n"
    "v_lshlrev_b32 v106, 7, v106\n"
    "v_mfma_f32_16x16x32_bf16 v[192:195], v[176:179], v[160:163], v[192:195]\n"
    "v_mfma_f32_16x16x32_bf16 v[196:199], v[176:179], v[164:167], v[196:199]\n"
    "v_add_u32_dpp v52, v105, v48 quad_perm:[0,0,0,0] row_mask:0xf bank_mask:0xf\n"
    "v_mfma_f32_16x16x32_bf16 v[196:199], v[144:147], v[132:135], v[196:199]\n"
    "v_mfma_f32_16x16x32_bf16 v[200:203], v[144:147], v[136:139], v[200:203]\n"
    "v_add_u32_dpp v53, v105, v49 quad_perm:[1,1,1,1] row_mask:0xf bank_mask:0xf\n"
    "v_mfma_f32_16x16x32_bf16 v[200:203], v[176:179], v[168:171], v[200:203]\n"
    "v_mfma_f32_16x16x32_bf16 v[204:207], v[176:179], v[172:175], v[204:207]\n"
    "v_add_u32_dpp v54, v105, v50 quad_perm:[2,2,2,2] row_mask:0xf bank_mask:0xf\n"
    "v_mfma_f32_16x16x32_bf16 v[204:207], v[144:147], v[140:143], v[204:207]\n"
    "v_mfma_f32_16x16x32_bf16 v[220:223], v[148:151], v[140:143], v[220:223]\n"
    "v_add_u32_dpp v55, v105, v51 quad_perm:[3,3,3,3] row_mask:0xf bank_mask:0xf\n"
    "v_mfma_f32_16x16x32_bf16 v[220:223], v[180:183], v[172:175], v[220:223]\n"
    "v_mfma_f32_16x16x32_bf16 v[216:219], v[180:183], v[168:171], v[216:219]\n"
    "v_and_b32 v22, 1, v22\n"
    "v_mfma_f32_16x16x32_bf16 v[216:219], v[148:151], v[136:139], v[216:219]\n"
    "v_mfma_f32_16x16x32_bf16 v[212:215], v[148:151], v[132:135], v[212:215]\n"
    "v_lshlrev_b32 v22, 1, v22\n"
    "v_mfma_f32_16x16x32_bf16 v[212:215], v[180:183], v[164:167], v[212:215]\n"
    "v_mfma_f32_16x16x32_bf16 v[208:211], v[180:183], v[160:163], v[208:211]\n"
    "v_lshl_add_u32 v22, v12, 3, v22\n"
    "v_mfma_f32_16x16x32_bf16 v[208:211], v[148:151], v[128:131], v[208:211]\n"
    "v_mfma_f32_16x16x32_bf16 v[224:227], v[152:155], v[128:131], v[224:227]\n"
    "s_mov_b32 s40, 0x05040100\n"
    "v_mfma_f32_16x16x32_bf16 v[224:227], v[184:187], v[160:163], v[224:227]\n"
    "v_mfma_f32_16x16x32_bf16 v[228:231], v[184:187], v[164:167], v[228:231]\n"
    "v_bfe_u32 v24, s40, v13, 8\n"
    "v_mfma_f32_16x16x32_bf16 v[228:231], v[152:155], v[132:135], v[228:231]\n"
    "v_mfma_f32_16x16x32_bf16 v[232:235], v[152:155], v[136:139], v[232:235]\n"
    "v_add_u32 v22, v22, v24\n"
    "v_mfma_f32_16x16x32_bf16 v[232:235], v[184:187], v[168:171], v[232:235]\n"
    "v_mfma_f32_16x16x32_bf16 v[236:239], v[184:187], v[172:175], v[236:239]\n"
    "s_mov_b32 s40, 0x1a191818\n"
    "v_mfma_f32_16x16x32_bf16 v[236:239], v[152:155], v[140:143], v[236:239]\n"
    "v_mfma_f32_16x16x32_bf16 v[252:255], v[156:159], v[140:143], v[252:255]\n"
    "v_bfe_u32 v24, s40, v13, 8\n"
    "v_mfma_f32_16x16x32_bf16 v[252:255], v[188:191], v[172:175], v[252:255]\n"
    "v_mfma_f32_16x16x32_bf16 v[248:251], v[188:191], v[168:171], v[248:251]\n"
    "v_cndmask_b32 v22, v22, v24, vcc\n"
    "v_mfma_f32_16x16x32_bf16 v[248:251], v[156:159], v[136:139], v[248:251]\n"
    "v_mfma_f32_16x16x32_bf16 v[244:247], v[156:159], v[132:135], v[244:247]\n"
    "v_mad_u32_u24 v5, v22, s41, v21\n"
    "v_mfma_f32_16x16x32_bf16 v[244:247], v[188:191], v[164:167], v[244:247]\n"
    "v_mfma_f32_16x16x32_bf16 v[240:243], v[188:191], v[160:163], v[240:243]\n"
    "v_mfma_f32_16x16x32_bf16 v[240:243], v[156:159], v[128:131], v[240:243]\n"
    "s_waitcnt vmcnt(7)\n"
    "s_barrier\n"
    "s_add_u32 m0, s35, 0\n"
    "s_nop 0\n"
    "global_load_lds_dwordx4 v46, s[30:31] offset:0\n"
    "ds_read_b128 v[128:131], v56 offset:49152\n"
    "ds_read_b128 v[132:135], v56 offset:51200\n"
    "ds_read_b128 v[136:139], v56 offset:53248\n"
    "ds_read_b128 v[140:143], v56 offset:55296\n"
    "global_load_lds_dwordx4 v47, s[30:31] offset:1024\n"
    "s_add_u32 s30, s30, 212992\n"
    "s_addc_u32 s31, s31, 0\n"
    "ds_read_b64_tr_b16 v[144:145], v76 offset:49152\n"
    "ds_read_b64_tr_b16 v[146:147], v76 offset:50176\n"
    "ds_read_b64_tr_b16 v[148:149], v77 offset:49152\n"
    "ds_read_b64_tr_b16 v[150:151], v77 offset:50176\n"
    "s_add_u32 m0, s34, 0\n"
    "s_nop 0\n"
    "global_load_lds_dwordx4 v52, s[36:37] offset:0\n"
    "ds_read_b64_tr_b16 v[152:153], v78 offset:49152\n"
    "ds_read_b64_tr_b16 v[154:155], v78 offset:50176\n"
    "ds_read_b64_tr_b16 v[156:157], v79 offset:49152\n"
    "ds_read_b64_tr_b16 v[158:159], v79 offset:50176\n"
    "global_load_lds_dwordx4 v53, s[36:37] offset:1024\n"
    "ds_read_b128 v[160:163], v57 offset:49152\n"
    "ds_read_b128 v[164:167], v57 offset:51200\n"
    "ds_read_b128 v[168:171], v57 offset:53248\n"
    "ds_read_b128 v[172:175], v57 offset:55296\n"
    "global_load_lds_dwordx4 v54, s[36:37] offset:2048\n"
    "ds_read_b64_tr_b16 v[176:177], v76 offset:57344\n"
    "ds_read_b64_tr_b16 v[178:179], v76 offset:58368\n"
    "ds_read_b64_tr_b16 v[180:181], v77 offset:57344\n"
    "ds_read_b64_tr_b16 v[182:183], v77 offset:58368\n"
    "global_load_lds_dwordx4 v55, s[36:37] offset:3072\n"
    "ds_read_b64_tr_b16 v[184:185], v78 offset:57344\n"
    "ds_read_b64_tr_b16 v[186:187], v78 offset:58368\n"
    "ds_read_b64_tr_b16 v[188:189], v79 offset:57344\n"
    "ds_read_b64_tr_b16 v[190:191], v79 offset:58368\n"
    "s_waitcnt vmcnt(11) lgkmcnt(0)\n"
    "s_barrier\n"
    "v_mfma_f32_16x16x32_bf16 v[192:195], v[144:147], v[128:131], v[192:195]\n"
    "v_lshlrev_b32 v107, 7, v107\n"
    "v_mfma_f32_16x16x32_bf16 v[192:195], v[176:179], v[160:163], v[192:195]\n"
    "v_add_u32_dpp v52, v106, v48 quad_perm:[0,0,0,0] row_mask:0xf bank_mask:0xf\n"
    "v_mfma_f32_16x16x32_bf16 v[196:199], v[176:179], v[164:167], v[196:199]\n"
    "v_mfma_f32_16x16x32_bf16 v[196:199], v[144:147], v[132:135], v[196:199]\n"
    "v_add_u32_dpp v53, v106, v49 quad_perm:[1,1,1,1] row_mask:0xf bank_mask:0xf\n"
    "v_mfma_f32_16x16x32_bf16 v[200:203], v[144:147], v[136:139], v[200:203]\n"
    "v_mfma_f32_16x16x32_bf16 v[200:203], v[176:179], v[168:171], v[200:203]\n"
    "v_add_u32_dpp v54, v106, v50 quad_perm:[2,2,2,2] row_mask:0xf bank_mask:0xf\n"
    "v_mfma_f32_16x16x32_bf16 v[204:207], v[176:179], v[172:175], v[204:207]\n"
    "v_mfma_f32_16x16x32_bf16 v[204:207], v[144:147], v[140:143], v[204:207]\n"
    "v_add_u32_dpp v55, v106, v51 quad_perm:[3,3,3,3] row_mask:0xf bank_mask:0xf\n"
    "v_mfma_f32_16x16x32_bf16 v[220:223], v[148:151], v[140:143], v[220:223]\n"
    "v_mfma_f32_16x16x32_bf16 v[220:223], v[180:183], v[172:175], v[220:223]\n"
    "v_xor_b32 v22, 1, v12\n"
    "v_mfma_f32_16x16x32_bf16 v[216:219], v[180:183], v[168:171], v[216:219]\n"
    "v_mfma_f32_16x16x32_bf16 v[216:219], v[148:151], v[136:139], v[216:219]\n"
    "v_and_b32 v22, 1, v22\n"
    "v_mfma_f32_16x16x32_bf16 v[212:215], v[148:151], v[132:135], v[212:215]\n"
    "v_mfma_f32_16x16x32_bf16 v[212:215], v[180:183], v[164:167], v[212:215]\n"
    "v_lshlrev_b32 v22, 1, v22\n"
    "v_mfma_f32_16x16x32_bf16 v[208:211], v[180:183], v[160:163], v[208:211]\n"
    "v_mfma_f32_16x16x32_bf16 v[208:211], v[148:151], v[128:131], v[208:211]\n"
    "v_lshl_add_u32 v22, v12, 3, v22\n"
    "v_mfma_f32_16x16x32_bf16 v[224:227], v[152:155], v[128:131], v[224:227]\n"
    "v_mfma_f32_16x16x32_bf16 v[224:227], v[184:187], v[160:163], v[224:227]\n"
    "s_mov_b32 s40, 0x05040100\n"
    "v_mfma_f32_16x16x32_bf16 v[228:231], v[184:187], v[164:167], v[228:231]\n"
    "v_mfma_f32_16x16x32_bf16 v[228:231], v[152:155], v[132:135], v[228:231]\n"
    "v_bfe_u32 v24, s40, v13, 8\n"
    "v_mfma_f32_16x16x32_bf16 v[232:235], v[152:155], v[136:139], v[232:235]\n"
    "v_mfma_f32_16x16x32_bf16 v[232:235], v[184:187], v[168:171], v[232:235]\n"
    "v_add_u32 v22, v22, v24\n"
    "v_mfma_f32_16x16x32_bf16 v[236:239], v[184:187], v[172:175], v[236:239]\n"
    "v_mfma_f32_16x16x32_bf16 v[236:239], v[152:155], v[140:143], v[236:239]\n"
    "s_mov_b32 s40, 0x1a191918\n"
    "v_mfma_f32_16x16x32_bf16 v[252:255], v[156:159], v[140:143], v[252:255]\n"
    "v_mfma_f32_16x16x32_bf16 v[252:255], v[188:191], v[172:175], v[252:255]\n"
    "v_bfe_u32 v24, s40, v13, 8\n"
    "v_mfma_f32_16x16x32_bf16 v[248:251], v[188:191], v[168:171], v[248:251]\n"
    "v_mfma_f32_16x16x32_bf16 v[248:251], v[156:159], v[136:139], v[248:251]\n"
    "v_cndmask_b32 v22, v22, v24, vcc\n"
    "v_mfma_f32_16x16x32_bf16 v[244:247], v[156:159], v[132:135], v[244:247]\n"
    "v_mfma_f32_16x16x32_bf16 v[244:247], v[188:191], v[164:167], v[244:247]\n"
    "v_mad_u32_u24 v6, v22, s41, v21\n"
    "v_mfma_f32_16x16x32_bf16 v[240:243], v[188:191], v[160:163], v[240:243]\n"
    "v_mfma_f32_16x16x32_bf16 v[240:243], v[156:159], v[128:131], v[240:243]\n"
    "s_waitcnt vmcnt(7)\n"
    "s_barrier\n"
    "s_add_u32 m0, s35, 49152\n"
    "s_nop 0\n"
    "global_load_lds_dwordx4 v46, s[30:31] offset:0\n"
    "ds_read_b128 v[128:131], v58 offset:0\n"
    "ds_read_b128 v[132:135], v58 offset:2048\n"
    "ds_read_b128 v[136:139], v58 offset:4096\n"
    "ds_read_b128 v[140:143], v58 offset:6144\n"
    "global_load_lds_dwordx4 v47, s[30:31] offset:1024\n"
    "s_add_u32 s30, s30, 212992\n"
    "s_addc_u32 s31, s31, 0\n"
    "ds_read_b64_tr_b16 v[144:145], v80 offset:0\n"
    "ds_read_b64_tr_b16 v[146:147], v80 offset:1024\n"
    "ds_read_b64_tr_b16 v[148:149], v81 offset:0\n"
    "ds_read_b64_tr_b16 v[150:151], v81 offset:1024\n"
    "s_add_u32 m0, s34, 49152\n"
    "s_nop 0\n"
    "global_load_lds_dwordx4 v52, s[36:37] offset:0\n"
    "ds_read_b64_tr_b16 v[152:153], v82 offset:0\n"
    "ds_read_b64_tr_b16 v[154:155], v82 offset:1024\n"
    "ds_read_b64_tr_b16 v[156:157], v83 offset:0\n"
    "ds_read_b64_tr_b16 v[158:159], v83 offset:1024\n"
    "global_load_lds_dwordx4 v53, s[36:37] offset:1024\n"
    "ds_read_b128 v[160:163], v59 offset:0\n"
    "ds_read_b128 v[164:167], v59 offset:2048\n"
    "ds_read_b128 v[168:171], v59 offset:4096\n"
    "ds_read_b128 v[172:175], v59 offset:6144\n"
    "global_load_lds_dwordx4 v54, s[36:37] offset:2048\n"
    "ds_read_b64_tr_b16 v[176:177], v80 offset:8192\n"
    "ds_read_b64_tr_b16 v[178:179], v80 offset:9216\n"
    "ds_read_b64_tr_b16 v[180:181], v81 offset:8192\n"
    "ds_read_b64_tr_b16 v[182:183], v81 offset:9216\n"
    "global_load_lds_dwordx4 v55, s[36:37] offset:3072\n"
    "ds_read_b64_tr_b16 v[184:185], v82 offset:8192\n"
    "ds_read_b64_tr_b16 v[186:187], v82 offset:9216\n"
    "ds_read_b64_tr_b16 v[188:189], v83 offset:8192\n"
    "ds_read_b64_tr_b16 v[190:191], v83 offset:9216\n"
    "s_waitcnt vmcnt(10) lgkmcnt(0)\n"
    "s_barrier\n"
    "v_mfma_f32_16x16x32_bf16 v[192:195], v[144:147], v[128:131], v[192:195]\n"
    "v_lshlrev_b32 v108, 7, v108\n"
    "v_mfma_f32_16x16x32_bf16 v[192:195], v[176:179], v[160:163], v[192:195]\n"
    "v_mfma_f32_16x16x32_bf16 v[196:199], v[176:179], v[164:167], v[196:199]\n"
    "v_add_u32_dpp v52, v107, v48 quad_perm:[0,0,0,0] row_mask:0xf bank_mask:0xf\n"
    "v_mfma_f32_16x16x32_bf16 v[196:199], v[144:147], v[132:135], v[196:199]\n"
    "v_mfma_f32_16x16x32_bf16 v[200:203], v[144:147], v[136:139], v[200:203]\n"
    "v_add_u32_dpp v53, v107, v49 quad_perm:[1,1,1,1] row_mask:0xf bank_mask:0xf\n"
    "v_mfma_f32_16x16x32_bf16 v[200:203], v[176:179], v[168:171], v[200:203]\n"
    "v_mfma_f32_16x16x32_bf16 v[204:207], v[176:179], v[172:175], v[204:207]\n"
    "v_add_u32_dpp v54, v107, v50 quad_perm:[2,2,2,2] row_mask:0xf bank_mask:0xf\n"
    "v_mfma_f32_16x16x32_bf16 v[204:207], v[144:147], v[140:143], v[204:207]\n"
    "v_mfma_f32_16x16x32_bf16 v[220:223], v[148:151], v[140:143], v[220:223]\n"
    "v_add_u32_dpp v55, v107, v51 quad_perm:[3,3,3,3] row_mask:0xf bank_mask:0xf\n"
    "v_mfma_f32_16x16x32_bf16 v[220:223], v[180:183], v[172:175], v[220:223]\n"
    "v_mfma_f32_16x16x32_bf16 v[216:219], v[180:183], v[168:171], v[216:219]\n"
    "v_bfrev_b32 v14, v12\n"
    "v_mfma_f32_16x16x32_bf16 v[216:219], v[148:151], v[136:139], v[216:219]\n"
    "v_mfma_f32_16x16x32_bf16 v[212:215], v[148:151], v[132:135], v[212:215]\n"
    "v_lshrrev_b32 v14, 26, v14\n"
    "v_mfma_f32_16x16x32_bf16 v[212:215], v[180:183], v[164:167], v[212:215]\n"
    "v_mfma_f32_16x16x32_bf16 v[208:211], v[180:183], v[160:163], v[208:211]\n"
    "s_add_u32 s40, s22, 3\n"
    "v_mfma_f32_16x16x32_bf16 v[208:211], v[148:151], v[128:131], v[208:211]\n"
    "v_mfma_f32_16x16x32_bf16 v[224:227], v[152:155], v[128:131], v[224:227]\n"
    "s_and_b32 s40, s40, 3\n"
    "v_mfma_f32_16x16x32_bf16 v[224:227], v[184:187], v[160:163], v[224:227]\n"
    "v_mfma_f32_16x16x32_bf16 v[228:231], v[184:187], v[164:167], v[228:231]\n"
    "s_lshl2_add_u32 s40, s40, s22\n"
    "v_mfma_f32_16x16x32_bf16 v[228:231], v[152:155], v[132:135], v[228:231]\n"
    "v_mfma_f32_16x16x32_bf16 v[232:235], v[152:155], v[136:139], v[232:235]\n"
    "s_add_u32 s40, s40, s58\n"
    "v_mfma_f32_16x16x32_bf16 v[232:235], v[184:187], v[168:171], v[232:235]\n"
    "v_mfma_f32_16x16x32_bf16 v[236:239], v[184:187], v[172:175], v[236:239]\n"
    "s_lshl_b32 s40, s40, 2\n"
    "v_mfma_f32_16x16x32_bf16 v[236:239], v[152:155], v[140:143], v[236:239]\n"
    "v_mfma_f32_16x16x32_bf16 v[252:255], v[156:159], v[140:143], v[252:255]\n"
    "s_add_u32 s40, s40, -450048\n"
    "v_mfma_f32_16x16x32_bf16 v[252:255], v[188:191], v[172:175], v[252:255]\n"
    "v_mfma_f32_16x16x32_bf16 v[248:251], v[188:191], v[168:171], v[248:251]\n"
    "v_add_u32 v14, s40, v14\n"
    "v_mfma_f32_16x16x32_bf16 v[248:251], v[156:159], v[136:139], v[248:251]\n"
    "v_mfma_f32_16x16x32_bf16 v[244:247], v[156:159], v[132:135], v[244:247]\n"
    "v_and_b32 v2, 1, v1\n"
    "v_mfma_f32_16x16x32_bf16 v[244:247], v[188:191], v[164:167], v[244:247]\n"
    "v_mfma_f32_16x16x32_bf16 v[240:243], v[188:191], v[160:163], v[240:243]\n"
    "v_mfma_f32_16x16x32_bf16 v[240:243], v[156:159], v[128:131], v[240:243]\n"
    "s_waitcnt vmcnt(6)\n"
    "s_barrier\n"
    "s_add_u32 m0, s35, 98304\n"
    "s_nop 0\n"
    "global_load_lds_dwordx4 v46, s[30:31] offset:0\n"
    "ds_read_b128 v[128:131], v56 offset:0\n"
    "ds_read_b128 v[132:135], v56 offset:2048\n"
    "ds_read_b128 v[136:139], v56 offset:4096\n"
    "ds_read_b128 v[140:143], v56 offset:6144\n"
    "global_load_lds_dwordx4 v47, s[30:31] offset:1024\n"
    "s_bfe_u32 s66, s28, 0x80010\n"
    "s_mul_i32 s66, s66, 53248\n"
    "s_add_u32 s30, s6, s66\n"
    "s_addc_u32 s31, s7, 0\n"
    "ds_read_b64_tr_b16 v[144:145], v76 offset:0\n"
    "ds_read_b64_tr_b16 v[146:147], v76 offset:1024\n"
    "ds_read_b64_tr_b16 v[148:149], v77 offset:0\n"
    "ds_read_b64_tr_b16 v[150:151], v77 offset:1024\n"
    "s_add_u32 m0, s34, 98304\n"
    "s_nop 0\n"
    "global_load_lds_dwordx4 v52, s[36:37] offset:0\n"
    "ds_read_b64_tr_b16 v[152:153], v78 offset:0\n"
    "ds_read_b64_tr_b16 v[154:155], v78 offset:1024\n"
    "ds_read_b64_tr_b16 v[156:157], v79 offset:0\n"
    "ds_read_b64_tr_b16 v[158:159], v79 offset:1024\n"
    "global_load_lds_dwordx4 v53, s[36:37] offset:1024\n"
    "ds_read_b128 v[160:163], v57 offset:0\n"
    "ds_read_b128 v[164:167], v57 offset:2048\n"
    "ds_read_b128 v[168:171], v57 offset:4096\n"
    "ds_read_b128 v[172:175], v57 offset:6144\n"
    "global_load_lds_dwordx4 v54, s[36:37] offset:2048\n"
    "ds_read_b64_tr_b16 v[176:177], v76 offset:8192\n"
    "ds_read_b64_tr_b16 v[178:179], v76 offset:9216\n"
    "ds_read_b64_tr_b16 v[180:181], v77 offset:8192\n"
    "ds_read_b64_tr_b16 v[182:183], v77 offset:9216\n"
    "global_load_lds_dwordx4 v55, s[36:37] offset:3072\n"
    "ds_read_b64_tr_b16 v[184:185], v78 offset:8192\n"
    "ds_read_b64_tr_b16 v[186:187], v78 offset:9216\n"
    "ds_read_b64_tr_b16 v[188:189], v79 offset:8192\n"
    "ds_read_b64_tr_b16 v[190:191], v79 offset:9216\n"
    "s_waitcnt vmcnt(10) lgkmcnt(0)\n"
    "s_barrier\n"
    "v_mfma_f32_16x16x32_bf16 v[192:195], v[144:147], v[128:131], v[192:195]\n"
    "v_lshlrev_b32 v109, 7, v109\n"
    "v_mfma_f32_16x16x32_bf16 v[192:195], v[176:179], v[160:163], v[192:195]\n"
    "v_mfma_f32_16x16x32_bf16 v[196:199], v[176:179], v[164:167], v[196:199]\n"
    "v_add_u32_dpp v52, v108, v48 quad_perm:[0,0,0,0] row_mask:0xf bank_mask:0xf\n"
    "v_mfma_f32_16x16x32_bf16 v[196:199], v[144:147], v[132:135], v[196:199]\n"
    "v_mfma_f32_16x16x32_bf16 v[200:203], v[144:147], v[136:139], v[200:203]\n"
    "v_add_u32_dpp v53, v108, v49 quad_perm:[1,1,1,1] row_mask:0xf bank_mask:0xf\n"
    "v_mfma_f32_16x16x32_bf16 v[200:203], v[176:179], v[168:171], v[200:203]\n"
    "v_mfma_f32_16x16x32_bf16 v[204:207], v[176:179], v[172:175], v[204:207]\n"
    "v_add_u32_dpp v54, v108, v50 quad_perm:[2,2,2,2] row_mask:0xf bank_mask:0xf\n"
    "v_mfma_f32_16x16x32_bf16 v[204:207], v[144:147], v[140:143], v[204:207]\n"
    "v_mfma_f32_16x16x32_bf16 v[220:223], v[148:151], v[140:143], v[220:223]\n"
    "v_add_u32_dpp v55, v108, v51 quad_perm:[3,3,3,3] row_mask:0xf bank_mask:0xf\n"
    "v_mfma_f32_16x16x32_bf16 v[220:223], v[180:183], v[172:175], v[220:223]\n"
    "v_mfma_f32_16x16x32_bf16 v[216:219], v[180:183], v[168:171], v[216:219]\n"
    "v_mul_u32_u24 v2, 0xaaaa, v2\n"
    "v_mfma_f32_16x16x32_bf16 v[216:219], v[148:151], v[136:139], v[216:219]\n"
    "v_mfma_f32_16x16x32_bf16 v[212:215], v[148:151], v[132:135], v[212:215]\n"
    "v_add_u32 v2, 0x4444, v2\n"
    "v_mfma_f32_16x16x32_bf16 v[212:215], v[180:183], v[164:167], v[212:215]\n"
    "v_mfma_f32_16x16x32_bf16 v[208:211], v[180:183], v[160:163], v[208:211]\n"
    "v_mad_u32_u24 v15, v17, 36, v19\n"
    "v_mfma_f32_16x16x32_bf16 v[208:211], v[148:151], v[128:131], v[208:211]\n"
    "v_mfma_f32_16x16x32_bf16 v[224:227], v[152:155], v[128:131], v[224:227]\n"
    "v_mad_u32_u24 v16, v18, 36, v20\n"
    "v_mfma_f32_16x16x32_bf16 v[224:227], v[184:187], v[160:163], v[224:227]\n"
    "v_mfma_f32_16x16x32_bf16 v[228:231], v[184:187], v[164:167], v[228:231]\n"
    "s_lshl4_add_u32 s41, s21, s22\n"
    "v_mfma_f32_16x16x32_bf16 v[228:231], v[152:155], v[132:135], v[228:231]\n"
    "v_mfma_f32_16x16x32_bf16 v[232:235], v[152:155], v[136:139], v[232:235]\n"
    "v_lshrrev_b32 v12, 5, v19\n"
    "v_mfma_f32_16x16x32_bf16 v[232:235], v[184:187], v[168:171], v[232:235]\n"
    "v_mfma_f32_16x16x32_bf16 v[236:239], v[184:187], v[172:175], v[236:239]\n"
    "v_sub_u32 v12, 1, v12\n"
    "v_mfma_f32_16x16x32_bf16 v[236:239], v[152:155], v[140:143], v[236:239]\n"
    "v_mfma_f32_16x16x32_bf16 v[252:255], v[156:159], v[140:143], v[252:255]\n"
    "v_and_b32 v12, 3, v12\n"
    "v_mfma_f32_16x16x32_bf16 v[252:255], v[188:191], v[172:175], v[252:255]\n"
    "v_mfma_f32_16x16x32_bf16 v[248:251], v[188:191], v[168:171], v[248:251]\n"
    "v_add_lshl_u32 v12, v12, s41, 12\n"
    "v_mfma_f32_16x16x32_bf16 v[248:251], v[156:159], v[136:139], v[248:251]\n"
    "v_mfma_f32_16x16x32_bf16 v[244:247], v[156:159], v[132:135], v[244:247]\n"
    "v_add_u32 v15, v12, v15\n"
    "v_mfma_f32_16x16x32_bf16 v[244:247], v[188:191], v[164:167], v[244:247]\n"
    "v_mfma_f32_16x16x32_bf16 v[240:243], v[188:191], v[160:163], v[240:243]\n"
    "v_mfma_f32_16x16x32_bf16 v[240:243], v[156:159], v[128:131], v[240:243]\n"
    "s_waitcnt vmcnt(6)\n"
    "s_barrier\n"
    "s_add_u32 m0, s35, 0\n"
    "s_nop 0\n"
    "global_load_lds_dwordx4 v46, s[30:31] offset:0\n"
    "ds_read_b128 v[128:131], v56 offset:49152\n"
    "ds_read_b128 v[132:135], v56 offset:51200\n"
    "ds_read_b128 v[136:139], v56 offset:53248\n"
    "ds_read_b128 v[140:143], v56 offset:55296\n"
    "global_load_lds_dwordx4 v47, s[30:31] offset:1024\n"
    "s_add_u32 s30, s30, 212992\n"
    "s_addc_u32 s31, s31, 0\n"
    "ds_read_b64_tr_b16 v[144:145], v76 offset:49152\n"
    "ds_read_b64_tr_b16 v[146:147], v76 offset:50176\n"
    "ds_read_b64_tr_b16 v[148:149], v77 offset:49152\n"
    "ds_read_b64_tr_b16 v[150:151], v77 offset:50176\n"
    "s_add_u32 m0, s34, 0\n"
    "s_nop 0\n"
    "global_load_lds_dwordx4 v52, s[36:37] offset:0\n"
    "ds_read_b64_tr_b16 v[152:153], v78 offset:49152\n"
    "ds_read_b64_tr_b16 v[154:155], v78 offset:50176\n"
    "ds_read_b64_tr_b16 v[156:157], v79 offset:49152\n"
    "ds_read_b64_tr_b16 v[158:159], v79 offset:50176\n"
    "global_load_lds_dwordx4 v53, s[36:37] offset:1024\n"
    "ds_read_b128 v[160:163], v57 offset:49152\n"
    "ds_read_b128 v[164:167], v57 offset:51200\n"
    "ds_read_b128 v[168:171], v57 offset:53248\n"
    "ds_read_b128 v[172:175], v57 offset:55296\n"
    "global_load_lds_dwordx4 v54, s[36:37] offset:2048\n"
    "ds_read_b64_tr_b16 v[176:177], v76 offset:57344\n"
    "ds_read_b64_tr_b16 v[178:179], v76 offset:58368\n"
    "ds_read_b64_tr_b16 v[180:181], v77 offset:57344\n"
    "ds_read_b64_tr_b16 v[182:183], v77 offset:58368\n"
    "global_load_lds_dwordx4 v55, s[36:37] offset:3072\n"
    "ds_read_b64_tr_b16 v[184:185], v78 offset:57344\n"
    "ds_read_b64_tr_b16 v[186:187], v78 offset:58368\n"
    "ds_read_b64_tr_b16 v[188:189], v79 offset:57344\n"
    "ds_read_b64_tr_b16 v[190:191], v79 offset:58368\n"
    "s_waitcnt vmcnt(10) lgkmcnt(0)\n"
    "s_barrier\n"
    "v_mfma_f32_16x16x32_bf16 v[192:195], v[144:147], v[128:131], v[192:195]\n"
    "v_lshlrev_b32 v110, 7, v110\n"
    "v_mfma_f32_16x16x32_bf16 v[192:195], v[176:179], v[160:163], v[192:195]\n"
    "v_add_u32_dpp v52, v109, v48 quad_perm:[0,0,0,0] row_mask:0xf bank_mask:0xf\n"
    "v_mfma_f32_16x16x32_bf16 v[196:199], v[176:179], v[164:167], v[196:199]\n"
    "v_mfma_f32_16x16x32_bf16 v[196:199], v[144:147], v[132:135], v[196:199]\n"
    "v_add_u32_dpp v53, v109, v49 quad_perm:[1,1,1,1] row_mask:0xf bank_mask:0xf\n"
    "v_mfma_f32_16x16x32_bf16 v[200:203], v[144:147], v[136:139], v[200:203]\n"
    "v_mfma_f32_16x16x32_bf16 v[200:203], v[176:179], v[168:171], v[200:203]\n"
    "v_add_u32_dpp v54, v109, v50 quad_perm:[2,2,2,2] row_mask:0xf bank_mask:0xf\n"
    "v_mfma_f32_16x16x32_bf16 v[204:207], v[176:179], v[172:175], v[204:207]\n"
    "v_mfma_f32_16x16x32_bf16 v[204:207], v[144:147], v[140:143], v[204:207]\n"
    "v_add_u32_dpp v55, v109, v51 quad_perm:[3,3,3,3] row_mask:0xf bank_mask:0xf\n"
    "v_mfma_f32_16x16x32_bf16 v[220:223], v[148:151], v[140:143], v[220:223]\n"
    "v_mfma_f32_16x16x32_bf16 v[220:223], v[180:183], v[172:175], v[220:223]\n"
    "v_lshrrev_b32 v12, 5, v20\n"
    "v_mfma_f32_16x16x32_bf16 v[216:219], v[180:183], v[168:171], v[216:219]\n"
    "v_mfma_f32_16x16x32_bf16 v[216:219], v[148:151], v[136:139], v[216:219]\n"
    "v_sub_u32 v12, 1, v12\n"
    "v_mfma_f32_16x16x32_bf16 v[212:215], v[148:151], v[132:135], v[212:215]\n"
    "v_mfma_f32_16x16x32_bf16 v[212:215], v[180:183], v[164:167], v[212:215]\n"
    "v_and_b32 v12, 3, v12\n"
    "v_mfma_f32_16x16x32_bf16 v[208:211], v[180:183], v[160:163], v[208:211]\n"
    "v_mfma_f32_16x16x32_bf16 v[208:211], v[148:151], v[128:131], v[208:211]\n"
    "v_add_lshl_u32 v12, v12, s41, 12\n"
    "v_mfma_f32_16x16x32_bf16 v[224:227], v[152:155], v[128:131], v[224:227]\n"
    "v_mfma_f32_16x16x32_bf16 v[224:227], v[184:187], v[160:163], v[224:227]\n"
    "v_add_u32 v16, v12, v16\n"
    "v_mfma_f32_16x16x32_bf16 v[228:231], v[184:187], v[164:167], v[228:231]\n"
    "v_mfma_f32_16x16x32_bf16 v[228:231], v[152:155], v[132:135], v[228:231]\n"
    "s_mul_i32 s41, s47, 13\n"
    "v_mfma_f32_16x16x32_bf16 v[232:235], v[152:155], v[136:139], v[232:235]\n"
    "v_mfma_f32_16x16x32_bf16 v[232:235], v[184:187], v[168:171], v[232:235]\n"
    "s_sub_u32 s41, s3, s41\n"
    "v_mfma_f32_16x16x32_bf16 v[236:239], v[184:187], v[172:175], v[236:239]\n"
    "v_mfma_f32_16x16x32_bf16 v[236:239], v[152:155], v[140:143], v[236:239]\n"
    "s_lshl1_add_u32 s41, s41, s21\n"
    "v_mfma_f32_16x16x32_bf16 v[252:255], v[156:159], v[140:143], v[252:255]\n"
    "v_mfma_f32_16x16x32_bf16 v[252:255], v[188:191], v[172:175], v[252:255]\n"
    "s_mul_i32 s41, s41, 4\n"
    "v_mfma_f32_16x16x32_bf16 v[248:251], v[188:191], v[168:171], v[248:251]\n"
    "v_mfma_f32_16x16x32_bf16 v[248:251], v[156:159], v[136:139], v[248:251]\n"
    "s_add_u32 s41, s41, s22\n"
    "v_mfma_f32_16x16x32_bf16 v[244:247], v[156:159], v[132:135], v[244:247]\n"
    "v_mfma_f32_16x16x32_bf16 v[244:247], v[188:191], v[164:167], v[244:247]\n"
    "v_mov_b32 v24, s41\n"
    "v_mfma_f32_16x16x32_bf16 v[240:243], v[188:191], v[160:163], v[240:243]\n"
    "v_mfma_f32_16x16x32_bf16 v[240:243], v[156:159], v[128:131], v[240:243]\n"
    "s_waitcnt vmcnt(6)\n"
    "s_barrier\n"
    "s_add_u32 m0, s35, 49152\n"
    "s_nop 0\n"
    "global_load_lds_dwordx4 v46, s[30:31] offset:0\n"
    "ds_read_b128 v[128:131], v58 offset:0\n"
    "ds_read_b128 v[132:135], v58 offset:2048\n"
    "ds_read_b128 v[136:139], v58 offset:4096\n"
    "ds_read_b128 v[140:143], v58 offset:6144\n"
    "global_load_lds_dwordx4 v47, s[30:31] offset:1024\n"
    "s_add_u32 s30, s30, 212992\n"
    "s_addc_u32 s31, s31, 0\n"
    "ds_read_b64_tr_b16 v[144:145], v80 offset:0\n"
    "ds_read_b64_tr_b16 v[146:147], v80 offset:1024\n"
    "ds_read_b64_tr_b16 v[148:149], v81 offset:0\n"
    "ds_read_b64_tr_b16 v[150:151], v81 offset:1024\n"
    "s_add_u32 m0, s34, 49152\n"
    "s_nop 0\n"
    "global_load_lds_dwordx4 v52, s[36:37] offset:0\n"
    "ds_read_b64_tr_b16 v[152:153], v82 offset:0\n"
    "ds_read_b64_tr_b16 v[154:155], v82 offset:1024\n"
    "ds_read_b64_tr_b16 v[156:157], v83 offset:0\n"
    "ds_read_b64_tr_b16 v[158:159], v83 offset:1024\n"
    "global_load_lds_dwordx4 v53, s[36:37] offset:1024\n"
    "ds_read_b128 v[160:163], v59 offset:0\n"
    "ds_read_b128 v[164:167], v59 offset:2048\n"
    "ds_read_b128 v[168:171], v59 offset:4096\n"
    "ds_read_b128 v[172:175], v59 offset:6144\n"
    "global_load_lds_dwordx4 v54, s[36:37] offset:2048\n"
    "ds_read_b64_tr_b16 v[176:177], v80 offset:8192\n"
    "ds_read_b64_tr_b16 v[178:179], v80 offset:9216\n"
    "ds_read_b64_tr_b16 v[180:181], v81 offset:8192\n"
    "ds_read_b64_tr_b16 v[182:183], v81 offset:9216\n"
    "global_load_lds_dwordx4 v55, s[36:37] offset:3072\n"
    "ds_read_b64_tr_b16 v[184:185], v82 offset:8192\n"
    "ds_read_b64_tr_b16 v[186:187], v82 offset:9216\n"
    "ds_read_b64_tr_b16 v[188:189], v83 offset:8192\n"
    "ds_read_b64_tr_b16 v[190:191], v83 offset:9216\n"
    "s_waitcnt vmcnt(10) lgkmcnt(0)\n"
    "s_barrier\n"
    "v_mfma_f32_16x16x32_bf16 v[192:195], v[144:147], v[128:131], v[192:195]\n"
    "v_lshlrev_b32 v111, 7, v111\n"
    "v_mfma_f32_16x16x32_bf16 v[192:195], v[176:179], v[160:163], v[192:195]\n"
    "v_mfma_f32_16x16x32_bf16 v[196:199], v[176:179], v[164:167], v[196:199]\n"
    "v_add_u32_dpp v52, v110, v48 quad_perm:[0,0,0,0] row_mask:0xf bank_mask:0xf\n"
    "v_mfma_f32_16x16x32_bf16 v[196:199], v[144:147], v[132:135], v[196:199]\n"
    "v_mfma_f32_16x16x32_bf16 v[200:203], v[144:147], v[136:139], v[200:203]\n"
    "v_add_u32_dpp v53, v110, v49 quad_perm:[1,1,1,1] row_mask:0xf bank_mask:0xf\n"
    "v_mfma_f32_16x16x32_bf16 v[200:203], v[176:179], v[168:171], v[200:203]\n"
    "v_mfma_f32_16x16x32_bf16 v[204:207], v[176:179], v[172:175], v[204:207]\n"
    "v_add_u32_dpp v54, v110, v50 quad_perm:[2,2,2,2] row_mask:0xf bank_mask:0xf\n"
    "v_mfma_f32_16x16x32_bf16 v[204:207], v[144:147], v[140:143], v[204:207]\n"
    "v_mfma_f32_16x16x32_bf16 v[220:223], v[148:151], v[140:143], v[220:223]\n"
    "v_add_u32_dpp v55, v110, v51 quad_perm:[3,3,3,3] row_mask:0xf bank_mask:0xf\n"
    "v_mfma_f32_16x16x32_bf16 v[220:223], v[180:183], v[172:175], v[220:223]\n"
    "v_mfma_f32_16x16x32_bf16 v[216:219], v[180:183], v[168:171], v[216:219]\n"
    "v_mfma_f32_16x16x32_bf16 v[216:219], v[148:151], v[136:139], v[216:219]\n"
    "v_readfirstlane_b32 s40, v24\n"
    "v_mfma_f32_16x16x32_bf16 v[212:215], v[148:151], v[132:135], v[212:215]\n"
    "v_mfma_f32_16x16x32_bf16 v[212:215], v[180:183], v[164:167], v[212:215]\n"
    "s_or_b32 s40, s40, s44\n"
    "v_mfma_f32_16x16x32_bf16 v[208:211], v[180:183], v[160:163], v[208:211]\n"
    "v_mfma_f32_16x16x32_bf16 v[208:211], v[148:151], v[128:131], v[208:211]\n"
    "s_lshl1_add_u32 s41, s40, s54\n"
    "v_mfma_f32_16x16x32_bf16 v[224:227], v[152:155], v[128:131], v[224:227]\n"
    "v_mfma_f32_16x16x32_bf16 v[224:227], v[184:187], v[160:163], v[224:227]\n"
    "s_cmpk_lt_u32 s40, 256\n"
    "v_mfma_f32_16x16x32_bf16 v[228:231], v[184:187], v[164:167], v[228:231]\n"
    "v_mfma_f32_16x16x32_bf16 v[228:231], v[152:155], v[132:135], v[228:231]\n"
    "s_cselect_b32 s41, s41, 2\n"
    "v_mfma_f32_16x16x32_bf16 v[232:235], v[152:155], v[136:139], v[232:235]\n"
    "v_mfma_f32_16x16x32_bf16 v[232:235], v[184:187], v[168:171], v[232:235]\n"
    "v_mfma_f32_16x16x32_bf16 v[236:239], v[184:187], v[172:175], v[236:239]\n"
    "v_mov_b32 v24, s41\n"
    "v_mfma_f32_16x16x32_bf16 v[236:239], v[152:155], v[140:143], v[236:239]\n"
    "v_mfma_f32_16x16x32_bf16 v[252:255], v[156:159], v[140:143], v[252:255]\n"
    "s_mul_i32 s41, s41, 52\n"
    "v_mfma_f32_16x16x32_bf16 v[252:255], v[188:191], v[172:175], v[252:255]\n"
    "v_mfma_f32_16x16x32_bf16 v[248:251], v[188:191], v[168:171], v[248:251]\n"
    "s_add_u32 s38, s4, s41\n"
    "v_mfma_f32_16x16x32_bf16 v[248:251], v[156:159], v[136:139], v[248:251]\n"
    "v_mfma_f32_16x16x32_bf16 v[244:247], v[156:159], v[132:135], v[244:247]\n"
    "s_addc_u32 s39, s5, 0\n"
    "v_mfma_f32_16x16x32_bf16 v[244:247], v[188:191], v[164:167], v[244:247]\n"
    "v_mfma_f32_16x16x32_bf16 v[240:243], v[188:191], v[160:163], v[240:243]\n"
    "v_mfma_f32_16x16x32_bf16 v[240:243], v[156:159], v[128:131], v[240:243]\n"
    "s_waitcnt vmcnt(6)\n"
    "s_barrier\n"
    "s_add_u32 m0, s35, 98304\n"
    "s_nop 0\n"
    "global_load_lds_dwordx4 v46, s[30:31] offset:0\n"
    "ds_read_b128 v[128:131], v56 offset:0\n"
    "ds_read_b128 v[132:135], v56 offset:2048\n"
    "ds_read_b128 v[136:139], v56 offset:4096\n"
    "global_load_lds_dwordx4 v47, s[30:31] offset:1024\n"
    "s_add_u32 s30, s30, 212992\n"
    "s_addc_u32 s31, s31, 0\n"
    "ds_read_b128 v[140:143], v56 offset:6144\n"
    "ds_read_b64_tr_b16 v[144:145], v76 offset:0\n"
    "ds_read_b64_tr_b16 v[146:147], v76 offset:1024\n"
    "s_add_u32 m0, s34, 98304\n"
    "s_nop 0\n"
    "global_load_lds_dwordx4 v52, s[36:37] offset:0\n"
    "ds_read_b64_tr_b16 v[148:149], v77 offset:0\n"
    "ds_read_b64_tr_b16 v[150:151], v77 offset:1024\n"
    "ds_read_b64_tr_b16 v[152:153], v78 offset:0\n"
    "global_load_lds_dwordx4 v53, s[36:37] offset:1024\n"
    "ds_read_b64_tr_b16 v[154:155], v78 offset:1024\n"
    "ds_read_b64_tr_b16 v[156:157], v79 offset:0\n"
    "ds_read_b64_tr_b16 v[158:159], v79 offset:1024\n"
    "global_load_lds_dwordx4 v54, s[36:37] offset:2048\n"
    "ds_read_b128 v[160:163], v57 offset:0\n"
    "ds_read_b128 v[164:167], v57 offset:2048\n"
    "ds_read_b128 v[168:171], v57 offset:4096\n"
    "global_load_lds_dwordx4 v55, s[36:37] offset:3072\n"
    "ds_read_b128 v[172:175], v57 offset:6144\n"
    "ds_read_b64_tr_b16 v[176:177], v76 offset:8192\n"
    "ds_read_b64_tr_b16 v[178:179], v76 offset:9216\n"
    "global_load_dwordx3 v[36:38], v17, s[38:39] offset:-104\n"
    "ds_read_b64_tr_b16 v[180:181], v77 offset:8192\n"
    "ds_read_b64_tr_b16 v[182:183], v77 offset:9216\n"
    "ds_read_b64_tr_b16 v[184:185], v78 offset:8192\n"
    "ds_read_b64_tr_b16 v[186:187], v78 offset:9216\n"
    "ds_read_b64_tr_b16 v[188:189], v79 offset:8192\n"
    "ds_read_b64_tr_b16 v[190:191], v79 offset:9216\n"
    "ds_read_b64 v[84:85], v42 offset:0\n"
    "ds_read_b64 v[86:87], v42 offset:32\n"
    "ds_read_b64 v[88:89], v42 offset:64\n"
    "ds_read_b64 v[90:91], v42 offset:96\n"
    "s_waitcnt vmcnt(11) lgkmcnt(0)\n"
    "s_barrier\n"
    "v_mfma_f32_16x16x32_bf16 v[192:195], v[144:147], v[128:131], v[192:195]\n"
    "v_lshlrev_b32 v112, 7, v112\n"
    "v_mfma_f32_16x16x32_bf16 v[192:195], v[176:179], v[160:163], v[192:195]\n"
    "v_add_u32_dpp v52, v111, v48 quad_perm:[0,0,0,0] row_mask:0xf bank_mask:0xf\n"
    "v_mfma_f32_16x16x32_bf16 v[196:199], v[176:179], v[164:167], v[196:199]\n"
    "v_add_u32_dpp v53, v111, v49 quad_perm:[1,1,1,1] row_mask:0xf bank_mask:0xf\n"
    "v_mfma_f32_16x16x32_bf16 v[196:199], v[144:147], v[132:135], v[196:199]\n"
    "v_add_u32_dpp v54, v111, v50 quad_perm:[2,2,2,2] row_mask:0xf bank_mask:0xf\n"
    "v_mfma_f32_16x16x32_bf16 v[200:203], v[144:147], v[136:139], v[200:203]\n"
    "v_add_u32_dpp v55, v111, v51 quad_perm:[3,3,3,3] row_mask:0xf bank_mask:0xf\n"
    "v_mfma_f32_16x16x32_bf16 v[200:203], v[176:179], v[168:171], v[200:203]\n"
    "v_lshlrev_b32 v60, 16, v84\n"
    "v_mfma_f32_16x16x32_bf16 v[204:207], v[176:179], v[172:175], v[204:207]\n"
    "v_and_b32 v61, 0xffff0000, v84\n"
    "v_mfma_f32_16x16x32_bf16 v[204:207], v[144:147], v[140:143], v[204:207]\n"
    "v_mfma_f32_16x16x32_bf16 v[220:223], v[148:151], v[140:143], v[220:223]\n"
    "v_lshlrev_b32 v62, 16, v85\n"
    "v_mfma_f32_16x16x32_bf16 v[220:223], v[180:183], v[172:175], v[220:223]\n"
    "v_and_b32 v63, 0xffff0000, v85\n"
    "v_mfma_f32_16x16x32_bf16 v[216:219], v[180:183], v[168:171], v[216:219]\n"
    "v_lshlrev_b32 v64, 16, v86\n"
    "v_mfma_f32_16x16x32_bf16 v[216:219], v[148:151], v[136:139], v[216:219]\n"
    "v_and_b32 v65, 0xffff0000, v86\n"
    "v_mfma_f32_16x16x32_bf16 v[212:215], v[148:151], v[132:135], v[212:215]\n"
    "v_lshlrev_b32 v66, 16, v87\n"
    "v_mfma_f32_16x16x32_bf16 v[212:215], v[180:183], v[164:167], v[212:215]\n"
    "v_and_b32 v67, 0xffff0000, v87\n"
    "v_mfma_f32_16x16x32_bf16 v[208:211], v[180:183], v[160:163], v[208:211]\n"
    "v_lshlrev_b32 v68, 16, v88\n"
    "v_mfma_f32_16x16x32_bf16 v[208:211], v[148:151], v[128:131], v[208:211]\n"
    "v_mfma_f32_16x16x32_bf16 v[224:227], v[152:155], v[128:131], v[224:227]\n"
    "v_and_b32 v69, 0xffff0000, v88\n"
    "v_mfma_f32_16x16x32_bf16 v[224:227], v[184:187], v[160:163], v[224:227]\n"
    "v_lshlrev_b32 v70, 16, v89\n"
    "v_mfma_f32_16x16x32_bf16 v[228:231], v[184:187], v[164:167], v[228:231]\n"
    "v_and_b32 v71, 0xffff0000, v89\n"
    "v_mfma_f32_16x16x32_bf16 v[228:231], v[152:155], v[132:135], v[228:231]\n"
    "v_lshlrev_b32 v72, 16, v90\n"
    "v_mfma_f32_16x16x32_bf16 v[232:235], v[152:155], v[136:139], v[232:235]\n"
    "v_and_b32 v73, 0xffff0000, v90\n"
    "v_mfma_f32_16x16x32_bf16 v[232:235], v[184:187], v[168:171], v[232:235]\n"
    "v_lshlrev_b32 v74, 16, v91\n"
    "v_mfma_f32_16x16x32_bf16 v[236:239], v[184:187], v[172:175], v[236:239]\n"
    "v_and_b32 v75, 0xffff0000, v91\n"
    "v_mfma_f32_16x16x32_bf16 v[236:239], v[152:155], v[140:143], v[236:239]\n"
    "v_mfma_f32_16x16x32_bf16 v[252:255], v[156:159], v[140:143], v[252:255]\n"
    "s_lshl3_add_u32 s40, s2, 4096\n"
    "v_mfma_f32_16x16x32_bf16 v[252:255], v[188:191], v[172:175], v[252:255]\n"
    "s_lshl_b32 s41, s2, 2\n"
    "v_mfma_f32_16x16x32_bf16 v[248:251], v[188:191], v[168:171], v[248:251]\n"
    "v_lshlrev_b32 v11, 3, v1\n"
    "v_mfma_f32_16x16x32_bf16 v[248:251], v[156:159], v[136:139], v[248:251]\n"
    "v_add_u32 v11, 40960, v11\n"
    "v_mfma_f32_16x16x32_bf16 v[244:247], v[156:159], v[132:135], v[244:247]\n"
    "v_writelane_b32 v11, s40, 8\n"
    "v_mfma_f32_16x16x32_bf16 v[244:247], v[188:191], v[164:167], v[244:247]\n"
    "v_writelane_b32 v11, s41, 9\n"
    "v_mfma_f32_16x16x32_bf16 v[240:243], v[188:191], v[160:163], v[240:243]\n"
    "v_mfma_f32_16x16x32_bf16 v[240:243], v[156:159], v[128:131], v[240:243]\n"
    "s_waitcnt vmcnt(7)\n"
    "s_barrier\n"
    "s_add_u32 m0, s35, 0\n"
    "s_nop 0\n"
    "global_load_lds_dwordx4 v46, s[30:31] offset:0\n"
    "ds_read_b128 v[128:131], v56 offset:49152\n"
    "ds_read_b128 v[132:135], v56 offset:51200\n"
    "ds_read_b128 v[136:139], v56 offset:53248\n"
    "global_load_lds_dwordx4 v47, s[30:31] offset:1024\n"
    "s_bfe_u32 s66, s28, 0x80018\n"
    "s_mul_i32 s66, s66, 53248\n"
    "s_add_u32 s30, s6, s66\n"
    "s_addc_u32 s31, s7, 0\n"
    "ds_read_b128 v[140:143], v56 offset:55296\n"
    "ds_read_b64_tr_b16 v[144:145], v76 offset:49152\n"
    "ds_read_b64_tr_b16 v[146:147], v76 offset:50176\n"
    "s_add_u32 m0, s34, 0\n"
    "s_nop 0\n"
    "global_load_lds_dwordx4 v52, s[36:37] offset:0\n"
    "ds_read_b64_tr_b16 v[148:149], v77 offset:49152\n"
    "ds_read_b64_tr_b16 v[150:151], v77 offset:50176\n"
    "ds_read_b64_tr_b16 v[152:153], v78 offset:49152\n"
    "global_load_lds_dwordx4 v53, s[36:37] offset:1024\n"
    "ds_read_b64_tr_b16 v[154:155], v78 offset:50176\n"
    "ds_read_b64_tr_b16 v[156:157], v79 offset:49152\n"
    "ds_read_b64_tr_b16 v[158:159], v79 offset:50176\n"
    "global_load_lds_dwordx4 v54, s[36:37] offset:2048\n"
    "ds_read_b128 v[160:163], v57 offset:49152\n"
    "ds_read_b128 v[164:167], v57 offset:51200\n"
    "ds_read_b128 v[168:171], v57 offset:53248\n"
    "global_load_lds_dwordx4 v55, s[36:37] offset:3072\n"
    "ds_read_b128 v[172:175], v57 offset:55296\n"
    "ds_read_b64_tr_b16 v[176:177], v76 offset:57344\n"
    "ds_read_b64_tr_b16 v[178:179], v76 offset:58368\n"
    "global_load_dword v41, v18, s[38:39] offset:-8\n"
    "ds_read_b64_tr_b16 v[180:181], v77 offset:57344\n"
    "ds_read_b64_tr_b16 v[182:183], v77 offset:58368\n"
    "ds_read_b64_tr_b16 v[184:185], v78 offset:57344\n"
    "ds_read_b64_tr_b16 v[186:187], v78 offset:58368\n"
    "ds_read_b64_tr_b16 v[188:189], v79 offset:57344\n"
    "ds_read_b64_tr_b16 v[190:191], v79 offset:58368\n"
    "s_waitcnt vmcnt(12) lgkmcnt(0)\n"
    "s_barrier\n"
    "v_mfma_f32_16x16x32_bf16 v[192:195], v[144:147], v[128:131], v[192:195]\n"
    "v_mfma_f32_16x16x32_bf16 v[192:195], v[176:179], v[160:163], v[192:195]\n"
    "v_lshlrev_b32 v113, 7, v113\n"
    "v_mfma_f32_16x16x32_bf16 v[196:199], v[176:179], v[164:167], v[196:199]\n"
    "v_mfma_f32_16x16x32_bf16 v[196:199], v[144:147], v[132:135], v[196:199]\n"
    "v_add_u32_dpp v52, v112, v48 quad_perm:[0,0,0,0] row_mask:0xf bank_mask:0xf\n"
    "v_mfma_f32_16x16x32_bf16 v[200:203], v[144:147], v[136:139], v[200:203]\n"
    "v_mfma_f32_16x16x32_bf16 v[200:203], v[176:179], v[168:171], v[200:203]\n"
    "v_add_u32_dpp v53, v112, v49 quad_perm:[1,1,1,1] row_mask:0xf bank_mask:0xf\n"
    "v_mfma_f32_16x16x32_bf16 v[204:207], v[176:179], v[172:175], v[204:207]\n"
    "v_mfma_f32_16x16x32_bf16 v[204:207], v[144:147], v[140:143], v[204:207]\n"
    "v_add_u32_dpp v54, v112, v50 quad_perm:[2,2,2,2] row_mask:0xf bank_mask:0xf\n"
    "v_mfma_f32_16x16x32_bf16 v[220:223], v[148:151], v[140:143], v[220:223]\n"
    "v_mfma_f32_16x16x32_bf16 v[220:223], v[180:183], v[172:175], v[220:223]\n"
    "v_add_u32_dpp v55, v112, v51 quad_perm:[3,3,3,3] row_mask:0xf bank_mask:0xf\n"
    "v_mfma_f32_16x16x32_bf16 v[216:219], v[180:183], v[168:171], v[216:219]\n"
    "v_mfma_f32_16x16x32_bf16 v[216:219], v[148:151], v[136:139], v[216:219]\n"
    "v_mfma_f32_16x16x32_bf16 v[212:215], v[148:151], v[132:135], v[212:215]\n"
    "v_mfma_f32_16x16x32_bf16 v[212:215], v[180:183], v[164:167], v[212:215]\n"
    "v_mfma_f32_16x16x32_bf16 v[208:211], v[180:183], v[160:163], v[208:211]\n"
    "v_add_f32 v192, v192, v60\n"
    "v_mfma_f32_16x16x32_bf16 v[208:211], v[148:151], v[128:131], v[208:211]\n"
    "v_add_f32 v193, v193, v61\n"
    "v_mfma_f32_16x16x32_bf16 v[224:227], v[152:155], v[128:131], v[224:227]\n"
    "v_add_f32 v194, v194, v62\n"
    "v_mfma_f32_16x16x32_bf16 v[224:227], v[184:187], v[160:163], v[224:227]\n"
    "v_add_f32 v195, v195, v63\n"
    "v_mfma_f32_16x16x32_bf16 v[228:231], v[184:187], v[164:167], v[228:231]\n"
    "v_add_f32 v196, v196, v60\n"
    "v_mfma_f32_16x16x32_bf16 v[228:231], v[152:155], v[132:135], v[228:231]\n"
    "v_add_f32 v197, v197, v61\n"
    "v_mfma_f32_16x16x32_bf16 v[232:235], v[152:155], v[136:139], v[232:235]\n"
    "v_add_f32 v198, v198, v62\n"
    "v_mfma_f32_16x16x32_bf16 v[232:235], v[184:187], v[168:171], v[232:235]\n"
    "v_add_f32 v199, v199, v63\n"
    "v_mfma_f32_16x16x32_bf16 v[236:239], v[184:187], v[172:175], v[236:239]\n"
    "v_add_f32 v200, v200, v60\n"
    "v_mfma_f32_16x16x32_bf16 v[236:239], v[152:155], v[140:143], v[236:239]\n"
    "v_add_f32 v201, v201, v61\n"
    "v_mfma_f32_16x16x32_bf16 v[252:255], v[156:159], v[140:143], v[252:255]\n"
    "v_add_f32 v202, v202, v62\n"
    "v_mfma_f32_16x16x32_bf16 v[252:255], v[188:191], v[172:175], v[252:255]\n"
    "v_add_f32 v203, v203, v63\n"
    "v_mfma_f32_16x16x32_bf16 v[248:251], v[188:191], v[168:171], v[248:251]\n"
    "v_add_f32 v204, v204, v60\n"
    "v_mfma_f32_16x16x32_bf16 v[248:251], v[156:159], v[136:139], v[248:251]\n"
    "v_add_f32 v205, v205, v61\n"
    "v_mfma_f32_16x16x32_bf16 v[244:247], v[156:159], v[132:135], v[244:247]\n"
    "v_add_f32 v206, v206, v62\n"
    "v_mfma_f32_16x16x32_bf16 v[244:247], v[188:191], v[164:167], v[244:247]\n"
    "v_add_f32 v207, v207, v63\n"
    "v_mfma_f32_16x16x32_bf16 v[240:243], v[188:191], v[160:163], v[240:243]\n"
    "v_mfma_f32_16x16x32_bf16 v[240:243], v[156:159], v[128:131], v[240:243]\n"
    "s_waitcnt vmcnt(8)\n"
    "s_barrier\n"
    "s_add_u32 m0, s35, 49152\n"
    "s_nop 0\n"
    "global_load_lds_dwordx4 v46, s[30:31] offset:0\n"
    "ds_read_b128 v[128:131], v58 offset:0\n"
    "ds_read_b128 v[132:135], v58 offset:2048\n"
    "ds_read_b128 v[136:139], v58 offset:4096\n"
    "global_load_lds_dwordx4 v47, s[30:31] offset:1024\n"
    "s_add_u32 s30, s30, 212992\n"
    "s_addc_u32 s31, s31, 0\n"
    "ds_read_b128 v[140:143], v58 offset:6144\n"
    "ds_read_b64_tr_b16 v[144:145], v80 offset:0\n"
    "ds_read_b64_tr_b16 v[146:147], v80 offset:1024\n"
    "s_add_u32 m0, s34, 49152\n"
    "s_nop 0\n"
    "global_load_lds_dwordx4 v52, s[36:37] offset:0\n"
    "ds_read_b64_tr_b16 v[148:149], v81 offset:0\n"
    "ds_read_b64_tr_b16 v[150:151], v81 offset:1024\n"
    "ds_read_b64_tr_b16 v[152:153], v82 offset:0\n"
    "global_load_lds_dwordx4 v53, s[36:37] offset:1024\n"
    "ds_read_b64_tr_b16 v[154:155], v82 offset:1024\n"
    "ds_read_b64_tr_b16 v[156:157], v83 offset:0\n"
    "ds_read_b64_tr_b16 v[158:159], v83 offset:1024\n"
    "global_load_lds_dwordx4 v54, s[36:37] offset:2048\n"
    "ds_read_b128 v[160:163], v59 offset:0\n"
    "ds_read_b128 v[164:167], v59 offset:2048\n"
    "ds_read_b128 v[168:171], v59 offset:4096\n"
    "global_load_lds_dwordx4 v55, s[36:37] offset:3072\n"
    "ds_read_b128 v[172:175], v59 offset:6144\n"
    "ds_read_b64_tr_b16 v[176:177], v80 offset:8192\n"
    "ds_read_b64_tr_b16 v[178:179], v80 offset:9216\n"
    "s_mov_b32 m0, s64\n"
    "s_nop 0\n"
    "global_load_lds_dword v43, s[18:19]\n"
    "ds_read_b64_tr_b16 v[180:181], v81 offset:8192\n"
    "ds_read_b64_tr_b16 v[182:183], v81 offset:9216\n"
    "ds_read_b64_tr_b16 v[184:185], v82 offset:8192\n"
    "ds_read_b64_tr_b16 v[186:187], v82 offset:9216\n"
    "ds_read_b64_tr_b16 v[188:189], v83 offset:8192\n"
    "ds_read_b64_tr_b16 v[190:191], v83 offset:9216\n"
    "s_waitcnt vmcnt(12) lgkmcnt(0)\n"
    "s_barrier\n"
    "v_mfma_f32_16x16x32_bf16 v[192:195], v[144:147], v[128:131], v[192:195]\n"
    "v_mfma_f32_16x16x32_bf16 v[192:195], v[176:179], v[160:163], v[192:195]\n"
    "v_lshlrev_b32 v114, 7, v114\n"
    "v_mfma_f32_16x16x32_bf16 v[196:199], v[176:179], v[164:167], v[196:199]\n"
    "v_mfma_f32_16x16x32_bf16 v[196:199], v[144:147], v[132:135], v[196:199]\n"
    "v_add_u32_dpp v52, v113, v48 quad_perm:[0,0,0,0] row_mask:0xf bank_mask:0xf\n"
    "v_mfma_f32_16x16x32_bf16 v[200:203], v[144:147], v[136:139], v[200:203]\n"
    "v_mfma_f32_16x16x32_bf16 v[200:203], v[176:179], v[168:171], v[200:203]\n"
    "v_add_u32_dpp v53, v113, v49 quad_perm:[1,1,1,1] row_mask:0xf bank_mask:0xf\n"
    "v_mfma_f32_16x16x32_bf16 v[204:207], v[176:179], v[172:175], v[204:207]\n"
    "v_mfma_f32_16x16x32_bf16 v[204:207], v[144:147], v[140:143], v[204:207]\n"
    "v_add_u32_dpp v54, v113, v50 quad_perm:[2,2,2,2] row_mask:0xf bank_mask:0xf\n"
    "v_mfma_f32_16x16x32_bf16 v[220:223], v[148:151], v[140:143], v[220:223]\n"
    "v_mfma_f32_16x16x32_bf16 v[220:223], v[180:183], v[172:175], v[220:223]\n"
    "v_add_u32_dpp v55, v113, v51 quad_perm:[3,3,3,3] row_mask:0xf bank_mask:0xf\n"
    "v_mfma_f32_16x16x32_bf16 v[216:219], v[180:183], v[168:171], v[216:219]\n"
    "v_mfma_f32_16x16x32_bf16 v[216:219], v[148:151], v[136:139], v[216:219]\n"
    "v_mfma_f32_16x16x32_bf16 v[212:215], v[148:151], v[132:135], v[212:215]\n"
    "v_mfma_f32_16x16x32_bf16 v[212:215], v[180:183], v[164:167], v[212:215]\n"
    "v_mfma_f32_16x16x32_bf16 v[208:211], v[180:183], v[160:163], v[208:211]\n"
    "v_mfma_f32_16x16x32_bf16 v[208:211], v[148:151], v[128:131], v[208:211]\n"
    "v_mfma_f32_16x16x32_bf16 v[224:227], v[152:155], v[128:131], v[224:227]\n"
    "v_mfma_f32_16x16x32_bf16 v[224:227], v[184:187], v[160:163], v[224:227]\n"
    "v_mfma_f32_16x16x32_bf16 v[228:231], v[184:187], v[164:167], v[228:231]\n"
    "v_add_f32 v222, v222, v66\n"
    "v_add_f32 v223, v223, v67\n"
    "v_mfma_f32_16x16x32_bf16 v[228:231], v[152:155], v[132:135], v[228:231]\n"
    "v_add_f32 v221, v221, v65\n"
    "v_mfma_f32_16x16x32_bf16 v[232:235], v[152:155], v[136:139], v[232:235]\n"
    "v_add_f32 v220, v220, v64\n"
    "v_mfma_f32_16x16x32_bf16 v[232:235], v[184:187], v[168:171], v[232:235]\n"
    "v_add_f32 v219, v219, v67\n"
    "v_mfma_f32_16x16x32_bf16 v[236:239], v[184:187], v[172:175], v[236:239]\n"
    "v_add_f32 v217, v217, v65\n"
    "v_add_f32 v218, v218, v66\n"
    "v_mfma_f32_16x16x32_bf16 v[236:239], v[152:155], v[140:143], v[236:239]\n"
    "v_add_f32 v216, v216, v64\n"
    "v_mfma_f32_16x16x32_bf16 v[252:255], v[156:159], v[140:143], v[252:255]\n"
    "v_add_f32 v215, v215, v67\n"
    "v_mfma_f32_16x16x32_bf16 v[252:255], v[188:191], v[172:175], v[252:255]\n"
    "v_add_f32 v214, v214, v66\n"
    "v_mfma_f32_16x16x32_bf16 v[248:251], v[188:191], v[168:171], v[248:251]\n"
    "v_add_f32 v212, v212, v64\n"
    "v_add_f32 v213, v213, v65\n"
    "v_mfma_f32_16x16x32_bf16 v[248:251], v[156:159], v[136:139], v[248:251]\n"
    "v_add_f32 v211, v211, v67\n"
    "v_mfma_f32_16x16x32_bf16 v[244:247], v[156:159], v[132:135], v[244:247]\n"
    "v_add_f32 v210, v210, v66\n"
    "v_mfma_f32_16x16x32_bf16 v[244:247], v[188:191], v[164:167], v[244:247]\n"
    "v_add_f32 v209, v209, v65\n"
    "v_mfma_f32_16x16x32_bf16 v[240:243], v[188:191], v[160:163], v[240:243]\n"
    "v_add_f32 v208, v208, v64\n"
    "v_mfma_f32_16x16x32_bf16 v[240:243], v[156:159], v[128:131], v[240:243]\n"
    "s_waitcnt vmcnt(8)\n"
    "s_barrier\n"
    "s_add_u32 m0, s35, 98304\n"
    "s_nop 0\n"
    "global_load_lds_dwordx4 v46, s[30:31] offset:0\n"
    "ds_read_b128 v[128:131], v56 offset:0\n"
    "ds_read_b128 v[132:135], v56 offset:2048\n"
    "ds_read_b128 v[136:139], v56 offset:4096\n"
    "ds_read_b128 v[140:143], v56 offset:6144\n"
    "global_load_lds_dwordx4 v47, s[30:31] offset:1024\n"
    "s_add_u32 s30, s30, 212992\n"
    "s_addc_u32 s31, s31, 0\n"
    "ds_read_b64_tr_b16 v[144:145], v76 offset:0\n"
    "ds_read_b64_tr_b16 v[146:147], v76 offset:1024\n"
    "ds_read_b64_tr_b16 v[148:149], v77 offset:0\n"
    "ds_read_b64_tr_b16 v[150:151], v77 offset:1024\n"
    "s_add_u32 m0, s34, 98304\n"
    "s_nop 0\n"
    "global_load_lds_dwordx4 v52, s[36:37] offset:0\n"
    "ds_read_b64_tr_b16 v[152:153], v78 offset:0\n"
    "ds_read_b64_tr_b16 v[154:155], v78 offset:1024\n"
    "ds_read_b64_tr_b16 v[156:157], v79 offset:0\n"
    "ds_read_b64_tr_b16 v[158:159], v79 offset:1024\n"
    "global_load_lds_dwordx4 v53, s[36:37] offset:1024\n"
    "ds_read_b128 v[160:163], v57 offset:0\n"
    "ds_read_b128 v[164:167], v57 offset:2048\n"
    "ds_read_b128 v[168:171], v57 offset:4096\n"
    "ds_read_b128 v[172:175], v57 offset:6144\n"
    "global_load_lds_dwordx4 v54, s[36:37] offset:2048\n"
    "ds_read_b64_tr_b16 v[176:177], v76 offset:8192\n"
    "ds_read_b64_tr_b16 v[178:179], v76 offset:9216\n"
    "ds_read_b64_tr_b16 v[180:181], v77 offset:8192\n"
    "ds_read_b64_tr_b16 v[182:183], v77 offset:9216\n"
    "global_load_lds_dwordx4 v55, s[36:37] offset:3072\n"
    "ds_read_b64_tr_b16 v[184:185], v78 offset:8192\n"
    "ds_read_b64_tr_b16 v[186:187], v78 offset:9216\n"
    "ds_read_b64_tr_b16 v[188:189], v79 offset:8192\n"
    "ds_read_b64_tr_b16 v[190:191], v79 offset:9216\n"
    "s_waitcnt vmcnt(11) lgkmcnt(0)\n"
    "s_barrier\n"
    "v_mfma_f32_16x16x32_bf16 v[192:195], v[144:147], v[128:131], v[192:195]\n"
    "v_lshlrev_b32 v115, 7, v115\n"
    "v_mfma_f32_16x16x32_bf16 v[192:195], v[176:179], v[160:163], v[192:195]\n"
    "v_mfma_f32_16x16x32_bf16 v[196:199], v[176:179], v[164:167], v[196:199]\n"
    "v_mfma_f32_16x16x32_bf16 v[196:199], v[144:147], v[132:135], v[196:199]\n"
    "v_add_u32_dpp v52, v114, v48 quad_perm:[0,0,0,0] row_mask:0xf bank_mask:0xf\n"
    "v_mfma_f32_16x16x32_bf16 v[200:203], v[144:147], v[136:139], v[200:203]\n"
    "v_mfma_f32_16x16x32_bf16 v[200:203], v[176:179], v[168:171], v[200:203]\n"
    "v_mfma_f32_16x16x32_bf16 v[204:207], v[176:179], v[172:175], v[204:207]\n"
    "v_add_u32_dpp v53, v114, v49 quad_perm:[1,1,1,1] row_mask:0xf bank_mask:0xf\n"
    "v_mfma_f32_16x16x32_bf16 v[204:207], v[144:147], v[140:143], v[204:207]\n"
    "v_mfma_f32_16x16x32_bf16 v[220:223], v[148:151], v[140:143], v[220:223]\n"
    "v_mfma_f32_16x16x32_bf16 v[220:223], v[180:183], v[172:175], v[220:223]\n"
    "v_mfma_f32_16x16x32_bf16 v[216:219], v[180:183], v[168:171], v[216:219]\n"
    "v_add_u32_dpp v54, v114, v50 quad_perm:[2,2,2,2] row_mask:0xf bank_mask:0xf\n"
    "v_mfma_f32_16x16x32_bf16 v[216:219], v[148:151], v[136:139], v[216:219]\n"
    "v_mfma_f32_16x16x32_bf16 v[212:215], v[148:151], v[132:135], v[212:215]\n"
    "v_mfma_f32_16x16x32_bf16 v[212:215], v[180:183], v[164:167], v[212:215]\n"
    "v_add_u32_dpp v55, v114, v51 quad_perm:[3,3,3,3] row_mask:0xf bank_mask:0xf\n"
    "v_mfma_f32_16x16x32_bf16 v[208:211], v[180:183], v[160:163], v[208:211]\n"
    "v_mfma_f32_16x16x32_bf16 v[208:211], v[148:151], v[128:131], v[208:211]\n"
    "v_mfma_f32_16x16x32_bf16 v[224:227], v[152:155], v[128:131], v[224:227]\n"
    "v_mfma_f32_16x16x32_bf16 v[224:227], v[184:187], v[160:163], v[224:227]\n"
    "v_lshl_or_b32 v36, v36, 7, v19\n"
    "v_mfma_f32_16x16x32_bf16 v[228:231], v[184:187], v[164:167], v[228:231]\n"
    "v_mfma_f32_16x16x32_bf16 v[228:231], v[152:155], v[132:135], v[228:231]\n"
    "v_mfma_f32_16x16x32_bf16 v[232:235], v[152:155], v[136:139], v[232:235]\n"
    "v_lshl_or_b32 v37, v37, 7, v19\n"
    "v_mfma_f32_16x16x32_bf16 v[232:235], v[184:187], v[168:171], v[232:235]\n"
    "v_mfma_f32_16x16x32_bf16 v[236:239], v[184:187], v[172:175], v[236:239]\n"
    "v_mfma_f32_16x16x32_bf16 v[236:239], v[152:155], v[140:143], v[236:239]\n"
    "v_mfma_f32_16x16x32_bf16 v[252:255], v[156:159], v[140:143], v[252:255]\n"
    "v_lshl_or_b32 v38, v38, 7, v19\n"
    "v_mfma_f32_16x16x32_bf16 v[252:255], v[188:191], v[172:175], v[252:255]\n"
    "v_mfma_f32_16x16x32_bf16 v[248:251], v[188:191], v[168:171], v[248:251]\n"
    "v_mfma_f32_16x16x32_bf16 v[248:251], v[156:159], v[136:139], v[248:251]\n"
    "v_lshl_or_b32 v41, v41, 7, v20\n"
    "v_mfma_f32_16x16x32_bf16 v[244:247], v[156:159], v[132:135], v[244:247]\n"
    "v_mfma_f32_16x16x32_bf16 v[244:247], v[188:191], v[164:167], v[244:247]\n"
    "v_mfma_f32_16x16x32_bf16 v[240:243], v[188:191], v[160:163], v[240:243]\n"
    "v_mfma_f32_16x16x32_bf16 v[240:243], v[156:159], v[128:131], v[240:243]\n"
    "s_waitcnt vmcnt(7)\n"
    "s_barrier\n"
    "s_cmp_eq_u32 s20, 3\n"
    "s_cbranch_scc1 .Lxd_on1\n"
    ".Lxd_on1r:\n"
    "s_add_u32 m0, s35, 0\n"
    "s_nop 0\n"
    "global_load_lds_dwordx4 v46, s[30:31] offset:0\n"
    "ds_read_b128 v[128:131], v56 offset:49152\n"
    "ds_read_b128 v[132:135], v56 offset:51200\n"
    "ds_read_b128 v[136:139], v56 offset:53248\n"
    "global_load_lds_dwordx4 v47, s[30:31] offset:1024\n"
    "s_add_u32 s30, s30, 212992\n"
    "s_addc_u32 s31, s31, 0\n"
    "ds_read_b128 v[140:143], v56 offset:55296\n"
    "ds_read_b64_tr_b16 v[144:145], v76 offset:49152\n"
    "ds_read_b64_tr_b16 v[146:147], v76 offset:50176\n"
    "s_add_u32 m0, s34, 0\n"
    "s_nop 0\n"
    "global_load_lds_dwordx4 v52, s[36:37] offset:0\n"
    "ds_read_b64_tr_b16 v[148:149], v77 offset:49152\n"
    "ds_read_b64_tr_b16 v[150:151], v77 offset:50176\n"
    "ds_read_b64_tr_b16 v[152:153], v78 offset:49152\n"
    "global_load_lds_dwordx4 v53, s[36:37] offset:1024\n"
    "ds_read_b64_tr_b16 v[154:155], v78 offset:50176\n"
    "ds_read_b64_tr_b16 v[156:157], v79 offset:49152\n"
    "ds_read_b64_tr_b16 v[158:159], v79 offset:50176\n"
    "global_load_lds_dwordx4 v54, s[36:37] offset:2048\n"
    "ds_read_b128 v[160:163], v57 offset:49152\n"
    "ds_read_b128 v[164:167], v57 offset:51200\n"
    "ds_read_b128 v[168:171], v57 offset:53248\n"
    "global_load_lds_dwordx4 v55, s[36:37] offset:3072\n"
    "ds_read_b128 v[172:175], v57 offset:55296\n"
    "ds_read_b64_tr_b16 v[176:177], v76 offset:57344\n"
    "ds_read_b64_tr_b16 v[178:179], v76 offset:58368\n"
    "global_load_dwordx4 v[28:31], v36, s[8:9]\n"
    "ds_read_b64_tr_b16 v[180:181], v77 offset:57344\n"
    "ds_read_b64_tr_b16 v[182:183], v77 offset:58368\n"
    "ds_read_b64_tr_b16 v[184:185], v78 offset:57344\n"
    "ds_read_b64_tr_b16 v[186:187], v78 offset:58368\n"
    "ds_read_b64_tr_b16 v[188:189], v79 offset:57344\n"
    "ds_read_b64_tr_b16 v[190:191], v79 offset:58368\n"
    "s_waitcnt vmcnt(11) lgkmcnt(0)\n"
    "s_barrier\n"
    "v_mfma_f32_16x16x32_bf16 v[192:195], v[144:147], v[128:131], v[192:195]\n"
    "v_mfma_f32_16x16x32_bf16 v[192:195], v[176:179], v[160:163], v[192:195]\n"
    "v_add_f32 v224, v224, v68\n"
    "v_add_f32 v225, v225, v69\n"
    "v_mfma_f32_16x16x32_bf16 v[196:199], v[176:179], v[164:167], v[196:199]\n"
    "v_add_f32 v226, v226, v70\n"
    "v_mfma_f32_16x16x32_bf16 v[196:199], v[144:147], v[132:135], v[196:199]\n"
    "v_add_f32 v227, v227, v71\n"
    "v_mfma_f32_16x16x32_bf16 v[200:203], v[144:147], v[136:139], v[200:203]\n"
    "v_add_f32 v228, v228, v68\n"
    "v_mfma_f32_16x16x32_bf16 v[200:203], v[176:179], v[168:171], v[200:203]\n"
    "v_add_f32 v229, v229, v69\n"
    "v_add_f32 v230, v230, v70\n"
    "v_mfma_f32_16x16x32_bf16 v[204:207], v[176:179], v[172:175], v[204:207]\n"
    "v_add_f32 v231, v231, v71\n"
    "v_mfma_f32_16x16x32_bf16 v[204:207], v[144:147], v[140:143], v[204:207]\n"
    "v_add_f32 v232, v232, v68\n"
    "v_mfma_f32_16x16x32_bf16 v[220:223], v[148:151], v[140:143], v[220:223]\n"
    "v_add_f32 v233, v233, v69\n"
    "v_mfma_f32_16x16x32_bf16 v[220:223], v[180:183], v[172:175], v[220:223]\n"
    "v_add_f32 v234, v234, v70\n"
    "v_add_f32 v235, v235, v71\n"
    "v_mfma_f32_16x16x32_bf16 v[216:219], v[180:183], v[168:171], v[216:219]\n"
    "v_add_f32 v236, v236, v68\n"
    "v_mfma_f32_16x16x32_bf16 v[216:219], v[148:151], v[136:139], v[216:219]\n"
    "v_add_f32 v237, v237, v69\n"
    "v_mfma_f32_16x16x32_bf16 v[212:215], v[148:151], v[132:135], v[212:215]\n"
    "v_add_f32 v238, v238, v70\n"
    "v_mfma_f32_16x16x32_bf16 v[212:215], v[180:183], v[164:167], v[212:215]\n"
    "v_add_f32 v239, v239, v71\n"
    "v_mfma_f32_16x16x32_bf16 v[208:211], v[180:183], v[160:163], v[208:211]\n"
    "v_mfma_f32_16x16x32_bf16 v[208:211], v[148:151], v[128:131], v[208:211]\n"
    "v_mfma_f32_16x16x32_bf16 v[224:227], v[152:155], v[128:131], v[224:227]\n"
    "v_mfma_f32_16x16x32_bf16 v[224:227], v[184:187], v[160:163], v[224:227]\n"
    "v_mfma_f32_16x16x32_bf16 v[228:231], v[184:187], v[164:167], v[228:231]\n"
    "v_lshlrev_b32 v116, 7, v116\n"
    "v_mfma_f32_16x16x32_bf16 v[228:231], v[152:155], v[132:135], v[228:231]\n"
    "v_mfma_f32_16x16x32_bf16 v[232:235], v[152:155], v[136:139], v[232:235]\n"
    "v_add_u32_dpp v52, v115, v48 quad_perm:[0,0,0,0] row_mask:0xf bank_mask:0xf\n"
    "v_mfma_f32_16x16x32_bf16 v[232:235], v[184:187], v[168:171], v[232:235]\n"
    "v_mfma_f32_16x16x32_bf16 v[236:239], v[184:187], v[172:175], v[236:239]\n"
    "v_mfma_f32_16x16x32_bf16 v[236:239], v[152:155], v[140:143], v[236:239]\n"
    "v_add_u32_dpp v53, v115, v49 quad_perm:[1,1,1,1] row_mask:0xf bank_mask:0xf\n"
    "v_mfma_f32_16x16x32_bf16 v[252:255], v[156:159], v[140:143], v[252:255]\n"
    "v_mfma_f32_16x16x32_bf16 v[252:255], v[188:191], v[172:175], v[252:255]\n"
    "v_add_u32_dpp v54, v115, v50 quad_perm:[2,2,2,2] row_mask:0xf bank_mask:0xf\n"
    "v_mfma_f32_16x16x32_bf16 v[248:251], v[188:191], v[168:171], v[248:251]\n"
    "v_mfma_f32_16x16x32_bf16 v[248:251], v[156:159], v[136:139], v[248:251]\n"
    "v_mfma_f32_16x16x32_bf16 v[244:247], v[156:159], v[132:135], v[244:247]\n"
    "v_add_u32_dpp v55, v115, v51 quad_perm:[3,3,3,3] row_mask:0xf bank_mask:0xf\n"
    "v_mfma_f32_16x16x32_bf16 v[244:247], v[188:191], v[164:167], v[244:247]\n"
    "v_mfma_f32_16x16x32_bf16 v[240:243], v[188:191], v[160:163], v[240:243]\n"
    "v_mfma_f32_16x16x32_bf16 v[240:243], v[156:159], v[128:131], v[240:243]\n"
    "s_waitcnt vmcnt(7)\n"
    "s_barrier\n"
    "s_add_u32 m0, s35, 49152\n"
    "s_nop 0\n"
    "global_load_lds_dwordx4 v46, s[30:31] offset:0\n"
    "ds_read_b128 v[128:131], v58 offset:0\n"
    "ds_read_b128 v[132:135], v58 offset:2048\n"
    "ds_read_b128 v[136:139], v58 offset:4096\n"
    "global_load_lds_dwordx4 v47, s[30:31] offset:1024\n"
    "s_bfe_u32 s66, s29, 0x80000\n"
    "s_mul_i32 s66, s66, 53248\n"
    "s_add_u32 s30, s6, s66\n"
    "s_addc_u32 s31, s7, 0\n"
    "ds_read_b128 v[140:143], v58 offset:6144\n"
    "ds_read_b64_tr_b16 v[144:145], v80 offset:0\n"
    "ds_read_b64_tr_b16 v[146:147], v80 offset:1024\n"
    "s_add_u32 m0, s34, 49152\n"
    "s_nop 0\n"
    "global_load_lds_dwordx4 v52, s[36:37] offset:0\n"
    "ds_read_b64_tr_b16 v[148:149], v81 offset:0\n"
    "ds_read_b64_tr_b16 v[150:151], v81 offset:1024\n"
    "ds_read_b64_tr_b16 v[152:153], v82 offset:0\n"
    "global_load_lds_dwordx4 v53, s[36:37] offset:1024\n"
    "ds_read_b64_tr_b16 v[154:155], v82 offset:1024\n"
    "ds_read_b64_tr_b16 v[156:157], v83 offset:0\n"
    "ds_read_b64_tr_b16 v[158:159], v83 offset:1024\n"
    "global_load_lds_dwordx4 v54, s[36:37] offset:2048\n"
    "ds_read_b128 v[160:163], v59 offset:0\n"
    "ds_read_b128 v[164:167], v59 offset:2048\n"
    "ds_read_b128 v[168:171], v59 offset:4096\n"
    "global_load_lds_dwordx4 v55, s[36:37] offset:3072\n"
    "ds_read_b128 v[172:175], v59 offset:6144\n"
    "ds_read_b64_tr_b16 v[176:177], v80 offset:8192\n"
    "ds_read_b64_tr_b16 v[178:179], v80 offset:9216\n"
    "global_load_dwordx4 v[32:35], v37, s[8:9]\n"
    "ds_read_b64_tr_b16 v[180:181], v81 offset:8192\n"
    "ds_read_b64_tr_b16 v[182:183], v81 offset:9216\n"
    "ds_read_b64_tr_b16 v[184:185], v82 offset:8192\n"
    "ds_read_b64_tr_b16 v[186:187], v82 offset:9216\n"
    "ds_read_b64_tr_b16 v[188:189], v83 offset:8192\n"
    "ds_read_b64_tr_b16 v[190:191], v83 offset:9216\n"
    "s_waitcnt vmcnt(12) lgkmcnt(0)\n"
    "s_barrier\n"
    "v_mfma_f32_16x16x32_bf16 v[192:195], v[144:147], v[128:131], v[192:195]\n"
    "v_mfma_f32_16x16x32_bf16 v[192:195], v[176:179], v[160:163], v[192:195]\n"
    "v_mfma_f32_16x16x32_bf16 v[196:199], v[176:179], v[164:167], v[196:199]\n"
    "v_mfma_f32_16x16x32_bf16 v[196:199], v[144:147], v[132:135], v[196:199]\n"
    "v_mfma_f32_16x16x32_bf16 v[200:203], v[144:147], v[136:139], v[200:203]\n"
    "v_add_f32 v240, v240, v72\n"
    "v_mfma_f32_16x16x32_bf16 v[200:203], v[176:179], v[168:171], v[200:203]\n"
    "v_add_f32 v241, v241, v73\n"
    "v_mfma_f32_16x16x32_bf16 v[204:207], v[176:179], v[172:175], v[204:207]\n"
    "v_add_f32 v242, v242, v74\n"
    "v_mfma_f32_16x16x32_bf16 v[204:207], v[144:147], v[140:143], v[204:207]\n"
    "v_add_f32 v243, v243, v75\n"
    "v_mfma_f32_16x16x32_bf16 v[220:223], v[148:151], v[140:143], v[220:223]\n"
    "v_add_f32 v244, v244, v72\n"
    "v_mfma_f32_16x16x32_bf16 v[220:223], v[180:183], v[172:175], v[220:223]\n"
    "v_add_f32 v245, v245, v73\n"
    "v_mfma_f32_16x16x32_bf16 v[216:219], v[180:183], v[168:171], v[216:219]\n"
    "v_add_f32 v246, v246, v74\n"
    "v_mfma_f32_16x16x32_bf16 v[216:219], v[148:151], v[136:139], v[216:219]\n"
    "v_add_f32 v247, v247, v75\n"
    "v_mfma_f32_16x16x32_bf16 v[212:215], v[148:151], v[132:135], v[212:215]\n"
    "v_add_f32 v248, v248, v72\n"
    "v_mfma_f32_16x16x32_bf16 v[212:215], v[180:183], v[164:167], v[212:215]\n"
    "v_add_f32 v249, v249, v73\n"
    "v_mfma_f32_16x16x32_bf16 v[208:211], v[180:183], v[160:163], v[208:211]\n"
    "v_add_f32 v250, v250, v74\n"
    "v_mfma_f32_16x16x32_bf16 v[208:211], v[148:151], v[128:131], v[208:211]\n"
    "v_add_f32 v251, v251, v75\n"
    "v_mfma_f32_16x16x32_bf16 v[224:227], v[152:155], v[128:131], v[224:227]\n"
    "v_add_f32 v252, v252, v72\n"
    "v_mfma_f32_16x16x32_bf16 v[224:227], v[184:187], v[160:163], v[224:227]\n"
    "v_add_f32 v253, v253, v73\n"
    "v_mfma_f32_16x16x32_bf16 v[228:231], v[184:187], v[164:167], v[228:231]\n"
    "v_lshlrev_b32 v117, 7, v117\n"
    "v_add_f32 v254, v254, v74\n"
    "v_mfma_f32_16x16x32_bf16 v[228:231], v[152:155], v[132:135], v[228:231]\n"
    "v_add_f32 v255, v255, v75\n"
    "v_mfma_f32_16x16x32_bf16 v[232:235], v[152:155], v[136:139], v[232:235]\n"
    "v_add_u32_dpp v52, v116, v48 quad_perm:[0,0,0,0] row_mask:0xf bank_mask:0xf\n"
    "v_mfma_f32_16x16x32_bf16 v[232:235], v[184:187], v[168:171], v[232:235]\n"
    "v_mfma_f32_16x16x32_bf16 v[236:239], v[184:187], v[172:175], v[236:239]\n"
    "v_mfma_f32_16x16x32_bf16 v[236:239], v[152:155], v[140:143], v[236:239]\n"
    "v_add_u32_dpp v53, v116, v49 quad_perm:[1,1,1,1] row_mask:0xf bank_mask:0xf\n"
    "v_mfma_f32_16x16x32_bf16 v[252:255], v[156:159], v[140:143], v[252:255]\n"
    "v_mfma_f32_16x16x32_bf16 v[252:255], v[188:191], v[172:175], v[252:255]\n"
    "v_add_u32_dpp v54, v116, v50 quad_perm:[2,2,2,2] row_mask:0xf bank_mask:0xf\n"
    "v_mfma_f32_16x16x32_bf16 v[248:251], v[188:191], v[168:171], v[248:251]\n"
    "v_mfma_f32_16x16x32_bf16 v[248:251], v[156:159], v[136:139], v[248:251]\n"
    "v_mfma_f32_16x16x32_bf16 v[244:247], v[156:159], v[132:135], v[244:247]\n"
    "v_add_u32_dpp v55, v116, v51 quad_perm:[3,3,3,3] row_mask:0xf bank_mask:0xf\n"
    "v_mfma_f32_16x16x32_bf16 v[244:247], v[188:191], v[164:167], v[244:247]\n"
    "v_mfma_f32_16x16x32_bf16 v[240:243], v[188:191], v[160:163], v[240:243]\n"
    "v_mfma_f32_16x16x32_bf16 v[240:243], v[156:159], v[128:131], v[240:243]\n"
    "s_waitcnt vmcnt(8)\n"
    "s_barrier\n"
    "s_add_u32 m0, s35, 98304\n"
    "s_nop 0\n"
    "global_load_lds_dwordx4 v46, s[30:31] offset:0\n"
    "ds_read_b128 v[128:131], v56 offset:0\n"
    "ds_read_b128 v[132:135], v56 offset:2048\n"
    "ds_read_b128 v[136:139], v56 offset:4096\n"
    "global_load_lds_dwordx4 v47, s[30:31] offset:1024\n"
    "s_add_u32 s30, s30, 212992\n"
    "s_addc_u32 s31, s31, 0\n"
    "ds_read_b128 v[140:143], v56 offset:6144\n"
    "ds_read_b64_tr_b16 v[144:145], v76 offset:0\n"
    "ds_read_b64_tr_b16 v[146:147], v76 offset:1024\n"
    "s_add_u32 m0, s34, 98304\n"
    "s_nop 0\n"
    "global_load_lds_dwordx4 v52, s[36:37] offset:0\n"
    "ds_read_b64_tr_b16 v[148:149], v77 offset:0\n"
    "ds_read_b64_tr_b16 v[150:151], v77 offset:1024\n"
    "ds_read_b64_tr_b16 v[152:153], v78 offset:0\n"
    "global_load_lds_dwordx4 v53, s[36:37] offset:1024\n"
    "ds_read_b64_tr_b16 v[154:155], v78 offset:1024\n"
    "ds_read_b64_tr_b16 v[156:157], v79 offset:0\n"
    "ds_read_b64_tr_b16 v[158:159], v79 offset:1024\n"
    "global_load_lds_dwordx4 v54, s[36:37] offset:2048\n"
    "ds_read_b128 v[160:163], v57 offset:0\n"
    "ds_read_b128 v[164:167], v57 offset:2048\n"
    "ds_read_b128 v[168:171], v57 offset:4096\n"
    "global_load_lds_dwordx4 v55, s[36:37] offset:3072\n"
    "ds_read_b128 v[172:175], v57 offset:6144\n"
    "ds_read_b64_tr_b16 v[176:177], v76 offset:8192\n"
    "ds_read_b64_tr_b16 v[178:179], v76 offset:9216\n"
    "global_load_dword v40, v41, s[8:9]\n"
    "ds_read_b64_tr_b16 v[180:181], v77 offset:8192\n"
    "ds_read_b64_tr_b16 v[182:183], v77 offset:9216\n"
    "ds_read_b64_tr_b16 v[184:185], v78 offset:8192\n"
    "ds_read_b64_tr_b16 v[186:187], v78 offset:9216\n"
    "ds_read_b64_tr_b16 v[188:189], v79 offset:8192\n"
    "ds_read_b64_tr_b16 v[190:191], v79 offset:9216\n"
    "s_waitcnt vmcnt(12) lgkmcnt(0)\n"
    "s_barrier\n"
    "v_mfma_f32_16x16x32_bf16 v[192:195], v[144:147], v[128:131], v[192:195]\n"
    "v_lshlrev_b32 v118, 7, v118\n"
    "v_mfma_f32_16x16x32_bf16 v[192:195], v[176:179], v[160:163], v[192:195]\n"
    "v_add_u32_dpp v52, v117, v48 quad_perm:[0,0,0,0] row_mask:0xf bank_mask:0xf\n"
    "v_mfma_f32_16x16x32_bf16 v[196:199], v[176:179], v[164:167], v[196:199]\n"
    "v_add_u32_dpp v53, v117, v49 quad_perm:[1,1,1,1] row_mask:0xf bank_mask:0xf\n"
    "v_mfma_f32_16x16x32_bf16 v[196:199], v[144:147], v[132:135], v[196:199]\n"
    "v_add_u32_dpp v54, v117, v50 quad_perm:[2,2,2,2] row_mask:0xf bank_mask:0xf\n"
    "v_mfma_f32_16x16x32_bf16 v[200:203], v[144:147], v[136:139], v[200:203]\n"
    "v_add_u32_dpp v55, v117, v51 quad_perm:[3,3,3,3] row_mask:0xf bank_mask:0xf\n"
    "v_mfma_f32_16x16x32_bf16 v[200:203], v[176:179], v[168:171], v[200:203]\n"
    "v_readlane_b32 s25, v26, 9\n"
    "v_mfma_f32_16x16x32_bf16 v[204:207], v[176:179], v[172:175], v[204:207]\n"
    "v_readlane_b32 s40, v26, 8\n"
    "v_mfma_f32_16x16x32_bf16 v[204:207], v[144:147], v[140:143], v[204:207]\n"
    "v_readlane_b32 s41, v27, 8\n"
    "v_mfma_f32_16x16x32_bf16 v[220:223], v[148:151], v[140:143], v[220:223]\n"
    "s_mul_i32 s42, s25, 0x9e3779b1\n"
    "v_mfma_f32_16x16x32_bf16 v[220:223], v[180:183], v[172:175], v[220:223]\n"
    "s_lshr_b32 s42, s42, 24\n"
    "v_mfma_f32_16x16x32_bf16 v[216:219], v[180:183], v[168:171], v[216:219]\n"
    "s_xor_b32 s42, s42, s40\n"
    "v_mfma_f32_16x16x32_bf16 v[216:219], v[148:151], v[136:139], v[216:219]\n"
    "s_and_b32 s42, s42, 0xff\n"
    "v_mfma_f32_16x16x32_bf16 v[212:215], v[148:151], v[132:135], v[212:215]\n"
    "s_xor_b32 s41, s41, s25\n"
    "v_mfma_f32_16x16x32_bf16 v[212:215], v[180:183], v[164:167], v[212:215]\n"
    "s_xor_b32 s41, s41, 0xa5c30f96\n"
    "v_mfma_f32_16x16x32_bf16 v[208:211], v[180:183], v[160:163], v[208:211]\n"
    "s_or_b32 s42, s41, s42\n"
    "v_mfma_f32_16x16x32_bf16 v[208:211], v[148:151], v[128:131], v[208:211]\n"
    "v_mfma_f32_16x16x32_bf16 v[224:227], v[152:155], v[128:131], v[224:227]\n"
    "s_lshr_b32 s41, s40, 8\n"
    "v_mfma_f32_16x16x32_bf16 v[224:227], v[184:187], v[160:163], v[224:227]\n"
    "s_mul_i32 s40, s41, 0x9e3779b1\n"
    "v_mfma_f32_16x16x32_bf16 v[228:231], v[184:187], v[164:167], v[228:231]\n"
    "s_xor_b32 s41, s41, 0xa5c30f96\n"
    "v_mfma_f32_16x16x32_bf16 v[228:231], v[152:155], v[132:135], v[228:231]\n"
    "s_lshr_b32 s40, s40, 24\n"
    "v_mfma_f32_16x16x32_bf16 v[232:235], v[152:155], v[136:139], v[232:235]\n"
    "v_xor_b32 v13, s40, v26\n"
    "v_mfma_f32_16x16x32_bf16 v[232:235], v[184:187], v[168:171], v[232:235]\n"
    "v_xor_b32 v22, s41, v27\n"
    "v_mfma_f32_16x16x32_bf16 v[236:239], v[184:187], v[172:175], v[236:239]\n"
    "v_lshl_or_b32 v13, v13, 24, v22\n"
    "v_mfma_f32_16x16x32_bf16 v[236:239], v[152:155], v[140:143], v[236:239]\n"
    "v_cmp_ne_u32 s[66:67], 0, v13\n"
    "v_mfma_f32_16x16x32_bf16 v[252:255], v[156:159], v[140:143], v[252:255]\n"
    "s_and_b32 s66, s66, 0xff\n"
    "v_mfma_f32_16x16x32_bf16 v[252:255], v[188:191], v[172:175], v[252:255]\n"
    "s_or_b32 s42, s42, s66\n"
    "v_mfma_f32_16x16x32_bf16 v[248:251], v[188:191], v[168:171], v[248:251]\n"
    "s_cmp_eq_u32 s20, 3\n"
    "v_mfma_f32_16x16x32_bf16 v[248:251], v[156:159], v[136:139], v[248:251]\n"
    "s_cselect_b32 s42, s42, 0\n"
    "v_mfma_f32_16x16x32_bf16 v[244:247], v[156:159], v[132:135], v[244:247]\n"
    "s_cmp_lg_u32 s42, 0\n"
    "v_mfma_f32_16x16x32_bf16 v[244:247], v[188:191], v[164:167], v[244:247]\n"
    "s_cbranch_scc1 .Lxd_pv2\n"
    ".Lxd_pv2r:\n"
    "v_mfma_f32_16x16x32_bf16 v[240:243], v[188:191], v[160:163], v[240:243]\n"
    "v_mfma_f32_16x16x32_bf16 v[240:243], v[156:159], v[128:131], v[240:243]\n"
    "s_waitcnt vmcnt(8)\n"
    "s_barrier\n"
    "s_add_u32 m0, s35, 0\n"
    "s_nop 0\n"
    "global_load_lds_dwordx4 v46, s[30:31] offset:0\n"
    "ds_read_b128 v[128:131], v56 offset:49152\n"
    "ds_read_b128 v[132:135], v56 offset:51200\n"
    "ds_read_b128 v[136:139], v56 offset:53248\n"
    "global_load_lds_dwordx4 v47, s[30:31] offset:1024\n"
    "s_add_u32 s30, s30, 212992\n"
    "s_addc_u32 s31, s31, 0\n"
    "ds_read_b128 v[140:143], v56 offset:55296\n"
    "ds_read_b64_tr_b16 v[144:145], v76 offset:49152\n"
    "ds_read_b64_tr_b16 v[146:147], v76 offset:50176\n"
    "s_add_u32 m0, s34, 0\n"
    "s_nop 0\n"
    "global_load_lds_dwordx4 v52, s[36:37] offset:0\n"
    "ds_read_b64_tr_b16 v[148:149], v77 offset:49152\n"
    "ds_read_b64_tr_b16 v[150:151], v77 offset:50176\n"
    "ds_read_b64_tr_b16 v[152:153], v78 offset:49152\n"
    "global_load_lds_dwordx4 v53, s[36:37] offset:1024\n"
    "ds_read_b64_tr_b16 v[154:155], v78 offset:50176\n"
    "ds_read_b64_tr_b16 v[156:157], v79 offset:49152\n"
    "ds_read_b64_tr_b16 v[158:159], v79 offset:50176\n"
    "global_load_lds_dwordx4 v54, s[36:37] offset:2048\n"
    "ds_read_b128 v[160:163], v57 offset:49152\n"
    "ds_read_b128 v[164:167], v57 offset:51200\n"
    "ds_read_b128 v[168:171], v57 offset:53248\n"
    "global_load_lds_dwordx4 v55, s[36:37] offset:3072\n"
    "ds_read_b128 v[172:175], v57 offset:55296\n"
    "ds_read_b64_tr_b16 v[176:177], v76 offset:57344\n"
    "ds_read_b64_tr_b16 v[178:179], v76 offset:58368\n"
    "global_load_dwordx4 v[36:39], v38, s[8:9]\n"
    "ds_read_b64_tr_b16 v[180:181], v77 offset:57344\n"
    "ds_read_b64_tr_b16 v[182:183], v77 offset:58368\n"
    "ds_read_b64_tr_b16 v[184:185], v78 offset:57344\n"
    "ds_read_b64_tr_b16 v[186:187], v78 offset:58368\n"
    "ds_read_b64_tr_b16 v[188:189], v79 offset:57344\n"
    "ds_read_b64_tr_b16 v[190:191], v79 offset:58368\n"
    "ds_read_b64 v[84:85], v42 offset:0\n"
    "ds_read_b64 v[86:87], v42 offset:32\n"
    "ds_read_b64 v[88:89], v42 offset:64\n"
    "ds_read_b64 v[90:91], v42 offset:96\n"
    "s_waitcnt vmcnt(12) lgkmcnt(0)\n"
    "s_barrier\n"
    "v_mfma_f32_16x16x32_bf16 v[192:195], v[144:147], v[128:131], v[192:195]\n"
    "v_lshlrev_b32 v119, 7, v119\n"
    "v_mfma_f32_16x16x32_bf16 v[192:195], v[176:179], v[160:163], v[192:195]\n"
    "v_add_u32_dpp v52, v118, v48 quad_perm:[0,0,0,0] row_mask:0xf bank_mask:0xf\n"
    "v_mfma_f32_16x16x32_bf16 v[196:199], v[176:179], v[164:167], v[196:199]\n"
    "v_add_u32_dpp v53, v118, v49 quad_perm:[1,1,1,1] row_mask:0xf bank_mask:0xf\n"
    "v_mfma_f32_16x16x32_bf16 v[196:199], v[144:147], v[132:135], v[196:199]\n"
    "v_mfma_f32_16x16x32_bf16 v[200:203], v[144:147], v[136:139], v[200:203]\n"
    "v_add_u32_dpp v54, v118, v50 quad_perm:[2,2,2,2] row_mask:0xf bank_mask:0xf\n"
    "v_mfma_f32_16x16x32_bf16 v[200:203], v[176:179], v[168:171], v[200:203]\n"
    "v_add_u32_dpp v55, v118, v51 quad_perm:[3,3,3,3] row_mask:0xf bank_mask:0xf\n"
    "v_mfma_f32_16x16x32_bf16 v[204:207], v[176:179], v[172:175], v[204:207]\n"
    "v_mfma_f32_16x16x32_bf16 v[204:207], v[144:147], v[140:143], v[204:207]\n"
    "v_lshlrev_b32 v60, 16, v84\n"
    "v_mfma_f32_16x16x32_bf16 v[220:223], v[148:151], v[140:143], v[220:223]\n"
    "v_and_b32 v61, 0xffff0000, v84\n"
    "v_mfma_f32_16x16x32_bf16 v[220:223], v[180:183], v[172:175], v[220:223]\n"
    "v_mfma_f32_16x16x32_bf16 v[216:219], v[180:183], v[168:171], v[216:219]\n"
    "v_lshlrev_b32 v62, 16, v85\n"
    "v_mfma_f32_16x16x32_bf16 v[216:219], v[148:151], v[136:139], v[216:219]\n"
    "v_and_b32 v63, 0xffff0000, v85\n"
    "v_mfma_f32_16x16x32_bf16 v[212:215], v[148:151], v[132:135], v[212:215]\n"
    "v_mfma_f32_16x16x32_bf16 v[212:215], v[180:183], v[164:167], v[212:215]\n"
    "v_lshlrev_b32 v64, 16, v86\n"
    "v_mfma_f32_16x16x32_bf16 v[208:211], v[180:183], v[160:163], v[208:211]\n"
    "v_and_b32 v65, 0xffff0000, v86\n"
    "v_mfma_f32_16x16x32_bf16 v[208:211], v[148:151], v[128:131], v[208:211]\n"
    "v_mfma_f32_16x16x32_bf16 v[224:227], v[152:155], v[128:131], v[224:227]\n"
    "v_lshlrev_b32 v66, 16, v87\n"
    "v_mfma_f32_16x16x32_bf16 v[224:227], v[184:187], v[160:163], v[224:227]\n"
    "v_and_b32 v67, 0xffff0000, v87\n"
    "v_mfma_f32_16x16x32_bf16 v[228:231], v[184:187], v[164:167], v[228:231]\n"
    "v_mfma_f32_16x16x32_bf16 v[228:231], v[152:155], v[132:135], v[228:231]\n"
    "v_lshlrev_b32 v68, 16, v88\n"
    "v_mfma_f32_16x16x32_bf16 v[232:235], v[152:155], v[136:139], v[232:235]\n"
    "v_and_b32 v69, 0xffff0000, v88\n"
    "v_mfma_f32_16x16x32_bf16 v[232:235], v[184:187], v[168:171], v[232:235]\n"
    "v_mfma_f32_16x16x32_bf16 v[236:239], v[184:187], v[172:175], v[236:239]\n"
    "v_lshlrev_b32 v70, 16, v89\n"
    "v_mfma_f32_16x16x32_bf16 v[236:239], v[152:155], v[140:143], v[236:239]\n"
    "v_and_b32 v71, 0xffff0000, v89\n"
    "v_mfma_f32_16x16x32_bf16 v[252:255], v[156:159], v[140:143], v[252:255]\n"
    "v_mfma_f32_16x16x32_bf16 v[252:255], v[188:191], v[172:175], v[252:255]\n"
    "v_lshlrev_b32 v72, 16, v90\n"
    "v_mfma_f32_16x16x32_bf16 v[248:251], v[188:191], v[168:171], v[248:251]\n"
    "v_and_b32 v73, 0xffff0000, v90\n"
    "v_mfma_f32_16x16x32_bf16 v[248:251], v[156:159], v[136:139], v[248:251]\n"
    "v_mfma_f32_16x16x32_bf16 v[244:247], v[156:159], v[132:135], v[244:247]\n"
    "v_lshlrev_b32 v74, 16, v91\n"
    "v_mfma_f32_16x16x32_bf16 v[244:247], v[188:191], v[164:167], v[244:247]\n"
    "v_and_b32 v75, 0xffff0000, v91\n"
    "v_mfma_f32_16x16x32_bf16 v[240:243], v[188:191], v[160:163], v[240:243]\n"
    "v_mfma_f32_16x16x32_bf16 v[240:243], v[156:159], v[128:131], v[240:243]\n"
    "s_waitcnt vmcnt(8)\n"
    "s_barrier\n"
    "s_add_u32 m0, s35, 49152\n"
    "s_nop 0\n"
    "global_load_lds_dwordx4 v46, s[30:31] offset:0\n"
    "ds_read_b128 v[128:131], v58 offset:0\n"
    "ds_read_b128 v[132:135], v58 offset:2048\n"
    "ds_read_b128 v[136:139], v58 offset:4096\n"
    "ds_read_b128 v[140:143], v58 offset:6144\n"
    "global_load_lds_dwordx4 v47, s[30:31] offset:1024\n"
    "s_add_u32 s30, s30, 212992\n"
    "s_addc_u32 s31, s31, 0\n"
    "ds_read_b64_tr_b16 v[144:145], v80 offset:0\n"
    "ds_read_b64_tr_b16 v[146:147], v80 offset:1024\n"
    "ds_read_b64_tr_b16 v[148:149], v81 offset:0\n"
    "ds_read_b64_tr_b16 v[150:151], v81 offset:1024\n"
    "s_add_u32 m0, s34, 49152\n"
    "s_nop 0\n"
    "global_load_lds_dwordx4 v52, s[36:37] offset:0\n"
    "ds_read_b64_tr_b16 v[152:153], v82 offset:0\n"
    "ds_read_b64_tr_b16 v[154:155], v82 offset:1024\n"
    "ds_read_b64_tr_b16 v[156:157], v83 offset:0\n"
    "ds_read_b64_tr_b16 v[158:159], v83 offset:1024\n"
    "global_load_lds_dwordx4 v53, s[36:37] offset:1024\n"
    "ds_read_b128 v[160:163], v59 offset:0\n"
    "ds_read_b128 v[164:167], v59 offset:2048\n"
    "ds_read_b128 v[168:171], v59 offset:4096\n"
    "ds_read_b128 v[172:175], v59 offset:6144\n"
    "global_load_lds_dwordx4 v54, s[36:37] offset:2048\n"
    "ds_read_b64_tr_b16 v[176:177], v80 offset:8192\n"
    "ds_read_b64_tr_b16 v[178:179], v80 offset:9216\n"
    "ds_read_b64_tr_b16 v[180:181], v81 offset:8192\n"
    "ds_read_b64_tr_b16 v[182:183], v81 offset:9216\n"
    "global_load_lds_dwordx4 v55, s[36:37] offset:3072\n"
    "ds_read_b64_tr_b16 v[184:185], v82 offset:8192\n"
    "ds_read_b64_tr_b16 v[186:187], v82 offset:9216\n"
    "ds_read_b64_tr_b16 v[188:189], v83 offset:8192\n"
    "ds_read_b64_tr_b16 v[190:191], v83 offset:9216\n"
    "s_waitcnt vmcnt(11) lgkmcnt(0)\n"
    "s_barrier\n"
    "v_mfma_f32_16x16x32_bf16 v[192:195], v[144:147], v[128:131], v[192:195]\n"
    "v_lshlrev_b32 v120, 7, v120\n"
    "v_mfma_f32_16x16x32_bf16 v[192:195], v[176:179], v[160:163], v[192:195]\n"
    "v_add_u32_dpp v52, v119, v48 quad_perm:[0,0,0,0] row_mask:0xf bank_mask:0xf\n"
    "v_mfma_f32_16x16x32_bf16 v[196:199], v[176:179], v[164:167], v[196:199]\n"
    "v_mfma_f32_16x16x32_bf16 v[196:199], v[144:147], v[132:135], v[196:199]\n"
    "v_add_u32_dpp v53, v119, v49 quad_perm:[1,1,1,1] row_mask:0xf bank_mask:0xf\n"
    "v_mfma_f32_16x16x32_bf16 v[200:203], v[144:147], v[136:139], v[200:203]\n"
    "v_mfma_f32_16x16x32_bf16 v[200:203], v[176:179], v[168:171], v[200:203]\n"
    "v_add_u32_dpp v54, v119, v50 quad_perm:[2,2,2,2] row_mask:0xf bank_mask:0xf\n"
    "v_mfma_f32_16x16x32_bf16 v[204:207], v[176:179], v[172:175], v[204:207]\n"
    "v_add_u32_dpp v55, v119, v51 quad_perm:[3,3,3,3] row_mask:0xf bank_mask:0xf\n"
    "v_mfma_f32_16x16x32_bf16 v[204:207], v[144:147], v[140:143], v[204:207]\n"
    "v_mfma_f32_16x16x32_bf16 v[220:223], v[148:151], v[140:143], v[220:223]\n"
    "v_lshrrev_b32 v22, 8, v26\n"
    "v_mfma_f32_16x16x32_bf16 v[220:223], v[180:183], v[172:175], v[220:223]\n"
    "v_mfma_f32_16x16x32_bf16 v[216:219], v[180:183], v[168:171], v[216:219]\n"
    "v_mov_b32 v26, s40\n"
    "v_mfma_f32_16x16x32_bf16 v[216:219], v[148:151], v[136:139], v[216:219]\n"
    "v_mfma_f32_16x16x32_bf16 v[212:215], v[148:151], v[132:135], v[212:215]\n"
    "v_mov_b32 v27, s41\n"
    "v_mfma_f32_16x16x32_bf16 v[212:215], v[180:183], v[164:167], v[212:215]\n"
    "v_add_u32_dpp v22, v22, v22 row_shl:4 row_mask:0xf bank_mask:0xf\n"
    "v_mfma_f32_16x16x32_bf16 v[208:211], v[180:183], v[160:163], v[208:211]\n"
    "v_mfma_f32_16x16x32_bf16 v[208:211], v[148:151], v[128:131], v[208:211]\n"
    "v_add_u32_e64 v43, s25, 1\n"
    "v_mfma_f32_16x16x32_bf16 v[224:227], v[152:155], v[128:131], v[224:227]\n"
    "v_mfma_f32_16x16x32_bf16 v[224:227], v[184:187], v[160:163], v[224:227]\n"
    "v_lshlrev_b32 v8, 2, v1\n"
    "v_mfma_f32_16x16x32_bf16 v[228:231], v[184:187], v[164:167], v[228:231]\n"
    "v_max_u32_dpp v13, v22, v22 quad_perm:[1,0,3,2] row_mask:0xf bank_mask:0xf\n"
    "v_mfma_f32_16x16x32_bf16 v[228:231], v[152:155], v[132:135], v[228:231]\n"
    "v_mfma_f32_16x16x32_bf16 v[232:235], v[152:155], v[136:139], v[232:235]\n"
    "v_add_u32 v8, 154112, v8\n"
    "v_mfma_f32_16x16x32_bf16 v[232:235], v[184:187], v[168:171], v[232:235]\n"
    "v_mfma_f32_16x16x32_bf16 v[236:239], v[184:187], v[172:175], v[236:239]\n"
    "s_nop 0\n"
    "v_mfma_f32_16x16x32_bf16 v[236:239], v[152:155], v[140:143], v[236:239]\n"
    "v_mfma_f32_16x16x32_bf16 v[252:255], v[156:159], v[140:143], v[252:255]\n"
    "v_max_u32_dpp v13, v13, v13 quad_perm:[2,3,0,1] row_mask:0xf bank_mask:0xf\n"
    "v_mfma_f32_16x16x32_bf16 v[252:255], v[188:191], v[172:175], v[252:255]\n"
    "v_sub_u32 v22, v22, v13\n"
    "v_mfma_f32_16x16x32_bf16 v[248:251], v[188:191], v[168:171], v[248:251]\n"
    "v_mfma_f32_16x16x32_bf16 v[248:251], v[156:159], v[136:139], v[248:251]\n"
    "v_subrev_u32 v13, 818, v13\n"
    "v_mfma_f32_16x16x32_bf16 v[244:247], v[156:159], v[132:135], v[244:247]\n"
    "v_mfma_f32_16x16x32_bf16 v[244:247], v[188:191], v[164:167], v[244:247]\n"
    "v_ldexp_f32 v22, 1.0, v22\n"
    "v_mfma_f32_16x16x32_bf16 v[240:243], v[188:191], v[160:163], v[240:243]\n"
    "v_mfma_f32_16x16x32_bf16 v[240:243], v[156:159], v[128:131], v[240:243]\n"
    "s_waitcnt vmcnt(7)\n"
    "s_barrier\n"
    "s_add_u32 m0, s35, 98304\n"
    "s_nop 0\n"
    "global_load_lds_dwordx4 v46, s[30:31] offset:0\n"
    "ds_read_b128 v[128:131], v56 offset:0\n"
    "ds_read_b128 v[132:135], v56 offset:2048\n"
    "ds_read_b128 v[136:139], v56 offset:4096\n"
    "ds_read_b128 v[140:143], v56 offset:6144\n"
    "global_load_lds_dwordx4 v47, s[30:31] offset:1024\n"
    "s_bfe_u32 s66, s29, 0x80008\n"
    "s_mul_i32 s66, s66, 53248\n"
    "s_add_u32 s30, s6, s66\n"
    "s_addc_u32 s31, s7, 0\n"
    "ds_read_b64_tr_b16 v[144:145], v76 offset:0\n"
    "ds_read_b64_tr_b16 v[146:147], v76 offset:1024\n"
    "ds_read_b64_tr_b16 v[148:149], v77 offset:0\n"
    "ds_read_b64_tr_b16 v[150:151], v77 offset:1024\n"
    "s_add_u32 m0, s34, 98304\n"
    "s_nop 0\n"
    "global_load_lds_dwordx4 v52, s[36:37] offset:0\n"
    "ds_read_b64_tr_b16 v[152:153], v78 offset:0\n"
    "ds_read_b64_tr_b16 v[154:155], v78 offset:1024\n"
    "ds_read_b64_tr_b16 v[156:157], v79 offset:0\n"
    "ds_read_b64_tr_b16 v[158:159], v79 offset:1024\n"
    "global_load_lds_dwordx4 v53, s[36:37] offset:1024\n"
    "ds_read_b128 v[160:163], v57 offset:0\n"
    "ds_read_b128 v[164:167], v57 offset:2048\n"
    "ds_read_b128 v[168:171], v57 offset:4096\n"
    "ds_read_b128 v[172:175], v57 offset:6144\n"
    "global_load_lds_dwordx4 v54, s[36:37] offset:2048\n"
    "ds_read_b64_tr_b16 v[176:177], v76 offset:8192\n"
    "ds_read_b64_tr_b16 v[178:179], v76 offset:9216\n"
    "ds_read_b64_tr_b16 v[180:181], v77 offset:8192\n"
    "ds_read_b64_tr_b16 v[182:183], v77 offset:9216\n"
    "global_load_lds_dwordx4 v55, s[36:37] offset:3072\n"
    "ds_read_b64_tr_b16 v[184:185], v78 offset:8192\n"
    "ds_read_b64_tr_b16 v[186:187], v78 offset:9216\n"
    "ds_read_b64_tr_b16 v[188:189], v79 offset:8192\n"
    "ds_read_b64_tr_b16 v[190:191], v79 offset:9216\n"
    "s_waitcnt vmcnt(10) lgkmcnt(0)\n"
    "s_barrier\n"
    "v_mfma_f32_16x16x32_bf16 v[192:195], v[144:147], v[128:131], v[192:195]\n"
    "v_lshlrev_b32 v121, 7, v121\n"
    "v_mfma_f32_16x16x32_bf16 v[192:195], v[176:179], v[160:163], v[192:195]\n"
    "v_add_u32_dpp v52, v120, v48 quad_perm:[0,0,0,0] row_mask:0xf bank_mask:0xf\n"
    "v_mfma_f32_16x16x32_bf16 v[196:199], v[176:179], v[164:167], v[196:199]\n"
    "v_mfma_f32_16x16x32_bf16 v[196:199], v[144:147], v[132:135], v[196:199]\n"
    "v_add_u32_dpp v53, v120, v49 quad_perm:[1,1,1,1] row_mask:0xf bank_mask:0xf\n"
    "v_mfma_f32_16x16x32_bf16 v[200:203], v[144:147], v[136:139], v[200:203]\n"
    "v_add_u32_dpp v54, v120, v50 quad_perm:[2,2,2,2] row_mask:0xf bank_mask:0xf\n"
    "v_mfma_f32_16x16x32_bf16 v[200:203], v[176:179], v[168:171], v[200:203]\n"
    "v_mfma_f32_16x16x32_bf16 v[204:207], v[176:179], v[172:175], v[204:207]\n"
    "v_add_u32_dpp v55, v120, v51 quad_perm:[3,3,3,3] row_mask:0xf bank_mask:0xf\n"
    "v_mfma_f32_16x16x32_bf16 v[204:207], v[144:147], v[140:143], v[204:207]\n"
    "v_mfma_f32_16x16x32_bf16 v[220:223], v[148:151], v[140:143], v[220:223]\n"
    "s_movk_i32 s43, 15744\n"
    "v_pk_maximum3_f16 v21, v28, v28, s43 op_sel_hi:[1,1,0] neg_lo:[0,1,0] neg_hi:[0,1,0]\n"
    "v_mfma_f32_16x16x32_bf16 v[220:223], v[180:183], v[172:175], v[220:223]\n"
    "v_pk_maximum3_f16 v21, v29, v29, v21 neg_lo:[0,1,0] neg_hi:[0,1,0]\n"
    "v_mfma_f32_16x16x32_bf16 v[216:219], v[180:183], v[168:171], v[216:219]\n"
    "v_mfma_f32_16x16x32_bf16 v[216:219], v[148:151], v[136:139], v[216:219]\n"
    "v_pk_maximum3_f16 v21, v30, v30, v21 neg_lo:[0,1,0] neg_hi:[0,1,0]\n"
    "v_mfma_f32_16x16x32_bf16 v[212:215], v[148:151], v[132:135], v[212:215]\n"
    "v_mfma_f32_16x16x32_bf16 v[212:215], v[180:183], v[164:167], v[212:215]\n"
    "v_pk_maximum3_f16 v21, v31, v31, v21 neg_lo:[0,1,0] neg_hi:[0,1,0]\n"
    "v_mfma_f32_16x16x32_bf16 v[208:211], v[180:183], v[160:163], v[208:211]\n"
    "v_pk_maximum3_f16 v21, v32, v32, v21 neg_lo:[0,1,0] neg_hi:[0,1,0]\n"
    "v_mfma_f32_16x16x32_bf16 v[208:211], v[148:151], v[128:131], v[208:211]\n"
    "v_mfma_f32_16x16x32_bf16 v[224:227], v[152:155], v[128:131], v[224:227]\n"
    "v_pk_maximum3_f16 v21, v33, v33, v21 neg_lo:[0,1,0] neg_hi:[0,1,0]\n"
    "v_mfma_f32_16x16x32_bf16 v[224:227], v[184:187], v[160:163], v[224:227]\n"
    "v_pk_maximum3_f16 v21, v34, v34, v21 neg_lo:[0,1,0] neg_hi:[0,1,0]\n"
    "v_mfma_f32_16x16x32_bf16 v[228:231], v[184:187], v[164:167], v[228:231]\n"
    "v_mfma_f32_16x16x32_bf16 v[228:231], v[152:155], v[132:135], v[228:231]\n"
    "v_pk_maximum3_f16 v21, v35, v35, v21 neg_lo:[0,1,0] neg_hi:[0,1,0]\n"
    "v_mfma_f32_16x16x32_bf16 v[232:235], v[152:155], v[136:139], v[232:235]\n"
    "v_mfma_f32_16x16x32_bf16 v[232:235], v[184:187], v[168:171], v[232:235]\n"
    "v_pk_maximum3_f16 v21, v36, v36, v21 neg_lo:[0,1,0] neg_hi:[0,1,0]\n"
    "v_mfma_f32_16x16x32_bf16 v[236:239], v[184:187], v[172:175], v[236:239]\n"
    "v_pk_maximum3_f16 v21, v37, v37, v21 neg_lo:[0,1,0] neg_hi:[0,1,0]\n"
    "v_mfma_f32_16x16x32_bf16 v[236:239], v[152:155], v[140:143], v[236:239]\n"
    "v_mfma_f32_16x16x32_bf16 v[252:255], v[156:159], v[140:143], v[252:255]\n"
    "v_pk_maximum3_f16 v21, v38, v38, v21 neg_lo:[0,1,0] neg_hi:[0,1,0]\n"
    "v_mfma_f32_16x16x32_bf16 v[252:255], v[188:191], v[172:175], v[252:255]\n"
    "v_mfma_f32_16x16x32_bf16 v[248:251], v[188:191], v[168:171], v[248:251]\n"
    "v_pk_maximum3_f16 v21, v39, v39, v21 neg_lo:[0,1,0] neg_hi:[0,1,0]\n"
    "v_mfma_f32_16x16x32_bf16 v[248:251], v[156:159], v[136:139], v[248:251]\n"
    "v_pk_maximum3_f16 v21, v40, v40, v21 neg_lo:[0,1,0] neg_hi:[0,1,0]\n"
    "v_mfma_f32_16x16x32_bf16 v[244:247], v[156:159], v[132:135], v[244:247]\n"
    "v_mfma_f32_16x16x32_bf16 v[244:247], v[188:191], v[164:167], v[244:247]\n"
    "v_pk_max_u16 v21, v21, v21 op_sel:[0,1] op_sel_hi:[1,0]\n"
    "v_mfma_f32_16x16x32_bf16 v[240:243], v[188:191], v[160:163], v[240:243]\n"
    "v_mfma_f32_16x16x32_bf16 v[240:243], v[156:159], v[128:131], v[240:243]\n"
    "s_waitcnt vmcnt(6)\n"
    "s_barrier\n"
    "s_add_u32 m0, s35, 0\n"
    "s_nop 0\n"
    "global_load_lds_dwordx4 v46, s[30:31] offset:0\n"
    "ds_read_b128 v[128:131], v56 offset:49152\n"
    "ds_read_b128 v[132:135], v56 offset:51200\n"
    "ds_read_b128 v[136:139], v56 offset:53248\n"
    "ds_read_b128 v[140:143], v56 offset:55296\n"
    "global_load_lds_dwordx4 v47, s[30:31] offset:1024\n"
    "s_add_u32 s30, s30, 212992\n"
    "s_addc_u32 s31, s31, 0\n"
    "ds_read_b64_tr_b16 v[144:145], v76 offset:49152\n"
    "ds_read_b64_tr_b16 v[146:147], v76 offset:50176\n"
    "ds_read_b64_tr_b16 v[148:149], v77 offset:49152\n"
    "ds_read_b64_tr_b16 v[150:151], v77 offset:50176\n"
    "s_add_u32 m0, s34, 0\n"
    "s_nop 0\n"
    "global_load_lds_dwordx4 v52, s[36:37] offset:0\n"
    "ds_read_b64_tr_b16 v[152:153], v78 offset:49152\n"
    "ds_read_b64_tr_b16 v[154:155], v78 offset:50176\n"
    "ds_read_b64_tr_b16 v[156:157], v79 offset:49152\n"
    "ds_read_b64_tr_b16 v[158:159], v79 offset:50176\n"
    "global_load_lds_dwordx4 v53, s[36:37] offset:1024\n"
    "ds_read_b128 v[160:163], v57 offset:49152\n"
    "ds_read_b128 v[164:167], v57 offset:51200\n"
    "ds_read_b128 v[168:171], v57 offset:53248\n"
    "ds_read_b128 v[172:175], v57 offset:55296\n"
    "global_load_lds_dwordx4 v54, s[36:37] offset:2048\n"
    "ds_read_b64_tr_b16 v[176:177], v76 offset:57344\n"
    "ds_read_b64_tr_b16 v[178:179], v76 offset:58368\n"
    "ds_read_b64_tr_b16 v[180:181], v77 offset:57344\n"
    "ds_read_b64_tr_b16 v[182:183], v77 offset:58368\n"
    "global_load_lds_dwordx4 v55, s[36:37] offset:3072\n"
    "ds_read_b64_tr_b16 v[184:185], v78 offset:57344\n"
    "ds_read_b64_tr_b16 v[186:187], v78 offset:58368\n"
    "ds_read_b64_tr_b16 v[188:189], v79 offset:57344\n"
    "ds_read_b64_tr_b16 v[190:191], v79 offset:58368\n"
    "s_waitcnt vmcnt(10) lgkmcnt(0)\n"
    "s_barrier\n"
    "v_mfma_f32_16x16x32_bf16 v[192:195], v[144:147], v[128:131], v[192:195]\n"
    "v_add_u32_dpp v52, v121, v48 quad_perm:[0,0,0,0] row_mask:0xf bank_mask:0xf\n"
    "v_mfma_f32_16x16x32_bf16 v[192:195], v[176:179], v[160:163], v[192:195]\n"
    "v_add_u32_dpp v53, v121, v49 quad_perm:[1,1,1,1] row_mask:0xf bank_mask:0xf\n"
    "v_mfma_f32_16x16x32_bf16 v[196:199], v[176:179], v[164:167], v[196:199]\n"
    "v_add_u32_dpp v54, v121, v50 quad_perm:[2,2,2,2] row_mask:0xf bank_mask:0xf\n"
    "v_mfma_f32_16x16x32_bf16 v[196:199], v[144:147], v[132:135], v[196:199]\n"
    "v_add_u32_dpp v55, v121, v51 quad_perm:[3,3,3,3] row_mask:0xf bank_mask:0xf\n"
    "v_mfma_f32_16x16x32_bf16 v[200:203], v[144:147], v[136:139], v[200:203]\n"
    "v_bfe_u32 v41, v1, 4, 1\n"
    "v_mfma_f32_16x16x32_bf16 v[200:203], v[176:179], v[168:171], v[200:203]\n"
    "v_lshlrev_b32 v41, 2, v41\n"
    "v_mfma_f32_16x16x32_bf16 v[204:207], v[176:179], v[172:175], v[204:207]\n"
    "v_add_u32 v41, 154112, v41\n"
    "v_mfma_f32_16x16x32_bf16 v[204:207], v[144:147], v[140:143], v[204:207]\n"
    "v_mfma_f32_16x16x32_bf16 v[220:223], v[148:151], v[140:143], v[220:223]\n"
    "v_max_u32_dpp v21, v21, v21 quad_perm:[1,0,3,2] row_mask:0xf bank_mask:0xf\n"
    "v_mfma_f32_16x16x32_bf16 v[220:223], v[180:183], v[172:175], v[220:223]\n"
    "v_mfma_f32_16x16x32_bf16 v[216:219], v[180:183], v[168:171], v[216:219]\n"
    "v_max_u32_dpp v21, v21, v21 quad_perm:[2,3,0,1] row_mask:0xf bank_mask:0xf\n"
    "v_mfma_f32_16x16x32_bf16 v[216:219], v[148:151], v[136:139], v[216:219]\n"
    "v_mfma_f32_16x16x32_bf16 v[212:215], v[148:151], v[132:135], v[212:215]\n"
    "v_max_u32_dpp v21, v21, v21 row_half_mirror row_mask:0xf bank_mask:0xf\n"
    "v_mfma_f32_16x16x32_bf16 v[212:215], v[180:183], v[164:167], v[212:215]\n"
    "v_mfma_f32_16x16x32_bf16 v[208:211], v[180:183], v[160:163], v[208:211]\n"
    "v_max_u32_dpp v21, v21, v21 row_mirror row_mask:0xf bank_mask:0xf\n"
    "v_mfma_f32_16x16x32_bf16 v[208:211], v[148:151], v[128:131], v[208:211]\n"
    "v_mfma_f32_16x16x32_bf16 v[224:227], v[152:155], v[128:131], v[224:227]\n"
    "v_max_u32_dpp v21, v21, v21 row_bcast:15 row_mask:0xa bank_mask:0xf\n"
    "v_mfma_f32_16x16x32_bf16 v[224:227], v[184:187], v[160:163], v[224:227]\n"
    "v_mfma_f32_16x16x32_bf16 v[228:231], v[184:187], v[164:167], v[228:231]\n"
    "v_max_u32_dpp v21, v21, v21 row_bcast:31 row_mask:0xc bank_mask:0xf\n"
    "v_mfma_f32_16x16x32_bf16 v[228:231], v[152:155], v[132:135], v[228:231]\n"
    "v_mfma_f32_16x16x32_bf16 v[232:235], v[152:155], v[136:139], v[232:235]\n"
    "v_readlane_b32 s47, v21, 63\n"
    "v_mfma_f32_16x16x32_bf16 v[232:235], v[184:187], v[168:171], v[232:235]\n"
    "v_mfma_f32_16x16x32_bf16 v[236:239], v[184:187], v[172:175], v[236:239]\n"
    "s_and_b32 s50, s47, 0x7f80\n"
    "v_mfma_f32_16x16x32_bf16 v[236:239], v[152:155], v[140:143], v[236:239]\n"
    "s_lshr_b32 s47, s50, 3\n"
    "v_mfma_f32_16x16x32_bf16 v[252:255], v[156:159], v[140:143], v[252:255]\n"
    "s_sub_u32 s50, s50, 2816\n"
    "v_mfma_f32_16x16x32_bf16 v[252:255], v[188:191], v[172:175], v[252:255]\n"
    "s_mul_i32 s51, s50, -8\n"
    "v_mfma_f32_16x16x32_bf16 v[248:251], v[188:191], v[168:171], v[248:251]\n"
    "v_mfma_f32_16x16x32_bf16 v[248:251], v[156:159], v[136:139], v[248:251]\n"
    "v_mfma_f32_16x16x32_bf16 v[244:247], v[156:159], v[132:135], v[244:247]\n"
    "v_mfma_f32_16x16x32_bf16 v[244:247], v[188:191], v[164:167], v[244:247]\n"
    "v_mfma_f32_16x16x32_bf16 v[240:243], v[188:191], v[160:163], v[240:243]\n"
    "v_mfma_f32_16x16x32_bf16 v[240:243], v[156:159], v[128:131], v[240:243]\n"
    "s_waitcnt vmcnt(6)\n"
    "s_barrier\n"
    "s_add_u32 m0, s35, 49152\n"
    "s_nop 0\n"
    "global_load_lds_dwordx4 v46, s[30:31] offset:0\n"
    "ds_read_b128 v[128:131], v58 offset:0\n"
    "ds_read_b128 v[132:135], v58 offset:2048\n"
    "ds_read_b128 v[136:139], v58 offset:4096\n"
    "ds_read_b128 v[140:143], v58 offset:6144\n"
    "global_load_lds_dwordx4 v47, s[30:31] offset:1024\n"
    "ds_read_b64_tr_b16 v[144:145], v80 offset:0\n"
    "ds_read_b64_tr_b16 v[146:147], v80 offset:1024\n"
    "ds_read_b64_tr_b16 v[148:149], v81 offset:0\n"
    "ds_read_b64_tr_b16 v[150:151], v81 offset:1024\n"
    "s_add_u32 m0, s34, 49152\n"
    "s_nop 0\n"
    "global_load_lds_dwordx4 v52, s[36:37] offset:0\n"
    "ds_read_b64_tr_b16 v[152:153], v82 offset:0\n"
    "ds_read_b64_tr_b16 v[154:155], v82 offset:1024\n"
    "ds_read_b64_tr_b16 v[156:157], v83 offset:0\n"
    "ds_read_b64_tr_b16 v[158:159], v83 offset:1024\n"
    "global_load_lds_dwordx4 v53, s[36:37] offset:1024\n"
    "ds_read_b128 v[160:163], v59 offset:0\n"
    "ds_read_b128 v[164:167], v59 offset:2048\n"
    "ds_read_b128 v[168:171], v59 offset:4096\n"
    "ds_read_b128 v[172:175], v59 offset:6144\n"
    "global_load_lds_dwordx4 v54, s[36:37] offset:2048\n"
    "ds_read_b64_tr_b16 v[176:177], v80 offset:8192\n"
    "ds_read_b64_tr_b16 v[178:179], v80 offset:9216\n"
    "ds_read_b64_tr_b16 v[180:181], v81 offset:8192\n"
    "ds_read_b64_tr_b16 v[182:183], v81 offset:9216\n"
    "global_load_lds_dwordx4 v55, s[36:37] offset:3072\n"
    "ds_read_b64_tr_b16 v[184:185], v82 offset:8192\n"
    "ds_read_b64_tr_b16 v[186:187], v82 offset:9216\n"
    "ds_read_b64_tr_b16 v[188:189], v83 offset:8192\n"
    "ds_read_b64_tr_b16 v[190:191], v83 offset:9216\n"
    "s_waitcnt vmcnt(10) lgkmcnt(0)\n"
    "s_barrier\n"
    "v_mfma_f32_16x16x32_bf16 v[192:195], v[144:147], v[128:131], v[192:195]\n"
    "v_pk_maximum3_f16 v21, v28, v28, s50 op_sel_hi:[1,1,0] neg_lo:[0,1,0] neg_hi:[0,1,0]\n"
    "v_mfma_f32_16x16x32_bf16 v[192:195], v[176:179], v[160:163], v[192:195]\n"
    "v_pk_mad_u16 v21, v21, 8, s51 op_sel_hi:[1,0,0]\n"
    "v_mfma_f32_16x16x32_bf16 v[196:199], v[176:179], v[164:167], v[196:199]\n"
    "v_bfi_b32 v28, s57, v21, v28\n"
    "v_mfma_f32_16x16x32_bf16 v[196:199], v[144:147], v[132:135], v[196:199]\n"
    "v_mfma_f32_16x16x32_bf16 v[200:203], v[144:147], v[136:139], v[200:203]\n"
    "v_pk_maximum3_f16 v12, v29, v29, s50 op_sel_hi:[1,1,0] neg_lo:[0,1,0] neg_hi:[0,1,0]\n"
    "v_mfma_f32_16x16x32_bf16 v[200:203], v[176:179], v[168:171], v[200:203]\n"
    "v_pk_mad_u16 v12, v12, 8, s51 op_sel_hi:[1,0,0]\n"
    "v_mfma_f32_16x16x32_bf16 v[204:207], v[176:179], v[172:175], v[204:207]\n"
    "v_bfi_b32 v29, s57, v12, v29\n"
    "v_mfma_f32_16x16x32_bf16 v[204:207], v[144:147], v[140:143], v[204:207]\n"
    "v_mfma_f32_16x16x32_bf16 v[220:223], v[148:151], v[140:143], v[220:223]\n"
    "v_pk_maximum3_f16 v21, v30, v30, s50 op_sel_hi:[1,1,0] neg_lo:[0,1,0] neg_hi:[0,1,0]\n"
    "v_mfma_f32_16x16x32_bf16 v[220:223], v[180:183], v[172:175], v[220:223]\n"
    "v_pk_mad_u16 v21, v21, 8, s51 op_sel_hi:[1,0,0]\n"
    "v_mfma_f32_16x16x32_bf16 v[216:219], v[180:183], v[168:171], v[216:219]\n"
    "v_bfi_b32 v30, s57, v21, v30\n"
    "v_mfma_f32_16x16x32_bf16 v[216:219], v[148:151], v[136:139], v[216:219]\n"
    "v_mfma_f32_16x16x32_bf16 v[212:215], v[148:151], v[132:135], v[212:215]\n"
    "v_pk_maximum3_f16 v12, v31, v31, s50 op_sel_hi:[1,1,0] neg_lo:[0,1,0] neg_hi:[0,1,0]\n"
    "v_mfma_f32_16x16x32_bf16 v[212:215], v[180:183], v[164:167], v[212:215]\n"
    "v_pk_mad_u16 v12, v12, 8, s51 op_sel_hi:[1,0,0]\n"
    "v_mfma_f32_16x16x32_bf16 v[208:211], v[180:183], v[160:163], v[208:211]\n"
    "v_bfi_b32 v31, s57, v12, v31\n"
    "v_mfma_f32_16x16x32_bf16 v[208:211], v[148:151], v[128:131], v[208:211]\n"
    "v_mfma_f32_16x16x32_bf16 v[224:227], v[152:155], v[128:131], v[224:227]\n"
    "v_pk_maximum3_f16 v21, v32, v32, s50 op_sel_hi:[1,1,0] neg_lo:[0,1,0] neg_hi:[0,1,0]\n"
    "v_mfma_f32_16x16x32_bf16 v[224:227], v[184:187], v[160:163], v[224:227]\n"
    "v_pk_mad_u16 v21, v21, 8, s51 op_sel_hi:[1,0,0]\n"
    "v_mfma_f32_16x16x32_bf16 v[228:231], v[184:187], v[164:167], v[228:231]\n"
    "v_bfi_b32 v32, s57, v21, v32\n"
    "v_mfma_f32_16x16x32_bf16 v[228:231], v[152:155], v[132:135], v[228:231]\n"
    "v_mfma_f32_16x16x32_bf16 v[232:235], v[152:155], v[136:139], v[232:235]\n"
    "v_pk_maximum3_f16 v12, v33, v33, s50 op_sel_hi:[1,1,0] neg_lo:[0,1,0] neg_hi:[0,1,0]\n"
    "v_mfma_f32_16x16x32_bf16 v[232:235], v[184:187], v[168:171], v[232:235]\n"
    "v_pk_mad_u16 v12, v12, 8, s51 op_sel_hi:[1,0,0]\n"
    "v_mfma_f32_16x16x32_bf16 v[236:239], v[184:187], v[172:175], v[236:239]\n"
    "v_bfi_b32 v33, s57, v12, v33\n"
    "v_mfma_f32_16x16x32_bf16 v[236:239], v[152:155], v[140:143], v[236:239]\n"
    "v_mfma_f32_16x16x32_bf16 v[252:255], v[156:159], v[140:143], v[252:255]\n"
    "v_pk_maximum3_f16 v21, v34, v34, s50 op_sel_hi:[1,1,0] neg_lo:[0,1,0] neg_hi:[0,1,0]\n"
    "v_mfma_f32_16x16x32_bf16 v[252:255], v[188:191], v[172:175], v[252:255]\n"
    "v_pk_mad_u16 v21, v21, 8, s51 op_sel_hi:[1,0,0]\n"
    "v_mfma_f32_16x16x32_bf16 v[248:251], v[188:191], v[168:171], v[248:251]\n"
    "v_bfi_b32 v34, s57, v21, v34\n"
    "v_mfma_f32_16x16x32_bf16 v[248:251], v[156:159], v[136:139], v[248:251]\n"
    "v_mfma_f32_16x16x32_bf16 v[244:247], v[156:159], v[132:135], v[244:247]\n"
    "v_lshlrev_b32 v52, 4, v1\n"
    "v_mfma_f32_16x16x32_bf16 v[244:247], v[188:191], v[164:167], v[244:247]\n"
    "v_add_u32 v53, 4096, v52\n"
    "v_mfma_f32_16x16x32_bf16 v[240:243], v[188:191], v[160:163], v[240:243]\n"
    "v_mfma_f32_16x16x32_bf16 v[240:243], v[156:159], v[128:131], v[240:243]\n"
    "s_waitcnt vmcnt(6)\n"
    "s_barrier\n"
    "s_mul_i32 s42, s20, 5376\n"
    "s_add_u32 s40, s42, 1048576\n"
    "s_add_u32 s38, s16, s40\n"
    "s_addc_u32 s39, s17, 0\n"
    "s_add_u32 m0, s42, 98304\n"
    "s_nop 0\n"
    "global_load_lds_dwordx4 v52, s[38:39] offset:0 sc1\n"
    "ds_read_b128 v[128:131], v56 offset:0\n"
    "ds_read_b128 v[132:135], v56 offset:2048\n"
    "ds_read_b128 v[136:139], v56 offset:4096\n"
    "ds_read_b128 v[140:143], v56 offset:6144\n"
    "global_load_lds_dwordx4 v52, s[38:39] offset:1024 sc1\n"
    "ds_read_b64_tr_b16 v[144:145], v76 offset:0\n"
    "ds_read_b64_tr_b16 v[146:147], v76 offset:1024\n"
    "ds_read_b64_tr_b16 v[148:149], v77 offset:0\n"
    "ds_read_b64_tr_b16 v[150:151], v77 offset:1024\n"
    "global_load_lds_dwordx4 v52, s[38:39] offset:2048 sc1\n"
    "ds_read_b64_tr_b16 v[152:153], v78 offset:0\n"
    "ds_read_b64_tr_b16 v[154:155], v78 offset:1024\n"
    "ds_read_b64_tr_b16 v[156:157], v79 offset:0\n"
    "ds_read_b64_tr_b16 v[158:159], v79 offset:1024\n"
    "global_load_lds_dwordx4 v52, s[38:39] offset:3072 sc1\n"
    "ds_read_b128 v[160:163], v57 offset:0\n"
    "ds_read_b128 v[164:167], v57 offset:2048\n"
    "ds_read_b128 v[168:171], v57 offset:4096\n"
    "ds_read_b128 v[172:175], v57 offset:6144\n"
    "s_add_u32 m0, s42, 102400\n"
    "s_nop 0\n"
    "global_load_lds_dwordx4 v53, s[38:39] offset:0 sc1\n"
    "ds_read_b64_tr_b16 v[176:177], v76 offset:8192\n"
    "ds_read_b64_tr_b16 v[178:179], v76 offset:9216\n"
    "ds_read_b64_tr_b16 v[180:181], v77 offset:8192\n"
    "ds_read_b64_tr_b16 v[182:183], v77 offset:9216\n"
    "global_load_lds_dwordx4 v53, s[38:39] offset:256 sc1\n"
    "ds_read_b64_tr_b16 v[184:185], v78 offset:8192\n"
    "ds_read_b64_tr_b16 v[186:187], v78 offset:9216\n"
    "ds_read_b64_tr_b16 v[188:189], v79 offset:8192\n"
    "ds_read_b64_tr_b16 v[190:191], v79 offset:9216\n"
    "s_waitcnt vmcnt(10) lgkmcnt(0)\n"
    "s_barrier\n"
    "v_mfma_f32_16x16x32_bf16 v[192:195], v[144:147], v[128:131], v[192:195]\n"
    "v_pk_maximum3_f16 v12, v35, v35, s50 op_sel_hi:[1,1,0] neg_lo:[0,1,0] neg_hi:[0,1,0]\n"
    "v_mfma_f32_16x16x32_bf16 v[192:195], v[176:179], v[160:163], v[192:195]\n"
    "v_pk_mad_u16 v12, v12, 8, s51 op_sel_hi:[1,0,0]\n"
    "v_mfma_f32_16x16x32_bf16 v[196:199], v[176:179], v[164:167], v[196:199]\n"
    "v_bfi_b32 v35, s57, v12, v35\n"
    "v_mfma_f32_16x16x32_bf16 v[196:199], v[144:147], v[132:135], v[196:199]\n"
    "v_pk_maximum3_f16 v21, v36, v36, s50 op_sel_hi:[1,1,0] neg_lo:[0,1,0] neg_hi:[0,1,0]\n"
    "v_mfma_f32_16x16x32_bf16 v[200:203], v[144:147], v[136:139], v[200:203]\n"
    "v_pk_mad_u16 v21, v21, 8, s51 op_sel_hi:[1,0,0]\n"
    "v_mfma_f32_16x16x32_bf16 v[200:203], v[176:179], v[168:171], v[200:203]\n"
    "v_bfi_b32 v36, s57, v21, v36\n"
    "v_mfma_f32_16x16x32_bf16 v[204:207], v[176:179], v[172:175], v[204:207]\n"
    "v_pk_maximum3_f16 v12, v37, v37, s50 op_sel_hi:[1,1,0] neg_lo:[0,1,0] neg_hi:[0,1,0]\n"
    "v_mfma_f32_16x16x32_bf16 v[204:207], v[144:147], v[140:143], v[204:207]\n"
    "v_mfma_f32_16x16x32_bf16 v[220:223], v[148:151], v[140:143], v[220:223]\n"
    "v_pk_mad_u16 v12, v12, 8, s51 op_sel_hi:[1,0,0]\n"
    "v_mfma_f32_16x16x32_bf16 v[220:223], v[180:183], v[172:175], v[220:223]\n"
    "v_bfi_b32 v37, s57, v12, v37\n"
    "v_mfma_f32_16x16x32_bf16 v[216:219], v[180:183], v[168:171], v[216:219]\n"
    "v_pk_maximum3_f16 v21, v38, v38, s50 op_sel_hi:[1,1,0] neg_lo:[0,1,0] neg_hi:[0,1,0]\n"
    "v_mfma_f32_16x16x32_bf16 v[216:219], v[148:151], v[136:139], v[216:219]\n"
    "v_pk_mad_u16 v21, v21, 8, s51 op_sel_hi:[1,0,0]\n"
    "v_mfma_f32_16x16x32_bf16 v[212:215], v[148:151], v[132:135], v[212:215]\n"
    "v_bfi_b32 v38, s57, v21, v38\n"
    "v_mfma_f32_16x16x32_bf16 v[212:215], v[180:183], v[164:167], v[212:215]\n"
    "v_pk_maximum3_f16 v12, v39, v39, s50 op_sel_hi:[1,1,0] neg_lo:[0,1,0] neg_hi:[0,1,0]\n"
    "v_mfma_f32_16x16x32_bf16 v[208:211], v[180:183], v[160:163], v[208:211]\n"
    "v_pk_mad_u16 v12, v12, 8, s51 op_sel_hi:[1,0,0]\n"
    "v_mfma_f32_16x16x32_bf16 v[208:211], v[148:151], v[128:131], v[208:211]\n"
    "v_mfma_f32_16x16x32_bf16 v[224:227], v[152:155], v[128:131], v[224:227]\n"
    "v_bfi_b32 v39, s57, v12, v39\n"
    "v_mfma_f32_16x16x32_bf16 v[224:227], v[184:187], v[160:163], v[224:227]\n"
    "v_pk_maximum3_f16 v21, v40, v40, s50 op_sel_hi:[1,1,0] neg_lo:[0,1,0] neg_hi:[0,1,0]\n"
    "v_mfma_f32_16x16x32_bf16 v[228:231], v[184:187], v[164:167], v[228:231]\n"
    "v_pk_mad_u16 v21, v21, 8, s51 op_sel_hi:[1,0,0]\n"
    "v_mfma_f32_16x16x32_bf16 v[228:231], v[152:155], v[132:135], v[228:231]\n"
    "v_bfi_b32 v40, s57, v21, v40\n"
    "v_mfma_f32_16x16x32_bf16 v[232:235], v[152:155], v[136:139], v[232:235]\n"
    "s_sub_u32 s43, 4112, s47\n"
    "s_lshl_b32 s43, s43, 19\n"
    "v_cvt_pkrtz_f16_f32 v12, s43, s43\n"
    "v_mfma_f32_16x16x32_bf16 v[232:235], v[184:187], v[168:171], v[232:235]\n"
    "v_lshl_or_b32 v21, v12, 16, s47\n"
    "v_mfma_f32_16x16x32_bf16 v[236:239], v[184:187], v[172:175], v[236:239]\n"
    "s_getpc_b64 s[66:67]\n"
    ".Lxd_en3:\n"
    "v_mfma_f32_16x16x32_bf16 v[236:239], v[152:155], v[140:143], v[236:239]\n"
    "v_mfma_f32_16x16x32_bf16 v[252:255], v[156:159], v[140:143], v[252:255]\n"
    "s_mov_b32 s40, .Lxd_ph1-.Lxd_en3\n"
    "v_mfma_f32_16x16x32_bf16 v[252:255], v[188:191], v[172:175], v[252:255]\n"
    "s_cmp_eq_u32 s22, 1\n"
    "s_cselect_b32 s40, .Lxd_ph0-.Lxd_en3, s40\n"
    "v_mfma_f32_16x16x32_bf16 v[248:251], v[188:191], v[168:171], v[248:251]\n"
    "s_cmp_eq_u32 s22, 2\n"
    "s_cselect_b32 s40, .Lxd_pl3-.Lxd_en3, s40\n"
    "v_mfma_f32_16x16x32_bf16 v[248:251], v[156:159], v[136:139], v[248:251]\n"
    "s_cmp_eq_u32 s22, 3\n"
    "s_cselect_b32 s40, .Lxd_pl2-.Lxd_en3, s40\n"
    "v_mfma_f32_16x16x32_bf16 v[244:247], v[156:159], v[132:135], v[244:247]\n"
    "s_ashr_i32 s41, s40, 31\n"
    "v_mfma_f32_16x16x32_bf16 v[244:247], v[188:191], v[164:167], v[244:247]\n"
    "s_add_u32 s66, s66, s40\n"
    "s_addc_u32 s67, s67, s41\n"
    "v_mfma_f32_16x16x32_bf16 v[240:243], v[188:191], v[160:163], v[240:243]\n"
    "v_mfma_f32_16x16x32_bf16 v[240:243], v[156:159], v[128:131], v[240:243]\n"
    "s_waitcnt vmcnt(6)\n"
    "s_barrier\n"
    "ds_read_b128 v[128:131], v56 offset:49152\n"
    "ds_read_b128 v[132:135], v56 offset:51200\n"
    "ds_read_b128 v[136:139], v56 offset:53248\n"
    "ds_read_b128 v[140:143], v56 offset:55296\n"
    "ds_read_b64_tr_b16 v[144:145], v76 offset:49152\n"
    "ds_read_b64_tr_b16 v[146:147], v76 offset:50176\n"
    "ds_read_b64_tr_b16 v[148:149], v77 offset:49152\n"
    "ds_read_b64_tr_b16 v[150:151], v77 offset:50176\n"
    "ds_read_b64_tr_b16 v[152:153], v78 offset:49152\n"
    "ds_read_b64_tr_b16 v[154:155], v78 offset:50176\n"
    "ds_read_b64_tr_b16 v[156:157], v79 offset:49152\n"
    "ds_read_b64_tr_b16 v[158:159], v79 offset:50176\n"
    "ds_read_b128 v[160:163], v57 offset:49152\n"
    "ds_read_b128 v[164:167], v57 offset:51200\n"
    "ds_read_b128 v[168:171], v57 offset:53248\n"
    "ds_read_b128 v[172:175], v57 offset:55296\n"
    "ds_read_b64_tr_b16 v[176:177], v76 offset:57344\n"
    "ds_read_b64_tr_b16 v[178:179], v76 offset:58368\n"
    "ds_read_b64_tr_b16 v[180:181], v77 offset:57344\n"
    "ds_read_b64_tr_b16 v[182:183], v77 offset:58368\n"
    "ds_read_b64_tr_b16 v[184:185], v78 offset:57344\n"
    "ds_read_b64_tr_b16 v[186:187], v78 offset:58368\n"
    "ds_read_b64_tr_b16 v[188:189], v79 offset:57344\n"
    "ds_read_b64_tr_b16 v[190:191], v79 offset:58368\n"
    "ds_write_b32 v92, v93\n"
    "s_cmp_lg_u32 s20, 3\n"
    "s_cbranch_scc1 .Lxd_on4\n"
    "s_mov_b64 exec, 1\n"
    "ds_write_b64 v8, v[26:27]\n"
    "s_mov_b64 exec, 15\n"
    "ds_write2_b32 v8, v22, v13 offset0:4 offset1:8\n"
    "s_mov_b64 exec, -1\n"
    ".Lxd_on4:\n"
    "s_waitcnt vmcnt(0) lgkmcnt(0)\n"
    "s_cmp_lg_u32 s20, 3\n"
    "s_cbranch_scc1 .Lxd_on5\n"
    "s_mov_b64 exec, 0x200\n"
    "global_store_dword v11, v43, s[16:17] sc1\n"
    "s_mov_b64 exec, -1\n"
    ".Lxd_on5:\n"
    "s_barrier\n"
    "s_setprio 3\n"
    "v_mfma_f32_16x16x32_bf16 v[192:195], v[144:147], v[128:131], v[192:195]\n"
    "v_add_u32 v42, 98304, v52\n"
    "v_mfma_f32_16x16x32_bf16 v[192:195], v[176:179], v[160:163], v[192:195]\n"
    "s_mov_b32 s64, 0xbdd2d3e8\n"
    "v_mfma_f32_16x16x32_bf16 v[196:199], v[176:179], v[164:167], v[196:199]\n"
    "s_mov_b32 s65, 0xc0135761\n"
    "v_mfma_f32_16x16x32_bf16 v[196:199], v[144:147], v[132:135], v[196:199]\n"
    "s_cmp_lt_u32 s22, 2\n"
    "v_mfma_f32_16x16x32_bf16 v[200:203], v[144:147], v[136:139], v[200:203]\n"
    "s_cselect_b32 s43, 16, 0\n"
    "v_mfma_f32_16x16x32_bf16 v[200:203], v[176:179], v[168:171], v[200:203]\n"
    "v_mfma_f32_16x16x32_bf16 v[204:207], v[176:179], v[172:175], v[204:207]\n"
    "s_and_b32 s41, s20, 1\n"
    "v_mfma_f32_16x16x32_bf16 v[204:207], v[144:147], v[140:143], v[204:207]\n"
    "s_lshl1_add_u32 s40, s21, s41\n"
    "v_mfma_f32_16x16x32_bf16 v[220:223], v[148:151], v[140:143], v[220:223]\n"
    "s_lshl_b32 s41, s40, 8\n"
    "v_mfma_f32_16x16x32_bf16 v[220:223], v[180:183], v[172:175], v[220:223]\n"
    "s_add_u32 s41, s41, 149504\n"
    "v_mfma_f32_16x16x32_bf16 v[216:219], v[180:183], v[168:171], v[216:219]\n"
    "v_mfma_f32_16x16x32_bf16 v[216:219], v[148:151], v[136:139], v[216:219]\n"
    "v_lshl_add_u32 v11, v1, 2, s41\n"
    "v_mfma_f32_16x16x32_bf16 v[212:215], v[148:151], v[132:135], v[212:215]\n"
    "s_lshl_b32 s40, s40, 6\n"
    "v_mfma_f32_16x16x32_bf16 v[212:215], v[180:183], v[164:167], v[212:215]\n"
    "s_add_u32 s40, s40, s24\n"
    "v_mfma_f32_16x16x32_bf16 v[208:211], v[180:183], v[160:163], v[208:211]\n"
    "s_lshl_b32 s41, s23, 12\n"
    "v_mfma_f32_16x16x32_bf16 v[208:211], v[148:151], v[128:131], v[208:211]\n"
    "v_mfma_f32_16x16x32_bf16 v[224:227], v[152:155], v[128:131], v[224:227]\n"
    "s_add_u32 s40, s40, s41\n"
    "v_mfma_f32_16x16x32_bf16 v[224:227], v[184:187], v[160:163], v[224:227]\n"
    "s_lshl3_add_u32 s40, s40, 65536\n"
    "v_mfma_f32_16x16x32_bf16 v[228:231], v[184:187], v[164:167], v[228:231]\n"
    "s_add_u32 s34, s40, s16\n"
    "v_mfma_f32_16x16x32_bf16 v[228:231], v[152:155], v[132:135], v[228:231]\n"
    "s_addc_u32 s35, s17, 0\n"
    "v_mfma_f32_16x16x32_bf16 v[232:235], v[152:155], v[136:139], v[232:235]\n"
    "v_mfma_f32_16x16x32_bf16 v[232:235], v[184:187], v[168:171], v[232:235]\n"
    "v_mov_b32 v13, s58\n"
    "v_mfma_f32_16x16x32_bf16 v[236:239], v[184:187], v[172:175], v[236:239]\n"
    "s_mov_b32 s36, 0\n"
    "v_mfma_f32_16x16x32_bf16 v[236:239], v[152:155], v[140:143], v[236:239]\n"
    "s_sub_u32 s40, 1, s22\n"
    "v_mfma_f32_16x16x32_bf16 v[252:255], v[156:159], v[140:143], v[252:255]\n"
    "s_and_b32 s40, s40, 3\n"
    "v_mfma_f32_16x16x32_bf16 v[252:255], v[188:191], v[172:175], v[252:255]\n"
    "v_mfma_f32_16x16x32_bf16 v[248:251], v[188:191], v[168:171], v[248:251]\n"
    "v_lshl_add_u32 v7, s40, 12, v3\n"
    "v_mfma_f32_16x16x32_bf16 v[248:251], v[156:159], v[136:139], v[248:251]\n"
    "v_lshl_add_u32 v8, s40, 12, v4\n"
    "v_mfma_f32_16x16x32_bf16 v[244:247], v[156:159], v[132:135], v[244:247]\n"
    "v_lshl_add_u32 v9, s40, 12, v5\n"
    "v_mfma_f32_16x16x32_bf16 v[244:247], v[188:191], v[164:167], v[244:247]\n"
    "v_lshl_add_u32 v10, s40, 12, v6\n"
    "v_mfma_f32_16x16x32_bf16 v[240:243], v[188:191], v[160:163], v[240:243]\n"
    "v_mfma_f32_16x16x32_bf16 v[240:243], v[156:159], v[128:131], v[240:243]\n"
    "s_cmp_lg_u32 s21, 0\n"
    "s_cbranch_scc1 .Lxd_g1\n"
    "s_barrier\n"
    ".Lxd_g1:\n"
    "s_setprio 2\n"
    "ds_write_b32 v25, v24 offset:20\n"
    "ds_write_b128 v15, v[28:31] offset:0\n"
    "ds_write_b128 v15, v[32:35] offset:144\n"
    "ds_write_b128 v15, v[36:39] offset:288\n"
    "ds_write_b32 v16, v40 offset:3456\n"
    "ds_write_b32 v16, v12 offset:3744\n"
    "s_mov_b64 exec, 1\n"
    "ds_or_b32 v25, v21 offset:532\n"
    "s_mov_b64 exec, -1\n"
    "ds_read2_b32 v[26:27], v41 offset1:1\n"
    "ds_read_b32 v0, v41 offset:16\n"
    "ds_read_b32 v23, v41 offset:24\n"
    "s_load_dword s26, s[68:69], 0\n"
    "v_add_u32 v25, s43, v25\n"
    "ds_read_b128 v[92:95], v42 offset:0\n"
    "ds_read_b128 v[96:99], v42 offset:1024\n"
    "ds_read_b128 v[100:103], v42 offset:2048\n"
    "ds_read_b128 v[104:107], v42 offset:3072\n"
    "ds_read_b128 v[108:111], v42 offset:4096\n"
    "ds_read_b128 v[112:115], v42 offset:5120\n"
    "ds_read_b128 v[116:119], v42 offset:6144\n"
    "ds_read_b128 v[120:123], v42 offset:7168\n"
    "ds_read_b128 v[124:127], v42 offset:8192\n"
    "v_pk_mul_f32 v[76:77], v[192:193], v[192:193]\n"
    "v_pk_mul_f32 v[78:79], v[196:197], v[196:197]\n"
    "v_pk_mul_f32 v[80:81], v[200:201], v[200:201]\n"
    "v_pk_mul_f32 v[82:83], v[204:205], v[204:205]\n"
    "v_pk_fma_f32 v[76:77], v[76:77], s[64:65], s[64:65] op_sel:[0,0,1] op_sel_hi:[1,0,1]\n"
    "v_pk_fma_f32 v[78:79], v[78:79], s[64:65], s[64:65] op_sel:[0,0,1] op_sel_hi:[1,0,1]\n"
    "v_pk_fma_f32 v[80:81], v[80:81], s[64:65], s[64:65] op_sel:[0,0,1] op_sel_hi:[1,0,1]\n"
    "v_pk_fma_f32 v[82:83], v[82:83], s[64:65], s[64:65] op_sel:[0,0,1] op_sel_hi:[1,0,1]\n"
    "v_pk_mul_f32 v[76:77], v[76:77], v[192:193]\n"
    "v_pk_mul_f32 v[78:79], v[78:79], v[196:197]\n"
    "v_pk_mul_f32 v[80:81], v[80:81], v[200:201]\n"
    "v_pk_mul_f32 v[82:83], v[82:83], v[204:205]\n"
    "v_exp_f32 v76, v76\n"
    "v_exp_f32 v77, v77\n"
    "ds_read_b128 v[128:131], v42 offset:9216\n"
    "v_exp_f32 v78, v78\n"
    "v_exp_f32 v79, v79\n"
    "v_exp_f32 v80, v80\n"
    "ds_read_b128 v[132:135], v42 offset:10240\n"
    "v_exp_f32 v81, v81\n"
    "v_exp_f32 v82, v82\n"
    "v_exp_f32 v83, v83\n"
    "ds_read_b128 v[136:139], v42 offset:11264\n"
    "v_pk_mul_f32 v[192:193], v[192:193], v[60:61]\n"
    "v_pk_mul_f32 v[196:197], v[196:197], v[60:61]\n"
    "v_pk_mul_f32 v[200:201], v[200:201], v[60:61]\n"
    "v_pk_mul_f32 v[204:205], v[204:205], v[60:61]\n"
    "v_pk_add_f32 v[76:77], v[76:77], 1.0 op_sel_hi:[1,0]\n"
    "v_pk_add_f32 v[78:79], v[78:79], 1.0 op_sel_hi:[1,0]\n"
    "v_pk_add_f32 v[80:81], v[80:81], 1.0 op_sel_hi:[1,0]\n"
    "v_pk_add_f32 v[82:83], v[82:83], 1.0 op_sel_hi:[1,0]\n"
    "v_rcp_f32 v76, v76\n"
    "v_rcp_f32 v77, v77\n"
    "ds_read_b128 v[140:143], v42 offset:12288\n"
    "v_rcp_f32 v78, v78\n"
    "v_rcp_f32 v79, v79\n"
    "v_rcp_f32 v80, v80\n"
    "v_rcp_f32 v81, v81\n"
    "ds_read_b128 v[144:147], v42 offset:13312\n"
    "v_rcp_f32 v82, v82\n"
    "v_rcp_f32 v83, v83\n"
    "v_pk_mul_f32 v[84:85], v[192:193], v[76:77]\n"
    "v_pk_mul_f32 v[86:87], v[196:197], v[78:79]\n"
    "v_pk_mul_f32 v[88:89], v[200:201], v[80:81]\n"
    "v_pk_mul_f32 v[90:91], v[204:205], v[82:83]\n"
    "v_pk_mul_f32 v[76:77], v[194:195], v[194:195]\n"
    "v_pk_mul_f32 v[78:79], v[198:199], v[198:199]\n"
    "v_pk_mul_f32 v[80:81], v[202:203], v[202:203]\n"
    "v_pk_mul_f32 v[82:83], v[206:207], v[206:207]\n"
    "v_pk_fma_f32 v[76:77], v[76:77], s[64:65], s[64:65] op_sel:[0,0,1] op_sel_hi:[1,0,1]\n"
    "v_pk_fma_f32 v[78:79], v[78:79], s[64:65], s[64:65] op_sel:[0,0,1] op_sel_hi:[1,0,1]\n"
    "v_pk_fma_f32 v[80:81], v[80:81], s[64:65], s[64:65] op_sel:[0,0,1] op_sel_hi:[1,0,1]\n"
    "v_pk_fma_f32 v[82:83], v[82:83], s[64:65], s[64:65] op_sel:[0,0,1] op_sel_hi:[1,0,1]\n"
    "v_pk_mul_f32 v[76:77], v[76:77], v[194:195]\n"
    "v_pk_mul_f32 v[78:79], v[78:79], v[198:199]\n"
    "v_pk_mul_f32 v[80:81], v[80:81], v[202:203]\n"
    "v_pk_mul_f32 v[82:83], v[82:83], v[206:207]\n"
    "v_exp_f32 v76, v76\n"
    "v_exp_f32 v77, v77\n"
    "ds_read_b128 v[148:151], v42 offset:14336\n"
    "v_exp_f32 v78, v78\n"
    "v_exp_f32 v79, v79\n"
    "v_exp_f32 v80, v80\n"
    "ds_read_b128 v[152:155], v42 offset:15360\n"
    "v_exp_f32 v81, v81\n"
    "v_exp_f32 v82, v82\n"
    "v_exp_f32 v83, v83\n"
    "ds_read_b128 v[156:159], v42 offset:16384\n"
    "v_pk_mul_f32 v[194:195], v[194:195], v[62:63]\n"
    "v_pk_mul_f32 v[198:199], v[198:199], v[62:63]\n"
    "v_pk_mul_f32 v[202:203], v[202:203], v[62:63]\n"
    "v_pk_mul_f32 v[206:207], v[206:207], v[62:63]\n"
    "v_pk_add_f32 v[76:77], v[76:77], 1.0 op_sel_hi:[1,0]\n"
    "v_pk_add_f32 v[78:79], v[78:79], 1.0 op_sel_hi:[1,0]\n"
    "v_pk_add_f32 v[80:81], v[80:81], 1.0 op_sel_hi:[1,0]\n"
    "v_pk_add_f32 v[82:83], v[82:83], 1.0 op_sel_hi:[1,0]\n"
    "v_rcp_f32 v76, v76\n"
    "v_rcp_f32 v77, v77\n"
    "ds_read_b128 v[160:163], v42 offset:17408\n"
    "v_rcp_f32 v78, v78\n"
    "v_rcp_f32 v79, v79\n"
    "v_rcp_f32 v80, v80\n"
    "v_rcp_f32 v81, v81\n"
    "ds_read_b128 v[164:167], v42 offset:18432\n"
    "v_rcp_f32 v82, v82\n"
    "v_rcp_f32 v83, v83\n"
    "v_pk_fma_f32 v[84:85], v[194:195], v[76:77], v[84:85]\n"
    "v_pk_fma_f32 v[86:87], v[198:199], v[78:79], v[86:87]\n"
    "v_pk_fma_f32 v[88:89], v[202:203], v[80:81], v[88:89]\n"
    "v_pk_fma_f32 v[90:91], v[206:207], v[82:83], v[90:91]\n"
    "ds_read_b32 v22, v13 offset:512\n"
    "v_pk_mul_f32 v[76:77], v[208:209], v[208:209]\n"
    "v_pk_mul_f32 v[78:79], v[212:213], v[212:213]\n"
    "v_pk_mul_f32 v[80:81], v[216:217], v[216:217]\n"
    "v_pk_mul_f32 v[82:83], v[220:221], v[220:221]\n"
    "v_pk_fma_f32 v[76:77], v[76:77], s[64:65], s[64:65] op_sel:[0,0,1] op_sel_hi:[1,0,1]\n"
    "v_pk_fma_f32 v[78:79], v[78:79], s[64:65], s[64:65] op_sel:[0,0,1] op_sel_hi:[1,0,1]\n"
    "v_pk_fma_f32 v[80:81], v[80:81], s[64:65], s[64:65] op_sel:[0,0,1] op_sel_hi:[1,0,1]\n"
    "v_pk_fma_f32 v[82:83], v[82:83], s[64:65], s[64:65] op_sel:[0,0,1] op_sel_hi:[1,0,1]\n"
    "v_pk_mul_f32 v[76:77], v[76:77], v[208:209]\n"
    "v_pk_mul_f32 v[78:79], v[78:79], v[212:213]\n"
    "v_pk_mul_f32 v[80:81], v[80:81], v[216:217]\n"
    "v_pk_mul_f32 v[82:83], v[82:83], v[220:221]\n"
    "v_exp_f32 v76, v76\n"
    "v_exp_f32 v77, v77\n"
    "ds_read_b128 v[168:171], v42 offset:19456\n"
    "v_exp_f32 v78, v78\n"
    "v_exp_f32 v79, v79\n"
    "v_exp_f32 v80, v80\n"
    "ds_read_b128 v[172:175], v42 offset:20480\n"
    "v_exp_f32 v81, v81\n"
    "v_exp_f32 v82, v82\n"
    "v_exp_f32 v83, v83\n"
    "ds_read_b128 v[176:179], v42 offset:21504\n"
    "v_pk_mul_f32 v[208:209], v[208:209], v[64:65]\n"
    "v_pk_mul_f32 v[212:213], v[212:213], v[64:65]\n"
    "v_pk_mul_f32 v[216:217], v[216:217], v[64:65]\n"
    "v_pk_mul_f32 v[220:221], v[220:221], v[64:65]\n"
    "v_pk_add_f32 v[76:77], v[76:77], 1.0 op_sel_hi:[1,0]\n"
    "v_pk_add_f32 v[78:79], v[78:79], 1.0 op_sel_hi:[1,0]\n"
    "v_pk_add_f32 v[80:81], v[80:81], 1.0 op_sel_hi:[1,0]\n"
    "v_pk_add_f32 v[82:83], v[82:83], 1.0 op_sel_hi:[1,0]\n"
    "v_rcp_f32 v76, v76\n"
    "v_rcp_f32 v77, v77\n"
    "ds_read_b128 v[180:183], v42 offset:22528\n"
    "v_rcp_f32 v78, v78\n"
    "v_rcp_f32 v79, v79\n"
    "v_rcp_f32 v80, v80\n"
    "v_rcp_f32 v81, v81\n"
    "ds_read_b128 v[184:187], v42 offset:23552\n"
    "v_rcp_f32 v82, v82\n"
    "v_rcp_f32 v83, v83\n"
    "v_pk_fma_f32 v[84:85], v[208:209], v[76:77], v[84:85]\n"
    "v_pk_fma_f32 v[86:87], v[212:213], v[78:79], v[86:87]\n"
    "v_pk_fma_f32 v[88:89], v[216:217], v[80:81], v[88:89]\n"
    "v_pk_fma_f32 v[90:91], v[220:221], v[82:83], v[90:91]\n"
    "s_waitcnt lgkmcnt(5)\n"
    "v_readfirstlane_b32 s45, v22\n"
    "s_cmp_lt_i32 s45, 16\n"
    "s_cbranch_scc1 .Lxd_ck6\n"
    ".Lxd_ck6r:\n"
    "ds_read_b64_tr_b16 v[44:45], v9 offset:0\n"
    "ds_read_b64_tr_b16 v[46:47], v10 offset:0\n"
    "ds_read_b64_tr_b16 v[48:49], v7 offset:0\n"
    "ds_read_b64_tr_b16 v[50:51], v8 offset:576\n"
    "ds_read_b64_tr_b16 v[52:53], v8 offset:1152\n"
    "ds_read_b64_tr_b16 v[54:55], v8 offset:1728\n"
    "ds_read_b64_tr_b16 v[56:57], v8 offset:2304\n"
    "ds_read_b64_tr_b16 v[58:59], v8 offset:2880\n"
    "v_readfirstlane_b32 s28, v26\n"
    "v_readfirstlane_b32 s29, v27\n"
    "s_cmp_eq_u32 s22, 0\n"
    "s_cbranch_scc1 .Lxd_on7\n"
    ".Lxd_on7r:\n"
    "s_setprio 1\n"
    "v_pk_mul_f32 v[76:77], v[210:211], v[210:211]\n"
    "v_pk_mul_f32 v[78:79], v[214:215], v[214:215]\n"
    "v_pk_mul_f32 v[80:81], v[218:219], v[218:219]\n"
    "v_pk_mul_f32 v[82:83], v[222:223], v[222:223]\n"
    "v_pk_fma_f32 v[76:77], v[76:77], s[64:65], s[64:65] op_sel:[0,0,1] op_sel_hi:[1,0,1]\n"
    "v_pk_fma_f32 v[78:79], v[78:79], s[64:65], s[64:65] op_sel:[0,0,1] op_sel_hi:[1,0,1]\n"
    "v_pk_fma_f32 v[80:81], v[80:81], s[64:65], s[64:65] op_sel:[0,0,1] op_sel_hi:[1,0,1]\n"
    "v_pk_fma_f32 v[82:83], v[82:83], s[64:65], s[64:65] op_sel:[0,0,1] op_sel_hi:[1,0,1]\n"
    "v_pk_mul_f32 v[76:77], v[76:77], v[210:211]\n"
    "v_pk_mul_f32 v[78:79], v[78:79], v[214:215]\n"
    "v_pk_mul_f32 v[80:81], v[80:81], v[218:219]\n"
    "v_pk_mul_f32 v[82:83], v[82:83], v[222:223]\n"
    "v_exp_f32 v76, v76\n"
    "v_exp_f32 v77, v77\n"
    "ds_read_b128 v[188:191], v42 offset:24576\n"
    "v_exp_f32 v78, v78\n"
    "v_exp_f32 v79, v79\n"
    "v_exp_f32 v80, v80\n"
    "ds_read_b128 v[192:195], v42 offset:25600\n"
    "v_exp_f32 v81, v81\n"
    "v_exp_f32 v82, v82\n"
    "v_exp_f32 v83, v83\n"
    "ds_read_b128 v[196:199], v42 offset:26624\n"
    "v_pk_mul_f32 v[210:211], v[210:211], v[66:67]\n"
    "v_pk_mul_f32 v[214:215], v[214:215], v[66:67]\n"
    "v_pk_mul_f32 v[218:219], v[218:219], v[66:67]\n"
    "v_pk_mul_f32 v[222:223], v[222:223], v[66:67]\n"
    "v_pk_add_f32 v[76:77], v[76:77], 1.0 op_sel_hi:[1,0]\n"
    "v_pk_add_f32 v[78:79], v[78:79], 1.0 op_sel_hi:[1,0]\n"
    "v_pk_add_f32 v[80:81], v[80:81], 1.0 op_sel_hi:[1,0]\n"
    "v_pk_add_f32 v[82:83], v[82:83], 1.0 op_sel_hi:[1,0]\n"
    "v_rcp_f32 v76, v76\n"
    "v_rcp_f32 v77, v77\n"
    "ds_read_b128 v[200:203], v42 offset:27648\n"
    "v_rcp_f32 v78, v78\n"
    "v_rcp_f32 v79, v79\n"
    "v_rcp_f32 v80, v80\n"
    "v_rcp_f32 v81, v81\n"
    "ds_read_b128 v[204:207], v42 offset:28672\n"
    "v_rcp_f32 v82, v82\n"
    "v_rcp_f32 v83, v83\n"
    "v_pk_fma_f32 v[84:85], v[210:211], v[76:77], v[84:85]\n"
    "v_pk_fma_f32 v[86:87], v[214:215], v[78:79], v[86:87]\n"
    "v_pk_fma_f32 v[88:89], v[218:219], v[80:81], v[88:89]\n"
    "v_pk_fma_f32 v[90:91], v[222:223], v[82:83], v[90:91]\n"
    "v_pk_mul_f32 v[76:77], v[224:225], v[224:225]\n"
    "v_pk_mul_f32 v[78:79], v[228:229], v[228:229]\n"
    "v_pk_mul_f32 v[80:81], v[232:233], v[232:233]\n"
    "v_pk_mul_f32 v[82:83], v[236:237], v[236:237]\n"
    "v_pk_fma_f32 v[76:77], v[76:77], s[64:65], s[64:65] op_sel:[0,0,1] op_sel_hi:[1,0,1]\n"
    "v_pk_fma_f32 v[78:79], v[78:79], s[64:65], s[64:65] op_sel:[0,0,1] op_sel_hi:[1,0,1]\n"
    "v_pk_fma_f32 v[80:81], v[80:81], s[64:65], s[64:65] op_sel:[0,0,1] op_sel_hi:[1,0,1]\n"
    "v_pk_fma_f32 v[82:83], v[82:83], s[64:65], s[64:65] op_sel:[0,0,1] op_sel_hi:[1,0,1]\n"
    "v_pk_mul_f32 v[76:77], v[76:77], v[224:225]\n"
    "v_pk_mul_f32 v[78:79], v[78:79], v[228:229]\n"
    "v_pk_mul_f32 v[80:81], v[80:81], v[232:233]\n"
    "v_pk_mul_f32 v[82:83], v[82:83], v[236:237]\n"
    "v_exp_f32 v76, v76\n"
    "v_exp_f32 v77, v77\n"
    "ds_read_b128 v[208:211], v42 offset:29696\n"
    "v_exp_f32 v78, v78\n"
    "v_exp_f32 v79, v79\n"
    "v_exp_f32 v80, v80\n"
    "ds_read_b128 v[212:215], v42 offset:30720\n"
    "v_exp_f32 v81, v81\n"
    "v_exp_f32 v82, v82\n"
    "v_exp_f32 v83, v83\n"
    "ds_read_b128 v[216:219], v42 offset:31744\n"
    "v_pk_mul_f32 v[224:225], v[224:225], v[68:69]\n"
    "v_pk_mul_f32 v[228:229], v[228:229], v[68:69]\n"
    "v_pk_mul_f32 v[232:233], v[232:233], v[68:69]\n"
    "v_pk_mul_f32 v[236:237], v[236:237], v[68:69]\n"
    "v_pk_add_f32 v[76:77], v[76:77], 1.0 op_sel_hi:[1,0]\n"
    "v_pk_add_f32 v[78:79], v[78:79], 1.0 op_sel_hi:[1,0]\n"
    "v_pk_add_f32 v[80:81], v[80:81], 1.0 op_sel_hi:[1,0]\n"
    "v_pk_add_f32 v[82:83], v[82:83], 1.0 op_sel_hi:[1,0]\n"
    "v_rcp_f32 v76, v76\n"
    "v_rcp_f32 v77, v77\n"
    "ds_read_b128 v[220:223], v42 offset:32768\n"
    "v_rcp_f32 v78, v78\n"
    "v_rcp_f32 v79, v79\n"
    "v_rcp_f32 v80, v80\n"
    "v_rcp_f32 v81, v81\n"
    "v_rcp_f32 v82, v82\n"
    "v_rcp_f32 v83, v83\n"
    "v_pk_fma_f32 v[84:85], v[224:225], v[76:77], v[84:85]\n"
    "v_pk_fma_f32 v[86:87], v[228:229], v[78:79], v[86:87]\n"
    "v_pk_fma_f32 v[88:89], v[232:233], v[80:81], v[88:89]\n"
    "v_pk_fma_f32 v[90:91], v[236:237], v[82:83], v[90:91]\n"
    "v_pk_mul_f32 v[76:77], v[226:227], v[226:227]\n"
    "v_pk_mul_f32 v[78:79], v[230:231], v[230:231]\n"
    "v_pk_mul_f32 v[80:81], v[234:235], v[234:235]\n"
    "v_pk_mul_f32 v[82:83], v[238:239], v[238:239]\n"
    "v_pk_fma_f32 v[76:77], v[76:77], s[64:65], s[64:65] op_sel:[0,0,1] op_sel_hi:[1,0,1]\n"
    "v_pk_fma_f32 v[78:79], v[78:79], s[64:65], s[64:65] op_sel:[0,0,1] op_sel_hi:[1,0,1]\n"
    "v_pk_fma_f32 v[80:81], v[80:81], s[64:65], s[64:65] op_sel:[0,0,1] op_sel_hi:[1,0,1]\n"
    "v_pk_fma_f32 v[82:83], v[82:83], s[64:65], s[64:65] op_sel:[0,0,1] op_sel_hi:[1,0,1]\n"
    "v_pk_mul_f32 v[76:77], v[76:77], v[226:227]\n"
    "v_pk_mul_f32 v[78:79], v[78:79], v[230:231]\n"
    "v_pk_mul_f32 v[80:81], v[80:81], v[234:235]\n"
    "v_pk_mul_f32 v[82:83], v[82:83], v[238:239]\n"
    "v_exp_f32 v76, v76\n"
    "v_exp_f32 v77, v77\n"
    "v_exp_f32 v78, v78\n"
    "v_exp_f32 v79, v79\n"
    "v_exp_f32 v80, v80\n"
    "v_exp_f32 v81, v81\n"
    "v_exp_f32 v82, v82\n"
    "v_exp_f32 v83, v83\n"
    "v_pk_mul_f32 v[226:227], v[226:227], v[70:71]\n"
    "v_pk_mul_f32 v[230:231], v[230:231], v[70:71]\n"
    "v_pk_mul_f32 v[234:235], v[234:235], v[70:71]\n"
    "v_pk_mul_f32 v[238:239], v[238:239], v[70:71]\n"
    "v_pk_add_f32 v[76:77], v[76:77], 1.0 op_sel_hi:[1,0]\n"
    "v_pk_add_f32 v[78:79], v[78:79], 1.0 op_sel_hi:[1,0]\n"
    "v_pk_add_f32 v[80:81], v[80:81], 1.0 op_sel_hi:[1,0]\n"
    "v_pk_add_f32 v[82:83], v[82:83], 1.0 op_sel_hi:[1,0]\n"
    "v_rcp_f32 v76, v76\n"
    "v_rcp_f32 v77, v77\n"
    "v_rcp_f32 v78, v78\n"
    "v_rcp_f32 v79, v79\n"
    "v_rcp_f32 v80, v80\n"
    "v_rcp_f32 v81, v81\n"
    "v_rcp_f32 v82, v82\n"
    "v_rcp_f32 v83, v83\n"
    "v_pk_fma_f32 v[84:85], v[226:227], v[76:77], v[84:85]\n"
    "v_pk_fma_f32 v[86:87], v[230:231], v[78:79], v[86:87]\n"
    "v_pk_fma_f32 v[88:89], v[234:235], v[80:81], v[88:89]\n"
    "v_pk_fma_f32 v[90:91], v[238:239], v[82:83], v[90:91]\n"
    "s_cmp_eq_u32 s22, 0\n"
    "s_cbranch_scc1 .Lxd_on9\n"
    ".Lxd_on9r:\n"
    "s_cmp_eq_u32 s22, 1\n"
    "s_cbranch_scc1 .Lxd_on10\n"
    ".Lxd_on10r:\n"
    "s_setprio 0\n"
    "v_pk_mul_f32 v[76:77], v[240:241], v[240:241]\n"
    "v_pk_mul_f32 v[78:79], v[244:245], v[244:245]\n"
    "v_pk_mul_f32 v[80:81], v[248:249], v[248:249]\n"
    "v_pk_mul_f32 v[82:83], v[252:253], v[252:253]\n"
    "v_pk_fma_f32 v[76:77], v[76:77], s[64:65], s[64:65] op_sel:[0,0,1] op_sel_hi:[1,0,1]\n"
    "v_pk_fma_f32 v[78:79], v[78:79], s[64:65], s[64:65] op_sel:[0,0,1] op_sel_hi:[1,0,1]\n"
    "v_pk_fma_f32 v[80:81], v[80:81], s[64:65], s[64:65] op_sel:[0,0,1] op_sel_hi:[1,0,1]\n"
    "v_pk_fma_f32 v[82:83], v[82:83], s[64:65], s[64:65] op_sel:[0,0,1] op_sel_hi:[1,0,1]\n"
    "v_pk_mul_f32 v[76:77], v[76:77], v[240:241]\n"
    "v_pk_mul_f32 v[78:79], v[78:79], v[244:245]\n"
    "v_pk_mul_f32 v[80:81], v[80:81], v[248:249]\n"
    "v_pk_mul_f32 v[82:83], v[82:83], v[252:253]\n"
    "v_exp_f32 v76, v76\n"
    "v_exp_f32 v77, v77\n"
    "ds_read_b128 v[224:227], v42 offset:33792\n"
    "v_exp_f32 v78, v78\n"
    "v_exp_f32 v79, v79\n"
    "v_exp_f32 v80, v80\n"
    "ds_read_b128 v[228:231], v42 offset:34816\n"
    "v_exp_f32 v81, v81\n"
    "v_exp_f32 v82, v82\n"
    "v_exp_f32 v83, v83\n"
    "ds_read_b128 v[232:235], v42 offset:35840\n"
    "v_pk_mul_f32 v[240:241], v[240:241], v[72:73]\n"
    "v_pk_mul_f32 v[244:245], v[244:245], v[72:73]\n"
    "v_pk_mul_f32 v[248:249], v[248:249], v[72:73]\n"
    "v_pk_mul_f32 v[252:253], v[252:253], v[72:73]\n"
    "v_pk_add_f32 v[76:77], v[76:77], 1.0 op_sel_hi:[1,0]\n"
    "v_pk_add_f32 v[78:79], v[78:79], 1.0 op_sel_hi:[1,0]\n"
    "v_pk_add_f32 v[80:81], v[80:81], 1.0 op_sel_hi:[1,0]\n"
    "v_pk_add_f32 v[82:83], v[82:83], 1.0 op_sel_hi:[1,0]\n"
    "v_rcp_f32 v76, v76\n"
    "v_rcp_f32 v77, v77\n"
    "ds_read_b128 v[236:239], v42 offset:36864\n"
    "v_rcp_f32 v78, v78\n"
    "v_rcp_f32 v79, v79\n"
    "v_rcp_f32 v80, v80\n"
    "v_rcp_f32 v81, v81\n"
    "v_rcp_f32 v82, v82\n"
    "v_rcp_f32 v83, v83\n"
    "v_pk_fma_f32 v[84:85], v[240:241], v[76:77], v[84:85]\n"
    "v_pk_fma_f32 v[86:87], v[244:245], v[78:79], v[86:87]\n"
    "v_pk_fma_f32 v[88:89], v[248:249], v[80:81], v[88:89]\n"
    "v_pk_fma_f32 v[90:91], v[252:253], v[82:83], v[90:91]\n"
    "v_pk_mul_f32 v[76:77], v[242:243], v[242:243]\n"
    "v_pk_mul_f32 v[78:79], v[246:247], v[246:247]\n"
    "v_pk_mul_f32 v[80:81], v[250:251], v[250:251]\n"
    "v_pk_mul_f32 v[82:83], v[254:255], v[254:255]\n"
    "v_pk_fma_f32 v[76:77], v[76:77], s[64:65], s[64:65] op_sel:[0,0,1] op_sel_hi:[1,0,1]\n"
    "v_pk_fma_f32 v[78:79], v[78:79], s[64:65], s[64:65] op_sel:[0,0,1] op_sel_hi:[1,0,1]\n"
    "v_pk_fma_f32 v[80:81], v[80:81], s[64:65], s[64:65] op_sel:[0,0,1] op_sel_hi:[1,0,1]\n"
    "v_pk_fma_f32 v[82:83], v[82:83], s[64:65], s[64:65] op_sel:[0,0,1] op_sel_hi:[1,0,1]\n"
    "v_pk_mul_f32 v[76:77], v[76:77], v[242:243]\n"
    "v_pk_mul_f32 v[78:79], v[78:79], v[246:247]\n"
    "v_pk_mul_f32 v[80:81], v[80:81], v[250:251]\n"
    "v_pk_mul_f32 v[82:83], v[82:83], v[254:255]\n"
    "v_exp_f32 v76, v76\n"
    "v_exp_f32 v77, v77\n"
    "v_exp_f32 v78, v78\n"
    "v_exp_f32 v79, v79\n"
    "v_exp_f32 v80, v80\n"
    "v_exp_f32 v81, v81\n"
    "v_exp_f32 v82, v82\n"
    "v_exp_f32 v83, v83\n"
    "v_pk_mul_f32 v[242:243], v[242:243], v[74:75]\n"
    "v_pk_mul_f32 v[246:247], v[246:247], v[74:75]\n"
    "v_pk_mul_f32 v[250:251], v[250:251], v[74:75]\n"
    "v_pk_mul_f32 v[254:255], v[254:255], v[74:75]\n"
    "v_pk_add_f32 v[76:77], v[76:77], 1.0 op_sel_hi:[1,0]\n"
    "v_pk_add_f32 v[78:79], v[78:79], 1.0 op_sel_hi:[1,0]\n"
    "v_pk_add_f32 v[80:81], v[80:81], 1.0 op_sel_hi:[1,0]\n"
    "v_pk_add_f32 v[82:83], v[82:83], 1.0 op_sel_hi:[1,0]\n"
    "v_rcp_f32 v76, v76\n"
    "v_rcp_f32 v77, v77\n"
    "v_rcp_f32 v78, v78\n"
    "v_rcp_f32 v79, v79\n"
    "v_rcp_f32 v80, v80\n"
    "v_rcp_f32 v81, v81\n"
    "v_rcp_f32 v82, v82\n"
    "v_rcp_f32 v83, v83\n"
    "v_pk_fma_f32 v[84:85], v[242:243], v[76:77], v[84:85]\n"
    "v_pk_fma_f32 v[86:87], v[246:247], v[78:79], v[86:87]\n"
    "v_pk_fma_f32 v[88:89], v[250:251], v[80:81], v[88:89]\n"
    "v_pk_fma_f32 v[90:91], v[254:255], v[82:83], v[90:91]\n"
    "ds_read_b128 v[240:243], v42 offset:37888\n"
    "ds_read_b128 v[244:247], v42 offset:38912\n"
    "ds_read_b128 v[248:251], v42 offset:39936\n"
    "ds_read_b128 v[252:255], v42 offset:40960\n"
    "s_cmp_eq_u32 s21, 0\n"
    "s_cbranch_scc1 .Lxd_gb0\n"
    "s_barrier\n"
    "s_setprio 3\n"
    ".Lxd_gb0:\n"
    "v_add_f32 v84, v84, v85\n"
    "v_add_f32 v86, v86, v87\n"
    "v_add_f32 v88, v88, v89\n"
    "v_add_f32 v90, v90, v91\n"
    "s_waitcnt lgkmcnt(15)\n"
    "v_pk_mul_f16 v60, v44, s45 op_sel:[0,1]\n"
    "v_pk_mul_f16 v61, v45, s45 op_sel:[0,1]\n"
    "v_permlane32_swap_b32 v84, v88\n"
    "v_permlane32_swap_b32 v86, v90\n"
    "v_add_f32 v84, v84, v88\n"
    "v_add_f32 v86, v86, v90\n"
    "v_pk_mul_f16 v62, v46, s45 op_sel:[0,1]\n"
    "v_pk_mul_f16 v63, v47, s45 op_sel:[0,1]\n"
    "v_permlane16_swap_b32 v84, v86\n"
    "v_add_f32 v84, v84, v86\n"
    "v_and_b32 v84, -2, v84\n"
    "ds_wrxchg_rtn_b32 v85, v11, v84\n"
    "v_pk_mul_f16 v64, v44, v46\n"
    "v_pk_mul_f16 v65, v45, v47\n"
    "v_pk_mul_f16 v66, v48, v48 op_sel_hi:[0,1]\n"
    "v_pk_mul_f16 v67, v48, v49 op_sel_hi:[0,1]\n"
    "v_lshlrev_b32 v86, 3, v1\n"
    "v_mov_b64 v[88:89], 0\n"
    "v_mov_b64 v[90:91], 0\n"
    "s_waitcnt lgkmcnt(0)\n"
    "v_cmp_ne_u32 vcc, -1, v85\n"
    "v_add_f32 v84, v84, v85\n"
    "v_add_u32 v84, 0x80, v84\n"
    "v_and_b32 v84, 0xffffff00, v84\n"
    "v_or_b32 v26, s28, v84\n"
    "v_mov_b32 v27, s29\n"
    "s_cbranch_vccz .Lxd_nost\n"
    "s_mov_b64 exec, vcc\n"
    "global_store_dwordx2 v86, v[26:27], s[34:35] sc1\n"
    "s_mov_b64 exec, -1\n"
    ".Lxd_nost:\n"
    "s_cmp_lg_u32 s21, 0\n"
    "s_cbranch_scc1 .Lxd_gb1\n"
    "s_barrier\n"
    ".Lxd_gb1:\n"
    ".Lxd_enter:\n"
    "s_cmp_eq_u32 s21, 0\n"
    "s_cselect_b64 vcc, 0, -1\n"
    "s_cbranch_scc1 .Lxd_pr\n"
    "s_setprio 1\n"
    ".Lxd_pr:\n"
    "s_cmp_lg_u32 s36, 0\n"
    "s_cbranch_scc1 .Lxd_pl1\n"
    "s_setpc_b64 s[66:67]\n"
    ".Lxd_ph0:\n"
    "s_cbranch_vccnz .Lxd_pr12\n"
    "s_setprio 2\n"
    ".Lxd_pr12:\n"
    "v_smfmac_f32_16x16x64_f16 v[88:91], v[112:115], v[60:67], v2\n"
    "v_fmac_f32 v11, v76, v80\n"
    "v_fmac_f32 v11, v77, v81\n"
    "v_fmac_f32 v11, v78, v82\n"
    "v_fmac_f32 v11, v79, v83\n"
    "v_mfma_f32_16x16x32_f16 v[80:83], v[92:95], v[60:63], 0\n"
    "v_pk_mul_f16 v68, v58, v58 op_sel_hi:[0,1]\n"
    "v_pk_mul_f16 v69, v58, v49 op_sel_hi:[0,1]\n"
    "v_mfma_f32_16x16x32_f16 v[80:83], v[104:107], v[64:67], v[80:83]\n"
    "v_pk_mul_f16 v70, v48, v58 op_sel_hi:[0,1]\n"
    "v_pk_mul_f16 v71, v48, v59 op_sel_hi:[0,1]\n"
    "v_mfma_f32_16x16x32_f16 v[76:79], v[100:103], v[64:67], 0\n"
    "v_fmac_f32 v11, v84, v85\n"
    "v_fma_f32 v10, v86, v87, v11\n"
    "v_mfma_f32_16x16x32_f16 v[84:87], v[108:111], v[64:67], 0\n"
    "v_pk_mul_f16 v72, v50, v58 op_sel_hi:[0,1]\n"
    "v_pk_mul_f16 v73, v50, v59 op_sel_hi:[0,1]\n"
    "v_mfma_f32_16x16x32_f16 v[84:87], v[96:99], v[60:63], v[84:87]\n"
    "v_pk_mul_f16 v74, v52, v58 op_sel_hi:[0,1]\n"
    "v_pk_mul_f16 v75, v52, v59 op_sel_hi:[0,1]\n"
    "s_waitcnt vmcnt(0)\n"
    "v_mfma_f32_16x16x32_f16 v[76:79], v[116:119], v[68:71], v[76:79]\n"
    "v_pk_mul_f16 v60, v54, v58 op_sel_hi:[0,1]\n"
    "v_pk_mul_f16 v61, v54, v59 op_sel_hi:[0,1]\n"
    "v_mfma_f32_16x16x32_f16 v[76:79], v[128:131], v[72:75], v[76:79]\n"
    "v_pk_mul_f16 v62, v56, v58 op_sel_hi:[0,1]\n"
    "v_readfirstlane_b32 s40, v24\n"
    "s_or_b32 s40, s40, s44\n"
    "s_lshl1_add_u32 s41, s40, s54\n"
    "s_cmpk_lt_u32 s40, 256\n"
    "s_cselect_b32 s41, s41, 2\n"
    "v_mov_b32 v12, s41\n"
    "s_cbranch_scc0 .Lxd_tf11\n"
    ".Lxd_tf11r:\n"
    "s_mul_i32 s41, s41, 52\n"
    "s_add_u32 s38, s4, s41\n"
    "s_addc_u32 s39, s5, 0\n"
    "v_mfma_f32_16x16x32_f16 v[80:83], v[132:135], v[72:75], v[80:83]\n"
    "v_pk_mul_f16 v63, v56, v59 op_sel_hi:[0,1]\n"
    "ds_write_b32 v25, v12 offset:20\n"
    "global_load_dwordx3 v[36:38], v17, s[38:39] offset:-104\n"
    "global_load_dword v41, v18, s[38:39] offset:-8\n"
    "v_mfma_f32_16x16x32_f16 v[80:83], v[120:123], v[68:71], v[80:83]\n"
    "v_pk_mul_f16 v64, v56, v56 op_sel_hi:[0,1]\n"
    "v_xor_b32 v15, 0x4000, v15\n"
    "v_mfma_f32_16x16x32_f16 v[84:87], v[124:127], v[68:71], v[84:87]\n"
    "v_pk_mul_f16 v65, v56, v49 op_sel_hi:[0,1]\n"
    "v_xor_b32 v16, 0x4000, v16\n"
    "v_mfma_f32_16x16x32_f16 v[84:87], v[136:139], v[72:75], v[84:87]\n"
    "v_pk_mul_f16 v66, v48, v56 op_sel_hi:[0,1]\n"
    "v_permlane32_swap_b32 v7, v8\n"
    "v_permlane32_swap_b32 v9, v10\n"
    "v_smfmac_f32_16x16x64_f16 v[88:91], v[140:143], v[68:75], v2\n"
    "v_pk_mul_f16 v67, v48, v57 op_sel_hi:[0,1]\n"
    "v_mul_f32 v7, v0, v7\n"
    "v_mul_f32 v9, v0, v9\n"
    "v_mfma_f32_16x16x32_f16 v[76:79], v[144:147], v[60:63], v[76:79]\n"
    "v_pk_mul_f16 v68, v50, v56 op_sel_hi:[0,1]\n"
    "v_pk_mul_f16 v69, v50, v57 op_sel_hi:[0,1]\n"
    "v_mfma_f32_16x16x32_f16 v[76:79], v[156:159], v[64:67], v[76:79]\n"
    "v_pk_mul_f16 v70, v52, v56 op_sel_hi:[0,1]\n"
    "v_fmac_f32 v7, v23, v8\n"
    "v_fmac_f32 v9, v23, v10\n"
    "v_mfma_f32_16x16x32_f16 v[80:83], v[160:163], v[64:67], v[80:83]\n"
    "v_pk_mul_f16 v71, v52, v57 op_sel_hi:[0,1]\n"
    "v_permlane16_swap_b32 v7, v9\n"
    "v_mfma_f32_16x16x32_f16 v[80:83], v[148:151], v[60:63], v[80:83]\n"
    "v_pk_mul_f16 v72, v54, v56 op_sel_hi:[0,1]\n"
    "ds_read_b32 v22, v25 offset:516\n"
    "v_mfma_f32_16x16x32_f16 v[84:87], v[152:155], v[60:63], v[84:87]\n"
    "v_pk_mul_f16 v73, v54, v57 op_sel_hi:[0,1]\n"
    "v_add_f32 v7, v7, v9\n"
    "v_mfma_f32_16x16x32_f16 v[84:87], v[164:167], v[64:67], v[84:87]\n"
    "v_pk_mul_f16 v74, v54, v54 op_sel_hi:[0,1]\n"
    "v_add_f32_dpp v7, v7, v7 quad_perm:[1,0,3,2] row_mask:0xf bank_mask:0xf\n"
    "v_smfmac_f32_16x16x64_f16 v[88:91], v[168:171], v[60:67], v2\n"
    "v_pk_mul_f16 v75, v54, v49 op_sel_hi:[0,1]\n"
    "v_add_f32_dpp v7, v7, v7 quad_perm:[2,3,0,1] row_mask:0xf bank_mask:0xf\n"
    "v_mfma_f32_16x16x32_f16 v[76:79], v[172:175], v[68:71], v[76:79]\n"
    "v_pk_mul_f16 v60, v48, v54 op_sel_hi:[0,1]\n"
    "v_pk_mul_f16 v61, v48, v55 op_sel_hi:[0,1]\n"
    "v_mfma_f32_16x16x32_f16 v[76:79], v[184:187], v[72:75], v[76:79]\n"
    "v_pk_mul_f16 v62, v50, v54 op_sel_hi:[0,1]\n"
    "v_add_f32_dpp v7, v7, v7 row_half_mirror row_mask:0xf bank_mask:0xf\n"
    "v_mfma_f32_16x16x32_f16 v[80:83], v[188:191], v[72:75], v[80:83]\n"
    "v_pk_mul_f16 v63, v50, v55 op_sel_hi:[0,1]\n"
    "s_waitcnt lgkmcnt(0)\n"
    "v_readfirstlane_b32 s45, v22\n"
    "s_cmp_lt_i32 s45, 16\n"
    "s_cbranch_scc1 .Lxd_ck14\n"
    ".Lxd_ck14r:\n"
    "v_mfma_f32_16x16x32_f16 v[80:83], v[176:179], v[68:71], v[80:83]\n"
    "v_pk_mul_f16 v64, v52, v54 op_sel_hi:[0,1]\n"
    "ds_read_b64_tr_b16 v[44:45], v5 offset:4096\n"
    "v_mfma_f32_16x16x32_f16 v[84:87], v[180:183], v[68:71], v[84:87]\n"
    "v_pk_mul_f16 v65, v52, v55 op_sel_hi:[0,1]\n"
    "ds_read_b64_tr_b16 v[46:47], v6 offset:4096\n"
    "v_mfma_f32_16x16x32_f16 v[84:87], v[192:195], v[72:75], v[84:87]\n"
    "v_pk_mul_f16 v66, v52, v52 op_sel_hi:[0,1]\n"
    "ds_read_b64_tr_b16 v[58:59], v4 offset:6976\n"
    "s_cbranch_vccnz .Lxd_pr13\n"
    "s_setprio 0\n"
    ".Lxd_pr13:\n"
    "v_smfmac_f32_16x16x64_f16 v[88:91], v[196:199], v[68:75], v2\n"
    "v_pk_mul_f16 v67, v52, v49 op_sel_hi:[0,1]\n"
    "ds_read_b64_tr_b16 v[56:57], v4 offset:6400\n"
    "v_mfma_f32_16x16x32_f16 v[76:79], v[200:203], v[60:63], v[76:79]\n"
    "v_pk_mul_f16 v68, v48, v52 op_sel_hi:[0,1]\n"
    "v_pk_mul_f16 v69, v48, v53 op_sel_hi:[0,1]\n"
    "v_mfma_f32_16x16x32_f16 v[76:79], v[212:215], v[64:67], v[76:79]\n"
    "v_pk_mul_f16 v70, v50, v52 op_sel_hi:[0,1]\n"
    "ds_read_b64_tr_b16 v[54:55], v4 offset:5824\n"
    "v_mfma_f32_16x16x32_f16 v[80:83], v[216:219], v[64:67], v[80:83]\n"
    "v_pk_mul_f16 v71, v50, v53 op_sel_hi:[0,1]\n"
    "v_add_f32_dpp v7, v7, v7 row_mirror row_mask:0xf bank_mask:0xf\n"
    "v_mfma_f32_16x16x32_f16 v[80:83], v[204:207], v[60:63], v[80:83]\n"
    "v_pk_mul_f16 v72, v50, v50 op_sel_hi:[0,1]\n"
    "ds_write_b32 v14, v7\n"
    "v_add_u32 v14, 64, v14\n"
    "v_mfma_f32_16x16x32_f16 v[84:87], v[208:211], v[60:63], v[84:87]\n"
    "v_pk_mul_f16 v73, v50, v49 op_sel_hi:[0,1]\n"
    "v_pk_mul_f16 v74, v48, v50 op_sel_hi:[0,1]\n"
    "v_mfma_f32_16x16x32_f16 v[84:87], v[220:223], v[64:67], v[84:87]\n"
    "v_pk_mul_f16 v75, v48, v51 op_sel_hi:[0,1]\n"
    "v_smfmac_f32_16x16x64_f16 v[88:91], v[224:227], v[60:67], v2\n"
    "ds_read_b64_tr_b16 v[48:49], v3 offset:4096\n"
    "ds_read_b64_tr_b16 v[50:51], v4 offset:4672\n"
    "ds_read_b64_tr_b16 v[52:53], v4 offset:5248\n"
    "v_smfmac_f32_16x16x64_f16 v[88:91], v[252:255], v[68:75], v2\n"
    "s_waitcnt lgkmcnt(7)\n"
    "v_pk_mul_f16 v60, v44, s45 op_sel:[0,1]\n"
    "v_pk_mul_f16 v61, v45, s45 op_sel:[0,1]\n"
    "v_mfma_f32_16x16x32_f16 v[76:79], v[228:231], v[68:71], v[76:79]\n"
    "v_pk_mul_f16 v62, v46, s45 op_sel:[0,1]\n"
    "v_pk_mul_f16 v63, v47, s45 op_sel:[0,1]\n"
    "v_mfma_f32_16x16x32_f16 v[76:79], v[240:243], v[72:75], v[76:79]\n"
    "v_pk_mul_f16 v64, v44, v46\n"
    "v_pk_mul_f16 v65, v45, v47\n"
    "v_mfma_f32_16x16x32_f16 v[80:83], v[244:247], v[72:75], v[80:83]\n"
    "v_add_f32 v11, v88, v89\n"
    "v_add_f32 v13, v90, v91\n"
    "v_mfma_f32_16x16x32_f16 v[80:83], v[232:235], v[68:71], v[80:83]\n"
    "v_mov_b64 v[88:89], 0\n"
    "v_mov_b64 v[90:91], 0\n"
    "v_mfma_f32_16x16x32_f16 v[84:87], v[236:239], v[68:71], v[84:87]\n"
    "s_waitcnt lgkmcnt(0)\n"
    "v_pk_mul_f16 v66, v48, v48 op_sel_hi:[0,1]\n"
    "v_pk_mul_f16 v67, v48, v49 op_sel_hi:[0,1]\n"
    "v_mfma_f32_16x16x32_f16 v[84:87], v[248:251], v[72:75], v[84:87]\n"
    "v_mul_f32 v11, v11, v13\n"
    "s_cmp_eq_u32 s45, -1\n"
    "s_cbranch_scc1 .Lxd_ex0\n"
    "s_cmp_lg_u32 s36, 0\n"
    "s_cbranch_scc1 .Lxd_pl1\n"
    ".Lxd_ph1:\n"
    "s_cbranch_vccnz .Lxd_pr15\n"
    "s_setprio 2\n"
    ".Lxd_pr15:\n"
    "v_smfmac_f32_16x16x64_f16 v[88:91], v[112:115], v[60:67], v2\n"
    "v_fmac_f32 v11, v76, v80\n"
    "v_fmac_f32 v11, v77, v81\n"
    "v_fmac_f32 v11, v78, v82\n"
    "v_fmac_f32 v11, v79, v83\n"
    "v_mfma_f32_16x16x32_f16 v[80:83], v[92:95], v[60:63], 0\n"
    "v_pk_mul_f16 v68, v58, v58 op_sel_hi:[0,1]\n"
    "v_pk_mul_f16 v69, v58, v49 op_sel_hi:[0,1]\n"
    "v_mfma_f32_16x16x32_f16 v[80:83], v[104:107], v[64:67], v[80:83]\n"
    "v_pk_mul_f16 v70, v48, v58 op_sel_hi:[0,1]\n"
    "v_pk_mul_f16 v71, v48, v59 op_sel_hi:[0,1]\n"
    "v_mfma_f32_16x16x32_f16 v[76:79], v[100:103], v[64:67], 0\n"
    "v_fmac_f32 v11, v84, v85\n"
    "v_fma_f32 v7, v86, v87, v11\n"
    "v_mfma_f32_16x16x32_f16 v[84:87], v[108:111], v[64:67], 0\n"
    "v_pk_mul_f16 v72, v50, v58 op_sel_hi:[0,1]\n"
    "v_pk_mul_f16 v73, v50, v59 op_sel_hi:[0,1]\n"
    "v_mfma_f32_16x16x32_f16 v[84:87], v[96:99], v[60:63], v[84:87]\n"
    "v_pk_mul_f16 v74, v52, v58 op_sel_hi:[0,1]\n"
    "v_pk_mul_f16 v75, v52, v59 op_sel_hi:[0,1]\n"
    "s_waitcnt vmcnt(0)\n"
    "v_mfma_f32_16x16x32_f16 v[76:79], v[116:119], v[68:71], v[76:79]\n"
    "v_pk_mul_f16 v60, v54, v58 op_sel_hi:[0,1]\n"
    "v_pk_mul_f16 v61, v54, v59 op_sel_hi:[0,1]\n"
    "v_mfma_f32_16x16x32_f16 v[76:79], v[128:131], v[72:75], v[76:79]\n"
    "v_pk_mul_f16 v62, v56, v58 op_sel_hi:[0,1]\n"
    "v_lshl_or_b32 v36, v36, 7, v19\n"
    "global_load_dwordx4 v[28:31], v36, s[8:9]\n"
    "v_mfma_f32_16x16x32_f16 v[80:83], v[132:135], v[72:75], v[80:83]\n"
    "v_pk_mul_f16 v63, v56, v59 op_sel_hi:[0,1]\n"
    "v_lshl_or_b32 v37, v37, 7, v19\n"
    "global_load_dwordx4 v[32:35], v37, s[8:9]\n"
    "v_mfma_f32_16x16x32_f16 v[80:83], v[120:123], v[68:71], v[80:83]\n"
    "v_pk_mul_f16 v64, v56, v56 op_sel_hi:[0,1]\n"
    "v_lshl_or_b32 v41, v41, 7, v20\n"
    "global_load_dword v40, v41, s[8:9]\n"
    "v_mfma_f32_16x16x32_f16 v[84:87], v[124:127], v[68:71], v[84:87]\n"
    "v_pk_mul_f16 v65, v56, v49 op_sel_hi:[0,1]\n"
    "v_lshl_or_b32 v38, v38, 7, v19\n"
    "global_load_dwordx4 v[36:39], v38, s[8:9]\n"
    "v_mfma_f32_16x16x32_f16 v[84:87], v[136:139], v[72:75], v[84:87]\n"
    "v_pk_mul_f16 v66, v48, v56 op_sel_hi:[0,1]\n"
    "v_smfmac_f32_16x16x64_f16 v[88:91], v[140:143], v[68:75], v2\n"
    "v_pk_mul_f16 v67, v48, v57 op_sel_hi:[0,1]\n"
    "v_mfma_f32_16x16x32_f16 v[76:79], v[144:147], v[60:63], v[76:79]\n"
    "v_pk_mul_f16 v68, v50, v56 op_sel_hi:[0,1]\n"
    "v_pk_mul_f16 v69, v50, v57 op_sel_hi:[0,1]\n"
    "v_mfma_f32_16x16x32_f16 v[76:79], v[156:159], v[64:67], v[76:79]\n"
    "v_pk_mul_f16 v70, v52, v56 op_sel_hi:[0,1]\n"
    "v_mfma_f32_16x16x32_f16 v[80:83], v[160:163], v[64:67], v[80:83]\n"
    "v_pk_mul_f16 v71, v52, v57 op_sel_hi:[0,1]\n"
    "v_mfma_f32_16x16x32_f16 v[80:83], v[148:151], v[60:63], v[80:83]\n"
    "v_pk_mul_f16 v72, v54, v56 op_sel_hi:[0,1]\n"
    "ds_read_b32 v22, v25 offset:520\n"
    "v_mfma_f32_16x16x32_f16 v[84:87], v[152:155], v[60:63], v[84:87]\n"
    "v_pk_mul_f16 v73, v54, v57 op_sel_hi:[0,1]\n"
    "v_mfma_f32_16x16x32_f16 v[84:87], v[164:167], v[64:67], v[84:87]\n"
    "v_pk_mul_f16 v74, v54, v54 op_sel_hi:[0,1]\n"
    "v_smfmac_f32_16x16x64_f16 v[88:91], v[168:171], v[60:67], v2\n"
    "v_pk_mul_f16 v75, v54, v49 op_sel_hi:[0,1]\n"
    "v_mfma_f32_16x16x32_f16 v[76:79], v[172:175], v[68:71], v[76:79]\n"
    "v_pk_mul_f16 v60, v48, v54 op_sel_hi:[0,1]\n"
    "v_pk_mul_f16 v61, v48, v55 op_sel_hi:[0,1]\n"
    "v_mfma_f32_16x16x32_f16 v[76:79], v[184:187], v[72:75], v[76:79]\n"
    "v_pk_mul_f16 v62, v50, v54 op_sel_hi:[0,1]\n"
    "v_mfma_f32_16x16x32_f16 v[80:83], v[188:191], v[72:75], v[80:83]\n"
    "v_pk_mul_f16 v63, v50, v55 op_sel_hi:[0,1]\n"
    "s_waitcnt lgkmcnt(0)\n"
    "v_readfirstlane_b32 s45, v22\n"
    "s_cmp_lt_i32 s45, 16\n"
    "s_cbranch_scc1 .Lxd_ck17\n"
    ".Lxd_ck17r:\n"
    "v_mfma_f32_16x16x32_f16 v[80:83], v[176:179], v[68:71], v[80:83]\n"
    "v_pk_mul_f16 v64, v52, v54 op_sel_hi:[0,1]\n"
    "ds_read_b64_tr_b16 v[44:45], v5 offset:8192\n"
    "v_mfma_f32_16x16x32_f16 v[84:87], v[180:183], v[68:71], v[84:87]\n"
    "v_pk_mul_f16 v65, v52, v55 op_sel_hi:[0,1]\n"
    "ds_read_b64_tr_b16 v[46:47], v6 offset:8192\n"
    "v_mfma_f32_16x16x32_f16 v[84:87], v[192:195], v[72:75], v[84:87]\n"
    "v_pk_mul_f16 v66, v52, v52 op_sel_hi:[0,1]\n"
    "ds_read_b64_tr_b16 v[58:59], v4 offset:11072\n"
    "s_cbranch_vccnz .Lxd_pr16\n"
    "s_setprio 0\n"
    ".Lxd_pr16:\n"
    "v_smfmac_f32_16x16x64_f16 v[88:91], v[196:199], v[68:75], v2\n"
    "v_pk_mul_f16 v67, v52, v49 op_sel_hi:[0,1]\n"
    "ds_read_b64_tr_b16 v[56:57], v4 offset:10496\n"
    "v_mfma_f32_16x16x32_f16 v[76:79], v[200:203], v[60:63], v[76:79]\n"
    "v_pk_mul_f16 v68, v48, v52 op_sel_hi:[0,1]\n"
    "v_pk_mul_f16 v69, v48, v53 op_sel_hi:[0,1]\n"
    "v_mfma_f32_16x16x32_f16 v[76:79], v[212:215], v[64:67], v[76:79]\n"
    "v_pk_mul_f16 v70, v50, v52 op_sel_hi:[0,1]\n"
    "ds_read_b64_tr_b16 v[54:55], v4 offset:9920\n"
    "v_mfma_f32_16x16x32_f16 v[80:83], v[216:219], v[64:67], v[80:83]\n"
    "v_pk_mul_f16 v71, v50, v53 op_sel_hi:[0,1]\n"
    "v_mfma_f32_16x16x32_f16 v[80:83], v[204:207], v[60:63], v[80:83]\n"
    "v_pk_mul_f16 v72, v50, v50 op_sel_hi:[0,1]\n"
    "v_mfma_f32_16x16x32_f16 v[84:87], v[208:211], v[60:63], v[84:87]\n"
    "v_pk_mul_f16 v73, v50, v49 op_sel_hi:[0,1]\n"
    "v_pk_mul_f16 v74, v48, v50 op_sel_hi:[0,1]\n"
    "v_mfma_f32_16x16x32_f16 v[84:87], v[220:223], v[64:67], v[84:87]\n"
    "v_pk_mul_f16 v75, v48, v51 op_sel_hi:[0,1]\n"
    "v_smfmac_f32_16x16x64_f16 v[88:91], v[224:227], v[60:67], v2\n"
    "ds_read_b64_tr_b16 v[48:49], v3 offset:8192\n"
    "ds_read_b64_tr_b16 v[50:51], v4 offset:8768\n"
    "ds_read_b64_tr_b16 v[52:53], v4 offset:9344\n"
    "v_smfmac_f32_16x16x64_f16 v[88:91], v[252:255], v[68:75], v2\n"
    "s_waitcnt lgkmcnt(6)\n"
    "v_pk_mul_f16 v60, v44, s45 op_sel:[0,1]\n"
    "v_pk_mul_f16 v61, v45, s45 op_sel:[0,1]\n"
    "v_mfma_f32_16x16x32_f16 v[76:79], v[228:231], v[68:71], v[76:79]\n"
    "v_pk_mul_f16 v62, v46, s45 op_sel:[0,1]\n"
    "v_pk_mul_f16 v63, v47, s45 op_sel:[0,1]\n"
    "v_mfma_f32_16x16x32_f16 v[76:79], v[240:243], v[72:75], v[76:79]\n"
    "v_pk_mul_f16 v64, v44, v46\n"
    "v_pk_mul_f16 v65, v45, v47\n"
    "v_mfma_f32_16x16x32_f16 v[80:83], v[244:247], v[72:75], v[80:83]\n"
    "v_add_f32 v11, v88, v89\n"
    "v_add_f32 v13, v90, v91\n"
    "v_mfma_f32_16x16x32_f16 v[80:83], v[232:235], v[68:71], v[80:83]\n"
    "v_mov_b64 v[88:89], 0\n"
    "v_mov_b64 v[90:91], 0\n"
    "v_mfma_f32_16x16x32_f16 v[84:87], v[236:239], v[68:71], v[84:87]\n"
    "s_waitcnt lgkmcnt(0)\n"
    "v_pk_mul_f16 v66, v48, v48 op_sel_hi:[0,1]\n"
    "v_pk_mul_f16 v67, v48, v49 op_sel_hi:[0,1]\n"
    "v_mfma_f32_16x16x32_f16 v[84:87], v[248:251], v[72:75], v[84:87]\n"
    "v_mul_f32 v11, v11, v13\n"
    "s_cmp_eq_u32 s45, -1\n"
    "s_cbranch_scc1 .Lxd_ex1\n"
    ".Lxd_ph2:\n"
    "s_cbranch_vccnz .Lxd_pr18\n"
    "s_setprio 2\n"
    ".Lxd_pr18:\n"
    "v_smfmac_f32_16x16x64_f16 v[88:91], v[112:115], v[60:67], v2\n"
    "v_fmac_f32 v11, v76, v80\n"
    "v_fmac_f32 v11, v77, v81\n"
    "v_fmac_f32 v11, v78, v82\n"
    "v_fmac_f32 v11, v79, v83\n"
    "v_mfma_f32_16x16x32_f16 v[80:83], v[92:95], v[60:63], 0\n"
    "v_pk_mul_f16 v68, v58, v58 op_sel_hi:[0,1]\n"
    "v_pk_mul_f16 v69, v58, v49 op_sel_hi:[0,1]\n"
    "v_mfma_f32_16x16x32_f16 v[80:83], v[104:107], v[64:67], v[80:83]\n"
    "v_pk_mul_f16 v70, v48, v58 op_sel_hi:[0,1]\n"
    "v_pk_mul_f16 v71, v48, v59 op_sel_hi:[0,1]\n"
    "v_mfma_f32_16x16x32_f16 v[76:79], v[100:103], v[64:67], 0\n"
    "v_fmac_f32 v11, v84, v85\n"
    "v_fma_f32 v8, v86, v87, v11\n"
    "v_mfma_f32_16x16x32_f16 v[84:87], v[108:111], v[64:67], 0\n"
    "v_pk_mul_f16 v72, v50, v58 op_sel_hi:[0,1]\n"
    "v_pk_mul_f16 v73, v50, v59 op_sel_hi:[0,1]\n"
    "v_mfma_f32_16x16x32_f16 v[84:87], v[96:99], v[60:63], v[84:87]\n"
    "v_pk_mul_f16 v74, v52, v58 op_sel_hi:[0,1]\n"
    "v_pk_mul_f16 v75, v52, v59 op_sel_hi:[0,1]\n"
    "s_waitcnt vmcnt(0)\n"
    "v_mfma_f32_16x16x32_f16 v[76:79], v[116:119], v[68:71], v[76:79]\n"
    "v_pk_mul_f16 v60, v54, v58 op_sel_hi:[0,1]\n"
    "v_pk_mul_f16 v61, v54, v59 op_sel_hi:[0,1]\n"
    "s_movk_i32 s43, 15744\n"
    "v_pk_maximum3_f16 v21, v28, v28, s43 op_sel_hi:[1,1,0] neg_lo:[0,1,0] neg_hi:[0,1,0]\n"
    "v_mfma_f32_16x16x32_f16 v[76:79], v[128:131], v[72:75], v[76:79]\n"
    "v_pk_mul_f16 v62, v56, v58 op_sel_hi:[0,1]\n"
    "v_pk_maximum3_f16 v21, v29, v29, v21 neg_lo:[0,1,0] neg_hi:[0,1,0]\n"
    "v_mfma_f32_16x16x32_f16 v[80:83], v[132:135], v[72:75], v[80:83]\n"
    "v_pk_mul_f16 v63, v56, v59 op_sel_hi:[0,1]\n"
    "v_pk_maximum3_f16 v21, v30, v30, v21 neg_lo:[0,1,0] neg_hi:[0,1,0]\n"
    "v_mfma_f32_16x16x32_f16 v[80:83], v[120:123], v[68:71], v[80:83]\n"
    "v_pk_mul_f16 v64, v56, v56 op_sel_hi:[0,1]\n"
    "v_pk_maximum3_f16 v21, v31, v31, v21 neg_lo:[0,1,0] neg_hi:[0,1,0]\n"
    "v_mfma_f32_16x16x32_f16 v[84:87], v[124:127], v[68:71], v[84:87]\n"
    "v_pk_mul_f16 v65, v56, v49 op_sel_hi:[0,1]\n"
    "v_pk_maximum3_f16 v21, v32, v32, v21 neg_lo:[0,1,0] neg_hi:[0,1,0]\n"
    "v_mfma_f32_16x16x32_f16 v[84:87], v[136:139], v[72:75], v[84:87]\n"
    "v_pk_mul_f16 v66, v48, v56 op_sel_hi:[0,1]\n"
    "v_pk_maximum3_f16 v21, v33, v33, v21 neg_lo:[0,1,0] neg_hi:[0,1,0]\n"
    "v_smfmac_f32_16x16x64_f16 v[88:91], v[140:143], v[68:75], v2\n"
    "v_pk_mul_f16 v67, v48, v57 op_sel_hi:[0,1]\n"
    "v_pk_maximum3_f16 v21, v34, v34, v21 neg_lo:[0,1,0] neg_hi:[0,1,0]\n"
    "v_mfma_f32_16x16x32_f16 v[76:79], v[144:147], v[60:63], v[76:79]\n"
    "v_pk_mul_f16 v68, v50, v56 op_sel_hi:[0,1]\n"
    "v_pk_mul_f16 v69, v50, v57 op_sel_hi:[0,1]\n"
    "v_pk_maximum3_f16 v21, v35, v35, v21 neg_lo:[0,1,0] neg_hi:[0,1,0]\n"
    "v_mfma_f32_16x16x32_f16 v[76:79], v[156:159], v[64:67], v[76:79]\n"
    "v_pk_mul_f16 v70, v52, v56 op_sel_hi:[0,1]\n"
    "v_pk_maximum3_f16 v21, v36, v36, v21 neg_lo:[0,1,0] neg_hi:[0,1,0]\n"
    "v_mfma_f32_16x16x32_f16 v[80:83], v[160:163], v[64:67], v[80:83]\n"
    "v_pk_mul_f16 v71, v52, v57 op_sel_hi:[0,1]\n"
    "v_pk_maximum3_f16 v21, v37, v37, v21 neg_lo:[0,1,0] neg_hi:[0,1,0]\n"
    "v_mfma_f32_16x16x32_f16 v[80:83], v[148:151], v[60:63], v[80:83]\n"
    "v_pk_mul_f16 v72, v54, v56 op_sel_hi:[0,1]\n"
    "ds_read_b32 v22, v25 offset:524\n"
    "v_pk_maximum3_f16 v21, v38, v38, v21 neg_lo:[0,1,0] neg_hi:[0,1,0]\n"
    "v_mfma_f32_16x16x32_f16 v[84:87], v[152:155], v[60:63], v[84:87]\n"
    "v_pk_mul_f16 v73, v54, v57 op_sel_hi:[0,1]\n"
    "v_pk_maximum3_f16 v21, v39, v39, v21 neg_lo:[0,1,0] neg_hi:[0,1,0]\n"
    "v_mfma_f32_16x16x32_f16 v[84:87], v[164:167], v[64:67], v[84:87]\n"
    "v_pk_mul_f16 v74, v54, v54 op_sel_hi:[0,1]\n"
    "v_pk_maximum3_f16 v21, v40, v40, v21 neg_lo:[0,1,0] neg_hi:[0,1,0]\n"
    "v_smfmac_f32_16x16x64_f16 v[88:91], v[168:171], v[60:67], v2\n"
    "v_pk_mul_f16 v75, v54, v49 op_sel_hi:[0,1]\n"
    "v_pk_max_u16 v21, v21, v21 op_sel:[0,1] op_sel_hi:[1,0]\n"
    "v_mfma_f32_16x16x32_f16 v[76:79], v[172:175], v[68:71], v[76:79]\n"
    "v_pk_mul_f16 v60, v48, v54 op_sel_hi:[0,1]\n"
    "v_pk_mul_f16 v61, v48, v55 op_sel_hi:[0,1]\n"
    "v_max_u32_dpp v21, v21, v21 quad_perm:[1,0,3,2] row_mask:0xf bank_mask:0xf\n"
    "v_mfma_f32_16x16x32_f16 v[76:79], v[184:187], v[72:75], v[76:79]\n"
    "v_pk_mul_f16 v62, v50, v54 op_sel_hi:[0,1]\n"
    "v_max_u32_dpp v21, v21, v21 quad_perm:[2,3,0,1] row_mask:0xf bank_mask:0xf\n"
    "v_mfma_f32_16x16x32_f16 v[80:83], v[188:191], v[72:75], v[80:83]\n"
    "v_pk_mul_f16 v63, v50, v55 op_sel_hi:[0,1]\n"
    "s_waitcnt lgkmcnt(0)\n"
    "v_readfirstlane_b32 s45, v22\n"
    "s_cmp_lt_i32 s45, 16\n"
    "s_cbranch_scc1 .Lxd_ck20\n"
    ".Lxd_ck20r:\n"
    "v_max_u32_dpp v21, v21, v21 row_half_mirror row_mask:0xf bank_mask:0xf\n"
    "v_mfma_f32_16x16x32_f16 v[80:83], v[176:179], v[68:71], v[80:83]\n"
    "v_pk_mul_f16 v64, v52, v54 op_sel_hi:[0,1]\n"
    "ds_read_b64_tr_b16 v[44:45], v5 offset:12288\n"
    "v_max_u32_dpp v21, v21, v21 row_mirror row_mask:0xf bank_mask:0xf\n"
    "v_mfma_f32_16x16x32_f16 v[84:87], v[180:183], v[68:71], v[84:87]\n"
    "v_pk_mul_f16 v65, v52, v55 op_sel_hi:[0,1]\n"
    "ds_read_b64_tr_b16 v[46:47], v6 offset:12288\n"
    "v_max_u32_dpp v21, v21, v21 row_bcast:15 row_mask:0xa bank_mask:0xf\n"
    "v_mfma_f32_16x16x32_f16 v[84:87], v[192:195], v[72:75], v[84:87]\n"
    "v_pk_mul_f16 v66, v52, v52 op_sel_hi:[0,1]\n"
    "ds_read_b64_tr_b16 v[58:59], v4 offset:15168\n"
    "v_max_u32_dpp v21, v21, v21 row_bcast:31 row_mask:0xc bank_mask:0xf\n"
    "s_cbranch_vccnz .Lxd_pr19\n"
    "s_setprio 0\n"
    ".Lxd_pr19:\n"
    "v_smfmac_f32_16x16x64_f16 v[88:91], v[196:199], v[68:75], v2\n"
    "v_pk_mul_f16 v67, v52, v49 op_sel_hi:[0,1]\n"
    "ds_read_b64_tr_b16 v[56:57], v4 offset:14592\n"
    "v_readlane_b32 s47, v21, 63\n"
    "v_mfma_f32_16x16x32_f16 v[76:79], v[200:203], v[60:63], v[76:79]\n"
    "v_pk_mul_f16 v68, v48, v52 op_sel_hi:[0,1]\n"
    "v_pk_mul_f16 v69, v48, v53 op_sel_hi:[0,1]\n"
    "s_and_b32 s50, s47, 0x7f80\n"
    "s_lshr_b32 s47, s50, 3\n"
    "s_sub_u32 s50, s50, 2816\n"
    "s_mul_i32 s51, s50, -8\n"
    "v_mfma_f32_16x16x32_f16 v[76:79], v[212:215], v[64:67], v[76:79]\n"
    "v_pk_mul_f16 v70, v50, v52 op_sel_hi:[0,1]\n"
    "ds_read_b64_tr_b16 v[54:55], v4 offset:14016\n"
    "v_pk_maximum3_f16 v21, v28, v28, s50 op_sel_hi:[1,1,0] neg_lo:[0,1,0] neg_hi:[0,1,0]\n"
    "v_mfma_f32_16x16x32_f16 v[80:83], v[216:219], v[64:67], v[80:83]\n"
    "v_pk_mul_f16 v71, v50, v53 op_sel_hi:[0,1]\n"
    "v_pk_mad_u16 v21, v21, 8, s51 op_sel_hi:[1,0,0]\n"
    "v_mfma_f32_16x16x32_f16 v[80:83], v[204:207], v[60:63], v[80:83]\n"
    "v_pk_mul_f16 v72, v50, v50 op_sel_hi:[0,1]\n"
    "v_bfi_b32 v28, s57, v21, v28\n"
    "v_mfma_f32_16x16x32_f16 v[84:87], v[208:211], v[60:63], v[84:87]\n"
    "v_pk_mul_f16 v73, v50, v49 op_sel_hi:[0,1]\n"
    "v_pk_mul_f16 v74, v48, v50 op_sel_hi:[0,1]\n"
    "v_pk_maximum3_f16 v12, v29, v29, s50 op_sel_hi:[1,1,0] neg_lo:[0,1,0] neg_hi:[0,1,0]\n"
    "v_mfma_f32_16x16x32_f16 v[84:87], v[220:223], v[64:67], v[84:87]\n"
    "v_pk_mul_f16 v75, v48, v51 op_sel_hi:[0,1]\n"
    "v_pk_mad_u16 v12, v12, 8, s51 op_sel_hi:[1,0,0]\n"
    "v_smfmac_f32_16x16x64_f16 v[88:91], v[224:227], v[60:67], v2\n"
    "ds_read_b64_tr_b16 v[48:49], v3 offset:12288\n"
    "ds_read_b64_tr_b16 v[50:51], v4 offset:12864\n"
    "ds_read_b64_tr_b16 v[52:53], v4 offset:13440\n"
    "v_bfi_b32 v29, s57, v12, v29\n"
    "v_smfmac_f32_16x16x64_f16 v[88:91], v[252:255], v[68:75], v2\n"
    "s_waitcnt lgkmcnt(6)\n"
    "v_pk_mul_f16 v60, v44, s45 op_sel:[0,1]\n"
    "v_pk_mul_f16 v61, v45, s45 op_sel:[0,1]\n"
    "v_mfma_f32_16x16x32_f16 v[76:79], v[228:231], v[68:71], v[76:79]\n"
    "v_pk_mul_f16 v62, v46, s45 op_sel:[0,1]\n"
    "v_pk_mul_f16 v63, v47, s45 op_sel:[0,1]\n"
    "v_mfma_f32_16x16x32_f16 v[76:79], v[240:243], v[72:75], v[76:79]\n"
    "v_pk_mul_f16 v64, v44, v46\n"
    "v_pk_mul_f16 v65, v45, v47\n"
    "v_mfma_f32_16x16x32_f16 v[80:83], v[244:247], v[72:75], v[80:83]\n"
    "v_add_f32 v11, v88, v89\n"
    "v_add_f32 v13, v90, v91\n"
    "v_mfma_f32_16x16x32_f16 v[80:83], v[232:235], v[68:71], v[80:83]\n"
    "v_mov_b64 v[88:89], 0\n"
    "v_mov_b64 v[90:91], 0\n"
    "v_mfma_f32_16x16x32_f16 v[84:87], v[236:239], v[68:71], v[84:87]\n"
    "s_waitcnt lgkmcnt(0)\n"
    "v_pk_mul_f16 v66, v48, v48 op_sel_hi:[0,1]\n"
    "v_pk_mul_f16 v67, v48, v49 op_sel_hi:[0,1]\n"
    "v_mfma_f32_16x16x32_f16 v[84:87], v[248:251], v[72:75], v[84:87]\n"
    "v_mul_f32 v11, v11, v13\n"
    "s_cmp_eq_u32 s45, -1\n"
    "s_cbranch_scc1 .Lxd_ex2\n"
    ".Lxd_ph3:\n"
    "s_cbranch_vccnz .Lxd_pr21\n"
    "s_setprio 2\n"
    ".Lxd_pr21:\n"
    "v_smfmac_f32_16x16x64_f16 v[88:91], v[112:115], v[60:67], v2\n"
    "v_fmac_f32 v11, v76, v80\n"
    "v_fmac_f32 v11, v77, v81\n"
    "v_fmac_f32 v11, v78, v82\n"
    "v_fmac_f32 v11, v79, v83\n"
    "v_mfma_f32_16x16x32_f16 v[80:83], v[92:95], v[60:63], 0\n"
    "v_pk_mul_f16 v68, v58, v58 op_sel_hi:[0,1]\n"
    "v_pk_mul_f16 v69, v58, v49 op_sel_hi:[0,1]\n"
    "v_xor_b32 v3, 0x4000, v3\n"
    "v_xor_b32 v4, 0x4000, v4\n"
    "v_mfma_f32_16x16x32_f16 v[80:83], v[104:107], v[64:67], v[80:83]\n"
    "v_pk_mul_f16 v70, v48, v58 op_sel_hi:[0,1]\n"
    "v_pk_mul_f16 v71, v48, v59 op_sel_hi:[0,1]\n"
    "v_xor_b32 v5, 0x4000, v5\n"
    "v_mfma_f32_16x16x32_f16 v[76:79], v[100:103], v[64:67], 0\n"
    "v_fmac_f32 v11, v84, v85\n"
    "v_fma_f32 v9, v86, v87, v11\n"
    "v_xor_b32 v6, 0x4000, v6\n"
    "v_mfma_f32_16x16x32_f16 v[84:87], v[108:111], v[64:67], 0\n"
    "v_pk_mul_f16 v72, v50, v58 op_sel_hi:[0,1]\n"
    "v_pk_mul_f16 v73, v50, v59 op_sel_hi:[0,1]\n"
    "v_pk_maximum3_f16 v21, v30, v30, s50 op_sel_hi:[1,1,0] neg_lo:[0,1,0] neg_hi:[0,1,0]\n"
    "v_pk_mad_u16 v21, v21, 8, s51 op_sel_hi:[1,0,0]\n"
    "v_mfma_f32_16x16x32_f16 v[84:87], v[96:99], v[60:63], v[84:87]\n"
    "v_pk_mul_f16 v74, v52, v58 op_sel_hi:[0,1]\n"
    "v_pk_mul_f16 v75, v52, v59 op_sel_hi:[0,1]\n"
    "v_bfi_b32 v30, s57, v21, v30\n"
    "v_mfma_f32_16x16x32_f16 v[76:79], v[116:119], v[68:71], v[76:79]\n"
    "v_pk_mul_f16 v60, v54, v58 op_sel_hi:[0,1]\n"
    "v_pk_mul_f16 v61, v54, v59 op_sel_hi:[0,1]\n"
    "v_pk_maximum3_f16 v12, v31, v31, s50 op_sel_hi:[1,1,0] neg_lo:[0,1,0] neg_hi:[0,1,0]\n"
    "v_mfma_f32_16x16x32_f16 v[76:79], v[128:131], v[72:75], v[76:79]\n"
    "v_pk_mul_f16 v62, v56, v58 op_sel_hi:[0,1]\n"
    "v_pk_mad_u16 v12, v12, 8, s51 op_sel_hi:[1,0,0]\n"
    "v_mfma_f32_16x16x32_f16 v[80:83], v[132:135], v[72:75], v[80:83]\n"
    "v_pk_mul_f16 v63, v56, v59 op_sel_hi:[0,1]\n"
    "v_bfi_b32 v31, s57, v12, v31\n"
    "v_pk_maximum3_f16 v21, v32, v32, s50 op_sel_hi:[1,1,0] neg_lo:[0,1,0] neg_hi:[0,1,0]\n"
    "v_mfma_f32_16x16x32_f16 v[80:83], v[120:123], v[68:71], v[80:83]\n"
    "v_pk_mul_f16 v64, v56, v56 op_sel_hi:[0,1]\n"
    "v_pk_mad_u16 v21, v21, 8, s51 op_sel_hi:[1,0,0]\n"
    "v_mfma_f32_16x16x32_f16 v[84:87], v[124:127], v[68:71], v[84:87]\n"
    "v_pk_mul_f16 v65, v56, v49 op_sel_hi:[0,1]\n"
    "v_bfi_b32 v32, s57, v21, v32\n"
    "v_mfma_f32_16x16x32_f16 v[84:87], v[136:139], v[72:75], v[84:87]\n"
    "v_pk_mul_f16 v66, v48, v56 op_sel_hi:[0,1]\n"
    "v_pk_maximum3_f16 v12, v33, v33, s50 op_sel_hi:[1,1,0] neg_lo:[0,1,0] neg_hi:[0,1,0]\n"
    "v_pk_mad_u16 v12, v12, 8, s51 op_sel_hi:[1,0,0]\n"
    "v_smfmac_f32_16x16x64_f16 v[88:91], v[140:143], v[68:75], v2\n"
    "v_pk_mul_f16 v67, v48, v57 op_sel_hi:[0,1]\n"
    "v_bfi_b32 v33, s57, v12, v33\n"
    "v_mfma_f32_16x16x32_f16 v[76:79], v[144:147], v[60:63], v[76:79]\n"
    "v_pk_mul_f16 v68, v50, v56 op_sel_hi:[0,1]\n"
    "v_pk_mul_f16 v69, v50, v57 op_sel_hi:[0,1]\n"
    "v_pk_maximum3_f16 v21, v34, v34, s50 op_sel_hi:[1,1,0] neg_lo:[0,1,0] neg_hi:[0,1,0]\n"
    "v_mfma_f32_16x16x32_f16 v[76:79], v[156:159], v[64:67], v[76:79]\n"
    "v_pk_mul_f16 v70, v52, v56 op_sel_hi:[0,1]\n"
    "v_pk_mad_u16 v21, v21, 8, s51 op_sel_hi:[1,0,0]\n"
    "v_mfma_f32_16x16x32_f16 v[80:83], v[160:163], v[64:67], v[80:83]\n"
    "v_pk_mul_f16 v71, v52, v57 op_sel_hi:[0,1]\n"
    "v_bfi_b32 v34, s57, v21, v34\n"
    "v_pk_maximum3_f16 v12, v35, v35, s50 op_sel_hi:[1,1,0] neg_lo:[0,1,0] neg_hi:[0,1,0]\n"
    "v_mfma_f32_16x16x32_f16 v[80:83], v[148:151], v[60:63], v[80:83]\n"
    "v_pk_mul_f16 v72, v54, v56 op_sel_hi:[0,1]\n"
    "ds_read_b32 v22, v25 offset:528\n"
    "v_pk_mad_u16 v12, v12, 8, s51 op_sel_hi:[1,0,0]\n"
    "v_mfma_f32_16x16x32_f16 v[84:87], v[152:155], v[60:63], v[84:87]\n"
    "v_pk_mul_f16 v73, v54, v57 op_sel_hi:[0,1]\n"
    "v_bfi_b32 v35, s57, v12, v35\n"
    "v_mfma_f32_16x16x32_f16 v[84:87], v[164:167], v[64:67], v[84:87]\n"
    "v_pk_mul_f16 v74, v54, v54 op_sel_hi:[0,1]\n"
    "v_pk_maximum3_f16 v21, v36, v36, s50 op_sel_hi:[1,1,0] neg_lo:[0,1,0] neg_hi:[0,1,0]\n"
    "v_pk_mad_u16 v21, v21, 8, s51 op_sel_hi:[1,0,0]\n"
    "v_smfmac_f32_16x16x64_f16 v[88:91], v[168:171], v[60:67], v2\n"
    "v_pk_mul_f16 v75, v54, v49 op_sel_hi:[0,1]\n"
    "v_bfi_b32 v36, s57, v21, v36\n"
    "v_mfma_f32_16x16x32_f16 v[76:79], v[172:175], v[68:71], v[76:79]\n"
    "v_pk_mul_f16 v60, v48, v54 op_sel_hi:[0,1]\n"
    "v_pk_mul_f16 v61, v48, v55 op_sel_hi:[0,1]\n"
    "v_pk_maximum3_f16 v12, v37, v37, s50 op_sel_hi:[1,1,0] neg_lo:[0,1,0] neg_hi:[0,1,0]\n"
    "v_mfma_f32_16x16x32_f16 v[76:79], v[184:187], v[72:75], v[76:79]\n"
    "v_pk_mul_f16 v62, v50, v54 op_sel_hi:[0,1]\n"
    "v_pk_mad_u16 v12, v12, 8, s51 op_sel_hi:[1,0,0]\n"
    "v_mfma_f32_16x16x32_f16 v[80:83], v[188:191], v[72:75], v[80:83]\n"
    "v_pk_mul_f16 v63, v50, v55 op_sel_hi:[0,1]\n"
    "s_waitcnt lgkmcnt(0)\n"
    "v_readfirstlane_b32 s45, v22\n"
    "s_cmp_lt_i32 s45, 16\n"
    "s_cbranch_scc1 .Lxd_ck23\n"
    ".Lxd_ck23r:\n"
    "v_bfi_b32 v37, s57, v12, v37\n"
    "v_pk_maximum3_f16 v21, v38, v38, s50 op_sel_hi:[1,1,0] neg_lo:[0,1,0] neg_hi:[0,1,0]\n"
    "v_mfma_f32_16x16x32_f16 v[80:83], v[176:179], v[68:71], v[80:83]\n"
    "v_pk_mul_f16 v64, v52, v54 op_sel_hi:[0,1]\n"
    "ds_read_b64_tr_b16 v[44:45], v5 offset:0\n"
    "s_and_b32 s40, s45, 15\n"
    "s_cbranch_scc1 .Lxd_dr24\n"
    "s_cmp_lt_u32 s55, 32\n"
    "s_cselect_b32 s44, 0, -1\n"
    "s_add_u32 s55, s55, 4\n"
    "v_add_u32_e64 v24, s44, 1\n"
    "s_mov_b64 exec, 1\n"
    "global_atomic_add v24, v17, v24, s[52:53] sc0 sc1\n"
    "s_mov_b64 exec, -1\n"
    ".Lxd_dr24r:\n"
    "v_pk_mad_u16 v21, v21, 8, s51 op_sel_hi:[1,0,0]\n"
    "v_mfma_f32_16x16x32_f16 v[84:87], v[180:183], v[68:71], v[84:87]\n"
    "v_pk_mul_f16 v65, v52, v55 op_sel_hi:[0,1]\n"
    "ds_read_b64_tr_b16 v[46:47], v6 offset:0\n"
    "v_bfi_b32 v38, s57, v21, v38\n"
    "v_mfma_f32_16x16x32_f16 v[84:87], v[192:195], v[72:75], v[84:87]\n"
    "v_pk_mul_f16 v66, v52, v52 op_sel_hi:[0,1]\n"
    "ds_read_b64_tr_b16 v[58:59], v4 offset:2880\n"
    "v_pk_maximum3_f16 v12, v39, v39, s50 op_sel_hi:[1,1,0] neg_lo:[0,1,0] neg_hi:[0,1,0]\n"
    "v_pk_mad_u16 v12, v12, 8, s51 op_sel_hi:[1,0,0]\n"
    "s_cbranch_vccnz .Lxd_pr22\n"
    "s_setprio 0\n"
    ".Lxd_pr22:\n"
    "v_smfmac_f32_16x16x64_f16 v[88:91], v[196:199], v[68:75], v2\n"
    "v_pk_mul_f16 v67, v52, v49 op_sel_hi:[0,1]\n"
    "ds_read_b64_tr_b16 v[56:57], v4 offset:2304\n"
    "v_bfi_b32 v39, s57, v12, v39\n"
    "v_mfma_f32_16x16x32_f16 v[76:79], v[200:203], v[60:63], v[76:79]\n"
    "v_pk_mul_f16 v68, v48, v52 op_sel_hi:[0,1]\n"
    "v_pk_mul_f16 v69, v48, v53 op_sel_hi:[0,1]\n"
    "v_pk_maximum3_f16 v21, v40, v40, s50 op_sel_hi:[1,1,0] neg_lo:[0,1,0] neg_hi:[0,1,0]\n"
    "v_mfma_f32_16x16x32_f16 v[76:79], v[212:215], v[64:67], v[76:79]\n"
    "v_pk_mul_f16 v70, v50, v52 op_sel_hi:[0,1]\n"
    "ds_read_b64_tr_b16 v[54:55], v4 offset:1728\n"
    "v_pk_mad_u16 v21, v21, 8, s51 op_sel_hi:[1,0,0]\n"
    "v_mfma_f32_16x16x32_f16 v[80:83], v[216:219], v[64:67], v[80:83]\n"
    "v_pk_mul_f16 v71, v50, v53 op_sel_hi:[0,1]\n"
    "v_bfi_b32 v40, s57, v21, v40\n"
    "s_sub_u32 s43, 4112, s47\n"
    "s_lshl_b32 s43, s43, 19\n"
    "v_cvt_pkrtz_f16_f32 v12, s43, s43\n"
    "v_mfma_f32_16x16x32_f16 v[80:83], v[204:207], v[60:63], v[80:83]\n"
    "v_pk_mul_f16 v72, v50, v50 op_sel_hi:[0,1]\n"
    "v_lshl_or_b32 v21, v12, 16, s47\n"
    "v_mfma_f32_16x16x32_f16 v[84:87], v[208:211], v[60:63], v[84:87]\n"
    "v_pk_mul_f16 v73, v50, v49 op_sel_hi:[0,1]\n"
    "v_pk_mul_f16 v74, v48, v50 op_sel_hi:[0,1]\n"
    "ds_write_b128 v15, v[28:31] offset:0\n"
    "ds_write_b128 v15, v[32:35] offset:144\n"
    "ds_write_b128 v15, v[36:39] offset:288\n"
    "v_mfma_f32_16x16x32_f16 v[84:87], v[220:223], v[64:67], v[84:87]\n"
    "v_pk_mul_f16 v75, v48, v51 op_sel_hi:[0,1]\n"
    "ds_write_b32 v16, v40 offset:3456\n"
    "ds_write_b32 v16, v12 offset:3744\n"
    "s_mov_b64 exec, 1\n"
    "ds_or_b32 v25, v21 offset:532\n"
    "s_mov_b64 exec, -1\n"
    "v_smfmac_f32_16x16x64_f16 v[88:91], v[224:227], v[60:67], v2\n"
    "ds_read_b64_tr_b16 v[48:49], v3 offset:0\n"
    "ds_read_b64_tr_b16 v[50:51], v4 offset:576\n"
    "ds_read_b64_tr_b16 v[52:53], v4 offset:1152\n"
    "v_smfmac_f32_16x16x64_f16 v[88:91], v[252:255], v[68:75], v2\n"
    "s_waitcnt lgkmcnt(12)\n"
    "v_pk_mul_f16 v60, v44, s45 op_sel:[0,1]\n"
    "v_pk_mul_f16 v61, v45, s45 op_sel:[0,1]\n"
    "v_mfma_f32_16x16x32_f16 v[76:79], v[228:231], v[68:71], v[76:79]\n"
    "v_pk_mul_f16 v62, v46, s45 op_sel:[0,1]\n"
    "v_pk_mul_f16 v63, v47, s45 op_sel:[0,1]\n"
    "v_mfma_f32_16x16x32_f16 v[76:79], v[240:243], v[72:75], v[76:79]\n"
    "v_pk_mul_f16 v64, v44, v46\n"
    "v_pk_mul_f16 v65, v45, v47\n"
    "v_mfma_f32_16x16x32_f16 v[80:83], v[244:247], v[72:75], v[80:83]\n"
    "v_add_f32 v11, v88, v89\n"
    "v_add_f32 v13, v90, v91\n"
    "v_mfma_f32_16x16x32_f16 v[80:83], v[232:235], v[68:71], v[80:83]\n"
    "v_mov_b64 v[88:89], 0\n"
    "v_mov_b64 v[90:91], 0\n"
    "v_mfma_f32_16x16x32_f16 v[84:87], v[236:239], v[68:71], v[84:87]\n"
    "s_waitcnt lgkmcnt(0)\n"
    "v_pk_mul_f16 v66, v48, v48 op_sel_hi:[0,1]\n"
    "v_pk_mul_f16 v67, v48, v49 op_sel_hi:[0,1]\n"
    "v_mfma_f32_16x16x32_f16 v[84:87], v[248:251], v[72:75], v[84:87]\n"
    "v_mul_f32 v11, v11, v13\n"
    "v_add_u32 v25, 16, v25\n"
    "s_cmp_eq_u32 s45, -1\n"
    "s_cbranch_scc1 .Lxd_ex3\n"
    "s_cmp_eq_u32 s36, 0\n"
    "s_cbranch_scc1 .Lxd_ph0\n"
    ".Lxd_pl0:\n"
    "s_cbranch_vccnz .Lxd_pr25\n"
    "s_setprio 2\n"
    ".Lxd_pr25:\n"
    "v_smfmac_f32_16x16x64_f16 v[88:91], v[112:115], v[60:67], v2\n"
    "v_fmac_f32 v11, v76, v80\n"
    "v_fmac_f32 v11, v77, v81\n"
    "v_fmac_f32 v11, v78, v82\n"
    "v_fmac_f32 v11, v79, v83\n"
    "v_mfma_f32_16x16x32_f16 v[80:83], v[92:95], v[60:63], 0\n"
    "v_pk_mul_f16 v68, v58, v58 op_sel_hi:[0,1]\n"
    "v_pk_mul_f16 v69, v58, v49 op_sel_hi:[0,1]\n"
    "v_mfma_f32_16x16x32_f16 v[80:83], v[104:107], v[64:67], v[80:83]\n"
    "v_pk_mul_f16 v70, v48, v58 op_sel_hi:[0,1]\n"
    "v_pk_mul_f16 v71, v48, v59 op_sel_hi:[0,1]\n"
    "v_mfma_f32_16x16x32_f16 v[76:79], v[100:103], v[64:67], 0\n"
    "v_fmac_f32 v11, v84, v85\n"
    "v_fma_f32 v10, v86, v87, v11\n"
    "v_mfma_f32_16x16x32_f16 v[84:87], v[108:111], v[64:67], 0\n"
    "v_pk_mul_f16 v72, v50, v58 op_sel_hi:[0,1]\n"
    "v_pk_mul_f16 v73, v50, v59 op_sel_hi:[0,1]\n"
    "v_mfma_f32_16x16x32_f16 v[84:87], v[96:99], v[60:63], v[84:87]\n"
    "v_pk_mul_f16 v74, v52, v58 op_sel_hi:[0,1]\n"
    "v_pk_mul_f16 v75, v52, v59 op_sel_hi:[0,1]\n"
    "v_mfma_f32_16x16x32_f16 v[76:79], v[116:119], v[68:71], v[76:79]\n"
    "v_pk_mul_f16 v60, v54, v58 op_sel_hi:[0,1]\n"
    "v_pk_mul_f16 v61, v54, v59 op_sel_hi:[0,1]\n"
    "v_mfma_f32_16x16x32_f16 v[76:79], v[128:131], v[72:75], v[76:79]\n"
    "v_pk_mul_f16 v62, v56, v58 op_sel_hi:[0,1]\n"
    "v_mfma_f32_16x16x32_f16 v[80:83], v[132:135], v[72:75], v[80:83]\n"
    "v_pk_mul_f16 v63, v56, v59 op_sel_hi:[0,1]\n"
    "v_mfma_f32_16x16x32_f16 v[80:83], v[120:123], v[68:71], v[80:83]\n"
    "v_pk_mul_f16 v64, v56, v56 op_sel_hi:[0,1]\n"
    "v_mfma_f32_16x16x32_f16 v[84:87], v[124:127], v[68:71], v[84:87]\n"
    "v_pk_mul_f16 v65, v56, v49 op_sel_hi:[0,1]\n"
    "v_mfma_f32_16x16x32_f16 v[84:87], v[136:139], v[72:75], v[84:87]\n"
    "v_pk_mul_f16 v66, v48, v56 op_sel_hi:[0,1]\n"
    "v_permlane32_swap_b32 v7, v8\n"
    "v_permlane32_swap_b32 v9, v10\n"
    "v_smfmac_f32_16x16x64_f16 v[88:91], v[140:143], v[68:75], v2\n"
    "v_pk_mul_f16 v67, v48, v57 op_sel_hi:[0,1]\n"
    "v_mul_f32 v7, v0, v7\n"
    "v_mul_f32 v9, v0, v9\n"
    "v_mfma_f32_16x16x32_f16 v[76:79], v[144:147], v[60:63], v[76:79]\n"
    "v_pk_mul_f16 v68, v50, v56 op_sel_hi:[0,1]\n"
    "v_pk_mul_f16 v69, v50, v57 op_sel_hi:[0,1]\n"
    "v_mfma_f32_16x16x32_f16 v[76:79], v[156:159], v[64:67], v[76:79]\n"
    "v_pk_mul_f16 v70, v52, v56 op_sel_hi:[0,1]\n"
    "v_fmac_f32 v7, v23, v8\n"
    "v_fmac_f32 v9, v23, v10\n"
    "v_mfma_f32_16x16x32_f16 v[80:83], v[160:163], v[64:67], v[80:83]\n"
    "v_pk_mul_f16 v71, v52, v57 op_sel_hi:[0,1]\n"
    "v_permlane16_swap_b32 v7, v9\n"
    "v_mfma_f32_16x16x32_f16 v[80:83], v[148:151], v[60:63], v[80:83]\n"
    "v_pk_mul_f16 v72, v54, v56 op_sel_hi:[0,1]\n"
    "ds_read_b32 v22, v25 offset:516\n"
    "v_mfma_f32_16x16x32_f16 v[84:87], v[152:155], v[60:63], v[84:87]\n"
    "v_pk_mul_f16 v73, v54, v57 op_sel_hi:[0,1]\n"
    "v_add_f32 v7, v7, v9\n"
    "v_mfma_f32_16x16x32_f16 v[84:87], v[164:167], v[64:67], v[84:87]\n"
    "v_pk_mul_f16 v74, v54, v54 op_sel_hi:[0,1]\n"
    "v_add_f32_dpp v7, v7, v7 quad_perm:[1,0,3,2] row_mask:0xf bank_mask:0xf\n"
    "v_smfmac_f32_16x16x64_f16 v[88:91], v[168:171], v[60:67], v2\n"
    "v_pk_mul_f16 v75, v54, v49 op_sel_hi:[0,1]\n"
    "v_add_f32_dpp v7, v7, v7 quad_perm:[2,3,0,1] row_mask:0xf bank_mask:0xf\n"
    "v_mfma_f32_16x16x32_f16 v[76:79], v[172:175], v[68:71], v[76:79]\n"
    "v_pk_mul_f16 v60, v48, v54 op_sel_hi:[0,1]\n"
    "v_pk_mul_f16 v61, v48, v55 op_sel_hi:[0,1]\n"
    "v_mfma_f32_16x16x32_f16 v[76:79], v[184:187], v[72:75], v[76:79]\n"
    "v_pk_mul_f16 v62, v50, v54 op_sel_hi:[0,1]\n"
    "v_add_f32_dpp v7, v7, v7 row_half_mirror row_mask:0xf bank_mask:0xf\n"
    "v_mfma_f32_16x16x32_f16 v[80:83], v[188:191], v[72:75], v[80:83]\n"
    "v_pk_mul_f16 v63, v50, v55 op_sel_hi:[0,1]\n"
    "s_waitcnt lgkmcnt(0)\n"
    "v_readfirstlane_b32 s45, v22\n"
    "s_cmp_lt_i32 s45, 16\n"
    "s_cbranch_scc1 .Lxd_ck27\n"
    ".Lxd_ck27r:\n"
    "v_mfma_f32_16x16x32_f16 v[80:83], v[176:179], v[68:71], v[80:83]\n"
    "v_pk_mul_f16 v64, v52, v54 op_sel_hi:[0,1]\n"
    "ds_read_b64_tr_b16 v[44:45], v5 offset:4096\n"
    "v_mfma_f32_16x16x32_f16 v[84:87], v[180:183], v[68:71], v[84:87]\n"
    "v_pk_mul_f16 v65, v52, v55 op_sel_hi:[0,1]\n"
    "ds_read_b64_tr_b16 v[46:47], v6 offset:4096\n"
    "v_mfma_f32_16x16x32_f16 v[84:87], v[192:195], v[72:75], v[84:87]\n"
    "v_pk_mul_f16 v66, v52, v52 op_sel_hi:[0,1]\n"
    "ds_read_b64_tr_b16 v[58:59], v4 offset:6976\n"
    "s_cbranch_vccnz .Lxd_pr26\n"
    "s_setprio 0\n"
    ".Lxd_pr26:\n"
    "v_smfmac_f32_16x16x64_f16 v[88:91], v[196:199], v[68:75], v2\n"
    "v_pk_mul_f16 v67, v52, v49 op_sel_hi:[0,1]\n"
    "ds_read_b64_tr_b16 v[56:57], v4 offset:6400\n"
    "v_mfma_f32_16x16x32_f16 v[76:79], v[200:203], v[60:63], v[76:79]\n"
    "v_pk_mul_f16 v68, v48, v52 op_sel_hi:[0,1]\n"
    "v_pk_mul_f16 v69, v48, v53 op_sel_hi:[0,1]\n"
    "v_mfma_f32_16x16x32_f16 v[76:79], v[212:215], v[64:67], v[76:79]\n"
    "v_pk_mul_f16 v70, v50, v52 op_sel_hi:[0,1]\n"
    "ds_read_b64_tr_b16 v[54:55], v4 offset:5824\n"
    "v_mfma_f32_16x16x32_f16 v[80:83], v[216:219], v[64:67], v[80:83]\n"
    "v_pk_mul_f16 v71, v50, v53 op_sel_hi:[0,1]\n"
    "v_add_f32_dpp v7, v7, v7 row_mirror row_mask:0xf bank_mask:0xf\n"
    "v_mfma_f32_16x16x32_f16 v[80:83], v[204:207], v[60:63], v[80:83]\n"
    "v_pk_mul_f16 v72, v50, v50 op_sel_hi:[0,1]\n"
    "ds_write_b32 v14, v7\n"
    "v_add_u32 v14, 64, v14\n"
    "v_mfma_f32_16x16x32_f16 v[84:87], v[208:211], v[60:63], v[84:87]\n"
    "v_pk_mul_f16 v73, v50, v49 op_sel_hi:[0,1]\n"
    "v_pk_mul_f16 v74, v48, v50 op_sel_hi:[0,1]\n"
    "v_mfma_f32_16x16x32_f16 v[84:87], v[220:223], v[64:67], v[84:87]\n"
    "v_pk_mul_f16 v75, v48, v51 op_sel_hi:[0,1]\n"
    "v_smfmac_f32_16x16x64_f16 v[88:91], v[224:227], v[60:67], v2\n"
    "ds_read_b64_tr_b16 v[48:49], v3 offset:4096\n"
    "ds_read_b64_tr_b16 v[50:51], v4 offset:4672\n"
    "ds_read_b64_tr_b16 v[52:53], v4 offset:5248\n"
    "v_smfmac_f32_16x16x64_f16 v[88:91], v[252:255], v[68:75], v2\n"
    "s_waitcnt lgkmcnt(7)\n"
    "v_pk_mul_f16 v60, v44, s45 op_sel:[0,1]\n"
    "v_pk_mul_f16 v61, v45, s45 op_sel:[0,1]\n"
    "v_mfma_f32_16x16x32_f16 v[76:79], v[228:231], v[68:71], v[76:79]\n"
    "v_pk_mul_f16 v62, v46, s45 op_sel:[0,1]\n"
    "v_pk_mul_f16 v63, v47, s45 op_sel:[0,1]\n"
    "v_mfma_f32_16x16x32_f16 v[76:79], v[240:243], v[72:75], v[76:79]\n"
    "v_pk_mul_f16 v64, v44, v46\n"
    "v_pk_mul_f16 v65, v45, v47\n"
    "v_mfma_f32_16x16x32_f16 v[80:83], v[244:247], v[72:75], v[80:83]\n"
    "v_add_f32 v11, v88, v89\n"
    "v_add_f32 v13, v90, v91\n"
    "v_mfma_f32_16x16x32_f16 v[80:83], v[232:235], v[68:71], v[80:83]\n"
    "v_mov_b64 v[88:89], 0\n"
    "v_mov_b64 v[90:91], 0\n"
    "v_mfma_f32_16x16x32_f16 v[84:87], v[236:239], v[68:71], v[84:87]\n"
    "s_waitcnt lgkmcnt(0)\n"
    "v_pk_mul_f16 v66, v48, v48 op_sel_hi:[0,1]\n"
    "v_pk_mul_f16 v67, v48, v49 op_sel_hi:[0,1]\n"
    "v_mfma_f32_16x16x32_f16 v[84:87], v[248:251], v[72:75], v[84:87]\n"
    "v_mul_f32 v11, v11, v13\n"
    "s_cmp_eq_u32 s45, -1\n"
    "s_cbranch_scc1 .Lxd_ex0\n"
    ".Lxd_pl1:\n"
    "s_cbranch_vccnz .Lxd_pr28\n"
    "s_setprio 2\n"
    ".Lxd_pr28:\n"
    "v_smfmac_f32_16x16x64_f16 v[88:91], v[112:115], v[60:67], v2\n"
    "v_fmac_f32 v11, v76, v80\n"
    "v_fmac_f32 v11, v77, v81\n"
    "v_fmac_f32 v11, v78, v82\n"
    "v_fmac_f32 v11, v79, v83\n"
    "v_mfma_f32_16x16x32_f16 v[80:83], v[92:95], v[60:63], 0\n"
    "v_pk_mul_f16 v68, v58, v58 op_sel_hi:[0,1]\n"
    "v_pk_mul_f16 v69, v58, v49 op_sel_hi:[0,1]\n"
    "v_mfma_f32_16x16x32_f16 v[80:83], v[104:107], v[64:67], v[80:83]\n"
    "v_pk_mul_f16 v70, v48, v58 op_sel_hi:[0,1]\n"
    "v_pk_mul_f16 v71, v48, v59 op_sel_hi:[0,1]\n"
    "v_mfma_f32_16x16x32_f16 v[76:79], v[100:103], v[64:67], 0\n"
    "v_fmac_f32 v11, v84, v85\n"
    "v_fma_f32 v7, v86, v87, v11\n"
    "v_mfma_f32_16x16x32_f16 v[84:87], v[108:111], v[64:67], 0\n"
    "v_pk_mul_f16 v72, v50, v58 op_sel_hi:[0,1]\n"
    "v_pk_mul_f16 v73, v50, v59 op_sel_hi:[0,1]\n"
    "v_mfma_f32_16x16x32_f16 v[84:87], v[96:99], v[60:63], v[84:87]\n"
    "v_pk_mul_f16 v74, v52, v58 op_sel_hi:[0,1]\n"
    "v_pk_mul_f16 v75, v52, v59 op_sel_hi:[0,1]\n"
    "v_mfma_f32_16x16x32_f16 v[76:79], v[116:119], v[68:71], v[76:79]\n"
    "v_pk_mul_f16 v60, v54, v58 op_sel_hi:[0,1]\n"
    "v_pk_mul_f16 v61, v54, v59 op_sel_hi:[0,1]\n"
    "v_mfma_f32_16x16x32_f16 v[76:79], v[128:131], v[72:75], v[76:79]\n"
    "v_pk_mul_f16 v62, v56, v58 op_sel_hi:[0,1]\n"
    "v_mfma_f32_16x16x32_f16 v[80:83], v[132:135], v[72:75], v[80:83]\n"
    "v_pk_mul_f16 v63, v56, v59 op_sel_hi:[0,1]\n"
    "v_mfma_f32_16x16x32_f16 v[80:83], v[120:123], v[68:71], v[80:83]\n"
    "v_pk_mul_f16 v64, v56, v56 op_sel_hi:[0,1]\n"
    "v_mfma_f32_16x16x32_f16 v[84:87], v[124:127], v[68:71], v[84:87]\n"
    "v_pk_mul_f16 v65, v56, v49 op_sel_hi:[0,1]\n"
    "v_mfma_f32_16x16x32_f16 v[84:87], v[136:139], v[72:75], v[84:87]\n"
    "v_pk_mul_f16 v66, v48, v56 op_sel_hi:[0,1]\n"
    "v_smfmac_f32_16x16x64_f16 v[88:91], v[140:143], v[68:75], v2\n"
    "v_pk_mul_f16 v67, v48, v57 op_sel_hi:[0,1]\n"
    "v_mfma_f32_16x16x32_f16 v[76:79], v[144:147], v[60:63], v[76:79]\n"
    "v_pk_mul_f16 v68, v50, v56 op_sel_hi:[0,1]\n"
    "v_pk_mul_f16 v69, v50, v57 op_sel_hi:[0,1]\n"
    "v_mfma_f32_16x16x32_f16 v[76:79], v[156:159], v[64:67], v[76:79]\n"
    "v_pk_mul_f16 v70, v52, v56 op_sel_hi:[0,1]\n"
    "v_mfma_f32_16x16x32_f16 v[80:83], v[160:163], v[64:67], v[80:83]\n"
    "v_pk_mul_f16 v71, v52, v57 op_sel_hi:[0,1]\n"
    "v_mfma_f32_16x16x32_f16 v[80:83], v[148:151], v[60:63], v[80:83]\n"
    "v_pk_mul_f16 v72, v54, v56 op_sel_hi:[0,1]\n"
    "ds_read_b32 v22, v25 offset:520\n"
    "v_mfma_f32_16x16x32_f16 v[84:87], v[152:155], v[60:63], v[84:87]\n"
    "v_pk_mul_f16 v73, v54, v57 op_sel_hi:[0,1]\n"
    "v_mfma_f32_16x16x32_f16 v[84:87], v[164:167], v[64:67], v[84:87]\n"
    "v_pk_mul_f16 v74, v54, v54 op_sel_hi:[0,1]\n"
    "v_smfmac_f32_16x16x64_f16 v[88:91], v[168:171], v[60:67], v2\n"
    "v_pk_mul_f16 v75, v54, v49 op_sel_hi:[0,1]\n"
    "v_mfma_f32_16x16x32_f16 v[76:79], v[172:175], v[68:71], v[76:79]\n"
    "v_pk_mul_f16 v60, v48, v54 op_sel_hi:[0,1]\n"
    "v_pk_mul_f16 v61, v48, v55 op_sel_hi:[0,1]\n"
    "v_mfma_f32_16x16x32_f16 v[76:79], v[184:187], v[72:75], v[76:79]\n"
    "v_pk_mul_f16 v62, v50, v54 op_sel_hi:[0,1]\n"
    "v_mfma_f32_16x16x32_f16 v[80:83], v[188:191], v[72:75], v[80:83]\n"
    "v_pk_mul_f16 v63, v50, v55 op_sel_hi:[0,1]\n"
    "s_waitcnt lgkmcnt(0)\n"
    "v_readfirstlane_b32 s45, v22\n"
    "s_cmp_lt_i32 s45, 16\n"
    "s_cbranch_scc1 .Lxd_ck30\n"
    ".Lxd_ck30r:\n"
    "v_mfma_f32_16x16x32_f16 v[80:83], v[176:179], v[68:71], v[80:83]\n"
    "v_pk_mul_f16 v64, v52, v54 op_sel_hi:[0,1]\n"
    "ds_read_b64_tr_b16 v[44:45], v5 offset:8192\n"
    "v_mfma_f32_16x16x32_f16 v[84:87], v[180:183], v[68:71], v[84:87]\n"
    "v_pk_mul_f16 v65, v52, v55 op_sel_hi:[0,1]\n"
    "ds_read_b64_tr_b16 v[46:47], v6 offset:8192\n"
    "v_mfma_f32_16x16x32_f16 v[84:87], v[192:195], v[72:75], v[84:87]\n"
    "v_pk_mul_f16 v66, v52, v52 op_sel_hi:[0,1]\n"
    "ds_read_b64_tr_b16 v[58:59], v4 offset:11072\n"
    "s_cbranch_vccnz .Lxd_pr29\n"
    "s_setprio 0\n"
    ".Lxd_pr29:\n"
    "v_smfmac_f32_16x16x64_f16 v[88:91], v[196:199], v[68:75], v2\n"
    "v_pk_mul_f16 v67, v52, v49 op_sel_hi:[0,1]\n"
    "ds_read_b64_tr_b16 v[56:57], v4 offset:10496\n"
    "v_mfma_f32_16x16x32_f16 v[76:79], v[200:203], v[60:63], v[76:79]\n"
    "v_pk_mul_f16 v68, v48, v52 op_sel_hi:[0,1]\n"
    "v_pk_mul_f16 v69, v48, v53 op_sel_hi:[0,1]\n"
    "v_mfma_f32_16x16x32_f16 v[76:79], v[212:215], v[64:67], v[76:79]\n"
    "v_pk_mul_f16 v70, v50, v52 op_sel_hi:[0,1]\n"
    "ds_read_b64_tr_b16 v[54:55], v4 offset:9920\n"
    "v_mfma_f32_16x16x32_f16 v[80:83], v[216:219], v[64:67], v[80:83]\n"
    "v_pk_mul_f16 v71, v50, v53 op_sel_hi:[0,1]\n"
    "v_mfma_f32_16x16x32_f16 v[80:83], v[204:207], v[60:63], v[80:83]\n"
    "v_pk_mul_f16 v72, v50, v50 op_sel_hi:[0,1]\n"
    "v_mfma_f32_16x16x32_f16 v[84:87], v[208:211], v[60:63], v[84:87]\n"
    "v_pk_mul_f16 v73, v50, v49 op_sel_hi:[0,1]\n"
    "v_pk_mul_f16 v74, v48, v50 op_sel_hi:[0,1]\n"
    "v_mfma_f32_16x16x32_f16 v[84:87], v[220:223], v[64:67], v[84:87]\n"
    "v_pk_mul_f16 v75, v48, v51 op_sel_hi:[0,1]\n"
    "v_smfmac_f32_16x16x64_f16 v[88:91], v[224:227], v[60:67], v2\n"
    "ds_read_b64_tr_b16 v[48:49], v3 offset:8192\n"
    "ds_read_b64_tr_b16 v[50:51], v4 offset:8768\n"
    "ds_read_b64_tr_b16 v[52:53], v4 offset:9344\n"
    "v_smfmac_f32_16x16x64_f16 v[88:91], v[252:255], v[68:75], v2\n"
    "s_waitcnt lgkmcnt(6)\n"
    "v_pk_mul_f16 v60, v44, s45 op_sel:[0,1]\n"
    "v_pk_mul_f16 v61, v45, s45 op_sel:[0,1]\n"
    "v_mfma_f32_16x16x32_f16 v[76:79], v[228:231], v[68:71], v[76:79]\n"
    "v_pk_mul_f16 v62, v46, s45 op_sel:[0,1]\n"
    "v_pk_mul_f16 v63, v47, s45 op_sel:[0,1]\n"
    "v_mfma_f32_16x16x32_f16 v[76:79], v[240:243], v[72:75], v[76:79]\n"
    "v_pk_mul_f16 v64, v44, v46\n"
    "v_pk_mul_f16 v65, v45, v47\n"
    "v_mfma_f32_16x16x32_f16 v[80:83], v[244:247], v[72:75], v[80:83]\n"
    "v_add_f32 v11, v88, v89\n"
    "v_add_f32 v13, v90, v91\n"
    "v_mfma_f32_16x16x32_f16 v[80:83], v[232:235], v[68:71], v[80:83]\n"
    "v_mov_b64 v[88:89], 0\n"
    "v_mov_b64 v[90:91], 0\n"
    "v_mfma_f32_16x16x32_f16 v[84:87], v[236:239], v[68:71], v[84:87]\n"
    "s_waitcnt lgkmcnt(0)\n"
    "v_pk_mul_f16 v66, v48, v48 op_sel_hi:[0,1]\n"
    "v_pk_mul_f16 v67, v48, v49 op_sel_hi:[0,1]\n"
    "v_mfma_f32_16x16x32_f16 v[84:87], v[248:251], v[72:75], v[84:87]\n"
    "v_mul_f32 v11, v11, v13\n"
    "s_cmp_eq_u32 s45, -1\n"
    "s_cbranch_scc1 .Lxd_ex1\n"
    ".Lxd_pl2:\n"
    "s_cbranch_vccnz .Lxd_pr31\n"
    "s_setprio 2\n"
    ".Lxd_pr31:\n"
    "v_smfmac_f32_16x16x64_f16 v[88:91], v[112:115], v[60:67], v2\n"
    "v_fmac_f32 v11, v76, v80\n"
    "v_fmac_f32 v11, v77, v81\n"
    "v_fmac_f32 v11, v78, v82\n"
    "v_fmac_f32 v11, v79, v83\n"
    "v_mfma_f32_16x16x32_f16 v[80:83], v[92:95], v[60:63], 0\n"
    "v_pk_mul_f16 v68, v58, v58 op_sel_hi:[0,1]\n"
    "v_pk_mul_f16 v69, v58, v49 op_sel_hi:[0,1]\n"
    "v_mfma_f32_16x16x32_f16 v[80:83], v[104:107], v[64:67], v[80:83]\n"
    "v_pk_mul_f16 v70, v48, v58 op_sel_hi:[0,1]\n"
    "v_pk_mul_f16 v71, v48, v59 op_sel_hi:[0,1]\n"
    "v_mfma_f32_16x16x32_f16 v[76:79], v[100:103], v[64:67], 0\n"
    "v_fmac_f32 v11, v84, v85\n"
    "v_fma_f32 v8, v86, v87, v11\n"
    "v_mfma_f32_16x16x32_f16 v[84:87], v[108:111], v[64:67], 0\n"
    "v_pk_mul_f16 v72, v50, v58 op_sel_hi:[0,1]\n"
    "v_pk_mul_f16 v73, v50, v59 op_sel_hi:[0,1]\n"
    "v_mfma_f32_16x16x32_f16 v[84:87], v[96:99], v[60:63], v[84:87]\n"
    "v_pk_mul_f16 v74, v52, v58 op_sel_hi:[0,1]\n"
    "v_pk_mul_f16 v75, v52, v59 op_sel_hi:[0,1]\n"
    "v_mfma_f32_16x16x32_f16 v[76:79], v[116:119], v[68:71], v[76:79]\n"
    "v_pk_mul_f16 v60, v54, v58 op_sel_hi:[0,1]\n"
    "v_pk_mul_f16 v61, v54, v59 op_sel_hi:[0,1]\n"
    "v_mfma_f32_16x16x32_f16 v[76:79], v[128:131], v[72:75], v[76:79]\n"
    "v_pk_mul_f16 v62, v56, v58 op_sel_hi:[0,1]\n"
    "v_mfma_f32_16x16x32_f16 v[80:83], v[132:135], v[72:75], v[80:83]\n"
    "v_pk_mul_f16 v63, v56, v59 op_sel_hi:[0,1]\n"
    "v_mfma_f32_16x16x32_f16 v[80:83], v[120:123], v[68:71], v[80:83]\n"
    "v_pk_mul_f16 v64, v56, v56 op_sel_hi:[0,1]\n"
    "v_mfma_f32_16x16x32_f16 v[84:87], v[124:127], v[68:71], v[84:87]\n"
    "v_pk_mul_f16 v65, v56, v49 op_sel_hi:[0,1]\n"
    "v_mfma_f32_16x16x32_f16 v[84:87], v[136:139], v[72:75], v[84:87]\n"
    "v_pk_mul_f16 v66, v48, v56 op_sel_hi:[0,1]\n"
    "v_smfmac_f32_16x16x64_f16 v[88:91], v[140:143], v[68:75], v2\n"
    "v_pk_mul_f16 v67, v48, v57 op_sel_hi:[0,1]\n"
    "v_mfma_f32_16x16x32_f16 v[76:79], v[144:147], v[60:63], v[76:79]\n"
    "v_pk_mul_f16 v68, v50, v56 op_sel_hi:[0,1]\n"
    "v_pk_mul_f16 v69, v50, v57 op_sel_hi:[0,1]\n"
    "v_mfma_f32_16x16x32_f16 v[76:79], v[156:159], v[64:67], v[76:79]\n"
    "v_pk_mul_f16 v70, v52, v56 op_sel_hi:[0,1]\n"
    "v_mfma_f32_16x16x32_f16 v[80:83], v[160:163], v[64:67], v[80:83]\n"
    "v_pk_mul_f16 v71, v52, v57 op_sel_hi:[0,1]\n"
    "v_mfma_f32_16x16x32_f16 v[80:83], v[148:151], v[60:63], v[80:83]\n"
    "v_pk_mul_f16 v72, v54, v56 op_sel_hi:[0,1]\n"
    "ds_read_b32 v22, v25 offset:524\n"
    "v_mfma_f32_16x16x32_f16 v[84:87], v[152:155], v[60:63], v[84:87]\n"
    "v_pk_mul_f16 v73, v54, v57 op_sel_hi:[0,1]\n"
    "v_mfma_f32_16x16x32_f16 v[84:87], v[164:167], v[64:67], v[84:87]\n"
    "v_pk_mul_f16 v74, v54, v54 op_sel_hi:[0,1]\n"
    "v_smfmac_f32_16x16x64_f16 v[88:91], v[168:171], v[60:67], v2\n"
    "v_pk_mul_f16 v75, v54, v49 op_sel_hi:[0,1]\n"
    "v_mfma_f32_16x16x32_f16 v[76:79], v[172:175], v[68:71], v[76:79]\n"
    "v_pk_mul_f16 v60, v48, v54 op_sel_hi:[0,1]\n"
    "v_pk_mul_f16 v61, v48, v55 op_sel_hi:[0,1]\n"
    "v_mfma_f32_16x16x32_f16 v[76:79], v[184:187], v[72:75], v[76:79]\n"
    "v_pk_mul_f16 v62, v50, v54 op_sel_hi:[0,1]\n"
    "v_mfma_f32_16x16x32_f16 v[80:83], v[188:191], v[72:75], v[80:83]\n"
    "v_pk_mul_f16 v63, v50, v55 op_sel_hi:[0,1]\n"
    "s_waitcnt lgkmcnt(0)\n"
    "v_readfirstlane_b32 s45, v22\n"
    "s_cmp_lt_i32 s45, 16\n"
    "s_cbranch_scc1 .Lxd_ck33\n"
    ".Lxd_ck33r:\n"
    "v_mfma_f32_16x16x32_f16 v[80:83], v[176:179], v[68:71], v[80:83]\n"
    "v_pk_mul_f16 v64, v52, v54 op_sel_hi:[0,1]\n"
    "ds_read_b64_tr_b16 v[44:45], v5 offset:12288\n"
    "v_mfma_f32_16x16x32_f16 v[84:87], v[180:183], v[68:71], v[84:87]\n"
    "v_pk_mul_f16 v65, v52, v55 op_sel_hi:[0,1]\n"
    "ds_read_b64_tr_b16 v[46:47], v6 offset:12288\n"
    "v_mfma_f32_16x16x32_f16 v[84:87], v[192:195], v[72:75], v[84:87]\n"
    "v_pk_mul_f16 v66, v52, v52 op_sel_hi:[0,1]\n"
    "ds_read_b64_tr_b16 v[58:59], v4 offset:15168\n"
    "s_cbranch_vccnz .Lxd_pr32\n"
    "s_setprio 0\n"
    ".Lxd_pr32:\n"
    "v_smfmac_f32_16x16x64_f16 v[88:91], v[196:199], v[68:75], v2\n"
    "v_pk_mul_f16 v67, v52, v49 op_sel_hi:[0,1]\n"
    "ds_read_b64_tr_b16 v[56:57], v4 offset:14592\n"
    "v_mfma_f32_16x16x32_f16 v[76:79], v[200:203], v[60:63], v[76:79]\n"
    "v_pk_mul_f16 v68, v48, v52 op_sel_hi:[0,1]\n"
    "v_pk_mul_f16 v69, v48, v53 op_sel_hi:[0,1]\n"
    "v_mfma_f32_16x16x32_f16 v[76:79], v[212:215], v[64:67], v[76:79]\n"
    "v_pk_mul_f16 v70, v50, v52 op_sel_hi:[0,1]\n"
    "ds_read_b64_tr_b16 v[54:55], v4 offset:14016\n"
    "v_mfma_f32_16x16x32_f16 v[80:83], v[216:219], v[64:67], v[80:83]\n"
    "v_pk_mul_f16 v71, v50, v53 op_sel_hi:[0,1]\n"
    "v_mfma_f32_16x16x32_f16 v[80:83], v[204:207], v[60:63], v[80:83]\n"
    "v_pk_mul_f16 v72, v50, v50 op_sel_hi:[0,1]\n"
    "v_mfma_f32_16x16x32_f16 v[84:87], v[208:211], v[60:63], v[84:87]\n"
    "v_pk_mul_f16 v73, v50, v49 op_sel_hi:[0,1]\n"
    "v_pk_mul_f16 v74, v48, v50 op_sel_hi:[0,1]\n"
    "v_mfma_f32_16x16x32_f16 v[84:87], v[220:223], v[64:67], v[84:87]\n"
    "v_pk_mul_f16 v75, v48, v51 op_sel_hi:[0,1]\n"
    "v_smfmac_f32_16x16x64_f16 v[88:91], v[224:227], v[60:67], v2\n"
    "ds_read_b64_tr_b16 v[48:49], v3 offset:12288\n"
    "ds_read_b64_tr_b16 v[50:51], v4 offset:12864\n"
    "ds_read_b64_tr_b16 v[52:53], v4 offset:13440\n"
    "v_smfmac_f32_16x16x64_f16 v[88:91], v[252:255], v[68:75], v2\n"
    "s_waitcnt lgkmcnt(6)\n"
    "v_pk_mul_f16 v60, v44, s45 op_sel:[0,1]\n"
    "v_pk_mul_f16 v61, v45, s45 op_sel:[0,1]\n"
    "v_mfma_f32_16x16x32_f16 v[76:79], v[228:231], v[68:71], v[76:79]\n"
    "v_pk_mul_f16 v62, v46, s45 op_sel:[0,1]\n"
    "v_pk_mul_f16 v63, v47, s45 op_sel:[0,1]\n"
    "v_mfma_f32_16x16x32_f16 v[76:79], v[240:243], v[72:75], v[76:79]\n"
    "v_pk_mul_f16 v64, v44, v46\n"
    "v_pk_mul_f16 v65, v45, v47\n"
    "v_mfma_f32_16x16x32_f16 v[80:83], v[244:247], v[72:75], v[80:83]\n"
    "v_add_f32 v11, v88, v89\n"
    "v_add_f32 v13, v90, v91\n"
    "v_mfma_f32_16x16x32_f16 v[80:83], v[232:235], v[68:71], v[80:83]\n"
    "v_mov_b64 v[88:89], 0\n"
    "v_mov_b64 v[90:91], 0\n"
    "v_mfma_f32_16x16x32_f16 v[84:87], v[236:239], v[68:71], v[84:87]\n"
    "s_waitcnt lgkmcnt(0)\n"
    "v_pk_mul_f16 v66, v48, v48 op_sel_hi:[0,1]\n"
    "v_pk_mul_f16 v67, v48, v49 op_sel_hi:[0,1]\n"
    "v_mfma_f32_16x16x32_f16 v[84:87], v[248:251], v[72:75], v[84:87]\n"
    "v_mul_f32 v11, v11, v13\n"
    "s_cmp_eq_u32 s45, -1\n"
    "s_cbranch_scc1 .Lxd_ex2\n"
    ".Lxd_pl3:\n"
    "s_cbranch_vccnz .Lxd_pr34\n"
    "s_setprio 2\n"
    ".Lxd_pr34:\n"
    "v_smfmac_f32_16x16x64_f16 v[88:91], v[112:115], v[60:67], v2\n"
    "v_fmac_f32 v11, v76, v80\n"
    "v_fmac_f32 v11, v77, v81\n"
    "v_fmac_f32 v11, v78, v82\n"
    "v_fmac_f32 v11, v79, v83\n"
    "v_mfma_f32_16x16x32_f16 v[80:83], v[92:95], v[60:63], 0\n"
    "v_pk_mul_f16 v68, v58, v58 op_sel_hi:[0,1]\n"
    "v_pk_mul_f16 v69, v58, v49 op_sel_hi:[0,1]\n"
    "v_mfma_f32_16x16x32_f16 v[80:83], v[104:107], v[64:67], v[80:83]\n"
    "v_pk_mul_f16 v70, v48, v58 op_sel_hi:[0,1]\n"
    "v_pk_mul_f16 v71, v48, v59 op_sel_hi:[0,1]\n"
    "v_mfma_f32_16x16x32_f16 v[76:79], v[100:103], v[64:67], 0\n"
    "v_fmac_f32 v11, v84, v85\n"
    "v_fma_f32 v9, v86, v87, v11\n"
    "v_mfma_f32_16x16x32_f16 v[84:87], v[108:111], v[64:67], 0\n"
    "v_pk_mul_f16 v72, v50, v58 op_sel_hi:[0,1]\n"
    "v_pk_mul_f16 v73, v50, v59 op_sel_hi:[0,1]\n"
    "v_mfma_f32_16x16x32_f16 v[84:87], v[96:99], v[60:63], v[84:87]\n"
    "v_pk_mul_f16 v74, v52, v58 op_sel_hi:[0,1]\n"
    "v_pk_mul_f16 v75, v52, v59 op_sel_hi:[0,1]\n"
    "v_mfma_f32_16x16x32_f16 v[76:79], v[116:119], v[68:71], v[76:79]\n"
    "v_pk_mul_f16 v60, v54, v58 op_sel_hi:[0,1]\n"
    "v_pk_mul_f16 v61, v54, v59 op_sel_hi:[0,1]\n"
    "v_mfma_f32_16x16x32_f16 v[76:79], v[128:131], v[72:75], v[76:79]\n"
    "v_pk_mul_f16 v62, v56, v58 op_sel_hi:[0,1]\n"
    "s_cmp_eq_u32 s36, 0\n"
    "s_cbranch_scc1 .Lxd_on36\n"
    ".Lxd_on36r:\n"
    "v_mfma_f32_16x16x32_f16 v[80:83], v[132:135], v[72:75], v[80:83]\n"
    "v_pk_mul_f16 v63, v56, v59 op_sel_hi:[0,1]\n"
    "v_xor_b32 v3, 0x4000, v3\n"
    "v_mfma_f32_16x16x32_f16 v[80:83], v[120:123], v[68:71], v[80:83]\n"
    "v_pk_mul_f16 v64, v56, v56 op_sel_hi:[0,1]\n"
    "v_xor_b32 v4, 0x4000, v4\n"
    "v_mfma_f32_16x16x32_f16 v[84:87], v[124:127], v[68:71], v[84:87]\n"
    "v_pk_mul_f16 v65, v56, v49 op_sel_hi:[0,1]\n"
    "v_xor_b32 v5, 0x4000, v5\n"
    "v_mfma_f32_16x16x32_f16 v[84:87], v[136:139], v[72:75], v[84:87]\n"
    "v_pk_mul_f16 v66, v48, v56 op_sel_hi:[0,1]\n"
    "v_xor_b32 v6, 0x4000, v6\n"
    "v_smfmac_f32_16x16x64_f16 v[88:91], v[140:143], v[68:75], v2\n"
    "v_pk_mul_f16 v67, v48, v57 op_sel_hi:[0,1]\n"
    "v_mfma_f32_16x16x32_f16 v[76:79], v[144:147], v[60:63], v[76:79]\n"
    "v_pk_mul_f16 v68, v50, v56 op_sel_hi:[0,1]\n"
    "v_pk_mul_f16 v69, v50, v57 op_sel_hi:[0,1]\n"
    "v_mfma_f32_16x16x32_f16 v[76:79], v[156:159], v[64:67], v[76:79]\n"
    "v_pk_mul_f16 v70, v52, v56 op_sel_hi:[0,1]\n"
    "v_mfma_f32_16x16x32_f16 v[80:83], v[160:163], v[64:67], v[80:83]\n"
    "v_pk_mul_f16 v71, v52, v57 op_sel_hi:[0,1]\n"
    "v_mfma_f32_16x16x32_f16 v[80:83], v[148:151], v[60:63], v[80:83]\n"
    "v_pk_mul_f16 v72, v54, v56 op_sel_hi:[0,1]\n"
    "ds_read_b32 v22, v25 offset:528\n"
    "v_mfma_f32_16x16x32_f16 v[84:87], v[152:155], v[60:63], v[84:87]\n"
    "v_pk_mul_f16 v73, v54, v57 op_sel_hi:[0,1]\n"
    "v_mfma_f32_16x16x32_f16 v[84:87], v[164:167], v[64:67], v[84:87]\n"
    "v_pk_mul_f16 v74, v54, v54 op_sel_hi:[0,1]\n"
    "v_smfmac_f32_16x16x64_f16 v[88:91], v[168:171], v[60:67], v2\n"
    "v_pk_mul_f16 v75, v54, v49 op_sel_hi:[0,1]\n"
    "v_mfma_f32_16x16x32_f16 v[76:79], v[172:175], v[68:71], v[76:79]\n"
    "v_pk_mul_f16 v60, v48, v54 op_sel_hi:[0,1]\n"
    "v_pk_mul_f16 v61, v48, v55 op_sel_hi:[0,1]\n"
    "v_mfma_f32_16x16x32_f16 v[76:79], v[184:187], v[72:75], v[76:79]\n"
    "v_pk_mul_f16 v62, v50, v54 op_sel_hi:[0,1]\n"
    "v_mfma_f32_16x16x32_f16 v[80:83], v[188:191], v[72:75], v[80:83]\n"
    "v_pk_mul_f16 v63, v50, v55 op_sel_hi:[0,1]\n"
    "s_waitcnt lgkmcnt(0)\n"
    "v_readfirstlane_b32 s45, v22\n"
    "s_cmp_lt_i32 s45, 16\n"
    "s_cbranch_scc1 .Lxd_ck38\n"
    ".Lxd_ck38r:\n"
    "v_mfma_f32_16x16x32_f16 v[80:83], v[176:179], v[68:71], v[80:83]\n"
    "v_pk_mul_f16 v64, v52, v54 op_sel_hi:[0,1]\n"
    "ds_read_b64_tr_b16 v[44:45], v5 offset:0\n"
    "v_mfma_f32_16x16x32_f16 v[84:87], v[180:183], v[68:71], v[84:87]\n"
    "v_pk_mul_f16 v65, v52, v55 op_sel_hi:[0,1]\n"
    "ds_read_b64_tr_b16 v[46:47], v6 offset:0\n"
    "v_mfma_f32_16x16x32_f16 v[84:87], v[192:195], v[72:75], v[84:87]\n"
    "v_pk_mul_f16 v66, v52, v52 op_sel_hi:[0,1]\n"
    "ds_read_b64_tr_b16 v[58:59], v4 offset:2880\n"
    "s_cbranch_vccnz .Lxd_pr35\n"
    "s_setprio 0\n"
    ".Lxd_pr35:\n"
    "v_smfmac_f32_16x16x64_f16 v[88:91], v[196:199], v[68:75], v2\n"
    "v_pk_mul_f16 v67, v52, v49 op_sel_hi:[0,1]\n"
    "ds_read_b64_tr_b16 v[56:57], v4 offset:2304\n"
    "v_mfma_f32_16x16x32_f16 v[76:79], v[200:203], v[60:63], v[76:79]\n"
    "v_pk_mul_f16 v68, v48, v52 op_sel_hi:[0,1]\n"
    "v_pk_mul_f16 v69, v48, v53 op_sel_hi:[0,1]\n"
    "v_mfma_f32_16x16x32_f16 v[76:79], v[212:215], v[64:67], v[76:79]\n"
    "v_pk_mul_f16 v70, v50, v52 op_sel_hi:[0,1]\n"
    "ds_read_b64_tr_b16 v[54:55], v4 offset:1728\n"
    "v_mfma_f32_16x16x32_f16 v[80:83], v[216:219], v[64:67], v[80:83]\n"
    "v_pk_mul_f16 v71, v50, v53 op_sel_hi:[0,1]\n"
    "v_mfma_f32_16x16x32_f16 v[80:83], v[204:207], v[60:63], v[80:83]\n"
    "v_pk_mul_f16 v72, v50, v50 op_sel_hi:[0,1]\n"
    "v_mfma_f32_16x16x32_f16 v[84:87], v[208:211], v[60:63], v[84:87]\n"
    "v_pk_mul_f16 v73, v50, v49 op_sel_hi:[0,1]\n"
    "v_pk_mul_f16 v74, v48, v50 op_sel_hi:[0,1]\n"
    "v_mfma_f32_16x16x32_f16 v[84:87], v[220:223], v[64:67], v[84:87]\n"
    "v_pk_mul_f16 v75, v48, v51 op_sel_hi:[0,1]\n"
    "v_smfmac_f32_16x16x64_f16 v[88:91], v[224:227], v[60:67], v2\n"
    "ds_read_b64_tr_b16 v[48:49], v3 offset:0\n"
    "ds_read_b64_tr_b16 v[50:51], v4 offset:576\n"
    "ds_read_b64_tr_b16 v[52:53], v4 offset:1152\n"
    "v_smfmac_f32_16x16x64_f16 v[88:91], v[252:255], v[68:75], v2\n"
    "s_waitcnt lgkmcnt(6)\n"
    "v_pk_mul_f16 v60, v44, s45 op_sel:[0,1]\n"
    "v_pk_mul_f16 v61, v45, s45 op_sel:[0,1]\n"
    "v_mfma_f32_16x16x32_f16 v[76:79], v[228:231], v[68:71], v[76:79]\n"
    "v_pk_mul_f16 v62, v46, s45 op_sel:[0,1]\n"
    "v_pk_mul_f16 v63, v47, s45 op_sel:[0,1]\n"
    "v_mfma_f32_16x16x32_f16 v[76:79], v[240:243], v[72:75], v[76:79]\n"
    "v_pk_mul_f16 v64, v44, v46\n"
    "v_pk_mul_f16 v65, v45, v47\n"
    "v_mfma_f32_16x16x32_f16 v[80:83], v[244:247], v[72:75], v[80:83]\n"
    "v_add_f32 v11, v88, v89\n"
    "v_add_f32 v13, v90, v91\n"
    "v_mfma_f32_16x16x32_f16 v[80:83], v[232:235], v[68:71], v[80:83]\n"
    "v_mov_b64 v[88:89], 0\n"
    "v_mov_b64 v[90:91], 0\n"
    "v_mfma_f32_16x16x32_f16 v[84:87], v[236:239], v[68:71], v[84:87]\n"
    "s_waitcnt lgkmcnt(0)\n"
    "v_pk_mul_f16 v66, v48, v48 op_sel_hi:[0,1]\n"
    "v_pk_mul_f16 v67, v48, v49 op_sel_hi:[0,1]\n"
    "v_mfma_f32_16x16x32_f16 v[84:87], v[248:251], v[72:75], v[84:87]\n"
    "v_mul_f32 v11, v11, v13\n"
    "v_add_u32 v25, 16, v25\n"
    "s_cmp_eq_u32 s45, -1\n"
    "s_cbranch_scc1 .Lxd_ex3\n"
    "s_cmp_eq_u32 s36, 0\n"
    "s_cbranch_scc1 .Lxd_ph0\n"
    "s_branch .Lxd_pl0\n"
    ".Lxd_ex3:\n"
    "v_fmac_f32 v11, v76, v80\n"
    "v_fmac_f32 v11, v77, v81\n"
    "v_fmac_f32 v11, v78, v82\n"
    "v_fmac_f32 v11, v79, v83\n"
    "s_nop 0\n"
    "v_fmac_f32 v11, v84, v85\n"
    "v_fma_f32 v10, v86, v87, v11\n"
    "s_branch .Lxd_fin\n"
    ".Lxd_ex2:\n"
    "v_fmac_f32 v11, v76, v80\n"
    "v_fmac_f32 v11, v77, v81\n"
    "v_fmac_f32 v11, v78, v82\n"
    "v_fmac_f32 v11, v79, v83\n"
    "s_nop 0\n"
    "v_fmac_f32 v11, v84, v85\n"
    "v_fma_f32 v9, v86, v87, v11\n"
    "s_branch .Lxd_fin\n"
    ".Lxd_ex1:\n"
    "v_fmac_f32 v11, v76, v80\n"
    "v_fmac_f32 v11, v77, v81\n"
    "v_fmac_f32 v11, v78, v82\n"
    "v_fmac_f32 v11, v79, v83\n"
    "s_nop 0\n"
    "v_fmac_f32 v11, v84, v85\n"
    "v_fma_f32 v8, v86, v87, v11\n"
    "s_branch .Lxd_fin\n"
    ".Lxd_ex0:\n"
    "v_fmac_f32 v11, v76, v80\n"
    "v_fmac_f32 v11, v77, v81\n"
    "v_fmac_f32 v11, v78, v82\n"
    "v_fmac_f32 v11, v79, v83\n"
    "s_nop 0\n"
    "v_fmac_f32 v11, v84, v85\n"
    "v_fma_f32 v7, v86, v87, v11\n"
    ".Lxd_fin:\n"
    "s_nop 1\n"
    "v_permlane32_swap_b32 v7, v8\n"
    "v_permlane32_swap_b32 v9, v10\n"
    "v_mul_f32 v7, v0, v7\n"
    "v_mul_f32 v9, v0, v9\n"
    "v_fmac_f32 v7, v23, v8\n"
    "v_fmac_f32 v9, v23, v10\n"
    "s_nop 1\n"
    "v_permlane16_swap_b32 v7, v9\n"
    "v_add_f32 v7, v7, v9\n"
    "s_nop 1\n"
    "v_add_f32_dpp v7, v7, v7 quad_perm:[1,0,3,2] row_mask:0xf bank_mask:0xf\n"
    "s_nop 1\n"
    "v_add_f32_dpp v7, v7, v7 quad_perm:[2,3,0,1] row_mask:0xf bank_mask:0xf\n"
    "s_nop 1\n"
    "v_add_f32_dpp v7, v7, v7 row_half_mirror row_mask:0xf bank_mask:0xf\n"
    "s_nop 1\n"
    "v_add_f32_dpp v7, v7, v7 row_mirror row_mask:0xf bank_mask:0xf\n"
    "ds_write_b32 v14, v7\n"
    "v_add_u32 v14, 64, v14\n"
    "s_cmp_lg_u32 s56, 2\n"
    "s_cbranch_scc1 .Lxd_f2\n"
    ".Lxd_f3:\n"
    "v_lshl_add_u32 v12, v18, 6, v20\n"
    "v_lshlrev_b32 v13, 2, v12\n"
    "v_add_u32 v12, 150528, v12\n"
    "v_add_u32 v13, 152128, v13\n"
    "v_mov_b32 v21, 154144\n"
    "s_lshl_b32 s26, s26, 16\n"
    "s_waitcnt vmcnt(0) lgkmcnt(0)\n"
    "s_barrier\n"
    "s_cmp_lg_u32 s20, 0\n"
    "s_cbranch_scc1 .Lxd_done\n"
    "ds_read_b128 v[28:31], v13\n"
    "ds_read_b32 v32, v12\n"
    "ds_read_u16 v33, v12 offset:512\n"
    "ds_read_b32 v34, v12 offset:1024\n"
    "ds_read_b32 v35, v21\n"
    "s_waitcnt lgkmcnt(0)\n"
    "v_add_f32 v28, v28, v29\n"
    "v_add_f32 v30, v30, v31\n"
    "v_cmp_lt_i32 vcc, 0, v32\n"
    "v_lshrrev_b32 v33, 4, v33\n"
    "v_add_f32 v28, v28, v30\n"
    "v_lshl_add_u32 v33, v33, 2, v35\n"
    "v_add_f32 v34, s26, v34\n"
    "v_ldexp_f32 v28, v28, v33\n"
    "v_add_f32 v28, v28, v34\n"
    "v_cvt_pk_bf16_f32 v28, v28, v28\n"
    "s_mov_b64 exec, vcc\n"
    "global_store_short v32, v28, s[70:71] offset:-2\n"
    "s_mov_b64 exec, -1\n"
    ".Lxd_done:\n"
    "s_endpgm\n"
    ".Lxd_cin:\n"
    "s_lshr_b32 s20, s43, 6\n"
    "v_and_b32 v1, 63, v0\n"
    "v_lshlrev_b32 v42, 4, v1\n"
    "s_cmp_ge_u32 s2, 8\n"
    "s_cbranch_scc1 .Lxd_help\n"
    "s_and_b32 s40, s2, 3\n"
    "s_lshl2_add_u32 s23, s40, s20\n"
    "s_lshl1_add_u32 s41, s40, s20\n"
    "s_add_u32 s41, s41, 12\n"
    "s_cmp_ge_u32 s20, 4\n"
    "s_cselect_b32 s23, s41, s23\n"
    "s_add_u32 s41, s40, 24\n"
    "s_cmp_ge_u32 s20, 6\n"
    "s_cselect_b32 s23, s41, s23\n"
    "s_cselect_b32 s24, 1, 0\n"
    "s_min_u32 s34, s23, 25\n"
    "s_lshr_b32 s35, s2, 2\n"
    "s_cmp_ge_u32 s2, 4\n"
    "s_cbranch_scc1 .Lxd_bR\n"
    "v_and_b32 v190, 31, v0\n"
    "v_min_u32 v190, 25, v190\n"
    "v_mul_u32_u24 v190, 52, v190\n"
    "v_bfe_u32 v191, v0, 5, 1\n"
    "v_lshl_add_u32 v190, v191, 4, v190\n"
    "s_mul_i32 s40, s34, 1352\n"
    "s_xor_b32 s66, s20, 7\n"
    "s_or_b32 s66, s66, s2\n"
    "s_waitcnt lgkmcnt(0)\n"
    "s_load_dwordx4 s[68:71], s[0:1], 64\n"
    "s_add_u32 s38, s12, s40\n"
    "s_addc_u32 s39, s13, 0\n"
    "s_cmp_lg_u32 s66, 0\n"
    "s_cbranch_scc1 .Lxd_on39\n"
    "v_and_b32 v188, 31, v0\n"
    "v_lshlrev_b32 v188, 7, v188\n"
    "v_add_u32 v188, 32768, v188\n"
    "v_mov_b32 v176, 140\n"
    "global_store_dword v188, v176, s[16:17] sc1\n"
    "global_load_dwordx4 v[180:183], v42, s[16:17] sc1\n"
    ".Lxd_on39:\n"
    "global_load_dwordx4 v[80:83], v190, s[38:39] offset:0\n"
    "global_load_dwordx4 v[84:87], v190, s[38:39] offset:32\n"
    "s_getpc_b64 s[60:61]\n"
    "s_add_u32 s60, s60, xd_tab@rel32@lo+4\n"
    "s_addc_u32 s61, s61, xd_tab@rel32@hi+12\n"
    "v_lshl_add_u32 v188, s24, 10, v42\n"
    "s_lshl1_add_u32 s40, s24, s35\n"
    "v_lshl_add_u32 v189, s40, 11, v42\n"
    "s_load_dword s25, s[16:17], 0\n"
    "global_load_dwordx4 v[44:47], v188, s[60:61]\n"
    "global_load_dwordx4 v[48:51], v189, s[60:61] offset:2048\n"
    "global_load_dwordx4 v[52:55], v189, s[60:61] offset:3072\n"
    "s_setprio 0\n"
    "v_mov_b64 v[188:189], 0\n"
    "v_lshlrev_b32 v190, 3, v0\n"
    "v_lshrrev_b32 v186, 5, v1\n"
    "ds_write_b64 v190, v[188:189] offset:36864\n"
    "v_bfe_u32 v190, v0, 1, 4\n"
    "v_lshlrev_b32 v72, 2, v186\n"
    "v_sub_u32 v190, v190, v72\n"
    "v_and_b32 v72, 16, v42\n"
    "s_movk_i32 s40, 0x3f80\n"
    "v_lshlrev_b32_e64 v191, v72, s40\n"
    "v_cmp_eq_u32 s[60:61], 0, v190\n"
    "v_cmp_eq_u32 s[62:63], 1, v190\n"
    "v_cmp_eq_u32 s[64:65], 2, v190\n"
    "v_cmp_eq_u32 s[50:51], 3, v190\n"
    "v_cndmask_b32 v72, 0, v191, s[60:61]\n"
    "v_cndmask_b32 v73, 0, v191, s[62:63]\n"
    "v_cndmask_b32 v74, 0, v191, s[64:65]\n"
    "v_cndmask_b32 v75, 0, v191, s[50:51]\n"
    "v_cmp_eq_u32 s[60:61], 8, v190\n"
    "v_cmp_eq_u32 s[62:63], 9, v190\n"
    "v_cmp_eq_u32 s[64:65], 10, v190\n"
    "v_cmp_eq_u32 s[50:51], 11, v190\n"
    "v_cndmask_b32 v76, 0, v191, s[60:61]\n"
    "v_cndmask_b32 v77, 0, v191, s[62:63]\n"
    "v_cndmask_b32 v78, 0, v191, s[64:65]\n"
    "v_cndmask_b32 v79, 0, v191, s[50:51]\n"
    "s_lshl_b32 s30, s20, 12\n"
    "s_add_u32 s30, s30, 65536\n"
    "v_and_b32 v188, 31, v0\n"
    "v_mul_u32_u24 v192, 528, v186\n"
    "v_lshl_add_u32 v192, v188, 2, v192\n"
    "v_add_u32 v192, s30, v192\n"
    "s_lshl_b32 s40, s35, 6\n"
    "s_add_u32 s40, s40, s20\n"
    "s_lshl1_add_u32 s41, s20, s35\n"
    "s_add_u32 s41, s41, 120\n"
    "s_cmp_ge_u32 s20, 4\n"
    "s_cselect_b32 s40, s41, s40\n"
    "s_lshl1_add_u32 s41, s35, 384\n"
    "s_cmp_ge_u32 s20, 6\n"
    "s_cselect_b32 s40, s41, s40\n"
    "s_cselect_b32 s42, 1, 192\n"
    "s_and_b32 s41, s2, 3\n"
    "s_lshl2_add_u32 s40, s41, s40\n"
    "v_lshrrev_b32 v188, 3, v1\n"
    "v_mul_u32_u24 v188, 448, v188\n"
    "v_bfe_u32 v189, v0, 2, 1\n"
    "v_mad_u32_u24 v188, v189, s42, v188\n"
    "v_and_b32 v189, 48, v42\n"
    "v_add3_u32 v188, v188, v189, s40\n"
    "v_lshlrev_b32 v193, 4, v188\n"
    "v_mov_b32 v177, 0\n"
    "s_waitcnt vmcnt(0) lgkmcnt(0)\n"
    "s_cmp_lg_u32 s66, 0\n"
    "s_cbranch_scc1 .Lxd_on40\n"
    "s_mov_b32 s40, 0x9e3779b1\n"
    "v_mul_lo_u32 v144, v180, s40\n"
    "v_xor_b32 v145, 0xa5c30f96, v180\n"
    "v_alignbit_b32 v144, s25, v144, 24\n"
    "v_mul_lo_u32 v146, v181, s40\n"
    "v_xor_b32 v147, 0xa5c30f96, v181\n"
    "v_alignbit_b32 v146, s25, v146, 24\n"
    "v_mul_lo_u32 v148, v182, s40\n"
    "v_xor_b32 v149, 0xa5c30f96, v182\n"
    "v_alignbit_b32 v148, s25, v148, 24\n"
    "v_mul_lo_u32 v150, v183, s40\n"
    "v_xor_b32 v151, 0xa5c30f96, v183\n"
    "v_alignbit_b32 v150, s25, v150, 24\n"
    "v_lshlrev_b32 v180, 1, v42\n"
    "s_add_u32 s40, s16, 4096\n"
    "s_addc_u32 s41, s17, 0\n"
    "global_store_dwordx4 v180, v[144:147], s[40:41] sc1\n"
    "global_store_dwordx4 v180, v[148:151], s[40:41] offset:16 sc1\n"
    ".Lxd_on40:\n"
    "s_barrier\n"
    "s_branch .Lxd_bD\n"
    ".Lxd_bR:\n"
    "s_mul_i32 s40, s20, 4480\n"
    "s_min_u32 s40, s40, 30672\n"
    "s_mov_b32 m0, s40\n"
    "s_add_u32 s40, s40, 70304\n"
    "v_lshrrev_b32 v190, 2, v1\n"
    "v_and_b32 v188, 48, v42\n"
    "s_movk_i32 s42, 1352\n"
    "v_min_u32 v191, 9, v190\n"
    "v_mad_u32_u24 v190, v190, s42, v188\n"
    "v_mad_u32_u24 v191, v191, s42, v188\n"
    "v_add_u32 v191, 20608, v191\n"
    "s_mul_i32 s41, s34, 52\n"
    "s_add_u32 s41, s41, 35152\n"
    "s_waitcnt lgkmcnt(0)\n"
    "s_load_dwordx4 s[68:71], s[0:1], 64\n"
    "s_add_u32 s38, s12, s40\n"
    "s_addc_u32 s39, s13, 0\n"
    "global_load_lds_dwordx4 v42, s[38:39] offset:0\n"
    "global_load_lds_dwordx4 v42, s[38:39] offset:1024\n"
    "global_load_lds_dwordx4 v42, s[38:39] offset:2048\n"
    "global_load_lds_dwordx4 v42, s[38:39] offset:3072\n"
    "global_load_lds_dwordx4 v42, s[38:39] offset:3456\n"
    "s_cmp_lg_u32 s20, 7\n"
    "s_cbranch_scc1 .Lxd_on41\n"
    "v_min_u32 v188, 38, v1\n"
    "s_mov_b32 m0, 40960\n"
    "v_lshlrev_b32 v188, 2, v188\n"
    "global_load_lds_dword v188, s[14:15]\n"
    ".Lxd_on41:\n"
    "s_lshl_b32 s28, s20, 11\n"
    "s_add_u32 s28, s28, 49152\n"
    "s_mov_b32 m0, s28\n"
    "s_add_u32 s38, s12, s41\n"
    "s_addc_u32 s39, s13, 0\n"
    "global_load_lds_dwordx4 v190, s[38:39] offset:0\n"
    "global_load_lds_dwordx4 v191, s[38:39] offset:1024\n"
    "s_getpc_b64 s[60:61]\n"
    "s_add_u32 s60, s60, xd_tab@rel32@lo+4\n"
    "s_addc_u32 s61, s61, xd_tab@rel32@hi+12\n"
    "v_lshl_add_u32 v188, s24, 10, v42\n"
    "s_lshl1_add_u32 s40, s24, s35\n"
    "v_lshl_add_u32 v189, s40, 11, v42\n"
    "s_load_dword s25, s[16:17], 0\n"
    "global_load_dwordx4 v[44:47], v188, s[60:61]\n"
    "global_load_dwordx4 v[48:51], v189, s[60:61] offset:2048\n"
    "global_load_dwordx4 v[52:55], v189, s[60:61] offset:3072\n"
    "s_setprio 0\n"
    "v_mov_b64 v[188:189], 0\n"
    "v_lshlrev_b32 v190, 3, v0\n"
    "v_lshrrev_b32 v186, 5, v1\n"
    "ds_write_b64 v190, v[188:189] offset:36864\n"
    "v_lshrrev_b32 v184, 4, v1\n"
    "v_and_b32 v185, 15, v0\n"
    "v_lshrrev_b32 v190, 2, v185\n"
    "v_lshl_add_u32 v64, v184, 3, v190\n"
    "v_add_u32 v65, 4, v64\n"
    "s_lshr_b32 s21, s20, 2\n"
    "s_and_b32 s43, s20, 3\n"
    "s_mul_i32 s40, s21, 13\n"
    "s_lshl2_add_u32 s40, s43, s40\n"
    "v_and_b32 v191, 3, v0\n"
    "v_add_lshl_u32 v188, v191, s40, 3\n"
    "v_min_u32 v64, 25, v64\n"
    "v_mad_u32_u24 v64, v64, s42, v188\n"
    "v_min_u32 v65, 25, v65\n"
    "v_mad_u32_u24 v65, v65, s42, v188\n"
    "v_lshl_add_u32 v188, s43, 4, v185\n"
    "v_cmp_lt_u32 vcc, 25, v188\n"
    "v_cmp_lt_u32 s[60:61], 51, v188\n"
    "v_lshl_add_u32 v66, s21, 6, v188\n"
    "v_cndmask_b32_e64 v189, 0, 6, vcc\n"
    "v_add_lshl_u32 v66, v66, v189, 1\n"
    "v_mov_b32 v189, 0x40000\n"
    "v_cmp_gt_u32 vcc, 26, v1\n"
    "v_cndmask_b32 v66, v66, v189, s[60:61]\n"
    "v_lshlrev_b32 v67, 6, v1\n"
    "v_cndmask_b32 v67, v189, v67, vcc\n"
    "v_lshlrev_b32 v68, 4, v184\n"
    "v_add_u32 v68, 40960, v68\n"
    "v_lshlrev_b32 v69, 1, v1\n"
    "v_lshl_add_u32 v190, v186, 3, v190\n"
    "v_and_b32 v188, 1, v184\n"
    "v_lshl_add_u32 v188, v188, 2, v191\n"
    "v_cmp_eq_u32 vcc, 7, v188\n"
    "v_lshlrev_b32 v188, 3, v188\n"
    "v_lshl_add_u32 v70, v190, 6, v188\n"
    "v_add_u32 v71, s28, v70\n"
    "v_mov_b32 v188, 38912\n"
    "v_cndmask_b32 v71, v71, v188, vcc\n"
    "v_cmp_gt_u32 vcc, 3, v184\n"
    "s_waitcnt vmcnt(5) lgkmcnt(0)\n"
    "s_barrier\n"
    "ds_read2_b64 v[176:179], v68 offset0:13 offset1:14\n"
    "ds_read_u16 v68, v69 offset:41012\n"
    "ds_read_u16 v69, v69 offset:40960\n"
    "ds_read_b64_tr_b16 v[112:113], v64 offset:0\n"
    "ds_read_b64_tr_b16 v[114:115], v65 offset:0\n"
    "ds_read_b64_tr_b16 v[116:117], v64 offset:208\n"
    "ds_read_b64_tr_b16 v[118:119], v65 offset:208\n"
    "ds_read_b64_tr_b16 v[120:121], v64 offset:416\n"
    "ds_read_b64_tr_b16 v[122:123], v65 offset:416\n"
    "ds_read_b64_tr_b16 v[124:125], v64 offset:624\n"
    "ds_read_b64_tr_b16 v[126:127], v65 offset:624\n"
    "ds_read_b64_tr_b16 v[128:129], v64 offset:832\n"
    "ds_read_b64_tr_b16 v[130:131], v65 offset:832\n"
    "ds_read_b64_tr_b16 v[132:133], v64 offset:1040\n"
    "ds_read_b64_tr_b16 v[134:135], v65 offset:1040\n"
    "ds_read_b64_tr_b16 v[136:137], v64 offset:1248\n"
    "ds_read_b64_tr_b16 v[138:139], v65 offset:1248\n"
    "s_waitcnt lgkmcnt(14)\n"
    "v_cndmask_b32 v177, 0, v177, vcc\n"
    "v_cndmask_b32 v178, 0, v178, vcc\n"
    "v_cndmask_b32 v179, 0, v179, vcc\n"
    "ds_write_b16 v67, v68 offset:36920\n"
    "s_waitcnt lgkmcnt(13)\n"
    "v_mfma_f32_16x16x32_bf16 v[144:147], v[176:179], v[112:115], 0\n"
    "s_waitcnt lgkmcnt(11)\n"
    "v_mfma_f32_16x16x32_bf16 v[148:151], v[176:179], v[116:119], 0\n"
    "s_waitcnt lgkmcnt(9)\n"
    "v_mfma_f32_16x16x32_bf16 v[152:155], v[176:179], v[120:123], 0\n"
    "s_waitcnt lgkmcnt(7)\n"
    "v_mfma_f32_16x16x32_bf16 v[156:159], v[176:179], v[124:127], 0\n"
    "s_waitcnt lgkmcnt(5)\n"
    "v_mfma_f32_16x16x32_bf16 v[160:163], v[176:179], v[128:131], 0\n"
    "s_waitcnt lgkmcnt(3)\n"
    "v_mfma_f32_16x16x32_bf16 v[164:167], v[176:179], v[132:135], 0\n"
    "s_waitcnt lgkmcnt(1)\n"
    "v_mfma_f32_16x16x32_bf16 v[168:171], v[176:179], v[136:139], 0\n"
    "s_waitcnt vmcnt(3)\n"
    "ds_read_b64_tr_b16 v[80:81], v71 offset:0\n"
    "ds_read_b64_tr_b16 v[82:83], v71 offset:256\n"
    "ds_read_b64_tr_b16 v[84:85], v71 offset:1024\n"
    "ds_read_b64_tr_b16 v[86:87], v71 offset:1280\n"
    "v_cvt_pk_bf16_f32 v144, v144, v144\n"
    "ds_write_b16 v66, v144 offset:36864\n"
    "v_cvt_pk_bf16_f32 v148, v148, v148\n"
    "ds_write_b16 v66, v148 offset:37120\n"
    "v_cvt_pk_bf16_f32 v152, v152, v152\n"
    "ds_write_b16 v66, v152 offset:37376\n"
    "v_cvt_pk_bf16_f32 v156, v156, v156\n"
    "ds_write_b16 v66, v156 offset:37632\n"
    "v_cvt_pk_bf16_f32 v160, v160, v160\n"
    "ds_write_b16 v66, v160 offset:37888\n"
    "v_cvt_pk_bf16_f32 v164, v164, v164\n"
    "ds_write_b16 v66, v164 offset:38144\n"
    "s_cmp_lg_u32 s21, 0\n"
    "s_cbranch_scc1 .Lxd_on42\n"
    "v_cvt_pk_bf16_f32 v168, v168, v168\n"
    "ds_write_b16 v66, v168 offset:38400\n"
    ".Lxd_on42:\n"
    "v_lshlrev_b32 v177, 16, v69\n"
    "s_waitcnt lgkmcnt(0)\n"
    "s_barrier\n"
    "ds_read_b64_tr_b16 v[72:73], v70 offset:36864\n"
    "ds_read_b64_tr_b16 v[74:75], v70 offset:37120\n"
    "ds_read_b64_tr_b16 v[76:77], v70 offset:37888\n"
    "ds_read_b64_tr_b16 v[78:79], v70 offset:38144\n"
    "s_lshl_b32 s30, s20, 12\n"
    "s_add_u32 s30, s30, 65536\n"
    "v_and_b32 v188, 31, v0\n"
    "v_mul_u32_u24 v192, 528, v186\n"
    "v_lshl_add_u32 v192, v188, 2, v192\n"
    "v_add_u32 v192, s30, v192\n"
    "s_lshl_b32 s40, s35, 6\n"
    "s_add_u32 s40, s40, s20\n"
    "s_lshl1_add_u32 s41, s20, s35\n"
    "s_add_u32 s41, s41, 120\n"
    "s_cmp_ge_u32 s20, 4\n"
    "s_cselect_b32 s40, s41, s40\n"
    "s_lshl1_add_u32 s41, s35, 384\n"
    "s_cmp_ge_u32 s20, 6\n"
    "s_cselect_b32 s40, s41, s40\n"
    "s_cselect_b32 s42, 1, 192\n"
    "s_and_b32 s41, s2, 3\n"
    "s_lshl2_add_u32 s40, s41, s40\n"
    "v_lshrrev_b32 v188, 3, v1\n"
    "v_mul_u32_u24 v188, 448, v188\n"
    "v_bfe_u32 v189, v0, 2, 1\n"
    "v_mad_u32_u24 v188, v189, s42, v188\n"
    "v_and_b32 v189, 48, v42\n"
    "v_add3_u32 v188, v188, v189, s40\n"
    "v_lshlrev_b32 v193, 4, v188\n"
    "s_waitcnt vmcnt(0) lgkmcnt(0)\n"
    ".Lxd_bD:\n"
    "v_mfma_f32_32x32x16_bf16 v[96:111], v[72:75], v[80:83], 0\n"
    "v_mfma_f32_32x32x16_bf16 v[96:111], v[80:83], v[72:75], v[96:111]\n"
    "v_mfma_f32_32x32x16_bf16 v[96:111], v[76:79], v[84:87], v[96:111]\n"
    "v_mfma_f32_32x32x16_bf16 v[96:111], v[84:87], v[76:79], v[96:111]\n"
    "v_readlane_b32 s43, v177, s34\n"
    "v_mad_u32_u16 v64, v44, 4, s30 op_sel:[0,0,0,0]\n"
    "v_mad_u32_u16 v65, v44, 4, s30 op_sel:[1,0,0,0]\n"
    "v_mad_u32_u16 v66, v45, 4, s30 op_sel:[0,0,0,0]\n"
    "v_mad_u32_u16 v67, v45, 4, s30 op_sel:[1,0,0,0]\n"
    "v_mad_u32_u16 v68, v46, 4, s30 op_sel:[0,0,0,0]\n"
    "v_mad_u32_u16 v69, v46, 4, s30 op_sel:[1,0,0,0]\n"
    "v_mad_u32_u16 v70, v47, 4, s30 op_sel:[0,0,0,0]\n"
    "v_mad_u32_u16 v71, v47, 4, s30 op_sel:[1,0,0,0]\n"
    "v_mov_b32 v184, 40944\n"
    "s_and_b32 s25, s25, 0xffffff\n"
    "s_mul_i32 s40, s25, 0x9e3779b1\n"
    "s_xor_b32 s41, s25, 0xa5c30f96\n"
    "s_lshr_b32 s40, s40, 24\n"
    "v_writelane_b32 v108, s43, 60\n"
    "ds_write2_b32 v192, v96, v97 offset0:0 offset1:33\n"
    "ds_write2_b32 v192, v98, v99 offset0:66 offset1:99\n"
    "v_add_u32 v192, 1056, v192\n"
    "ds_write2_b32 v192, v100, v101 offset0:0 offset1:33\n"
    "ds_write2_b32 v192, v102, v103 offset0:66 offset1:99\n"
    "v_add_u32 v192, 1056, v192\n"
    "ds_write2_b32 v192, v104, v105 offset0:0 offset1:33\n"
    "ds_write2_b32 v192, v106, v107 offset0:66 offset1:99\n"
    "v_add_u32 v192, 1056, v192\n"
    "ds_write2_b32 v192, v108, v109 offset0:0 offset1:33\n"
    "ds_read_b32 v56, v64\n"
    "ds_read_b32 v57, v65\n"
    "ds_read_b32 v58, v66\n"
    "ds_read_b32 v59, v67\n"
    "ds_read_b32 v60, v68\n"
    "ds_read_b32 v61, v69\n"
    "ds_read_b32 v62, v70\n"
    "ds_read_b32 v63, v71\n"
    "s_waitcnt lgkmcnt(6)\n"
    "v_pk_mul_f32 v[56:57], v[56:57], v[48:49]\n"
    "v_max3_f32 v188, |v56|, |v57|, 0\n"
    "s_waitcnt lgkmcnt(4)\n"
    "v_pk_mul_f32 v[58:59], v[58:59], v[50:51]\n"
    "v_max3_f32 v188, |v58|, |v59|, v188\n"
    "s_waitcnt lgkmcnt(2)\n"
    "v_pk_mul_f32 v[60:61], v[60:61], v[52:53]\n"
    "v_max3_f32 v188, |v60|, |v61|, v188\n"
    "s_waitcnt lgkmcnt(0)\n"
    "v_pk_mul_f32 v[62:63], v[62:63], v[54:55]\n"
    "v_max3_f32 v188, |v62|, |v63|, v188\n"
    "s_nop 1\n"
    "v_max_u32_dpp v188, v188, v188 quad_perm:[1,0,3,2] row_mask:0xf bank_mask:0xf\n"
    "s_nop 1\n"
    "v_max_u32_dpp v188, v188, v188 quad_perm:[2,3,0,1] row_mask:0xf bank_mask:0xf\n"
    "s_nop 1\n"
    "v_max_u32_dpp v188, v188, v188 row_half_mirror row_mask:0xf bank_mask:0xf\n"
    "s_nop 1\n"
    "v_max_u32_dpp v188, v188, v188 row_mirror row_mask:0xf bank_mask:0xf\n"
    "s_mov_b32 exec_lo, 0x10001\n"
    "s_mov_b32 exec_hi, 1\n"
    "ds_max_u32 v184, v188\n"
    "s_mov_b64 exec, -1\n"
    "s_add_u32 s38, s16, 1047552\n"
    "s_addc_u32 s39, s17, 0\n"
    "s_cmp_lt_u32 s23, 26\n"
    "s_cselect_b32 s43, 0xfff8fff8, 0\n"
    "s_waitcnt lgkmcnt(0)\n"
    "s_barrier\n"
    "ds_read_b32 v188, v184\n"
    "v_cmp_le_u32 vcc, 0x400, v193\n"
    "s_cmp_eq_u32 s20, 7\n"
    "s_cselect_b32 s42, 0, 48\n"
    "s_bfm_b64 s[60:61], s42, 0\n"
    "s_and_b64 s[60:61], s[60:61], vcc\n"
    "s_waitcnt lgkmcnt(0)\n"
    "v_lshrrev_b32 v188, 23, v188\n"
    "v_max_u32 v188, 40, v188\n"
    "v_min_u32 v188, 250, v188\n"
    "v_sub_u32 v190, 268, v188\n"
    "v_lshlrev_b32 v190, 23, v190\n"
    "v_pk_mul_f32 v[56:57], v[56:57], v[190:191] op_sel_hi:[1,0]\n"
    "v_cvt_pkrtz_f16_f32 v56, v56, v57\n"
    "v_add_u32 v56, 0x40004, v56\n"
    "v_and_b32 v56, s43, v56\n"
    "v_pk_mul_f32 v[58:59], v[58:59], v[190:191] op_sel_hi:[1,0]\n"
    "v_cvt_pkrtz_f16_f32 v57, v58, v59\n"
    "v_add_u32 v57, 0x40004, v57\n"
    "v_and_b32 v57, s43, v57\n"
    "v_pk_mul_f32 v[60:61], v[60:61], v[190:191] op_sel_hi:[1,0]\n"
    "v_cvt_pkrtz_f16_f32 v58, v60, v61\n"
    "v_add_u32 v58, 0x40004, v58\n"
    "v_and_b32 v58, s43, v58\n"
    "v_pk_mul_f32 v[62:63], v[62:63], v[190:191] op_sel_hi:[1,0]\n"
    "v_cvt_pkrtz_f16_f32 v59, v62, v63\n"
    "v_add_u32 v59, 0x40004, v59\n"
    "v_and_b32 v59, s43, v59\n"
    "v_lshl_or_b32 v188, v188, 8, s40\n"
    "v_mov_b32 v189, s41\n"
    "s_lshl3_add_u32 s40, s2, 40960\n"
    "v_mov_b32 v190, s40\n"
    "s_cmp_eq_u32 s20, 7\n"
    "s_cbranch_scc1 .Lxd_b3\n"
    "s_mov_b64 exec, s[60:61]\n"
    "global_store_dwordx4 v193, v[56:59], s[38:39] sc1\n"
    "s_mov_b64 exec, -1\n"
    ".Lxd_b3:\n"
    "s_waitcnt vmcnt(0)\n"
    "s_barrier\n"
    "s_cmp_lg_u32 s20, 0\n"
    "s_cbranch_scc1 .Lxd_on43\n"
    "s_mov_b64 exec, 1\n"
    "global_store_dwordx2 v190, v[188:189], s[16:17] sc1\n"
    "s_mov_b64 exec, -1\n"
    ".Lxd_on43:\n"
    "s_branch .Lxd_h1\n"
    ".Lxd_help:\n"
    "s_lshr_b32 s41, s2, 3\n"
    "s_sub_u32 s41, s41, 1\n"
    "s_cmp_lg_u32 s20, 7\n"
    "s_cbranch_scc1 .Lxd_on45\n"
    "s_mul_i32 s40, s41, 60\n"
    "v_min_u32 v7, 59, v1\n"
    "v_add_lshl_u32 v7, v7, s40, 7\n"
    "s_getpc_b64 s[60:61]\n"
    ".Lxd_pc44:\n"
    "s_sub_u32 s60, s60, .Lxd_pc44-.Lxd_top\n"
    "s_subb_u32 s61, s61, 0\n"
    "v_min_u32 v7, .Lxd_end-.Lxd_top-4, v7\n"
    "global_load_dword v7, v7, s[60:61]\n"
    ".Lxd_on45:\n"
    "v_mul_u32_u24 v68, 52, v1\n"
    "v_lshrrev_b32 v68, 8, v68\n"
    "v_mad_i32_i24 v69, v68, -5, v1\n"
    "s_and_b32 s40, s2, 0xf8\n"
    "s_add_u32 s40, s40, s20\n"
    "s_mul_i32 s40, s40, 13\n"
    "s_sub_u32 s40, s40, 104\n"
    "v_add_u32 v68, s40, v68\n"
    "v_min_u32 v68, 0x1ff, v68\n"
    "v_lshrrev_b32 v70, 8, v68\n"
    "s_movk_i32 s40, 1792\n"
    "v_mad_u32_u24 v68, v70, s40, v68\n"
    "s_and_b32 s40, s2, 7\n"
    "v_lshl_add_u32 v68, s40, 8, v68\n"
    "v_mul_u32_u24 v68, 104, v68\n"
    "s_lshl_b32 s40, s2, 2\n"
    "s_lshr_b32 s40, 0xa8864220, s40\n"
    "s_and_b32 s40, s40, 14\n"
    "s_lshl_b32 s40, s40, 1\n"
    "v_add3_u32 v70, v69, s40, 2\n"
    "v_subrev_u32 v71, 24, v70\n"
    "v_min_u32 v70, v70, v71\n"
    "v_lshl_add_u32 v70, v70, 2, v68\n"
    "s_waitcnt lgkmcnt(0)\n"
    "s_load_dwordx4 s[68:71], s[0:1], 64\n"
    "global_load_dword v60, v70, s[4:5]\n"
    "v_add3_u32 v70, v69, s40, 7\n"
    "v_subrev_u32 v71, 24, v70\n"
    "v_min_u32 v70, v70, v71\n"
    "v_lshl_add_u32 v70, v70, 2, v68\n"
    "global_load_dword v61, v70, s[4:5]\n"
    "v_add3_u32 v70, v69, s40, 12\n"
    "v_subrev_u32 v71, 24, v70\n"
    "v_min_u32 v70, v70, v71\n"
    "v_lshl_add_u32 v70, v70, 2, v68\n"
    "global_load_dword v62, v70, s[4:5]\n"
    "v_add3_u32 v70, v69, s40, 17\n"
    "v_subrev_u32 v71, 24, v70\n"
    "v_min_u32 v70, v70, v71\n"
    "v_lshl_add_u32 v70, v70, 2, v68\n"
    "global_load_dword v63, v70, s[4:5]\n"
    "v_add3_u32 v70, v69, s40, 22\n"
    "v_subrev_u32 v71, 24, v70\n"
    "v_min_u32 v70, v70, v71\n"
    "v_add_u32 v71, 22, v69\n"
    "v_cmp_lt_u32 vcc, 23, v71\n"
    "v_min_u32 v71, 25, v71\n"
    "s_nop 0\n"
    "v_cndmask_b32 v70, v70, v71, vcc\n"
    "v_lshl_add_u32 v70, v70, 2, v68\n"
    "global_load_dword v64, v70, s[4:5]\n"
    ".Lxd_h1:\n"
    "s_lshr_b32 s3, s2, 4\n"
    "s_mul_i32 s40, s3, 3\n"
    "s_add_u32 s47, s2, s40\n"
    "s_and_b32 s47, s47, 15\n"
    "s_lshr_b32 s21, s20, 2\n"
    "s_lshl1_add_u32 s22, s21, s20\n"
    "s_and_b32 s22, s22, 3\n"
    "s_lshl_b32 s58, s21, 8\n"
    "s_add_u32 s58, s58, 150528\n"
    "s_mov_b32 s40, s47\n"
    "s_mul_i32 s54, s40, 512\n"
    "s_add_u32 s54, s54, 2\n"
    "s_lshl_b32 s40, s40, 7\n"
    "s_add_u32 s40, s40, 32768\n"
    "s_add_u32 s52, s16, s40\n"
    "s_addc_u32 s53, s17, 0\n"
    "s_mov_b32 s57, 0x7fff7fff\n"
    "s_mov_b32 s56, 0\n"
    "s_mov_b32 s27, 0\n"
    "s_mov_b32 s55, 9\n"
    "s_mov_b32 s44, 0\n"
    "v_lshrrev_b32 v17, 3, v1\n"
    "v_mul_u32_u24 v17, 12, v17\n"
    "v_and_b32 v19, 7, v1\n"
    "v_lshlrev_b32 v19, 4, v19\n"
    "v_lshrrev_b32 v18, 5, v1\n"
    "v_lshlrev_b32 v18, 2, v18\n"
    "v_and_b32 v20, 31, v1\n"
    "v_lshlrev_b32 v20, 2, v20\n"
    "s_lshl2_add_u32 s40, s22, s58\n"
    "s_sub_u32 s40, s40, 20\n"
    "v_mov_b32 v25, s40\n"
    "s_lshl1_add_u32 s23, s3, s21\n"
    "s_mul_i32 s23, s23, 6\n"
    "s_add_u32 s23, s23, s22\n"
    "s_add_u32 s23, s23, 104\n"
    "v_mov_b32 v24, s23\n"
    "v_lshlrev_b32 v13, 2, v0\n"
    "v_readfirstlane_b32 s40, v24\n"
    "s_or_b32 s40, s40, s44\n"
    "s_lshl1_add_u32 s41, s40, s54\n"
    "s_cmpk_lt_u32 s40, 256\n"
    "s_cselect_b32 s41, s41, 2\n"
    "v_mov_b32 v12, s41\n"
    "s_mul_i32 s41, s41, 52\n"
    "s_add_u32 s38, s4, s41\n"
    "s_addc_u32 s39, s5, 0\n"
    "global_load_dwordx3 v[36:38], v17, s[38:39] offset:-104\n"
    "global_load_dword v41, v18, s[38:39] offset:-8\n"
    "s_setprio 0\n"
    "v_mov_b32 v21, 0\n"
    "v_add_u32 v13, 149504, v13\n"
    "ds_write_b32 v13, v21\n"
    "s_cmp_lt_u32 s2, 8\n"
    "s_cbranch_scc1 .Lxd_hp46\n"
    "s_lshl_b32 s40, s2, 2\n"
    "s_lshr_b32 s40, 0xa8864220, s40\n"
    "s_and_b32 s40, s40, 14\n"
    "s_mul_i32 s40, s40, 3328\n"
    "s_and_b32 s41, s2, 0xf8\n"
    "s_add_u32 s41, s41, s20\n"
    "s_mul_i32 s41, s41, 42\n"
    "s_add_u32 s40, s40, s41\n"
    "s_add_u32 s40, s40, 4656\n"
    "v_min_u32 v68, 41, v1\n"
    "v_add_lshl_u32 v68, v68, s40, 7\n"
    "s_mov_b32 m0, 159744\n"
    "s_nop 0\n"
    "global_load_lds_dword v68, s[6:7]\n"
    "s_waitcnt vmcnt(3)\n"
    "s_mov_b32 m0, 159744\n"
    "v_lshlrev_b32 v60, 7, v60\n"
    "global_load_lds_dword v60, s[8:9]\n"
    "s_waitcnt vmcnt(2)\n"
    "s_branch .Lxd_h3\n"
    ".Lxd_hp46:\n"
    "s_waitcnt vmcnt(0)\n"
    ".Lxd_h3:\n"
    "v_lshl_or_b32 v36, v36, 7, v19\n"
    "v_lshl_or_b32 v37, v37, 7, v19\n"
    "v_lshl_or_b32 v38, v38, 7, v19\n"
    "v_lshl_or_b32 v41, v41, 7, v20\n"
    "global_load_dwordx4 v[28:31], v36, s[8:9]\n"
    "global_load_dwordx4 v[32:35], v37, s[8:9]\n"
    "global_load_dword v40, v41, s[8:9]\n"
    "global_load_dwordx4 v[36:39], v38, s[8:9]\n"
    "s_waitcnt lgkmcnt(0)\n"
    "s_barrier\n"
    "ds_write_b32 v25, v12 offset:20\n"
    "s_load_dword s26, s[68:69], 0\n"
    "s_lshl_b32 s43, s21, 16\n"
    "v_lshrrev_b32 v12, 4, v1\n"
    "v_bfe_u32 v13, v1, 2, 2\n"
    "v_and_b32 v21, 3, v1\n"
    "s_lshl_b32 s40, s22, 5\n"
    "s_add_u32 s40, s40, s43\n"
    "v_lshl_add_u32 v21, v21, 3, s40\n"
    "s_movk_i32 s41, 144\n"
    "v_add_u32 v22, v12, v13\n"
    "v_and_b32 v22, 3, v22\n"
    "v_mad_u32_u24 v4, v22, s41, v21\n"
    "v_cmp_gt_u32 vcc, 2, v13\n"
    "v_add_u32 v24, 22, v13\n"
    "v_lshlrev_b32 v13, 3, v13\n"
    "v_cndmask_b32 v22, v24, v22, vcc\n"
    "v_mad_u32_u24 v3, v22, s41, v21\n"
    "v_cmp_eq_u32 vcc, 3, v12\n"
    "v_xor_b32 v22, 0, v12\n"
    "v_and_b32 v22, 1, v22\n"
    "v_lshlrev_b32 v22, 1, v22\n"
    "v_lshl_add_u32 v22, v12, 3, v22\n"
    "s_mov_b32 s40, 0x05040100\n"
    "v_bfe_u32 v24, s40, v13, 8\n"
    "v_add_u32 v22, v22, v24\n"
    "s_mov_b32 s40, 0x1a191818\n"
    "v_bfe_u32 v24, s40, v13, 8\n"
    "v_cndmask_b32 v22, v22, v24, vcc\n"
    "v_mad_u32_u24 v5, v22, s41, v21\n"
    "v_xor_b32 v22, 1, v12\n"
    "v_and_b32 v22, 1, v22\n"
    "s_cmp_lt_u32 s2, 8\n"
    "s_cbranch_scc1 .Lxd_hp47\n"
    "s_mov_b32 m0, 159744\n"
    "v_lshlrev_b32 v61, 7, v61\n"
    "global_load_lds_dword v61, s[8:9]\n"
    "s_mov_b32 m0, 159744\n"
    "v_lshlrev_b32 v62, 7, v62\n"
    "global_load_lds_dword v62, s[8:9]\n"
    ".Lxd_hp47:\n"
    "v_lshlrev_b32 v22, 1, v22\n"
    "v_lshl_add_u32 v22, v12, 3, v22\n"
    "s_mov_b32 s40, 0x05040100\n"
    "v_bfe_u32 v24, s40, v13, 8\n"
    "v_add_u32 v22, v22, v24\n"
    "s_mov_b32 s40, 0x1a191918\n"
    "v_bfe_u32 v24, s40, v13, 8\n"
    "v_cndmask_b32 v22, v22, v24, vcc\n"
    "v_mad_u32_u24 v6, v22, s41, v21\n"
    "v_bfrev_b32 v14, v12\n"
    "v_lshrrev_b32 v14, 26, v14\n"
    "s_add_u32 s40, s22, 3\n"
    "s_and_b32 s40, s40, 3\n"
    "s_lshl2_add_u32 s40, s40, s22\n"
    "s_add_u32 s40, s40, s58\n"
    "s_lshl_b32 s40, s40, 2\n"
    "s_add_u32 s40, s40, -450048\n"
    "v_add_u32 v14, s40, v14\n"
    "v_and_b32 v2, 1, v1\n"
    "v_mul_u32_u24 v2, 0xaaaa, v2\n"
    "v_add_u32 v2, 0x4444, v2\n"
    "v_mad_u32_u24 v15, v17, 36, v19\n"
    "v_mad_u32_u24 v16, v18, 36, v20\n"
    "s_lshl4_add_u32 s41, s21, s22\n"
    "v_lshrrev_b32 v12, 5, v19\n"
    "v_sub_u32 v12, 1, v12\n"
    "v_and_b32 v12, 3, v12\n"
    "v_add_lshl_u32 v12, v12, s41, 12\n"
    "v_add_u32 v15, v12, v15\n"
    "v_lshrrev_b32 v12, 5, v20\n"
    "v_sub_u32 v12, 1, v12\n"
    "v_and_b32 v12, 3, v12\n"
    "v_add_lshl_u32 v12, v12, s41, 12\n"
    "v_add_u32 v16, v12, v16\n"
    "s_lshl3_add_u32 s40, s2, 4096\n"
    "s_lshl_b32 s41, s2, 2\n"
    "v_lshlrev_b32 v11, 3, v1\n"
    "v_add_u32 v11, 40960, v11\n"
    "v_writelane_b32 v11, s40, 8\n"
    "v_writelane_b32 v11, s41, 9\n"
    "v_add_u32 v43, 4096, v42\n"
    "s_cmp_lt_u32 s2, 8\n"
    "s_cbranch_scc1 .Lxd_hp48\n"
    "s_waitcnt vmcnt(2)\n"
    "s_branch .Lxd_h2\n"
    ".Lxd_hp48:\n"
    "s_waitcnt vmcnt(0)\n"
    ".Lxd_h2:\n"
    "s_cmp_lg_u32 s20, 7\n"
    "s_cbranch_scc1 .Lxd_on49\n"
    "s_mov_b64 exec, 0x3ff\n"
    "global_load_dwordx2 v[26:27], v11, s[16:17] sc1\n"
    "s_mov_b64 exec, -1\n"
    ".Lxd_on49:\n"
    "s_movk_i32 s43, 15744\n"
    "v_pk_maximum3_f16 v21, v28, v28, s43 op_sel_hi:[1,1,0] neg_lo:[0,1,0] neg_hi:[0,1,0]\n"
    "v_pk_maximum3_f16 v21, v29, v29, v21 neg_lo:[0,1,0] neg_hi:[0,1,0]\n"
    "v_pk_maximum3_f16 v21, v30, v30, v21 neg_lo:[0,1,0] neg_hi:[0,1,0]\n"
    "v_pk_maximum3_f16 v21, v31, v31, v21 neg_lo:[0,1,0] neg_hi:[0,1,0]\n"
    "v_pk_maximum3_f16 v21, v32, v32, v21 neg_lo:[0,1,0] neg_hi:[0,1,0]\n"
    "v_pk_maximum3_f16 v21, v33, v33, v21 neg_lo:[0,1,0] neg_hi:[0,1,0]\n"
    "v_pk_maximum3_f16 v21, v34, v34, v21 neg_lo:[0,1,0] neg_hi:[0,1,0]\n"
    "v_pk_maximum3_f16 v21, v35, v35, v21 neg_lo:[0,1,0] neg_hi:[0,1,0]\n"
    "v_pk_maximum3_f16 v21, v36, v36, v21 neg_lo:[0,1,0] neg_hi:[0,1,0]\n"
    "v_pk_maximum3_f16 v21, v37, v37, v21 neg_lo:[0,1,0] neg_hi:[0,1,0]\n"
    "v_pk_maximum3_f16 v21, v38, v38, v21 neg_lo:[0,1,0] neg_hi:[0,1,0]\n"
    "v_pk_maximum3_f16 v21, v39, v39, v21 neg_lo:[0,1,0] neg_hi:[0,1,0]\n"
    "v_pk_maximum3_f16 v21, v40, v40, v21 neg_lo:[0,1,0] neg_hi:[0,1,0]\n"
    "v_pk_max_u16 v21, v21, v21 op_sel:[0,1] op_sel_hi:[1,0]\n"
    "s_nop 1\n"
    "v_max_u32_dpp v21, v21, v21 quad_perm:[1,0,3,2] row_mask:0xf bank_mask:0xf\n"
    "s_nop 1\n"
    "v_max_u32_dpp v21, v21, v21 quad_perm:[2,3,0,1] row_mask:0xf bank_mask:0xf\n"
    "s_nop 1\n"
    "v_max_u32_dpp v21, v21, v21 row_half_mirror row_mask:0xf bank_mask:0xf\n"
    "s_nop 1\n"
    "v_max_u32_dpp v21, v21, v21 row_mirror row_mask:0xf bank_mask:0xf\n"
    "s_nop 1\n"
    "v_max_u32_dpp v21, v21, v21 row_bcast:15 row_mask:0xa bank_mask:0xf\n"
    "s_nop 1\n"
    "v_max_u32_dpp v21, v21, v21 row_bcast:31 row_mask:0xc bank_mask:0xf\n"
    "s_nop 1\n"
    "v_readlane_b32 s47, v21, 63\n"
    "s_and_b32 s50, s47, 0x7f80\n"
    "s_lshr_b32 s47, s50, 3\n"
    "s_sub_u32 s50, s50, 2816\n"
    "s_mul_i32 s51, s50, -8\n"
    "v_pk_maximum3_f16 v21, v28, v28, s50 op_sel_hi:[1,1,0] neg_lo:[0,1,0] neg_hi:[0,1,0]\n"
    "v_pk_mad_u16 v21, v21, 8, s51 op_sel_hi:[1,0,0]\n"
    "v_bfi_b32 v28, s57, v21, v28\n"
    "v_pk_maximum3_f16 v12, v29, v29, s50 op_sel_hi:[1,1,0] neg_lo:[0,1,0] neg_hi:[0,1,0]\n"
    "v_pk_mad_u16 v12, v12, 8, s51 op_sel_hi:[1,0,0]\n"
    "v_bfi_b32 v29, s57, v12, v29\n"
    "v_pk_maximum3_f16 v21, v30, v30, s50 op_sel_hi:[1,1,0] neg_lo:[0,1,0] neg_hi:[0,1,0]\n"
    "v_pk_mad_u16 v21, v21, 8, s51 op_sel_hi:[1,0,0]\n"
    "v_bfi_b32 v30, s57, v21, v30\n"
    "v_pk_maximum3_f16 v12, v31, v31, s50 op_sel_hi:[1,1,0] neg_lo:[0,1,0] neg_hi:[0,1,0]\n"
    "v_pk_mad_u16 v12, v12, 8, s51 op_sel_hi:[1,0,0]\n"
    "v_bfi_b32 v31, s57, v12, v31\n"
    "v_pk_maximum3_f16 v21, v32, v32, s50 op_sel_hi:[1,1,0] neg_lo:[0,1,0] neg_hi:[0,1,0]\n"
    "v_pk_mad_u16 v21, v21, 8, s51 op_sel_hi:[1,0,0]\n"
    "v_bfi_b32 v32, s57, v21, v32\n"
    "v_pk_maximum3_f16 v12, v33, v33, s50 op_sel_hi:[1,1,0] neg_lo:[0,1,0] neg_hi:[0,1,0]\n"
    "v_pk_mad_u16 v12, v12, 8, s51 op_sel_hi:[1,0,0]\n"
    "v_bfi_b32 v33, s57, v12, v33\n"
    "v_pk_maximum3_f16 v21, v34, v34, s50 op_sel_hi:[1,1,0] neg_lo:[0,1,0] neg_hi:[0,1,0]\n"
    "v_pk_mad_u16 v21, v21, 8, s51 op_sel_hi:[1,0,0]\n"
    "v_bfi_b32 v34, s57, v21, v34\n"
    "v_pk_maximum3_f16 v12, v35, v35, s50 op_sel_hi:[1,1,0] neg_lo:[0,1,0] neg_hi:[0,1,0]\n"
    "v_pk_mad_u16 v12, v12, 8, s51 op_sel_hi:[1,0,0]\n"
    "v_bfi_b32 v35, s57, v12, v35\n"
    "v_pk_maximum3_f16 v21, v36, v36, s50 op_sel_hi:[1,1,0] neg_lo:[0,1,0] neg_hi:[0,1,0]\n"
    "v_pk_mad_u16 v21, v21, 8, s51 op_sel_hi:[1,0,0]\n"
    "v_bfi_b32 v36, s57, v21, v36\n"
    "v_pk_maximum3_f16 v12, v37, v37, s50 op_sel_hi:[1,1,0] neg_lo:[0,1,0] neg_hi:[0,1,0]\n"
    "v_pk_mad_u16 v12, v12, 8, s51 op_sel_hi:[1,0,0]\n"
    "v_bfi_b32 v37, s57, v12, v37\n"
    "v_pk_maximum3_f16 v21, v38, v38, s50 op_sel_hi:[1,1,0] neg_lo:[0,1,0] neg_hi:[0,1,0]\n"
    "v_pk_mad_u16 v21, v21, 8, s51 op_sel_hi:[1,0,0]\n"
    "v_bfi_b32 v38, s57, v21, v38\n"
    "v_pk_maximum3_f16 v12, v39, v39, s50 op_sel_hi:[1,1,0] neg_lo:[0,1,0] neg_hi:[0,1,0]\n"
    "v_pk_mad_u16 v12, v12, 8, s51 op_sel_hi:[1,0,0]\n"
    "v_bfi_b32 v39, s57, v12, v39\n"
    "v_pk_maximum3_f16 v21, v40, v40, s50 op_sel_hi:[1,1,0] neg_lo:[0,1,0] neg_hi:[0,1,0]\n"
    "v_pk_mad_u16 v21, v21, 8, s51 op_sel_hi:[1,0,0]\n"
    "v_bfi_b32 v40, s57, v21, v40\n"
    "s_sub_u32 s43, 4112, s47\n"
    "s_lshl_b32 s43, s43, 19\n"
    "v_cvt_pkrtz_f16_f32 v12, s43, s43\n"
    "v_lshl_or_b32 v21, v12, 16, s47\n"
    "ds_write_b128 v15, v[28:31] offset:0\n"
    "ds_write_b128 v15, v[32:35] offset:144\n"
    "ds_write_b128 v15, v[36:39] offset:288\n"
    "ds_write_b32 v16, v40 offset:3456\n"
    "ds_write_b32 v16, v12 offset:3744\n"
    "s_mov_b64 exec, 1\n"
    "ds_or_b32 v25, v21 offset:532\n"
    "s_mov_b64 exec, -1\n"
    "s_cmp_lt_u32 s22, 2\n"
    "s_cselect_b32 s40, 16, 0\n"
    "v_add_u32 v25, s40, v25\n"
    "s_add_u32 s23, s23, 4\n"
    "v_mov_b32 v24, s23\n"
    "s_mov_b32 s36, 0\n"
    "v_mov_b32 v69, v43\n"
    "v_mov_b32 v68, v42\n"
    "s_cmp_lg_u32 s22, 0\n"
    "s_cbranch_scc1 .Lxd_on50\n"
    "v_readfirstlane_b32 s40, v24\n"
    "s_or_b32 s40, s40, s44\n"
    "s_lshl1_add_u32 s41, s40, s54\n"
    "s_cmpk_lt_u32 s40, 256\n"
    "s_cselect_b32 s41, s41, 2\n"
    "v_mov_b32 v12, s41\n"
    "s_mul_i32 s41, s41, 52\n"
    "s_add_u32 s38, s4, s41\n"
    "s_addc_u32 s39, s5, 0\n"
    "ds_write_b32 v25, v12 offset:20\n"
    "v_xor_b32 v15, 0x4000, v15\n"
    "v_xor_b32 v16, 0x4000, v16\n"
    "global_load_dwordx3 v[36:38], v17, s[38:39] offset:-104\n"
    "global_load_dword v41, v18, s[38:39] offset:-8\n"
    ".Lxd_on50:\n"
    "s_cmp_lg_u32 s20, 7\n"
    "s_cbranch_scc1 .Lxd_on52\n"
    "s_waitcnt vmcnt(0)\n"
    "v_readlane_b32 s25, v26, 9\n"
    "v_readlane_b32 s40, v26, 8\n"
    "v_readlane_b32 s41, v27, 8\n"
    "s_mul_i32 s42, s25, 0x9e3779b1\n"
    "s_lshr_b32 s42, s42, 24\n"
    "s_xor_b32 s42, s42, s40\n"
    "s_and_b32 s42, s42, 0xff\n"
    "s_xor_b32 s41, s41, s25\n"
    "s_xor_b32 s41, s41, 0xa5c30f96\n"
    "s_or_b32 s42, s41, s42\n"
    "s_lshr_b32 s41, s40, 8\n"
    "s_mul_i32 s40, s41, 0x9e3779b1\n"
    "s_xor_b32 s41, s41, 0xa5c30f96\n"
    "s_lshr_b32 s40, s40, 24\n"
    "v_xor_b32 v13, s40, v26\n"
    "v_xor_b32 v22, s41, v27\n"
    "v_lshl_or_b32 v13, v13, 24, v22\n"
    "v_cmp_ne_u32 s[66:67], 0, v13\n"
    "s_and_b32 s66, s66, 0xff\n"
    "s_or_b32 s42, s42, s66\n"
    "s_cmp_eq_u32 s20, 7\n"
    "s_cselect_b32 s42, s42, 0\n"
    "s_cmp_lg_u32 s42, 0\n"
    "s_cbranch_scc1 .Lxd_pv51\n"
    ".Lxd_pv51r:\n"
    "v_lshrrev_b32 v22, 8, v26\n"
    "v_mov_b32 v26, s40\n"
    "v_mov_b32 v27, s41\n"
    "v_add_u32_dpp v22, v22, v22 row_shl:4 row_mask:0xf bank_mask:0xf\n"
    "v_add_u32_e64 v43, s25, 1\n"
    "v_lshlrev_b32 v8, 2, v1\n"
    "v_max_u32_dpp v13, v22, v22 quad_perm:[1,0,3,2] row_mask:0xf bank_mask:0xf\n"
    "v_add_u32 v8, 154112, v8\n"
    "s_nop 0\n"
    "v_max_u32_dpp v13, v13, v13 quad_perm:[2,3,0,1] row_mask:0xf bank_mask:0xf\n"
    "v_sub_u32 v22, v22, v13\n"
    "v_subrev_u32 v13, 818, v13\n"
    "v_ldexp_f32 v22, 1.0, v22\n"
    "s_mov_b64 exec, 1\n"
    "ds_write_b64 v8, v[26:27]\n"
    "s_mov_b64 exec, 15\n"
    "ds_write2_b32 v8, v22, v13 offset0:4 offset1:8\n"
    "s_mov_b64 exec, 0x200\n"
    "global_store_dword v11, v43, s[16:17] sc1\n"
    "s_mov_b64 exec, -1\n"
    ".Lxd_on52:\n"
    "s_waitcnt lgkmcnt(0)\n"
    "s_barrier\n"
    "s_mul_i32 s42, s20, 5376\n"
    "s_add_u32 s40, s42, 1048576\n"
    "s_add_u32 s38, s16, s40\n"
    "s_addc_u32 s39, s17, 0\n"
    "s_add_u32 m0, s42, 98304\n"
    "s_nop 0\n"
    "global_load_lds_dwordx4 v68, s[38:39] offset:0 sc1\n"
    "global_load_lds_dwordx4 v68, s[38:39] offset:1024 sc1\n"
    "global_load_lds_dwordx4 v68, s[38:39] offset:2048 sc1\n"
    "global_load_lds_dwordx4 v68, s[38:39] offset:3072 sc1\n"
    "s_add_u32 m0, s42, 102400\n"
    "s_nop 0\n"
    "global_load_lds_dwordx4 v69, s[38:39] offset:0 sc1\n"
    "global_load_lds_dwordx4 v69, s[38:39] offset:256 sc1\n"
    "s_getpc_b64 s[66:67]\n"
    ".Lxd_en53:\n"
    "s_mov_b32 s40, .Lxd_ph1-.Lxd_en53\n"
    "s_cmp_eq_u32 s22, 1\n"
    "s_cselect_b32 s40, .Lxd_ph0-.Lxd_en53, s40\n"
    "s_cmp_eq_u32 s22, 2\n"
    "s_cselect_b32 s40, .Lxd_pl3-.Lxd_en53, s40\n"
    "s_cmp_eq_u32 s22, 3\n"
    "s_cselect_b32 s40, .Lxd_pl2-.Lxd_en53, s40\n"
    "s_ashr_i32 s41, s40, 31\n"
    "s_add_u32 s66, s66, s40\n"
    "s_addc_u32 s67, s67, s41\n"
    "v_mov_b32 v13, 154112\n"
    "ds_read_b64 v[26:27], v13\n"
    "v_add_u32 v42, 98304, v42\n"
    "v_mov_b32 v13, s58\n"
    "s_sub_u32 s40, 1, s22\n"
    "s_and_b32 s40, s40, 3\n"
    "v_lshl_add_u32 v7, s40, 12, v3\n"
    "v_lshl_add_u32 v8, s40, 12, v4\n"
    "v_lshl_add_u32 v9, s40, 12, v5\n"
    "v_lshl_add_u32 v10, s40, 12, v6\n"
    "v_bfe_u32 v21, v1, 4, 1\n"
    "v_lshlrev_b32 v21, 2, v21\n"
    "v_add_u32 v21, 154112, v21\n"
    "ds_read_b32 v0, v21 offset:16\n"
    "ds_read_b32 v23, v21 offset:24\n"
    "ds_read_b32 v22, v13 offset:512\n"
    "ds_read_b64_tr_b16 v[44:45], v9 offset:0\n"
    "ds_read_b64_tr_b16 v[46:47], v10 offset:0\n"
    "ds_read_b64_tr_b16 v[48:49], v7 offset:0\n"
    "ds_read_b64_tr_b16 v[50:51], v8 offset:576\n"
    "ds_read_b64_tr_b16 v[52:53], v8 offset:1152\n"
    "ds_read_b64_tr_b16 v[54:55], v8 offset:1728\n"
    "ds_read_b64_tr_b16 v[56:57], v8 offset:2304\n"
    "ds_read_b64_tr_b16 v[58:59], v8 offset:2880\n"
    "v_mov_b64 v[88:89], 0\n"
    "v_mov_b64 v[90:91], 0\n"
    "s_waitcnt vmcnt(0)\n"
    "s_barrier\n"
    "ds_read_b128 v[92:95], v42 offset:0\n"
    "ds_read_b128 v[96:99], v42 offset:1024\n"
    "ds_read_b128 v[100:103], v42 offset:2048\n"
    "ds_read_b128 v[104:107], v42 offset:3072\n"
    "ds_read_b128 v[108:111], v42 offset:4096\n"
    "ds_read_b128 v[112:115], v42 offset:5120\n"
    "ds_read_b128 v[116:119], v42 offset:6144\n"
    "ds_read_b128 v[120:123], v42 offset:7168\n"
    "ds_read_b128 v[124:127], v42 offset:8192\n"
    "ds_read_b128 v[128:131], v42 offset:9216\n"
    "ds_read_b128 v[132:135], v42 offset:10240\n"
    "ds_read_b128 v[136:139], v42 offset:11264\n"
    "ds_read_b128 v[140:143], v42 offset:12288\n"
    "ds_read_b128 v[144:147], v42 offset:13312\n"
    "s_cmp_lt_u32 s2, 8\n"
    "s_cbranch_scc1 .Lxd_hp54\n"
    "s_mov_b32 m0, 159744\n"
    "v_lshlrev_b32 v63, 7, v63\n"
    "global_load_lds_dword v63, s[8:9]\n"
    "s_mov_b32 m0, 159744\n"
    "v_lshlrev_b32 v64, 7, v64\n"
    "global_load_lds_dword v64, s[8:9]\n"
    ".Lxd_hp54:\n"
    "ds_read_b128 v[148:151], v42 offset:14336\n"
    "ds_read_b128 v[152:155], v42 offset:15360\n"
    "ds_read_b128 v[156:159], v42 offset:16384\n"
    "ds_read_b128 v[160:163], v42 offset:17408\n"
    "ds_read_b128 v[164:167], v42 offset:18432\n"
    "ds_read_b128 v[168:171], v42 offset:19456\n"
    "ds_read_b128 v[172:175], v42 offset:20480\n"
    "ds_read_b128 v[176:179], v42 offset:21504\n"
    "ds_read_b128 v[180:183], v42 offset:22528\n"
    "ds_read_b128 v[184:187], v42 offset:23552\n"
    "ds_read_b128 v[188:191], v42 offset:24576\n"
    "ds_read_b128 v[192:195], v42 offset:25600\n"
    "ds_read_b128 v[196:199], v42 offset:26624\n"
    "ds_read_b128 v[200:203], v42 offset:27648\n"
    "ds_read_b128 v[204:207], v42 offset:28672\n"
    "ds_read_b128 v[208:211], v42 offset:29696\n"
    "ds_read_b128 v[212:215], v42 offset:30720\n"
    "ds_read_b128 v[216:219], v42 offset:31744\n"
    "ds_read_b128 v[220:223], v42 offset:32768\n"
    "ds_read_b128 v[224:227], v42 offset:33792\n"
    "ds_read_b128 v[228:231], v42 offset:34816\n"
    "ds_read_b128 v[232:235], v42 offset:35840\n"
    "ds_read_b128 v[236:239], v42 offset:36864\n"
    "ds_read_b128 v[240:243], v42 offset:37888\n"
    "ds_read_b128 v[244:247], v42 offset:38912\n"
    "ds_read_b128 v[248:251], v42 offset:39936\n"
    "ds_read_b128 v[252:255], v42 offset:40960\n"
    "s_waitcnt lgkmcnt(15)\n"
    "v_readfirstlane_b32 s45, v22\n"
    "v_readfirstlane_b32 s28, v26\n"
    "v_readfirstlane_b32 s29, v27\n"
    "v_pk_mul_f16 v64, v44, v46\n"
    "v_pk_mul_f16 v65, v45, v47\n"
    "v_pk_mul_f16 v66, v48, v48 op_sel_hi:[0,1]\n"
    "v_pk_mul_f16 v67, v48, v49 op_sel_hi:[0,1]\n"
    "v_pk_mul_f16 v60, v44, s45 op_sel:[0,1]\n"
    "v_pk_mul_f16 v61, v45, s45 op_sel:[0,1]\n"
    "v_pk_mul_f16 v62, v46, s45 op_sel:[0,1]\n"
    "v_pk_mul_f16 v63, v47, s45 op_sel:[0,1]\n"
    "s_waitcnt lgkmcnt(0)\n"
    "s_branch .Lxd_enter\n"
    ".Lxd_slow:\n"
    "s_cmp_eq_u32 s45, -1\n"
    "s_cbranch_scc1 .Lxd_sl7\n"
    "s_mov_b32 s59, 0x100000\n"
    "s_and_b32 s40, s45, 0xff0\n"
    "s_cbranch_scc0 .Lxd_sl1\n"
    ".Lxd_sl2:\n"
    "s_cmp_ge_i32 s45, -16\n"
    "s_cbranch_scc1 .Lxd_sl3\n"
    "s_bitset0_b32 s45, 31\n"
    ".Lxd_sl6:\n"
    "s_cmp_lg_u32 s56, 0\n"
    "s_cbranch_scc1 .Lxd_sl9\n"
    "v_readfirstlane_b32 s40, v13\n"
    "s_lshl4_add_u32 s41, s22, s58\n"
    "s_mov_b32 s56, 2\n"
    "s_cmp_lt_u32 s40, s41\n"
    "s_cbranch_scc0 .Lxd_gran\n"
    ".Lxd_sl9:\n"
    "s_setpc_b64 s[60:61]\n"
    ".Lxd_sl1:\n"
    "s_sleep 1\n"
    "ds_read_b32 v22, v13 offset:512\n"
    "s_waitcnt lgkmcnt(0)\n"
    "v_readfirstlane_b32 s45, v22\n"
    "s_sub_u32 s59, s59, 1\n"
    "s_cbranch_scc1 .Lxd_sl5\n"
    "s_and_b32 s40, s45, 0xff0\n"
    "s_cbranch_scc0 .Lxd_sl1\n"
    "s_cmp_gt_i32 s45, 0\n"
    "s_cbranch_scc0 .Lxd_sl2\n"
    "s_setpc_b64 s[60:61]\n"
    ".Lxd_sl3:\n"
    "v_lshl_add_u32 v13, v1, 2, v13\n"
    "s_mov_b64 exec, 7\n"
    ".Lxd_sl4:\n"
    "ds_read_b32 v22, v13 offset:4\n"
    "s_waitcnt lgkmcnt(0)\n"
    "v_cmp_eq_u32 s[40:41], 0, v22\n"
    "v_cmp_ne_u32 s[42:43], -1, v22\n"
    "s_sub_u32 s59, s59, 1\n"
    "s_cbranch_scc1 .Lxd_sl5\n"
    "s_cmp_lg_u32 s40, 0\n"
    "s_cbranch_scc1 .Lxd_sl4\n"
    "s_cmp_eq_u32 s42, 0\n"
    "s_cbranch_scc1 .Lxd_sl5\n"
    "s_mov_b64 exec, -1\n"
    "s_mov_b32 s45, 1\n"
    "s_mov_b32 s56, 0\n"
    "s_setpc_b64 s[60:61]\n"
    ".Lxd_sl5:\n"
    "s_mov_b64 exec, -1\n"
    "s_mov_b32 s45, -1\n"
    ".Lxd_sl7:\n"
    "s_cmp_lg_u32 s56, 1\n"
    "s_cbranch_scc1 .Lxd_sl6\n"
    "s_branch .Lxd_dsum\n"
    ".Lxd_gran:\n"
    "s_lshl4_add_u32 s40, s22, s58\n"
    "v_lshrrev_b32 v13, 4, v1\n"
    "v_lshl_add_u32 v13, v13, 2, s40\n"
    "s_waitcnt vmcnt(0)\n"
    "ds_read2_b32 v[26:27], v13 offset1:16\n"
    "v_and_b32 v13, 15, v1\n"
    "v_lshlrev_b32 v13, 15, v13\n"
    "s_mov_b32 s42, 0x1fff1fff\n"
    "s_mov_b32 s43, s42\n"
    "s_add_u32 s40, s16, 65528\n"
    "s_addc_u32 s41, s17, 0\n"
    "v_mov_b32 v42, 0\n"
    "s_mov_b32 s56, 2\n"
    "s_waitcnt lgkmcnt(0)\n"
    "v_cmp_lt_i32 s[30:31], 0, v26\n"
    "v_cmp_lt_i32 s[64:65], 0, v27\n"
    "v_lshl_add_u32 v43, v26, 2, v13\n"
    "v_lshl_add_u32 v27, v27, 2, v13\n"
    "v_mov_b32 v26, 0\n"
    "s_and_b64 s[30:31], s[30:31], s[42:43]\n"
    "s_cbranch_scc0 .Lxd_gr30\n"
    "s_mov_b64 exec, s[30:31]\n"
    "global_load_dwordx2 v[42:43], v43, s[40:41] sc1\n"
    "s_mov_b64 exec, -1\n"
    "s_mov_b32 s56, 1\n"
    ".Lxd_gr30:\n"
    "s_and_b64 s[64:65], s[64:65], s[42:43]\n"
    "s_cbranch_scc0 .Lxd_gr64\n"
    "s_mov_b64 exec, s[64:65]\n"
    "global_load_dwordx2 v[26:27], v27, s[40:41] sc1\n"
    "s_mov_b64 exec, -1\n"
    "s_mov_b32 s56, 1\n"
    ".Lxd_gr64:\n"
    "s_setpc_b64 s[60:61]\n"
    ".Lxd_dsum:\n"
    "s_lshl4_add_u32 s42, s22, s58\n"
    "s_waitcnt vmcnt(0)\n"
    "s_cmp_eq_u64 s[30:31], 0\n"
    "s_cbranch_scc1 .Lxd_ds30\n"
    "v_xor_b32 v13, s28, v42\n"
    "v_xor_b32 v22, s29, v43\n"
    "v_lshl_or_b32 v13, v13, 24, v22\n"
    "v_and_b32 v22, 0xffffff00, v42\n"
    "v_cmp_ne_u32 s[40:41], 0, v13\n"
    "v_lshrrev_b32 v13, 4, v1\n"
    "v_add_f32_dpp v22, v22, v22 quad_perm:[1,0,3,2] row_mask:0xf bank_mask:0xf\n"
    "s_and_b64 s[40:41], s[40:41], s[30:31]\n"
    "s_cbranch_scc1 .Lxd_ds9\n"
    "v_add_f32_dpp v22, v22, v22 quad_perm:[2,3,0,1] row_mask:0xf bank_mask:0xf\n"
    "v_lshl_add_u32 v13, v13, 2, s42\n"
    "s_nop 0\n"
    "v_add_f32_dpp v22, v22, v22 row_half_mirror row_mask:0xf bank_mask:0xf\n"
    "s_nop 1\n"
    "v_add_f32_dpp v22, v22, v22 row_mirror row_mask:0xf bank_mask:0xf\n"
    "ds_write_b32 v13, v22 offset:1024\n"
    ".Lxd_ds30:\n"
    "s_cmp_eq_u64 s[64:65], 0\n"
    "s_cbranch_scc1 .Lxd_ds64\n"
    "v_xor_b32 v13, s28, v26\n"
    "v_xor_b32 v22, s29, v27\n"
    "v_lshl_or_b32 v13, v13, 24, v22\n"
    "v_and_b32 v22, 0xffffff00, v26\n"
    "v_cmp_ne_u32 s[40:41], 0, v13\n"
    "v_lshrrev_b32 v13, 4, v1\n"
    "v_add_f32_dpp v22, v22, v22 quad_perm:[1,0,3,2] row_mask:0xf bank_mask:0xf\n"
    "s_and_b64 s[40:41], s[40:41], s[64:65]\n"
    "s_cbranch_scc1 .Lxd_ds9\n"
    "v_add_f32_dpp v22, v22, v22 quad_perm:[2,3,0,1] row_mask:0xf bank_mask:0xf\n"
    "v_lshl_add_u32 v13, v13, 2, s42\n"
    "s_nop 0\n"
    "v_add_f32_dpp v22, v22, v22 row_half_mirror row_mask:0xf bank_mask:0xf\n"
    "s_nop 1\n"
    "v_add_f32_dpp v22, v22, v22 row_mirror row_mask:0xf bank_mask:0xf\n"
    "ds_write_b32 v13, v22 offset:1088\n"
    ".Lxd_ds64:\n"
    "s_mov_b32 s56, 2\n"
    ".Lxd_ds9:\n"
    "s_setpc_b64 s[60:61]\n"
    ".Lxd_mark:\n"
    "v_lshl_add_u32 v13, v1, 2, v13\n"
    "s_add_u32 s40, s58, 248\n"
    "v_mov_b32 v24, -1\n"
    "v_cmp_gt_u32 s[42:43], s40, v13\n"
    "s_mov_b32 s62, 0x88888888\n"
    "s_mov_b32 s63, s62\n"
    "s_and_b64 exec, s[42:43], s[62:63]\n"
    "ds_write_b32 v13, v24 offset:8\n"
    "s_mov_b64 exec, s[42:43]\n"
    "v_cndmask_b32 v24, 0, -16, s[62:63]\n"
    "s_brev_b32 s40, 1\n"
    "v_writelane_b32 v24, s40, 2\n"
    "s_lshl_b32 s40, 1, s22\n"
    "v_or_b32 v24, s40, v24\n"
    "s_mov_b32 s36, 1\n"
    "ds_or_b32 v13, v24 offset:520\n"
    "s_mov_b64 exec, -1\n"
    "s_setpc_b64 s[60:61]\n"
    ".Lxd_tf11:\n"
    "v_mov_b32 v12, -1\n"
    "v_mov_b32 v13, v25\n"
    "s_call_b64 s[60:61], .Lxd_mark\n"
    "s_branch .Lxd_tf11r\n"
    ".Lxd_ck14:\n"
    "v_add_u32 v13, 4, v25\n"
    "s_call_b64 s[60:61], .Lxd_slow\n"
    "s_branch .Lxd_ck14r\n"
    ".Lxd_ck17:\n"
    "v_add_u32 v13, 8, v25\n"
    "s_call_b64 s[60:61], .Lxd_slow\n"
    "s_branch .Lxd_ck17r\n"
    ".Lxd_ck20:\n"
    "v_add_u32 v13, 12, v25\n"
    "s_call_b64 s[60:61], .Lxd_slow\n"
    "s_branch .Lxd_ck20r\n"
    ".Lxd_ck23:\n"
    "v_add_u32 v13, 16, v25\n"
    "s_call_b64 s[60:61], .Lxd_slow\n"
    "s_branch .Lxd_ck23r\n"
    ".Lxd_dr24:\n"
    "v_add_u32 v13, 16, v25\n"
    "s_call_b64 s[60:61], .Lxd_mark\n"
    "s_branch .Lxd_dr24r\n"
    ".Lxd_ck27:\n"
    "v_add_u32 v13, 4, v25\n"
    "s_call_b64 s[60:61], .Lxd_slow\n"
    "s_branch .Lxd_ck27r\n"
    ".Lxd_ck30:\n"
    "v_add_u32 v13, 8, v25\n"
    "s_call_b64 s[60:61], .Lxd_slow\n"
    "s_branch .Lxd_ck30r\n"
    ".Lxd_ck33:\n"
    "v_add_u32 v13, 12, v25\n"
    "s_call_b64 s[60:61], .Lxd_slow\n"
    "s_branch .Lxd_ck33r\n"
    ".Lxd_dr37:\n"
    "v_add_u32 v13, 16, v25\n"
    "s_call_b64 s[60:61], .Lxd_mark\n"
    "s_branch .Lxd_dr37r\n"
    ".Lxd_on36:\n"
    "s_and_b32 s40, s45, 15\n"
    "s_cbranch_scc1 .Lxd_dr37\n"
    "v_mov_b32 v24, 1\n"
    "s_mov_b64 exec, 1\n"
    "global_atomic_add v24, v17, v24, s[52:53] sc0 sc1\n"
    "s_mov_b64 exec, -1\n"
    ".Lxd_dr37r:\n"
    "s_branch .Lxd_on36r\n"
    ".Lxd_ck38:\n"
    "v_add_u32 v13, 16, v25\n"
    "s_call_b64 s[60:61], .Lxd_slow\n"
    "s_branch .Lxd_ck38r\n"
    ".Lxd_f2:\n"
    "s_mov_b32 s59, 0x20000\n"
    ".Lxd_f4:\n"
    "s_call_b64 s[60:61], .Lxd_dsum\n"
    "s_cmp_eq_u32 s56, 2\n"
    "s_cbranch_scc1 .Lxd_f3\n"
    "s_sleep 2\n"
    "s_call_b64 s[60:61], .Lxd_gran\n"
    "s_sub_u32 s59, s59, 1\n"
    "s_cbranch_scc0 .Lxd_f4\n"
    "s_waitcnt vmcnt(0)\n"
    "s_branch .Lxd_f3\n"
    ".Lxd_on1:\n"
    "s_mov_b64 exec, 0x3ff\n"
    "global_load_dwordx2 v[26:27], v11, s[16:17] sc1\n"
    "s_mov_b64 exec, -1\n"
    "s_branch .Lxd_on1r\n"
    ".Lxd_pv2:\n"
    "s_mov_b32 s59, 0x20000\n"
    ".Lxd_pv2l:\n"
    "s_sleep 2\n"
    "s_mov_b64 exec, 0x3ff\n"
    "global_load_dwordx2 v[26:27], v11, s[16:17] sc1\n"
    "s_mov_b64 exec, -1\n"
    "s_waitcnt vmcnt(0)\n"
    "v_readlane_b32 s25, v26, 9\n"
    "v_readlane_b32 s40, v26, 8\n"
    "v_readlane_b32 s41, v27, 8\n"
    "s_mul_i32 s42, s25, 0x9e3779b1\n"
    "s_lshr_b32 s42, s42, 24\n"
    "s_xor_b32 s42, s42, s40\n"
    "s_and_b32 s42, s42, 0xff\n"
    "s_xor_b32 s41, s41, s25\n"
    "s_xor_b32 s41, s41, 0xa5c30f96\n"
    "s_or_b32 s42, s41, s42\n"
    "s_lshr_b32 s41, s40, 8\n"
    "s_mul_i32 s40, s41, 0x9e3779b1\n"
    "s_xor_b32 s41, s41, 0xa5c30f96\n"
    "s_lshr_b32 s40, s40, 24\n"
    "v_xor_b32 v13, s40, v26\n"
    "v_xor_b32 v22, s41, v27\n"
    "v_lshl_or_b32 v13, v13, 24, v22\n"
    "v_cmp_ne_u32 s[66:67], 0, v13\n"
    "s_and_b32 s66, s66, 0xff\n"
    "s_or_b32 s42, s42, s66\n"
    "s_cmp_eq_u32 s42, 0\n"
    "s_cbranch_scc1 .Lxd_pv2r\n"
    "s_sub_u32 s59, s59, 1\n"
    "s_cbranch_scc0 .Lxd_pv2l\n"
    "s_branch .Lxd_pv2r\n"
    ".Lxd_ck6:\n"
    "v_mov_b32 v13, s58\n"
    "s_call_b64 s[60:61], .Lxd_slow\n"
    "s_branch .Lxd_ck6r\n"
    ".Lxd_on7:\n"
    "v_mov_b32 v24, 1\n"
    "s_mov_b64 exec, 1\n"
    "global_atomic_add v24, v17, v24, s[52:53] sc0 sc1\n"
    "s_mov_b64 exec, -1\n"
    "s_branch .Lxd_on7r\n"
    ".Lxd_tf8:\n"
    "v_mov_b32 v12, -1\n"
    "v_mov_b32 v13, v25\n"
    "s_call_b64 s[60:61], .Lxd_mark\n"
    "s_branch .Lxd_tf8r\n"
    ".Lxd_on9:\n"
    "s_waitcnt vmcnt(0)\n"
    "v_readfirstlane_b32 s40, v24\n"
    "s_or_b32 s40, s40, s44\n"
    "s_lshl1_add_u32 s41, s40, s54\n"
    "s_cmpk_lt_u32 s40, 256\n"
    "s_cselect_b32 s41, s41, 2\n"
    "v_mov_b32 v12, s41\n"
    "s_cbranch_scc0 .Lxd_tf8\n"
    ".Lxd_tf8r:\n"
    "s_mul_i32 s41, s41, 52\n"
    "s_add_u32 s38, s4, s41\n"
    "s_addc_u32 s39, s5, 0\n"
    "ds_write_b32 v25, v12 offset:20\n"
    "v_xor_b32 v15, 0x4000, v15\n"
    "v_xor_b32 v16, 0x4000, v16\n"
    "global_load_dwordx3 v[36:38], v17, s[38:39] offset:-104\n"
    "global_load_dword v41, v18, s[38:39] offset:-8\n"
    "s_branch .Lxd_on9r\n"
    ".Lxd_on10:\n"
    "v_mov_b32 v24, 1\n"
    "s_mov_b64 exec, 1\n"
    "global_atomic_add v24, v17, v24, s[52:53] sc0 sc1\n"
    "s_mov_b64 exec, -1\n"
    "s_branch .Lxd_on10r\n"
    ".Lxd_pv51:\n"
    "s_mov_b32 s59, 0x20000\n"
    ".Lxd_pv51l:\n"
    "s_sleep 2\n"
    "s_mov_b64 exec, 0x3ff\n"
    "global_load_dwordx2 v[26:27], v11, s[16:17] sc1\n"
    "s_mov_b64 exec, -1\n"
    "s_waitcnt vmcnt(0)\n"
    "v_readlane_b32 s25, v26, 9\n"
    "v_readlane_b32 s40, v26, 8\n"
    "v_readlane_b32 s41, v27, 8\n"
    "s_mul_i32 s42, s25, 0x9e3779b1\n"
    "s_lshr_b32 s42, s42, 24\n"
    "s_xor_b32 s42, s42, s40\n"
    "s_and_b32 s42, s42, 0xff\n"
    "s_xor_b32 s41, s41, s25\n"
    "s_xor_b32 s41, s41, 0xa5c30f96\n"
    "s_or_b32 s42, s41, s42\n"
    "s_lshr_b32 s41, s40, 8\n"
    "s_mul_i32 s40, s41, 0x9e3779b1\n"
    "s_xor_b32 s41, s41, 0xa5c30f96\n"
    "s_lshr_b32 s40, s40, 24\n"
    "v_xor_b32 v13, s40, v26\n"
    "v_xor_b32 v22, s41, v27\n"
    "v_lshl_or_b32 v13, v13, 24, v22\n"
    "v_cmp_ne_u32 s[66:67], 0, v13\n"
    "s_and_b32 s66, s66, 0xff\n"
    "s_or_b32 s42, s42, s66\n"
    "s_cmp_eq_u32 s42, 0\n"
    "s_cbranch_scc1 .Lxd_pv51r\n"
    "s_sub_u32 s59, s59, 1\n"
    "s_cbranch_scc0 .Lxd_pv51l\n"
    "s_branch .Lxd_pv51r\n"
    ".Lxd_end:\n"
    : "+{v0}"(tid) : "{s[0:1]}"(__builtin_amdgcn_kernarg_segment_ptr()), "{s2}"(blockIdx.x)
    : "memory", "vcc", "scc", "v1", "v2", "v3", "v4", "v5", "v6", "v7", "v8", "v9", "v10", "v11", "v12", "v13", "v14", "v15", "v16", "v17", "v18", "v19", "v20", "v21", "v22", "v23", "v24", "v25", "v26", "v27", "v28", "v29", "v30", "v31", "v32", "v33", "v34", "v35", "v36", "v37", "v38", "v39", "v40", "v41", "v42", "v43", "v44", "v45", "v46", "v47", "v48", "v49", "v50", "v51", "v52", "v53", "v54", "v55", "v56", "v57", "v58", "v59", "v60", "v61", "v62", "v63", "v64", "v65", "v66", "v67", "v68", "v69", "v70", "v71", "v72", "v73", "v74", "v75", "v76", "v77", "v78", "v79", "v80", "v81", "v82", "v83", "v84", "v85", "v86", "v87", "v88", "v89", "v90", "v91", "v92", "v93", "v94", "v95", "v96", "v97", "v98", "v99", "v100", "v101", "v102", "v103", "v104", "v105", "v106", "v107", "v108", "v109", "v110", "v111", "v112", "v113", "v114", "v115", "v116", "v117", "v118", "v119", "v120", "v121", "v122", "v123", "v124", "v125", "v126", "v127", "v128", "v129", "v130", "v131", "v132", "v133", "v134", "v135", "v136", "v137", "v138", "v139", "v140", "v141", "v142", "v143", "v144", "v145", "v146", "v147", "v148", "v149", "v150", "v151", "v152", "v153", "v154", "v155", "v156", "v157", "v158", "v159", "v160", "v161", "v162", "v163", "v164", "v165", "v166", "v167", "v168", "v169", "v170", "v171", "v172", "v173", "v174", "v175", "v176", "v177", "v178", "v179", "v180", "v181", "v182", "v183", "v184", "v185", "v186", "v187", "v188", "v189", "v190", "v191", "v192", "v193", "v194", "v195", "v196", "v197", "v198", "v199", "v200", "v201", "v202", "v203", "v204", "v205", "v206", "v207", "v208", "v209", "v210", "v211", "v212", "v213", "v214", "v215", "v216", "v217", "v218", "v219", "v220", "v221", "v222", "v223", "v224", "v225", "v226", "v227", "v228", "v229", "v230", "v231", "v232", "v233", "v234", "v235", "v236", "v237", "v238", "v239", "v240", "v241", "v242", "v243", "v244", "v245", "v246", "v247", "v248", "v249", "v250", "v251", "v252", "v253", "v254", "v255", "s3", "s4", "s5", "s6", "s7", "s8", "s9", "s10", "s11", "s12", "s13", "s14", "s15", "s16", "s17", "s18", "s19", "s20", "s21", "s22", "s23", "s24", "s25", "s26", "s27", "s28", "s29", "s30", "s31", "s34", "s35", "s36", "s37", "s38", "s39", "s40", "s41", "s42", "s43", "s44", "s45", "s46", "s47", "s48", "s49", "s50", "s51", "s52", "s53", "s54", "s55", "s56", "s57", "s58", "s59", "s60", "s61", "s62", "s63", "s64", "s65", "s66", "s67", "s68", "s69", "s70", "s71");
  asm volatile("" :: "v"((__attribute__((address_space(3))) char*)shm));
}

extern "C" void kernel_launch(void* const* d_in, const int* in_sizes, int n_in, void* d_out, int out_size,
                              void* d_ws, size_t ws_size, hipStream_t stream) {
  xd_kernel<<<256, 512, 0, stream>>>((const int*)d_in[0], (const u16*)d_in[4], (const u16*)d_in[1], (const u16*)d_in[5], (const u16*)d_in[2], (const u16*)d_in[3], (char*)d_ws,
                                     (const u16*)d_in[6], (const u16*)d_in[7], (u16*)d_out);
}
```

```cpp
#include <hip/hip_runtime.h>
typedef unsigned short u16;

__device__ const __attribute__((aligned(16), used, visibility("hidden"))) u16 xd_tab[5120] asm("xd_tab") = {924, 925, 928, 929, 926, 927, 930, 931, 934, 935, 938, 939, 932, 933, 936, 937, 940, 941, 944, 945, 942, 943, 946, 947, 948, 0, 949, 0, 0, 0, 0, 0, 2, 36, 138, 172, 0, 1, 24, 25, 338, 372, 474, 508, 34, 35, 57, 58, 546, 580, 682, 716, 68, 69, 90, 91, 816, 817, 850, 952, 102, 99, 123, 124, 680, 681, 684, 685, 20, 21, 22, 23, 714, 715, 717, 718, 54, 55, 56, 53, 748, 749, 750, 751, 88, 89, 86, 87, 782, 779, 783, 784, 122, 119, 120, 121, 152, 153, 154, 155, 284, 285, 286, 287, 186, 187, 188, 185, 318, 319, 320, 317, 220, 221, 218, 219, 352, 353, 350, 351, 254, 251, 252, 253, 386, 383, 384, 385, 416, 417, 418, 419, 548, 549, 550, 551, 450, 451, 452, 449, 582, 583, 584, 581, 484, 485, 482, 483, 616, 617, 614, 615, 518, 515, 516, 517, 650, 647, 648, 649, 544, 545, 552, 553, 16, 17, 18, 19, 578, 579, 585, 586, 50, 51, 52, 49, 612, 613, 618, 619, 84, 85, 82, 83, 646, 643, 651, 652, 118, 115, 116, 117, 148, 149, 150, 151, 280, 281, 282, 283, 182, 183, 184, 181, 314, 315, 316, 313, 216, 217, 214, 215, 348, 349, 346, 347, 250, 247, 248, 249, 382, 379, 380, 381, 412, 413, 414, 415, 408, 409, 420, 421, 446, 447, 448, 445, 442, 443, 453, 454, 480, 481, 478, 479, 476, 477, 486, 487, 514, 511, 512, 513, 510, 507, 519, 520, 12, 13, 14, 15, 144, 145, 146, 147, 46, 47, 48, 45, 178, 179, 180, 177, 80, 81, 78, 79, 212, 213, 210, 211, 114, 111, 112, 113, 246, 243, 244, 245, 276, 277, 278, 279, 272, 273, 288, 289, 310, 311, 312, 309, 306, 307, 321, 322, 344, 345, 342, 343, 340, 341, 354, 355, 378, 375, 376, 377, 374, 371, 387, 388, 8, 9, 10, 11, 140, 141, 142, 143, 42, 43, 44, 41, 174, 175, 176, 173, 76, 77, 74, 75, 208, 209, 206, 207, 110, 107, 108, 109, 242, 239, 240, 241, 136, 137, 156, 157, 4, 5, 6, 7, 170, 171, 189, 190, 38, 39, 40, 37, 204, 205, 222, 223, 72, 73, 70, 71, 238, 235, 255, 256, 106, 103, 104, 105, 0, 0, 0, 0, 0, 0, 0, 0, 0, 0, 0, 0, 0, 0, 0, 0, 0, 0, 0, 0, 0, 0, 0, 0, 0, 0, 0, 0, 0, 0, 0, 0, 0, 0, 0, 0, 0, 0, 0, 0, 0, 0, 0, 0, 0, 0, 0, 0, 0, 0, 0, 0, 0, 0, 0, 0, 0, 0, 0, 0, 0, 0, 0, 0, 0, 0, 0, 0, 0, 0, 0, 0, 0, 0, 0, 0, 0, 0, 0, 0, 0, 0, 0, 0, 0, 0, 0, 0, 0, 0, 0, 0, 0, 0, 0, 0, 0, 0, 0, 0, 0, 0, 0, 0, 0, 0, 0, 0, 0, 0, 0, 0, 0, 0, 0, 0, 0, 0, 0, 0, 0, 0, 0, 0, 0, 0, 0, 0, 924, 925, 926, 927, 934, 935, 932, 933, 940, 941, 942, 943, 948, 0, 0, 0, 2, 36, 0, 1, 338, 372, 34, 35, 546, 580, 68, 69, 816, 817, 102, 99, 928, 929, 930, 931, 938, 939, 936, 937, 944, 945, 946, 947, 949, 0, 0, 0, 138, 172, 24, 25, 474, 508, 57, 58, 682, 716, 90, 91, 850, 952, 123, 124, 680, 681, 20, 21, 714, 715, 54, 55, 748, 749, 88, 89, 782, 779, 122, 119, 152, 153, 284, 285, 186, 187, 318, 319, 220, 221, 352, 353, 254, 251, 386, 383, 684, 685, 22, 23, 717, 718, 56, 53, 750, 751, 86, 87, 783, 784, 120, 121, 154, 155, 286, 287, 188, 185, 320, 317, 218, 219, 350, 351, 252, 253, 384, 385, 416, 417, 548, 549, 450, 451, 582, 583, 484, 485, 616, 617, 518, 515, 650, 647, 544, 545, 16, 17, 578, 579, 50, 51, 612, 613, 84, 85, 646, 643, 118, 115, 418, 419, 550, 551, 452, 449, 584, 581, 482, 483, 614, 615, 516, 517, 648, 649, 552, 553, 18, 19, 585, 586, 52, 49, 618, 619, 82, 83, 651, 652, 116, 117, 148, 149, 280, 281, 182, 183, 314, 315, 216, 217, 348, 349, 250, 247, 382, 379, 412, 413, 408, 409, 446, 447, 442, 443, 480, 481, 476, 477, 514, 511, 510, 507, 150, 151, 282, 283, 184, 181, 316, 313, 214, 215, 346, 347, 248, 249, 380, 381, 414, 415, 420, 421, 448, 445, 453, 454, 478, 479, 486, 487, 512, 513, 519, 520, 12, 13, 144, 145, 46, 47, 178, 179, 80, 81, 212, 213, 114, 111, 246, 243, 276, 277, 272, 273, 310, 311, 306, 307, 344, 345, 340, 341, 378, 375, 374, 371, 14, 15, 146, 147, 48, 45, 180, 177, 78, 79, 210, 211, 112, 113, 244, 245, 278, 279, 288, 289, 312, 309, 321, 322, 342, 343, 354, 355, 376, 377, 387, 388, 8, 9, 140, 141, 42, 43, 174, 175, 76, 77, 208, 209, 110, 107, 242, 239, 136, 137, 4, 5, 170, 171, 38, 39, 204, 205, 72, 73, 238, 235, 106, 103, 10, 11, 142, 143, 44, 41, 176, 173, 74, 75, 206, 207, 108, 109, 240, 241, 156, 157, 6, 7, 189, 190, 40, 37, 222, 223, 70, 71, 255, 256, 104, 105, 0, 0, 0, 0, 0, 0, 0, 0, 0, 0, 0, 0, 0, 0, 0, 0, 0, 0, 0, 0, 0, 0, 0, 0, 0, 0, 0, 0, 0, 0, 0, 0, 0, 0, 0, 0, 0, 0, 0, 0, 0, 0, 0, 0, 0, 0, 0, 0, 0, 0, 0, 0, 0, 0, 0, 0, 0, 0, 0, 0, 0, 0, 0, 0, 0, 0, 0, 0, 0, 0, 0, 0, 0, 0, 0, 0, 0, 0, 0, 0, 0, 0, 0, 0, 0, 0, 0, 0, 0, 0, 0, 0, 0, 0, 0, 0, 0, 0, 0, 0, 0, 0, 0, 0, 0, 0, 0, 0, 0, 0, 0, 0, 0, 0, 0, 0, 0, 0, 0, 0, 0, 0, 0, 0, 0, 0, 0, 0, 0, 0, 0, 0, 0, 0, 0, 0, 0, 0, 0, 0, 0, 0, 0, 0, 0, 0, 0, 0, 0, 0, 0, 0, 0, 0, 0, 0, 0, 0, 0, 0, 0, 16256, 0, 16256, 0, 16256, 0, 16256, 0, 16256, 0, 16256, 0, 16256, 0, 16256, 0, 16256, 0, 16256, 0, 16256, 0, 16256, 0, 16128, 0, 16256, 0, 16128, 0, 0, 0, 16128, 0, 16256, 0, 16256, 0, 16256, 0, 16128, 0, 16256, 0, 16256, 0, 16256, 0, 16128, 0, 16256, 0, 16256, 0, 16256, 0, 16128, 0, 16256, 0, 16256, 0, 16256, 0, 16256, 0, 16256, 0, 16256, 0, 16256, 0, 16256, 0, 16256, 0, 16256, 0, 16256, 0, 16256, 0, 16256, 0, 16256, 0, 16256, 0, 16256, 0, 16256, 0, 16256, 0, 16256, 0, 16256, 0, 16256, 0, 16256, 0, 16256, 0, 16256, 0, 16256, 0, 16256, 0, 16256, 0, 16256, 0, 16256, 0, 16256, 0, 16256, 0, 16256, 0, 16256, 0, 16256, 0, 16256, 0, 16128, 0, 16256, 0, 16256, 0, 16256, 0, 16128, 0, 16256, 0, 16256, 0, 16256, 0, 16128, 0, 16256, 0, 16256, 0, 16256, 0, 16128, 0, 16256, 0, 16256, 0, 16256, 0, 16256, 0, 16256, 0, 16256, 0, 16256, 0, 16256, 0, 16256, 0, 16256, 0, 16256, 0, 16256, 0, 16256, 0, 16256, 0, 16256, 0, 16256, 0, 16256, 0, 16256, 0, 16256, 0, 16256, 0, 16256, 0, 16256, 0, 16256, 0, 16256, 0, 16256, 0, 16256, 0, 16256, 0, 16256, 0, 16256, 0, 16256, 0, 16256, 0, 16256, 0, 16256, 0, 16256, 0, 16256, 0, 16256, 0, 16256, 0, 16256, 0, 16256, 0, 16256, 0, 16256, 0, 16256, 0, 16256, 0, 16256, 0, 16256, 0, 16256, 0, 16256, 0, 16256, 0, 16256, 0, 16256, 0, 16256, 0, 16256, 0, 16256, 0, 16256, 0, 16256, 0, 16256, 0, 16256, 0, 16256, 0, 16256, 0, 16256, 0, 16256, 0, 16256, 0, 16256, 0, 16256, 0, 16256, 0, 16256, 0, 16256, 0, 16256, 0, 16256, 0, 16256, 0, 16256, 0, 16256, 0, 16256, 0, 16256, 0, 16256, 0, 16256, 0, 16256, 0, 16256, 0, 16256, 0, 16256, 0, 16256, 0, 16256, 0, 16256, 0, 16128, 0, 16256, 0, 16256, 0, 16256, 0, 16128, 0, 16256, 0, 16256, 0, 16256, 0, 16128, 0, 16256, 0, 16256, 0, 16256, 0, 16128, 0, 16256, 0, 16256, 0, 16256, 0, 0, 0, 0, 0, 0, 0, 0, 0, 0, 0, 0, 0, 0, 0, 0, 0, 0, 0, 0, 0, 0, 0, 0, 0, 0, 0, 0, 0, 0, 0, 0, 0, 0, 0, 0, 0, 0, 0, 0, 0, 0, 0, 0, 0, 0, 0, 0, 0, 0, 0, 0, 0, 0, 0, 0, 0, 0, 0, 0, 0, 0, 0, 0, 0, 0, 0, 0, 0, 0, 0, 0, 0, 0, 0, 0, 0, 0, 0, 0, 0, 0, 0, 0, 0, 0, 0, 0, 0, 0, 0, 0, 0, 0, 0, 0, 0, 0, 0, 0, 0, 0, 0, 0, 0, 0, 0, 0, 0, 0, 0, 0, 0, 0, 0, 0, 0, 0, 0, 0, 0, 0, 0, 0, 0, 0, 0, 0, 0, 0, 0, 0, 0, 0, 0, 0, 0, 0, 0, 0, 0, 0, 0, 0, 0, 0, 0, 0, 0, 0, 0, 0, 0, 0, 0, 0, 0, 0, 0, 0, 0, 16128, 0, 16256, 0, 16256, 0, 16256, 0, 16128, 0, 16256, 0, 16256, 0, 16256, 0, 16128, 0, 16256, 0, 16256, 0, 16256, 0, 16128, 0, 16256, 0, 16256, 0, 16256, 0, 16256, 0, 16256, 0, 16256, 0, 16256, 0, 16256, 0, 16256, 0, 16256, 0, 16256, 0, 16256, 0, 16256, 0, 16256, 0, 16256, 0, 16256, 0, 16256, 0, 16256, 0, 16256, 0, 16256, 0, 16256, 0, 16256, 0, 16256, 0, 16256, 0, 16256, 0, 16256, 0, 16256, 0, 16256, 0, 16256, 0, 16256, 0, 16256, 0, 16256, 0, 16256, 0, 16256, 0, 16256, 0, 16256, 0, 16256, 0, 16256, 0, 16256, 0, 16256, 0, 16256, 0, 16256, 0, 16256, 0, 16256, 0, 16256, 0, 16256, 0, 16256, 0, 16256, 0, 16256, 0, 16256, 0, 16256, 0, 16256, 0, 16256, 0, 16256, 0, 16256, 0, 16256, 0, 16256, 0, 16256, 0, 16256, 0, 16256, 0, 16256, 0, 16256, 0, 16256, 0, 16256, 0, 16256, 0, 16256, 0, 16256, 0, 16256, 0, 16256, 0, 16256, 0, 16256, 0, 16256, 0, 16256, 0, 16256, 0, 16256, 0, 16256, 0, 16256, 0, 16256, 0, 16256, 0, 16256, 0, 16256, 0, 16256, 0, 16256, 0, 16128, 0, 16256, 0, 16256, 0, 16256, 0, 16128, 0, 16256, 0, 16256, 0, 16256, 0, 16128, 0, 16256, 0, 16256, 0, 16256, 0, 16128, 0, 16256, 0, 16256, 0, 16256, 0, 16256, 0, 16256, 0, 16256, 0, 16256, 0, 16256, 0, 16256, 0, 16256, 0, 16256, 0, 16256, 0, 16256, 0, 16256, 0, 16256, 0, 16256, 0, 16256, 0, 16256, 0, 16256, 0, 16128, 0, 16256, 0, 16256, 0, 16256, 0, 16128, 0, 16256, 0, 16256, 0, 16256, 0, 16128, 0, 16256, 0, 16256, 0, 16256, 0, 16128, 0, 16256, 0, 16256, 0, 16256, 0, 16256, 0, 16256, 0, 16256, 0, 16256, 0, 16256, 0, 16256, 0, 16256, 0, 16256, 0, 16256, 0, 16256, 0, 16256, 0, 16256, 0, 16256, 0, 16256, 0, 16256, 0, 16256, 0, 16256, 0, 16256, 0, 16256, 0, 16256, 0, 16256, 0, 16256, 0, 16256, 0, 16256, 0, 16256, 0, 16256, 0, 16256, 0, 16256, 0, 16256, 0, 16256, 0, 16256, 0, 16256, 0, 0, 0, 0, 0, 0, 0, 0, 0, 0, 0, 0, 0, 0, 0, 0, 0, 0, 0, 0, 0, 0, 0, 0, 0, 0, 0, 0, 0, 0, 0, 0, 0, 0, 0, 0, 0, 0, 0, 0, 0, 0, 0, 0, 0, 0, 0, 0, 0, 0, 0, 0, 0, 0, 0, 0, 0, 0, 0, 0, 0, 0, 0, 0, 0, 0, 0, 0, 0, 0, 0, 0, 0, 0, 0, 0, 0, 0, 0, 0, 0, 0, 0, 0, 0, 0, 0, 0, 0, 0, 0, 0, 0, 0, 0, 0, 0, 0, 0, 0, 0, 0, 0, 0, 0, 0, 0, 0, 0, 0, 0, 0, 0, 0, 0, 0, 0, 0, 0, 0, 0, 0, 0, 0, 0, 0, 0, 0, 0, 16768, 0, 16768, 0, 16768, 0, 16768, 0, 16768, 0, 16768, 0, 16768, 0, 16768, 0, 16768, 0, 16768, 0, 16768, 0, 16768, 0, 16768, 0, 0, 0, 16768, 0, 0, 0, 16256, 0, 16256, 0, 16256, 0, 16256, 0, 16256, 0, 16256, 0, 16256, 0, 16256, 0, 16256, 0, 16256, 0, 16256, 0, 16256, 0, 16128, 0, 16256, 0, 16128, 0, 17280, 0, 16128, 0, 16256, 0, 16256, 0, 16256, 0, 16128, 0, 16256, 0, 16256, 0, 16256, 0, 16128, 0, 16256, 0, 16256, 0, 16256, 0, 16128, 0, 16256, 0, 16256, 0, 16256, 0, 16256, 0, 16256, 0, 16256, 0, 16256, 0, 16256, 0, 16256, 0, 16256, 0, 16256, 0, 16256, 0, 16256, 0, 16256, 0, 16256, 0, 16256, 0, 16256, 0, 16256, 0, 16256, 0, 16256, 0, 16256, 0, 16256, 0, 16256, 0, 16256, 0, 16256, 0, 16256, 0, 16256, 0, 16256, 0, 16256, 0, 16256, 0, 16256, 0, 16256, 0, 16256, 0, 16256, 0, 16256, 0, 16128, 0, 16256, 0, 16256, 0, 16256, 0, 16128, 0, 16256, 0, 16256, 0, 16256, 0, 16128, 0, 16256, 0, 16256, 0, 16256, 0, 16128, 0, 16256, 0, 16256, 0, 16256, 0, 16256, 0, 16256, 0, 16256, 0, 16256, 0, 16256, 0, 16256, 0, 16256, 0, 16256, 0, 16256, 0, 16256, 0, 16256, 0, 16256, 0, 16256, 0, 16256, 0, 16256, 0, 16256, 0, 16256, 0, 16256, 0, 16256, 0, 16256, 0, 16256, 0, 16256, 0, 16256, 0, 16256, 0, 16256, 0, 16256, 0, 16256, 0, 16256, 0, 16256, 0, 16256, 0, 16256, 0, 16256, 0, 16256, 0, 16256, 0, 16256, 0, 16256, 0, 16256, 0, 16256, 0, 16256, 0, 16256, 0, 16256, 0, 16256, 0, 16256, 0, 16256, 0, 16256, 0, 16256, 0, 16256, 0, 16256, 0, 16256, 0, 16256, 0, 16256, 0, 16256, 0, 16256, 0, 16256, 0, 16256, 0, 16256, 0, 16256, 0, 16256, 0, 16256, 0, 16256, 0, 16256, 0, 16256, 0, 16256, 0, 16256, 0, 16256, 0, 16256, 0, 16256, 0, 16256, 0, 16256, 0, 16256, 0, 16256, 0, 16256, 0, 16256, 0, 16256, 0, 16256, 0, 16256, 0, 16256, 0, 16256, 0, 16256, 0, 16256, 0, 16128, 0, 16256, 0, 16256, 0, 16256, 0, 16128, 0, 16256, 0, 16256, 0, 16256, 0, 16128, 0, 16256, 0, 16256, 0, 16256, 0, 16128, 0, 16256, 0, 16256, 0, 16256, 0, 0, 0, 0, 0, 0, 0, 0, 0, 0, 0, 0, 0, 0, 0, 0, 0, 0, 0, 0, 0, 0, 0, 0, 0, 0, 0, 0, 0, 0, 0, 0, 0, 0, 0, 0, 0, 0, 0, 0, 0, 0, 0, 0, 0, 0, 0, 0, 0, 0, 0, 0, 0, 0, 0, 0, 0, 0, 0, 0, 0, 0, 0, 0, 0, 0, 0, 0, 0, 0, 0, 0, 0, 0, 0, 0, 0, 0, 0, 0, 0, 0, 0, 0, 0, 0, 0, 0, 0, 0, 0, 0, 0, 0, 0, 0, 0, 0, 0, 0, 0, 0, 0, 0, 0, 0, 0, 0, 0, 0, 0, 0, 0, 0, 0, 0, 0, 0, 0, 0, 0, 0, 0, 0, 0, 0, 0, 0, 0, 16768, 0, 16768, 0, 16768, 0, 16768, 0, 16768, 0, 16768, 0, 16768, 0, 16768, 0, 16768, 0, 16768, 0, 16768, 0, 16768, 0, 0, 0, 0, 0, 0, 0, 0, 0, 16128, 0, 16256, 0, 16256, 0, 16256, 0, 16128, 0, 16256, 0, 16256, 0, 16256, 0, 16128, 0, 16256, 0, 16256, 0, 16256, 0, 16128, 0, 16256, 0, 16256, 0, 16256, 0, 16256, 0, 16256, 0, 16256, 0, 16256, 0, 16256, 0, 16256, 0, 16256, 0, 16256, 0, 16256, 0, 16256, 0, 16256, 0, 16256, 0, 16256, 0, 16256, 0, 16256, 0, 16256, 0, 16256, 0, 16256, 0, 16256, 0, 16256, 0, 16256, 0, 16256, 0, 16256, 0, 16256, 0, 16256, 0, 16256, 0, 16256, 0, 16256, 0, 16256, 0, 16256, 0, 16256, 0, 16256, 0, 16256, 0, 16256, 0, 16256, 0, 16256, 0, 16256, 0, 16256, 0, 16256, 0, 16256, 0, 16256, 0, 16256, 0, 16256, 0, 16256, 0, 16256, 0, 16256, 0, 16256, 0, 16256, 0, 16256, 0, 16256, 0, 16256, 0, 16256, 0, 16256, 0, 16256, 0, 16256, 0, 16256, 0, 16256, 0, 16256, 0, 16256, 0, 16256, 0, 16256, 0, 16256, 0, 16256, 0, 16256, 0, 16256, 0, 16256, 0, 16256, 0, 16256, 0, 16256, 0, 16256, 0, 16256, 0, 16256, 0, 16256, 0, 16256, 0, 16256, 0, 16256, 0, 16256, 0, 16256, 0, 16256, 0, 16256, 0, 16128, 0, 16256, 0, 16256, 0, 16256, 0, 16128, 0, 16256, 0, 16256, 0, 16256, 0, 16128, 0, 16256, 0, 16256, 0, 16256, 0, 16128, 0, 16256, 0, 16256, 0, 16256, 0, 16256, 0, 16256, 0, 16256, 0, 16256, 0, 16256, 0, 16256, 0, 16256, 0, 16256, 0, 16256, 0, 16256, 0, 16256, 0, 16256, 0, 16256, 0, 16256, 0, 16256, 0, 16256, 0, 16128, 0, 16256, 0, 16256, 0, 16256, 0, 16128, 0, 16256, 0, 16256, 0, 16256, 0, 16128, 0, 16256, 0, 16256, 0, 16256, 0, 16128, 0, 16256, 0, 16256, 0, 16256, 0, 16256, 0, 16256, 0, 16256, 0, 16256, 0, 16256, 0, 16256, 0, 16256, 0, 16256, 0, 16256, 0, 16256, 0, 16256, 0, 16256, 0, 16256, 0, 16256, 0, 16256, 0, 16256, 0, 16256, 0, 16256, 0, 16256, 0, 16256, 0, 16256, 0, 16256, 0, 16256, 0, 16256, 0, 16256, 0, 16256, 0, 16256, 0, 16256, 0, 16256, 0, 16256, 0, 16256, 0, 16256, 0, 0, 0, 0, 0, 0, 0, 0, 0, 0, 0, 0, 0, 0, 0, 0, 0, 0, 0, 0, 0, 0, 0, 0, 0, 0, 0, 0, 0, 0, 0, 0, 0, 0, 0, 0, 0, 0, 0, 0, 0, 0, 0, 0, 0, 0, 0, 0, 0, 0, 0, 0, 0, 0, 0, 0, 0, 0, 0, 0, 0, 0, 0, 0, 0, 0, 0, 0, 0, 0, 0, 0, 0, 0, 0, 0, 0, 0, 0, 0, 0, 0, 0, 0, 0, 0, 0, 0, 0, 0, 0, 0, 0, 0, 0, 0, 0, 0, 0, 0, 0, 0, 0, 0, 0, 0, 0, 0, 0, 0, 0, 0, 0, 0, 0, 0, 0, 0, 0, 0, 0, 0, 0, 0, 0, 0, 0, 0, 0, 0, 0, 0, 0, 0, 0, 0, 0, 0, 0, 0, 0, 0, 0, 0, 0, 16256, 0, 16256, 0, 16128, 0, 16256, 0, 16256, 0, 16256, 0, 16128, 0, 16256, 0, 0, 0, 0, 0, 0, 0, 0, 0, 0, 0, 0, 0, 0, 0, 0, 0, 16256, 0, 16256, 0, 16256, 0, 16256, 0, 16256, 0, 16256, 0, 16256, 0, 16256, 0, 16128, 0, 16256, 0, 16256, 0, 16256, 0, 16128, 0, 16256, 0, 16256, 0, 16256, 0, 16256, 0, 16256, 0, 16256, 0, 16256, 0, 16256, 0, 16256, 0, 16256, 0, 16256, 0, 16256, 0, 16256, 0, 16256, 0, 16256, 0, 16256, 0, 16256, 0, 16256, 0, 16256, 0, 16256, 0, 16256, 0, 16256, 0, 16256, 0, 16256, 0, 16256, 0, 16256, 0, 16256, 0, 16256, 0, 16256, 0, 16256, 0, 16256, 0, 16256, 0, 16256, 0, 16256, 0, 16256, 0, 16128, 0, 16256, 0, 16256, 0, 16256, 0, 16128, 0, 16256, 0, 16256, 0, 16256, 0, 16256, 0, 16256, 0, 16256, 0, 16256, 0, 16256, 0, 16256, 0, 16256, 0, 16256, 0, 16256, 0, 16256, 0, 16256, 0, 16256, 0, 16256, 0, 16256, 0, 16256, 0, 16256, 0, 16256, 0, 16256, 0, 16256, 0, 16256, 0, 16256, 0, 16256, 0, 16256, 0, 16256, 0, 16256, 0, 16256, 0, 16128, 0, 16256, 0, 16256, 0, 16256, 0, 16128, 0, 16256, 0, 16256, 0, 16256, 0, 16256, 0, 16256, 0, 16256, 0, 16256, 0, 16256, 0, 16256, 0, 16256, 0, 16256, 0, 16256, 0, 16256, 0, 16256, 0, 16256, 0, 16256, 0, 16256, 0, 16256, 0, 16256, 0, 16256, 0, 16256, 0, 16256, 0, 16256, 0, 16256, 0, 16256, 0, 16256, 0, 16256, 0, 16128, 0, 16256, 0, 16256, 0, 16256, 0, 16128, 0, 16256, 0, 16256, 0, 16256, 0, 16256, 0, 16256, 0, 16256, 0, 16256, 0, 16256, 0, 16256, 0, 16256, 0, 16256, 0, 16256, 0, 16256, 0, 16256, 0, 16256, 0, 16256, 0, 16256, 0, 16256, 0, 16256, 0, 16256, 0, 16256, 0, 16256, 0, 16256, 0, 16256, 0, 16256, 0, 16128, 0, 16256, 0, 16256, 0, 16256, 0, 16128, 0, 16256, 0, 16256, 0, 16256, 0, 16256, 0, 16256, 0, 16256, 0, 16256, 0, 16256, 0, 16256, 0, 16256, 0, 16256, 0, 16256, 0, 16256, 0, 16256, 0, 16256, 0, 16256, 0, 16256, 0, 16256, 0, 16256, 0, 0, 0, 0, 0, 0, 0, 0, 0, 0, 0, 0, 0, 0, 0, 0, 0, 0, 0, 0, 0, 0, 0, 0, 0, 0, 0, 0, 0, 0, 0, 0, 0, 0, 0, 0, 0, 0, 0, 0, 0, 0, 0, 0, 0, 0, 0, 0, 0, 0, 0, 0, 0, 0, 0, 0, 0, 0, 0, 0, 0, 0, 0, 0, 0, 0, 0, 0, 0, 0, 0, 0, 0, 0, 0, 0, 0, 0, 0, 0, 0, 0, 0, 0, 0, 0, 0, 0, 0, 0, 0, 0, 0, 0, 0, 0, 0, 0, 0, 0, 0, 0, 0, 0, 0, 0, 0, 0, 0, 0, 0, 0, 0, 0, 0, 0, 0, 0, 0, 0, 0, 0, 0, 0, 0, 0, 0, 0, 0, 0, 0, 0, 0, 0, 0, 0, 0, 0, 0, 0, 0, 0, 0, 0, 0, 16256, 0, 16256, 0, 16128, 0, 16256, 0, 16128, 0, 16256, 0, 16128, 0, 16256, 0, 0, 0, 0, 0, 0, 0, 0, 0, 0, 0, 0, 0, 0, 0, 0, 0, 16256, 0, 16256, 0, 16256, 0, 16256, 0, 16128, 0, 0, 0, 16256, 0, 16256, 0, 16128, 0, 16256, 0, 16256, 0, 16256, 0, 16128, 0, 16256, 0, 16256, 0, 16256, 0, 16256, 0, 16256, 0, 16256, 0, 16256, 0, 16256, 0, 16256, 0, 16256, 0, 16256, 0, 16256, 0, 16256, 0, 16256, 0, 16256, 0, 16256, 0, 16256, 0, 16256, 0, 16256, 0, 16256, 0, 16256, 0, 16256, 0, 16256, 0, 16256, 0, 16256, 0, 16256, 0, 16256, 0, 16256, 0, 16256, 0, 16256, 0, 16256, 0, 16256, 0, 16256, 0, 16256, 0, 16256, 0, 16128, 0, 16256, 0, 16256, 0, 16256, 0, 16128, 0, 16256, 0, 16256, 0, 16256, 0, 16256, 0, 16256, 0, 16256, 0, 16256, 0, 16256, 0, 16256, 0, 16256, 0, 16256, 0, 16256, 0, 16256, 0, 16256, 0, 16256, 0, 16256, 0, 16256, 0, 16256, 0, 16256, 0, 16256, 0, 16256, 0, 16256, 0, 16256, 0, 16256, 0, 16256, 0, 16256, 0, 16256, 0, 16256, 0, 16256, 0, 16128, 0, 16256, 0, 16256, 0, 16256, 0, 16128, 0, 16256, 0, 16256, 0, 16256, 0, 16256, 0, 16256, 0, 16256, 0, 16256, 0, 16256, 0, 16256, 0, 16256, 0, 16256, 0, 16256, 0, 16256, 0, 16256, 0, 16256, 0, 16256, 0, 16256, 0, 16256, 0, 16256, 0, 16256, 0, 16256, 0, 16256, 0, 16256, 0, 16256, 0, 16256, 0, 16256, 0, 16256, 0, 16128, 0, 16256, 0, 16256, 0, 16256, 0, 16128, 0, 16256, 0, 16256, 0, 16256, 0, 16256, 0, 16256, 0, 16256, 0, 16256, 0, 16256, 0, 16256, 0, 16256, 0, 16256, 0, 16256, 0, 16256, 0, 16256, 0, 16256, 0, 16256, 0, 16256, 0, 16256, 0, 16256, 0, 16256, 0, 16256, 0, 16256, 0, 16256, 0, 16256, 0, 16256, 0, 16128, 0, 16256, 0, 16256, 0, 16256, 0, 16128, 0, 16256, 0, 16256, 0, 16256, 0, 16256, 0, 16256, 0, 16256, 0, 16256, 0, 16256, 0, 16256, 0, 16256, 0, 16256, 0, 16256, 0, 16256, 0, 16256, 0, 16256, 0, 16256, 0, 16256, 0, 16256, 0, 16256, 0, 0, 0, 0, 0, 0, 0, 0, 0, 0, 0, 0, 0, 0, 0, 0, 0, 0, 0, 0, 0, 0, 0, 0, 0, 0, 0, 0, 0, 0, 0, 0, 0, 0, 0, 0, 0, 0, 0, 0, 0, 0, 0, 0, 0, 0, 0, 0, 0, 0, 0, 0, 0, 0, 0, 0, 0, 0, 0, 0, 0, 0, 0, 0, 0, 0, 0, 0, 0, 0, 0, 0, 0, 0, 0, 0, 0, 0, 0, 0, 0, 0, 0, 0, 0, 0, 0, 0, 0, 0, 0, 0, 0, 0, 0, 0, 0, 0, 0, 0, 0, 0, 0, 0, 0, 0, 0, 0, 0, 0, 0, 0, 0, 0, 0, 0, 0, 0, 0, 0, 0, 0, 0, 0, 0, 0, 0, 0, 0, 16768, 0, 16768, 0, 16768, 0, 16768, 0, 16768, 0, 16768, 0, 16768, 0, 16768, 0, 16256, 0, 16256, 0, 16128, 0, 16256, 0, 16256, 0, 16256, 0, 16128, 0, 16256, 0, 16768, 0, 16768, 0, 16768, 0, 16768, 0, 16768, 0, 16768, 0, 16768, 0, 16768, 0, 16256, 0, 16256, 0, 16256, 0, 16256, 0, 16256, 0, 16256, 0, 16256, 0, 16256, 0, 16128, 0, 16256, 0, 16256, 0, 16256, 0, 16128, 0, 16256, 0, 16256, 0, 16256, 0, 16256, 0, 16256, 0, 16256, 0, 16256, 0, 16256, 0, 16256, 0, 16256, 0, 16256, 0, 16256, 0, 16256, 0, 16256, 0, 16256, 0, 16256, 0, 16256, 0, 16256, 0, 16256, 0, 16256, 0, 16256, 0, 16256, 0, 16256, 0, 16256, 0, 16256, 0, 16256, 0, 16256, 0, 16256, 0, 16256, 0, 16256, 0, 16256, 0, 16256, 0, 16256, 0, 16256, 0, 16256, 0, 16128, 0, 16256, 0, 16256, 0, 16256, 0, 16128, 0, 16256, 0, 16256, 0, 16256, 0, 16256, 0, 16256, 0, 16256, 0, 16256, 0, 16256, 0, 16256, 0, 16256, 0, 16256, 0, 16256, 0, 16256, 0, 16256, 0, 16256, 0, 16256, 0, 16256, 0, 16256, 0, 16256, 0, 16256, 0, 16256, 0, 16256, 0, 16256, 0, 16256, 0, 16256, 0, 16256, 0, 16256, 0, 16256, 0, 16256, 0, 16128, 0, 16256, 0, 16256, 0, 16256, 0, 16128, 0, 16256, 0, 16256, 0, 16256, 0, 16256, 0, 16256, 0, 16256, 0, 16256, 0, 16256, 0, 16256, 0, 16256, 0, 16256, 0, 16256, 0, 16256, 0, 16256, 0, 16256, 0, 16256, 0, 16256, 0, 16256, 0, 16256, 0, 16256, 0, 16256, 0, 16256, 0, 16256, 0, 16256, 0, 16256, 0, 16256, 0, 16256, 0, 16128, 0, 16256, 0, 16256, 0, 16256, 0, 16128, 0, 16256, 0, 16256, 0, 16256, 0, 16256, 0, 16256, 0, 16256, 0, 16256, 0, 16256, 0, 16256, 0, 16256, 0, 16256, 0, 16256, 0, 16256, 0, 16256, 0, 16256, 0, 16256, 0, 16256, 0, 16256, 0, 16256, 0, 16256, 0, 16256, 0, 16256, 0, 16256, 0, 16256, 0, 16256, 0, 16128, 0, 16256, 0, 16256, 0, 16256, 0, 16128, 0, 16256, 0, 16256, 0, 16256, 0, 16256, 0, 16256, 0, 16256, 0, 16256, 0, 16256, 0, 16256, 0, 16256, 0, 16256, 0, 16256, 0, 16256, 0, 16256, 0, 16256, 0, 16256, 0, 16256, 0, 16256, 0, 16256, 0, 0, 0, 0, 0, 0, 0, 0, 0, 0, 0, 0, 0, 0, 0, 0, 0, 0, 0, 0, 0, 0, 0, 0, 0, 0, 0, 0, 0, 0, 0, 0, 0, 0, 0, 0, 0, 0, 0, 0, 0, 0, 0, 0, 0, 0, 0, 0, 0, 0, 0, 0, 0, 0, 0, 0, 0, 0, 0, 0, 0, 0, 0, 0, 0, 0, 0, 0, 0, 0, 0, 0, 0, 0, 0, 0, 0, 0, 0, 0, 0, 0, 0, 0, 0, 0, 0, 0, 0, 0, 0, 0, 0, 0, 0, 0, 0, 0, 0, 0, 0, 0, 0, 0, 0, 0, 0, 0, 0, 0, 0, 0, 0, 0, 0, 0, 0, 0, 0, 0, 0, 0, 0, 0, 0, 0, 0, 0, 0, 16768, 0, 16768, 0, 16768, 0, 16768, 0, 16768, 0, 0, 0, 0, 0, 0, 0, 16256, 0, 16256, 0, 16128, 0, 16256, 0, 16128, 0, 16256, 0, 16128, 0, 16256, 0, 16768, 0, 16768, 0, 16768, 0, 16768, 0, 16768, 0, 0, 0, 0, 0, 0, 0, 16256, 0, 16256, 0, 16256, 0, 16256, 0, 16128, 0, 17280, 0, 16256, 0, 16256, 0, 16128, 0, 16256, 0, 16256, 0, 16256, 0, 16128, 0, 16256, 0, 16256, 0, 16256, 0, 16256, 0, 16256, 0, 16256, 0, 16256, 0, 16256, 0, 16256, 0, 16256, 0, 16256, 0, 16256, 0, 16256, 0, 16256, 0, 16256, 0, 16256, 0, 16256, 0, 16256, 0, 16256, 0, 16256, 0, 16256, 0, 16256, 0, 16256, 0, 16256, 0, 16256, 0, 16256, 0, 16256, 0, 16256, 0, 16256, 0, 16256, 0, 16256, 0, 16256, 0, 16256, 0, 16256, 0, 16256, 0, 16128, 0, 16256, 0, 16256, 0, 16256, 0, 16128, 0, 16256, 0, 16256, 0, 16256, 0, 16256, 0, 16256, 0, 16256, 0, 16256, 0, 16256, 0, 16256, 0, 16256, 0, 16256, 0, 16256, 0, 16256, 0, 16256, 0, 16256, 0, 16256, 0, 16256, 0, 16256, 0, 16256, 0, 16256, 0, 16256, 0, 16256, 0, 16256, 0, 16256, 0, 16256, 0, 16256, 0, 16256, 0, 16256, 0, 16256, 0, 16128, 0, 16256, 0, 16256, 0, 16256, 0, 16128, 0, 16256, 0, 16256, 0, 16256, 0, 16256, 0, 16256, 0, 16256, 0, 16256, 0, 16256, 0, 16256, 0, 16256, 0, 16256, 0, 16256, 0, 16256, 0, 16256, 0, 16256, 0, 16256, 0, 16256, 0, 16256, 0, 16256, 0, 16256, 0, 16256, 0, 16256, 0, 16256, 0, 16256, 0, 16256, 0, 16256, 0, 16256, 0, 16128, 0, 16256, 0, 16256, 0, 16256, 0, 16128, 0, 16256, 0, 16256, 0, 16256, 0, 16256, 0, 16256, 0, 16256, 0, 16256, 0, 16256, 0, 16256, 0, 16256, 0, 16256, 0, 16256, 0, 16256, 0, 16256, 0, 16256, 0, 16256, 0, 16256, 0, 16256, 0, 16256, 0, 16256, 0, 16256, 0, 16256, 0, 16256, 0, 16256, 0, 16256, 0, 16128, 0, 16256, 0, 16256, 0, 16256, 0, 16128, 0, 16256, 0, 16256, 0, 16256, 0, 16256, 0, 16256, 0, 16256, 0, 16256, 0, 16256, 0, 16256, 0, 16256, 0, 16256, 0, 16256, 0, 16256, 0, 16256, 0, 16256, 0, 16256, 0, 16256, 0, 16256, 0, 16256, 0, 0, 0, 0, 0, 0, 0, 0, 0, 0, 0, 0, 0, 0, 0, 0, 0, 0, 0, 0, 0, 0, 0, 0, 0, 0, 0, 0, 0, 0, 0, 0, 0, 0, 0, 0, 0, 0, 0, 0, 0, 0, 0, 0, 0, 0, 0, 0, 0, 0, 0, 0, 0, 0, 0, 0, 0, 0, 0, 0, 0, 0, 0, 0, 0, 0, 0, 0, 0, 0, 0, 0, 0, 0, 0, 0, 0, 0, 0, 0, 0, 0, 0, 0, 0, 0, 0, 0, 0, 0, 0, 0, 0, 0, 0, 0, 0, 0, 0, 0, 0, 0, 0, 0, 0, 0, 0, 0, 0, 0, 0, 0, 0, 0, 0, 0, 0, 0, 0, 0, 0, 0, 0, 0, 0, 0, 0, 0};

__global__ __launch_bounds__(512) void xd_kernel(const int* idx, const u16* w1, const u16* table, const u16* b1, const u16* ws_cin, const u16* wout, char* ws, const u16* w2, const u16* b_out, u16* out) {
  __shared__ __attribute__((aligned(1024))) char shm[163840];
  unsigned tid = threadIdx.x;
  asm volatile(
    "s_load_dwordx16 s[4:19], s[0:1], 0\n"
    ".Lxd_top:\n"
    "v_readfirstlane_b32 s43, v0\n"
    "s_setprio 3\n"
    "s_cmp_lt_u32 s2, 48\n"
    "s_cbranch_scc1 .Lxd_cin\n"
    "s_and_b32 s24, s2, 15\n"
    "s_mul_i32 s40, s43, 36\n"
    "s_mul_i32 s41, s24, 26624\n"
    "s_add_u32 s38, s40, s41\n"
    "v_lshlrev_b32 v86, 4, v0\n"
    "s_lshl_b32 s34, s43, 6\n"
    "s_add_u32 m0, s34, 98304\n"
    "s_waitcnt lgkmcnt(0)\n"
    "s_add_u32 s38, s4, s38\n"
    "s_addc_u32 s39, s5, 0\n"
    "global_load_lds_dwordx4 v86, s[38:39] offset:0\n"
    "global_load_lds_dwordx4 v86, s[38:39] offset:1024\n"
    "global_load_lds_dwordx4 v86, s[38:39] offset:2048\n"
    "global_load_lds_dwordx4 v86, s[38:39] offset:2304\n"
    "s_sub_u32 s3, s2, 48\n"
    "s_lshl_b32 s40, s2, 2\n"
    "s_lshr_b32 s23, s3, 4\n"
    "s_lshr_b32 s40, 0xa8864220, s40\n"
    "s_and_b32 s42, s40, 14\n"
    "v_bfe_u32 v85, v0, 4, 2\n"
    "v_lshlrev_b32 v87, 1, v0\n"
    "v_and_b32 v87, 0xe0, v87\n"
    "v_and_b32 v88, 0xf0, v86\n"
    "v_xor_b32 v87, v87, v88\n"
    "s_movk_i32 s41, 3328\n"
    "v_mad_u32_u24 v46, v85, s41, v87\n"
    "v_add_u32 v47, 12288, v46\n"
    "s_mul_i32 s41, s43, 416\n"
    "s_lshl_b32 s40, s23, 8\n"
    "s_add_u32 s41, s41, s40\n"
    "s_mul_i32 s30, s42, 425984\n"
    "s_lshl_b32 s35, s43, 5\n"
    "s_bitset1_b32 s35, 15\n"
    "s_add_u32 s6, s6, s41\n"
    "s_addc_u32 s7, s7, 0\n"
    "s_add_u32 s30, s6, s30\n"
    "s_addc_u32 s31, s7, 0\n"
    "s_add_u32 m0, s35, 0\n"
    "s_nop 0\n"
    "global_load_lds_dwordx4 v46, s[30:31] offset:0\n"
    "global_load_lds_dwordx4 v47, s[30:31] offset:1024\n"
    "s_add_u32 s30, s30, 212992\n"
    "s_addc_u32 s31, s31, 0\n"
    "s_setprio 2\n"
    "v_and_b32 v84, 3, v0\n"
    "v_bfe_u32 v44, v0, 3, 3\n"
    "v_lshl_add_u32 v44, v84, 3, v44\n"
    "s_add_u32 s40, s34, 98304\n"
    "v_mul_u32_u24 v44, 104, v44\n"
    "v_add_u32 v44, s40, v44\n"
    "v_lshl_add_u32 v45, s42, 3, v44\n"
    "v_xor_b32 v51, v0, v86\n"
    "v_and_b32 v86, 0x70, v86\n"
    "v_bfi_b32 v86, 48, v51, v86\n"
    "v_xor_b32 v51, 64, v86\n"
    "v_add_u32 v48, 3072, v86\n"
    "v_add_u32 v49, 2048, v51\n"
    "v_add_u32 v50, 1024, v86\n"
    "s_sub_u32 s36, s8, 3072\n"
    "s_subb_u32 s37, s9, 0\n"
    "s_add_u32 m0, s35, 49152\n"
    "s_nop 0\n"
    "global_load_lds_dwordx4 v46, s[30:31] offset:0\n"
    "global_load_lds_dwordx4 v47, s[30:31] offset:1024\n"
    "s_add_u32 s30, s30, 212992\n"
    "s_addc_u32 s31, s31, 0\n"
    "s_add_u32 m0, s35, 98304\n"
    "s_nop 0\n"
    "global_load_lds_dwordx4 v46, s[30:31] offset:0\n"
    "global_load_lds_dwordx4 v47, s[30:31] offset:1024\n"
    "s_add_u32 s30, s30, 212992\n"
    "s_addc_u32 s31, s31, 0\n"
    "s_waitcnt vmcnt(6)\n"
    "ds_read2_b64 v[96:99], v45 offset1:1\n"
    "s_lshr_b32 s20, s43, 6\n"
    "s_lshr_b32 s21, s43, 8\n"
    "v_and_b32 v1, 63, v0\n"
    "s_waitcnt lgkmcnt(0)\n"
    "v_lshlrev_b32 v96, 7, v96\n"
    "v_lshlrev_b32 v97, 7, v97\n"
    "v_lshlrev_b32 v98, 7, v98\n"
    "v_add_u32_dpp v52, v96, v48 quad_perm:[0,0,0,0] row_mask:0xf bank_mask:0xf\n"
    "v_add_u32_dpp v53, v96, v49 quad_perm:[1,1,1,1] row_mask:0xf bank_mask:0xf\n"
    "v_add_u32_dpp v54, v96, v50 quad_perm:[2,2,2,2] row_mask:0xf bank_mask:0xf\n"
    "v_add_u32_dpp v55, v96, v51 quad_perm:[3,3,3,3] row_mask:0xf bank_mask:0xf\n"
    "s_add_u32 m0, s34, 0\n"
    "s_nop 0\n"
    "global_load_lds_dwordx4 v52, s[36:37] offset:0\n"
    "global_load_lds_dwordx4 v53, s[36:37] offset:1024\n"
    "global_load_lds_dwordx4 v54, s[36:37] offset:2048\n"
    "global_load_lds_dwordx4 v55, s[36:37] offset:3072\n"
    "s_setprio 1\n"
    "v_add_u32_dpp v52, v97, v48 quad_perm:[0,0,0,0] row_mask:0xf bank_mask:0xf\n"
    "v_add_u32_dpp v53, v97, v49 quad_perm:[1,1,1,1] row_mask:0xf bank_mask:0xf\n"
    "v_add_u32_dpp v54, v97, v50 quad_perm:[2,2,2,2] row_mask:0xf bank_mask:0xf\n"
    "v_add_u32_dpp v55, v97, v51 quad_perm:[3,3,3,3] row_mask:0xf bank_mask:0xf\n"
    "s_load_dwordx4 s[68:71], s[0:1], 64\n"
    "s_lshl2_add_u32 s41, s42, 8\n"
    "s_mov_b32 s28, 0x30201000\n"
    "s_movk_i32 s29, 0x5040\n"
    "s_lshr_b64 s[42:43], s[28:29], s41\n"
    "s_sub_u32 s41, 48, s41\n"
    "s_lshl_b64 s[28:29], s[28:29], s41\n"
    "s_or_b64 s[28:29], s[28:29], s[42:43]\n"
    "s_and_b32 s29, s29, 0xff\n"
    "s_or_b32 s29, s29, 0x6000\n"
    "s_bfe_u32 s40, s28, 0x80000\n"
    "v_add_u32 v45, s40, v44\n"
    "ds_read2_b64 v[100:103], v45 offset1:1\n"
    "s_bfe_u32 s40, s28, 0x80008\n"
    "v_add_u32 v45, s40, v44\n"
    "ds_read2_b64 v[104:107], v45 offset1:1\n"
    "s_bfe_u32 s40, s28, 0x80010\n"
    "v_add_u32 v45, s40, v44\n"
    "ds_read2_b64 v[108:111], v45 offset1:1\n"
    "s_bfe_u32 s40, s28, 0x80018\n"
    "v_add_u32 v45, s40, v44\n"
    "ds_read2_b64 v[112:115], v45 offset1:1\n"
    "s_bfe_u32 s40, s29, 0x80000\n"
    "v_add_u32 v45, s40, v44\n"
    "ds_read2_b64 v[116:119], v45 offset1:1\n"
    "s_bfe_u32 s40, s29, 0x80008\n"
    "v_add_u32 v45, s40, v44\n"
    "ds_read_b64 v[120:121], v45\n"
    "v_lshrrev_b32 v86, 5, v88\n"
    "v_xor_b32 v86, v86, v85\n"
    "v_lshlrev_b32 v86, 4, v86\n"
    "v_lshl_add_u32 v86, v88, 3, v86\n"
    "s_and_b32 s41, s20, 1\n"
    "s_lshl1_add_u32 s40, s21, s41\n"
    "v_lshl_add_u32 v56, s40, 13, v86\n"
    "v_xor_b32 v57, 64, v56\n"
    "v_lshrrev_b32 v86, 6, v88\n"
    "v_lshl_add_u32 v88, v85, 3, v86\n"
    "v_bfe_u32 v86, v0, 2, 3\n"
    "s_and_b32 s40, s20, 2\n"
    "s_lshl1_add_u32 s40, s40, 1024\n"
    "v_xor_b32 v86, s40, v86\n"
    "v_lshlrev_b32 v86, 5, v86\n"
    "v_lshl_add_u32 v86, v88, 8, v86\n"
    "v_lshl_add_u32 v76, v84, 3, v86\n"
    "v_xor_b32 v77, 32, v76\n"
    "v_xor_b32 v78, 64, v76\n"
    "v_xor_b32 v79, 96, v76\n"
    "v_lshlrev_b32 v92, 2, v0\n"
    "v_add_u32_e64 v93, s21, -1\n"
    "v_add_u32 v92, 149504, v92\n"
    "ds_write_b32 v92, v93\n"
    "v_lshlrev_b32 v99, 7, v99\n"
    "s_setprio 0\n"
    "s_cmp_eq_u32 s21, 0\n"
    "s_cbranch_scc1 .Lxd_g0\n"
    "s_waitcnt vmcnt(8)\n"
    "s_barrier\n"
    ".Lxd_g0:\n"
    "s_add_u32 m0, s34, 49152\n"
    "s_nop 0\n"
    "global_load_lds_dwordx4 v52, s[36:37] offset:0\n"
    "global_load_lds_dwordx4 v53, s[36:37] offset:1024\n"
    "global_load_lds_dwordx4 v54, s[36:37] offset:2048\n"
    "global_load_lds_dwordx4 v55, s[36:37] offset:3072\n"
    "v_add_u32_dpp v52, v98, v48 quad_perm:[0,0,0,0] row_mask:0xf bank_mask:0xf\n"
    "v_add_u32_dpp v53, v98, v49 quad_perm:[1,1,1,1] row_mask:0xf bank_mask:0xf\n"
    "v_add_u32_dpp v54, v98, v50 quad_perm:[2,2,2,2] row_mask:0xf bank_mask:0xf\n"
    "v_add_u32_dpp v55, v98, v51 quad_perm:[3,3,3,3] row_mask:0xf bank_mask:0xf\n"
    "s_waitcnt vmcnt(4) lgkmcnt(0)\n"
    "s_barrier\n"
    "ds_read_b128 v[128:131], v56 offset:0\n"
    "ds_read_b128 v[132:135], v56 offset:2048\n"
    "ds_read_b128 v[136:139], v56 offset:4096\n"
    "ds_read_b128 v[140:143], v56 offset:6144\n"
    "ds_read_b64_tr_b16 v[144:145], v76 offset:0\n"
    "ds_read_b64_tr_b16 v[146:147], v76 offset:1024\n"
    "ds_read_b64_tr_b16 v[148:149], v77 offset:0\n"
    "ds_read_b64_tr_b16 v[150:151], v77 offset:1024\n"
    "ds_read_b64_tr_b16 v[152:153], v78 offset:0\n"
    "ds_read_b64_tr_b16 v[154:155], v78 offset:1024\n"
    "ds_read_b64_tr_b16 v[156:157], v79 offset:0\n"
    "ds_read_b64_tr_b16 v[158:159], v79 offset:1024\n"
    "ds_read_b128 v[160:163], v57 offset:0\n"
    "ds_read_b128 v[164:167], v57 offset:2048\n"
    "ds_read_b128 v[168:171], v57 offset:4096\n"
    "ds_read_b128 v[172:175], v57 offset:6144\n"
    "ds_read_b64_tr_b16 v[176:177], v76 offset:8192\n"
    "ds_read_b64_tr_b16 v[178:179], v76 offset:9216\n"
    "ds_read_b64_tr_b16 v[180:181], v77 offset:8192\n"
    "ds_read_b64_tr_b16 v[182:183], v77 offset:9216\n"
    "ds_read_b64_tr_b16 v[184:185], v78 offset:8192\n"
    "ds_read_b64_tr_b16 v[186:187], v78 offset:9216\n"
    "ds_read_b64_tr_b16 v[188:189], v79 offset:8192\n"
    "ds_read_b64_tr_b16 v[190:191], v79 offset:9216\n"
    "s_add_u32 m0, s34, 98304\n"
    "s_nop 0\n"
    "global_load_lds_dwordx4 v52, s[36:37] offset:0\n"
    "global_load_lds_dwordx4 v53, s[36:37] offset:1024\n"
    "global_load_lds_dwordx4 v54, s[36:37] offset:2048\n"
    "global_load_lds_dwordx4 v55, s[36:37] offset:3072\n"
    "s_waitcnt lgkmcnt(0)\n"
    "s_barrier\n"
    "v_mfma_f32_16x16x32_bf16 v[192:195], v[144:147], v[128:131], 0\n"
    "v_lshlrev_b32 v100, 7, v100\n"
    "v_mfma_f32_16x16x32_bf16 v[192:195], v[176:179], v[160:163], v[192:195]\n"
    "v_add_u32_dpp v52, v99, v48 quad_perm:[0,0,0,0] row_mask:0xf bank_mask:0xf\n"
    "v_mfma_f32_16x16x32_bf16 v[196:199], v[176:179], v[164:167], 0\n"
    "v_mfma_f32_16x16x32_bf16 v[196:199], v[144:147], v[132:135], v[196:199]\n"
    "v_add_u32_dpp v53, v99, v49 quad_perm:[1,1,1,1] row_mask:0xf bank_mask:0xf\n"
    "v_mfma_f32_16x16x32_bf16 v[200:203], v[144:147], v[136:139], 0\n"
    "v_add_u32_dpp v54, v99, v50 quad_perm:[2,2,2,2] row_mask:0xf bank_mask:0xf\n"
    "v_mfma_f32_16x16x32_bf16 v[200:203], v[176:179], v[168:171], v[200:203]\n"
    "v_mfma_f32_16x16x32_bf16 v[204:207], v[176:179], v[172:175], 0\n"
    "v_add_u32_dpp v55, v99, v51 quad_perm:[3,3,3,3] row_mask:0xf bank_mask:0xf\n"
    "v_mfma_f32_16x16x32_bf16 v[204:207], v[144:147], v[140:143], v[204:207]\n"
    "s_and_b32 s40, s20, 2\n"
    "v_mfma_f32_16x16x32_bf16 v[220:223], v[148:151], v[140:143], 0\n"
    "v_mfma_f32_16x16x32_bf16 v[220:223], v[180:183], v[172:175], v[220:223]\n"
    "s_lshl2_add_u32 s40, s23, s40\n"
    "v_mfma_f32_16x16x32_bf16 v[216:219], v[180:183], v[168:171], 0\n"
    "s_lshl_b32 s40, s40, 6\n"
    "v_mfma_f32_16x16x32_bf16 v[216:219], v[148:151], v[136:139], v[216:219]\n"
    "v_mfma_f32_16x16x32_bf16 v[212:215], v[148:151], v[132:135], 0\n"
    "v_and_b32 v43, 31, v0\n"
    "v_mfma_f32_16x16x32_bf16 v[212:215], v[180:183], v[164:167], v[212:215]\n"
    "s_lshl_b32 s64, s20, 8\n"
    "v_mfma_f32_16x16x32_bf16 v[208:211], v[180:183], v[160:163], 0\n"
    "v_mfma_f32_16x16x32_bf16 v[208:211], v[148:151], v[128:131], v[208:211]\n"
    "s_add_u32 s64, s64, 147456\n"
    "v_mfma_f32_16x16x32_bf16 v[224:227], v[152:155], v[128:131], 0\n"
    "v_mfma_f32_16x16x32_bf16 v[224:227], v[184:187], v[160:163], v[224:227]\n"
    "v_lshl_add_u32 v43, v43, 2, s40\n"
    "v_mfma_f32_16x16x32_bf16 v[228:231], v[184:187], v[164:167], 0\n"
    "v_lshl_add_u32 v42, v85, 3, s64\n"
    "v_mfma_f32_16x16x32_bf16 v[228:231], v[152:155], v[132:135], v[228:231]\n"
    "v_mfma_f32_16x16x32_bf16 v[232:235], v[152:155], v[136:139], 0\n"
    "s_lshl_b32 s24, s24, 8\n"
    "v_mfma_f32_16x16x32_bf16 v[232:235], v[184:187], v[168:171], v[232:235]\n"
    "v_add_u32 v58, 98304, v56\n"
    "v_mfma_f32_16x16x32_bf16 v[236:239], v[184:187], v[172:175], 0\n"
    "v_mfma_f32_16x16x32_bf16 v[236:239], v[152:155], v[140:143], v[236:239]\n"
    "v_add_u32 v59, 98304, v57\n"
    "v_mfma_f32_16x16x32_bf16 v[252:255], v[156:159], v[140:143], 0\n"
    "v_add_u32 v80, 98304, v76\n"
    "v_mfma_f32_16x16x32_bf16 v[252:255], v[188:191], v[172:175], v[252:255]\n"
    "v_mfma_f32_16x16x32_bf16 v[248:251], v[188:191], v[168:171], 0\n"
    "v_add_u32 v81, 98304, v77\n"
    "v_mfma_f32_16x16x32_bf16 v[248:251], v[156:159], v[136:139], v[248:251]\n"
    "v_add_u32 v82, 98304, v78\n"
    "v_mfma_f32_16x16x32_bf16 v[244:247], v[156:159], v[132:135], 0\n"
    "v_mfma_f32_16x16x32_bf16 v[244:247], v[188:191], v[164:167], v[244:247]\n"
    "v_add_u32 v83, 98304, v79\n"
    "v_mfma_f32_16x16x32_bf16 v[240:243], v[188:191], v[160:163], 0\n"
    "v_mfma_f32_16x16x32_bf16 v[240:243], v[156:159], v[128:131], v[240:243]\n"
    "s_waitcnt vmcnt(4)\n"
    "s_barrier\n"
    "s_add_u32 m0, s35, 0\n"
    "s_nop 0\n"
    "global_load_lds_dwordx4 v46, s[30:31] offset:0\n"
    "ds_read_b128 v[128:131], v56 offset:49152\n"
    "ds_read_b128 v[132:135], v56 offset:51200\n"
    "ds_read_b128 v[136:139], v56 offset:53248\n"
    "ds_read_b128 v[140:143], v56 offset:55296\n"
    "global_load_lds_dwordx4 v47, s[30:31] offset:1024\n"
    "s_bfe_u32 s66, s28, 0x80000\n"
    "s_mul_i32 s66, s66, 53248\n"
    "s_add_u32 s30, s6, s66\n"
    "s_addc_u32 s31, s7, 0\n"
    "ds_read_b64_tr_b16 v[144:145], v76 offset:49152\n"
    "ds_read_b64_tr_b16 v[146:147], v76 offset:50176\n"
    "ds_read_b64_tr_b16 v[148:149], v77 offset:49152\n"
    "ds_read_b64_tr_b16 v[150:151], v77 offset:50176\n"
    "s_add_u32 m0, s34, 0\n"
    "s_nop 0\n"
    "global_load_lds_dwordx4 v52, s[36:37] offset:0\n"
    "ds_read_b64_tr_b16 v[152:153], v78 offset:49152\n"
    "ds_read_b64_tr_b16 v[154:155], v78 offset:50176\n"
    "ds_read_b64_tr_b16 v[156:157], v79 offset:49152\n"
    "ds_read_b64_tr_b16 v[158:159], v79 offset:50176\n"
    "global_load_lds_dwordx4 v53, s[36:37] offset:1024\n"
    "ds_read_b128 v[160:163], v57 offset:49152\n"
    "ds_read_b128 v[164:167], v57 offset:51200\n"
    "ds_read_b128 v[168:171], v57 offset:53248\n"
    "ds_read_b128 v[172:175], v57 offset:55296\n"
    "global_load_lds_dwordx4 v54, s[36:37] offset:2048\n"
    "ds_read_b64_tr_b16 v[176:177], v76 offset:57344\n"
    "ds_read_b64_tr_b16 v[178:179], v76 offset:58368\n"
    "ds_read_b64_tr_b16 v[180:181], v77 offset:57344\n"
    "ds_read_b64_tr_b16 v[182:183], v77 offset:58368\n"
    "global_load_lds_dwordx4 v55, s[36:37] offset:3072\n"
    "ds_read_b64_tr_b16 v[184:185], v78 offset:57344\n"
    "ds_read_b64_tr_b16 v[186:187], v78 offset:58368\n"
    "ds_read_b64_tr_b16 v[188:189], v79 offset:57344\n"
    "ds_read_b64_tr_b16 v[190:191], v79 offset:58368\n"
    "s_waitcnt lgkmcnt(0)\n"
    "s_barrier\n"
    "v_mfma_f32_16x16x32_bf16 v[192:195], v[144:147], v[128:131], v[192:195]\n"
    "v_lshlrev_b32 v101, 7, v101\n"
    "v_mfma_f32_16x16x32_bf16 v[192:195], v[176:179], v[160:163], v[192:195]\n"
    "v_mfma_f32_16x16x32_bf16 v[196:199], v[176:179], v[164:167], v[196:199]\n"
    "v_add_u32_dpp v52, v100, v48 quad_perm:[0,0,0,0] row_mask:0xf bank_mask:0xf\n"
    "v_mfma_f32_16x16x32_bf16 v[196:199], v[144:147], v[132:135], v[196:199]\n"
    "v_mfma_f32_16x16x32_bf16 v[200:203], v[144:147], v[136:139], v[200:203]\n"
    "v_add_u32_dpp v53, v100, v49 quad_perm:[1,1,1,1] row_mask:0xf bank_mask:0xf\n"
    "v_mfma_f32_16x16x32_bf16 v[200:203], v[176:179], v[168:171], v[200:203]\n"
    "v_mfma_f32_16x16x32_bf16 v[204:207], v[176:179], v[172:175], v[204:207]\n"
    "v_add_u32_dpp v54, v100, v50 quad_perm:[2,2,2,2] row_mask:0xf bank_mask:0xf\n"
    "v_mfma_f32_16x16x32_bf16 v[204:207], v[144:147], v[140:143], v[204:207]\n"
    "v_mfma_f32_16x16x32_bf16 v[220:223], v[148:151], v[140:143], v[220:223]\n"
    "v_add_u32_dpp v55, v100, v51 quad_perm:[3,3,3,3] row_mask:0xf bank_mask:0xf\n"
    "v_mfma_f32_16x16x32_bf16 v[220:223], v[180:183], v[172:175], v[220:223]\n"
    "v_mfma_f32_16x16x32_bf16 v[216:219], v[180:183], v[168:171], v[216:219]\n"
    "s_mul_i32 s47, s3, 5042\n"
    "v_mfma_f32_16x16x32_bf16 v[216:219], v[148:151], v[136:139], v[216:219]\n"
    "v_mfma_f32_16x16x32_bf16 v[212:215], v[148:151], v[132:135], v[212:215]\n"
    "s_lshr_b32 s47, s47, 16\n"
    "v_mfma_f32_16x16x32_bf16 v[212:215], v[180:183], v[164:167], v[212:215]\n"
    "v_mfma_f32_16x16x32_bf16 v[208:211], v[180:183], v[160:163], v[208:211]\n"
    "s_lshr_b32 s21, s20, 2\n"
    "v_mfma_f32_16x16x32_bf16 v[208:211], v[148:151], v[128:131], v[208:211]\n"
    "v_mfma_f32_16x16x32_bf16 v[224:227], v[152:155], v[128:131], v[224:227]\n"
    "s_lshl1_add_u32 s22, s21, s20\n"
    "v_mfma_f32_16x16x32_bf16 v[224:227], v[184:187], v[160:163], v[224:227]\n"
    "v_mfma_f32_16x16x32_bf16 v[228:231], v[184:187], v[164:167], v[228:231]\n"
    "s_and_b32 s22, s22, 3\n"
    "v_mfma_f32_16x16x32_bf16 v[228:231], v[152:155], v[132:135], v[228:231]\n"
    "v_mfma_f32_16x16x32_bf16 v[232:235], v[152:155], v[136:139], v[232:235]\n"
    "s_lshl_b32 s58, s21, 8\n"
    "v_mfma_f32_16x16x32_bf16 v[232:235], v[184:187], v[168:171], v[232:235]\n"
    "v_mfma_f32_16x16x32_bf16 v[236:239], v[184:187], v[172:175], v[236:239]\n"
    "s_add_u32 s58, s58, 150528\n"
    "v_mfma_f32_16x16x32_bf16 v[236:239], v[152:155], v[140:143], v[236:239]\n"
    "v_mfma_f32_16x16x32_bf16 v[252:255], v[156:159], v[140:143], v[252:255]\n"
    "s_mov_b32 s40, s47\n"
    "v_mfma_f32_16x16x32_bf16 v[252:255], v[188:191], v[172:175], v[252:255]\n"
    "v_mfma_f32_16x16x32_bf16 v[248:251], v[188:191], v[168:171], v[248:251]\n"
    "s_mul_i32 s54, s40, 512\n"
    "v_mfma_f32_16x16x32_bf16 v[248:251], v[156:159], v[136:139], v[248:251]\n"
    "v_mfma_f32_16x16x32_bf16 v[244:247], v[156:159], v[132:135], v[244:247]\n"
    "s_add_u32 s54, s54, 2\n"
    "v_mfma_f32_16x16x32_bf16 v[244:247], v[188:191], v[164:167], v[244:247]\n"
    "v_mfma_f32_16x16x32_bf16 v[240:243], v[188:191], v[160:163], v[240:243]\n"
    "v_mfma_f32_16x16x32_bf16 v[240:243], v[156:159], v[128:131], v[240:243]\n"
    "s_waitcnt vmcnt(6)\n"
    "s_barrier\n"
    "s_add_u32 m0, s35, 49152\n"
    "s_nop 0\n"
    "global_load_lds_dwordx4 v46, s[30:31] offset:0\n"
    "ds_read_b128 v[128:131], v58 offset:0\n"
    "ds_read_b128 v[132:135], v58 offset:2048\n"
    "ds_read_b128 v[136:139], v58 offset:4096\n"
    "ds_read_b128 v[140:143], v58 offset:6144\n"
    "global_load_lds_dwordx4 v47, s[30:31] offset:1024\n"
    "s_add_u32 s30, s30, 212992\n"
    "s_addc_u32 s31, s31, 0\n"
    "ds_read_b64_tr_b16 v[144:145], v80 offset:0\n"
    "ds_read_b64_tr_b16 v[146:147], v80 offset:1024\n"
    "ds_read_b64_tr_b16 v[148:149], v81 offset:0\n"
    "ds_read_b64_tr_b16 v[150:151], v81 offset:1024\n"
    "s_add_u32 m0, s34, 49152\n"
    "s_nop 0\n"
    "global_load_lds_dwordx4 v52, s[36:37] offset:0\n"
    "ds_read_b64_tr_b16 v[152:153], v82 offset:0\n"
    "ds_read_b64_tr_b16 v[154:155], v82 offset:1024\n"
    "ds_read_b64_tr_b16 v[156:157], v83 offset:0\n"
    "ds_read_b64_tr_b16 v[158:159], v83 offset:1024\n"
    "global_load_lds_dwordx4 v53, s[36:37] offset:1024\n"
    "ds_read_b128 v[160:163], v59 offset:0\n"
    "ds_read_b128 v[164:167], v59 offset:2048\n"
    "ds_read_b128 v[168:171], v59 offset:4096\n"
    "ds_read_b128 v[172:175], v59 offset:6144\n"
    "global_load_lds_dwordx4 v54, s[36:37] offset:2048\n"
    "ds_read_b64_tr_b16 v[176:177], v80 offset:8192\n"
    "ds_read_b64_tr_b16 v[178:179], v80 offset:9216\n"
    "ds_read_b64_tr_b16 v[180:181], v81 offset:8192\n"
    "ds_read_b64_tr_b16 v[182:183], v81 offset:9216\n"
    "global_load_lds_dwordx4 v55, s[36:37] offset:3072\n"
    "ds_read_b64_tr_b16 v[184:185], v82 offset:8192\n"
    "ds_read_b64_tr_b16 v[186:187], v82 offset:9216\n"
    "ds_read_b64_tr_b16 v[188:189], v83 offset:8192\n"
    "ds_read_b64_tr_b16 v[190:191], v83 offset:9216\n"
    "s_waitcnt vmcnt(10) lgkmcnt(0)\n"
    "s_barrier\n"
    "v_mfma_f32_16x16x32_bf16 v[192:195], v[144:147], v[128:131], v[192:195]\n"
    "v_lshlrev_b32 v102, 7, v102\n"
    "v_mfma_f32_16x16x32_bf16 v[192:195], v[176:179], v[160:163], v[192:195]\n"
    "v_mfma_f32_16x16x32_bf16 v[196:199], v[176:179], v[164:167], v[196:199]\n"
    "v_add_u32_dpp v52, v101, v48 quad_perm:[0,0,0,0] row_mask:0xf bank_mask:0xf\n"
    "v_mfma_f32_16x16x32_bf16 v[196:199], v[144:147], v[132:135], v[196:199]\n"
    "v_mfma_f32_16x16x32_bf16 v[200:203], v[144:147], v[136:139], v[200:203]\n"
    "v_add_u32_dpp v53, v101, v49 quad_perm:[1,1,1,1] row_mask:0xf bank_mask:0xf\n"
    "v_mfma_f32_16x16x32_bf16 v[200:203], v[176:179], v[168:171], v[200:203]\n"
    "v_mfma_f32_16x16x32_bf16 v[204:207], v[176:179], v[172:175], v[204:207]\n"
    "v_add_u32_dpp v54, v101, v50 quad_perm:[2,2,2,2] row_mask:0xf bank_mask:0xf\n"
    "v_mfma_f32_16x16x32_bf16 v[204:207], v[144:147], v[140:143], v[204:207]\n"
    "v_mfma_f32_16x16x32_bf16 v[220:223], v[148:151], v[140:143], v[220:223]\n"
    "v_add_u32_dpp v55, v101, v51 quad_perm:[3,3,3,3] row_mask:0xf bank_mask:0xf\n"
    "v_mfma_f32_16x16x32_bf16 v[220:223], v[180:183], v[172:175], v[220:223]\n"
    "v_mfma_f32_16x16x32_bf16 v[216:219], v[180:183], v[168:171], v[216:219]\n"
    "s_lshl_b32 s40, s40, 7\n"
    "v_mfma_f32_16x16x32_bf16 v[216:219], v[148:151], v[136:139], v[216:219]\n"
    "v_mfma_f32_16x16x32_bf16 v[212:215], v[148:151], v[132:135], v[212:215]\n"
    "s_add_u32 s40, s40, 32768\n"
    "v_mfma_f32_16x16x32_bf16 v[212:215], v[180:183], v[164:167], v[212:215]\n"
    "v_mfma_f32_16x16x32_bf16 v[208:211], v[180:183], v[160:163], v[208:211]\n"
    "s_add_u32 s52, s16, s40\n"
    "s_addc_u32 s53, s17, 0\n"
    "v_mfma_f32_16x16x32_bf16 v[208:211], v[148:151], v[128:131], v[208:211]\n"
    "v_mfma_f32_16x16x32_bf16 v[224:227], v[152:155], v[128:131], v[224:227]\n"
    "s_mov_b32 s57, 0x7fff7fff\n"
    "v_mfma_f32_16x16x32_bf16 v[224:227], v[184:187], v[160:163], v[224:227]\n"
    "v_mfma_f32_16x16x32_bf16 v[228:231], v[184:187], v[164:167], v[228:231]\n"
    "s_mov_b32 s56, 0\n"
    "v_mfma_f32_16x16x32_bf16 v[228:231], v[152:155], v[132:135], v[228:231]\n"
    "v_mfma_f32_16x16x32_bf16 v[232:235], v[152:155], v[136:139], v[232:235]\n"
    "s_mov_b32 s27, 0\n"
    "v_mfma_f32_16x16x32_bf16 v[232:235], v[184:187], v[168:171], v[232:235]\n"
    "v_mfma_f32_16x16x32_bf16 v[236:239], v[184:187], v[172:175], v[236:239]\n"
    "s_mov_b32 s55, 9\n"
    "v_mfma_f32_16x16x32_bf16 v[236:239], v[152:155], v[140:143], v[236:239]\n"
    "v_mfma_f32_16x16x32_bf16 v[252:255], v[156:159], v[140:143], v[252:255]\n"
    "s_mov_b32 s44, 0\n"
    "v_mfma_f32_16x16x32_bf16 v[252:255], v[188:191], v[172:175], v[252:255]\n"
    "v_mfma_f32_16x16x32_bf16 v[248:251], v[188:191], v[168:171], v[248:251]\n"
    "s_mov_b32 s46, 0\n"
    "v_mfma_f32_16x16x32_bf16 v[248:251], v[156:159], v[136:139], v[248:251]\n"
    "v_mfma_f32_16x16x32_bf16 v[244:247], v[156:159], v[132:135], v[244:247]\n"
    "v_lshrrev_b32 v17, 3, v1\n"
    "v_mfma_f32_16x16x32_bf16 v[244:247], v[188:191], v[164:167], v[244:247]\n"
    "v_mfma_f32_16x16x32_bf16 v[240:243], v[188:191], v[160:163], v[240:243]\n"
    "v_mfma_f32_16x16x32_bf16 v[240:243], v[156:159], v[128:131], v[240:243]\n"
    "s_waitcnt vmcnt(6)\n"
    "s_barrier\n"
    "s_add_u32 m0, s35, 98304\n"
    "s_nop 0\n"
    "global_load_lds_dwordx4 v46, s[30:31] offset:0\n"
    "ds_read_b128 v[128:131], v56 offset:0\n"
    "ds_read_b128 v[132:135], v56 offset:2048\n"
    "ds_read_b128 v[136:139], v56 offset:4096\n"
    "ds_read_b128 v[140:143], v56 offset:6144\n"
    "global_load_lds_dwordx4 v47, s[30:31] offset:1024\n"
    "s_add_u32 s30, s30, 212992\n"
    "s_addc_u32 s31, s31, 0\n"
    "ds_read_b64_tr_b16 v[144:145], v76 offset:0\n"
    "ds_read_b64_tr_b16 v[146:147], v76 offset:1024\n"
    "ds_read_b64_tr_b16 v[148:149], v77 offset:0\n"
    "ds_read_b64_tr_b16 v[150:151], v77 offset:1024\n"
    "s_add_u32 m0, s34, 98304\n"
    "s_nop 0\n"
    "global_load_lds_dwordx4 v52, s[36:37] offset:0\n"
    "ds_read_b64_tr_b16 v[152:153], v78 offset:0\n"
    "ds_read_b64_tr_b16 v[154:155], v78 offset:1024\n"
    "ds_read_b64_tr_b16 v[156:157], v79 offset:0\n"
    "ds_read_b64_tr_b16 v[158:159], v79 offset:1024\n"
    "global_load_lds_dwordx4 v53, s[36:37] offset:1024\n"
    "ds_read_b128 v[160:163], v57 offset:0\n"
    "ds_read_b128 v[164:167], v57 offset:2048\n"
    "ds_read_b128 v[168:171], v57 offset:4096\n"
    "ds_read_b128 v[172:175], v57 offset:6144\n"
    "global_load_lds_dwordx4 v54, s[36:37] offset:2048\n"
    "ds_read_b64_tr_b16 v[176:177], v76 offset:8192\n"
    "ds_read_b64_tr_b16 v[178:179], v76 offset:9216\n"
    "ds_read_b64_tr_b16 v[180:181], v77 offset:8192\n"
    "ds_read_b64_tr_b16 v[182:183], v77 offset:9216\n"
    "global_load_lds_dwordx4 v55, s[36:37] offset:3072\n"
    "ds_read_b64_tr_b16 v[184:185], v78 offset:8192\n"
    "ds_read_b64_tr_b16 v[186:187], v78 offset:9216\n"
    "ds_read_b64_tr_b16 v[188:189], v79 offset:8192\n"
    "ds_read_b64_tr_b16 v[190:191], v79 offset:9216\n"
    "s_waitcnt vmcnt(10) lgkmcnt(0)\n"
    "s_barrier\n"
    "v_mfma_f32_16x16x32_bf16 v[192:195], v[144:147], v[128:131], v[192:195]\n"
    "v_lshlrev_b32 v103, 7, v103\n"
    "v_mfma_f32_16x16x32_bf16 v[192:195], v[176:179], v[160:163], v[192:195]\n"
    "v_mfma_f32_16x16x32_bf16 v[196:199], v[176:179], v[164:167], v[196:199]\n"
    "v_add_u32_dpp v52, v102, v48 quad_perm:[0,0,0,0] row_mask:0xf bank_mask:0xf\n"
    "v_mfma_f32_16x16x32_bf16 v[196:199], v[144:147], v[132:135], v[196:199]\n"
    "v_mfma_f32_16x16x32_bf16 v[200:203], v[144:147], v[136:139], v[200:203]\n"
    "v_add_u32_dpp v53, v102, v49 quad_perm:[1,1,1,1] row_mask:0xf bank_mask:0xf\n"
    "v_mfma_f32_16x16x32_bf16 v[200:203], v[176:179], v[168:171], v[200:203]\n"
    "v_mfma_f32_16x16x32_bf16 v[204:207], v[176:179], v[172:175], v[204:207]\n"
    "v_add_u32_dpp v54, v102, v50 quad_perm:[2,2,2,2] row_mask:0xf bank_mask:0xf\n"
    "v_mfma_f32_16x16x32_bf16 v[204:207], v[144:147], v[140:143], v[204:207]\n"
    "v_mfma_f32_16x16x32_bf16 v[220:223], v[148:151], v[140:143], v[220:223]\n"
    "v_add_u32_dpp v55, v102, v51 quad_perm:[3,3,3,3] row_mask:0xf bank_mask:0xf\n"
    "v_mfma_f32_16x16x32_bf16 v[220:223], v[180:183], v[172:175], v[220:223]\n"
    "v_mfma_f32_16x16x32_bf16 v[216:219], v[180:183], v[168:171], v[216:219]\n"
    "v_mul_u32_u24 v17, 12, v17\n"
    "v_mfma_f32_16x16x32_bf16 v[216:219], v[148:151], v[136:139], v[216:219]\n"
    "v_mfma_f32_16x16x32_bf16 v[212:215], v[148:151], v[132:135], v[212:215]\n"
    "v_and_b32 v19, 7, v1\n"
    "v_mfma_f32_16x16x32_bf16 v[212:215], v[180:183], v[164:167], v[212:215]\n"
    "v_mfma_f32_16x16x32_bf16 v[208:211], v[180:183], v[160:163], v[208:211]\n"
    "v_lshlrev_b32 v19, 4, v19\n"
    "v_mfma_f32_16x16x32_bf16 v[208:211], v[148:151], v[128:131], v[208:211]\n"
    "v_mfma_f32_16x16x32_bf16 v[224:227], v[152:155], v[128:131], v[224:227]\n"
    "v_lshrrev_b32 v18, 5, v1\n"
    "v_mfma_f32_16x16x32_bf16 v[224:227], v[184:187], v[160:163], v[224:227]\n"
    "v_mfma_f32_16x16x32_bf16 v[228:231], v[184:187], v[164:167], v[228:231]\n"
    "v_lshlrev_b32 v18, 2, v18\n"
    "v_mfma_f32_16x16x32_bf16 v[228:231], v[152:155], v[132:135], v[228:231]\n"
    "v_mfma_f32_16x16x32_bf16 v[232:235], v[152:155], v[136:139], v[232:235]\n"
    "v_and_b32 v20, 31, v1\n"
    "v_mfma_f32_16x16x32_bf16 v[232:235], v[184:187], v[168:171], v[232:235]\n"
    "v_mfma_f32_16x16x32_bf16 v[236:239], v[184:187], v[172:175], v[236:239]\n"
    "v_lshlrev_b32 v20, 2, v20\n"
    "v_mfma_f32_16x16x32_bf16 v[236:239], v[152:155], v[140:143], v[236:239]\n"
    "v_mfma_f32_16x16x32_bf16 v[252:255], v[156:159], v[140:143], v[252:255]\n"
    "s_lshl2_add_u32 s40, s22, s58\n"
    "v_mfma_f32_16x16x32_bf16 v[252:255], v[188:191], v[172:175], v[252:255]\n"
    "v_mfma_f32_16x16x32_bf16 v[248:251], v[188:191], v[168:171], v[248:251]\n"
    "s_sub_u32 s40, s40, 20\n"
    "v_mfma_f32_16x16x32_bf16 v[248:251], v[156:159], v[136:139], v[248:251]\n"
    "v_mfma_f32_16x16x32_bf16 v[244:247], v[156:159], v[132:135], v[244:247]\n"
    "v_mov_b32 v25, s40\n"
    "v_mfma_f32_16x16x32_bf16 v[244:247], v[188:191], v[164:167], v[244:247]\n"
    "v_mfma_f32_16x16x32_bf16 v[240:243], v[188:191], v[160:163], v[240:243]\n"
    "v_mfma_f32_16x16x32_bf16 v[240:243], v[156:159], v[128:131], v[240:243]\n"
    "s_waitcnt vmcnt(6)\n"
    "s_barrier\n"
    "s_add_u32 m0, s35, 0\n"
    "s_nop 0\n"
    "global_load_lds_dwordx4 v46, s[30:31] offset:0\n"
    "ds_read_b128 v[128:131], v56 offset:49152\n"
    "ds_read_b128 v[132:135], v56 offset:51200\n"
    "ds_read_b128 v[136:139], v56 offset:53248\n"
    "ds_read_b128 v[140:143], v56 offset:55296\n"
    "global_load_lds_dwordx4 v47, s[30:31] offset:1024\n"
    "s_add_u32 s30, s30, 212992\n"
    "s_addc_u32 s31, s31, 0\n"
    "ds_read_b64_tr_b16 v[144:145], v76 offset:49152\n"
    "ds_read_b64_tr_b16 v[146:147], v76 offset:50176\n"
    "ds_read_b64_tr_b16 v[148:149], v77 offset:49152\n"
    "ds_read_b64_tr_b16 v[150:151], v77 offset:50176\n"
    "s_add_u32 m0, s34, 0\n"
    "s_nop 0\n"
    "global_load_lds_dwordx4 v52, s[36:37] offset:0\n"
    "ds_read_b64_tr_b16 v[152:153], v78 offset:49152\n"
    "ds_read_b64_tr_b16 v[154:155], v78 offset:50176\n"
    "ds_read_b64_tr_b16 v[156:157], v79 offset:49152\n"
    "ds_read_b64_tr_b16 v[158:159], v79 offset:50176\n"
    "global_load_lds_dwordx4 v53, s[36:37] offset:1024\n"
    "ds_read_b128 v[160:163], v57 offset:49152\n"
    "ds_read_b128 v[164:167], v57 offset:51200\n"
    "ds_read_b128 v[168:171], v57 offset:53248\n"
    "ds_read_b128 v[172:175], v57 offset:55296\n"
    "global_load_lds_dwordx4 v54, s[36:37] offset:2048\n"
    "ds_read_b64_tr_b16 v[176:177], v76 offset:57344\n"
    "ds_read_b64_tr_b16 v[178:179], v76 offset:58368\n"
    "ds_read_b64_tr_b16 v[180:181], v77 offset:57344\n"
    "ds_read_b64_tr_b16 v[182:183], v77 offset:58368\n"
    "global_load_lds_dwordx4 v55, s[36:37] offset:3072\n"
    "ds_read_b64_tr_b16 v[184:185], v78 offset:57344\n"
    "ds_read_b64_tr_b16 v[186:187], v78 offset:58368\n"
    "ds_read_b64_tr_b16 v[188:189], v79 offset:57344\n"
    "ds_read_b64_tr_b16 v[190:191], v79 offset:58368\n"
    "s_waitcnt vmcnt(10) lgkmcnt(0)\n"
    "s_barrier\n"
    "v_mfma_f32_16x16x32_bf16 v[192:195], v[144:147], v[128:131], v[192:195]\n"
    "v_lshlrev_b32 v104, 7, v104\n"
    "v_mfma_f32_16x16x32_bf16 v[192:195], v[176:179], v[160:163], v[192:195]\n"
    "v_mfma_f32_16x16x32_bf16 v[196:199], v[176:179], v[164:167], v[196:199]\n"
    "v_add_u32_dpp v52, v103, v48 quad_perm:[0,0,0,0] row_mask:0xf bank_mask:0xf\n"
    "v_mfma_f32_16x16x32_bf16 v[196:199], v[144:147], v[132:135], v[196:199]\n"
    "v_mfma_f32_16x16x32_bf16 v[200:203], v[144:147], v[136:139], v[200:203]\n"
    "v_add_u32_dpp v53, v103, v49 quad_perm:[1,1,1,1] row_mask:0xf bank_mask:0xf\n"
    "v_mfma_f32_16x16x32_bf16 v[200:203], v[176:179], v[168:171], v[200:203]\n"
    "v_mfma_f32_16x16x32_bf16 v[204:207], v[176:179], v[172:175], v[204:207]\n"
    "v_add_u32_dpp v54, v103, v50 quad_perm:[2,2,2,2] row_mask:0xf bank_mask:0xf\n"
    "v_mfma_f32_16x16x32_bf16 v[204:207], v[144:147], v[140:143], v[204:207]\n"
    "v_mfma_f32_16x16x32_bf16 v[220:223], v[148:151], v[140:143], v[220:223]\n"
    "v_add_u32_dpp v55, v103, v51 quad_perm:[3,3,3,3] row_mask:0xf bank_mask:0xf\n"
    "v_mfma_f32_16x16x32_bf16 v[220:223], v[180:183], v[172:175], v[220:223]\n"
    "v_mfma_f32_16x16x32_bf16 v[216:219], v[180:183], v[168:171], v[216:219]\n"
    "s_lshl_b32 s43, s21, 16\n"
    "v_mfma_f32_16x16x32_bf16 v[216:219], v[148:151], v[136:139], v[216:219]\n"
    "v_mfma_f32_16x16x32_bf16 v[212:215], v[148:151], v[132:135], v[212:215]\n"
    "v_lshrrev_b32 v12, 4, v1\n"
    "v_mfma_f32_16x16x32_bf16 v[212:215], v[180:183], v[164:167], v[212:215]\n"
    "v_mfma_f32_16x16x32_bf16 v[208:211], v[180:183], v[160:163], v[208:211]\n"
    "v_bfe_u32 v13, v1, 2, 2\n"
    "v_mfma_f32_16x16x32_bf16 v[208:211], v[148:151], v[128:131], v[208:211]\n"
    "v_mfma_f32_16x16x32_bf16 v[224:227], v[152:155], v[128:131], v[224:227]\n"
    "v_and_b32 v21, 3, v1\n"
    "v_mfma_f32_16x16x32_bf16 v[224:227], v[184:187], v[160:163], v[224:227]\n"
    "v_mfma_f32_16x16x32_bf16 v[228:231], v[184:187], v[164:167], v[228:231]\n"
    "s_lshl_b32 s40, s22, 5\n"
    "v_mfma_f32_16x16x32_bf16 v[228:231], v[152:155], v[132:135], v[228:231]\n"
    "v_mfma_f32_16x16x32_bf16 v[232:235], v[152:155], v[136:139], v[232:235]\n"
    "s_add_u32 s40, s40, s43\n"
    "v_mfma_f32_16x16x32_bf16 v[232:235], v[184:187], v[168:171], v[232:235]\n"
    "v_mfma_f32_16x16x32_bf16 v[236:239], v[184:187], v[172:175], v[236:239]\n"
    "v_lshl_add_u32 v21, v21, 3, s40\n"
    "v_mfma_f32_16x16x32_bf16 v[236:239], v[152:155], v[140:143], v[236:239]\n"
    "v_mfma_f32_16x16x32_bf16 v[252:255], v[156:159], v[140:143], v[252:255]\n"
    "s_movk_i32 s41, 144\n"
    "v_mfma_f32_16x16x32_bf16 v[252:255], v[188:191], v[172:175], v[252:255]\n"
    "v_mfma_f32_16x16x32_bf16 v[248:251], v[188:191], v[168:171], v[248:251]\n"
    "v_add_u32 v22, v12, v13\n"
    "v_mfma_f32_16x16x32_bf16 v[248:251], v[156:159], v[136:139], v[248:251]\n"
    "v_mfma_f32_16x16x32_bf16 v[244:247], v[156:159], v[132:135], v[244:247]\n"
    "v_and_b32 v22, 3, v22\n"
    "v_mfma_f32_16x16x32_bf16 v[244:247], v[188:191], v[164:167], v[244:247]\n"
    "v_mfma_f32_16x16x32_bf16 v[240:243], v[188:191], v[160:163], v[240:243]\n"
    "v_mfma_f32_16x16x32_bf16 v[240:243], v[156:159], v[128:131], v[240:243]\n"
    "s_waitcnt vmcnt(6)\n"
    "s_barrier\n"
    "s_add_u32 m0, s35, 49152\n"
    "s_nop 0\n"
    "global_load_lds_dwordx4 v46, s[30:31] offset:0\n"
    "ds_read_b128 v[128:131], v58 offset:0\n"
    "ds_read_b128 v[132:135], v58 offset:2048\n"
    "ds_read_b128 v[136:139], v58 offset:4096\n"
    "ds_read_b128 v[140:143], v58 offset:6144\n"
    "global_load_lds_dwordx4 v47, s[30:31] offset:1024\n"
    "s_bfe_u32 s66, s28, 0x80008\n"
    "s_mul_i32 s66, s66, 53248\n"
    "s_add_u32 s30, s6, s66\n"
    "s_addc_u32 s31, s7, 0\n"
    "ds_read_b64_tr_b16 v[144:145], v80 offset:0\n"
    "ds_read_b64_tr_b16 v[146:147], v80 offset:1024\n"
    "ds_read_b64_tr_b16 v[148:149], v81 offset:0\n"
    "ds_read_b64_tr_b16 v[150:151], v81 offset:1024\n"
    "s_add_u32 m0, s34, 49152\n"
    "s_nop 0\n"
    "global_load_lds_dwordx4 v52, s[36:37] offset:0\n"
    "ds_read_b64_tr_b16 v[152:153], v82 offset:0\n"
    "ds_read_b64_tr_b16 v[154:155], v82 offset:1024\n"
    "ds_read_b64_tr_b16 v[156:157], v83 offset:0\n"
    "ds_read_b64_tr_b16 v[158:159], v83 offset:1024\n"
    "global_load_lds_dwordx4 v53, s[36:37] offset:1024\n"
    "ds_read_b128 v[160:163], v59 offset:0\n"
    "ds_read_b128 v[164:167], v59 offset:2048\n"
    "ds_read_b128 v[168:171], v59 offset:4096\n"
    "ds_read_b128 v[172:175], v59 offset:6144\n"
    "global_load_lds_dwordx4 v54, s[36:37] offset:2048\n"
    "ds_read_b64_tr_b16 v[176:177], v80 offset:8192\n"
    "ds_read_b64_tr_b16 v[178:179], v80 offset:9216\n"
    "ds_read_b64_tr_b16 v[180:181], v81 offset:8192\n"
    "ds_read_b64_tr_b16 v[182:183], v81 offset:9216\n"
    "global_load_lds_dwordx4 v55, s[36:37] offset:3072\n"
    "ds_read_b64_tr_b16 v[184:185], v82 offset:8192\n"
    "ds_read_b64_tr_b16 v[186:187], v82 offset:9216\n"
    "ds_read_b64_tr_b16 v[188:189], v83 offset:8192\n"
    "ds_read_b64_tr_b16 v[190:191], v83 offset:9216\n"
    "s_waitcnt vmcnt(10) lgkmcnt(0)\n"
    "s_barrier\n"
    "v_mfma_f32_16x16x32_bf16 v[192:195], v[144:147], v[128:131], v[192:195]\n"
    "v_lshlrev_b32 v105, 7, v105\n"
    "v_mfma_f32_16x16x32_bf16 v[192:195], v[176:179], v[160:163], v[192:195]\n"
    "v_mfma_f32_16x16x32_bf16 v[196:199], v[176:179], v[164:167], v[196:199]\n"
    "v_add_u32_dpp v52, v104, v48 quad_perm:[0,0,0,0] row_mask:0xf bank_mask:0xf\n"
    "v_mfma_f32_16x16x32_bf16 v[196:199], v[144:147], v[132:135], v[196:199]\n"
    "v_mfma_f32_16x16x32_bf16 v[200:203], v[144:147], v[136:139], v[200:203]\n"
    "v_add_u32_dpp v53, v104, v49 quad_perm:[1,1,1,1] row_mask:0xf bank_mask:0xf\n"
    "v_mfma_f32_16x16x32_bf16 v[200:203], v[176:179], v[168:171], v[200:203]\n"
    "v_mfma_f32_16x16x32_bf16 v[204:207], v[176:179], v[172:175], v[204:207]\n"
    "v_add_u32_dpp v54, v104, v50 quad_perm:[2,2,2,2] row_mask:0xf bank_mask:0xf\n"
    "v_mfma_f32_16x16x32_bf16 v[204:207], v[144:147], v[140:143], v[204:207]\n"
    "v_mfma_f32_16x16x32_bf16 v[220:223], v[148:151], v[140:143], v[220:223]\n"
    "v_add_u32_dpp v55, v104, v51 quad_perm:[3,3,3,3] row_mask:0xf bank_mask:0xf\n"
    "v_mfma_f32_16x16x32_bf16 v[220:223], v[180:183], v[172:175], v[220:223]\n"
    "v_mfma_f32_16x16x32_bf16 v[216:219], v[180:183], v[168:171], v[216:219]\n"
    "v_mad_u32_u24 v4, v22, s41, v21\n"
    "v_mfma_f32_16x16x32_bf16 v[216:219], v[148:151], v[136:139], v[216:219]\n"
    "v_mfma_f32_16x16x32_bf16 v[212:215], v[148:151], v[132:135], v[212:215]\n"
    "v_cmp_gt_u32 vcc, 2, v13\n"
    "v_mfma_f32_16x16x32_bf16 v[212:215], v[180:183], v[164:167], v[212:215]\n"
    "v_mfma_f32_16x16x32_bf16 v[208:211], v[180:183], v[160:163], v[208:211]\n"
    "v_add_u32 v24, 22, v13\n"
    "v_mfma_f32_16x16x32_bf16 v[208:211], v[148:151], v[128:131], v[208:211]\n"
    "v_mfma_f32_16x16x32_bf16 v[224:227], v[152:155], v[128:131], v[224:227]\n"
    "v_lshlrev_b32 v13, 3, v13\n"
    "v_mfma_f32_16x16x32_bf16 v[224:227], v[184:187], v[160:163], v[224:227]\n"
    "v_mfma_f32_16x16x32_bf16 v[228:231], v[184:187], v[164:167], v[228:231]\n"
    "v_cndmask_b32 v22, v24, v22, vcc\n"
    "v_mfma_f32_16x16x32_bf16 v[228:231], v[152:155], v[132:135], v[228:231]\n"
    "v_mfma_f32_16x16x32_bf16 v[232:235], v[152:155], v[136:139], v[232:235]\n"
    "v_mad_u32_u24 v3, v22, s41, v21\n"
    "v_mfma_f32_16x16x32_bf16 v[232:235], v[184:187], v[168:171], v[232:235]\n"
    "v_mfma_f32_16x16x32_bf16 v[236:239], v[184:187], v[172:175], v[236:239]\n"
    "v_cmp_eq_u32 vcc, 3, v12\n"
    "v_mfma_f32_16x16x32_bf16 v[236:239], v[152:155], v[140:143], v[236:239]\n"
    "v_mfma_f32_16x16x32_bf16 v[252:255], v[156:159], v[140:143], v[252:255]\n"
    "v_xor_b32 v22, 0, v12\n"
    "v_mfma_f32_16x16x32_bf16 v[252:255], v[188:191], v[172:175], v[252:255]\n"
    "v_mfma_f32_16x16x32_bf16 v[248:251], v[188:191], v[168:171], v[248:251]\n"
    "v_and_b32 v22, 1, v22\n"
    "v_mfma_f32_16x16x32_bf16 v[248:251], v[156:159], v[136:139], v[248:251]\n"
    "v_mfma_f32_16x16x32_bf16 v[244:247], v[156:159], v[132:135], v[244:247]\n"
    "v_lshlrev_b32 v22, 1, v22\n"
    "v_mfma_f32_16x16x32_bf16 v[244:247], v[188:191], v[164:167], v[244:247]\n"
    "v_mfma_f32_16x16x32_bf16 v[240:243], v[188:191], v[160:163], v[240:243]\n"
    "v_mfma_f32_16x16x32_bf16 v[240:243], v[156:159], v[128:131], v[240:243]\n"
    "s_waitcnt vmcnt(6)\n"
    "s_barrier\n"
    "s_add_u32 m0, s35, 98304\n"
    "s_nop 0\n"
    "global_load_lds_dwordx4 v46, s[30:31] offset:0\n"
    "ds_read_b128 v[128:131], v56 offset:0\n"
    "ds_read_b128 v[132:135], v56 offset:2048\n"
    "ds_read_b128 v[136:139], v56 offset:4096\n"
    "global_load_lds_dwordx4 v47, s[30:31] offset:1024\n"
    "s_add_u32 s30, s30, 212992\n"
    "s_addc_u32 s31, s31, 0\n"
    "ds_read_b128 v[140:143], v56 offset:6144\n"
    "ds_read_b64_tr_b16 v[144:145], v76 offset:0\n"
    "ds_read_b64_tr_b16 v[146:147], v76 offset:1024\n"
    "s_add_u32 m0, s34, 98304\n"
    "s_nop 0\n"
    "global_load_lds_dwordx4 v52, s[36:37] offset:0\n"
    "ds_read_b64_tr_b16 v[148:149], v77 offset:0\n"
    "ds_read_b64_tr_b16 v[150:151], v77 offset:1024\n"
    "ds_read_b64_tr_b16 v[152:153], v78 offset:0\n"
    "global_load_lds_dwordx4 v53, s[36:37] offset:1024\n"
    "ds_read_b64_tr_b16 v[154:155], v78 offset:1024\n"
    "ds_read_b64_tr_b16 v[156:157], v79 offset:0\n"
    "ds_read_b64_tr_b16 v[158:159], v79 offset:1024\n"
    "global_load_lds_dwordx4 v54, s[36:37] offset:2048\n"
    "ds_read_b128 v[160:163], v57 offset:0\n"
    "ds_read_b128 v[164:167], v57 offset:2048\n"
    "ds_read_b128 v[168:171], v57 offset:4096\n"
    "global_load_lds_dwordx4 v55, s[36:37] offset:3072\n"
    "ds_read_b128 v[172:175], v57 offset:6144\n"
    "ds_read_b64_tr_b16 v[176:177], v76 offset:8192\n"
    "ds_read_b64_tr_b16 v[178:179], v76 offset:9216\n"
    "s_mov_b32 m0, s64\n"
    "s_nop 0\n"
    "global_load_lds_dword v43, s[10:11]\n"
    "ds_read_b64_tr_b16 v[180:181], v77 offset:8192\n"
    "ds_read_b64_tr_b16 v[182:183], v77 offset:9216\n"
    "ds_read_b64_tr_b16 v[184:185], v78 offset:8192\n"
    "ds_read_b64_tr_b16 v[186:187], v78 offset:9216\n"
    "ds_read_b64_tr_b16 v[188:189], v79 offset:8192\n"
    "ds_read_b64_tr_b16 v[190:191], v79 offset:9216\n"
    "s_waitcnt vmcnt(11) lgkmcnt(0)\n"
    "s_barrier\n"
    "v_mfma_f32_16x16x32_bf16 v[192:195], v[144:147], v[128:131], v[192:195]\n"
    "v_lshlrev_b32 v106, 7, v106\n"
    "v_mfma_f32_16x16x32_bf16 v[192:195], v[176:179], v[160:163], v[192:195]\n"
    "v_mfma_f32_16x16x32_bf16 v[196:199], v[176:179], v[164:167], v[196:199]\n"
    "v_add_u32_dpp v52, v105, v48 quad_perm:[0,0,0,0] row_mask:0xf bank_mask:0xf\n"
    "v_mfma_f32_16x16x32_bf16 v[196:199], v[144:147], v[132:135], v[196:199]\n"
    "v_mfma_f32_16x16x32_bf16 v[200:203], v[144:147], v[136:139], v[200:203]\n"
    "v_add_u32_dpp v53, v105, v49 quad_perm:[1,1,1,1] row_mask:0xf bank_mask:0xf\n"
    "v_mfma_f32_16x16x32_bf16 v[200:203], v[176:179], v[168:171], v[200:203]\n"
    "v_mfma_f32_16x16x32_bf16 v[204:207], v[176:179], v[172:175], v[204:207]\n"
    "v_add_u32_dpp v54, v105, v50 quad_perm:[2,2,2,2] row_mask:0xf bank_mask:0xf\n"
    "v_mfma_f32_16x16x32_bf16 v[204:207], v[144:147], v[140:143], v[204:207]\n"
    "v_mfma_f32_16x16x32_bf16 v[220:223], v[148:151], v[140:143], v[220:223]\n"
    "v_add_u32_dpp v55, v105, v51 quad_perm:[3,3,3,3] row_mask:0xf bank_mask:0xf\n"
    "v_mfma_f32_16x16x32_bf16 v[220:223], v[180:183], v[172:175], v[220:223]\n"
    "v_mfma_f32_16x16x32_bf16 v[216:219], v[180:183], v[168:171], v[216:219]\n"
    "v_lshl_add_u32 v22, v12, 3, v22\n"
    "v_mfma_f32_16x16x32_bf16 v[216:219], v[148:151], v[136:139], v[216:219]\n"
    "v_mfma_f32_16x16x32_bf16 v[212:215], v[148:151], v[132:135], v[212:215]\n"
    "s_mov_b32 s40, 0x05040100\n"
    "v_mfma_f32_16x16x32_bf16 v[212:215], v[180:183], v[164:167], v[212:215]\n"
    "v_mfma_f32_16x16x32_bf16 v[208:211], v[180:183], v[160:163], v[208:211]\n"
    "v_bfe_u32 v24, s40, v13, 8\n"
    "v_mfma_f32_16x16x32_bf16 v[208:211], v[148:151], v[128:131], v[208:211]\n"
    "v_mfma_f32_16x16x32_bf16 v[224:227], v[152:155], v[128:131], v[224:227]\n"
    "v_add_u32 v22, v22, v24\n"
    "v_mfma_f32_16x16x32_bf16 v[224:227], v[184:187], v[160:163], v[224:227]\n"
    "v_mfma_f32_16x16x32_bf16 v[228:231], v[184:187], v[164:167], v[228:231]\n"
    "s_mov_b32 s40, 0x1a191818\n"
    "v_mfma_f32_16x16x32_bf16 v[228:231], v[152:155], v[132:135], v[228:231]\n"
    "v_mfma_f32_16x16x32_bf16 v[232:235], v[152:155], v[136:139], v[232:235]\n"
    "v_bfe_u32 v24, s40, v13, 8\n"
    "v_mfma_f32_16x16x32_bf16 v[232:235], v[184:187], v[168:171], v[232:235]\n"
    "v_mfma_f32_16x16x32_bf16 v[236:239], v[184:187], v[172:175], v[236:239]\n"
    "v_cndmask_b32 v22, v22, v24, vcc\n"
    "v_mfma_f32_16x16x32_bf16 v[236:239], v[152:155], v[140:143], v[236:239]\n"
    "v_mfma_f32_16x16x32_bf16 v[252:255], v[156:159], v[140:143], v[252:255]\n"
    "v_mad_u32_u24 v5, v22, s41, v21\n"
    "v_mfma_f32_16x16x32_bf16 v[252:255], v[188:191], v[172:175], v[252:255]\n"
    "v_mfma_f32_16x16x32_bf16 v[248:251], v[188:191], v[168:171], v[248:251]\n"
    "v_xor_b32 v22, 1, v12\n"
    "v_mfma_f32_16x16x32_bf16 v[248:251], v[156:159], v[136:139], v[248:251]\n"
    "v_mfma_f32_16x16x32_bf16 v[244:247], v[156:159], v[132:135], v[244:247]\n"
    "v_and_b32 v22, 1, v22\n"
    "v_mfma_f32_16x16x32_bf16 v[244:247], v[188:191], v[164:167], v[244:247]\n"
    "v_mfma_f32_16x16x32_bf16 v[240:243], v[188:191], v[160:163], v[240:243]\n"
    "v_mfma_f32_16x16x32_bf16 v[240:243], v[156:159], v[128:131], v[240:243]\n"
    "s_waitcnt vmcnt(7)\n"
    "s_barrier\n"
    "s_add_u32 m0, s35, 0\n"
    "s_nop 0\n"
    "global_load_lds_dwordx4 v46, s[30:31] offset:0\n"
    "ds_read_b128 v[128:131], v56 offset:49152\n"
    "ds_read_b128 v[132:135], v56 offset:51200\n"
    "ds_read_b128 v[136:139], v56 offset:53248\n"
    "ds_read_b128 v[140:143], v56 offset:55296\n"
    "global_load_lds_dwordx4 v47, s[30:31] offset:1024\n"
    "s_add_u32 s30, s30, 212992\n"
    "s_addc_u32 s31, s31, 0\n"
    "ds_read_b64_tr_b16 v[144:145], v76 offset:49152\n"
    "ds_read_b64_tr_b16 v[146:147], v76 offset:50176\n"
    "ds_read_b64_tr_b16 v[148:149], v77 offset:49152\n"
    "ds_read_b64_tr_b16 v[150:151], v77 offset:50176\n"
    "s_add_u32 m0, s34, 0\n"
    "s_nop 0\n"
    "global_load_lds_dwordx4 v52, s[36:37] offset:0\n"
    "ds_read_b64_tr_b16 v[152:153], v78 offset:49152\n"
    "ds_read_b64_tr_b16 v[154:155], v78 offset:50176\n"
    "ds_read_b64_tr_b16 v[156:157], v79 offset:49152\n"
    "ds_read_b64_tr_b16 v[158:159], v79 offset:50176\n"
    "global_load_lds_dwordx4 v53, s[36:37] offset:1024\n"
    "ds_read_b128 v[160:163], v57 offset:49152\n"
    "ds_read_b128 v[164:167], v57 offset:51200\n"
    "ds_read_b128 v[168:171], v57 offset:53248\n"
    "ds_read_b128 v[172:175], v57 offset:55296\n"
    "global_load_lds_dwordx4 v54, s[36:37] offset:2048\n"
    "ds_read_b64_tr_b16 v[176:177], v76 offset:57344\n"
    "ds_read_b64_tr_b16 v[178:179], v76 offset:58368\n"
    "ds_read_b64_tr_b16 v[180:181], v77 offset:57344\n"
    "ds_read_b64_tr_b16 v[182:183], v77 offset:58368\n"
    "global_load_lds_dwordx4 v55, s[36:37] offset:3072\n"
    "ds_read_b64_tr_b16 v[184:185], v78 offset:57344\n"
    "ds_read_b64_tr_b16 v[186:187], v78 offset:58368\n"
    "ds_read_b64_tr_b16 v[188:189], v79 offset:57344\n"
    "ds_read_b64_tr_b16 v[190:191], v79 offset:58368\n"
    "s_waitcnt vmcnt(11) lgkmcnt(0)\n"
    "s_barrier\n"
    "v_mfma_f32_16x16x32_bf16 v[192:195], v[144:147], v[128:131], v[192:195]\n"
    "v_lshlrev_b32 v107, 7, v107\n"
    "v_mfma_f32_16x16x32_bf16 v[192:195], v[176:179], v[160:163], v[192:195]\n"
    "v_mfma_f32_16x16x32_bf16 v[196:199], v[176:179], v[164:167], v[196:199]\n"
    "v_add_u32_dpp v52, v106, v48 quad_perm:[0,0,0,0] row_mask:0xf bank_mask:0xf\n"
    "v_mfma_f32_16x16x32_bf16 v[196:199], v[144:147], v[132:135], v[196:199]\n"
    "v_mfma_f32_16x16x32_bf16 v[200:203], v[144:147], v[136:139], v[200:203]\n"
    "v_add_u32_dpp v53, v106, v49 quad_perm:[1,1,1,1] row_mask:0xf bank_mask:0xf\n"
    "v_mfma_f32_16x16x32_bf16 v[200:203], v[176:179], v[168:171], v[200:203]\n"
    "v_mfma_f32_16x16x32_bf16 v[204:207], v[176:179], v[172:175], v[204:207]\n"
    "v_add_u32_dpp v54, v106, v50 quad_perm:[2,2,2,2] row_mask:0xf bank_mask:0xf\n"
    "v_mfma_f32_16x16x32_bf16 v[204:207], v[144:147], v[140:143], v[204:207]\n"
    "v_mfma_f32_16x16x32_bf16 v[220:223], v[148:151], v[140:143], v[220:223]\n"
    "v_add_u32_dpp v55, v106, v51 quad_perm:[3,3,3,3] row_mask:0xf bank_mask:0xf\n"
    "v_mfma_f32_16x16x32_bf16 v[220:223], v[180:183], v[172:175], v[220:223]\n"
    "v_mfma_f32_16x16x32_bf16 v[216:219], v[180:183], v[168:171], v[216:219]\n"
    "v_lshlrev_b32 v22, 1, v22\n"
    "v_mfma_f32_16x16x32_bf16 v[216:219], v[148:151], v[136:139], v[216:219]\n"
    "v_mfma_f32_16x16x32_bf16 v[212:215], v[148:151], v[132:135], v[212:215]\n"
    "v_lshl_add_u32 v22, v12, 3, v22\n"
    "v_mfma_f32_16x16x32_bf16 v[212:215], v[180:183], v[164:167], v[212:215]\n"
    "v_mfma_f32_16x16x32_bf16 v[208:211], v[180:183], v[160:163], v[208:211]\n"
    "s_mov_b32 s40, 0x05040100\n"
    "v_mfma_f32_16x16x32_bf16 v[208:211], v[148:151], v[128:131], v[208:211]\n"
    "v_mfma_f32_16x16x32_bf16 v[224:227], v[152:155], v[128:131], v[224:227]\n"
    "v_bfe_u32 v24, s40, v13, 8\n"
    "v_mfma_f32_16x16x32_bf16 v[224:227], v[184:187], v[160:163], v[224:227]\n"
    "v_mfma_f32_16x16x32_bf16 v[228:231], v[184:187], v[164:167], v[228:231]\n"
    "v_add_u32 v22, v22, v24\n"
    "v_mfma_f32_16x16x32_bf16 v[228:231], v[152:155], v[132:135], v[228:231]\n"
    "v_mfma_f32_16x16x32_bf16 v[232:235], v[152:155], v[136:139], v[232:235]\n"
    "s_mov_b32 s40, 0x1a191918\n"
    "v_mfma_f32_16x16x32_bf16 v[232:235], v[184:187], v[168:171], v[232:235]\n"
    "v_mfma_f32_16x16x32_bf16 v[236:239], v[184:187], v[172:175], v[236:239]\n"
    "v_bfe_u32 v24, s40, v13, 8\n"
    "v_mfma_f32_16x16x32_bf16 v[236:239], v[152:155], v[140:143], v[236:239]\n"
    "v_mfma_f32_16x16x32_bf16 v[252:255], v[156:159], v[140:143], v[252:255]\n"
    "v_cndmask_b32 v22, v22, v24, vcc\n"
    "v_mfma_f32_16x16x32_bf16 v[252:255], v[188:191], v[172:175], v[252:255]\n"
    "v_mfma_f32_16x16x32_bf16 v[248:251], v[188:191], v[168:171], v[248:251]\n"
    "v_mad_u32_u24 v6, v22, s41, v21\n"
    "v_mfma_f32_16x16x32_bf16 v[248:251], v[156:159], v[136:139], v[248:251]\n"
    "v_mfma_f32_16x16x32_bf16 v[244:247], v[156:159], v[132:135], v[244:247]\n"
    "v_bfrev_b32 v14, v12\n"
    "v_mfma_f32_16x16x32_bf16 v[244:247], v[188:191], v[164:167], v[244:247]\n"
    "v_mfma_f32_16x16x32_bf16 v[240:243], v[188:191], v[160:163], v[240:243]\n"
    "v_mfma_f32_16x16x32_bf16 v[240:243], v[156:159], v[128:131], v[240:243]\n"
    "s_waitcnt vmcnt(7)\n"
    "s_barrier\n"
    "s_add_u32 m0, s35, 49152\n"
    "s_nop 0\n"
    "global_load_lds_dwordx4 v46, s[30:31] offset:0\n"
    "ds_read_b128 v[128:131], v58 offset:0\n"
    "ds_read_b128 v[132:135], v58 offset:2048\n"
    "ds_read_b128 v[136:139], v58 offset:4096\n"
    "ds_read_b128 v[140:143], v58 offset:6144\n"
    "global_load_lds_dwordx4 v47, s[30:31] offset:1024\n"
    "s_add_u32 s30, s30, 212992\n"
    "s_addc_u32 s31, s31, 0\n"
    "ds_read_b64_tr_b16 v[144:145], v80 offset:0\n"
    "ds_read_b64_tr_b16 v[146:147], v80 offset:1024\n"
    "ds_read_b64_tr_b16 v[148:149], v81 offset:0\n"
    "ds_read_b64_tr_b16 v[150:151], v81 offset:1024\n"
    "s_add_u32 m0, s34, 49152\n"
    "s_nop 0\n"
    "global_load_lds_dwordx4 v52, s[36:37] offset:0\n"
    "ds_read_b64_tr_b16 v[152:153], v82 offset:0\n"
    "ds_read_b64_tr_b16 v[154:155], v82 offset:1024\n"
    "ds_read_b64_tr_b16 v[156:157], v83 offset:0\n"
    "ds_read_b64_tr_b16 v[158:159], v83 offset:1024\n"
    "global_load_lds_dwordx4 v53, s[36:37] offset:1024\n"
    "ds_read_b128 v[160:163], v59 offset:0\n"
    "ds_read_b128 v[164:167], v59 offset:2048\n"
    "ds_read_b128 v[168:171], v59 offset:4096\n"
    "ds_read_b128 v[172:175], v59 offset:6144\n"
    "global_load_lds_dwordx4 v54, s[36:37] offset:2048\n"
    "ds_read_b64_tr_b16 v[176:177], v80 offset:8192\n"
    "ds_read_b64_tr_b16 v[178:179], v80 offset:9216\n"
    "ds_read_b64_tr_b16 v[180:181], v81 offset:8192\n"
    "ds_read_b64_tr_b16 v[182:183], v81 offset:9216\n"
    "global_load_lds_dwordx4 v55, s[36:37] offset:3072\n"
    "ds_read_b64_tr_b16 v[184:185], v82 offset:8192\n"
    "ds_read_b64_tr_b16 v[186:187], v82 offset:9216\n"
    "ds_read_b64_tr_b16 v[188:189], v83 offset:8192\n"
    "ds_read_b64_tr_b16 v[190:191], v83 offset:9216\n"
    "s_waitcnt vmcnt(10) lgkmcnt(0)\n"
    "s_barrier\n"
    "v_mfma_f32_16x16x32_bf16 v[192:195], v[144:147], v[128:131], v[192:195]\n"
    "v_lshlrev_b32 v108, 7, v108\n"
    "v_mfma_f32_16x16x32_bf16 v[192:195], v[176:179], v[160:163], v[192:195]\n"
    "v_mfma_f32_16x16x32_bf16 v[196:199], v[176:179], v[164:167], v[196:199]\n"
    "v_add_u32_dpp v52, v107, v48 quad_perm:[0,0,0,0] row_mask:0xf bank_mask:0xf\n"
    "v_mfma_f32_16x16x32_bf16 v[196:199], v[144:147], v[132:135], v[196:199]\n"
    "v_mfma_f32_16x16x32_bf16 v[200:203], v[144:147], v[136:139], v[200:203]\n"
    "v_add_u32_dpp v53, v107, v49 quad_perm:[1,1,1,1] row_mask:0xf bank_mask:0xf\n"
    "v_mfma_f32_16x16x32_bf16 v[200:203], v[176:179], v[168:171], v[200:203]\n"
    "v_mfma_f32_16x16x32_bf16 v[204:207], v[176:179], v[172:175], v[204:207]\n"
    "v_add_u32_dpp v54, v107, v50 quad_perm:[2,2,2,2] row_mask:0xf bank_mask:0xf\n"
    "v_mfma_f32_16x16x32_bf16 v[204:207], v[144:147], v[140:143], v[204:207]\n"
    "v_mfma_f32_16x16x32_bf16 v[220:223], v[148:151], v[140:143], v[220:223]\n"
    "v_add_u32_dpp v55, v107, v51 quad_perm:[3,3,3,3] row_mask:0xf bank_mask:0xf\n"
    "v_mfma_f32_16x16x32_bf16 v[220:223], v[180:183], v[172:175], v[220:223]\n"
    "v_mfma_f32_16x16x32_bf16 v[216:219], v[180:183], v[168:171], v[216:219]\n"
    "v_lshrrev_b32 v14, 26, v14\n"
    "v_mfma_f32_16x16x32_bf16 v[216:219], v[148:151], v[136:139], v[216:219]\n"
    "v_mfma_f32_16x16x32_bf16 v[212:215], v[148:151], v[132:135], v[212:215]\n"
    "s_add_u32 s40, s22, 3\n"
    "v_mfma_f32_16x16x32_bf16 v[212:215], v[180:183], v[164:167], v[212:215]\n"
    "v_mfma_f32_16x16x32_bf16 v[208:211], v[180:183], v[160:163], v[208:211]\n"
    "s_and_b32 s40, s40, 3\n"
    "v_mfma_f32_16x16x32_bf16 v[208:211], v[148:151], v[128:131], v[208:211]\n"
    "v_mfma_f32_16x16x32_bf16 v[224:227], v[152:155], v[128:131], v[224:227]\n"
    "s_lshl2_add_u32 s40, s40, s22\n"
    "v_mfma_f32_16x16x32_bf16 v[224:227], v[184:187], v[160:163], v[224:227]\n"
    "v_mfma_f32_16x16x32_bf16 v[228:231], v[184:187], v[164:167], v[228:231]\n"
    "s_add_u32 s40, s40, s58\n"
    "v_mfma_f32_16x16x32_bf16 v[228:231], v[152:155], v[132:135], v[228:231]\n"
    "v_mfma_f32_16x16x32_bf16 v[232:235], v[152:155], v[136:139], v[232:235]\n"
    "s_lshl_b32 s40, s40, 2\n"
    "v_mfma_f32_16x16x32_bf16 v[232:235], v[184:187], v[168:171], v[232:235]\n"
    "v_mfma_f32_16x16x32_bf16 v[236:239], v[184:187], v[172:175], v[236:239]\n"
    "s_add_u32 s40, s40, -450048\n"
    "v_mfma_f32_16x16x32_bf16 v[236:239], v[152:155], v[140:143], v[236:239]\n"
    "v_mfma_f32_16x16x32_bf16 v[252:255], v[156:159], v[140:143], v[252:255]\n"
    "v_add_u32 v14, s40, v14\n"
    "v_mfma_f32_16x16x32_bf16 v[252:255], v[188:191], v[172:175], v[252:255]\n"
    "v_mfma_f32_16x16x32_bf16 v[248:251], v[188:191], v[168:171], v[248:251]\n"
    "v_and_b32 v2, 1, v1\n"
    "v_mfma_f32_16x16x32_bf16 v[248:251], v[156:159], v[136:139], v[248:251]\n"
    "v_mfma_f32_16x16x32_bf16 v[244:247], v[156:159], v[132:135], v[244:247]\n"
    "v_mul_u32_u24 v2, 0xaaaa, v2\n"
    "v_mfma_f32_16x16x32_bf16 v[244:247], v[188:191], v[164:167], v[244:247]\n"
    "v_mfma_f32_16x16x32_bf16 v[240:243], v[188:191], v[160:163], v[240:243]\n"
    "v_mfma_f32_16x16x32_bf16 v[240:243], v[156:159], v[128:131], v[240:243]\n"
    "s_waitcnt vmcnt(6)\n"
    "s_barrier\n"
    "s_add_u32 m0, s35, 98304\n"
    "s_nop 0\n"
    "global_load_lds_dwordx4 v46, s[30:31] offset:0\n"
    "ds_read_b128 v[128:131], v56 offset:0\n"
    "ds_read_b128 v[132:135], v56 offset:2048\n"
    "ds_read_b128 v[136:139], v56 offset:4096\n"
    "ds_read_b128 v[140:143], v56 offset:6144\n"
    "global_load_lds_dwordx4 v47, s[30:31] offset:1024\n"
    "s_bfe_u32 s66, s28, 0x80010\n"
    "s_mul_i32 s66, s66, 53248\n"
    "s_add_u32 s30, s6, s66\n"
    "s_addc_u32 s31, s7, 0\n"
    "ds_read_b64_tr_b16 v[144:145], v76 offset:0\n"
    "ds_read_b64_tr_b16 v[146:147], v76 offset:1024\n"
    "ds_read_b64_tr_b16 v[148:149], v77 offset:0\n"
    "ds_read_b64_tr_b16 v[150:151], v77 offset:1024\n"
    "s_add_u32 m0, s34, 98304\n"
    "s_nop 0\n"
    "global_load_lds_dwordx4 v52, s[36:37] offset:0\n"
    "ds_read_b64_tr_b16 v[152:153], v78 offset:0\n"
    "ds_read_b64_tr_b16 v[154:155], v78 offset:1024\n"
    "ds_read_b64_tr_b16 v[156:157], v79 offset:0\n"
    "ds_read_b64_tr_b16 v[158:159], v79 offset:1024\n"
    "global_load_lds_dwordx4 v53, s[36:37] offset:1024\n"
    "ds_read_b128 v[160:163], v57 offset:0\n"
    "ds_read_b128 v[164:167], v57 offset:2048\n"
    "ds_read_b128 v[168:171], v57 offset:4096\n"
    "ds_read_b128 v[172:175], v57 offset:6144\n"
    "global_load_lds_dwordx4 v54, s[36:37] offset:2048\n"
    "ds_read_b64_tr_b16 v[176:177], v76 offset:8192\n"
    "ds_read_b64_tr_b16 v[178:179], v76 offset:9216\n"
    "ds_read_b64_tr_b16 v[180:181], v77 offset:8192\n"
    "ds_read_b64_tr_b16 v[182:183], v77 offset:9216\n"
    "global_load_lds_dwordx4 v55, s[36:37] offset:3072\n"
    "ds_read_b64_tr_b16 v[184:185], v78 offset:8192\n"
    "ds_read_b64_tr_b16 v[186:187], v78 offset:9216\n"
    "ds_read_b64_tr_b16 v[188:189], v79 offset:8192\n"
    "ds_read_b64_tr_b16 v[190:191], v79 offset:9216\n"
    "s_waitcnt vmcnt(10) lgkmcnt(0)\n"
    "s_barrier\n"
    "v_mfma_f32_16x16x32_bf16 v[192:195], v[144:147], v[128:131], v[192:195]\n"
    "v_lshlrev_b32 v109, 7, v109\n"
    "v_mfma_f32_16x16x32_bf16 v[192:195], v[176:179], v[160:163], v[192:195]\n"
    "v_mfma_f32_16x16x32_bf16 v[196:199], v[176:179], v[164:167], v[196:199]\n"
    "v_add_u32_dpp v52, v108, v48 quad_perm:[0,0,0,0] row_mask:0xf bank_mask:0xf\n"
    "v_mfma_f32_16x16x32_bf16 v[196:199], v[144:147], v[132:135], v[196:199]\n"
    "v_mfma_f32_16x16x32_bf16 v[200:203], v[144:147], v[136:139], v[200:203]\n"
    "v_add_u32_dpp v53, v108, v49 quad_perm:[1,1,1,1] row_mask:0xf bank_mask:0xf\n"
    "v_mfma_f32_16x16x32_bf16 v[200:203], v[176:179], v[168:171], v[200:203]\n"
    "v_mfma_f32_16x16x32_bf16 v[204:207], v[176:179], v[172:175], v[204:207]\n"
    "v_add_u32_dpp v54, v108, v50 quad_perm:[2,2,2,2] row_mask:0xf bank_mask:0xf\n"
    "v_mfma_f32_16x16x32_bf16 v[204:207], v[144:147], v[140:143], v[204:207]\n"
    "v_mfma_f32_16x16x32_bf16 v[220:223], v[148:151], v[140:143], v[220:223]\n"
    "v_add_u32_dpp v55, v108, v51 quad_perm:[3,3,3,3] row_mask:0xf bank_mask:0xf\n"
    "v_mfma_f32_16x16x32_bf16 v[220:223], v[180:183], v[172:175], v[220:223]\n"
    "v_mfma_f32_16x16x32_bf16 v[216:219], v[180:183], v[168:171], v[216:219]\n"
    "v_add_u32 v2, 0x4444, v2\n"
    "v_mfma_f32_16x16x32_bf16 v[216:219], v[148:151], v[136:139], v[216:219]\n"
    "v_mfma_f32_16x16x32_bf16 v[212:215], v[148:151], v[132:135], v[212:215]\n"
    "v_mad_u32_u24 v15, v17, 36, v19\n"
    "v_mfma_f32_16x16x32_bf16 v[212:215], v[180:183], v[164:167], v[212:215]\n"
    "v_mfma_f32_16x16x32_bf16 v[208:211], v[180:183], v[160:163], v[208:211]\n"
    "v_mad_u32_u24 v16, v18, 36, v20\n"
    "v_mfma_f32_16x16x32_bf16 v[208:211], v[148:151], v[128:131], v[208:211]\n"
    "v_mfma_f32_16x16x32_bf16 v[224:227], v[152:155], v[128:131], v[224:227]\n"
    "s_lshl4_add_u32 s41, s21, s22\n"
    "v_mfma_f32_16x16x32_bf16 v[224:227], v[184:187], v[160:163], v[224:227]\n"
    "v_mfma_f32_16x16x32_bf16 v[228:231], v[184:187], v[164:167], v[228:231]\n"
    "v_lshrrev_b32 v12, 5, v19\n"
    "v_mfma_f32_16x16x32_bf16 v[228:231], v[152:155], v[132:135], v[228:231]\n"
    "v_mfma_f32_16x16x32_bf16 v[232:235], v[152:155], v[136:139], v[232:235]\n"
    "v_sub_u32 v12, 1, v12\n"
    "v_mfma_f32_16x16x32_bf16 v[232:235], v[184:187], v[168:171], v[232:235]\n"
    "v_mfma_f32_16x16x32_bf16 v[236:239], v[184:187], v[172:175], v[236:239]\n"
    "v_and_b32 v12, 3, v12\n"
    "v_mfma_f32_16x16x32_bf16 v[236:239], v[152:155], v[140:143], v[236:239]\n"
    "v_mfma_f32_16x16x32_bf16 v[252:255], v[156:159], v[140:143], v[252:255]\n"
    "v_add_lshl_u32 v12, v12, s41, 12\n"
    "v_mfma_f32_16x16x32_bf16 v[252:255], v[188:191], v[172:175], v[252:255]\n"
    "v_mfma_f32_16x16x32_bf16 v[248:251], v[188:191], v[168:171], v[248:251]\n"
    "v_add_u32 v15, v12, v15\n"
    "v_mfma_f32_16x16x32_bf16 v[248:251], v[156:159], v[136:139], v[248:251]\n"
    "v_mfma_f32_16x16x32_bf16 v[244:247], v[156:159], v[132:135], v[244:247]\n"
    "v_lshrrev_b32 v12, 5, v20\n"
    "v_mfma_f32_16x16x32_bf16 v[244:247], v[188:191], v[164:167], v[244:247]\n"
    "v_mfma_f32_16x16x32_bf16 v[240:243], v[188:191], v[160:163], v[240:243]\n"
    "v_mfma_f32_16x16x32_bf16 v[240:243], v[156:159], v[128:131], v[240:243]\n"
    "s_waitcnt vmcnt(6)\n"
    "s_barrier\n"
    "s_add_u32 m0, s35, 0\n"
    "s_nop 0\n"
    "global_load_lds_dwordx4 v46, s[30:31] offset:0\n"
    "ds_read_b128 v[128:131], v56 offset:49152\n"
    "ds_read_b128 v[132:135], v56 offset:51200\n"
    "ds_read_b128 v[136:139], v56 offset:53248\n"
    "ds_read_b128 v[140:143], v56 offset:55296\n"
    "global_load_lds_dwordx4 v47, s[30:31] offset:1024\n"
    "s_add_u32 s30, s30, 212992\n"
    "s_addc_u32 s31, s31, 0\n"
    "ds_read_b64_tr_b16 v[144:145], v76 offset:49152\n"
    "ds_read_b64_tr_b16 v[146:147], v76 offset:50176\n"
    "ds_read_b64_tr_b16 v[148:149], v77 offset:49152\n"
    "ds_read_b64_tr_b16 v[150:151], v77 offset:50176\n"
    "s_add_u32 m0, s34, 0\n"
    "s_nop 0\n"
    "global_load_lds_dwordx4 v52, s[36:37] offset:0\n"
    "ds_read_b64_tr_b16 v[152:153], v78 offset:49152\n"
    "ds_read_b64_tr_b16 v[154:155], v78 offset:50176\n"
    "ds_read_b64_tr_b16 v[156:157], v79 offset:49152\n"
    "ds_read_b64_tr_b16 v[158:159], v79 offset:50176\n"
    "global_load_lds_dwordx4 v53, s[36:37] offset:1024\n"
    "ds_read_b128 v[160:163], v57 offset:49152\n"
    "ds_read_b128 v[164:167], v57 offset:51200\n"
    "ds_read_b128 v[168:171], v57 offset:53248\n"
    "ds_read_b128 v[172:175], v57 offset:55296\n"
    "global_load_lds_dwordx4 v54, s[36:37] offset:2048\n"
    "ds_read_b64_tr_b16 v[176:177], v76 offset:57344\n"
    "ds_read_b64_tr_b16 v[178:179], v76 offset:58368\n"
    "ds_read_b64_tr_b16 v[180:181], v77 offset:57344\n"
    "ds_read_b64_tr_b16 v[182:183], v77 offset:58368\n"
    "global_load_lds_dwordx4 v55, s[36:37] offset:3072\n"
    "ds_read_b64_tr_b16 v[184:185], v78 offset:57344\n"
    "ds_read_b64_tr_b16 v[186:187], v78 offset:58368\n"
    "ds_read_b64_tr_b16 v[188:189], v79 offset:57344\n"
    "ds_read_b64_tr_b16 v[190:191], v79 offset:58368\n"
    "s_waitcnt vmcnt(10) lgkmcnt(0)\n"
    "s_barrier\n"
    "v_mfma_f32_16x16x32_bf16 v[192:195], v[144:147], v[128:131], v[192:195]\n"
    "v_lshlrev_b32 v110, 7, v110\n"
    "v_mfma_f32_16x16x32_bf16 v[192:195], v[176:179], v[160:163], v[192:195]\n"
    "v_mfma_f32_16x16x32_bf16 v[196:199], v[176:179], v[164:167], v[196:199]\n"
    "v_add_u32_dpp v52, v109, v48 quad_perm:[0,0,0,0] row_mask:0xf bank_mask:0xf\n"
    "v_mfma_f32_16x16x32_bf16 v[196:199], v[144:147], v[132:135], v[196:199]\n"
    "v_mfma_f32_16x16x32_bf16 v[200:203], v[144:147], v[136:139], v[200:203]\n"
    "v_add_u32_dpp v53, v109, v49 quad_perm:[1,1,1,1] row_mask:0xf bank_mask:0xf\n"
    "v_mfma_f32_16x16x32_bf16 v[200:203], v[176:179], v[168:171], v[200:203]\n"
    "v_mfma_f32_16x16x32_bf16 v[204:207], v[176:179], v[172:175], v[204:207]\n"
    "v_add_u32_dpp v54, v109, v50 quad_perm:[2,2,2,2] row_mask:0xf bank_mask:0xf\n"
    "v_mfma_f32_16x16x32_bf16 v[204:207], v[144:147], v[140:143], v[204:207]\n"
    "v_mfma_f32_16x16x32_bf16 v[220:223], v[148:151], v[140:143], v[220:223]\n"
    "v_add_u32_dpp v55, v109, v51 quad_perm:[3,3,3,3] row_mask:0xf bank_mask:0xf\n"
    "v_mfma_f32_16x16x32_bf16 v[220:223], v[180:183], v[172:175], v[220:223]\n"
    "v_mfma_f32_16x16x32_bf16 v[216:219], v[180:183], v[168:171], v[216:219]\n"
    "v_sub_u32 v12, 1, v12\n"
    "v_mfma_f32_16x16x32_bf16 v[216:219], v[148:151], v[136:139], v[216:219]\n"
    "v_mfma_f32_16x16x32_bf16 v[212:215], v[148:151], v[132:135], v[212:215]\n"
    "v_and_b32 v12, 3, v12\n"
    "v_mfma_f32_16x16x32_bf16 v[212:215], v[180:183], v[164:167], v[212:215]\n"
    "v_mfma_f32_16x16x32_bf16 v[208:211], v[180:183], v[160:163], v[208:211]\n"
    "v_add_lshl_u32 v12, v12, s41, 12\n"
    "v_mfma_f32_16x16x32_bf16 v[208:211], v[148:151], v[128:131], v[208:211]\n"
    "v_mfma_f32_16x16x32_bf16 v[224:227], v[152:155], v[128:131], v[224:227]\n"
    "v_add_u32 v16, v12, v16\n"
    "v_mfma_f32_16x16x32_bf16 v[224:227], v[184:187], v[160:163], v[224:227]\n"
    "v_mfma_f32_16x16x32_bf16 v[228:231], v[184:187], v[164:167], v[228:231]\n"
    "s_mul_i32 s41, s47, 13\n"
    "v_mfma_f32_16x16x32_bf16 v[228:231], v[152:155], v[132:135], v[228:231]\n"
    "v_mfma_f32_16x16x32_bf16 v[232:235], v[152:155], v[136:139], v[232:235]\n"
    "s_sub_u32 s41, s3, s41\n"
    "v_mfma_f32_16x16x32_bf16 v[232:235], v[184:187], v[168:171], v[232:235]\n"
    "v_mfma_f32_16x16x32_bf16 v[236:239], v[184:187], v[172:175], v[236:239]\n"
    "s_lshl1_add_u32 s41, s41, s21\n"
    "v_mfma_f32_16x16x32_bf16 v[236:239], v[152:155], v[140:143], v[236:239]\n"
    "v_mfma_f32_16x16x32_bf16 v[252:255], v[156:159], v[140:143], v[252:255]\n"
    "s_mul_i32 s41, s41, 3\n"
    "v_mfma_f32_16x16x32_bf16 v[252:255], v[188:191], v[172:175], v[252:255]\n"
    "v_mfma_f32_16x16x32_bf16 v[248:251], v[188:191], v[168:171], v[248:251]\n"
    "s_add_u32 s41, s41, s22\n"
    "v_mfma_f32_16x16x32_bf16 v[248:251], v[156:159], v[136:139], v[248:251]\n"
    "v_mfma_f32_16x16x32_bf16 v[244:247], v[156:159], v[132:135], v[244:247]\n"
    "v_mov_b32 v24, s41\n"
    "v_mfma_f32_16x16x32_bf16 v[244:247], v[188:191], v[164:167], v[244:247]\n"
    "v_mfma_f32_16x16x32_bf16 v[240:243], v[188:191], v[160:163], v[240:243]\n"
    "v_mfma_f32_16x16x32_bf16 v[240:243], v[156:159], v[128:131], v[240:243]\n"
    "s_waitcnt vmcnt(6)\n"
    "s_barrier\n"
    "s_add_u32 m0, s35, 49152\n"
    "s_nop 0\n"
    "global_load_lds_dwordx4 v46, s[30:31] offset:0\n"
    "ds_read_b128 v[128:131], v58 offset:0\n"
    "ds_read_b128 v[132:135], v58 offset:2048\n"
    "ds_read_b128 v[136:139], v58 offset:4096\n"
    "ds_read_b128 v[140:143], v58 offset:6144\n"
    "global_load_lds_dwordx4 v47, s[30:31] offset:1024\n"
    "s_add_u32 s30, s30, 212992\n"
    "s_addc_u32 s31, s31, 0\n"
    "ds_read_b64_tr_b16 v[144:145], v80 offset:0\n"
    "ds_read_b64_tr_b16 v[146:147], v80 offset:1024\n"
    "ds_read_b64_tr_b16 v[148:149], v81 offset:0\n"
    "ds_read_b64_tr_b16 v[150:151], v81 offset:1024\n"
    "s_add_u32 m0, s34, 49152\n"
    "s_nop 0\n"
    "global_load_lds_dwordx4 v52, s[36:37] offset:0\n"
    "ds_read_b64_tr_b16 v[152:153], v82 offset:0\n"
    "ds_read_b64_tr_b16 v[154:155], v82 offset:1024\n"
    "ds_read_b64_tr_b16 v[156:157], v83 offset:0\n"
    "ds_read_b64_tr_b16 v[158:159], v83 offset:1024\n"
    "global_load_lds_dwordx4 v53, s[36:37] offset:1024\n"
    "ds_read_b128 v[160:163], v59 offset:0\n"
    "ds_read_b128 v[164:167], v59 offset:2048\n"
    "ds_read_b128 v[168:171], v59 offset:4096\n"
    "ds_read_b128 v[172:175], v59 offset:6144\n"
    "global_load_lds_dwordx4 v54, s[36:37] offset:2048\n"
    "ds_read_b64_tr_b16 v[176:177], v80 offset:8192\n"
    "ds_read_b64_tr_b16 v[178:179], v80 offset:9216\n"
    "ds_read_b64_tr_b16 v[180:181], v81 offset:8192\n"
    "ds_read_b64_tr_b16 v[182:183], v81 offset:9216\n"
    "global_load_lds_dwordx4 v55, s[36:37] offset:3072\n"
    "ds_read_b64_tr_b16 v[184:185], v82 offset:8192\n"
    "ds_read_b64_tr_b16 v[186:187], v82 offset:9216\n"
    "ds_read_b64_tr_b16 v[188:189], v83 offset:8192\n"
    "ds_read_b64_tr_b16 v[190:191], v83 offset:9216\n"
    "s_waitcnt vmcnt(10) lgkmcnt(0)\n"
    "s_barrier\n"
    "v_mfma_f32_16x16x32_bf16 v[192:195], v[144:147], v[128:131], v[192:195]\n"
    "v_lshlrev_b32 v111, 7, v111\n"
    "v_mfma_f32_16x16x32_bf16 v[192:195], v[176:179], v[160:163], v[192:195]\n"
    "v_mfma_f32_16x16x32_bf16 v[196:199], v[176:179], v[164:167], v[196:199]\n"
    "v_add_u32_dpp v52, v110, v48 quad_perm:[0,0,0,0] row_mask:0xf bank_mask:0xf\n"
    "v_mfma_f32_16x16x32_bf16 v[196:199], v[144:147], v[132:135], v[196:199]\n"
    "v_mfma_f32_16x16x32_bf16 v[200:203], v[144:147], v[136:139], v[200:203]\n"
    "v_add_u32_dpp v53, v110, v49 quad_perm:[1,1,1,1] row_mask:0xf bank_mask:0xf\n"
    "v_mfma_f32_16x16x32_bf16 v[200:203], v[176:179], v[168:171], v[200:203]\n"
    "v_mfma_f32_16x16x32_bf16 v[204:207], v[176:179], v[172:175], v[204:207]\n"
    "v_add_u32_dpp v54, v110, v50 quad_perm:[2,2,2,2] row_mask:0xf bank_mask:0xf\n"
    "v_mfma_f32_16x16x32_bf16 v[204:207], v[144:147], v[140:143], v[204:207]\n"
    "v_mfma_f32_16x16x32_bf16 v[220:223], v[148:151], v[140:143], v[220:223]\n"
    "v_add_u32_dpp v55, v110, v51 quad_perm:[3,3,3,3] row_mask:0xf bank_mask:0xf\n"
    "v_mfma_f32_16x16x32_bf16 v[220:223], v[180:183], v[172:175], v[220:223]\n"
    "v_mfma_f32_16x16x32_bf16 v[216:219], v[180:183], v[168:171], v[216:219]\n"
    "v_mfma_f32_16x16x32_bf16 v[216:219], v[148:151], v[136:139], v[216:219]\n"
    "v_readfirstlane_b32 s40, v24\n"
    "v_mfma_f32_16x16x32_bf16 v[212:215], v[148:151], v[132:135], v[212:215]\n"
    "v_mfma_f32_16x16x32_bf16 v[212:215], v[180:183], v[164:167], v[212:215]\n"
    "s_or_b32 s40, s40, s44\n"
    "v_mfma_f32_16x16x32_bf16 v[208:211], v[180:183], v[160:163], v[208:211]\n"
    "v_mfma_f32_16x16x32_bf16 v[208:211], v[148:151], v[128:131], v[208:211]\n"
    "s_lshl1_add_u32 s41, s40, s54\n"
    "v_mfma_f32_16x16x32_bf16 v[224:227], v[152:155], v[128:131], v[224:227]\n"
    "v_mfma_f32_16x16x32_bf16 v[224:227], v[184:187], v[160:163], v[224:227]\n"
    "s_cmpk_lt_u32 s40, 256\n"
    "v_mfma_f32_16x16x32_bf16 v[228:231], v[184:187], v[164:167], v[228:231]\n"
    "v_mfma_f32_16x16x32_bf16 v[228:231], v[152:155], v[132:135], v[228:231]\n"
    "s_cselect_b32 s41, s41, 2\n"
    "v_mfma_f32_16x16x32_bf16 v[232:235], v[152:155], v[136:139], v[232:235]\n"
    "v_mfma_f32_16x16x32_bf16 v[232:235], v[184:187], v[168:171], v[232:235]\n"
    "v_mfma_f32_16x16x32_bf16 v[236:239], v[184:187], v[172:175], v[236:239]\n"
    "v_mov_b32 v24, s41\n"
    "v_mfma_f32_16x16x32_bf16 v[236:239], v[152:155], v[140:143], v[236:239]\n"
    "v_mfma_f32_16x16x32_bf16 v[252:255], v[156:159], v[140:143], v[252:255]\n"
    "s_mul_i32 s41, s41, 52\n"
    "v_mfma_f32_16x16x32_bf16 v[252:255], v[188:191], v[172:175], v[252:255]\n"
    "v_mfma_f32_16x16x32_bf16 v[248:251], v[188:191], v[168:171], v[248:251]\n"
    "s_add_u32 s38, s4, s41\n"
    "v_mfma_f32_16x16x32_bf16 v[248:251], v[156:159], v[136:139], v[248:251]\n"
    "v_mfma_f32_16x16x32_bf16 v[244:247], v[156:159], v[132:135], v[244:247]\n"
    "s_addc_u32 s39, s5, 0\n"
    "v_mfma_f32_16x16x32_bf16 v[244:247], v[188:191], v[164:167], v[244:247]\n"
    "v_mfma_f32_16x16x32_bf16 v[240:243], v[188:191], v[160:163], v[240:243]\n"
    "v_mfma_f32_16x16x32_bf16 v[240:243], v[156:159], v[128:131], v[240:243]\n"
    "s_waitcnt vmcnt(6)\n"
    "s_barrier\n"
    "s_add_u32 m0, s35, 98304\n"
    "s_nop 0\n"
    "global_load_lds_dwordx4 v46, s[30:31] offset:0\n"
    "ds_read_b128 v[128:131], v56 offset:0\n"
    "ds_read_b128 v[132:135], v56 offset:2048\n"
    "ds_read_b128 v[136:139], v56 offset:4096\n"
    "global_load_lds_dwordx4 v47, s[30:31] offset:1024\n"
    "s_add_u32 s30, s30, 212992\n"
    "s_addc_u32 s31, s31, 0\n"
    "ds_read_b128 v[140:143], v56 offset:6144\n"
    "ds_read_b64_tr_b16 v[144:145], v76 offset:0\n"
    "ds_read_b64_tr_b16 v[146:147], v76 offset:1024\n"
    "s_add_u32 m0, s34, 98304\n"
    "s_nop 0\n"
    "global_load_lds_dwordx4 v52, s[36:37] offset:0\n"
    "ds_read_b64_tr_b16 v[148:149], v77 offset:0\n"
    "ds_read_b64_tr_b16 v[150:151], v77 offset:1024\n"
    "ds_read_b64_tr_b16 v[152:153], v78 offset:0\n"
    "global_load_lds_dwordx4 v53, s[36:37] offset:1024\n"
    "ds_read_b64_tr_b16 v[154:155], v78 offset:1024\n"
    "ds_read_b64_tr_b16 v[156:157], v79 offset:0\n"
    "ds_read_b64_tr_b16 v[158:159], v79 offset:1024\n"
    "global_load_lds_dwordx4 v54, s[36:37] offset:2048\n"
    "ds_read_b128 v[160:163], v57 offset:0\n"
    "ds_read_b128 v[164:167], v57 offset:2048\n"
    "ds_read_b128 v[168:171], v57 offset:4096\n"
    "global_load_lds_dwordx4 v55, s[36:37] offset:3072\n"
    "ds_read_b128 v[172:175], v57 offset:6144\n"
    "ds_read_b64_tr_b16 v[176:177], v76 offset:8192\n"
    "ds_read_b64_tr_b16 v[178:179], v76 offset:9216\n"
    "global_load_dwordx3 v[36:38], v17, s[38:39] offset:-104\n"
    "ds_read_b64_tr_b16 v[180:181], v77 offset:8192\n"
    "ds_read_b64_tr_b16 v[182:183], v77 offset:9216\n"
    "ds_read_b64_tr_b16 v[184:185], v78 offset:8192\n"
    "ds_read_b64_tr_b16 v[186:187], v78 offset:9216\n"
    "ds_read_b64_tr_b16 v[188:189], v79 offset:8192\n"
    "ds_read_b64_tr_b16 v[190:191], v79 offset:9216\n"
    "ds_read_b64 v[84:85], v42 offset:0\n"
    "ds_read_b64 v[86:87], v42 offset:32\n"
    "ds_read_b64 v[88:89], v42 offset:64\n"
    "ds_read_b64 v[90:91], v42 offset:96\n"
    "s_waitcnt vmcnt(11) lgkmcnt(0)\n"
    "s_barrier\n"
    "v_mfma_f32_16x16x32_bf16 v[192:195], v[144:147], v[128:131], v[192:195]\n"
    "v_lshlrev_b32 v112, 7, v112\n"
    "v_mfma_f32_16x16x32_bf16 v[192:195], v[176:179], v[160:163], v[192:195]\n"
    "v_add_u32_dpp v52, v111, v48 quad_perm:[0,0,0,0] row_mask:0xf bank_mask:0xf\n"
    "v_mfma_f32_16x16x32_bf16 v[196:199], v[176:179], v[164:167], v[196:199]\n"
    "v_add_u32_dpp v53, v111, v49 quad_perm:[1,1,1,1] row_mask:0xf bank_mask:0xf\n"
    "v_mfma_f32_16x16x32_bf16 v[196:199], v[144:147], v[132:135], v[196:199]\n"
    "v_add_u32_dpp v54, v111, v50 quad_perm:[2,2,2,2] row_mask:0xf bank_mask:0xf\n"
    "v_mfma_f32_16x16x32_bf16 v[200:203], v[144:147], v[136:139], v[200:203]\n"
    "v_add_u32_dpp v55, v111, v51 quad_perm:[3,3,3,3] row_mask:0xf bank_mask:0xf\n"
    "v_mfma_f32_16x16x32_bf16 v[200:203], v[176:179], v[168:171], v[200:203]\n"
    "v_lshlrev_b32 v60, 16, v84\n"
    "v_mfma_f32_16x16x32_bf16 v[204:207], v[176:179], v[172:175], v[204:207]\n"
    "v_and_b32 v61, 0xffff0000, v84\n"
    "v_mfma_f32_16x16x32_bf16 v[204:207], v[144:147], v[140:143], v[204:207]\n"
    "v_mfma_f32_16x16x32_bf16 v[220:223], v[148:151], v[140:143], v[220:223]\n"
    "v_lshlrev_b32 v62, 16, v85\n"
    "v_mfma_f32_16x16x32_bf16 v[220:223], v[180:183], v[172:175], v[220:223]\n"
    "v_and_b32 v63, 0xffff0000, v85\n"
    "v_mfma_f32_16x16x32_bf16 v[216:219], v[180:183], v[168:171], v[216:219]\n"
    "v_lshlrev_b32 v64, 16, v86\n"
    "v_mfma_f32_16x16x32_bf16 v[216:219], v[148:151], v[136:139], v[216:219]\n"
    "v_and_b32 v65, 0xffff0000, v86\n"
    "v_mfma_f32_16x16x32_bf16 v[212:215], v[148:151], v[132:135], v[212:215]\n"
    "v_lshlrev_b32 v66, 16, v87\n"
    "v_mfma_f32_16x16x32_bf16 v[212:215], v[180:183], v[164:167], v[212:215]\n"
    "v_and_b32 v67, 0xffff0000, v87\n"
    "v_mfma_f32_16x16x32_bf16 v[208:211], v[180:183], v[160:163], v[208:211]\n"
    "v_lshlrev_b32 v68, 16, v88\n"
    "v_mfma_f32_16x16x32_bf16 v[208:211], v[148:151], v[128:131], v[208:211]\n"
    "v_mfma_f32_16x16x32_bf16 v[224:227], v[152:155], v[128:131], v[224:227]\n"
    "v_and_b32 v69, 0xffff0000, v88\n"
    "v_mfma_f32_16x16x32_bf16 v[224:227], v[184:187], v[160:163], v[224:227]\n"
    "v_lshlrev_b32 v70, 16, v89\n"
    "v_mfma_f32_16x16x32_bf16 v[228:231], v[184:187], v[164:167], v[228:231]\n"
    "v_and_b32 v71, 0xffff0000, v89\n"
    "v_mfma_f32_16x16x32_bf16 v[228:231], v[152:155], v[132:135], v[228:231]\n"
    "v_lshlrev_b32 v72, 16, v90\n"
    "v_mfma_f32_16x16x32_bf16 v[232:235], v[152:155], v[136:139], v[232:235]\n"
    "v_and_b32 v73, 0xffff0000, v90\n"
    "v_mfma_f32_16x16x32_bf16 v[232:235], v[184:187], v[168:171], v[232:235]\n"
    "v_lshlrev_b32 v74, 16, v91\n"
    "v_mfma_f32_16x16x32_bf16 v[236:239], v[184:187], v[172:175], v[236:239]\n"
    "v_and_b32 v75, 0xffff0000, v91\n"
    "v_mfma_f32_16x16x32_bf16 v[236:239], v[152:155], v[140:143], v[236:239]\n"
    "v_mfma_f32_16x16x32_bf16 v[252:255], v[156:159], v[140:143], v[252:255]\n"
    "s_lshl3_add_u32 s40, s2, 4096\n"
    "v_mfma_f32_16x16x32_bf16 v[252:255], v[188:191], v[172:175], v[252:255]\n"
    "s_lshl_b32 s41, s2, 2\n"
    "v_mfma_f32_16x16x32_bf16 v[248:251], v[188:191], v[168:171], v[248:251]\n"
    "v_lshlrev_b32 v11, 3, v1\n"
    "v_mfma_f32_16x16x32_bf16 v[248:251], v[156:159], v[136:139], v[248:251]\n"
    "v_add_u32 v11, 40960, v11\n"
    "v_mfma_f32_16x16x32_bf16 v[244:247], v[156:159], v[132:135], v[244:247]\n"
    "v_writelane_b32 v11, s40, 8\n"
    "v_mfma_f32_16x16x32_bf16 v[244:247], v[188:191], v[164:167], v[244:247]\n"
    "v_writelane_b32 v11, s41, 9\n"
    "v_mfma_f32_16x16x32_bf16 v[240:243], v[188:191], v[160:163], v[240:243]\n"
    "v_mfma_f32_16x16x32_bf16 v[240:243], v[156:159], v[128:131], v[240:243]\n"
    "s_waitcnt vmcnt(7)\n"
    "s_barrier\n"
    "s_add_u32 m0, s35, 0\n"
    "s_nop 0\n"
    "global_load_lds_dwordx4 v46, s[30:31] offset:0\n"
    "ds_read_b128 v[128:131], v56 offset:49152\n"
    "ds_read_b128 v[132:135], v56 offset:51200\n"
    "ds_read_b128 v[136:139], v56 offset:53248\n"
    "global_load_lds_dwordx4 v47, s[30:31] offset:1024\n"
    "s_bfe_u32 s66, s28, 0x80018\n"
    "s_mul_i32 s66, s66, 53248\n"
    "s_add_u32 s30, s6, s66\n"
    "s_addc_u32 s31, s7, 0\n"
    "ds_read_b128 v[140:143], v56 offset:55296\n"
    "ds_read_b64_tr_b16 v[144:145], v76 offset:49152\n"
    "ds_read_b64_tr_b16 v[146:147], v76 offset:50176\n"
    "s_add_u32 m0, s34, 0\n"
    "s_nop 0\n"
    "global_load_lds_dwordx4 v52, s[36:37] offset:0\n"
    "ds_read_b64_tr_b16 v[148:149], v77 offset:49152\n"
    "ds_read_b64_tr_b16 v[150:151], v77 offset:50176\n"
    "ds_read_b64_tr_b16 v[152:153], v78 offset:49152\n"
    "global_load_lds_dwordx4 v53, s[36:37] offset:1024\n"
    "ds_read_b64_tr_b16 v[154:155], v78 offset:50176\n"
    "ds_read_b64_tr_b16 v[156:157], v79 offset:49152\n"
    "ds_read_b64_tr_b16 v[158:159], v79 offset:50176\n"
    "global_load_lds_dwordx4 v54, s[36:37] offset:2048\n"
    "ds_read_b128 v[160:163], v57 offset:49152\n"
    "ds_read_b128 v[164:167], v57 offset:51200\n"
    "ds_read_b128 v[168:171], v57 offset:53248\n"
    "global_load_lds_dwordx4 v55, s[36:37] offset:3072\n"
    "ds_read_b128 v[172:175], v57 offset:55296\n"
    "ds_read_b64_tr_b16 v[176:177], v76 offset:57344\n"
    "ds_read_b64_tr_b16 v[178:179], v76 offset:58368\n"
    "global_load_dword v41, v18, s[38:39] offset:-8\n"
    "ds_read_b64_tr_b16 v[180:181], v77 offset:57344\n"
    "ds_read_b64_tr_b16 v[182:183], v77 offset:58368\n"
    "ds_read_b64_tr_b16 v[184:185], v78 offset:57344\n"
    "ds_read_b64_tr_b16 v[186:187], v78 offset:58368\n"
    "ds_read_b64_tr_b16 v[188:189], v79 offset:57344\n"
    "ds_read_b64_tr_b16 v[190:191], v79 offset:58368\n"
    "s_waitcnt vmcnt(12) lgkmcnt(0)\n"
    "s_barrier\n"
    "v_mfma_f32_16x16x32_bf16 v[192:195], v[144:147], v[128:131], v[192:195]\n"
    "v_mfma_f32_16x16x32_bf16 v[192:195], v[176:179], v[160:163], v[192:195]\n"
    "v_lshlrev_b32 v113, 7, v113\n"
    "v_mfma_f32_16x16x32_bf16 v[196:199], v[176:179], v[164:167], v[196:199]\n"
    "v_mfma_f32_16x16x32_bf16 v[196:199], v[144:147], v[132:135], v[196:199]\n"
    "v_add_u32_dpp v52, v112, v48 quad_perm:[0,0,0,0] row_mask:0xf bank_mask:0xf\n"
    "v_mfma_f32_16x16x32_bf16 v[200:203], v[144:147], v[136:139], v[200:203]\n"
    "v_mfma_f32_16x16x32_bf16 v[200:203], v[176:179], v[168:171], v[200:203]\n"
    "v_add_u32_dpp v53, v112, v49 quad_perm:[1,1,1,1] row_mask:0xf bank_mask:0xf\n"
    "v_mfma_f32_16x16x32_bf16 v[204:207], v[176:179], v[172:175], v[204:207]\n"
    "v_mfma_f32_16x16x32_bf16 v[204:207], v[144:147], v[140:143], v[204:207]\n"
    "v_add_u32_dpp v54, v112, v50 quad_perm:[2,2,2,2] row_mask:0xf bank_mask:0xf\n"
    "v_mfma_f32_16x16x32_bf16 v[220:223], v[148:151], v[140:143], v[220:223]\n"
    "v_mfma_f32_16x16x32_bf16 v[220:223], v[180:183], v[172:175], v[220:223]\n"
    "v_add_u32_dpp v55, v112, v51 quad_perm:[3,3,3,3] row_mask:0xf bank_mask:0xf\n"
    "v_mfma_f32_16x16x32_bf16 v[216:219], v[180:183], v[168:171], v[216:219]\n"
    "v_mfma_f32_16x16x32_bf16 v[216:219], v[148:151], v[136:139], v[216:219]\n"
    "v_mfma_f32_16x16x32_bf16 v[212:215], v[148:151], v[132:135], v[212:215]\n"
    "v_mfma_f32_16x16x32_bf16 v[212:215], v[180:183], v[164:167], v[212:215]\n"
    "v_mfma_f32_16x16x32_bf16 v[208:211], v[180:183], v[160:163], v[208:211]\n"
    "v_add_f32 v192, v192, v60\n"
    "v_mfma_f32_16x16x32_bf16 v[208:211], v[148:151], v[128:131], v[208:211]\n"
    "v_add_f32 v193, v193, v61\n"
    "v_mfma_f32_16x16x32_bf16 v[224:227], v[152:155], v[128:131], v[224:227]\n"
    "v_add_f32 v194, v194, v62\n"
    "v_mfma_f32_16x16x32_bf16 v[224:227], v[184:187], v[160:163], v[224:227]\n"
    "v_add_f32 v195, v195, v63\n"
    "v_mfma_f32_16x16x32_bf16 v[228:231], v[184:187], v[164:167], v[228:231]\n"
    "v_add_f32 v196, v196, v60\n"
    "v_mfma_f32_16x16x32_bf16 v[228:231], v[152:155], v[132:135], v[228:231]\n"
    "v_add_f32 v197, v197, v61\n"
    "v_mfma_f32_16x16x32_bf16 v[232:235], v[152:155], v[136:139], v[232:235]\n"
    "v_add_f32 v198, v198, v62\n"
    "v_mfma_f32_16x16x32_bf16 v[232:235], v[184:187], v[168:171], v[232:235]\n"
    "v_add_f32 v199, v199, v63\n"
    "v_mfma_f32_16x16x32_bf16 v[236:239], v[184:187], v[172:175], v[236:239]\n"
    "v_add_f32 v200, v200, v60\n"
    "v_mfma_f32_16x16x32_bf16 v[236:239], v[152:155], v[140:143], v[236:239]\n"
    "v_add_f32 v201, v201, v61\n"
    "v_mfma_f32_16x16x32_bf16 v[252:255], v[156:159], v[140:143], v[252:255]\n"
    "v_add_f32 v202, v202, v62\n"
    "v_mfma_f32_16x16x32_bf16 v[252:255], v[188:191], v[172:175], v[252:255]\n"
    "v_add_f32 v203, v203, v63\n"
    "v_mfma_f32_16x16x32_bf16 v[248:251], v[188:191], v[168:171], v[248:251]\n"
    "v_add_f32 v204, v204, v60\n"
    "v_mfma_f32_16x16x32_bf16 v[248:251], v[156:159], v[136:139], v[248:251]\n"
    "v_add_f32 v205, v205, v61\n"
    "v_mfma_f32_16x16x32_bf16 v[244:247], v[156:159], v[132:135], v[244:247]\n"
    "v_add_f32 v206, v206, v62\n"
    "v_mfma_f32_16x16x32_bf16 v[244:247], v[188:191], v[164:167], v[244:247]\n"
    "v_add_f32 v207, v207, v63\n"
    "v_mfma_f32_16x16x32_bf16 v[240:243], v[188:191], v[160:163], v[240:243]\n"
    "v_mfma_f32_16x16x32_bf16 v[240:243], v[156:159], v[128:131], v[240:243]\n"
    "s_waitcnt vmcnt(8)\n"
    "s_barrier\n"
    "s_add_u32 m0, s35, 49152\n"
    "s_nop 0\n"
    "global_load_lds_dwordx4 v46, s[30:31] offset:0\n"
    "ds_read_b128 v[128:131], v58 offset:0\n"
    "ds_read_b128 v[132:135], v58 offset:2048\n"
    "ds_read_b128 v[136:139], v58 offset:4096\n"
    "global_load_lds_dwordx4 v47, s[30:31] offset:1024\n"
    "s_add_u32 s30, s30, 212992\n"
    "s_addc_u32 s31, s31, 0\n"
    "ds_read_b128 v[140:143], v58 offset:6144\n"
    "ds_read_b64_tr_b16 v[144:145], v80 offset:0\n"
    "ds_read_b64_tr_b16 v[146:147], v80 offset:1024\n"
    "s_add_u32 m0, s34, 49152\n"
    "s_nop 0\n"
    "global_load_lds_dwordx4 v52, s[36:37] offset:0\n"
    "ds_read_b64_tr_b16 v[148:149], v81 offset:0\n"
    "ds_read_b64_tr_b16 v[150:151], v81 offset:1024\n"
    "ds_read_b64_tr_b16 v[152:153], v82 offset:0\n"
    "global_load_lds_dwordx4 v53, s[36:37] offset:1024\n"
    "ds_read_b64_tr_b16 v[154:155], v82 offset:1024\n"
    "ds_read_b64_tr_b16 v[156:157], v83 offset:0\n"
    "ds_read_b64_tr_b16 v[158:159], v83 offset:1024\n"
    "global_load_lds_dwordx4 v54, s[36:37] offset:2048\n"
    "ds_read_b128 v[160:163], v59 offset:0\n"
    "ds_read_b128 v[164:167], v59 offset:2048\n"
    "ds_read_b128 v[168:171], v59 offset:4096\n"
    "global_load_lds_dwordx4 v55, s[36:37] offset:3072\n"
    "ds_read_b128 v[172:175], v59 offset:6144\n"
    "ds_read_b64_tr_b16 v[176:177], v80 offset:8192\n"
    "ds_read_b64_tr_b16 v[178:179], v80 offset:9216\n"
    "s_mov_b32 m0, s64\n"
    "s_nop 0\n"
    "global_load_lds_dword v43, s[18:19]\n"
    "ds_read_b64_tr_b16 v[180:181], v81 offset:8192\n"
    "ds_read_b64_tr_b16 v[182:183], v81 offset:9216\n"
    "ds_read_b64_tr_b16 v[184:185], v82 offset:8192\n"
    "ds_read_b64_tr_b16 v[186:187], v82 offset:9216\n"
    "ds_read_b64_tr_b16 v[188:189], v83 offset:8192\n"
    "ds_read_b64_tr_b16 v[190:191], v83 offset:9216\n"
    "s_waitcnt vmcnt(12) lgkmcnt(0)\n"
    "s_barrier\n"
    "v_mfma_f32_16x16x32_bf16 v[192:195], v[144:147], v[128:131], v[192:195]\n"
    "v_mfma_f32_16x16x32_bf16 v[192:195], v[176:179], v[160:163], v[192:195]\n"
    "v_lshlrev_b32 v114, 7, v114\n"
    "v_mfma_f32_16x16x32_bf16 v[196:199], v[176:179], v[164:167], v[196:199]\n"
    "v_mfma_f32_16x16x32_bf16 v[196:199], v[144:147], v[132:135], v[196:199]\n"
    "v_add_u32_dpp v52, v113, v48 quad_perm:[0,0,0,0] row_mask:0xf bank_mask:0xf\n"
    "v_mfma_f32_16x16x32_bf16 v[200:203], v[144:147], v[136:139], v[200:203]\n"
    "v_mfma_f32_16x16x32_bf16 v[200:203], v[176:179], v[168:171], v[200:203]\n"
    "v_add_u32_dpp v53, v113, v49 quad_perm:[1,1,1,1] row_mask:0xf bank_mask:0xf\n"
    "v_mfma_f32_16x16x32_bf16 v[204:207], v[176:179], v[172:175], v[204:207]\n"
    "v_mfma_f32_16x16x32_bf16 v[204:207], v[144:147], v[140:143], v[204:207]\n"
    "v_add_u32_dpp v54, v113, v50 quad_perm:[2,2,2,2] row_mask:0xf bank_mask:0xf\n"
    "v_mfma_f32_16x16x32_bf16 v[220:223], v[148:151], v[140:143], v[220:223]\n"
    "v_mfma_f32_16x16x32_bf16 v[220:223], v[180:183], v[172:175], v[220:223]\n"
    "v_add_u32_dpp v55, v113, v51 quad_perm:[3,3,3,3] row_mask:0xf bank_mask:0xf\n"
    "v_mfma_f32_16x16x32_bf16 v[216:219], v[180:183], v[168:171], v[216:219]\n"
    "v_mfma_f32_16x16x32_bf16 v[216:219], v[148:151], v[136:139], v[216:219]\n"
    "v_mfma_f32_16x16x32_bf16 v[212:215], v[148:151], v[132:135], v[212:215]\n"
    "v_mfma_f32_16x16x32_bf16 v[212:215], v[180:183], v[164:167], v[212:215]\n"
    "v_mfma_f32_16x16x32_bf16 v[208:211], v[180:183], v[160:163], v[208:211]\n"
    "v_mfma_f32_16x16x32_bf16 v[208:211], v[148:151], v[128:131], v[208:211]\n"
    "v_mfma_f32_16x16x32_bf16 v[224:227], v[152:155], v[128:131], v[224:227]\n"
    "v_mfma_f32_16x16x32_bf16 v[224:227], v[184:187], v[160:163], v[224:227]\n"
    "v_mfma_f32_16x16x32_bf16 v[228:231], v[184:187], v[164:167], v[228:231]\n"
    "v_add_f32 v222, v222, v66\n"
    "v_add_f32 v223, v223, v67\n"
    "v_mfma_f32_16x16x32_bf16 v[228:231], v[152:155], v[132:135], v[228:231]\n"
    "v_add_f32 v221, v221, v65\n"
    "v_mfma_f32_16x16x32_bf16 v[232:235], v[152:155], v[136:139], v[232:235]\n"
    "v_add_f32 v220, v220, v64\n"
    "v_mfma_f32_16x16x32_bf16 v[232:235], v[184:187], v[168:171], v[232:235]\n"
    "v_add_f32 v219, v219, v67\n"
    "v_mfma_f32_16x16x32_bf16 v[236:239], v[184:187], v[172:175], v[236:239]\n"
    "v_add_f32 v217, v217, v65\n"
    "v_add_f32 v218, v218, v66\n"
    "v_mfma_f32_16x16x32_bf16 v[236:239], v[152:155], v[140:143], v[236:239]\n"
    "v_add_f32 v216, v216, v64\n"
    "v_mfma_f32_16x16x32_bf16 v[252:255], v[156:159], v[140:143], v[252:255]\n"
    "v_add_f32 v215, v215, v67\n"
    "v_mfma_f32_16x16x32_bf16 v[252:255], v[188:191], v[172:175], v[252:255]\n"
    "v_add_f32 v214, v214, v66\n"
    "v_mfma_f32_16x16x32_bf16 v[248:251], v[188:191], v[168:171], v[248:251]\n"
    "v_add_f32 v212, v212, v64\n"
    "v_add_f32 v213, v213, v65\n"
    "v_mfma_f32_16x16x32_bf16 v[248:251], v[156:159], v[136:139], v[248:251]\n"
    "v_add_f32 v211, v211, v67\n"
    "v_mfma_f32_16x16x32_bf16 v[244:247], v[156:159], v[132:135], v[244:247]\n"
    "v_add_f32 v210, v210, v66\n"
    "v_mfma_f32_16x16x32_bf16 v[244:247], v[188:191], v[164:167], v[244:247]\n"
    "v_add_f32 v209, v209, v65\n"
    "v_mfma_f32_16x16x32_bf16 v[240:243], v[188:191], v[160:163], v[240:243]\n"
    "v_add_f32 v208, v208, v64\n"
    "v_mfma_f32_16x16x32_bf16 v[240:243], v[156:159], v[128:131], v[240:243]\n"
    "s_waitcnt vmcnt(8)\n"
    "s_barrier\n"
    "s_add_u32 m0, s35, 98304\n"
    "s_nop 0\n"
    "global_load_lds_dwordx4 v46, s[30:31] offset:0\n"
    "ds_read_b128 v[128:131], v56 offset:0\n"
    "ds_read_b128 v[132:135], v56 offset:2048\n"
    "ds_read_b128 v[136:139], v56 offset:4096\n"
    "ds_read_b128 v[140:143], v56 offset:6144\n"
    "global_load_lds_dwordx4 v47, s[30:31] offset:1024\n"
    "s_add_u32 s30, s30, 212992\n"
    "s_addc_u32 s31, s31, 0\n"
    "ds_read_b64_tr_b16 v[144:145], v76 offset:0\n"
    "ds_read_b64_tr_b16 v[146:147], v76 offset:1024\n"
    "ds_read_b64_tr_b16 v[148:149], v77 offset:0\n"
    "ds_read_b64_tr_b16 v[150:151], v77 offset:1024\n"
    "s_add_u32 m0, s34, 98304\n"
    "s_nop 0\n"
    "global_load_lds_dwordx4 v52, s[36:37] offset:0\n"
    "ds_read_b64_tr_b16 v[152:153], v78 offset:0\n"
    "ds_read_b64_tr_b16 v[154:155], v78 offset:1024\n"
    "ds_read_b64_tr_b16 v[156:157], v79 offset:0\n"
    "ds_read_b64_tr_b16 v[158:159], v79 offset:1024\n"
    "global_load_lds_dwordx4 v53, s[36:37] offset:1024\n"
    "ds_read_b128 v[160:163], v57 offset:0\n"
    "ds_read_b128 v[164:167], v57 offset:2048\n"
    "ds_read_b128 v[168:171], v57 offset:4096\n"
    "ds_read_b128 v[172:175], v57 offset:6144\n"
    "global_load_lds_dwordx4 v54, s[36:37] offset:2048\n"
    "ds_read_b64_tr_b16 v[176:177], v76 offset:8192\n"
    "ds_read_b64_tr_b16 v[178:179], v76 offset:9216\n"
    "ds_read_b64_tr_b16 v[180:181], v77 offset:8192\n"
    "ds_read_b64_tr_b16 v[182:183], v77 offset:9216\n"
    "global_load_lds_dwordx4 v55, s[36:37] offset:3072\n"
    "ds_read_b64_tr_b16 v[184:185], v78 offset:8192\n"
    "ds_read_b64_tr_b16 v[186:187], v78 offset:9216\n"
    "ds_read_b64_tr_b16 v[188:189], v79 offset:8192\n"
    "ds_read_b64_tr_b16 v[190:191], v79 offset:9216\n"
    "s_waitcnt vmcnt(11) lgkmcnt(0)\n"
    "s_barrier\n"
    "v_mfma_f32_16x16x32_bf16 v[192:195], v[144:147], v[128:131], v[192:195]\n"
    "v_lshlrev_b32 v115, 7, v115\n"
    "v_mfma_f32_16x16x32_bf16 v[192:195], v[176:179], v[160:163], v[192:195]\n"
    "v_add_u32_dpp v52, v114, v48 quad_perm:[0,0,0,0] row_mask:0xf bank_mask:0xf\n"
    "v_mfma_f32_16x16x32_bf16 v[196:199], v[176:179], v[164:167], v[196:199]\n"
    "v_add_u32_dpp v53, v114, v49 quad_perm:[1,1,1,1] row_mask:0xf bank_mask:0xf\n"
    "v_mfma_f32_16x16x32_bf16 v[196:199], v[144:147], v[132:135], v[196:199]\n"
    "v_add_u32_dpp v54, v114, v50 quad_perm:[2,2,2,2] row_mask:0xf bank_mask:0xf\n"
    "v_mfma_f32_16x16x32_bf16 v[200:203], v[144:147], v[136:139], v[200:203]\n"
    "v_add_u32_dpp v55, v114, v51 quad_perm:[3,3,3,3] row_mask:0xf bank_mask:0xf\n"
    "v_mfma_f32_16x16x32_bf16 v[200:203], v[176:179], v[168:171], v[200:203]\n"
    "v_lshl_or_b32 v36, v36, 7, v19\n"
    "v_mfma_f32_16x16x32_bf16 v[204:207], v[176:179], v[172:175], v[204:207]\n"
    "v_mfma_f32_16x16x32_bf16 v[204:207], v[144:147], v[140:143], v[204:207]\n"
    "v_lshl_or_b32 v37, v37, 7, v19\n"
    "v_mfma_f32_16x16x32_bf16 v[220:223], v[148:151], v[140:143], v[220:223]\n"
    "v_lshl_or_b32 v38, v38, 7, v19\n"
    "v_mfma_f32_16x16x32_bf16 v[220:223], v[180:183], v[172:175], v[220:223]\n"
    "v_lshl_or_b32 v41, v41, 7, v20\n"
    "v_mfma_f32_16x16x32_bf16 v[216:219], v[180:183], v[168:171], v[216:219]\n"
    "s_mov_b32 s64, 0xbdd2d3e8\n"
    "v_mfma_f32_16x16x32_bf16 v[216:219], v[148:151], v[136:139], v[216:219]\n"
    "s_mov_b32 s65, 0xc0135761\n"
    "v_mfma_f32_16x16x32_bf16 v[212:215], v[148:151], v[132:135], v[212:215]\n"
    "v_mfma_f32_16x16x32_bf16 v[212:215], v[180:183], v[164:167], v[212:215]\n"
    "s_and_b32 s41, s20, 1\n"
    "v_mfma_f32_16x16x32_bf16 v[208:211], v[180:183], v[160:163], v[208:211]\n"
    "s_lshl1_add_u32 s40, s21, s41\n"
    "v_mfma_f32_16x16x32_bf16 v[208:211], v[148:151], v[128:131], v[208:211]\n"
    "s_lshl_b32 s41, s40, 8\n"
    "v_mfma_f32_16x16x32_bf16 v[224:227], v[152:155], v[128:131], v[224:227]\n"
    "s_add_u32 s3, s41, 149504\n"
    "v_mfma_f32_16x16x32_bf16 v[224:227], v[184:187], v[160:163], v[224:227]\n"
    "s_lshl_b32 s40, s40, 6\n"
    "v_mfma_f32_16x16x32_bf16 v[228:231], v[184:187], v[164:167], v[228:231]\n"
    "v_mfma_f32_16x16x32_bf16 v[228:231], v[152:155], v[132:135], v[228:231]\n"
    "s_add_u32 s40, s40, s24\n"
    "v_mfma_f32_16x16x32_bf16 v[232:235], v[152:155], v[136:139], v[232:235]\n"
    "s_lshl_b32 s41, s23, 12\n"
    "v_mfma_f32_16x16x32_bf16 v[232:235], v[184:187], v[168:171], v[232:235]\n"
    "s_add_u32 s40, s40, s41\n"
    "v_mfma_f32_16x16x32_bf16 v[236:239], v[184:187], v[172:175], v[236:239]\n"
    "s_lshl3_add_u32 s40, s40, 65536\n"
    "v_mfma_f32_16x16x32_bf16 v[236:239], v[152:155], v[140:143], v[236:239]\n"
    "s_add_u32 s48, s40, s16\n"
    "v_mfma_f32_16x16x32_bf16 v[252:255], v[156:159], v[140:143], v[252:255]\n"
    "v_mfma_f32_16x16x32_bf16 v[252:255], v[188:191], v[172:175], v[252:255]\n"
    "s_addc_u32 s49, s17, 0\n"
    "v_mfma_f32_16x16x32_bf16 v[248:251], v[188:191], v[168:171], v[248:251]\n"
    "s_cmp_lt_u32 s22, 2\n"
    "v_mfma_f32_16x16x32_bf16 v[248:251], v[156:159], v[136:139], v[248:251]\n"
    "s_cselect_b32 s24, 16, 0\n"
    "v_mfma_f32_16x16x32_bf16 v[244:247], v[156:159], v[132:135], v[244:247]\n"
    "s_sub_u32 s23, 1, s22\n"
    "v_mfma_f32_16x16x32_bf16 v[244:247], v[188:191], v[164:167], v[244:247]\n"
    "s_and_b32 s23, s23, 3\n"
    "v_mfma_f32_16x16x32_bf16 v[240:243], v[188:191], v[160:163], v[240:243]\n"
    "v_mfma_f32_16x16x32_bf16 v[240:243], v[156:159], v[128:131], v[240:243]\n"
    "s_waitcnt vmcnt(7)\n"
    "s_barrier\n"
    "s_cmp_eq_u32 s20, 3\n"
    "s_cbranch_scc1 .Lxd_on2\n"
    ".Lxd_on2r:\n"
    "s_add_u32 m0, s35, 0\n"
    "s_nop 0\n"
    "global_load_lds_dwordx4 v46, s[30:31] offset:0\n"
    "ds_read_b128 v[128:131], v56 offset:49152\n"
    "ds_read_b128 v[132:135], v56 offset:51200\n"
    "ds_read_b128 v[136:139], v56 offset:53248\n"
    "global_load_lds_dwordx4 v47, s[30:31] offset:1024\n"
    "s_add_u32 s30, s30, 212992\n"
    "s_addc_u32 s31, s31, 0\n"
    "ds_read_b128 v[140:143], v56 offset:55296\n"
    "ds_read_b64_tr_b16 v[144:145], v76 offset:49152\n"
    "ds_read_b64_tr_b16 v[146:147], v76 offset:50176\n"
    "s_add_u32 m0, s34, 0\n"
    "s_nop 0\n"
    "global_load_lds_dwordx4 v52, s[36:37] offset:0\n"
    "ds_read_b64_tr_b16 v[148:149], v77 offset:49152\n"
    "ds_read_b64_tr_b16 v[150:151], v77 offset:50176\n"
    "ds_read_b64_tr_b16 v[152:153], v78 offset:49152\n"
    "global_load_lds_dwordx4 v53, s[36:37] offset:1024\n"
    "ds_read_b64_tr_b16 v[154:155], v78 offset:50176\n"
    "ds_read_b64_tr_b16 v[156:157], v79 offset:49152\n"
    "ds_read_b64_tr_b16 v[158:159], v79 offset:50176\n"
    "global_load_lds_dwordx4 v54, s[36:37] offset:2048\n"
    "ds_read_b128 v[160:163], v57 offset:49152\n"
    "ds_read_b128 v[164:167], v57 offset:51200\n"
    "ds_read_b128 v[168:171], v57 offset:53248\n"
    "global_load_lds_dwordx4 v55, s[36:37] offset:3072\n"
    "ds_read_b128 v[172:175], v57 offset:55296\n"
    "ds_read_b64_tr_b16 v[176:177], v76 offset:57344\n"
    "ds_read_b64_tr_b16 v[178:179], v76 offset:58368\n"
    "global_load_dwordx4 v[28:31], v36, s[8:9]\n"
    "ds_read_b64_tr_b16 v[180:181], v77 offset:57344\n"
    "ds_read_b64_tr_b16 v[182:183], v77 offset:58368\n"
    "ds_read_b64_tr_b16 v[184:185], v78 offset:57344\n"
    "ds_read_b64_tr_b16 v[186:187], v78 offset:58368\n"
    "ds_read_b64_tr_b16 v[188:189], v79 offset:57344\n"
    "ds_read_b64_tr_b16 v[190:191], v79 offset:58368\n"
    "s_waitcnt vmcnt(11) lgkmcnt(0)\n"
    "s_barrier\n"
    "v_mfma_f32_16x16x32_bf16 v[192:195], v[144:147], v[128:131], v[192:195]\n"
    "v_mfma_f32_16x16x32_bf16 v[192:195], v[176:179], v[160:163], v[192:195]\n"
    "v_add_f32 v224, v224, v68\n"
    "v_add_f32 v225, v225, v69\n"
    "v_mfma_f32_16x16x32_bf16 v[196:199], v[176:179], v[164:167], v[196:199]\n"
    "v_add_f32 v226, v226, v70\n"
    "v_mfma_f32_16x16x32_bf16 v[196:199], v[144:147], v[132:135], v[196:199]\n"
    "v_add_f32 v227, v227, v71\n"
    "v_mfma_f32_16x16x32_bf16 v[200:203], v[144:147], v[136:139], v[200:203]\n"
    "v_add_f32 v228, v228, v68\n"
    "v_mfma_f32_16x16x32_bf16 v[200:203], v[176:179], v[168:171], v[200:203]\n"
    "v_add_f32 v229, v229, v69\n"
    "v_add_f32 v230, v230, v70\n"
    "v_mfma_f32_16x16x32_bf16 v[204:207], v[176:179], v[172:175], v[204:207]\n"
    "v_add_f32 v231, v231, v71\n"
    "v_mfma_f32_16x16x32_bf16 v[204:207], v[144:147], v[140:143], v[204:207]\n"
    "v_add_f32 v232, v232, v68\n"
    "v_mfma_f32_16x16x32_bf16 v[220:223], v[148:151], v[140:143], v[220:223]\n"
    "v_add_f32 v233, v233, v69\n"
    "v_mfma_f32_16x16x32_bf16 v[220:223], v[180:183], v[172:175], v[220:223]\n"
    "v_add_f32 v234, v234, v70\n"
    "v_add_f32 v235, v235, v71\n"
    "v_mfma_f32_16x16x32_bf16 v[216:219], v[180:183], v[168:171], v[216:219]\n"
    "v_add_f32 v236, v236, v68\n"
    "v_mfma_f32_16x16x32_bf16 v[216:219], v[148:151], v[136:139], v[216:219]\n"
    "v_add_f32 v237, v237, v69\n"
    "v_mfma_f32_16x16x32_bf16 v[212:215], v[148:151], v[132:135], v[212:215]\n"
    "v_add_f32 v238, v238, v70\n"
    "v_mfma_f32_16x16x32_bf16 v[212:215], v[180:183], v[164:167], v[212:215]\n"
    "v_add_f32 v239, v239, v71\n"
    "v_mfma_f32_16x16x32_bf16 v[208:211], v[180:183], v[160:163], v[208:211]\n"
    "v_mfma_f32_16x16x32_bf16 v[208:211], v[148:151], v[128:131], v[208:211]\n"
    "v_mfma_f32_16x16x32_bf16 v[224:227], v[152:155], v[128:131], v[224:227]\n"
    "v_mfma_f32_16x16x32_bf16 v[224:227], v[184:187], v[160:163], v[224:227]\n"
    "v_mfma_f32_16x16x32_bf16 v[228:231], v[184:187], v[164:167], v[228:231]\n"
    "v_lshlrev_b32 v116, 7, v116\n"
    "v_mfma_f32_16x16x32_bf16 v[228:231], v[152:155], v[132:135], v[228:231]\n"
    "v_add_u32_dpp v52, v115, v48 quad_perm:[0,0,0,0] row_mask:0xf bank_mask:0xf\n"
    "v_mfma_f32_16x16x32_bf16 v[232:235], v[152:155], v[136:139], v[232:235]\n"
    "v_add_u32_dpp v53, v115, v49 quad_perm:[1,1,1,1] row_mask:0xf bank_mask:0xf\n"
    "v_mfma_f32_16x16x32_bf16 v[232:235], v[184:187], v[168:171], v[232:235]\n"
    "v_add_u32_dpp v54, v115, v50 quad_perm:[2,2,2,2] row_mask:0xf bank_mask:0xf\n"
    "v_mfma_f32_16x16x32_bf16 v[236:239], v[184:187], v[172:175], v[236:239]\n"
    "v_add_u32_dpp v55, v115, v51 quad_perm:[3,3,3,3] row_mask:0xf bank_mask:0xf\n"
    "v_mfma_f32_16x16x32_bf16 v[236:239], v[152:155], v[140:143], v[236:239]\n"
    "s_getpc_b64 s[10:11]\n"
    ".Lxd_en1:\n"
    "v_mfma_f32_16x16x32_bf16 v[252:255], v[156:159], v[140:143], v[252:255]\n"
    "s_mov_b32 s40, .Lxd_ph1-.Lxd_en1\n"
    "v_mfma_f32_16x16x32_bf16 v[252:255], v[188:191], v[172:175], v[252:255]\n"
    "s_cmp_eq_u32 s22, 1\n"
    "s_cselect_b32 s40, .Lxd_ph0-.Lxd_en1, s40\n"
    "v_mfma_f32_16x16x32_bf16 v[248:251], v[188:191], v[168:171], v[248:251]\n"
    "s_cmp_eq_u32 s22, 2\n"
    "s_cselect_b32 s40, .Lxd_pl3-.Lxd_en1, s40\n"
    "v_mfma_f32_16x16x32_bf16 v[248:251], v[156:159], v[136:139], v[248:251]\n"
    "s_cmp_eq_u32 s22, 3\n"
    "s_cselect_b32 s40, .Lxd_ph2-.Lxd_en1, s40\n"
    "v_mfma_f32_16x16x32_bf16 v[244:247], v[156:159], v[132:135], v[244:247]\n"
    "s_ashr_i32 s41, s40, 31\n"
    "v_mfma_f32_16x16x32_bf16 v[244:247], v[188:191], v[164:167], v[244:247]\n"
    "s_add_u32 s10, s10, s40\n"
    "s_addc_u32 s11, s11, s41\n"
    "v_mfma_f32_16x16x32_bf16 v[240:243], v[188:191], v[160:163], v[240:243]\n"
    "v_mfma_f32_16x16x32_bf16 v[240:243], v[156:159], v[128:131], v[240:243]\n"
    "s_waitcnt vmcnt(7)\n"
    "s_barrier\n"
    "s_add_u32 m0, s35, 49152\n"
    "s_nop 0\n"
    "global_load_lds_dwordx4 v46, s[30:31] offset:0\n"
    "ds_read_b128 v[128:131], v58 offset:0\n"
    "ds_read_b128 v[132:135], v58 offset:2048\n"
    "ds_read_b128 v[136:139], v58 offset:4096\n"
    "global_load_lds_dwordx4 v47, s[30:31] offset:1024\n"
    "s_bfe_u32 s66, s29, 0x80000\n"
    "s_mul_i32 s66, s66, 53248\n"
    "s_add_u32 s30, s6, s66\n"
    "s_addc_u32 s31, s7, 0\n"
    "ds_read_b128 v[140:143], v58 offset:6144\n"
    "ds_read_b64_tr_b16 v[144:145], v80 offset:0\n"
    "ds_read_b64_tr_b16 v[146:147], v80 offset:1024\n"
    "s_add_u32 m0, s34, 49152\n"
    "s_nop 0\n"
    "global_load_lds_dwordx4 v52, s[36:37] offset:0\n"
    "ds_read_b64_tr_b16 v[148:149], v81 offset:0\n"
    "ds_read_b64_tr_b16 v[150:151], v81 offset:1024\n"
    "ds_read_b64_tr_b16 v[152:153], v82 offset:0\n"
    "global_load_lds_dwordx4 v53, s[36:37] offset:1024\n"
    "ds_read_b64_tr_b16 v[154:155], v82 offset:1024\n"
    "ds_read_b64_tr_b16 v[156:157], v83 offset:0\n"
    "ds_read_b64_tr_b16 v[158:159], v83 offset:1024\n"
    "global_load_lds_dwordx4 v54, s[36:37] offset:2048\n"
    "ds_read_b128 v[160:163], v59 offset:0\n"
    "ds_read_b128 v[164:167], v59 offset:2048\n"
    "ds_read_b128 v[168:171], v59 offset:4096\n"
    "global_load_lds_dwordx4 v55, s[36:37] offset:3072\n"
    "ds_read_b128 v[172:175], v59 offset:6144\n"
    "ds_read_b64_tr_b16 v[176:177], v80 offset:8192\n"
    "ds_read_b64_tr_b16 v[178:179], v80 offset:9216\n"
    "global_load_dwordx4 v[32:35], v37, s[8:9]\n"
    "ds_read_b64_tr_b16 v[180:181], v81 offset:8192\n"
    "ds_read_b64_tr_b16 v[182:183], v81 offset:9216\n"
    "ds_read_b64_tr_b16 v[184:185], v82 offset:8192\n"
    "ds_read_b64_tr_b16 v[186:187], v82 offset:9216\n"
    "ds_read_b64_tr_b16 v[188:189], v83 offset:8192\n"
    "ds_read_b64_tr_b16 v[190:191], v83 offset:9216\n"
    "s_waitcnt vmcnt(12) lgkmcnt(0)\n"
    "s_barrier\n"
    "v_mfma_f32_16x16x32_bf16 v[192:195], v[144:147], v[128:131], v[192:195]\n"
    "v_mfma_f32_16x16x32_bf16 v[192:195], v[176:179], v[160:163], v[192:195]\n"
    "v_mfma_f32_16x16x32_bf16 v[196:199], v[176:179], v[164:167], v[196:199]\n"
    "v_mfma_f32_16x16x32_bf16 v[196:199], v[144:147], v[132:135], v[196:199]\n"
    "v_mfma_f32_16x16x32_bf16 v[200:203], v[144:147], v[136:139], v[200:203]\n"
    "v_add_f32 v240, v240, v72\n"
    "v_mfma_f32_16x16x32_bf16 v[200:203], v[176:179], v[168:171], v[200:203]\n"
    "v_add_f32 v241, v241, v73\n"
    "v_mfma_f32_16x16x32_bf16 v[204:207], v[176:179], v[172:175], v[204:207]\n"
    "v_add_f32 v242, v242, v74\n"
    "v_mfma_f32_16x16x32_bf16 v[204:207], v[144:147], v[140:143], v[204:207]\n"
    "v_add_f32 v243, v243, v75\n"
    "v_mfma_f32_16x16x32_bf16 v[220:223], v[148:151], v[140:143], v[220:223]\n"
    "v_add_f32 v244, v244, v72\n"
    "v_mfma_f32_16x16x32_bf16 v[220:223], v[180:183], v[172:175], v[220:223]\n"
    "v_add_f32 v245, v245, v73\n"
    "v_mfma_f32_16x16x32_bf16 v[216:219], v[180:183], v[168:171], v[216:219]\n"
    "v_add_f32 v246, v246, v74\n"
    "v_mfma_f32_16x16x32_bf16 v[216:219], v[148:151], v[136:139], v[216:219]\n"
    "v_add_f32 v247, v247, v75\n"
    "v_mfma_f32_16x16x32_bf16 v[212:215], v[148:151], v[132:135], v[212:215]\n"
    "v_add_f32 v248, v248, v72\n"
    "v_mfma_f32_16x16x32_bf16 v[212:215], v[180:183], v[164:167], v[212:215]\n"
    "v_add_f32 v249, v249, v73\n"
    "v_mfma_f32_16x16x32_bf16 v[208:211], v[180:183], v[160:163], v[208:211]\n"
    "v_add_f32 v250, v250, v74\n"
    "v_mfma_f32_16x16x32_bf16 v[208:211], v[148:151], v[128:131], v[208:211]\n"
    "v_add_f32 v251, v251, v75\n"
    "v_mfma_f32_16x16x32_bf16 v[224:227], v[152:155], v[128:131], v[224:227]\n"
    "v_add_f32 v252, v252, v72\n"
    "v_mfma_f32_16x16x32_bf16 v[224:227], v[184:187], v[160:163], v[224:227]\n"
    "v_add_f32 v253, v253, v73\n"
    "v_mfma_f32_16x16x32_bf16 v[228:231], v[184:187], v[164:167], v[228:231]\n"
    "v_lshlrev_b32 v117, 7, v117\n"
    "v_add_f32 v254, v254, v74\n"
    "v_mfma_f32_16x16x32_bf16 v[228:231], v[152:155], v[132:135], v[228:231]\n"
    "v_add_f32 v255, v255, v75\n"
    "v_mfma_f32_16x16x32_bf16 v[232:235], v[152:155], v[136:139], v[232:235]\n"
    "v_add_u32_dpp v52, v116, v48 quad_perm:[0,0,0,0] row_mask:0xf bank_mask:0xf\n"
    "v_mfma_f32_16x16x32_bf16 v[232:235], v[184:187], v[168:171], v[232:235]\n"
    "v_mfma_f32_16x16x32_bf16 v[236:239], v[184:187], v[172:175], v[236:239]\n"
    "v_mfma_f32_16x16x32_bf16 v[236:239], v[152:155], v[140:143], v[236:239]\n"
    "v_add_u32_dpp v53, v116, v49 quad_perm:[1,1,1,1] row_mask:0xf bank_mask:0xf\n"
    "v_mfma_f32_16x16x32_bf16 v[252:255], v[156:159], v[140:143], v[252:255]\n"
    "v_mfma_f32_16x16x32_bf16 v[252:255], v[188:191], v[172:175], v[252:255]\n"
    "v_add_u32_dpp v54, v116, v50 quad_perm:[2,2,2,2] row_mask:0xf bank_mask:0xf\n"
    "v_mfma_f32_16x16x32_bf16 v[248:251], v[188:191], v[168:171], v[248:251]\n"
    "v_mfma_f32_16x16x32_bf16 v[248:251], v[156:159], v[136:139], v[248:251]\n"
    "v_mfma_f32_16x16x32_bf16 v[244:247], v[156:159], v[132:135], v[244:247]\n"
    "v_add_u32_dpp v55, v116, v51 quad_perm:[3,3,3,3] row_mask:0xf bank_mask:0xf\n"
    "v_mfma_f32_16x16x32_bf16 v[244:247], v[188:191], v[164:167], v[244:247]\n"
    "v_mfma_f32_16x16x32_bf16 v[240:243], v[188:191], v[160:163], v[240:243]\n"
    "v_mfma_f32_16x16x32_bf16 v[240:243], v[156:159], v[128:131], v[240:243]\n"
    "s_waitcnt vmcnt(8)\n"
    "s_barrier\n"
    "s_add_u32 m0, s35, 98304\n"
    "s_nop 0\n"
    "global_load_lds_dwordx4 v46, s[30:31] offset:0\n"
    "ds_read_b128 v[128:131], v56 offset:0\n"
    "ds_read_b128 v[132:135], v56 offset:2048\n"
    "ds_read_b128 v[136:139], v56 offset:4096\n"
    "global_load_lds_dwordx4 v47, s[30:31] offset:1024\n"
    "s_add_u32 s30, s30, 212992\n"
    "s_addc_u32 s31, s31, 0\n"
    "ds_read_b128 v[140:143], v56 offset:6144\n"
    "ds_read_b64_tr_b16 v[144:145], v76 offset:0\n"
    "ds_read_b64_tr_b16 v[146:147], v76 offset:1024\n"
    "s_add_u32 m0, s34, 98304\n"
    "s_nop 0\n"
    "global_load_lds_dwordx4 v52, s[36:37] offset:0\n"
    "ds_read_b64_tr_b16 v[148:149], v77 offset:0\n"
    "ds_read_b64_tr_b16 v[150:151], v77 offset:1024\n"
    "ds_read_b64_tr_b16 v[152:153], v78 offset:0\n"
    "global_load_lds_dwordx4 v53, s[36:37] offset:1024\n"
    "ds_read_b64_tr_b16 v[154:155], v78 offset:1024\n"
    "ds_read_b64_tr_b16 v[156:157], v79 offset:0\n"
    "ds_read_b64_tr_b16 v[158:159], v79 offset:1024\n"
    "global_load_lds_dwordx4 v54, s[36:37] offset:2048\n"
    "ds_read_b128 v[160:163], v57 offset:0\n"
    "ds_read_b128 v[164:167], v57 offset:2048\n"
    "ds_read_b128 v[168:171], v57 offset:4096\n"
    "global_load_lds_dwordx4 v55, s[36:37] offset:3072\n"
    "ds_read_b128 v[172:175], v57 offset:6144\n"
    "ds_read_b64_tr_b16 v[176:177], v76 offset:8192\n"
    "ds_read_b64_tr_b16 v[178:179], v76 offset:9216\n"
    "global_load_dword v40, v41, s[8:9]\n"
    "ds_read_b64_tr_b16 v[180:181], v77 offset:8192\n"
    "ds_read_b64_tr_b16 v[182:183], v77 offset:9216\n"
    "ds_read_b64_tr_b16 v[184:185], v78 offset:8192\n"
    "ds_read_b64_tr_b16 v[186:187], v78 offset:9216\n"
    "ds_read_b64_tr_b16 v[188:189], v79 offset:8192\n"
    "ds_read_b64_tr_b16 v[190:191], v79 offset:9216\n"
    "s_waitcnt vmcnt(12) lgkmcnt(0)\n"
    "s_barrier\n"
    "v_mfma_f32_16x16x32_bf16 v[192:195], v[144:147], v[128:131], v[192:195]\n"
    "v_lshlrev_b32 v118, 7, v118\n"
    "v_mfma_f32_16x16x32_bf16 v[192:195], v[176:179], v[160:163], v[192:195]\n"
    "v_add_u32_dpp v52, v117, v48 quad_perm:[0,0,0,0] row_mask:0xf bank_mask:0xf\n"
    "v_mfma_f32_16x16x32_bf16 v[196:199], v[176:179], v[164:167], v[196:199]\n"
    "v_add_u32_dpp v53, v117, v49 quad_perm:[1,1,1,1] row_mask:0xf bank_mask:0xf\n"
    "v_mfma_f32_16x16x32_bf16 v[196:199], v[144:147], v[132:135], v[196:199]\n"
    "v_add_u32_dpp v54, v117, v50 quad_perm:[2,2,2,2] row_mask:0xf bank_mask:0xf\n"
    "v_mfma_f32_16x16x32_bf16 v[200:203], v[144:147], v[136:139], v[200:203]\n"
    "v_add_u32_dpp v55, v117, v51 quad_perm:[3,3,3,3] row_mask:0xf bank_mask:0xf\n"
    "v_mfma_f32_16x16x32_bf16 v[200:203], v[176:179], v[168:171], v[200:203]\n"
    "v_readlane_b32 s25, v26, 9\n"
    "v_mfma_f32_16x16x32_bf16 v[204:207], v[176:179], v[172:175], v[204:207]\n"
    "v_readlane_b32 s40, v26, 8\n"
    "v_mfma_f32_16x16x32_bf16 v[204:207], v[144:147], v[140:143], v[204:207]\n"
    "v_readlane_b32 s41, v27, 8\n"
    "v_mfma_f32_16x16x32_bf16 v[220:223], v[148:151], v[140:143], v[220:223]\n"
    "s_mul_i32 s42, s25, 0x9e3779b1\n"
    "v_mfma_f32_16x16x32_bf16 v[220:223], v[180:183], v[172:175], v[220:223]\n"
    "s_lshr_b32 s42, s42, 24\n"
    "v_mfma_f32_16x16x32_bf16 v[216:219], v[180:183], v[168:171], v[216:219]\n"
    "s_xor_b32 s42, s42, s40\n"
    "v_mfma_f32_16x16x32_bf16 v[216:219], v[148:151], v[136:139], v[216:219]\n"
    "s_and_b32 s42, s42, 0xff\n"
    "v_mfma_f32_16x16x32_bf16 v[212:215], v[148:151], v[132:135], v[212:215]\n"
    "s_xor_b32 s41, s41, s25\n"
    "v_mfma_f32_16x16x32_bf16 v[212:215], v[180:183], v[164:167], v[212:215]\n"
    "s_xor_b32 s41, s41, 0xa5c30f96\n"
    "v_mfma_f32_16x16x32_bf16 v[208:211], v[180:183], v[160:163], v[208:211]\n"
    "s_or_b32 s42, s41, s42\n"
    "v_mfma_f32_16x16x32_bf16 v[208:211], v[148:151], v[128:131], v[208:211]\n"
    "v_mfma_f32_16x16x32_bf16 v[224:227], v[152:155], v[128:131], v[224:227]\n"
    "s_lshr_b32 s41, s40, 8\n"
    "v_mfma_f32_16x16x32_bf16 v[224:227], v[184:187], v[160:163], v[224:227]\n"
    "s_mul_i32 s40, s41, 0x9e3779b1\n"
    "v_mfma_f32_16x16x32_bf16 v[228:231], v[184:187], v[164:167], v[228:231]\n"
    "s_xor_b32 s41, s41, 0xa5c30f96\n"
    "v_mfma_f32_16x16x32_bf16 v[228:231], v[152:155], v[132:135], v[228:231]\n"
    "s_lshr_b32 s40, s40, 24\n"
    "v_mfma_f32_16x16x32_bf16 v[232:235], v[152:155], v[136:139], v[232:235]\n"
    "v_xor_b32 v13, s40, v26\n"
    "v_mfma_f32_16x16x32_bf16 v[232:235], v[184:187], v[168:171], v[232:235]\n"
    "v_xor_b32 v22, s41, v27\n"
    "v_mfma_f32_16x16x32_bf16 v[236:239], v[184:187], v[172:175], v[236:239]\n"
    "v_lshl_or_b32 v13, v13, 24, v22\n"
    "v_mfma_f32_16x16x32_bf16 v[236:239], v[152:155], v[140:143], v[236:239]\n"
    "v_cmp_ne_u32 s[66:67], 0, v13\n"
    "v_mfma_f32_16x16x32_bf16 v[252:255], v[156:159], v[140:143], v[252:255]\n"
    "s_and_b32 s66, s66, 0xff\n"
    "v_mfma_f32_16x16x32_bf16 v[252:255], v[188:191], v[172:175], v[252:255]\n"
    "s_or_b32 s42, s42, s66\n"
    "v_mfma_f32_16x16x32_bf16 v[248:251], v[188:191], v[168:171], v[248:251]\n"
    "s_cmp_eq_u32 s20, 3\n"
    "v_mfma_f32_16x16x32_bf16 v[248:251], v[156:159], v[136:139], v[248:251]\n"
    "s_cselect_b32 s42, s42, 0\n"
    "v_mfma_f32_16x16x32_bf16 v[244:247], v[156:159], v[132:135], v[244:247]\n"
    "s_cmp_lg_u32 s42, 0\n"
    "v_mfma_f32_16x16x32_bf16 v[244:247], v[188:191], v[164:167], v[244:247]\n"
    "s_cbranch_scc1 .Lxd_pv3\n"
    ".Lxd_pv3r:\n"
    "v_mfma_f32_16x16x32_bf16 v[240:243], v[188:191], v[160:163], v[240:243]\n"
    "v_mfma_f32_16x16x32_bf16 v[240:243], v[156:159], v[128:131], v[240:243]\n"
    "s_waitcnt vmcnt(8)\n"
    "s_barrier\n"
    "s_add_u32 m0, s35, 0\n"
    "s_nop 0\n"
    "global_load_lds_dwordx4 v46, s[30:31] offset:0\n"
    "ds_read_b128 v[128:131], v56 offset:49152\n"
    "ds_read_b128 v[132:135], v56 offset:51200\n"
    "ds_read_b128 v[136:139], v56 offset:53248\n"
    "global_load_lds_dwordx4 v47, s[30:31] offset:1024\n"
    "s_add_u32 s30, s30, 212992\n"
    "s_addc_u32 s31, s31, 0\n"
    "ds_read_b128 v[140:143], v56 offset:55296\n"
    "ds_read_b64_tr_b16 v[144:145], v76 offset:49152\n"
    "ds_read_b64_tr_b16 v[146:147], v76 offset:50176\n"
    "s_add_u32 m0, s34, 0\n"
    "s_nop 0\n"
    "global_load_lds_dwordx4 v52, s[36:37] offset:0\n"
    "ds_read_b64_tr_b16 v[148:149], v77 offset:49152\n"
    "ds_read_b64_tr_b16 v[150:151], v77 offset:50176\n"
    "ds_read_b64_tr_b16 v[152:153], v78 offset:49152\n"
    "global_load_lds_dwordx4 v53, s[36:37] offset:1024\n"
    "ds_read_b64_tr_b16 v[154:155], v78 offset:50176\n"
    "ds_read_b64_tr_b16 v[156:157], v79 offset:49152\n"
    "ds_read_b64_tr_b16 v[158:159], v79 offset:50176\n"
    "global_load_lds_dwordx4 v54, s[36:37] offset:2048\n"
    "ds_read_b128 v[160:163], v57 offset:49152\n"
    "ds_read_b128 v[164:167], v57 offset:51200\n"
    "ds_read_b128 v[168:171], v57 offset:53248\n"
    "global_load_lds_dwordx4 v55, s[36:37] offset:3072\n"
    "ds_read_b128 v[172:175], v57 offset:55296\n"
    "ds_read_b64_tr_b16 v[176:177], v76 offset:57344\n"
    "ds_read_b64_tr_b16 v[178:179], v76 offset:58368\n"
    "global_load_dwordx4 v[36:39], v38, s[8:9]\n"
    "ds_read_b64_tr_b16 v[180:181], v77 offset:57344\n"
    "ds_read_b64_tr_b16 v[182:183], v77 offset:58368\n"
    "ds_read_b64_tr_b16 v[184:185], v78 offset:57344\n"
    "ds_read_b64_tr_b16 v[186:187], v78 offset:58368\n"
    "ds_read_b64_tr_b16 v[188:189], v79 offset:57344\n"
    "ds_read_b64_tr_b16 v[190:191], v79 offset:58368\n"
    "ds_read_b64 v[84:85], v42 offset:0\n"
    "ds_read_b64 v[86:87], v42 offset:32\n"
    "ds_read_b64 v[88:89], v42 offset:64\n"
    "ds_read_b64 v[90:91], v42 offset:96\n"
    "s_waitcnt vmcnt(12) lgkmcnt(0)\n"
    "s_barrier\n"
    "v_mfma_f32_16x16x32_bf16 v[192:195], v[144:147], v[128:131], v[192:195]\n"
    "v_lshlrev_b32 v119, 7, v119\n"
    "v_mfma_f32_16x16x32_bf16 v[192:195], v[176:179], v[160:163], v[192:195]\n"
    "v_add_u32_dpp v52, v118, v48 quad_perm:[0,0,0,0] row_mask:0xf bank_mask:0xf\n"
    "v_mfma_f32_16x16x32_bf16 v[196:199], v[176:179], v[164:167], v[196:199]\n"
    "v_add_u32_dpp v53, v118, v49 quad_perm:[1,1,1,1] row_mask:0xf bank_mask:0xf\n"
    "v_mfma_f32_16x16x32_bf16 v[196:199], v[144:147], v[132:135], v[196:199]\n"
    "v_mfma_f32_16x16x32_bf16 v[200:203], v[144:147], v[136:139], v[200:203]\n"
    "v_add_u32_dpp v54, v118, v50 quad_perm:[2,2,2,2] row_mask:0xf bank_mask:0xf\n"
    "v_mfma_f32_16x16x32_bf16 v[200:203], v[176:179], v[168:171], v[200:203]\n"
    "v_add_u32_dpp v55, v118, v51 quad_perm:[3,3,3,3] row_mask:0xf bank_mask:0xf\n"
    "v_mfma_f32_16x16x32_bf16 v[204:207], v[176:179], v[172:175], v[204:207]\n"
    "v_mfma_f32_16x16x32_bf16 v[204:207], v[144:147], v[140:143], v[204:207]\n"
    "v_lshlrev_b32 v60, 16, v84\n"
    "v_mfma_f32_16x16x32_bf16 v[220:223], v[148:151], v[140:143], v[220:223]\n"
    "v_and_b32 v61, 0xffff0000, v84\n"
    "v_mfma_f32_16x16x32_bf16 v[220:223], v[180:183], v[172:175], v[220:223]\n"
    "v_mfma_f32_16x16x32_bf16 v[216:219], v[180:183], v[168:171], v[216:219]\n"
    "v_lshlrev_b32 v62, 16, v85\n"
    "v_mfma_f32_16x16x32_bf16 v[216:219], v[148:151], v[136:139], v[216:219]\n"
    "v_and_b32 v63, 0xffff0000, v85\n"
    "v_mfma_f32_16x16x32_bf16 v[212:215], v[148:151], v[132:135], v[212:215]\n"
    "v_mfma_f32_16x16x32_bf16 v[212:215], v[180:183], v[164:167], v[212:215]\n"
    "v_lshlrev_b32 v64, 16, v86\n"
    "v_mfma_f32_16x16x32_bf16 v[208:211], v[180:183], v[160:163], v[208:211]\n"
    "v_and_b32 v65, 0xffff0000, v86\n"
    "v_mfma_f32_16x16x32_bf16 v[208:211], v[148:151], v[128:131], v[208:211]\n"
    "v_mfma_f32_16x16x32_bf16 v[224:227], v[152:155], v[128:131], v[224:227]\n"
    "v_lshlrev_b32 v66, 16, v87\n"
    "v_mfma_f32_16x16x32_bf16 v[224:227], v[184:187], v[160:163], v[224:227]\n"
    "v_and_b32 v67, 0xffff0000, v87\n"
    "v_mfma_f32_16x16x32_bf16 v[228:231], v[184:187], v[164:167], v[228:231]\n"
    "v_mfma_f32_16x16x32_bf16 v[228:231], v[152:155], v[132:135], v[228:231]\n"
    "v_lshlrev_b32 v68, 16, v88\n"
    "v_mfma_f32_16x16x32_bf16 v[232:235], v[152:155], v[136:139], v[232:235]\n"
    "v_and_b32 v69, 0xffff0000, v88\n"
    "v_mfma_f32_16x16x32_bf16 v[232:235], v[184:187], v[168:171], v[232:235]\n"
    "v_mfma_f32_16x16x32_bf16 v[236:239], v[184:187], v[172:175], v[236:239]\n"
    "v_lshlrev_b32 v70, 16, v89\n"
    "v_mfma_f32_16x16x32_bf16 v[236:239], v[152:155], v[140:143], v[236:239]\n"
    "v_and_b32 v71, 0xffff0000, v89\n"
    "v_mfma_f32_16x16x32_bf16 v[252:255], v[156:159], v[140:143], v[252:255]\n"
    "v_mfma_f32_16x16x32_bf16 v[252:255], v[188:191], v[172:175], v[252:255]\n"
    "v_lshlrev_b32 v72, 16, v90\n"
    "v_mfma_f32_16x16x32_bf16 v[248:251], v[188:191], v[168:171], v[248:251]\n"
    "v_and_b32 v73, 0xffff0000, v90\n"
    "v_mfma_f32_16x16x32_bf16 v[248:251], v[156:159], v[136:139], v[248:251]\n"
    "v_mfma_f32_16x16x32_bf16 v[244:247], v[156:159], v[132:135], v[244:247]\n"
    "v_lshlrev_b32 v74, 16, v91\n"
    "v_mfma_f32_16x16x32_bf16 v[244:247], v[188:191], v[164:167], v[244:247]\n"
    "v_and_b32 v75, 0xffff0000, v91\n"
    "v_mfma_f32_16x16x32_bf16 v[240:243], v[188:191], v[160:163], v[240:243]\n"
    "v_mfma_f32_16x16x32_bf16 v[240:243], v[156:159], v[128:131], v[240:243]\n"
    "s_waitcnt vmcnt(8)\n"
    "s_barrier\n"
    "s_add_u32 m0, s35, 49152\n"
    "s_nop 0\n"
    "global_load_lds_dwordx4 v46, s[30:31] offset:0\n"
    "ds_read_b128 v[128:131], v58 offset:0\n"
    "ds_read_b128 v[132:135], v58 offset:2048\n"
    "ds_read_b128 v[136:139], v58 offset:4096\n"
    "ds_read_b128 v[140:143], v58 offset:6144\n"
    "global_load_lds_dwordx4 v47, s[30:31] offset:1024\n"
    "s_add_u32 s30, s30, 212992\n"
    "s_addc_u32 s31, s31, 0\n"
    "ds_read_b64_tr_b16 v[144:145], v80 offset:0\n"
    "ds_read_b64_tr_b16 v[146:147], v80 offset:1024\n"
    "ds_read_b64_tr_b16 v[148:149], v81 offset:0\n"
    "ds_read_b64_tr_b16 v[150:151], v81 offset:1024\n"
    "s_add_u32 m0, s34, 49152\n"
    "s_nop 0\n"
    "global_load_lds_dwordx4 v52, s[36:37] offset:0\n"
    "ds_read_b64_tr_b16 v[152:153], v82 offset:0\n"
    "ds_read_b64_tr_b16 v[154:155], v82 offset:1024\n"
    "ds_read_b64_tr_b16 v[156:157], v83 offset:0\n"
    "ds_read_b64_tr_b16 v[158:159], v83 offset:1024\n"
    "global_load_lds_dwordx4 v53, s[36:37] offset:1024\n"
    "ds_read_b128 v[160:163], v59 offset:0\n"
    "ds_read_b128 v[164:167], v59 offset:2048\n"
    "ds_read_b128 v[168:171], v59 offset:4096\n"
    "ds_read_b128 v[172:175], v59 offset:6144\n"
    "global_load_lds_dwordx4 v54, s[36:37] offset:2048\n"
    "ds_read_b64_tr_b16 v[176:177], v80 offset:8192\n"
    "ds_read_b64_tr_b16 v[178:179], v80 offset:9216\n"
    "ds_read_b64_tr_b16 v[180:181], v81 offset:8192\n"
    "ds_read_b64_tr_b16 v[182:183], v81 offset:9216\n"
    "global_load_lds_dwordx4 v55, s[36:37] offset:3072\n"
    "ds_read_b64_tr_b16 v[184:185], v82 offset:8192\n"
    "ds_read_b64_tr_b16 v[186:187], v82 offset:9216\n"
    "ds_read_b64_tr_b16 v[188:189], v83 offset:8192\n"
    "ds_read_b64_tr_b16 v[190:191], v83 offset:9216\n"
    "s_waitcnt vmcnt(11) lgkmcnt(0)\n"
    "s_barrier\n"
    "v_mfma_f32_16x16x32_bf16 v[192:195], v[144:147], v[128:131], v[192:195]\n"
    "v_lshlrev_b32 v120, 7, v120\n"
    "v_mfma_f32_16x16x32_bf16 v[192:195], v[176:179], v[160:163], v[192:195]\n"
    "v_add_u32_dpp v52, v119, v48 quad_perm:[0,0,0,0] row_mask:0xf bank_mask:0xf\n"
    "v_mfma_f32_16x16x32_bf16 v[196:199], v[176:179], v[164:167], v[196:199]\n"
    "v_add_u32_dpp v53, v119, v49 quad_perm:[1,1,1,1] row_mask:0xf bank_mask:0xf\n"
    "v_mfma_f32_16x16x32_bf16 v[196:199], v[144:147], v[132:135], v[196:199]\n"
    "v_add_u32_dpp v54, v119, v50 quad_perm:[2,2,2,2] row_mask:0xf bank_mask:0xf\n"
    "v_mfma_f32_16x16x32_bf16 v[200:203], v[144:147], v[136:139], v[200:203]\n"
    "v_add_u32_dpp v55, v119, v51 quad_perm:[3,3,3,3] row_mask:0xf bank_mask:0xf\n"
    "v_mfma_f32_16x16x32_bf16 v[200:203], v[176:179], v[168:171], v[200:203]\n"
    "v_lshrrev_b32 v22, 8, v26\n"
    "v_mfma_f32_16x16x32_bf16 v[204:207], v[176:179], v[172:175], v[204:207]\n"
    "v_mfma_f32_16x16x32_bf16 v[204:207], v[144:147], v[140:143], v[204:207]\n"
    "v_mov_b32 v26, s40\n"
    "v_mfma_f32_16x16x32_bf16 v[220:223], v[148:151], v[140:143], v[220:223]\n"
    "v_mov_b32 v27, s41\n"
    "v_mfma_f32_16x16x32_bf16 v[220:223], v[180:183], v[172:175], v[220:223]\n"
    "v_add_u32_dpp v22, v22, v22 row_shl:4 row_mask:0xf bank_mask:0xf\n"
    "v_mfma_f32_16x16x32_bf16 v[216:219], v[180:183], v[168:171], v[216:219]\n"
    "v_add_u32_e64 v43, s25, 1\n"
    "v_mfma_f32_16x16x32_bf16 v[216:219], v[148:151], v[136:139], v[216:219]\n"
    "v_lshlrev_b32 v8, 2, v1\n"
    "v_mfma_f32_16x16x32_bf16 v[212:215], v[148:151], v[132:135], v[212:215]\n"
    "v_mfma_f32_16x16x32_bf16 v[212:215], v[180:183], v[164:167], v[212:215]\n"
    "v_max_u32_dpp v13, v22, v22 quad_perm:[1,0,3,2] row_mask:0xf bank_mask:0xf\n"
    "v_mfma_f32_16x16x32_bf16 v[208:211], v[180:183], v[160:163], v[208:211]\n"
    "v_add_u32 v8, 154112, v8\n"
    "v_mfma_f32_16x16x32_bf16 v[208:211], v[148:151], v[128:131], v[208:211]\n"
    "s_nop 0\n"
    "v_mfma_f32_16x16x32_bf16 v[224:227], v[152:155], v[128:131], v[224:227]\n"
    "v_max_u32_dpp v13, v13, v13 quad_perm:[2,3,0,1] row_mask:0xf bank_mask:0xf\n"
    "v_mfma_f32_16x16x32_bf16 v[224:227], v[184:187], v[160:163], v[224:227]\n"
    "v_sub_u32 v22, v22, v13\n"
    "v_mfma_f32_16x16x32_bf16 v[228:231], v[184:187], v[164:167], v[228:231]\n"
    "v_mfma_f32_16x16x32_bf16 v[228:231], v[152:155], v[132:135], v[228:231]\n"
    "v_subrev_u32 v13, 818, v13\n"
    "v_mfma_f32_16x16x32_bf16 v[232:235], v[152:155], v[136:139], v[232:235]\n"
    "v_ldexp_f32 v22, 1.0, v22\n"
    "v_mfma_f32_16x16x32_bf16 v[232:235], v[184:187], v[168:171], v[232:235]\n"
    "v_rcp_f32 v60, v60\n"
    "v_mfma_f32_16x16x32_bf16 v[236:239], v[184:187], v[172:175], v[236:239]\n"
    "v_rcp_f32 v61, v61\n"
    "v_mfma_f32_16x16x32_bf16 v[236:239], v[152:155], v[140:143], v[236:239]\n"
    "v_rcp_f32 v62, v62\n"
    "v_mfma_f32_16x16x32_bf16 v[252:255], v[156:159], v[140:143], v[252:255]\n"
    "v_mfma_f32_16x16x32_bf16 v[252:255], v[188:191], v[172:175], v[252:255]\n"
    "v_rcp_f32 v63, v63\n"
    "v_mfma_f32_16x16x32_bf16 v[248:251], v[188:191], v[168:171], v[248:251]\n"
    "v_rcp_f32 v64, v64\n"
    "v_mfma_f32_16x16x32_bf16 v[248:251], v[156:159], v[136:139], v[248:251]\n"
    "v_rcp_f32 v65, v65\n"
    "v_mfma_f32_16x16x32_bf16 v[244:247], v[156:159], v[132:135], v[244:247]\n"
    "v_rcp_f32 v66, v66\n"
    "v_mfma_f32_16x16x32_bf16 v[244:247], v[188:191], v[164:167], v[244:247]\n"
    "v_rcp_f32 v67, v67\n"
    "v_mfma_f32_16x16x32_bf16 v[240:243], v[188:191], v[160:163], v[240:243]\n"
    "v_mfma_f32_16x16x32_bf16 v[240:243], v[156:159], v[128:131], v[240:243]\n"
    "s_waitcnt vmcnt(7)\n"
    "s_barrier\n"
    "s_add_u32 m0, s35, 98304\n"
    "s_nop 0\n"
    "global_load_lds_dwordx4 v46, s[30:31] offset:0\n"
    "ds_read_b128 v[128:131], v56 offset:0\n"
    "ds_read_b128 v[132:135], v56 offset:2048\n"
    "ds_read_b128 v[136:139], v56 offset:4096\n"
    "ds_read_b128 v[140:143], v56 offset:6144\n"
    "global_load_lds_dwordx4 v47, s[30:31] offset:1024\n"
    "s_bfe_u32 s66, s29, 0x80008\n"
    "s_mul_i32 s66, s66, 53248\n"
    "s_add_u32 s30, s6, s66\n"
    "s_addc_u32 s31, s7, 0\n"
    "ds_read_b64_tr_b16 v[144:145], v76 offset:0\n"
    "ds_read_b64_tr_b16 v[146:147], v76 offset:1024\n"
    "ds_read_b64_tr_b16 v[148:149], v77 offset:0\n"
    "ds_read_b64_tr_b16 v[150:151], v77 offset:1024\n"
    "s_add_u32 m0, s34, 98304\n"
    "s_nop 0\n"
    "global_load_lds_dwordx4 v52, s[36:37] offset:0\n"
    "ds_read_b64_tr_b16 v[152:153], v78 offset:0\n"
    "ds_read_b64_tr_b16 v[154:155], v78 offset:1024\n"
    "ds_read_b64_tr_b16 v[156:157], v79 offset:0\n"
    "ds_read_b64_tr_b16 v[158:159], v79 offset:1024\n"
    "global_load_lds_dwordx4 v53, s[36:37] offset:1024\n"
    "ds_read_b128 v[160:163], v57 offset:0\n"
    "ds_read_b128 v[164:167], v57 offset:2048\n"
    "ds_read_b128 v[168:171], v57 offset:4096\n"
    "ds_read_b128 v[172:175], v57 offset:6144\n"
    "global_load_lds_dwordx4 v54, s[36:37] offset:2048\n"
    "ds_read_b64_tr_b16 v[176:177], v76 offset:8192\n"
    "ds_read_b64_tr_b16 v[178:179], v76 offset:9216\n"
    "ds_read_b64_tr_b16 v[180:181], v77 offset:8192\n"
    "ds_read_b64_tr_b16 v[182:183], v77 offset:9216\n"
    "global_load_lds_dwordx4 v55, s[36:37] offset:3072\n"
    "ds_read_b64_tr_b16 v[184:185], v78 offset:8192\n"
    "ds_read_b64_tr_b16 v[186:187], v78 offset:9216\n"
    "ds_read_b64_tr_b16 v[188:189], v79 offset:8192\n"
    "ds_read_b64_tr_b16 v[190:191], v79 offset:9216\n"
    "s_waitcnt vmcnt(10) lgkmcnt(0)\n"
    "s_barrier\n"
    "v_mfma_f32_16x16x32_bf16 v[192:195], v[144:147], v[128:131], v[192:195]\n"
    "v_lshlrev_b32 v121, 7, v121\n"
    "v_mfma_f32_16x16x32_bf16 v[192:195], v[176:179], v[160:163], v[192:195]\n"
    "v_add_u32_dpp v52, v120, v48 quad_perm:[0,0,0,0] row_mask:0xf bank_mask:0xf\n"
    "v_mfma_f32_16x16x32_bf16 v[196:199], v[176:179], v[164:167], v[196:199]\n"
    "v_add_u32_dpp v53, v120, v49 quad_perm:[1,1,1,1] row_mask:0xf bank_mask:0xf\n"
    "v_mfma_f32_16x16x32_bf16 v[196:199], v[144:147], v[132:135], v[196:199]\n"
    "v_add_u32_dpp v54, v120, v50 quad_perm:[2,2,2,2] row_mask:0xf bank_mask:0xf\n"
    "v_mfma_f32_16x16x32_bf16 v[200:203], v[144:147], v[136:139], v[200:203]\n"
    "v_add_u32_dpp v55, v120, v51 quad_perm:[3,3,3,3] row_mask:0xf bank_mask:0xf\n"
    "v_mfma_f32_16x16x32_bf16 v[200:203], v[176:179], v[168:171], v[200:203]\n"
    "s_movk_i32 s43, 15744\n"
    "v_pk_maximum3_f16 v21, v28, v28, s43 op_sel_hi:[1,1,0] neg_lo:[0,1,0] neg_hi:[0,1,0]\n"
    "v_mfma_f32_16x16x32_bf16 v[204:207], v[176:179], v[172:175], v[204:207]\n"
    "v_pk_maximum3_f16 v21, v29, v29, v21 neg_lo:[0,1,0] neg_hi:[0,1,0]\n"
    "v_mfma_f32_16x16x32_bf16 v[204:207], v[144:147], v[140:143], v[204:207]\n"
    "v_mfma_f32_16x16x32_bf16 v[220:223], v[148:151], v[140:143], v[220:223]\n"
    "v_pk_maximum3_f16 v21, v30, v30, v21 neg_lo:[0,1,0] neg_hi:[0,1,0]\n"
    "v_mfma_f32_16x16x32_bf16 v[220:223], v[180:183], v[172:175], v[220:223]\n"
    "v_pk_maximum3_f16 v21, v31, v31, v21 neg_lo:[0,1,0] neg_hi:[0,1,0]\n"
    "v_mfma_f32_16x16x32_bf16 v[216:219], v[180:183], v[168:171], v[216:219]\n"
    "v_pk_maximum3_f16 v21, v32, v32, v21 neg_lo:[0,1,0] neg_hi:[0,1,0]\n"
    "v_mfma_f32_16x16x32_bf16 v[216:219], v[148:151], v[136:139], v[216:219]\n"
    "v_pk_maximum3_f16 v21, v33, v33, v21 neg_lo:[0,1,0] neg_hi:[0,1,0]\n"
    "v_mfma_f32_16x16x32_bf16 v[212:215], v[148:151], v[132:135], v[212:215]\n"
    "v_pk_maximum3_f16 v21, v34, v34, v21 neg_lo:[0,1,0] neg_hi:[0,1,0]\n"
    "v_mfma_f32_16x16x32_bf16 v[212:215], v[180:183], v[164:167], v[212:215]\n"
    "v_pk_maximum3_f16 v21, v35, v35, v21 neg_lo:[0,1,0] neg_hi:[0,1,0]\n"
    "v_mfma_f32_16x16x32_bf16 v[208:211], v[180:183], v[160:163], v[208:211]\n"
    "v_pk_maximum3_f16 v21, v36, v36, v21 neg_lo:[0,1,0] neg_hi:[0,1,0]\n"
    "v_mfma_f32_16x16x32_bf16 v[208:211], v[148:151], v[128:131], v[208:211]\n"
    "v_mfma_f32_16x16x32_bf16 v[224:227], v[152:155], v[128:131], v[224:227]\n"
    "v_pk_maximum3_f16 v21, v37, v37, v21 neg_lo:[0,1,0] neg_hi:[0,1,0]\n"
    "v_mfma_f32_16x16x32_bf16 v[224:227], v[184:187], v[160:163], v[224:227]\n"
    "v_pk_maximum3_f16 v21, v38, v38, v21 neg_lo:[0,1,0] neg_hi:[0,1,0]\n"
    "v_mfma_f32_16x16x32_bf16 v[228:231], v[184:187], v[164:167], v[228:231]\n"
    "v_pk_maximum3_f16 v21, v39, v39, v21 neg_lo:[0,1,0] neg_hi:[0,1,0]\n"
    "v_mfma_f32_16x16x32_bf16 v[228:231], v[152:155], v[132:135], v[228:231]\n"
    "v_pk_maximum3_f16 v21, v40, v40, v21 neg_lo:[0,1,0] neg_hi:[0,1,0]\n"
    "v_mfma_f32_16x16x32_bf16 v[232:235], v[152:155], v[136:139], v[232:235]\n"
    "v_pk_max_u16 v21, v21, v21 op_sel:[0,1] op_sel_hi:[1,0]\n"
    "v_mfma_f32_16x16x32_bf16 v[232:235], v[184:187], v[168:171], v[232:235]\n"
    "v_rcp_f32 v68, v68\n"
    "v_mfma_f32_16x16x32_bf16 v[236:239], v[184:187], v[172:175], v[236:239]\n"
    "v_rcp_f32 v69, v69\n"
    "v_mfma_f32_16x16x32_bf16 v[236:239], v[152:155], v[140:143], v[236:239]\n"
    "v_mfma_f32_16x16x32_bf16 v[252:255], v[156:159], v[140:143], v[252:255]\n"
    "v_rcp_f32 v70, v70\n"
    "v_mfma_f32_16x16x32_bf16 v[252:255], v[188:191], v[172:175], v[252:255]\n"
    "v_rcp_f32 v71, v71\n"
    "v_mfma_f32_16x16x32_bf16 v[248:251], v[188:191], v[168:171], v[248:251]\n"
    "v_rcp_f32 v72, v72\n"
    "v_mfma_f32_16x16x32_bf16 v[248:251], v[156:159], v[136:139], v[248:251]\n"
    "v_rcp_f32 v73, v73\n"
    "v_mfma_f32_16x16x32_bf16 v[244:247], v[156:159], v[132:135], v[244:247]\n"
    "v_rcp_f32 v74, v74\n"
    "v_mfma_f32_16x16x32_bf16 v[244:247], v[188:191], v[164:167], v[244:247]\n"
    "v_rcp_f32 v75, v75\n"
    "v_mfma_f32_16x16x32_bf16 v[240:243], v[188:191], v[160:163], v[240:243]\n"
    "v_mfma_f32_16x16x32_bf16 v[240:243], v[156:159], v[128:131], v[240:243]\n"
    "s_waitcnt vmcnt(6)\n"
    "s_barrier\n"
    "s_add_u32 m0, s35, 0\n"
    "s_nop 0\n"
    "global_load_lds_dwordx4 v46, s[30:31] offset:0\n"
    "ds_read_b128 v[128:131], v56 offset:49152\n"
    "ds_read_b128 v[132:135], v56 offset:51200\n"
    "ds_read_b128 v[136:139], v56 offset:53248\n"
    "ds_read_b128 v[140:143], v56 offset:55296\n"
    "global_load_lds_dwordx4 v47, s[30:31] offset:1024\n"
    "s_add_u32 s30, s30, 212992\n"
    "s_addc_u32 s31, s31, 0\n"
    "ds_read_b64_tr_b16 v[144:145], v76 offset:49152\n"
    "ds_read_b64_tr_b16 v[146:147], v76 offset:50176\n"
    "ds_read_b64_tr_b16 v[148:149], v77 offset:49152\n"
    "ds_read_b64_tr_b16 v[150:151], v77 offset:50176\n"
    "s_add_u32 m0, s34, 0\n"
    "s_nop 0\n"
    "global_load_lds_dwordx4 v52, s[36:37] offset:0\n"
    "ds_read_b64_tr_b16 v[152:153], v78 offset:49152\n"
    "ds_read_b64_tr_b16 v[154:155], v78 offset:50176\n"
    "ds_read_b64_tr_b16 v[156:157], v79 offset:49152\n"
    "ds_read_b64_tr_b16 v[158:159], v79 offset:50176\n"
    "global_load_lds_dwordx4 v53, s[36:37] offset:1024\n"
    "ds_read_b128 v[160:163], v57 offset:49152\n"
    "ds_read_b128 v[164:167], v57 offset:51200\n"
    "ds_read_b128 v[168:171], v57 offset:53248\n"
    "ds_read_b128 v[172:175], v57 offset:55296\n"
    "global_load_lds_dwordx4 v54, s[36:37] offset:2048\n"
    "ds_read_b64_tr_b16 v[176:177], v76 offset:57344\n"
    "ds_read_b64_tr_b16 v[178:179], v76 offset:58368\n"
    "ds_read_b64_tr_b16 v[180:181], v77 offset:57344\n"
    "ds_read_b64_tr_b16 v[182:183], v77 offset:58368\n"
    "global_load_lds_dwordx4 v55, s[36:37] offset:3072\n"
    "ds_read_b64_tr_b16 v[184:185], v78 offset:57344\n"
    "ds_read_b64_tr_b16 v[186:187], v78 offset:58368\n"
    "ds_read_b64_tr_b16 v[188:189], v79 offset:57344\n"
    "ds_read_b64_tr_b16 v[190:191], v79 offset:58368\n"
    "s_waitcnt vmcnt(10) lgkmcnt(0)\n"
    "s_barrier\n"
    "v_mfma_f32_16x16x32_bf16 v[192:195], v[144:147], v[128:131], v[192:195]\n"
    "v_add_u32_dpp v52, v121, v48 quad_perm:[0,0,0,0] row_mask:0xf bank_mask:0xf\n"
    "s_mov_b32 s42, 0x71800000\n"
    "v_mfma_f32_16x16x32_bf16 v[192:195], v[176:179], v[160:163], v[192:195]\n"
    "v_add_u32_dpp v53, v121, v49 quad_perm:[1,1,1,1] row_mask:0xf bank_mask:0xf\n"
    "v_mfma_f32_16x16x32_bf16 v[196:199], v[176:179], v[164:167], v[196:199]\n"
    "v_add_u32_dpp v54, v121, v50 quad_perm:[2,2,2,2] row_mask:0xf bank_mask:0xf\n"
    "v_mfma_f32_16x16x32_bf16 v[196:199], v[144:147], v[132:135], v[196:199]\n"
    "v_add_u32_dpp v55, v121, v51 quad_perm:[3,3,3,3] row_mask:0xf bank_mask:0xf\n"
    "v_mfma_f32_16x16x32_bf16 v[200:203], v[144:147], v[136:139], v[200:203]\n"
    "v_bfe_u32 v41, v1, 4, 1\n"
    "v_mfma_f32_16x16x32_bf16 v[200:203], v[176:179], v[168:171], v[200:203]\n"
    "v_lshlrev_b32 v41, 2, v41\n"
    "v_mfma_f32_16x16x32_bf16 v[204:207], v[176:179], v[172:175], v[204:207]\n"
    "v_add_u32 v41, 154112, v41\n"
    "v_mfma_f32_16x16x32_bf16 v[204:207], v[144:147], v[140:143], v[204:207]\n"
    "v_med3_f32 v60, v60, s42, -s42\n"
    "v_mfma_f32_16x16x32_bf16 v[220:223], v[148:151], v[140:143], v[220:223]\n"
    "v_max_u32_dpp v21, v21, v21 quad_perm:[1,0,3,2] row_mask:0xf bank_mask:0xf\n"
    "v_mfma_f32_16x16x32_bf16 v[220:223], v[180:183], v[172:175], v[220:223]\n"
    "v_med3_f32 v61, v61, s42, -s42\n"
    "v_mfma_f32_16x16x32_bf16 v[216:219], v[180:183], v[168:171], v[216:219]\n"
    "v_max_u32_dpp v21, v21, v21 quad_perm:[2,3,0,1] row_mask:0xf bank_mask:0xf\n"
    "v_mfma_f32_16x16x32_bf16 v[216:219], v[148:151], v[136:139], v[216:219]\n"
    "v_med3_f32 v62, v62, s42, -s42\n"
    "v_mfma_f32_16x16x32_bf16 v[212:215], v[148:151], v[132:135], v[212:215]\n"
    "v_max_u32_dpp v21, v21, v21 row_half_mirror row_mask:0xf bank_mask:0xf\n"
    "v_mfma_f32_16x16x32_bf16 v[212:215], v[180:183], v[164:167], v[212:215]\n"
    "v_med3_f32 v63, v63, s42, -s42\n"
    "v_mfma_f32_16x16x32_bf16 v[208:211], v[180:183], v[160:163], v[208:211]\n"
    "v_max_u32_dpp v21, v21, v21 row_mirror row_mask:0xf bank_mask:0xf\n"
    "v_mfma_f32_16x16x32_bf16 v[208:211], v[148:151], v[128:131], v[208:211]\n"
    "v_med3_f32 v64, v64, s42, -s42\n"
    "v_mfma_f32_16x16x32_bf16 v[224:227], v[152:155], v[128:131], v[224:227]\n"
    "v_max_u32_dpp v21, v21, v21 row_bcast:15 row_mask:0xa bank_mask:0xf\n"
    "v_mfma_f32_16x16x32_bf16 v[224:227], v[184:187], v[160:163], v[224:227]\n"
    "v_med3_f32 v65, v65, s42, -s42\n"
    "v_mfma_f32_16x16x32_bf16 v[228:231], v[184:187], v[164:167], v[228:231]\n"
    "v_max_u32_dpp v21, v21, v21 row_bcast:31 row_mask:0xc bank_mask:0xf\n"
    "v_mfma_f32_16x16x32_bf16 v[228:231], v[152:155], v[132:135], v[228:231]\n"
    "v_med3_f32 v66, v66, s42, -s42\n"
    "v_mfma_f32_16x16x32_bf16 v[232:235], v[152:155], v[136:139], v[232:235]\n"
    "v_readlane_b32 s47, v21, 63\n"
    "v_mfma_f32_16x16x32_bf16 v[232:235], v[184:187], v[168:171], v[232:235]\n"
    "v_med3_f32 v67, v67, s42, -s42\n"
    "v_mfma_f32_16x16x32_bf16 v[236:239], v[184:187], v[172:175], v[236:239]\n"
    "s_and_b32 s50, s47, 0x7f80\n"
    "v_mfma_f32_16x16x32_bf16 v[236:239], v[152:155], v[140:143], v[236:239]\n"
    "s_lshr_b32 s47, s50, 3\n"
    "v_mfma_f32_16x16x32_bf16 v[252:255], v[156:159], v[140:143], v[252:255]\n"
    "s_sub_u32 s50, s50, 2816\n"
    "v_med3_f32 v68, v68, s42, -s42\n"
    "v_mfma_f32_16x16x32_bf16 v[252:255], v[188:191], v[172:175], v[252:255]\n"
    "s_mul_i32 s51, s50, -8\n"
    "v_med3_f32 v69, v69, s42, -s42\n"
    "v_mfma_f32_16x16x32_bf16 v[248:251], v[188:191], v[168:171], v[248:251]\n"
    "v_med3_f32 v70, v70, s42, -s42\n"
    "v_mfma_f32_16x16x32_bf16 v[248:251], v[156:159], v[136:139], v[248:251]\n"
    "v_med3_f32 v71, v71, s42, -s42\n"
    "v_mfma_f32_16x16x32_bf16 v[244:247], v[156:159], v[132:135], v[244:247]\n"
    "v_med3_f32 v72, v72, s42, -s42\n"
    "v_mfma_f32_16x16x32_bf16 v[244:247], v[188:191], v[164:167], v[244:247]\n"
    "v_med3_f32 v73, v73, s42, -s42\n"
    "v_mfma_f32_16x16x32_bf16 v[240:243], v[188:191], v[160:163], v[240:243]\n"
    "v_med3_f32 v74, v74, s42, -s42\n"
    "v_mfma_f32_16x16x32_bf16 v[240:243], v[156:159], v[128:131], v[240:243]\n"
    "v_med3_f32 v75, v75, s42, -s42\n"
    "s_waitcnt vmcnt(6)\n"
    "s_barrier\n"
    "s_add_u32 m0, s35, 49152\n"
    "s_nop 0\n"
    "global_load_lds_dwordx4 v46, s[30:31] offset:0\n"
    "ds_read_b128 v[128:131], v58 offset:0\n"
    "ds_read_b128 v[132:135], v58 offset:2048\n"
    "ds_read_b128 v[136:139], v58 offset:4096\n"
    "ds_read_b128 v[140:143], v58 offset:6144\n"
    "global_load_lds_dwordx4 v47, s[30:31] offset:1024\n"
    "ds_read_b64_tr_b16 v[144:145], v80 offset:0\n"
    "ds_read_b64_tr_b16 v[146:147], v80 offset:1024\n"
    "ds_read_b64_tr_b16 v[148:149], v81 offset:0\n"
    "ds_read_b64_tr_b16 v[150:151], v81 offset:1024\n"
    "s_add_u32 m0, s34, 49152\n"
    "s_nop 0\n"
    "global_load_lds_dwordx4 v52, s[36:37] offset:0\n"
    "ds_read_b64_tr_b16 v[152:153], v82 offset:0\n"
    "ds_read_b64_tr_b16 v[154:155], v82 offset:1024\n"
    "ds_read_b64_tr_b16 v[156:157], v83 offset:0\n"
    "ds_read_b64_tr_b16 v[158:159], v83 offset:1024\n"
    "global_load_lds_dwordx4 v53, s[36:37] offset:1024\n"
    "ds_read_b128 v[160:163], v59 offset:0\n"
    "ds_read_b128 v[164:167], v59 offset:2048\n"
    "ds_read_b128 v[168:171], v59 offset:4096\n"
    "ds_read_b128 v[172:175], v59 offset:6144\n"
    "global_load_lds_dwordx4 v54, s[36:37] offset:2048\n"
    "ds_read_b64_tr_b16 v[176:177], v80 offset:8192\n"
    "ds_read_b64_tr_b16 v[178:179], v80 offset:9216\n"
    "ds_read_b64_tr_b16 v[180:181], v81 offset:8192\n"
    "ds_read_b64_tr_b16 v[182:183], v81 offset:9216\n"
    "global_load_lds_dwordx4 v55, s[36:37] offset:3072\n"
    "ds_read_b64_tr_b16 v[184:185], v82 offset:8192\n"
    "ds_read_b64_tr_b16 v[186:187], v82 offset:9216\n"
    "ds_read_b64_tr_b16 v[188:189], v83 offset:8192\n"
    "ds_read_b64_tr_b16 v[190:191], v83 offset:9216\n"
    "s_waitcnt vmcnt(10) lgkmcnt(0)\n"
    "s_barrier\n"
    "v_mfma_f32_16x16x32_bf16 v[192:195], v[144:147], v[128:131], v[192:195]\n"
    "v_pk_maximum3_f16 v21, v28, v28, s50 op_sel_hi:[1,1,0] neg_lo:[0,1,0] neg_hi:[0,1,0]\n"
    "v_mfma_f32_16x16x32_bf16 v[192:195], v[176:179], v[160:163], v[192:195]\n"
    "v_pk_mad_u16 v21, v21, 8, s51 op_sel_hi:[1,0,0]\n"
    "v_mfma_f32_16x16x32_bf16 v[196:199], v[176:179], v[164:167], v[196:199]\n"
    "v_bfi_b32 v28, s57, v21, v28\n"
    "v_mfma_f32_16x16x32_bf16 v[196:199], v[144:147], v[132:135], v[196:199]\n"
    "v_mfma_f32_16x16x32_bf16 v[200:203], v[144:147], v[136:139], v[200:203]\n"
    "v_pk_maximum3_f16 v12, v29, v29, s50 op_sel_hi:[1,1,0] neg_lo:[0,1,0] neg_hi:[0,1,0]\n"
    "v_mfma_f32_16x16x32_bf16 v[200:203], v[176:179], v[168:171], v[200:203]\n"
    "v_pk_mad_u16 v12, v12, 8, s51 op_sel_hi:[1,0,0]\n"
    "v_mfma_f32_16x16x32_bf16 v[204:207], v[176:179], v[172:175], v[204:207]\n"
    "v_bfi_b32 v29, s57, v12, v29\n"
    "v_mfma_f32_16x16x32_bf16 v[204:207], v[144:147], v[140:143], v[204:207]\n"
    "v_mfma_f32_16x16x32_bf16 v[220:223], v[148:151], v[140:143], v[220:223]\n"
    "v_pk_maximum3_f16 v21, v30, v30, s50 op_sel_hi:[1,1,0] neg_lo:[0,1,0] neg_hi:[0,1,0]\n"
    "v_mfma_f32_16x16x32_bf16 v[220:223], v[180:183], v[172:175], v[220:223]\n"
    "v_pk_mad_u16 v21, v21, 8, s51 op_sel_hi:[1,0,0]\n"
    "v_mfma_f32_16x16x32_bf16 v[216:219], v[180:183], v[168:171], v[216:219]\n"
    "v_bfi_b32 v30, s57, v21, v30\n"
    "v_mfma_f32_16x16x32_bf16 v[216:219], v[148:151], v[136:139], v[216:219]\n"
    "v_mfma_f32_16x16x32_bf16 v[212:215], v[148:151], v[132:135], v[212:215]\n"
    "v_pk_maximum3_f16 v12, v31, v31, s50 op_sel_hi:[1,1,0] neg_lo:[0,1,0] neg_hi:[0,1,0]\n"
    "v_mfma_f32_16x16x32_bf16 v[212:215], v[180:183], v[164:167], v[212:215]\n"
    "v_pk_mad_u16 v12, v12, 8, s51 op_sel_hi:[1,0,0]\n"
    "v_mfma_f32_16x16x32_bf16 v[208:211], v[180:183], v[160:163], v[208:211]\n"
    "v_bfi_b32 v31, s57, v12, v31\n"
    "v_mfma_f32_16x16x32_bf16 v[208:211], v[148:151], v[128:131], v[208:211]\n"
    "v_mfma_f32_16x16x32_bf16 v[224:227], v[152:155], v[128:131], v[224:227]\n"
    "v_pk_maximum3_f16 v21, v32, v32, s50 op_sel_hi:[1,1,0] neg_lo:[0,1,0] neg_hi:[0,1,0]\n"
    "v_mfma_f32_16x16x32_bf16 v[224:227], v[184:187], v[160:163], v[224:227]\n"
    "v_pk_mad_u16 v21, v21, 8, s51 op_sel_hi:[1,0,0]\n"
    "v_mfma_f32_16x16x32_bf16 v[228:231], v[184:187], v[164:167], v[228:231]\n"
    "v_bfi_b32 v32, s57, v21, v32\n"
    "v_mfma_f32_16x16x32_bf16 v[228:231], v[152:155], v[132:135], v[228:231]\n"
    "v_mfma_f32_16x16x32_bf16 v[232:235], v[152:155], v[136:139], v[232:235]\n"
    "v_pk_maximum3_f16 v12, v33, v33, s50 op_sel_hi:[1,1,0] neg_lo:[0,1,0] neg_hi:[0,1,0]\n"
    "v_mfma_f32_16x16x32_bf16 v[232:235], v[184:187], v[168:171], v[232:235]\n"
    "v_pk_mad_u16 v12, v12, 8, s51 op_sel_hi:[1,0,0]\n"
    "v_mfma_f32_16x16x32_bf16 v[236:239], v[184:187], v[172:175], v[236:239]\n"
    "v_bfi_b32 v33, s57, v12, v33\n"
    "v_mfma_f32_16x16x32_bf16 v[236:239], v[152:155], v[140:143], v[236:239]\n"
    "v_mfma_f32_16x16x32_bf16 v[252:255], v[156:159], v[140:143], v[252:255]\n"
    "v_pk_maximum3_f16 v21, v34, v34, s50 op_sel_hi:[1,1,0] neg_lo:[0,1,0] neg_hi:[0,1,0]\n"
    "v_mfma_f32_16x16x32_bf16 v[252:255], v[188:191], v[172:175], v[252:255]\n"
    "v_pk_mad_u16 v21, v21, 8, s51 op_sel_hi:[1,0,0]\n"
    "v_mfma_f32_16x16x32_bf16 v[248:251], v[188:191], v[168:171], v[248:251]\n"
    "v_bfi_b32 v34, s57, v21, v34\n"
    "v_mfma_f32_16x16x32_bf16 v[248:251], v[156:159], v[136:139], v[248:251]\n"
    "v_mfma_f32_16x16x32_bf16 v[244:247], v[156:159], v[132:135], v[244:247]\n"
    "v_lshlrev_b32 v52, 4, v1\n"
    "v_mfma_f32_16x16x32_bf16 v[244:247], v[188:191], v[164:167], v[244:247]\n"
    "v_add_u32 v53, 4096, v52\n"
    "v_mfma_f32_16x16x32_bf16 v[240:243], v[188:191], v[160:163], v[240:243]\n"
    "v_mfma_f32_16x16x32_bf16 v[240:243], v[156:159], v[128:131], v[240:243]\n"
    "s_waitcnt vmcnt(6)\n"
    "s_barrier\n"
    "s_mul_i32 s42, s20, 5376\n"
    "s_add_u32 s40, s42, 1048576\n"
    "s_add_u32 s38, s16, s40\n"
    "s_addc_u32 s39, s17, 0\n"
    "s_add_u32 m0, s42, 98304\n"
    "s_nop 0\n"
    "global_load_lds_dwordx4 v52, s[38:39] offset:0 sc1\n"
    "ds_read_b128 v[128:131], v56 offset:0\n"
    "ds_read_b128 v[132:135], v56 offset:2048\n"
    "ds_read_b128 v[136:139], v56 offset:4096\n"
    "ds_read_b128 v[140:143], v56 offset:6144\n"
    "global_load_lds_dwordx4 v52, s[38:39] offset:1024 sc1\n"
    "ds_read_b64_tr_b16 v[144:145], v76 offset:0\n"
    "ds_read_b64_tr_b16 v[146:147], v76 offset:1024\n"
    "ds_read_b64_tr_b16 v[148:149], v77 offset:0\n"
    "ds_read_b64_tr_b16 v[150:151], v77 offset:1024\n"
    "global_load_lds_dwordx4 v52, s[38:39] offset:2048 sc1\n"
    "ds_read_b64_tr_b16 v[152:153], v78 offset:0\n"
    "ds_read_b64_tr_b16 v[154:155], v78 offset:1024\n"
    "ds_read_b64_tr_b16 v[156:157], v79 offset:0\n"
    "ds_read_b64_tr_b16 v[158:159], v79 offset:1024\n"
    "global_load_lds_dwordx4 v52, s[38:39] offset:3072 sc1\n"
    "ds_read_b128 v[160:163], v57 offset:0\n"
    "ds_read_b128 v[164:167], v57 offset:2048\n"
    "ds_read_b128 v[168:171], v57 offset:4096\n"
    "ds_read_b128 v[172:175], v57 offset:6144\n"
    "s_add_u32 m0, s42, 102400\n"
    "s_nop 0\n"
    "global_load_lds_dwordx4 v53, s[38:39] offset:0 sc1\n"
    "ds_read_b64_tr_b16 v[176:177], v76 offset:8192\n"
    "ds_read_b64_tr_b16 v[178:179], v76 offset:9216\n"
    "ds_read_b64_tr_b16 v[180:181], v77 offset:8192\n"
    "ds_read_b64_tr_b16 v[182:183], v77 offset:9216\n"
    "global_load_lds_dwordx4 v53, s[38:39] offset:256 sc1\n"
    "ds_read_b64_tr_b16 v[184:185], v78 offset:8192\n"
    "ds_read_b64_tr_b16 v[186:187], v78 offset:9216\n"
    "ds_read_b64_tr_b16 v[188:189], v79 offset:8192\n"
    "ds_read_b64_tr_b16 v[190:191], v79 offset:9216\n"
    "s_waitcnt vmcnt(10) lgkmcnt(0)\n"
    "s_barrier\n"
    "v_mfma_f32_16x16x32_bf16 v[192:195], v[144:147], v[128:131], v[192:195]\n"
    "v_pk_maximum3_f16 v12, v35, v35, s50 op_sel_hi:[1,1,0] neg_lo:[0,1,0] neg_hi:[0,1,0]\n"
    "v_mfma_f32_16x16x32_bf16 v[192:195], v[176:179], v[160:163], v[192:195]\n"
    "v_pk_mad_u16 v12, v12, 8, s51 op_sel_hi:[1,0,0]\n"
    "v_mfma_f32_16x16x32_bf16 v[196:199], v[176:179], v[164:167], v[196:199]\n"
    "v_mfma_f32_16x16x32_bf16 v[196:199], v[144:147], v[132:135], v[196:199]\n"
    "v_bfi_b32 v35, s57, v12, v35\n"
    "v_mfma_f32_16x16x32_bf16 v[200:203], v[144:147], v[136:139], v[200:203]\n"
    "v_pk_maximum3_f16 v21, v36, v36, s50 op_sel_hi:[1,1,0] neg_lo:[0,1,0] neg_hi:[0,1,0]\n"
    "v_mfma_f32_16x16x32_bf16 v[200:203], v[176:179], v[168:171], v[200:203]\n"
    "v_mfma_f32_16x16x32_bf16 v[204:207], v[176:179], v[172:175], v[204:207]\n"
    "v_pk_mad_u16 v21, v21, 8, s51 op_sel_hi:[1,0,0]\n"
    "v_mfma_f32_16x16x32_bf16 v[204:207], v[144:147], v[140:143], v[204:207]\n"
    "v_bfi_b32 v36, s57, v21, v36\n"
    "v_mfma_f32_16x16x32_bf16 v[220:223], v[148:151], v[140:143], v[220:223]\n"
    "v_mfma_f32_16x16x32_bf16 v[220:223], v[180:183], v[172:175], v[220:223]\n"
    "v_pk_maximum3_f16 v12, v37, v37, s50 op_sel_hi:[1,1,0] neg_lo:[0,1,0] neg_hi:[0,1,0]\n"
    "v_mfma_f32_16x16x32_bf16 v[216:219], v[180:183], v[168:171], v[216:219]\n"
    "v_pk_mad_u16 v12, v12, 8, s51 op_sel_hi:[1,0,0]\n"
    "v_mfma_f32_16x16x32_bf16 v[216:219], v[148:151], v[136:139], v[216:219]\n"
    "v_mfma_f32_16x16x32_bf16 v[212:215], v[148:151], v[132:135], v[212:215]\n"
    "v_bfi_b32 v37, s57, v12, v37\n"
    "v_mfma_f32_16x16x32_bf16 v[212:215], v[180:183], v[164:167], v[212:215]\n"
    "v_pk_maximum3_f16 v21, v38, v38, s50 op_sel_hi:[1,1,0] neg_lo:[0,1,0] neg_hi:[0,1,0]\n"
    "v_mfma_f32_16x16x32_bf16 v[208:211], v[180:183], v[160:163], v[208:211]\n"
    "v_mfma_f32_16x16x32_bf16 v[208:211], v[148:151], v[128:131], v[208:211]\n"
    "v_pk_mad_u16 v21, v21, 8, s51 op_sel_hi:[1,0,0]\n"
    "v_mfma_f32_16x16x32_bf16 v[224:227], v[152:155], v[128:131], v[224:227]\n"
    "v_mfma_f32_16x16x32_bf16 v[224:227], v[184:187], v[160:163], v[224:227]\n"
    "v_bfi_b32 v38, s57, v21, v38\n"
    "v_mfma_f32_16x16x32_bf16 v[228:231], v[184:187], v[164:167], v[228:231]\n"
    "v_pk_maximum3_f16 v12, v39, v39, s50 op_sel_hi:[1,1,0] neg_lo:[0,1,0] neg_hi:[0,1,0]\n"
    "v_mfma_f32_16x16x32_bf16 v[228:231], v[152:155], v[132:135], v[228:231]\n"
    "v_mfma_f32_16x16x32_bf16 v[232:235], v[152:155], v[136:139], v[232:235]\n"
    "v_pk_mad_u16 v12, v12, 8, s51 op_sel_hi:[1,0,0]\n"
    "v_mfma_f32_16x16x32_bf16 v[232:235], v[184:187], v[168:171], v[232:235]\n"
    "v_bfi_b32 v39, s57, v12, v39\n"
    "v_mfma_f32_16x16x32_bf16 v[236:239], v[184:187], v[172:175], v[236:239]\n"
    "v_mfma_f32_16x16x32_bf16 v[236:239], v[152:155], v[140:143], v[236:239]\n"
    "v_pk_maximum3_f16 v21, v40, v40, s50 op_sel_hi:[1,1,0] neg_lo:[0,1,0] neg_hi:[0,1,0]\n"
    "v_mfma_f32_16x16x32_bf16 v[252:255], v[156:159], v[140:143], v[252:255]\n"
    "v_pk_mad_u16 v21, v21, 8, s51 op_sel_hi:[1,0,0]\n"
    "v_mfma_f32_16x16x32_bf16 v[252:255], v[188:191], v[172:175], v[252:255]\n"
    "v_mfma_f32_16x16x32_bf16 v[248:251], v[188:191], v[168:171], v[248:251]\n"
    "v_bfi_b32 v40, s57, v21, v40\n"
    "v_mfma_f32_16x16x32_bf16 v[248:251], v[156:159], v[136:139], v[248:251]\n"
    "s_sub_u32 s43, 4112, s47\n"
    "s_lshl_b32 s43, s43, 19\n"
    "v_cvt_pkrtz_f16_f32 v12, s43, s43\n"
    "v_mfma_f32_16x16x32_bf16 v[244:247], v[156:159], v[132:135], v[244:247]\n"
    "v_mfma_f32_16x16x32_bf16 v[244:247], v[188:191], v[164:167], v[244:247]\n"
    "v_lshl_or_b32 v21, v12, 16, s47\n"
    "v_mfma_f32_16x16x32_bf16 v[240:243], v[188:191], v[160:163], v[240:243]\n"
    "v_mfma_f32_16x16x32_bf16 v[240:243], v[156:159], v[128:131], v[240:243]\n"
    "s_waitcnt vmcnt(6)\n"
    "s_barrier\n"
    "ds_read_b128 v[128:131], v56 offset:49152\n"
    "ds_read_b128 v[132:135], v56 offset:51200\n"
    "ds_read_b128 v[136:139], v56 offset:53248\n"
    "ds_read_b128 v[140:143], v56 offset:55296\n"
    "ds_read_b64_tr_b16 v[144:145], v76 offset:49152\n"
    "ds_read_b64_tr_b16 v[146:147], v76 offset:50176\n"
    "ds_read_b64_tr_b16 v[148:149], v77 offset:49152\n"
    "ds_read_b64_tr_b16 v[150:151], v77 offset:50176\n"
    "ds_read_b64_tr_b16 v[152:153], v78 offset:49152\n"
    "ds_read_b64_tr_b16 v[154:155], v78 offset:50176\n"
    "ds_read_b64_tr_b16 v[156:157], v79 offset:49152\n"
    "ds_read_b64_tr_b16 v[158:159], v79 offset:50176\n"
    "ds_read_b128 v[160:163], v57 offset:49152\n"
    "ds_read_b128 v[164:167], v57 offset:51200\n"
    "ds_read_b128 v[168:171], v57 offset:53248\n"
    "ds_read_b128 v[172:175], v57 offset:55296\n"
    "ds_read_b64_tr_b16 v[176:177], v76 offset:57344\n"
    "ds_read_b64_tr_b16 v[178:179], v76 offset:58368\n"
    "ds_read_b64_tr_b16 v[180:181], v77 offset:57344\n"
    "ds_read_b64_tr_b16 v[182:183], v77 offset:58368\n"
    "ds_read_b64_tr_b16 v[184:185], v78 offset:57344\n"
    "ds_read_b64_tr_b16 v[186:187], v78 offset:58368\n"
    "ds_read_b64_tr_b16 v[188:189], v79 offset:57344\n"
    "ds_read_b64_tr_b16 v[190:191], v79 offset:58368\n"
    "s_cmp_lg_u32 s20, 3\n"
    "s_cbranch_scc1 .Lxd_on4\n"
    "s_mov_b64 exec, 1\n"
    "ds_write_b64 v8, v[26:27]\n"
    "s_mov_b64 exec, 15\n"
    "ds_write2_b32 v8, v22, v13 offset0:4 offset1:8\n"
    "s_mov_b64 exec, -1\n"
    ".Lxd_on4:\n"
    "s_waitcnt vmcnt(0) lgkmcnt(0)\n"
    "s_cmp_lg_u32 s20, 3\n"
    "s_cbranch_scc1 .Lxd_on5\n"
    "s_mov_b64 exec, 0x200\n"
    "global_store_dword v11, v43, s[16:17] sc1\n"
    "s_mov_b64 exec, -1\n"
    ".Lxd_on5:\n"
    "s_barrier\n"
    "s_setprio 3\n"
    "v_mfma_f32_16x16x32_bf16 v[192:195], v[144:147], v[128:131], v[192:195]\n"
    "v_add_u32 v42, 98304, v52\n"
    "v_mfma_f32_16x16x32_bf16 v[192:195], v[176:179], v[160:163], v[192:195]\n"
    "v_lshl_add_u32 v11, v1, 2, s3\n"
    "v_mfma_f32_16x16x32_bf16 v[196:199], v[176:179], v[164:167], v[196:199]\n"
    "v_mov_b32 v13, s58\n"
    "v_mfma_f32_16x16x32_bf16 v[196:199], v[144:147], v[132:135], v[196:199]\n"
    "s_mov_b32 s36, 0\n"
    "v_mfma_f32_16x16x32_bf16 v[200:203], v[144:147], v[136:139], v[200:203]\n"
    "v_mov_b32 v84, s65\n"
    "v_mfma_f32_16x16x32_bf16 v[200:203], v[176:179], v[168:171], v[200:203]\n"
    "v_lshl_add_u32 v7, s23, 12, v3\n"
    "v_mfma_f32_16x16x32_bf16 v[204:207], v[176:179], v[172:175], v[204:207]\n"
    "v_lshl_add_u32 v8, s23, 12, v4\n"
    "v_mfma_f32_16x16x32_bf16 v[204:207], v[144:147], v[140:143], v[204:207]\n"
    "v_lshl_add_u32 v9, s23, 12, v5\n"
    "v_mfma_f32_16x16x32_bf16 v[220:223], v[148:151], v[140:143], v[220:223]\n"
    "v_lshl_add_u32 v10, s23, 12, v6\n"
    "v_mfma_f32_16x16x32_bf16 v[220:223], v[180:183], v[172:175], v[220:223]\n"
    "v_mfma_f32_16x16x32_bf16 v[216:219], v[180:183], v[168:171], v[216:219]\n"
    "v_mul_f32 v76, v192, v192\n"
    "v_mul_f32 v77, v193, v193\n"
    "v_mfma_f32_16x16x32_bf16 v[216:219], v[148:151], v[136:139], v[216:219]\n"
    "v_mul_f32 v78, v196, v196\n"
    "v_mul_f32 v79, v197, v197\n"
    "v_mfma_f32_16x16x32_bf16 v[212:215], v[148:151], v[132:135], v[212:215]\n"
    "v_mul_f32 v80, v200, v200\n"
    "v_mul_f32 v81, v201, v201\n"
    "v_mfma_f32_16x16x32_bf16 v[212:215], v[180:183], v[164:167], v[212:215]\n"
    "v_mul_f32 v82, v204, v204\n"
    "v_mul_f32 v83, v205, v205\n"
    "v_mfma_f32_16x16x32_bf16 v[208:211], v[180:183], v[160:163], v[208:211]\n"
    "v_fma_f32 v76, v76, s64, v84\n"
    "v_fma_f32 v77, v77, s64, v84\n"
    "v_mfma_f32_16x16x32_bf16 v[208:211], v[148:151], v[128:131], v[208:211]\n"
    "v_fma_f32 v78, v78, s64, v84\n"
    "v_fma_f32 v79, v79, s64, v84\n"
    "v_mfma_f32_16x16x32_bf16 v[224:227], v[152:155], v[128:131], v[224:227]\n"
    "v_fma_f32 v80, v80, s64, v84\n"
    "v_fma_f32 v81, v81, s64, v84\n"
    "v_mfma_f32_16x16x32_bf16 v[224:227], v[184:187], v[160:163], v[224:227]\n"
    "v_fma_f32 v82, v82, s64, v84\n"
    "v_fma_f32 v83, v83, s64, v84\n"
    "v_mfma_f32_16x16x32_bf16 v[228:231], v[184:187], v[164:167], v[228:231]\n"
    "v_mul_f32 v76, v76, v192\n"
    "v_mul_f32 v77, v77, v193\n"
    "v_mfma_f32_16x16x32_bf16 v[228:231], v[152:155], v[132:135], v[228:231]\n"
    "v_mul_f32 v78, v78, v196\n"
    "v_mul_f32 v79, v79, v197\n"
    "v_mfma_f32_16x16x32_bf16 v[232:235], v[152:155], v[136:139], v[232:235]\n"
    "v_mul_f32 v80, v80, v200\n"
    "v_mul_f32 v81, v81, v201\n"
    "v_mfma_f32_16x16x32_bf16 v[232:235], v[184:187], v[168:171], v[232:235]\n"
    "v_mul_f32 v82, v82, v204\n"
    "v_mul_f32 v83, v83, v205\n"
    "v_mfma_f32_16x16x32_bf16 v[236:239], v[184:187], v[172:175], v[236:239]\n"
    "v_exp_f32 v76, v76\n"
    "v_mfma_f32_16x16x32_bf16 v[236:239], v[152:155], v[140:143], v[236:239]\n"
    "v_exp_f32 v77, v77\n"
    "v_mfma_f32_16x16x32_bf16 v[252:255], v[156:159], v[140:143], v[252:255]\n"
    "v_exp_f32 v78, v78\n"
    "v_mfma_f32_16x16x32_bf16 v[252:255], v[188:191], v[172:175], v[252:255]\n"
    "v_exp_f32 v79, v79\n"
    "v_mfma_f32_16x16x32_bf16 v[248:251], v[188:191], v[168:171], v[248:251]\n"
    "v_exp_f32 v80, v80\n"
    "v_mfma_f32_16x16x32_bf16 v[248:251], v[156:159], v[136:139], v[248:251]\n"
    "v_exp_f32 v81, v81\n"
    "v_mfma_f32_16x16x32_bf16 v[244:247], v[156:159], v[132:135], v[244:247]\n"
    "v_exp_f32 v82, v82\n"
    "v_mfma_f32_16x16x32_bf16 v[244:247], v[188:191], v[164:167], v[244:247]\n"
    "v_exp_f32 v83, v83\n"
    "v_mfma_f32_16x16x32_bf16 v[240:243], v[188:191], v[160:163], v[240:243]\n"
    "v_mfma_f32_16x16x32_bf16 v[240:243], v[156:159], v[128:131], v[240:243]\n"
    "s_cmp_lg_u32 s21, 0\n"
    "s_cbranch_scc1 .Lxd_g1\n"
    "s_barrier\n"
    ".Lxd_g1:\n"
    "s_setprio 2\n"
    "s_cmp_eq_u32 s22, 3\n"
    "s_cbranch_scc1 .Lxd_np\n"
    "ds_write_b32 v25, v24 offset:20\n"
    "ds_write_b128 v15, v[28:31] offset:0\n"
    "ds_write_b128 v15, v[32:35] offset:144\n"
    "ds_write_b128 v15, v[36:39] offset:288\n"
    "ds_write_b32 v16, v40 offset:3456\n"
    "ds_write_b32 v16, v12 offset:3744\n"
    "s_mov_b64 exec, 1\n"
    "ds_or_b32 v25, v21 offset:532\n"
    "s_mov_b64 exec, -1\n"
    ".Lxd_np:\n"
    "ds_read2_b32 v[26:27], v41 offset1:1\n"
    "ds_read_b32 v0, v41 offset:16\n"
    "ds_read_b32 v23, v41 offset:24\n"
    "s_load_dword s26, s[68:69], 0\n"
    "v_add_u32 v25, s24, v25\n"
    "ds_read_b128 v[92:95], v42 offset:0\n"
    "ds_read_b128 v[96:99], v42 offset:1024\n"
    "ds_read_b128 v[100:103], v42 offset:2048\n"
    "ds_read_b128 v[104:107], v42 offset:3072\n"
    "ds_read_b128 v[108:111], v42 offset:4096\n"
    "ds_read_b128 v[112:115], v42 offset:5120\n"
    "ds_read_b128 v[116:119], v42 offset:6144\n"
    "ds_read_b128 v[120:123], v42 offset:7168\n"
    "ds_read_b128 v[124:127], v42 offset:8192\n"
    "s_cmp_eq_u32 s22, 3\n"
    "s_cbranch_scc1 .Lxd_on6\n"
    ".Lxd_on6r:\n"
    "v_pk_fma_f32 v[76:77], v[76:77], v[60:61], v[60:61]\n"
    "v_pk_fma_f32 v[78:79], v[78:79], v[60:61], v[60:61]\n"
    "v_pk_fma_f32 v[80:81], v[80:81], v[60:61], v[60:61]\n"
    "v_pk_fma_f32 v[82:83], v[82:83], v[60:61], v[60:61]\n"
    "v_rcp_f32 v76, v76\n"
    "v_rcp_f32 v77, v77\n"
    "v_rcp_f32 v78, v78\n"
    "v_rcp_f32 v79, v79\n"
    "v_rcp_f32 v80, v80\n"
    "v_rcp_f32 v81, v81\n"
    "v_rcp_f32 v82, v82\n"
    "v_rcp_f32 v83, v83\n"
    "v_pk_mul_f32 v[84:85], v[192:193], v[76:77]\n"
    "v_pk_mul_f32 v[86:87], v[196:197], v[78:79]\n"
    "v_pk_mul_f32 v[88:89], v[200:201], v[80:81]\n"
    "v_pk_mul_f32 v[90:91], v[204:205], v[82:83]\n"
    "s_setprio 3\n"
    "v_pk_mul_f32 v[76:77], v[194:195], v[194:195]\n"
    "v_pk_mul_f32 v[78:79], v[198:199], v[198:199]\n"
    "v_pk_mul_f32 v[80:81], v[202:203], v[202:203]\n"
    "v_pk_mul_f32 v[82:83], v[206:207], v[206:207]\n"
    "v_pk_fma_f32 v[76:77], v[76:77], s[64:65], s[64:65] op_sel:[0,0,1] op_sel_hi:[1,0,1]\n"
    "v_pk_fma_f32 v[78:79], v[78:79], s[64:65], s[64:65] op_sel:[0,0,1] op_sel_hi:[1,0,1]\n"
    "v_pk_fma_f32 v[80:81], v[80:81], s[64:65], s[64:65] op_sel:[0,0,1] op_sel_hi:[1,0,1]\n"
    "v_pk_fma_f32 v[82:83], v[82:83], s[64:65], s[64:65] op_sel:[0,0,1] op_sel_hi:[1,0,1]\n"
    "v_pk_mul_f32 v[76:77], v[76:77], v[194:195]\n"
    "v_pk_mul_f32 v[78:79], v[78:79], v[198:199]\n"
    "v_pk_mul_f32 v[80:81], v[80:81], v[202:203]\n"
    "v_pk_mul_f32 v[82:83], v[82:83], v[206:207]\n"
    "v_exp_f32 v76, v76\n"
    "v_exp_f32 v77, v77\n"
    "v_exp_f32 v78, v78\n"
    "v_exp_f32 v79, v79\n"
    "v_exp_f32 v80, v80\n"
    "v_exp_f32 v81, v81\n"
    "v_exp_f32 v82, v82\n"
    "v_exp_f32 v83, v83\n"
    "v_pk_fma_f32 v[76:77], v[76:77], v[62:63], v[62:63]\n"
    "v_pk_fma_f32 v[78:79], v[78:79], v[62:63], v[62:63]\n"
    "v_pk_fma_f32 v[80:81], v[80:81], v[62:63], v[62:63]\n"
    "v_pk_fma_f32 v[82:83], v[82:83], v[62:63], v[62:63]\n"
    "v_rcp_f32 v76, v76\n"
    "v_rcp_f32 v77, v77\n"
    "v_rcp_f32 v78, v78\n"
    "v_rcp_f32 v79, v79\n"
    "v_rcp_f32 v80, v80\n"
    "v_rcp_f32 v81, v81\n"
    "v_rcp_f32 v82, v82\n"
    "v_rcp_f32 v83, v83\n"
    "v_pk_fma_f32 v[84:85], v[194:195], v[76:77], v[84:85]\n"
    "v_pk_fma_f32 v[86:87], v[198:199], v[78:79], v[86:87]\n"
    "v_pk_fma_f32 v[88:89], v[202:203], v[80:81], v[88:89]\n"
    "v_pk_fma_f32 v[90:91], v[206:207], v[82:83], v[90:91]\n"
    "ds_read_b32 v22, v13 offset:512\n"
    "v_pk_mul_f32 v[76:77], v[208:209], v[208:209]\n"
    "v_pk_mul_f32 v[78:79], v[212:213], v[212:213]\n"
    "v_pk_mul_f32 v[80:81], v[216:217], v[216:217]\n"
    "v_pk_mul_f32 v[82:83], v[220:221], v[220:221]\n"
    "v_pk_fma_f32 v[76:77], v[76:77], s[64:65], s[64:65] op_sel:[0,0,1] op_sel_hi:[1,0,1]\n"
    "v_pk_fma_f32 v[78:79], v[78:79], s[64:65], s[64:65] op_sel:[0,0,1] op_sel_hi:[1,0,1]\n"
    "v_pk_fma_f32 v[80:81], v[80:81], s[64:65], s[64:65] op_sel:[0,0,1] op_sel_hi:[1,0,1]\n"
    "v_pk_fma_f32 v[82:83], v[82:83], s[64:65], s[64:65] op_sel:[0,0,1] op_sel_hi:[1,0,1]\n"
    "v_pk_mul_f32 v[76:77], v[76:77], v[208:209]\n"
    "v_pk_mul_f32 v[78:79], v[78:79], v[212:213]\n"
    "v_pk_mul_f32 v[80:81], v[80:81], v[216:217]\n"
    "v_pk_mul_f32 v[82:83], v[82:83], v[220:221]\n"
    "v_exp_f32 v76, v76\n"
    "v_exp_f32 v77, v77\n"
    "ds_read_b128 v[128:131], v42 offset:9216\n"
    "v_exp_f32 v78, v78\n"
    "v_exp_f32 v79, v79\n"
    "v_exp_f32 v80, v80\n"
    "ds_read_b128 v[132:135], v42 offset:10240\n"
    "v_exp_f32 v81, v81\n"
    "v_exp_f32 v82, v82\n"
    "v_exp_f32 v83, v83\n"
    "ds_read_b128 v[136:139], v42 offset:11264\n"
    "v_pk_fma_f32 v[76:77], v[76:77], v[64:65], v[64:65]\n"
    "v_pk_fma_f32 v[78:79], v[78:79], v[64:65], v[64:65]\n"
    "v_pk_fma_f32 v[80:81], v[80:81], v[64:65], v[64:65]\n"
    "v_pk_fma_f32 v[82:83], v[82:83], v[64:65], v[64:65]\n"
    "v_rcp_f32 v76, v76\n"
    "v_rcp_f32 v77, v77\n"
    "ds_read_b128 v[140:143], v42 offset:12288\n"
    "v_rcp_f32 v78, v78\n"
    "v_rcp_f32 v79, v79\n"
    "v_rcp_f32 v80, v80\n"
    "ds_read_b128 v[144:147], v42 offset:13312\n"
    "v_rcp_f32 v81, v81\n"
    "v_rcp_f32 v82, v82\n"
    "v_rcp_f32 v83, v83\n"
    "ds_read_b128 v[148:151], v42 offset:14336\n"
    "v_pk_fma_f32 v[84:85], v[208:209], v[76:77], v[84:85]\n"
    "v_pk_fma_f32 v[86:87], v[212:213], v[78:79], v[86:87]\n"
    "v_pk_fma_f32 v[88:89], v[216:217], v[80:81], v[88:89]\n"
    "v_pk_fma_f32 v[90:91], v[220:221], v[82:83], v[90:91]\n"
    "s_waitcnt lgkmcnt(6)\n"
    "v_readfirstlane_b32 s45, v22\n"
    "s_cmp_lt_i32 s45, 16\n"
    "s_cbranch_scc1 .Lxd_ck7\n"
    ".Lxd_ck7r:\n"
    "ds_read_b64_tr_b16 v[44:45], v9 offset:0\n"
    "ds_read_b64_tr_b16 v[46:47], v10 offset:0\n"
    "ds_read_b64_tr_b16 v[48:49], v7 offset:0\n"
    "ds_read_b64_tr_b16 v[50:51], v8 offset:576\n"
    "ds_read_b64_tr_b16 v[52:53], v8 offset:1152\n"
    "ds_read_b64_tr_b16 v[54:55], v8 offset:1728\n"
    "ds_read_b64_tr_b16 v[56:57], v8 offset:2304\n"
    "ds_read_b64_tr_b16 v[58:59], v8 offset:2880\n"
    "v_readfirstlane_b32 s28, v26\n"
    "v_readfirstlane_b32 s29, v27\n"
    "s_cmp_eq_u32 s22, 3\n"
    "s_cbranch_scc1 .Lxd_on9\n"
    ".Lxd_on9r:\n"
    "s_cmp_eq_u32 s22, 0\n"
    "s_cbranch_scc1 .Lxd_on10\n"
    ".Lxd_on10r:\n"
    "s_setprio 2\n"
    "v_pk_mul_f32 v[76:77], v[210:211], v[210:211]\n"
    "v_pk_mul_f32 v[78:79], v[214:215], v[214:215]\n"
    "v_pk_mul_f32 v[80:81], v[218:219], v[218:219]\n"
    "v_pk_mul_f32 v[82:83], v[222:223], v[222:223]\n"
    "v_pk_fma_f32 v[76:77], v[76:77], s[64:65], s[64:65] op_sel:[0,0,1] op_sel_hi:[1,0,1]\n"
    "v_pk_fma_f32 v[78:79], v[78:79], s[64:65], s[64:65] op_sel:[0,0,1] op_sel_hi:[1,0,1]\n"
    "v_pk_fma_f32 v[80:81], v[80:81], s[64:65], s[64:65] op_sel:[0,0,1] op_sel_hi:[1,0,1]\n"
    "v_pk_fma_f32 v[82:83], v[82:83], s[64:65], s[64:65] op_sel:[0,0,1] op_sel_hi:[1,0,1]\n"
    "v_pk_mul_f32 v[76:77], v[76:77], v[210:211]\n"
    "v_pk_mul_f32 v[78:79], v[78:79], v[214:215]\n"
    "v_pk_mul_f32 v[80:81], v[80:81], v[218:219]\n"
    "v_pk_mul_f32 v[82:83], v[82:83], v[222:223]\n"
    "v_exp_f32 v76, v76\n"
    "v_exp_f32 v77, v77\n"
    "ds_read_b128 v[152:155], v42 offset:15360\n"
    "v_exp_f32 v78, v78\n"
    "v_exp_f32 v79, v79\n"
    "v_exp_f32 v80, v80\n"
    "ds_read_b128 v[156:159], v42 offset:16384\n"
    "v_exp_f32 v81, v81\n"
    "v_exp_f32 v82, v82\n"
    "v_exp_f32 v83, v83\n"
    "ds_read_b128 v[160:163], v42 offset:17408\n"
    "v_pk_fma_f32 v[76:77], v[76:77], v[66:67], v[66:67]\n"
    "v_pk_fma_f32 v[78:79], v[78:79], v[66:67], v[66:67]\n"
    "v_pk_fma_f32 v[80:81], v[80:81], v[66:67], v[66:67]\n"
    "v_pk_fma_f32 v[82:83], v[82:83], v[66:67], v[66:67]\n"
    "v_rcp_f32 v76, v76\n"
    "v_rcp_f32 v77, v77\n"
    "ds_read_b128 v[164:167], v42 offset:18432\n"
    "v_rcp_f32 v78, v78\n"
    "v_rcp_f32 v79, v79\n"
    "v_rcp_f32 v80, v80\n"
    "ds_read_b128 v[168:171], v42 offset:19456\n"
    "v_rcp_f32 v81, v81\n"
    "v_rcp_f32 v82, v82\n"
    "v_rcp_f32 v83, v83\n"
    "ds_read_b128 v[172:175], v42 offset:20480\n"
    "v_pk_fma_f32 v[84:85], v[210:211], v[76:77], v[84:85]\n"
    "v_pk_fma_f32 v[86:87], v[214:215], v[78:79], v[86:87]\n"
    "v_pk_fma_f32 v[88:89], v[218:219], v[80:81], v[88:89]\n"
    "v_pk_fma_f32 v[90:91], v[222:223], v[82:83], v[90:91]\n"
    "v_pk_mul_f32 v[76:77], v[224:225], v[224:225]\n"
    "v_pk_mul_f32 v[78:79], v[228:229], v[228:229]\n"
    "v_pk_mul_f32 v[80:81], v[232:233], v[232:233]\n"
    "v_pk_mul_f32 v[82:83], v[236:237], v[236:237]\n"
    "v_pk_fma_f32 v[76:77], v[76:77], s[64:65], s[64:65] op_sel:[0,0,1] op_sel_hi:[1,0,1]\n"
    "v_pk_fma_f32 v[78:79], v[78:79], s[64:65], s[64:65] op_sel:[0,0,1] op_sel_hi:[1,0,1]\n"
    "v_pk_fma_f32 v[80:81], v[80:81], s[64:65], s[64:65] op_sel:[0,0,1] op_sel_hi:[1,0,1]\n"
    "v_pk_fma_f32 v[82:83], v[82:83], s[64:65], s[64:65] op_sel:[0,0,1] op_sel_hi:[1,0,1]\n"
    "v_pk_mul_f32 v[76:77], v[76:77], v[224:225]\n"
    "v_pk_mul_f32 v[78:79], v[78:79], v[228:229]\n"
    "v_pk_mul_f32 v[80:81], v[80:81], v[232:233]\n"
    "v_pk_mul_f32 v[82:83], v[82:83], v[236:237]\n"
    "v_exp_f32 v76, v76\n"
    "v_exp_f32 v77, v77\n"
    "ds_read_b128 v[176:179], v42 offset:21504\n"
    "v_exp_f32 v78, v78\n"
    "v_exp_f32 v79, v79\n"
    "v_exp_f32 v80, v80\n"
    "ds_read_b128 v[180:183], v42 offset:22528\n"
    "v_exp_f32 v81, v81\n"
    "v_exp_f32 v82, v82\n"
    "v_exp_f32 v83, v83\n"
    "ds_read_b128 v[184:187], v42 offset:23552\n"
    "v_pk_fma_f32 v[76:77], v[76:77], v[68:69], v[68:69]\n"
    "v_pk_fma_f32 v[78:79], v[78:79], v[68:69], v[68:69]\n"
    "v_pk_fma_f32 v[80:81], v[80:81], v[68:69], v[68:69]\n"
    "v_pk_fma_f32 v[82:83], v[82:83], v[68:69], v[68:69]\n"
    "v_rcp_f32 v76, v76\n"
    "v_rcp_f32 v77, v77\n"
    "ds_read_b128 v[188:191], v42 offset:24576\n"
    "v_rcp_f32 v78, v78\n"
    "v_rcp_f32 v79, v79\n"
    "v_rcp_f32 v80, v80\n"
    "ds_read_b128 v[192:195], v42 offset:25600\n"
    "v_rcp_f32 v81, v81\n"
    "v_rcp_f32 v82, v82\n"
    "v_rcp_f32 v83, v83\n"
    "ds_read_b128 v[196:199], v42 offset:26624\n"
    "v_pk_fma_f32 v[84:85], v[224:225], v[76:77], v[84:85]\n"
    "v_pk_fma_f32 v[86:87], v[228:229], v[78:79], v[86:87]\n"
    "v_pk_fma_f32 v[88:89], v[232:233], v[80:81], v[88:89]\n"
    "v_pk_fma_f32 v[90:91], v[236:237], v[82:83], v[90:91]\n"
    "s_setprio 1\n"
    "v_pk_mul_f32 v[76:77], v[226:227], v[226:227]\n"
    "v_pk_mul_f32 v[78:79], v[230:231], v[230:231]\n"
    "v_pk_mul_f32 v[80:81], v[234:235], v[234:235]\n"
    "v_pk_mul_f32 v[82:83], v[238:239], v[238:239]\n"
    "v_pk_fma_f32 v[76:77], v[76:77], s[64:65], s[64:65] op_sel:[0,0,1] op_sel_hi:[1,0,1]\n"
    "v_pk_fma_f32 v[78:79], v[78:79], s[64:65], s[64:65] op_sel:[0,0,1] op_sel_hi:[1,0,1]\n"
    "v_pk_fma_f32 v[80:81], v[80:81], s[64:65], s[64:65] op_sel:[0,0,1] op_sel_hi:[1,0,1]\n"
    "v_pk_fma_f32 v[82:83], v[82:83], s[64:65], s[64:65] op_sel:[0,0,1] op_sel_hi:[1,0,1]\n"
    "v_pk_mul_f32 v[76:77], v[76:77], v[226:227]\n"
    "v_pk_mul_f32 v[78:79], v[78:79], v[230:231]\n"
    "v_pk_mul_f32 v[80:81], v[80:81], v[234:235]\n"
    "v_pk_mul_f32 v[82:83], v[82:83], v[238:239]\n"
    "v_exp_f32 v76, v76\n"
    "v_exp_f32 v77, v77\n"
    "ds_read_b128 v[200:203], v42 offset:27648\n"
    "v_exp_f32 v78, v78\n"
    "v_exp_f32 v79, v79\n"
    "v_exp_f32 v80, v80\n"
    "ds_read_b128 v[204:207], v42 offset:28672\n"
    "v_exp_f32 v81, v81\n"
    "v_exp_f32 v82, v82\n"
    "v_exp_f32 v83, v83\n"
    "ds_read_b128 v[208:211], v42 offset:29696\n"
    "v_pk_fma_f32 v[76:77], v[76:77], v[70:71], v[70:71]\n"
    "v_pk_fma_f32 v[78:79], v[78:79], v[70:71], v[70:71]\n"
    "v_pk_fma_f32 v[80:81], v[80:81], v[70:71], v[70:71]\n"
    "v_pk_fma_f32 v[82:83], v[82:83], v[70:71], v[70:71]\n"
    "v_rcp_f32 v76, v76\n"
    "v_rcp_f32 v77, v77\n"
    "ds_read_b128 v[212:215], v42 offset:30720\n"
    "v_rcp_f32 v78, v78\n"
    "v_rcp_f32 v79, v79\n"
    "v_rcp_f32 v80, v80\n"
    "ds_read_b128 v[216:219], v42 offset:31744\n"
    "v_rcp_f32 v81, v81\n"
    "v_rcp_f32 v82, v82\n"
    "v_rcp_f32 v83, v83\n"
    "ds_read_b128 v[220:223], v42 offset:32768\n"
    "v_pk_fma_f32 v[84:85], v[226:227], v[76:77], v[84:85]\n"
    "v_pk_fma_f32 v[86:87], v[230:231], v[78:79], v[86:87]\n"
    "v_pk_fma_f32 v[88:89], v[234:235], v[80:81], v[88:89]\n"
    "v_pk_fma_f32 v[90:91], v[238:239], v[82:83], v[90:91]\n"
    "s_cmp_eq_u32 s22, 3\n"
    "s_cbranch_scc1 .Lxd_on11\n"
    ".Lxd_on11r:\n"
    "s_cmp_eq_u32 s22, 0\n"
    "s_cbranch_scc1 .Lxd_on13\n"
    ".Lxd_on13r:\n"
    "s_cmp_eq_u32 s22, 1\n"
    "s_cbranch_scc1 .Lxd_on14\n"
    ".Lxd_on14r:\n"
    "v_pk_mul_f32 v[76:77], v[240:241], v[240:241]\n"
    "v_pk_mul_f32 v[78:79], v[244:245], v[244:245]\n"
    "v_pk_mul_f32 v[80:81], v[248:249], v[248:249]\n"
    "v_pk_mul_f32 v[82:83], v[252:253], v[252:253]\n"
    "v_pk_fma_f32 v[76:77], v[76:77], s[64:65], s[64:65] op_sel:[0,0,1] op_sel_hi:[1,0,1]\n"
    "v_pk_fma_f32 v[78:79], v[78:79], s[64:65], s[64:65] op_sel:[0,0,1] op_sel_hi:[1,0,1]\n"
    "v_pk_fma_f32 v[80:81], v[80:81], s[64:65], s[64:65] op_sel:[0,0,1] op_sel_hi:[1,0,1]\n"
    "v_pk_fma_f32 v[82:83], v[82:83], s[64:65], s[64:65] op_sel:[0,0,1] op_sel_hi:[1,0,1]\n"
    "v_pk_mul_f32 v[76:77], v[76:77], v[240:241]\n"
    "v_pk_mul_f32 v[78:79], v[78:79], v[244:245]\n"
    "v_pk_mul_f32 v[80:81], v[80:81], v[248:249]\n"
    "v_pk_mul_f32 v[82:83], v[82:83], v[252:253]\n"
    "v_exp_f32 v76, v76\n"
    "v_exp_f32 v77, v77\n"
    "v_exp_f32 v78, v78\n"
    "v_exp_f32 v79, v79\n"
    "v_exp_f32 v80, v80\n"
    "v_exp_f32 v81, v81\n"
    "v_exp_f32 v82, v82\n"
    "v_exp_f32 v83, v83\n"
    "v_pk_fma_f32 v[76:77], v[76:77], v[72:73], v[72:73]\n"
    "v_pk_fma_f32 v[78:79], v[78:79], v[72:73], v[72:73]\n"
    "v_pk_fma_f32 v[80:81], v[80:81], v[72:73], v[72:73]\n"
    "v_pk_fma_f32 v[82:83], v[82:83], v[72:73], v[72:73]\n"
    "v_rcp_f32 v76, v76\n"
    "v_rcp_f32 v77, v77\n"
    "v_rcp_f32 v78, v78\n"
    "v_rcp_f32 v79, v79\n"
    "v_rcp_f32 v80, v80\n"
    "v_rcp_f32 v81, v81\n"
    "v_rcp_f32 v82, v82\n"
    "v_rcp_f32 v83, v83\n"
    "v_pk_fma_f32 v[84:85], v[240:241], v[76:77], v[84:85]\n"
    "v_pk_fma_f32 v[86:87], v[244:245], v[78:79], v[86:87]\n"
    "v_pk_fma_f32 v[88:89], v[248:249], v[80:81], v[88:89]\n"
    "v_pk_fma_f32 v[90:91], v[252:253], v[82:83], v[90:91]\n"
    "s_setprio 0\n"
    "v_pk_mul_f32 v[76:77], v[242:243], v[242:243]\n"
    "v_pk_mul_f32 v[78:79], v[246:247], v[246:247]\n"
    "v_pk_mul_f32 v[80:81], v[250:251], v[250:251]\n"
    "v_pk_mul_f32 v[82:83], v[254:255], v[254:255]\n"
    "v_pk_fma_f32 v[76:77], v[76:77], s[64:65], s[64:65] op_sel:[0,0,1] op_sel_hi:[1,0,1]\n"
    "v_pk_fma_f32 v[78:79], v[78:79], s[64:65], s[64:65] op_sel:[0,0,1] op_sel_hi:[1,0,1]\n"
    "v_pk_fma_f32 v[80:81], v[80:81], s[64:65], s[64:65] op_sel:[0,0,1] op_sel_hi:[1,0,1]\n"
    "v_pk_fma_f32 v[82:83], v[82:83], s[64:65], s[64:65] op_sel:[0,0,1] op_sel_hi:[1,0,1]\n"
    "v_pk_mul_f32 v[76:77], v[76:77], v[242:243]\n"
    "v_pk_mul_f32 v[78:79], v[78:79], v[246:247]\n"
    "v_pk_mul_f32 v[80:81], v[80:81], v[250:251]\n"
    "v_pk_mul_f32 v[82:83], v[82:83], v[254:255]\n"
    "v_exp_f32 v76, v76\n"
    "v_exp_f32 v77, v77\n"
    "v_exp_f32 v78, v78\n"
    "v_exp_f32 v79, v79\n"
    "v_exp_f32 v80, v80\n"
    "v_exp_f32 v81, v81\n"
    "v_exp_f32 v82, v82\n"
    "v_exp_f32 v83, v83\n"
    "v_pk_fma_f32 v[76:77], v[76:77], v[74:75], v[74:75]\n"
    "v_pk_fma_f32 v[78:79], v[78:79], v[74:75], v[74:75]\n"
    "v_pk_fma_f32 v[80:81], v[80:81], v[74:75], v[74:75]\n"
    "v_pk_fma_f32 v[82:83], v[82:83], v[74:75], v[74:75]\n"
    "v_rcp_f32 v76, v76\n"
    "v_rcp_f32 v77, v77\n"
    "v_rcp_f32 v78, v78\n"
    "v_rcp_f32 v79, v79\n"
    "v_rcp_f32 v80, v80\n"
    "v_rcp_f32 v81, v81\n"
    "v_rcp_f32 v82, v82\n"
    "v_rcp_f32 v83, v83\n"
    "v_pk_fma_f32 v[84:85], v[242:243], v[76:77], v[84:85]\n"
    "v_pk_fma_f32 v[86:87], v[246:247], v[78:79], v[86:87]\n"
    "v_pk_fma_f32 v[88:89], v[250:251], v[80:81], v[88:89]\n"
    "v_pk_fma_f32 v[90:91], v[254:255], v[82:83], v[90:91]\n"
    "s_cmp_eq_u32 s21, 0\n"
    "s_cbranch_scc1 .Lxd_gb0\n"
    "s_barrier\n"
    "s_setprio 3\n"
    ".Lxd_gb0:\n"
    "v_add_f32 v84, v84, v85\n"
    "v_add_f32 v86, v86, v87\n"
    "v_add_f32 v88, v88, v89\n"
    "v_add_f32 v90, v90, v91\n"
    "s_waitcnt lgkmcnt(15)\n"
    "v_pk_mul_f16 v60, v44, s45 op_sel:[0,1]\n"
    "v_pk_mul_f16 v61, v45, s45 op_sel:[0,1]\n"
    "v_permlane32_swap_b32 v84, v88\n"
    "v_permlane32_swap_b32 v86, v90\n"
    "v_add_f32 v84, v84, v88\n"
    "v_add_f32 v86, v86, v90\n"
    "v_pk_mul_f16 v62, v46, s45 op_sel:[0,1]\n"
    "v_pk_mul_f16 v63, v47, s45 op_sel:[0,1]\n"
    "v_permlane16_swap_b32 v84, v86\n"
    "v_add_f32 v84, v84, v86\n"
    "v_and_b32 v84, -2, v84\n"
    "ds_wrxchg_rtn_b32 v85, v11, v84\n"
    "ds_read_b128 v[224:227], v42 offset:33792\n"
    "ds_read_b128 v[228:231], v42 offset:34816\n"
    "ds_read_b128 v[232:235], v42 offset:35840\n"
    "ds_read_b128 v[236:239], v42 offset:36864\n"
    "ds_read_b128 v[240:243], v42 offset:37888\n"
    "ds_read_b128 v[244:247], v42 offset:38912\n"
    "ds_read_b128 v[248:251], v42 offset:39936\n"
    "ds_read_b128 v[252:255], v42 offset:40960\n"
    "v_pk_mul_f16 v64, v44, v46\n"
    "v_pk_mul_f16 v65, v45, v47\n"
    "v_pk_mul_f16 v66, v48, v48 op_sel_hi:[0,1]\n"
    "v_pk_mul_f16 v67, v48, v49 op_sel_hi:[0,1]\n"
    "v_lshlrev_b32 v86, 3, v1\n"
    "v_mov_b64 v[88:89], 0\n"
    "v_mov_b64 v[90:91], 0\n"
    "s_waitcnt lgkmcnt(8)\n"
    "v_cmp_ne_u32 vcc, -1, v85\n"
    "v_add_f32 v84, v84, v85\n"
    "v_add_u32 v84, 0x80, v84\n"
    "v_and_b32 v84, 0xffffff00, v84\n"
    "v_or_b32 v26, s28, v84\n"
    "v_mov_b32 v27, s29\n"
    "s_cbranch_vccz .Lxd_nost\n"
    "s_mov_b64 exec, vcc\n"
    "global_store_dwordx2 v86, v[26:27], s[48:49] sc1\n"
    "s_mov_b64 exec, -1\n"
    ".Lxd_nost:\n"
    "s_cmp_lg_u32 s21, 0\n"
    "s_cbranch_scc1 .Lxd_gb1\n"
    "s_barrier\n"
    ".Lxd_gb1:\n"
    ".Lxd_enter:\n"
    "s_cmp_eq_u32 s21, 0\n"
    "s_cselect_b64 vcc, 0, -1\n"
    "s_cbranch_scc1 .Lxd_pr\n"
    "s_setprio 1\n"
    ".Lxd_pr:\n"
    "s_cmp_lg_u32 s36, 0\n"
    "s_cbranch_scc1 .Lxd_el\n"
    "s_setpc_b64 s[10:11]\n"
    ".Lxd_el:\n"
    "s_cmp_eq_u32 s22, 0\n"
    "s_cbranch_scc1 .Lxd_pl1\n"
    "s_branch .Lxd_pl2\n"
    ".Lxd_ph0:\n"
    "s_cbranch_vccnz .Lxd_pr16\n"
    "s_setprio 2\n"
    ".Lxd_pr16:\n"
    "v_smfmac_f32_16x16x64_f16 v[88:91], v[112:115], v[60:67], v2\n"
    "v_fmac_f32 v11, v76, v80\n"
    "v_fmac_f32 v11, v77, v81\n"
    "v_fmac_f32 v11, v78, v82\n"
    "v_fmac_f32 v11, v79, v83\n"
    "v_mfma_f32_16x16x32_f16 v[80:83], v[92:95], v[60:63], 0\n"
    "v_pk_mul_f16 v68, v58, v58 op_sel_hi:[0,1]\n"
    "v_pk_mul_f16 v69, v58, v49 op_sel_hi:[0,1]\n"
    "v_mfma_f32_16x16x32_f16 v[80:83], v[104:107], v[64:67], v[80:83]\n"
    "v_pk_mul_f16 v70, v48, v58 op_sel_hi:[0,1]\n"
    "v_pk_mul_f16 v71, v48, v59 op_sel_hi:[0,1]\n"
    "v_mfma_f32_16x16x32_f16 v[76:79], v[100:103], v[64:67], 0\n"
    "v_fmac_f32 v11, v84, v85\n"
    "v_fma_f32 v10, v86, v87, v11\n"
    "v_mfma_f32_16x16x32_f16 v[84:87], v[108:111], v[64:67], 0\n"
    "v_pk_mul_f16 v72, v50, v58 op_sel_hi:[0,1]\n"
    "v_pk_mul_f16 v73, v50, v59 op_sel_hi:[0,1]\n"
    "v_mfma_f32_16x16x32_f16 v[84:87], v[96:99], v[60:63], v[84:87]\n"
    "v_pk_mul_f16 v74, v52, v58 op_sel_hi:[0,1]\n"
    "v_pk_mul_f16 v75, v52, v59 op_sel_hi:[0,1]\n"
    "s_waitcnt vmcnt(0)\n"
    "v_mfma_f32_16x16x32_f16 v[76:79], v[116:119], v[68:71], v[76:79]\n"
    "v_pk_mul_f16 v60, v54, v58 op_sel_hi:[0,1]\n"
    "v_pk_mul_f16 v61, v54, v59 op_sel_hi:[0,1]\n"
    "v_mfma_f32_16x16x32_f16 v[76:79], v[128:131], v[72:75], v[76:79]\n"
    "v_pk_mul_f16 v62, v56, v58 op_sel_hi:[0,1]\n"
    "v_readfirstlane_b32 s40, v24\n"
    "s_or_b32 s40, s40, s44\n"
    "s_lshl1_add_u32 s41, s40, s54\n"
    "s_cmpk_lt_u32 s40, 256\n"
    "s_cselect_b32 s41, s41, 2\n"
    "v_mov_b32 v12, s41\n"
    "s_cbranch_scc0 .Lxd_tf15\n"
    ".Lxd_tf15r:\n"
    "s_mul_i32 s41, s41, 52\n"
    "s_add_u32 s38, s4, s41\n"
    "s_addc_u32 s39, s5, 0\n"
    "v_mfma_f32_16x16x32_f16 v[80:83], v[132:135], v[72:75], v[80:83]\n"
    "v_pk_mul_f16 v63, v56, v59 op_sel_hi:[0,1]\n"
    "ds_write_b32 v25, v12 offset:20\n"
    "global_load_dwordx3 v[36:38], v17, s[38:39] offset:-104\n"
    "global_load_dword v41, v18, s[38:39] offset:-8\n"
    "v_mfma_f32_16x16x32_f16 v[80:83], v[120:123], v[68:71], v[80:83]\n"
    "v_pk_mul_f16 v64, v56, v56 op_sel_hi:[0,1]\n"
    "v_xor_b32 v15, 0x4000, v15\n"
    "v_mfma_f32_16x16x32_f16 v[84:87], v[124:127], v[68:71], v[84:87]\n"
    "v_pk_mul_f16 v65, v56, v49 op_sel_hi:[0,1]\n"
    "v_xor_b32 v16, 0x4000, v16\n"
    "v_mfma_f32_16x16x32_f16 v[84:87], v[136:139], v[72:75], v[84:87]\n"
    "v_pk_mul_f16 v66, v48, v56 op_sel_hi:[0,1]\n"
    "v_permlane32_swap_b32 v7, v8\n"
    "v_permlane32_swap_b32 v9, v10\n"
    "v_smfmac_f32_16x16x64_f16 v[88:91], v[140:143], v[68:75], v2\n"
    "v_pk_mul_f16 v67, v48, v57 op_sel_hi:[0,1]\n"
    "v_mul_f32 v7, v0, v7\n"
    "v_mul_f32 v9, v0, v9\n"
    "v_mfma_f32_16x16x32_f16 v[76:79], v[144:147], v[60:63], v[76:79]\n"
    "v_pk_mul_f16 v68, v50, v56 op_sel_hi:[0,1]\n"
    "v_pk_mul_f16 v69, v50, v57 op_sel_hi:[0,1]\n"
    "v_mfma_f32_16x16x32_f16 v[76:79], v[156:159], v[64:67], v[76:79]\n"
    "v_pk_mul_f16 v70, v52, v56 op_sel_hi:[0,1]\n"
    "v_fmac_f32 v7, v23, v8\n"
    "v_fmac_f32 v9, v23, v10\n"
    "v_mfma_f32_16x16x32_f16 v[80:83], v[160:163], v[64:67], v[80:83]\n"
    "v_pk_mul_f16 v71, v52, v57 op_sel_hi:[0,1]\n"
    "v_permlane16_swap_b32 v7, v9\n"
    "v_mfma_f32_16x16x32_f16 v[80:83], v[148:151], v[60:63], v[80:83]\n"
    "v_pk_mul_f16 v72, v54, v56 op_sel_hi:[0,1]\n"
    "ds_read_b32 v22, v25 offset:516\n"
    "v_mfma_f32_16x16x32_f16 v[84:87], v[152:155], v[60:63], v[84:87]\n"
    "v_pk_mul_f16 v73, v54, v57 op_sel_hi:[0,1]\n"
    "v_add_f32 v7, v7, v9\n"
    "v_mfma_f32_16x16x32_f16 v[84:87], v[164:167], v[64:67], v[84:87]\n"
    "v_pk_mul_f16 v74, v54, v54 op_sel_hi:[0,1]\n"
    "v_add_f32_dpp v7, v7, v7 quad_perm:[1,0,3,2] row_mask:0xf bank_mask:0xf\n"
    "v_smfmac_f32_16x16x64_f16 v[88:91], v[168:171], v[60:67], v2\n"
    "v_pk_mul_f16 v75, v54, v49 op_sel_hi:[0,1]\n"
    "v_add_f32_dpp v7, v7, v7 quad_perm:[2,3,0,1] row_mask:0xf bank_mask:0xf\n"
    "v_mfma_f32_16x16x32_f16 v[76:79], v[172:175], v[68:71], v[76:79]\n"
    "v_pk_mul_f16 v60, v48, v54 op_sel_hi:[0,1]\n"
    "v_pk_mul_f16 v61, v48, v55 op_sel_hi:[0,1]\n"
    "v_mfma_f32_16x16x32_f16 v[76:79], v[184:187], v[72:75], v[76:79]\n"
    "v_pk_mul_f16 v62, v50, v54 op_sel_hi:[0,1]\n"
    "v_add_f32_dpp v7, v7, v7 row_half_mirror row_mask:0xf bank_mask:0xf\n"
    "v_mfma_f32_16x16x32_f16 v[80:83], v[188:191], v[72:75], v[80:83]\n"
    "v_pk_mul_f16 v63, v50, v55 op_sel_hi:[0,1]\n"
    "s_waitcnt lgkmcnt(0)\n"
    "v_readfirstlane_b32 s45, v22\n"
    "s_cmp_lt_i32 s45, 16\n"
    "s_cbranch_scc1 .Lxd_ck18\n"
    ".Lxd_ck18r:\n"
    "v_mfma_f32_16x16x32_f16 v[80:83], v[176:179], v[68:71], v[80:83]\n"
    "v_pk_mul_f16 v64, v52, v54 op_sel_hi:[0,1]\n"
    "ds_read_b64_tr_b16 v[44:45], v5 offset:4096\n"
    "v_mfma_f32_16x16x32_f16 v[84:87], v[180:183], v[68:71], v[84:87]\n"
    "v_pk_mul_f16 v65, v52, v55 op_sel_hi:[0,1]\n"
    "ds_read_b64_tr_b16 v[46:47], v6 offset:4096\n"
    "v_mfma_f32_16x16x32_f16 v[84:87], v[192:195], v[72:75], v[84:87]\n"
    "v_pk_mul_f16 v66, v52, v52 op_sel_hi:[0,1]\n"
    "ds_read_b64_tr_b16 v[58:59], v4 offset:6976\n"
    "s_cbranch_vccnz .Lxd_pr17\n"
    "s_setprio 0\n"
    ".Lxd_pr17:\n"
    "v_smfmac_f32_16x16x64_f16 v[88:91], v[196:199], v[68:75], v2\n"
    "v_pk_mul_f16 v67, v52, v49 op_sel_hi:[0,1]\n"
    "ds_read_b64_tr_b16 v[56:57], v4 offset:6400\n"
    "v_mfma_f32_16x16x32_f16 v[76:79], v[200:203], v[60:63], v[76:79]\n"
    "v_pk_mul_f16 v68, v48, v52 op_sel_hi:[0,1]\n"
    "v_pk_mul_f16 v69, v48, v53 op_sel_hi:[0,1]\n"
    "v_mfma_f32_16x16x32_f16 v[76:79], v[212:215], v[64:67], v[76:79]\n"
    "v_pk_mul_f16 v70, v50, v52 op_sel_hi:[0,1]\n"
    "ds_read_b64_tr_b16 v[54:55], v4 offset:5824\n"
    "v_mfma_f32_16x16x32_f16 v[80:83], v[216:219], v[64:67], v[80:83]\n"
    "v_pk_mul_f16 v71, v50, v53 op_sel_hi:[0,1]\n"
    "v_add_f32_dpp v7, v7, v7 row_mirror row_mask:0xf bank_mask:0xf\n"
    "v_mfma_f32_16x16x32_f16 v[80:83], v[204:207], v[60:63], v[80:83]\n"
    "v_pk_mul_f16 v72, v50, v50 op_sel_hi:[0,1]\n"
    "ds_write_b32 v14, v7\n"
    "v_add_u32 v14, 64, v14\n"
    "v_mfma_f32_16x16x32_f16 v[84:87], v[208:211], v[60:63], v[84:87]\n"
    "v_pk_mul_f16 v73, v50, v49 op_sel_hi:[0,1]\n"
    "v_pk_mul_f16 v74, v48, v50 op_sel_hi:[0,1]\n"
    "v_mfma_f32_16x16x32_f16 v[84:87], v[220:223], v[64:67], v[84:87]\n"
    "v_pk_mul_f16 v75, v48, v51 op_sel_hi:[0,1]\n"
    "v_smfmac_f32_16x16x64_f16 v[88:91], v[224:227], v[60:67], v2\n"
    "ds_read_b64_tr_b16 v[48:49], v3 offset:4096\n"
    "ds_read_b64_tr_b16 v[50:51], v4 offset:4672\n"
    "ds_read_b64_tr_b16 v[52:53], v4 offset:5248\n"
    "v_smfmac_f32_16x16x64_f16 v[88:91], v[252:255], v[68:75], v2\n"
    "s_waitcnt lgkmcnt(7)\n"
    "v_pk_mul_f16 v60, v44, s45 op_sel:[0,1]\n"
    "v_pk_mul_f16 v61, v45, s45 op_sel:[0,1]\n"
    "v_mfma_f32_16x16x32_f16 v[76:79], v[228:231], v[68:71], v[76:79]\n"
    "v_pk_mul_f16 v62, v46, s45 op_sel:[0,1]\n"
    "v_pk_mul_f16 v63, v47, s45 op_sel:[0,1]\n"
    "v_mfma_f32_16x16x32_f16 v[76:79], v[240:243], v[72:75], v[76:79]\n"
    "v_pk_mul_f16 v64, v44, v46\n"
    "v_pk_mul_f16 v65, v45, v47\n"
    "v_mfma_f32_16x16x32_f16 v[80:83], v[244:247], v[72:75], v[80:83]\n"
    "v_add_f32 v11, v88, v89\n"
    "v_add_f32 v13, v90, v91\n"
    "v_mfma_f32_16x16x32_f16 v[80:83], v[232:235], v[68:71], v[80:83]\n"
    "v_mov_b64 v[88:89], 0\n"
    "v_mov_b64 v[90:91], 0\n"
    "v_mfma_f32_16x16x32_f16 v[84:87], v[236:239], v[68:71], v[84:87]\n"
    "s_waitcnt lgkmcnt(0)\n"
    "v_pk_mul_f16 v66, v48, v48 op_sel_hi:[0,1]\n"
    "v_pk_mul_f16 v67, v48, v49 op_sel_hi:[0,1]\n"
    "v_mfma_f32_16x16x32_f16 v[84:87], v[248:251], v[72:75], v[84:87]\n"
    "v_mul_f32 v11, v11, v13\n"
    "s_cmp_eq_u32 s45, -1\n"
    "s_cbranch_scc1 .Lxd_ex0\n"
    "s_cmp_lg_u32 s36, 0\n"
    "s_cbranch_scc1 .Lxd_pl1\n"
    ".Lxd_ph1:\n"
    "s_cbranch_vccnz .Lxd_pr19\n"
    "s_setprio 2\n"
    ".Lxd_pr19:\n"
    "v_smfmac_f32_16x16x64_f16 v[88:91], v[112:115], v[60:67], v2\n"
    "v_fmac_f32 v11, v76, v80\n"
    "v_fmac_f32 v11, v77, v81\n"
    "v_fmac_f32 v11, v78, v82\n"
    "v_fmac_f32 v11, v79, v83\n"
    "v_mfma_f32_16x16x32_f16 v[80:83], v[92:95], v[60:63], 0\n"
    "v_pk_mul_f16 v68, v58, v58 op_sel_hi:[0,1]\n"
    "v_pk_mul_f16 v69, v58, v49 op_sel_hi:[0,1]\n"
    "v_mfma_f32_16x16x32_f16 v[80:83], v[104:107], v[64:67], v[80:83]\n"
    "v_pk_mul_f16 v70, v48, v58 op_sel_hi:[0,1]\n"
    "v_pk_mul_f16 v71, v48, v59 op_sel_hi:[0,1]\n"
    "v_mfma_f32_16x16x32_f16 v[76:79], v[100:103], v[64:67], 0\n"
    "v_fmac_f32 v11, v84, v85\n"
    "v_fma_f32 v7, v86, v87, v11\n"
    "v_mfma_f32_16x16x32_f16 v[84:87], v[108:111], v[64:67], 0\n"
    "v_pk_mul_f16 v72, v50, v58 op_sel_hi:[0,1]\n"
    "v_pk_mul_f16 v73, v50, v59 op_sel_hi:[0,1]\n"
    "v_mfma_f32_16x16x32_f16 v[84:87], v[96:99], v[60:63], v[84:87]\n"
    "v_pk_mul_f16 v74, v52, v58 op_sel_hi:[0,1]\n"
    "v_pk_mul_f16 v75, v52, v59 op_sel_hi:[0,1]\n"
    "s_waitcnt vmcnt(0)\n"
    "v_mfma_f32_16x16x32_f16 v[76:79], v[116:119], v[68:71], v[76:79]\n"
    "v_pk_mul_f16 v60, v54, v58 op_sel_hi:[0,1]\n"
    "v_pk_mul_f16 v61, v54, v59 op_sel_hi:[0,1]\n"
    "v_mfma_f32_16x16x32_f16 v[76:79], v[128:131], v[72:75], v[76:79]\n"
    "v_pk_mul_f16 v62, v56, v58 op_sel_hi:[0,1]\n"
    "v_lshl_or_b32 v36, v36, 7, v19\n"
    "global_load_dwordx4 v[28:31], v36, s[8:9]\n"
    "v_mfma_f32_16x16x32_f16 v[80:83], v[132:135], v[72:75], v[80:83]\n"
    "v_pk_mul_f16 v63, v56, v59 op_sel_hi:[0,1]\n"
    "v_lshl_or_b32 v37, v37, 7, v19\n"
    "global_load_dwordx4 v[32:35], v37, s[8:9]\n"
    "v_mfma_f32_16x16x32_f16 v[80:83], v[120:123], v[68:71], v[80:83]\n"
    "v_pk_mul_f16 v64, v56, v56 op_sel_hi:[0,1]\n"
    "v_lshl_or_b32 v41, v41, 7, v20\n"
    "global_load_dword v40, v41, s[8:9]\n"
    "v_mfma_f32_16x16x32_f16 v[84:87], v[124:127], v[68:71], v[84:87]\n"
    "v_pk_mul_f16 v65, v56, v49 op_sel_hi:[0,1]\n"
    "v_lshl_or_b32 v38, v38, 7, v19\n"
    "global_load_dwordx4 v[36:39], v38, s[8:9]\n"
    "v_mfma_f32_16x16x32_f16 v[84:87], v[136:139], v[72:75], v[84:87]\n"
    "v_pk_mul_f16 v66, v48, v56 op_sel_hi:[0,1]\n"
    "v_smfmac_f32_16x16x64_f16 v[88:91], v[140:143], v[68:75], v2\n"
    "v_pk_mul_f16 v67, v48, v57 op_sel_hi:[0,1]\n"
    "v_mfma_f32_16x16x32_f16 v[76:79], v[144:147], v[60:63], v[76:79]\n"
    "v_pk_mul_f16 v68, v50, v56 op_sel_hi:[0,1]\n"
    "v_pk_mul_f16 v69, v50, v57 op_sel_hi:[0,1]\n"
    "v_mfma_f32_16x16x32_f16 v[76:79], v[156:159], v[64:67], v[76:79]\n"
    "v_pk_mul_f16 v70, v52, v56 op_sel_hi:[0,1]\n"
    "v_mfma_f32_16x16x32_f16 v[80:83], v[160:163], v[64:67], v[80:83]\n"
    "v_pk_mul_f16 v71, v52, v57 op_sel_hi:[0,1]\n"
    "v_mfma_f32_16x16x32_f16 v[80:83], v[148:151], v[60:63], v[80:83]\n"
    "v_pk_mul_f16 v72, v54, v56 op_sel_hi:[0,1]\n"
    "ds_read_b32 v22, v25 offset:520\n"
    "v_mfma_f32_16x16x32_f16 v[84:87], v[152:155], v[60:63], v[84:87]\n"
    "v_pk_mul_f16 v73, v54, v57 op_sel_hi:[0,1]\n"
    "v_mfma_f32_16x16x32_f16 v[84:87], v[164:167], v[64:67], v[84:87]\n"
    "v_pk_mul_f16 v74, v54, v54 op_sel_hi:[0,1]\n"
    "v_smfmac_f32_16x16x64_f16 v[88:91], v[168:171], v[60:67], v2\n"
    "v_pk_mul_f16 v75, v54, v49 op_sel_hi:[0,1]\n"
    "v_mfma_f32_16x16x32_f16 v[76:79], v[172:175], v[68:71], v[76:79]\n"
    "v_pk_mul_f16 v60, v48, v54 op_sel_hi:[0,1]\n"
    "v_pk_mul_f16 v61, v48, v55 op_sel_hi:[0,1]\n"
    "v_mfma_f32_16x16x32_f16 v[76:79], v[184:187], v[72:75], v[76:79]\n"
    "v_pk_mul_f16 v62, v50, v54 op_sel_hi:[0,1]\n"
    "v_mfma_f32_16x16x32_f16 v[80:83], v[188:191], v[72:75], v[80:83]\n"
    "v_pk_mul_f16 v63, v50, v55 op_sel_hi:[0,1]\n"
    "s_waitcnt lgkmcnt(0)\n"
    "v_readfirstlane_b32 s45, v22\n"
    "s_cmp_lt_i32 s45, 16\n"
    "s_cbranch_scc1 .Lxd_ck21\n"
    ".Lxd_ck21r:\n"
    "v_mfma_f32_16x16x32_f16 v[80:83], v[176:179], v[68:71], v[80:83]\n"
    "v_pk_mul_f16 v64, v52, v54 op_sel_hi:[0,1]\n"
    "ds_read_b64_tr_b16 v[44:45], v5 offset:8192\n"
    "v_mfma_f32_16x16x32_f16 v[84:87], v[180:183], v[68:71], v[84:87]\n"
    "v_pk_mul_f16 v65, v52, v55 op_sel_hi:[0,1]\n"
    "ds_read_b64_tr_b16 v[46:47], v6 offset:8192\n"
    "v_mfma_f32_16x16x32_f16 v[84:87], v[192:195], v[72:75], v[84:87]\n"
    "v_pk_mul_f16 v66, v52, v52 op_sel_hi:[0,1]\n"
    "ds_read_b64_tr_b16 v[58:59], v4 offset:11072\n"
    "s_cbranch_vccnz .Lxd_pr20\n"
    "s_setprio 0\n"
    ".Lxd_pr20:\n"
    "v_smfmac_f32_16x16x64_f16 v[88:91], v[196:199], v[68:75], v2\n"
    "v_pk_mul_f16 v67, v52, v49 op_sel_hi:[0,1]\n"
    "ds_read_b64_tr_b16 v[56:57], v4 offset:10496\n"
    "v_mfma_f32_16x16x32_f16 v[76:79], v[200:203], v[60:63], v[76:79]\n"
    "v_pk_mul_f16 v68, v48, v52 op_sel_hi:[0,1]\n"
    "v_pk_mul_f16 v69, v48, v53 op_sel_hi:[0,1]\n"
    "v_mfma_f32_16x16x32_f16 v[76:79], v[212:215], v[64:67], v[76:79]\n"
    "v_pk_mul_f16 v70, v50, v52 op_sel_hi:[0,1]\n"
    "ds_read_b64_tr_b16 v[54:55], v4 offset:9920\n"
    "v_mfma_f32_16x16x32_f16 v[80:83], v[216:219], v[64:67], v[80:83]\n"
    "v_pk_mul_f16 v71, v50, v53 op_sel_hi:[0,1]\n"
    "v_mfma_f32_16x16x32_f16 v[80:83], v[204:207], v[60:63], v[80:83]\n"
    "v_pk_mul_f16 v72, v50, v50 op_sel_hi:[0,1]\n"
    "v_mfma_f32_16x16x32_f16 v[84:87], v[208:211], v[60:63], v[84:87]\n"
    "v_pk_mul_f16 v73, v50, v49 op_sel_hi:[0,1]\n"
    "v_pk_mul_f16 v74, v48, v50 op_sel_hi:[0,1]\n"
    "v_mfma_f32_16x16x32_f16 v[84:87], v[220:223], v[64:67], v[84:87]\n"
    "v_pk_mul_f16 v75, v48, v51 op_sel_hi:[0,1]\n"
    "v_smfmac_f32_16x16x64_f16 v[88:91], v[224:227], v[60:67], v2\n"
    "ds_read_b64_tr_b16 v[48:49], v3 offset:8192\n"
    "ds_read_b64_tr_b16 v[50:51], v4 offset:8768\n"
    "ds_read_b64_tr_b16 v[52:53], v4 offset:9344\n"
    "v_smfmac_f32_16x16x64_f16 v[88:91], v[252:255], v[68:75], v2\n"
    "s_waitcnt lgkmcnt(6)\n"
    "v_pk_mul_f16 v60, v44, s45 op_sel:[0,1]\n"
    "v_pk_mul_f16 v61, v45, s45 op_sel:[0,1]\n"
    "v_mfma_f32_16x16x32_f16 v[76:79], v[228:231], v[68:71], v[76:79]\n"
    "v_pk_mul_f16 v62, v46, s45 op_sel:[0,1]\n"
    "v_pk_mul_f16 v63, v47, s45 op_sel:[0,1]\n"
    "v_mfma_f32_16x16x32_f16 v[76:79], v[240:243], v[72:75], v[76:79]\n"
    "v_pk_mul_f16 v64, v44, v46\n"
    "v_pk_mul_f16 v65, v45, v47\n"
    "v_mfma_f32_16x16x32_f16 v[80:83], v[244:247], v[72:75], v[80:83]\n"
    "v_add_f32 v11, v88, v89\n"
    "v_add_f32 v13, v90, v91\n"
    "v_mfma_f32_16x16x32_f16 v[80:83], v[232:235], v[68:71], v[80:83]\n"
    "v_mov_b64 v[88:89], 0\n"
    "v_mov_b64 v[90:91], 0\n"
    "v_mfma_f32_16x16x32_f16 v[84:87], v[236:239], v[68:71], v[84:87]\n"
    "s_waitcnt lgkmcnt(0)\n"
    "v_pk_mul_f16 v66, v48, v48 op_sel_hi:[0,1]\n"
    "v_pk_mul_f16 v67, v48, v49 op_sel_hi:[0,1]\n"
    "v_mfma_f32_16x16x32_f16 v[84:87], v[248:251], v[72:75], v[84:87]\n"
    "v_mul_f32 v11, v11, v13\n"
    "s_cmp_eq_u32 s45, -1\n"
    "s_cbranch_scc1 .Lxd_ex1\n"
    ".Lxd_ph2:\n"
    "s_cbranch_vccnz .Lxd_pr22\n"
    "s_setprio 2\n"
    ".Lxd_pr22:\n"
    "v_smfmac_f32_16x16x64_f16 v[88:91], v[112:115], v[60:67], v2\n"
    "v_fmac_f32 v11, v76, v80\n"
    "v_fmac_f32 v11, v77, v81\n"
    "v_fmac_f32 v11, v78, v82\n"
    "v_fmac_f32 v11, v79, v83\n"
    "v_mfma_f32_16x16x32_f16 v[80:83], v[92:95], v[60:63], 0\n"
    "v_pk_mul_f16 v68, v58, v58 op_sel_hi:[0,1]\n"
    "v_pk_mul_f16 v69, v58, v49 op_sel_hi:[0,1]\n"
    "v_mfma_f32_16x16x32_f16 v[80:83], v[104:107], v[64:67], v[80:83]\n"
    "v_pk_mul_f16 v70, v48, v58 op_sel_hi:[0,1]\n"
    "v_pk_mul_f16 v71, v48, v59 op_sel_hi:[0,1]\n"
    "v_mfma_f32_16x16x32_f16 v[76:79], v[100:103], v[64:67], 0\n"
    "v_fmac_f32 v11, v84, v85\n"
    "v_fma_f32 v8, v86, v87, v11\n"
    "v_mfma_f32_16x16x32_f16 v[84:87], v[108:111], v[64:67], 0\n"
    "v_pk_mul_f16 v72, v50, v58 op_sel_hi:[0,1]\n"
    "v_pk_mul_f16 v73, v50, v59 op_sel_hi:[0,1]\n"
    "v_mfma_f32_16x16x32_f16 v[84:87], v[96:99], v[60:63], v[84:87]\n"
    "v_pk_mul_f16 v74, v52, v58 op_sel_hi:[0,1]\n"
    "v_pk_mul_f16 v75, v52, v59 op_sel_hi:[0,1]\n"
    "s_waitcnt vmcnt(0)\n"
    "v_mfma_f32_16x16x32_f16 v[76:79], v[116:119], v[68:71], v[76:79]\n"
    "v_pk_mul_f16 v60, v54, v58 op_sel_hi:[0,1]\n"
    "v_pk_mul_f16 v61, v54, v59 op_sel_hi:[0,1]\n"
    "s_movk_i32 s43, 15744\n"
    "v_pk_maximum3_f16 v21, v28, v28, s43 op_sel_hi:[1,1,0] neg_lo:[0,1,0] neg_hi:[0,1,0]\n"
    "v_mfma_f32_16x16x32_f16 v[76:79], v[128:131], v[72:75], v[76:79]\n"
    "v_pk_mul_f16 v62, v56, v58 op_sel_hi:[0,1]\n"
    "s_cmp_lg_u32 s46, 0\n"
    "s_cbranch_scc1 .Lxd_ht24\n"
    ".Lxd_ht24r:\n"
    "v_pk_maximum3_f16 v21, v29, v29, v21 neg_lo:[0,1,0] neg_hi:[0,1,0]\n"
    "v_mfma_f32_16x16x32_f16 v[80:83], v[132:135], v[72:75], v[80:83]\n"
    "v_pk_mul_f16 v63, v56, v59 op_sel_hi:[0,1]\n"
    "v_pk_maximum3_f16 v21, v30, v30, v21 neg_lo:[0,1,0] neg_hi:[0,1,0]\n"
    "v_mfma_f32_16x16x32_f16 v[80:83], v[120:123], v[68:71], v[80:83]\n"
    "v_pk_mul_f16 v64, v56, v56 op_sel_hi:[0,1]\n"
    "v_pk_maximum3_f16 v21, v31, v31, v21 neg_lo:[0,1,0] neg_hi:[0,1,0]\n"
    "v_mfma_f32_16x16x32_f16 v[84:87], v[124:127], v[68:71], v[84:87]\n"
    "v_pk_mul_f16 v65, v56, v49 op_sel_hi:[0,1]\n"
    "v_pk_maximum3_f16 v21, v32, v32, v21 neg_lo:[0,1,0] neg_hi:[0,1,0]\n"
    "v_mfma_f32_16x16x32_f16 v[84:87], v[136:139], v[72:75], v[84:87]\n"
    "v_pk_mul_f16 v66, v48, v56 op_sel_hi:[0,1]\n"
    "v_pk_maximum3_f16 v21, v33, v33, v21 neg_lo:[0,1,0] neg_hi:[0,1,0]\n"
    "v_smfmac_f32_16x16x64_f16 v[88:91], v[140:143], v[68:75], v2\n"
    "v_pk_mul_f16 v67, v48, v57 op_sel_hi:[0,1]\n"
    "v_pk_maximum3_f16 v21, v34, v34, v21 neg_lo:[0,1,0] neg_hi:[0,1,0]\n"
    "v_mfma_f32_16x16x32_f16 v[76:79], v[144:147], v[60:63], v[76:79]\n"
    "v_pk_mul_f16 v68, v50, v56 op_sel_hi:[0,1]\n"
    "v_pk_mul_f16 v69, v50, v57 op_sel_hi:[0,1]\n"
    "v_pk_maximum3_f16 v21, v35, v35, v21 neg_lo:[0,1,0] neg_hi:[0,1,0]\n"
    "v_mfma_f32_16x16x32_f16 v[76:79], v[156:159], v[64:67], v[76:79]\n"
    "v_pk_mul_f16 v70, v52, v56 op_sel_hi:[0,1]\n"
    "v_pk_maximum3_f16 v21, v36, v36, v21 neg_lo:[0,1,0] neg_hi:[0,1,0]\n"
    "v_mfma_f32_16x16x32_f16 v[80:83], v[160:163], v[64:67], v[80:83]\n"
    "v_pk_mul_f16 v71, v52, v57 op_sel_hi:[0,1]\n"
    "v_pk_maximum3_f16 v21, v37, v37, v21 neg_lo:[0,1,0] neg_hi:[0,1,0]\n"
    "v_mfma_f32_16x16x32_f16 v[80:83], v[148:151], v[60:63], v[80:83]\n"
    "v_pk_mul_f16 v72, v54, v56 op_sel_hi:[0,1]\n"
    "ds_read_b32 v22, v25 offset:524\n"
    "v_pk_maximum3_f16 v21, v38, v38, v21 neg_lo:[0,1,0] neg_hi:[0,1,0]\n"
    "v_mfma_f32_16x16x32_f16 v[84:87], v[152:155], v[60:63], v[84:87]\n"
    "v_pk_mul_f16 v73, v54, v57 op_sel_hi:[0,1]\n"
    "v_pk_maximum3_f16 v21, v39, v39, v21 neg_lo:[0,1,0] neg_hi:[0,1,0]\n"
    "v_mfma_f32_16x16x32_f16 v[84:87], v[164:167], v[64:67], v[84:87]\n"
    "v_pk_mul_f16 v74, v54, v54 op_sel_hi:[0,1]\n"
    "v_pk_maximum3_f16 v21, v40, v40, v21 neg_lo:[0,1,0] neg_hi:[0,1,0]\n"
    "v_smfmac_f32_16x16x64_f16 v[88:91], v[168:171], v[60:67], v2\n"
    "v_pk_mul_f16 v75, v54, v49 op_sel_hi:[0,1]\n"
    "v_pk_max_u16 v21, v21, v21 op_sel:[0,1] op_sel_hi:[1,0]\n"
    "v_mfma_f32_16x16x32_f16 v[76:79], v[172:175], v[68:71], v[76:79]\n"
    "v_pk_mul_f16 v60, v48, v54 op_sel_hi:[0,1]\n"
    "v_pk_mul_f16 v61, v48, v55 op_sel_hi:[0,1]\n"
    "v_max_u32_dpp v21, v21, v21 quad_perm:[1,0,3,2] row_mask:0xf bank_mask:0xf\n"
    "v_mfma_f32_16x16x32_f16 v[76:79], v[184:187], v[72:75], v[76:79]\n"
    "v_pk_mul_f16 v62, v50, v54 op_sel_hi:[0,1]\n"
    "v_max_u32_dpp v21, v21, v21 quad_perm:[2,3,0,1] row_mask:0xf bank_mask:0xf\n"
    "v_mfma_f32_16x16x32_f16 v[80:83], v[188:191], v[72:75], v[80:83]\n"
    "v_pk_mul_f16 v63, v50, v55 op_sel_hi:[0,1]\n"
    "s_waitcnt lgkmcnt(0)\n"
    "v_readfirstlane_b32 s45, v22\n"
    "s_cmp_lt_i32 s45, 16\n"
    "s_cbranch_scc1 .Lxd_ck25\n"
    ".Lxd_ck25r:\n"
    "v_max_u32_dpp v21, v21, v21 row_half_mirror row_mask:0xf bank_mask:0xf\n"
    "v_mfma_f32_16x16x32_f16 v[80:83], v[176:179], v[68:71], v[80:83]\n"
    "v_pk_mul_f16 v64, v52, v54 op_sel_hi:[0,1]\n"
    "ds_read_b64_tr_b16 v[44:45], v5 offset:12288\n"
    "v_max_u32_dpp v21, v21, v21 row_mirror row_mask:0xf bank_mask:0xf\n"
    "v_mfma_f32_16x16x32_f16 v[84:87], v[180:183], v[68:71], v[84:87]\n"
    "v_pk_mul_f16 v65, v52, v55 op_sel_hi:[0,1]\n"
    "ds_read_b64_tr_b16 v[46:47], v6 offset:12288\n"
    "v_max_u32_dpp v21, v21, v21 row_bcast:15 row_mask:0xa bank_mask:0xf\n"
    "v_mfma_f32_16x16x32_f16 v[84:87], v[192:195], v[72:75], v[84:87]\n"
    "v_pk_mul_f16 v66, v52, v52 op_sel_hi:[0,1]\n"
    "ds_read_b64_tr_b16 v[58:59], v4 offset:15168\n"
    "v_max_u32_dpp v21, v21, v21 row_bcast:31 row_mask:0xc bank_mask:0xf\n"
    "s_cbranch_vccnz .Lxd_pr23\n"
    "s_setprio 0\n"
    ".Lxd_pr23:\n"
    "v_smfmac_f32_16x16x64_f16 v[88:91], v[196:199], v[68:75], v2\n"
    "v_pk_mul_f16 v67, v52, v49 op_sel_hi:[0,1]\n"
    "ds_read_b64_tr_b16 v[56:57], v4 offset:14592\n"
    "v_readlane_b32 s47, v21, 63\n"
    "v_mfma_f32_16x16x32_f16 v[76:79], v[200:203], v[60:63], v[76:79]\n"
    "v_pk_mul_f16 v68, v48, v52 op_sel_hi:[0,1]\n"
    "v_pk_mul_f16 v69, v48, v53 op_sel_hi:[0,1]\n"
    "s_and_b32 s50, s47, 0x7f80\n"
    "s_lshr_b32 s47, s50, 3\n"
    "s_sub_u32 s50, s50, 2816\n"
    "s_mul_i32 s51, s50, -8\n"
    "v_mfma_f32_16x16x32_f16 v[76:79], v[212:215], v[64:67], v[76:79]\n"
    "v_pk_mul_f16 v70, v50, v52 op_sel_hi:[0,1]\n"
    "ds_read_b64_tr_b16 v[54:55], v4 offset:14016\n"
    "v_pk_maximum3_f16 v21, v28, v28, s50 op_sel_hi:[1,1,0] neg_lo:[0,1,0] neg_hi:[0,1,0]\n"
    "v_mfma_f32_16x16x32_f16 v[80:83], v[216:219], v[64:67], v[80:83]\n"
    "v_pk_mul_f16 v71, v50, v53 op_sel_hi:[0,1]\n"
    "v_pk_mad_u16 v21, v21, 8, s51 op_sel_hi:[1,0,0]\n"
    "v_mfma_f32_16x16x32_f16 v[80:83], v[204:207], v[60:63], v[80:83]\n"
    "v_pk_mul_f16 v72, v50, v50 op_sel_hi:[0,1]\n"
    "v_bfi_b32 v28, s57, v21, v28\n"
    "v_mfma_f32_16x16x32_f16 v[84:87], v[208:211], v[60:63], v[84:87]\n"
    "v_pk_mul_f16 v73, v50, v49 op_sel_hi:[0,1]\n"
    "v_pk_mul_f16 v74, v48, v50 op_sel_hi:[0,1]\n"
    "v_pk_maximum3_f16 v12, v29, v29, s50 op_sel_hi:[1,1,0] neg_lo:[0,1,0] neg_hi:[0,1,0]\n"
    "v_mfma_f32_16x16x32_f16 v[84:87], v[220:223], v[64:67], v[84:87]\n"
    "v_pk_mul_f16 v75, v48, v51 op_sel_hi:[0,1]\n"
    "v_pk_mad_u16 v12, v12, 8, s51 op_sel_hi:[1,0,0]\n"
    "v_smfmac_f32_16x16x64_f16 v[88:91], v[224:227], v[60:67], v2\n"
    "ds_read_b64_tr_b16 v[48:49], v3 offset:12288\n"
    "ds_read_b64_tr_b16 v[50:51], v4 offset:12864\n"
    "ds_read_b64_tr_b16 v[52:53], v4 offset:13440\n"
    "v_bfi_b32 v29, s57, v12, v29\n"
    "v_smfmac_f32_16x16x64_f16 v[88:91], v[252:255], v[68:75], v2\n"
    "s_waitcnt lgkmcnt(6)\n"
    "v_pk_mul_f16 v60, v44, s45 op_sel:[0,1]\n"
    "v_pk_mul_f16 v61, v45, s45 op_sel:[0,1]\n"
    "v_mfma_f32_16x16x32_f16 v[76:79], v[228:231], v[68:71], v[76:79]\n"
    "v_pk_mul_f16 v62, v46, s45 op_sel:[0,1]\n"
    "v_pk_mul_f16 v63, v47, s45 op_sel:[0,1]\n"
    "v_mfma_f32_16x16x32_f16 v[76:79], v[240:243], v[72:75], v[76:79]\n"
    "v_pk_mul_f16 v64, v44, v46\n"
    "v_pk_mul_f16 v65, v45, v47\n"
    "v_mfma_f32_16x16x32_f16 v[80:83], v[244:247], v[72:75], v[80:83]\n"
    "v_add_f32 v11, v88, v89\n"
    "v_add_f32 v13, v90, v91\n"
    "v_mfma_f32_16x16x32_f16 v[80:83], v[232:235], v[68:71], v[80:83]\n"
    "v_mov_b64 v[88:89], 0\n"
    "v_mov_b64 v[90:91], 0\n"
    "v_mfma_f32_16x16x32_f16 v[84:87], v[236:239], v[68:71], v[84:87]\n"
    "s_waitcnt lgkmcnt(0)\n"
    "v_pk_mul_f16 v66, v48, v48 op_sel_hi:[0,1]\n"
    "v_pk_mul_f16 v67, v48, v49 op_sel_hi:[0,1]\n"
    "v_mfma_f32_16x16x32_f16 v[84:87], v[248:251], v[72:75], v[84:87]\n"
    "v_mul_f32 v11, v11, v13\n"
    "s_cmp_eq_u32 s45, -1\n"
    "s_cbranch_scc1 .Lxd_ex2\n"
    ".Lxd_ph3:\n"
    "s_cbranch_vccnz .Lxd_pr26\n"
    "s_setprio 2\n"
    ".Lxd_pr26:\n"
    "v_smfmac_f32_16x16x64_f16 v[88:91], v[112:115], v[60:67], v2\n"
    "v_fmac_f32 v11, v76, v80\n"
    "v_fmac_f32 v11, v77, v81\n"
    "v_fmac_f32 v11, v78, v82\n"
    "v_fmac_f32 v11, v79, v83\n"
    "v_mfma_f32_16x16x32_f16 v[80:83], v[92:95], v[60:63], 0\n"
    "v_pk_mul_f16 v68, v58, v58 op_sel_hi:[0,1]\n"
    "v_pk_mul_f16 v69, v58, v49 op_sel_hi:[0,1]\n"
    "v_xor_b32 v3, 0x4000, v3\n"
    "v_xor_b32 v4, 0x4000, v4\n"
    "v_mfma_f32_16x16x32_f16 v[80:83], v[104:107], v[64:67], v[80:83]\n"
    "v_pk_mul_f16 v70, v48, v58 op_sel_hi:[0,1]\n"
    "v_pk_mul_f16 v71, v48, v59 op_sel_hi:[0,1]\n"
    "v_xor_b32 v5, 0x4000, v5\n"
    "v_mfma_f32_16x16x32_f16 v[76:79], v[100:103], v[64:67], 0\n"
    "v_fmac_f32 v11, v84, v85\n"
    "v_fma_f32 v9, v86, v87, v11\n"
    "v_xor_b32 v6, 0x4000, v6\n"
    "v_mfma_f32_16x16x32_f16 v[84:87], v[108:111], v[64:67], 0\n"
    "v_pk_mul_f16 v72, v50, v58 op_sel_hi:[0,1]\n"
    "v_pk_mul_f16 v73, v50, v59 op_sel_hi:[0,1]\n"
    "v_pk_maximum3_f16 v21, v30, v30, s50 op_sel_hi:[1,1,0] neg_lo:[0,1,0] neg_hi:[0,1,0]\n"
    "v_pk_mad_u16 v21, v21, 8, s51 op_sel_hi:[1,0,0]\n"
    "v_mfma_f32_16x16x32_f16 v[84:87], v[96:99], v[60:63], v[84:87]\n"
    "v_pk_mul_f16 v74, v52, v58 op_sel_hi:[0,1]\n"
    "v_pk_mul_f16 v75, v52, v59 op_sel_hi:[0,1]\n"
    "v_bfi_b32 v30, s57, v21, v30\n"
    "v_mfma_f32_16x16x32_f16 v[76:79], v[116:119], v[68:71], v[76:79]\n"
    "v_pk_mul_f16 v60, v54, v58 op_sel_hi:[0,1]\n"
    "v_pk_mul_f16 v61, v54, v59 op_sel_hi:[0,1]\n"
    "v_pk_maximum3_f16 v12, v31, v31, s50 op_sel_hi:[1,1,0] neg_lo:[0,1,0] neg_hi:[0,1,0]\n"
    "v_mfma_f32_16x16x32_f16 v[76:79], v[128:131], v[72:75], v[76:79]\n"
    "v_pk_mul_f16 v62, v56, v58 op_sel_hi:[0,1]\n"
    "v_pk_mad_u16 v12, v12, 8, s51 op_sel_hi:[1,0,0]\n"
    "v_mfma_f32_16x16x32_f16 v[80:83], v[132:135], v[72:75], v[80:83]\n"
    "v_pk_mul_f16 v63, v56, v59 op_sel_hi:[0,1]\n"
    "v_bfi_b32 v31, s57, v12, v31\n"
    "v_pk_maximum3_f16 v21, v32, v32, s50 op_sel_hi:[1,1,0] neg_lo:[0,1,0] neg_hi:[0,1,0]\n"
    "v_mfma_f32_16x16x32_f16 v[80:83], v[120:123], v[68:71], v[80:83]\n"
    "v_pk_mul_f16 v64, v56, v56 op_sel_hi:[0,1]\n"
    "v_pk_mad_u16 v21, v21, 8, s51 op_sel_hi:[1,0,0]\n"
    "v_mfma_f32_16x16x32_f16 v[84:87], v[124:127], v[68:71], v[84:87]\n"
    "v_pk_mul_f16 v65, v56, v49 op_sel_hi:[0,1]\n"
    "v_bfi_b32 v32, s57, v21, v32\n"
    "v_mfma_f32_16x16x32_f16 v[84:87], v[136:139], v[72:75], v[84:87]\n"
    "v_pk_mul_f16 v66, v48, v56 op_sel_hi:[0,1]\n"
    "v_pk_maximum3_f16 v12, v33, v33, s50 op_sel_hi:[1,1,0] neg_lo:[0,1,0] neg_hi:[0,1,0]\n"
    "v_pk_mad_u16 v12, v12, 8, s51 op_sel_hi:[1,0,0]\n"
    "v_smfmac_f32_16x16x64_f16 v[88:91], v[140:143], v[68:75], v2\n"
    "v_pk_mul_f16 v67, v48, v57 op_sel_hi:[0,1]\n"
    "v_bfi_b32 v33, s57, v12, v33\n"
    "v_mfma_f32_16x16x32_f16 v[76:79], v[144:147], v[60:63], v[76:79]\n"
    "v_pk_mul_f16 v68, v50, v56 op_sel_hi:[0,1]\n"
    "v_pk_mul_f16 v69, v50, v57 op_sel_hi:[0,1]\n"
    "v_pk_maximum3_f16 v21, v34, v34, s50 op_sel_hi:[1,1,0] neg_lo:[0,1,0] neg_hi:[0,1,0]\n"
    "v_mfma_f32_16x16x32_f16 v[76:79], v[156:159], v[64:67], v[76:79]\n"
    "v_pk_mul_f16 v70, v52, v56 op_sel_hi:[0,1]\n"
    "v_pk_mad_u16 v21, v21, 8, s51 op_sel_hi:[1,0,0]\n"
    "v_mfma_f32_16x16x32_f16 v[80:83], v[160:163], v[64:67], v[80:83]\n"
    "v_pk_mul_f16 v71, v52, v57 op_sel_hi:[0,1]\n"
    "v_bfi_b32 v34, s57, v21, v34\n"
    "v_pk_maximum3_f16 v12, v35, v35, s50 op_sel_hi:[1,1,0] neg_lo:[0,1,0] neg_hi:[0,1,0]\n"
    "v_mfma_f32_16x16x32_f16 v[80:83], v[148:151], v[60:63], v[80:83]\n"
    "v_pk_mul_f16 v72, v54, v56 op_sel_hi:[0,1]\n"
    "ds_read_b32 v22, v25 offset:528\n"
    "v_pk_mad_u16 v12, v12, 8, s51 op_sel_hi:[1,0,0]\n"
    "v_mfma_f32_16x16x32_f16 v[84:87], v[152:155], v[60:63], v[84:87]\n"
    "v_pk_mul_f16 v73, v54, v57 op_sel_hi:[0,1]\n"
    "v_bfi_b32 v35, s57, v12, v35\n"
    "v_mfma_f32_16x16x32_f16 v[84:87], v[164:167], v[64:67], v[84:87]\n"
    "v_pk_mul_f16 v74, v54, v54 op_sel_hi:[0,1]\n"
    "v_pk_maximum3_f16 v21, v36, v36, s50 op_sel_hi:[1,1,0] neg_lo:[0,1,0] neg_hi:[0,1,0]\n"
    "v_pk_mad_u16 v21, v21, 8, s51 op_sel_hi:[1,0,0]\n"
    "v_smfmac_f32_16x16x64_f16 v[88:91], v[168:171], v[60:67], v2\n"
    "v_pk_mul_f16 v75, v54, v49 op_sel_hi:[0,1]\n"
    "v_bfi_b32 v36, s57, v21, v36\n"
    "v_mfma_f32_16x16x32_f16 v[76:79], v[172:175], v[68:71], v[76:79]\n"
    "v_pk_mul_f16 v60, v48, v54 op_sel_hi:[0,1]\n"
    "v_pk_mul_f16 v61, v48, v55 op_sel_hi:[0,1]\n"
    "v_pk_maximum3_f16 v12, v37, v37, s50 op_sel_hi:[1,1,0] neg_lo:[0,1,0] neg_hi:[0,1,0]\n"
    "v_mfma_f32_16x16x32_f16 v[76:79], v[184:187], v[72:75], v[76:79]\n"
    "v_pk_mul_f16 v62, v50, v54 op_sel_hi:[0,1]\n"
    "v_pk_mad_u16 v12, v12, 8, s51 op_sel_hi:[1,0,0]\n"
    "v_mfma_f32_16x16x32_f16 v[80:83], v[188:191], v[72:75], v[80:83]\n"
    "v_pk_mul_f16 v63, v50, v55 op_sel_hi:[0,1]\n"
    "s_waitcnt lgkmcnt(0)\n"
    "v_readfirstlane_b32 s45, v22\n"
    "s_cmp_lt_i32 s45, 16\n"
    "s_cbranch_scc1 .Lxd_ck28\n"
    ".Lxd_ck28r:\n"
    "v_bfi_b32 v37, s57, v12, v37\n"
    "v_pk_maximum3_f16 v21, v38, v38, s50 op_sel_hi:[1,1,0] neg_lo:[0,1,0] neg_hi:[0,1,0]\n"
    "v_mfma_f32_16x16x32_f16 v[80:83], v[176:179], v[68:71], v[80:83]\n"
    "v_pk_mul_f16 v64, v52, v54 op_sel_hi:[0,1]\n"
    "ds_read_b64_tr_b16 v[44:45], v5 offset:0\n"
    "s_and_b32 s40, s45, 15\n"
    "s_cbranch_scc1 .Lxd_dr29\n"
    "s_cmp_lt_u32 s55, 32\n"
    "s_cselect_b32 s44, 0, -1\n"
    "s_add_u32 s55, s55, 4\n"
    "v_add_u32_e64 v24, s44, 1\n"
    "s_mov_b64 exec, 1\n"
    "global_atomic_add v24, v17, v24, s[52:53] sc0 sc1\n"
    "s_mov_b64 exec, -1\n"
    ".Lxd_dr29r:\n"
    "v_pk_mad_u16 v21, v21, 8, s51 op_sel_hi:[1,0,0]\n"
    "v_mfma_f32_16x16x32_f16 v[84:87], v[180:183], v[68:71], v[84:87]\n"
    "v_pk_mul_f16 v65, v52, v55 op_sel_hi:[0,1]\n"
    "ds_read_b64_tr_b16 v[46:47], v6 offset:0\n"
    "v_bfi_b32 v38, s57, v21, v38\n"
    "v_mfma_f32_16x16x32_f16 v[84:87], v[192:195], v[72:75], v[84:87]\n"
    "v_pk_mul_f16 v66, v52, v52 op_sel_hi:[0,1]\n"
    "ds_read_b64_tr_b16 v[58:59], v4 offset:2880\n"
    "v_pk_maximum3_f16 v12, v39, v39, s50 op_sel_hi:[1,1,0] neg_lo:[0,1,0] neg_hi:[0,1,0]\n"
    "v_pk_mad_u16 v12, v12, 8, s51 op_sel_hi:[1,0,0]\n"
    "s_cbranch_vccnz .Lxd_pr27\n"
    "s_setprio 0\n"
    ".Lxd_pr27:\n"
    "v_smfmac_f32_16x16x64_f16 v[88:91], v[196:199], v[68:75], v2\n"
    "v_pk_mul_f16 v67, v52, v49 op_sel_hi:[0,1]\n"
    "ds_read_b64_tr_b16 v[56:57], v4 offset:2304\n"
    "v_bfi_b32 v39, s57, v12, v39\n"
    "v_mfma_f32_16x16x32_f16 v[76:79], v[200:203], v[60:63], v[76:79]\n"
    "v_pk_mul_f16 v68, v48, v52 op_sel_hi:[0,1]\n"
    "v_pk_mul_f16 v69, v48, v53 op_sel_hi:[0,1]\n"
    "v_pk_maximum3_f16 v21, v40, v40, s50 op_sel_hi:[1,1,0] neg_lo:[0,1,0] neg_hi:[0,1,0]\n"
    "v_mfma_f32_16x16x32_f16 v[76:79], v[212:215], v[64:67], v[76:79]\n"
    "v_pk_mul_f16 v70, v50, v52 op_sel_hi:[0,1]\n"
    "ds_read_b64_tr_b16 v[54:55], v4 offset:1728\n"
    "v_pk_mad_u16 v21, v21, 8, s51 op_sel_hi:[1,0,0]\n"
    "v_mfma_f32_16x16x32_f16 v[80:83], v[216:219], v[64:67], v[80:83]\n"
    "v_pk_mul_f16 v71, v50, v53 op_sel_hi:[0,1]\n"
    "v_bfi_b32 v40, s57, v21, v40\n"
    "s_sub_u32 s43, 4112, s47\n"
    "s_lshl_b32 s43, s43, 19\n"
    "v_cvt_pkrtz_f16_f32 v12, s43, s43\n"
    "v_mfma_f32_16x16x32_f16 v[80:83], v[204:207], v[60:63], v[80:83]\n"
    "v_pk_mul_f16 v72, v50, v50 op_sel_hi:[0,1]\n"
    "v_lshl_or_b32 v21, v12, 16, s47\n"
    "v_mfma_f32_16x16x32_f16 v[84:87], v[208:211], v[60:63], v[84:87]\n"
    "v_pk_mul_f16 v73, v50, v49 op_sel_hi:[0,1]\n"
    "v_pk_mul_f16 v74, v48, v50 op_sel_hi:[0,1]\n"
    "ds_write_b128 v15, v[28:31] offset:0\n"
    "ds_write_b128 v15, v[32:35] offset:144\n"
    "ds_write_b128 v15, v[36:39] offset:288\n"
    "v_mfma_f32_16x16x32_f16 v[84:87], v[220:223], v[64:67], v[84:87]\n"
    "v_pk_mul_f16 v75, v48, v51 op_sel_hi:[0,1]\n"
    "ds_write_b32 v16, v40 offset:3456\n"
    "ds_write_b32 v16, v12 offset:3744\n"
    "s_mov_b64 exec, 1\n"
    "ds_or_b32 v25, v21 offset:532\n"
    "s_mov_b64 exec, -1\n"
    "v_smfmac_f32_16x16x64_f16 v[88:91], v[224:227], v[60:67], v2\n"
    "ds_read_b64_tr_b16 v[48:49], v3 offset:0\n"
    "ds_read_b64_tr_b16 v[50:51], v4 offset:576\n"
    "ds_read_b64_tr_b16 v[52:53], v4 offset:1152\n"
    "v_smfmac_f32_16x16x64_f16 v[88:91], v[252:255], v[68:75], v2\n"
    "s_waitcnt lgkmcnt(12)\n"
    "v_pk_mul_f16 v60, v44, s45 op_sel:[0,1]\n"
    "v_pk_mul_f16 v61, v45, s45 op_sel:[0,1]\n"
    "v_mfma_f32_16x16x32_f16 v[76:79], v[228:231], v[68:71], v[76:79]\n"
    "v_pk_mul_f16 v62, v46, s45 op_sel:[0,1]\n"
    "v_pk_mul_f16 v63, v47, s45 op_sel:[0,1]\n"
    "v_mfma_f32_16x16x32_f16 v[76:79], v[240:243], v[72:75], v[76:79]\n"
    "v_pk_mul_f16 v64, v44, v46\n"
    "v_pk_mul_f16 v65, v45, v47\n"
    "v_mfma_f32_16x16x32_f16 v[80:83], v[244:247], v[72:75], v[80:83]\n"
    "v_add_f32 v11, v88, v89\n"
    "v_add_f32 v13, v90, v91\n"
    "v_mfma_f32_16x16x32_f16 v[80:83], v[232:235], v[68:71], v[80:83]\n"
    "v_mov_b64 v[88:89], 0\n"
    "v_mov_b64 v[90:91], 0\n"
    "v_mfma_f32_16x16x32_f16 v[84:87], v[236:239], v[68:71], v[84:87]\n"
    "s_waitcnt lgkmcnt(0)\n"
    "v_pk_mul_f16 v66, v48, v48 op_sel_hi:[0,1]\n"
    "v_pk_mul_f16 v67, v48, v49 op_sel_hi:[0,1]\n"
    "v_mfma_f32_16x16x32_f16 v[84:87], v[248:251], v[72:75], v[84:87]\n"
    "v_mul_f32 v11, v11, v13\n"
    "v_add_u32 v25, 16, v25\n"
    "s_cmp_eq_u32 s45, -1\n"
    "s_cbranch_scc1 .Lxd_ex3\n"
    "s_cmp_eq_u32 s36, 0\n"
    "s_cbranch_scc1 .Lxd_ph0\n"
    ".Lxd_pl0:\n"
    "s_cbranch_vccnz .Lxd_pr30\n"
    "s_setprio 2\n"
    ".Lxd_pr30:\n"
    "v_smfmac_f32_16x16x64_f16 v[88:91], v[112:115], v[60:67], v2\n"
    "v_fmac_f32 v11, v76, v80\n"
    "v_fmac_f32 v11, v77, v81\n"
    "v_fmac_f32 v11, v78, v82\n"
    "v_fmac_f32 v11, v79, v83\n"
    "v_mfma_f32_16x16x32_f16 v[80:83], v[92:95], v[60:63], 0\n"
    "v_pk_mul_f16 v68, v58, v58 op_sel_hi:[0,1]\n"
    "v_pk_mul_f16 v69, v58, v49 op_sel_hi:[0,1]\n"
    "v_mfma_f32_16x16x32_f16 v[80:83], v[104:107], v[64:67], v[80:83]\n"
    "v_pk_mul_f16 v70, v48, v58 op_sel_hi:[0,1]\n"
    "v_pk_mul_f16 v71, v48, v59 op_sel_hi:[0,1]\n"
    "v_mfma_f32_16x16x32_f16 v[76:79], v[100:103], v[64:67], 0\n"
    "v_fmac_f32 v11, v84, v85\n"
    "v_fma_f32 v10, v86, v87, v11\n"
    "v_mfma_f32_16x16x32_f16 v[84:87], v[108:111], v[64:67], 0\n"
    "v_pk_mul_f16 v72, v50, v58 op_sel_hi:[0,1]\n"
    "v_pk_mul_f16 v73, v50, v59 op_sel_hi:[0,1]\n"
    "v_mfma_f32_16x16x32_f16 v[84:87], v[96:99], v[60:63], v[84:87]\n"
    "v_pk_mul_f16 v74, v52, v58 op_sel_hi:[0,1]\n"
    "v_pk_mul_f16 v75, v52, v59 op_sel_hi:[0,1]\n"
    "v_mfma_f32_16x16x32_f16 v[76:79], v[116:119], v[68:71], v[76:79]\n"
    "v_pk_mul_f16 v60, v54, v58 op_sel_hi:[0,1]\n"
    "v_pk_mul_f16 v61, v54, v59 op_sel_hi:[0,1]\n"
    "v_mfma_f32_16x16x32_f16 v[76:79], v[128:131], v[72:75], v[76:79]\n"
    "v_pk_mul_f16 v62, v56, v58 op_sel_hi:[0,1]\n"
    "v_mfma_f32_16x16x32_f16 v[80:83], v[132:135], v[72:75], v[80:83]\n"
    "v_pk_mul_f16 v63, v56, v59 op_sel_hi:[0,1]\n"
    "v_mfma_f32_16x16x32_f16 v[80:83], v[120:123], v[68:71], v[80:83]\n"
    "v_pk_mul_f16 v64, v56, v56 op_sel_hi:[0,1]\n"
    "v_mfma_f32_16x16x32_f16 v[84:87], v[124:127], v[68:71], v[84:87]\n"
    "v_pk_mul_f16 v65, v56, v49 op_sel_hi:[0,1]\n"
    "v_mfma_f32_16x16x32_f16 v[84:87], v[136:139], v[72:75], v[84:87]\n"
    "v_pk_mul_f16 v66, v48, v56 op_sel_hi:[0,1]\n"
    "v_permlane32_swap_b32 v7, v8\n"
    "v_permlane32_swap_b32 v9, v10\n"
    "v_smfmac_f32_16x16x64_f16 v[88:91], v[140:143], v[68:75], v2\n"
    "v_pk_mul_f16 v67, v48, v57 op_sel_hi:[0,1]\n"
    "v_mul_f32 v7, v0, v7\n"
    "v_mul_f32 v9, v0, v9\n"
    "v_mfma_f32_16x16x32_f16 v[76:79], v[144:147], v[60:63], v[76:79]\n"
    "v_pk_mul_f16 v68, v50, v56 op_sel_hi:[0,1]\n"
    "v_pk_mul_f16 v69, v50, v57 op_sel_hi:[0,1]\n"
    "v_mfma_f32_16x16x32_f16 v[76:79], v[156:159], v[64:67], v[76:79]\n"
    "v_pk_mul_f16 v70, v52, v56 op_sel_hi:[0,1]\n"
    "v_fmac_f32 v7, v23, v8\n"
    "v_fmac_f32 v9, v23, v10\n"
    "v_mfma_f32_16x16x32_f16 v[80:83], v[160:163], v[64:67], v[80:83]\n"
    "v_pk_mul_f16 v71, v52, v57 op_sel_hi:[0,1]\n"
    "v_permlane16_swap_b32 v7, v9\n"
    "v_mfma_f32_16x16x32_f16 v[80:83], v[148:151], v[60:63], v[80:83]\n"
    "v_pk_mul_f16 v72, v54, v56 op_sel_hi:[0,1]\n"
    "ds_read_b32 v22, v25 offset:516\n"
    "v_mfma_f32_16x16x32_f16 v[84:87], v[152:155], v[60:63], v[84:87]\n"
    "v_pk_mul_f16 v73, v54, v57 op_sel_hi:[0,1]\n"
    "v_add_f32 v7, v7, v9\n"
    "v_mfma_f32_16x16x32_f16 v[84:87], v[164:167], v[64:67], v[84:87]\n"
    "v_pk_mul_f16 v74, v54, v54 op_sel_hi:[0,1]\n"
    "v_add_f32_dpp v7, v7, v7 quad_perm:[1,0,3,2] row_mask:0xf bank_mask:0xf\n"
    "v_smfmac_f32_16x16x64_f16 v[88:91], v[168:171], v[60:67], v2\n"
    "v_pk_mul_f16 v75, v54, v49 op_sel_hi:[0,1]\n"
    "v_add_f32_dpp v7, v7, v7 quad_perm:[2,3,0,1] row_mask:0xf bank_mask:0xf\n"
    "v_mfma_f32_16x16x32_f16 v[76:79], v[172:175], v[68:71], v[76:79]\n"
    "v_pk_mul_f16 v60, v48, v54 op_sel_hi:[0,1]\n"
    "v_pk_mul_f16 v61, v48, v55 op_sel_hi:[0,1]\n"
    "v_mfma_f32_16x16x32_f16 v[76:79], v[184:187], v[72:75], v[76:79]\n"
    "v_pk_mul_f16 v62, v50, v54 op_sel_hi:[0,1]\n"
    "v_add_f32_dpp v7, v7, v7 row_half_mirror row_mask:0xf bank_mask:0xf\n"
    "v_mfma_f32_16x16x32_f16 v[80:83], v[188:191], v[72:75], v[80:83]\n"
    "v_pk_mul_f16 v63, v50, v55 op_sel_hi:[0,1]\n"
    "s_waitcnt lgkmcnt(0)\n"
    "v_readfirstlane_b32 s45, v22\n"
    "s_cmp_lt_i32 s45, 16\n"
    "s_cbranch_scc1 .Lxd_ck32\n"
    ".Lxd_ck32r:\n"
    "v_mfma_f32_16x16x32_f16 v[80:83], v[176:179], v[68:71], v[80:83]\n"
    "v_pk_mul_f16 v64, v52, v54 op_sel_hi:[0,1]\n"
    "ds_read_b64_tr_b16 v[44:45], v5 offset:4096\n"
    "v_mfma_f32_16x16x32_f16 v[84:87], v[180:183], v[68:71], v[84:87]\n"
    "v_pk_mul_f16 v65, v52, v55 op_sel_hi:[0,1]\n"
    "ds_read_b64_tr_b16 v[46:47], v6 offset:4096\n"
    "v_mfma_f32_16x16x32_f16 v[84:87], v[192:195], v[72:75], v[84:87]\n"
    "v_pk_mul_f16 v66, v52, v52 op_sel_hi:[0,1]\n"
    "ds_read_b64_tr_b16 v[58:59], v4 offset:6976\n"
    "s_cbranch_vccnz .Lxd_pr31\n"
    "s_setprio 0\n"
    ".Lxd_pr31:\n"
    "v_smfmac_f32_16x16x64_f16 v[88:91], v[196:199], v[68:75], v2\n"
    "v_pk_mul_f16 v67, v52, v49 op_sel_hi:[0,1]\n"
    "ds_read_b64_tr_b16 v[56:57], v4 offset:6400\n"
    "v_mfma_f32_16x16x32_f16 v[76:79], v[200:203], v[60:63], v[76:79]\n"
    "v_pk_mul_f16 v68, v48, v52 op_sel_hi:[0,1]\n"
    "v_pk_mul_f16 v69, v48, v53 op_sel_hi:[0,1]\n"
    "v_mfma_f32_16x16x32_f16 v[76:79], v[212:215], v[64:67], v[76:79]\n"
    "v_pk_mul_f16 v70, v50, v52 op_sel_hi:[0,1]\n"
    "ds_read_b64_tr_b16 v[54:55], v4 offset:5824\n"
    "v_mfma_f32_16x16x32_f16 v[80:83], v[216:219], v[64:67], v[80:83]\n"
    "v_pk_mul_f16 v71, v50, v53 op_sel_hi:[0,1]\n"
    "v_add_f32_dpp v7, v7, v7 row_mirror row_mask:0xf bank_mask:0xf\n"
    "v_mfma_f32_16x16x32_f16 v[80:83], v[204:207], v[60:63], v[80:83]\n"
    "v_pk_mul_f16 v72, v50, v50 op_sel_hi:[0,1]\n"
    "ds_write_b32 v14, v7\n"
    "v_add_u32 v14, 64, v14\n"
    "v_mfma_f32_16x16x32_f16 v[84:87], v[208:211], v[60:63], v[84:87]\n"
    "v_pk_mul_f16 v73, v50, v49 op_sel_hi:[0,1]\n"
    "v_pk_mul_f16 v74, v48, v50 op_sel_hi:[0,1]\n"
    "v_mfma_f32_16x16x32_f16 v[84:87], v[220:223], v[64:67], v[84:87]\n"
    "v_pk_mul_f16 v75, v48, v51 op_sel_hi:[0,1]\n"
    "v_smfmac_f32_16x16x64_f16 v[88:91], v[224:227], v[60:67], v2\n"
    "ds_read_b64_tr_b16 v[48:49], v3 offset:4096\n"
    "ds_read_b64_tr_b16 v[50:51], v4 offset:4672\n"
    "ds_read_b64_tr_b16 v[52:53], v4 offset:5248\n"
    "v_smfmac_f32_16x16x64_f16 v[88:91], v[252:255], v[68:75], v2\n"
    "s_waitcnt lgkmcnt(7)\n"
    "v_pk_mul_f16 v60, v44, s45 op_sel:[0,1]\n"
    "v_pk_mul_f16 v61, v45, s45 op_sel:[0,1]\n"
    "v_mfma_f32_16x16x32_f16 v[76:79], v[228:231], v[68:71], v[76:79]\n"
    "v_pk_mul_f16 v62, v46, s45 op_sel:[0,1]\n"
    "v_pk_mul_f16 v63, v47, s45 op_sel:[0,1]\n"
    "v_mfma_f32_16x16x32_f16 v[76:79], v[240:243], v[72:75], v[76:79]\n"
    "v_pk_mul_f16 v64, v44, v46\n"
    "v_pk_mul_f16 v65, v45, v47\n"
    "v_mfma_f32_16x16x32_f16 v[80:83], v[244:247], v[72:75], v[80:83]\n"
    "v_add_f32 v11, v88, v89\n"
    "v_add_f32 v13, v90, v91\n"
    "v_mfma_f32_16x16x32_f16 v[80:83], v[232:235], v[68:71], v[80:83]\n"
    "v_mov_b64 v[88:89], 0\n"
    "v_mov_b64 v[90:91], 0\n"
    "v_mfma_f32_16x16x32_f16 v[84:87], v[236:239], v[68:71], v[84:87]\n"
    "s_waitcnt lgkmcnt(0)\n"
    "v_pk_mul_f16 v66, v48, v48 op_sel_hi:[0,1]\n"
    "v_pk_mul_f16 v67, v48, v49 op_sel_hi:[0,1]\n"
    "v_mfma_f32_16x16x32_f16 v[84:87], v[248:251], v[72:75], v[84:87]\n"
    "v_mul_f32 v11, v11, v13\n"
    "s_cmp_eq_u32 s45, -1\n"
    "s_cbranch_scc1 .Lxd_ex0\n"
    ".Lxd_pl1:\n"
    "s_cbranch_vccnz .Lxd_pr33\n"
    "s_setprio 2\n"
    ".Lxd_pr33:\n"
    "v_smfmac_f32_16x16x64_f16 v[88:91], v[112:115], v[60:67], v2\n"
    "v_fmac_f32 v11, v76, v80\n"
    "v_fmac_f32 v11, v77, v81\n"
    "v_fmac_f32 v11, v78, v82\n"
    "v_fmac_f32 v11, v79, v83\n"
    "v_mfma_f32_16x16x32_f16 v[80:83], v[92:95], v[60:63], 0\n"
    "v_pk_mul_f16 v68, v58, v58 op_sel_hi:[0,1]\n"
    "v_pk_mul_f16 v69, v58, v49 op_sel_hi:[0,1]\n"
    "v_mfma_f32_16x16x32_f16 v[80:83], v[104:107], v[64:67], v[80:83]\n"
    "v_pk_mul_f16 v70, v48, v58 op_sel_hi:[0,1]\n"
    "v_pk_mul_f16 v71, v48, v59 op_sel_hi:[0,1]\n"
    "v_mfma_f32_16x16x32_f16 v[76:79], v[100:103], v[64:67], 0\n"
    "v_fmac_f32 v11, v84, v85\n"
    "v_fma_f32 v7, v86, v87, v11\n"
    "v_mfma_f32_16x16x32_f16 v[84:87], v[108:111], v[64:67], 0\n"
    "v_pk_mul_f16 v72, v50, v58 op_sel_hi:[0,1]\n"
    "v_pk_mul_f16 v73, v50, v59 op_sel_hi:[0,1]\n"
    "v_mfma_f32_16x16x32_f16 v[84:87], v[96:99], v[60:63], v[84:87]\n"
    "v_pk_mul_f16 v74, v52, v58 op_sel_hi:[0,1]\n"
    "v_pk_mul_f16 v75, v52, v59 op_sel_hi:[0,1]\n"
    "v_mfma_f32_16x16x32_f16 v[76:79], v[116:119], v[68:71], v[76:79]\n"
    "v_pk_mul_f16 v60, v54, v58 op_sel_hi:[0,1]\n"
    "v_pk_mul_f16 v61, v54, v59 op_sel_hi:[0,1]\n"
    "v_mfma_f32_16x16x32_f16 v[76:79], v[128:131], v[72:75], v[76:79]\n"
    "v_pk_mul_f16 v62, v56, v58 op_sel_hi:[0,1]\n"
    "v_mfma_f32_16x16x32_f16 v[80:83], v[132:135], v[72:75], v[80:83]\n"
    "v_pk_mul_f16 v63, v56, v59 op_sel_hi:[0,1]\n"
    "v_mfma_f32_16x16x32_f16 v[80:83], v[120:123], v[68:71], v[80:83]\n"
    "v_pk_mul_f16 v64, v56, v56 op_sel_hi:[0,1]\n"
    "v_mfma_f32_16x16x32_f16 v[84:87], v[124:127], v[68:71], v[84:87]\n"
    "v_pk_mul_f16 v65, v56, v49 op_sel_hi:[0,1]\n"
    "v_mfma_f32_16x16x32_f16 v[84:87], v[136:139], v[72:75], v[84:87]\n"
    "v_pk_mul_f16 v66, v48, v56 op_sel_hi:[0,1]\n"
    "v_smfmac_f32_16x16x64_f16 v[88:91], v[140:143], v[68:75], v2\n"
    "v_pk_mul_f16 v67, v48, v57 op_sel_hi:[0,1]\n"
    "v_mfma_f32_16x16x32_f16 v[76:79], v[144:147], v[60:63], v[76:79]\n"
    "v_pk_mul_f16 v68, v50, v56 op_sel_hi:[0,1]\n"
    "v_pk_mul_f16 v69, v50, v57 op_sel_hi:[0,1]\n"
    "v_mfma_f32_16x16x32_f16 v[76:79], v[156:159], v[64:67], v[76:79]\n"
    "v_pk_mul_f16 v70, v52, v56 op_sel_hi:[0,1]\n"
    "v_mfma_f32_16x16x32_f16 v[80:83], v[160:163], v[64:67], v[80:83]\n"
    "v_pk_mul_f16 v71, v52, v57 op_sel_hi:[0,1]\n"
    "v_mfma_f32_16x16x32_f16 v[80:83], v[148:151], v[60:63], v[80:83]\n"
    "v_pk_mul_f16 v72, v54, v56 op_sel_hi:[0,1]\n"
    "ds_read_b32 v22, v25 offset:520\n"
    "v_mfma_f32_16x16x32_f16 v[84:87], v[152:155], v[60:63], v[84:87]\n"
    "v_pk_mul_f16 v73, v54, v57 op_sel_hi:[0,1]\n"
    "v_mfma_f32_16x16x32_f16 v[84:87], v[164:167], v[64:67], v[84:87]\n"
    "v_pk_mul_f16 v74, v54, v54 op_sel_hi:[0,1]\n"
    "v_smfmac_f32_16x16x64_f16 v[88:91], v[168:171], v[60:67], v2\n"
    "v_pk_mul_f16 v75, v54, v49 op_sel_hi:[0,1]\n"
    "v_mfma_f32_16x16x32_f16 v[76:79], v[172:175], v[68:71], v[76:79]\n"
    "v_pk_mul_f16 v60, v48, v54 op_sel_hi:[0,1]\n"
    "v_pk_mul_f16 v61, v48, v55 op_sel_hi:[0,1]\n"
    "v_mfma_f32_16x16x32_f16 v[76:79], v[184:187], v[72:75], v[76:79]\n"
    "v_pk_mul_f16 v62, v50, v54 op_sel_hi:[0,1]\n"
    "v_mfma_f32_16x16x32_f16 v[80:83], v[188:191], v[72:75], v[80:83]\n"
    "v_pk_mul_f16 v63, v50, v55 op_sel_hi:[0,1]\n"
    "s_waitcnt lgkmcnt(0)\n"
    "v_readfirstlane_b32 s45, v22\n"
    "s_cmp_lt_i32 s45, 16\n"
    "s_cbranch_scc1 .Lxd_ck35\n"
    ".Lxd_ck35r:\n"
    "v_mfma_f32_16x16x32_f16 v[80:83], v[176:179], v[68:71], v[80:83]\n"
    "v_pk_mul_f16 v64, v52, v54 op_sel_hi:[0,1]\n"
    "ds_read_b64_tr_b16 v[44:45], v5 offset:8192\n"
    "v_mfma_f32_16x16x32_f16 v[84:87], v[180:183], v[68:71], v[84:87]\n"
    "v_pk_mul_f16 v65, v52, v55 op_sel_hi:[0,1]\n"
    "ds_read_b64_tr_b16 v[46:47], v6 offset:8192\n"
    "v_mfma_f32_16x16x32_f16 v[84:87], v[192:195], v[72:75], v[84:87]\n"
    "v_pk_mul_f16 v66, v52, v52 op_sel_hi:[0,1]\n"
    "ds_read_b64_tr_b16 v[58:59], v4 offset:11072\n"
    "s_cbranch_vccnz .Lxd_pr34\n"
    "s_setprio 0\n"
    ".Lxd_pr34:\n"
    "v_smfmac_f32_16x16x64_f16 v[88:91], v[196:199], v[68:75], v2\n"
    "v_pk_mul_f16 v67, v52, v49 op_sel_hi:[0,1]\n"
    "ds_read_b64_tr_b16 v[56:57], v4 offset:10496\n"
    "v_mfma_f32_16x16x32_f16 v[76:79], v[200:203], v[60:63], v[76:79]\n"
    "v_pk_mul_f16 v68, v48, v52 op_sel_hi:[0,1]\n"
    "v_pk_mul_f16 v69, v48, v53 op_sel_hi:[0,1]\n"
    "v_mfma_f32_16x16x32_f16 v[76:79], v[212:215], v[64:67], v[76:79]\n"
    "v_pk_mul_f16 v70, v50, v52 op_sel_hi:[0,1]\n"
    "ds_read_b64_tr_b16 v[54:55], v4 offset:9920\n"
    "v_mfma_f32_16x16x32_f16 v[80:83], v[216:219], v[64:67], v[80:83]\n"
    "v_pk_mul_f16 v71, v50, v53 op_sel_hi:[0,1]\n"
    "v_mfma_f32_16x16x32_f16 v[80:83], v[204:207], v[60:63], v[80:83]\n"
    "v_pk_mul_f16 v72, v50, v50 op_sel_hi:[0,1]\n"
    "v_mfma_f32_16x16x32_f16 v[84:87], v[208:211], v[60:63], v[84:87]\n"
    "v_pk_mul_f16 v73, v50, v49 op_sel_hi:[0,1]\n"
    "v_pk_mul_f16 v74, v48, v50 op_sel_hi:[0,1]\n"
    "v_mfma_f32_16x16x32_f16 v[84:87], v[220:223], v[64:67], v[84:87]\n"
    "v_pk_mul_f16 v75, v48, v51 op_sel_hi:[0,1]\n"
    "v_smfmac_f32_16x16x64_f16 v[88:91], v[224:227], v[60:67], v2\n"
    "ds_read_b64_tr_b16 v[48:49], v3 offset:8192\n"
    "ds_read_b64_tr_b16 v[50:51], v4 offset:8768\n"
    "ds_read_b64_tr_b16 v[52:53], v4 offset:9344\n"
    "v_smfmac_f32_16x16x64_f16 v[88:91], v[252:255], v[68:75], v2\n"
    "s_waitcnt lgkmcnt(6)\n"
    "v_pk_mul_f16 v60, v44, s45 op_sel:[0,1]\n"
    "v_pk_mul_f16 v61, v45, s45 op_sel:[0,1]\n"
    "v_mfma_f32_16x16x32_f16 v[76:79], v[228:231], v[68:71], v[76:79]\n"
    "v_pk_mul_f16 v62, v46, s45 op_sel:[0,1]\n"
    "v_pk_mul_f16 v63, v47, s45 op_sel:[0,1]\n"
    "v_mfma_f32_16x16x32_f16 v[76:79], v[240:243], v[72:75], v[76:79]\n"
    "v_pk_mul_f16 v64, v44, v46\n"
    "v_pk_mul_f16 v65, v45, v47\n"
    "v_mfma_f32_16x16x32_f16 v[80:83], v[244:247], v[72:75], v[80:83]\n"
    "v_add_f32 v11, v88, v89\n"
    "v_add_f32 v13, v90, v91\n"
    "v_mfma_f32_16x16x32_f16 v[80:83], v[232:235], v[68:71], v[80:83]\n"
    "v_mov_b64 v[88:89], 0\n"
    "v_mov_b64 v[90:91], 0\n"
    "v_mfma_f32_16x16x32_f16 v[84:87], v[236:239], v[68:71], v[84:87]\n"
    "s_waitcnt lgkmcnt(0)\n"
    "v_pk_mul_f16 v66, v48, v48 op_sel_hi:[0,1]\n"
    "v_pk_mul_f16 v67, v48, v49 op_sel_hi:[0,1]\n"
    "v_mfma_f32_16x16x32_f16 v[84:87], v[248:251], v[72:75], v[84:87]\n"
    "v_mul_f32 v11, v11, v13\n"
    "s_cmp_eq_u32 s45, -1\n"
    "s_cbranch_scc1 .Lxd_ex1\n"
    ".Lxd_pl2:\n"
    "s_cbranch_vccnz .Lxd_pr36\n"
    "s_setprio 2\n"
    ".Lxd_pr36:\n"
    "v_smfmac_f32_16x16x64_f16 v[88:91], v[112:115], v[60:67], v2\n"
    "v_fmac_f32 v11, v76, v80\n"
    "v_fmac_f32 v11, v77, v81\n"
    "v_fmac_f32 v11, v78, v82\n"
    "v_fmac_f32 v11, v79, v83\n"
    "v_mfma_f32_16x16x32_f16 v[80:83], v[92:95], v[60:63], 0\n"
    "v_pk_mul_f16 v68, v58, v58 op_sel_hi:[0,1]\n"
    "v_pk_mul_f16 v69, v58, v49 op_sel_hi:[0,1]\n"
    "v_mfma_f32_16x16x32_f16 v[80:83], v[104:107], v[64:67], v[80:83]\n"
    "v_pk_mul_f16 v70, v48, v58 op_sel_hi:[0,1]\n"
    "v_pk_mul_f16 v71, v48, v59 op_sel_hi:[0,1]\n"
    "v_mfma_f32_16x16x32_f16 v[76:79], v[100:103], v[64:67], 0\n"
    "v_fmac_f32 v11, v84, v85\n"
    "v_fma_f32 v8, v86, v87, v11\n"
    "v_mfma_f32_16x16x32_f16 v[84:87], v[108:111], v[64:67], 0\n"
    "v_pk_mul_f16 v72, v50, v58 op_sel_hi:[0,1]\n"
    "v_pk_mul_f16 v73, v50, v59 op_sel_hi:[0,1]\n"
    "v_mfma_f32_16x16x32_f16 v[84:87], v[96:99], v[60:63], v[84:87]\n"
    "v_pk_mul_f16 v74, v52, v58 op_sel_hi:[0,1]\n"
    "v_pk_mul_f16 v75, v52, v59 op_sel_hi:[0,1]\n"
    "v_mfma_f32_16x16x32_f16 v[76:79], v[116:119], v[68:71], v[76:79]\n"
    "v_pk_mul_f16 v60, v54, v58 op_sel_hi:[0,1]\n"
    "v_pk_mul_f16 v61, v54, v59 op_sel_hi:[0,1]\n"
    "v_mfma_f32_16x16x32_f16 v[76:79], v[128:131], v[72:75], v[76:79]\n"
    "v_pk_mul_f16 v62, v56, v58 op_sel_hi:[0,1]\n"
    "s_cmp_lg_u32 s46, 0\n"
    "s_cbranch_scc1 .Lxd_ht38\n"
    ".Lxd_ht38r:\n"
    "v_mfma_f32_16x16x32_f16 v[80:83], v[132:135], v[72:75], v[80:83]\n"
    "v_pk_mul_f16 v63, v56, v59 op_sel_hi:[0,1]\n"
    "v_mfma_f32_16x16x32_f16 v[80:83], v[120:123], v[68:71], v[80:83]\n"
    "v_pk_mul_f16 v64, v56, v56 op_sel_hi:[0,1]\n"
    "v_mfma_f32_16x16x32_f16 v[84:87], v[124:127], v[68:71], v[84:87]\n"
    "v_pk_mul_f16 v65, v56, v49 op_sel_hi:[0,1]\n"
    "v_mfma_f32_16x16x32_f16 v[84:87], v[136:139], v[72:75], v[84:87]\n"
    "v_pk_mul_f16 v66, v48, v56 op_sel_hi:[0,1]\n"
    "v_smfmac_f32_16x16x64_f16 v[88:91], v[140:143], v[68:75], v2\n"
    "v_pk_mul_f16 v67, v48, v57 op_sel_hi:[0,1]\n"
    "v_mfma_f32_16x16x32_f16 v[76:79], v[144:147], v[60:63], v[76:79]\n"
    "v_pk_mul_f16 v68, v50, v56 op_sel_hi:[0,1]\n"
    "v_pk_mul_f16 v69, v50, v57 op_sel_hi:[0,1]\n"
    "v_mfma_f32_16x16x32_f16 v[76:79], v[156:159], v[64:67], v[76:79]\n"
    "v_pk_mul_f16 v70, v52, v56 op_sel_hi:[0,1]\n"
    "v_mfma_f32_16x16x32_f16 v[80:83], v[160:163], v[64:67], v[80:83]\n"
    "v_pk_mul_f16 v71, v52, v57 op_sel_hi:[0,1]\n"
    "v_mfma_f32_16x16x32_f16 v[80:83], v[148:151], v[60:63], v[80:83]\n"
    "v_pk_mul_f16 v72, v54, v56 op_sel_hi:[0,1]\n"
    "ds_read_b32 v22, v25 offset:524\n"
    "v_mfma_f32_16x16x32_f16 v[84:87], v[152:155], v[60:63], v[84:87]\n"
    "v_pk_mul_f16 v73, v54, v57 op_sel_hi:[0,1]\n"
    "v_mfma_f32_16x16x32_f16 v[84:87], v[164:167], v[64:67], v[84:87]\n"
    "v_pk_mul_f16 v74, v54, v54 op_sel_hi:[0,1]\n"
    "v_smfmac_f32_16x16x64_f16 v[88:91], v[168:171], v[60:67], v2\n"
    "v_pk_mul_f16 v75, v54, v49 op_sel_hi:[0,1]\n"
    "v_mfma_f32_16x16x32_f16 v[76:79], v[172:175], v[68:71], v[76:79]\n"
    "v_pk_mul_f16 v60, v48, v54 op_sel_hi:[0,1]\n"
    "v_pk_mul_f16 v61, v48, v55 op_sel_hi:[0,1]\n"
    "v_mfma_f32_16x16x32_f16 v[76:79], v[184:187], v[72:75], v[76:79]\n"
    "v_pk_mul_f16 v62, v50, v54 op_sel_hi:[0,1]\n"
    "v_mfma_f32_16x16x32_f16 v[80:83], v[188:191], v[72:75], v[80:83]\n"
    "v_pk_mul_f16 v63, v50, v55 op_sel_hi:[0,1]\n"
    "s_waitcnt lgkmcnt(0)\n"
    "v_readfirstlane_b32 s45, v22\n"
    "s_cmp_lt_i32 s45, 16\n"
    "s_cbranch_scc1 .Lxd_ck39\n"
    ".Lxd_ck39r:\n"
    "v_mfma_f32_16x16x32_f16 v[80:83], v[176:179], v[68:71], v[80:83]\n"
    "v_pk_mul_f16 v64, v52, v54 op_sel_hi:[0,1]\n"
    "ds_read_b64_tr_b16 v[44:45], v5 offset:12288\n"
    "v_mfma_f32_16x16x32_f16 v[84:87], v[180:183], v[68:71], v[84:87]\n"
    "v_pk_mul_f16 v65, v52, v55 op_sel_hi:[0,1]\n"
    "ds_read_b64_tr_b16 v[46:47], v6 offset:12288\n"
    "v_mfma_f32_16x16x32_f16 v[84:87], v[192:195], v[72:75], v[84:87]\n"
    "v_pk_mul_f16 v66, v52, v52 op_sel_hi:[0,1]\n"
    "ds_read_b64_tr_b16 v[58:59], v4 offset:15168\n"
    "s_cbranch_vccnz .Lxd_pr37\n"
    "s_setprio 0\n"
    ".Lxd_pr37:\n"
    "v_smfmac_f32_16x16x64_f16 v[88:91], v[196:199], v[68:75], v2\n"
    "v_pk_mul_f16 v67, v52, v49 op_sel_hi:[0,1]\n"
    "ds_read_b64_tr_b16 v[56:57], v4 offset:14592\n"
    "v_mfma_f32_16x16x32_f16 v[76:79], v[200:203], v[60:63], v[76:79]\n"
    "v_pk_mul_f16 v68, v48, v52 op_sel_hi:[0,1]\n"
    "v_pk_mul_f16 v69, v48, v53 op_sel_hi:[0,1]\n"
    "v_mfma_f32_16x16x32_f16 v[76:79], v[212:215], v[64:67], v[76:79]\n"
    "v_pk_mul_f16 v70, v50, v52 op_sel_hi:[0,1]\n"
    "ds_read_b64_tr_b16 v[54:55], v4 offset:14016\n"
    "v_mfma_f32_16x16x32_f16 v[80:83], v[216:219], v[64:67], v[80:83]\n"
    "v_pk_mul_f16 v71, v50, v53 op_sel_hi:[0,1]\n"
    "v_mfma_f32_16x16x32_f16 v[80:83], v[204:207], v[60:63], v[80:83]\n"
    "v_pk_mul_f16 v72, v50, v50 op_sel_hi:[0,1]\n"
    "v_mfma_f32_16x16x32_f16 v[84:87], v[208:211], v[60:63], v[84:87]\n"
    "v_pk_mul_f16 v73, v50, v49 op_sel_hi:[0,1]\n"
    "v_pk_mul_f16 v74, v48, v50 op_sel_hi:[0,1]\n"
    "v_mfma_f32_16x16x32_f16 v[84:87], v[220:223], v[64:67], v[84:87]\n"
    "v_pk_mul_f16 v75, v48, v51 op_sel_hi:[0,1]\n"
    "v_smfmac_f32_16x16x64_f16 v[88:91], v[224:227], v[60:67], v2\n"
    "ds_read_b64_tr_b16 v[48:49], v3 offset:12288\n"
    "ds_read_b64_tr_b16 v[50:51], v4 offset:12864\n"
    "ds_read_b64_tr_b16 v[52:53], v4 offset:13440\n"
    "v_smfmac_f32_16x16x64_f16 v[88:91], v[252:255], v[68:75], v2\n"
    "s_waitcnt lgkmcnt(6)\n"
    "v_pk_mul_f16 v60, v44, s45 op_sel:[0,1]\n"
    "v_pk_mul_f16 v61, v45, s45 op_sel:[0,1]\n"
    "v_mfma_f32_16x16x32_f16 v[76:79], v[228:231], v[68:71], v[76:79]\n"
    "v_pk_mul_f16 v62, v46, s45 op_sel:[0,1]\n"
    "v_pk_mul_f16 v63, v47, s45 op_sel:[0,1]\n"
    "v_mfma_f32_16x16x32_f16 v[76:79], v[240:243], v[72:75], v[76:79]\n"
    "v_pk_mul_f16 v64, v44, v46\n"
    "v_pk_mul_f16 v65, v45, v47\n"
    "v_mfma_f32_16x16x32_f16 v[80:83], v[244:247], v[72:75], v[80:83]\n"
    "v_add_f32 v11, v88, v89\n"
    "v_add_f32 v13, v90, v91\n"
    "v_mfma_f32_16x16x32_f16 v[80:83], v[232:235], v[68:71], v[80:83]\n"
    "v_mov_b64 v[88:89], 0\n"
    "v_mov_b64 v[90:91], 0\n"
    "v_mfma_f32_16x16x32_f16 v[84:87], v[236:239], v[68:71], v[84:87]\n"
    "s_waitcnt lgkmcnt(0)\n"
    "v_pk_mul_f16 v66, v48, v48 op_sel_hi:[0,1]\n"
    "v_pk_mul_f16 v67, v48, v49 op_sel_hi:[0,1]\n"
    "v_mfma_f32_16x16x32_f16 v[84:87], v[248:251], v[72:75], v[84:87]\n"
    "v_mul_f32 v11, v11, v13\n"
    "s_cmp_eq_u32 s45, -1\n"
    "s_cbranch_scc1 .Lxd_ex2\n"
    ".Lxd_pl3:\n"
    "s_cbranch_vccnz .Lxd_pr40\n"
    "s_setprio 2\n"
    ".Lxd_pr40:\n"
    "v_smfmac_f32_16x16x64_f16 v[88:91], v[112:115], v[60:67], v2\n"
    "v_fmac_f32 v11, v76, v80\n"
    "v_fmac_f32 v11, v77, v81\n"
    "v_fmac_f32 v11, v78, v82\n"
    "v_fmac_f32 v11, v79, v83\n"
    "v_mfma_f32_16x16x32_f16 v[80:83], v[92:95], v[60:63], 0\n"
    "v_pk_mul_f16 v68, v58, v58 op_sel_hi:[0,1]\n"
    "v_pk_mul_f16 v69, v58, v49 op_sel_hi:[0,1]\n"
    "v_mfma_f32_16x16x32_f16 v[80:83], v[104:107], v[64:67], v[80:83]\n"
    "v_pk_mul_f16 v70, v48, v58 op_sel_hi:[0,1]\n"
    "v_pk_mul_f16 v71, v48, v59 op_sel_hi:[0,1]\n"
    "v_mfma_f32_16x16x32_f16 v[76:79], v[100:103], v[64:67], 0\n"
    "v_fmac_f32 v11, v84, v85\n"
    "v_fma_f32 v9, v86, v87, v11\n"
    "v_mfma_f32_16x16x32_f16 v[84:87], v[108:111], v[64:67], 0\n"
    "v_pk_mul_f16 v72, v50, v58 op_sel_hi:[0,1]\n"
    "v_pk_mul_f16 v73, v50, v59 op_sel_hi:[0,1]\n"
    "v_mfma_f32_16x16x32_f16 v[84:87], v[96:99], v[60:63], v[84:87]\n"
    "v_pk_mul_f16 v74, v52, v58 op_sel_hi:[0,1]\n"
    "v_pk_mul_f16 v75, v52, v59 op_sel_hi:[0,1]\n"
    "v_mfma_f32_16x16x32_f16 v[76:79], v[116:119], v[68:71], v[76:79]\n"
    "v_pk_mul_f16 v60, v54, v58 op_sel_hi:[0,1]\n"
    "v_pk_mul_f16 v61, v54, v59 op_sel_hi:[0,1]\n"
    "v_mfma_f32_16x16x32_f16 v[76:79], v[128:131], v[72:75], v[76:79]\n"
    "v_pk_mul_f16 v62, v56, v58 op_sel_hi:[0,1]\n"
    "s_cmp_eq_u32 s36, 0\n"
    "s_cbranch_scc1 .Lxd_on42\n"
    ".Lxd_on42r:\n"
    "v_mfma_f32_16x16x32_f16 v[80:83], v[132:135], v[72:75], v[80:83]\n"
    "v_pk_mul_f16 v63, v56, v59 op_sel_hi:[0,1]\n"
    "v_xor_b32 v3, 0x4000, v3\n"
    "v_mfma_f32_16x16x32_f16 v[80:83], v[120:123], v[68:71], v[80:83]\n"
    "v_pk_mul_f16 v64, v56, v56 op_sel_hi:[0,1]\n"
    "v_xor_b32 v4, 0x4000, v4\n"
    "v_mfma_f32_16x16x32_f16 v[84:87], v[124:127], v[68:71], v[84:87]\n"
    "v_pk_mul_f16 v65, v56, v49 op_sel_hi:[0,1]\n"
    "v_xor_b32 v5, 0x4000, v5\n"
    "v_mfma_f32_16x16x32_f16 v[84:87], v[136:139], v[72:75], v[84:87]\n"
    "v_pk_mul_f16 v66, v48, v56 op_sel_hi:[0,1]\n"
    "v_xor_b32 v6, 0x4000, v6\n"
    "v_smfmac_f32_16x16x64_f16 v[88:91], v[140:143], v[68:75], v2\n"
    "v_pk_mul_f16 v67, v48, v57 op_sel_hi:[0,1]\n"
    "v_mfma_f32_16x16x32_f16 v[76:79], v[144:147], v[60:63], v[76:79]\n"
    "v_pk_mul_f16 v68, v50, v56 op_sel_hi:[0,1]\n"
    "v_pk_mul_f16 v69, v50, v57 op_sel_hi:[0,1]\n"
    "v_mfma_f32_16x16x32_f16 v[76:79], v[156:159], v[64:67], v[76:79]\n"
    "v_pk_mul_f16 v70, v52, v56 op_sel_hi:[0,1]\n"
    "v_mfma_f32_16x16x32_f16 v[80:83], v[160:163], v[64:67], v[80:83]\n"
    "v_pk_mul_f16 v71, v52, v57 op_sel_hi:[0,1]\n"
    "v_mfma_f32_16x16x32_f16 v[80:83], v[148:151], v[60:63], v[80:83]\n"
    "v_pk_mul_f16 v72, v54, v56 op_sel_hi:[0,1]\n"
    "ds_read_b32 v22, v25 offset:528\n"
    "v_mfma_f32_16x16x32_f16 v[84:87], v[152:155], v[60:63], v[84:87]\n"
    "v_pk_mul_f16 v73, v54, v57 op_sel_hi:[0,1]\n"
    "v_mfma_f32_16x16x32_f16 v[84:87], v[164:167], v[64:67], v[84:87]\n"
    "v_pk_mul_f16 v74, v54, v54 op_sel_hi:[0,1]\n"
    "v_smfmac_f32_16x16x64_f16 v[88:91], v[168:171], v[60:67], v2\n"
    "v_pk_mul_f16 v75, v54, v49 op_sel_hi:[0,1]\n"
    "v_mfma_f32_16x16x32_f16 v[76:79], v[172:175], v[68:71], v[76:79]\n"
    "v_pk_mul_f16 v60, v48, v54 op_sel_hi:[0,1]\n"
    "v_pk_mul_f16 v61, v48, v55 op_sel_hi:[0,1]\n"
    "v_mfma_f32_16x16x32_f16 v[76:79], v[184:187], v[72:75], v[76:79]\n"
    "v_pk_mul_f16 v62, v50, v54 op_sel_hi:[0,1]\n"
    "v_mfma_f32_16x16x32_f16 v[80:83], v[188:191], v[72:75], v[80:83]\n"
    "v_pk_mul_f16 v63, v50, v55 op_sel_hi:[0,1]\n"
    "s_waitcnt lgkmcnt(0)\n"
    "v_readfirstlane_b32 s45, v22\n"
    "s_cmp_lt_i32 s45, 16\n"
    "s_cbranch_scc1 .Lxd_ck44\n"
    ".Lxd_ck44r:\n"
    "v_mfma_f32_16x16x32_f16 v[80:83], v[176:179], v[68:71], v[80:83]\n"
    "v_pk_mul_f16 v64, v52, v54 op_sel_hi:[0,1]\n"
    "ds_read_b64_tr_b16 v[44:45], v5 offset:0\n"
    "v_mfma_f32_16x16x32_f16 v[84:87], v[180:183], v[68:71], v[84:87]\n"
    "v_pk_mul_f16 v65, v52, v55 op_sel_hi:[0,1]\n"
    "ds_read_b64_tr_b16 v[46:47], v6 offset:0\n"
    "v_mfma_f32_16x16x32_f16 v[84:87], v[192:195], v[72:75], v[84:87]\n"
    "v_pk_mul_f16 v66, v52, v52 op_sel_hi:[0,1]\n"
    "ds_read_b64_tr_b16 v[58:59], v4 offset:2880\n"
    "s_cbranch_vccnz .Lxd_pr41\n"
    "s_setprio 0\n"
    ".Lxd_pr41:\n"
    "v_smfmac_f32_16x16x64_f16 v[88:91], v[196:199], v[68:75], v2\n"
    "v_pk_mul_f16 v67, v52, v49 op_sel_hi:[0,1]\n"
    "ds_read_b64_tr_b16 v[56:57], v4 offset:2304\n"
    "v_mfma_f32_16x16x32_f16 v[76:79], v[200:203], v[60:63], v[76:79]\n"
    "v_pk_mul_f16 v68, v48, v52 op_sel_hi:[0,1]\n"
    "v_pk_mul_f16 v69, v48, v53 op_sel_hi:[0,1]\n"
    "v_mfma_f32_16x16x32_f16 v[76:79], v[212:215], v[64:67], v[76:79]\n"
    "v_pk_mul_f16 v70, v50, v52 op_sel_hi:[0,1]\n"
    "ds_read_b64_tr_b16 v[54:55], v4 offset:1728\n"
    "v_mfma_f32_16x16x32_f16 v[80:83], v[216:219], v[64:67], v[80:83]\n"
    "v_pk_mul_f16 v71, v50, v53 op_sel_hi:[0,1]\n"
    "v_mfma_f32_16x16x32_f16 v[80:83], v[204:207], v[60:63], v[80:83]\n"
    "v_pk_mul_f16 v72, v50, v50 op_sel_hi:[0,1]\n"
    "v_mfma_f32_16x16x32_f16 v[84:87], v[208:211], v[60:63], v[84:87]\n"
    "v_pk_mul_f16 v73, v50, v49 op_sel_hi:[0,1]\n"
    "v_pk_mul_f16 v74, v48, v50 op_sel_hi:[0,1]\n"
    "v_mfma_f32_16x16x32_f16 v[84:87], v[220:223], v[64:67], v[84:87]\n"
    "v_pk_mul_f16 v75, v48, v51 op_sel_hi:[0,1]\n"
    "v_smfmac_f32_16x16x64_f16 v[88:91], v[224:227], v[60:67], v2\n"
    "ds_read_b64_tr_b16 v[48:49], v3 offset:0\n"
    "ds_read_b64_tr_b16 v[50:51], v4 offset:576\n"
    "ds_read_b64_tr_b16 v[52:53], v4 offset:1152\n"
    "v_smfmac_f32_16x16x64_f16 v[88:91], v[252:255], v[68:75], v2\n"
    "s_waitcnt lgkmcnt(6)\n"
    "v_pk_mul_f16 v60, v44, s45 op_sel:[0,1]\n"
    "v_pk_mul_f16 v61, v45, s45 op_sel:[0,1]\n"
    "v_mfma_f32_16x16x32_f16 v[76:79], v[228:231], v[68:71], v[76:79]\n"
    "v_pk_mul_f16 v62, v46, s45 op_sel:[0,1]\n"
    "v_pk_mul_f16 v63, v47, s45 op_sel:[0,1]\n"
    "v_mfma_f32_16x16x32_f16 v[76:79], v[240:243], v[72:75], v[76:79]\n"
    "v_pk_mul_f16 v64, v44, v46\n"
    "v_pk_mul_f16 v65, v45, v47\n"
    "v_mfma_f32_16x16x32_f16 v[80:83], v[244:247], v[72:75], v[80:83]\n"
    "v_add_f32 v11, v88, v89\n"
    "v_add_f32 v13, v90, v91\n"
    "v_mfma_f32_16x16x32_f16 v[80:83], v[232:235], v[68:71], v[80:83]\n"
    "v_mov_b64 v[88:89], 0\n"
    "v_mov_b64 v[90:91], 0\n"
    "v_mfma_f32_16x16x32_f16 v[84:87], v[236:239], v[68:71], v[84:87]\n"
    "s_waitcnt lgkmcnt(0)\n"
    "v_pk_mul_f16 v66, v48, v48 op_sel_hi:[0,1]\n"
    "v_pk_mul_f16 v67, v48, v49 op_sel_hi:[0,1]\n"
    "v_mfma_f32_16x16x32_f16 v[84:87], v[248:251], v[72:75], v[84:87]\n"
    "v_mul_f32 v11, v11, v13\n"
    "v_add_u32 v25, 16, v25\n"
    "s_cmp_eq_u32 s45, -1\n"
    "s_cbranch_scc1 .Lxd_ex3\n"
    "s_cmp_eq_u32 s36, 0\n"
    "s_cbranch_scc1 .Lxd_ph0\n"
    "s_branch .Lxd_pl0\n"
    ".Lxd_ex3:\n"
    "v_fmac_f32 v11, v76, v80\n"
    "v_fmac_f32 v11, v77, v81\n"
    "v_fmac_f32 v11, v78, v82\n"
    "v_fmac_f32 v11, v79, v83\n"
    "s_nop 0\n"
    "v_fmac_f32 v11, v84, v85\n"
    "v_fma_f32 v10, v86, v87, v11\n"
    "s_branch .Lxd_fin\n"
    ".Lxd_ex2:\n"
    "v_fmac_f32 v11, v76, v80\n"
    "v_fmac_f32 v11, v77, v81\n"
    "v_fmac_f32 v11, v78, v82\n"
    "v_fmac_f32 v11, v79, v83\n"
    "s_nop 0\n"
    "v_fmac_f32 v11, v84, v85\n"
    "v_fma_f32 v9, v86, v87, v11\n"
    "s_branch .Lxd_fin\n"
    ".Lxd_ex1:\n"
    "v_fmac_f32 v11, v76, v80\n"
    "v_fmac_f32 v11, v77, v81\n"
    "v_fmac_f32 v11, v78, v82\n"
    "v_fmac_f32 v11, v79, v83\n"
    "s_nop 0\n"
    "v_fmac_f32 v11, v84, v85\n"
    "v_fma_f32 v8, v86, v87, v11\n"
    "s_branch .Lxd_fin\n"
    ".Lxd_ex0:\n"
    "v_fmac_f32 v11, v76, v80\n"
    "v_fmac_f32 v11, v77, v81\n"
    "v_fmac_f32 v11, v78, v82\n"
    "v_fmac_f32 v11, v79, v83\n"
    "s_nop 0\n"
    "v_fmac_f32 v11, v84, v85\n"
    "v_fma_f32 v7, v86, v87, v11\n"
    ".Lxd_fin:\n"
    "s_nop 1\n"
    "v_permlane32_swap_b32 v7, v8\n"
    "v_permlane32_swap_b32 v9, v10\n"
    "v_mul_f32 v7, v0, v7\n"
    "v_mul_f32 v9, v0, v9\n"
    "v_fmac_f32 v7, v23, v8\n"
    "v_fmac_f32 v9, v23, v10\n"
    "s_nop 1\n"
    "v_permlane16_swap_b32 v7, v9\n"
    "v_add_f32 v7, v7, v9\n"
    "s_nop 1\n"
    "v_add_f32_dpp v7, v7, v7 quad_perm:[1,0,3,2] row_mask:0xf bank_mask:0xf\n"
    "s_nop 1\n"
    "v_add_f32_dpp v7, v7, v7 quad_perm:[2,3,0,1] row_mask:0xf bank_mask:0xf\n"
    "s_nop 1\n"
    "v_add_f32_dpp v7, v7, v7 row_half_mirror row_mask:0xf bank_mask:0xf\n"
    "s_nop 1\n"
    "v_add_f32_dpp v7, v7, v7 row_mirror row_mask:0xf bank_mask:0xf\n"
    "ds_write_b32 v14, v7\n"
    "v_add_u32 v14, 64, v14\n"
    "s_cmp_lg_u32 s56, 2\n"
    "s_cbranch_scc1 .Lxd_f2\n"
    ".Lxd_f3:\n"
    "v_lshl_add_u32 v12, v18, 6, v20\n"
    "v_lshlrev_b32 v13, 2, v12\n"
    "v_add_u32 v12, 150528, v12\n"
    "v_add_u32 v13, 152128, v13\n"
    "v_mov_b32 v21, 154144\n"
    "s_lshl_b32 s26, s26, 16\n"
    "s_waitcnt vmcnt(0) lgkmcnt(0)\n"
    "s_barrier\n"
    "s_cmp_lg_u32 s20, 0\n"
    "s_cbranch_scc1 .Lxd_done\n"
    "ds_read_b128 v[28:31], v13\n"
    "ds_read_b32 v32, v12\n"
    "ds_read_u16 v33, v12 offset:512\n"
    "ds_read_b32 v34, v12 offset:1024\n"
    "ds_read_b32 v35, v21\n"
    "s_waitcnt lgkmcnt(0)\n"
    "v_add_f32 v28, v28, v29\n"
    "v_add_f32 v30, v30, v31\n"
    "v_cmp_lt_i32 vcc, 0, v32\n"
    "v_lshrrev_b32 v33, 4, v33\n"
    "v_add_f32 v28, v28, v30\n"
    "v_lshl_add_u32 v33, v33, 2, v35\n"
    "v_add_f32 v34, s26, v34\n"
    "v_ldexp_f32 v28, v28, v33\n"
    "v_add_f32 v28, v28, v34\n"
    "v_cvt_pk_bf16_f32 v28, v28, v28\n"
    "s_mov_b64 exec, vcc\n"
    "global_store_short v32, v28, s[70:71] offset:-2\n"
    "s_mov_b64 exec, -1\n"
    ".Lxd_done:\n"
    "s_endpgm\n"
    ".Lxd_cin:\n"
    "s_lshr_b32 s20, s43, 6\n"
    "v_and_b32 v1, 63, v0\n"
    "v_lshlrev_b32 v42, 4, v1\n"
    "s_cmp_ge_u32 s2, 8\n"
    "s_cbranch_scc1 .Lxd_help\n"
    "s_and_b32 s40, s2, 3\n"
    "s_lshl2_add_u32 s23, s40, s20\n"
    "s_lshl1_add_u32 s41, s40, s20\n"
    "s_add_u32 s41, s41, 12\n"
    "s_cmp_ge_u32 s20, 4\n"
    "s_cselect_b32 s23, s41, s23\n"
    "s_add_u32 s41, s40, 24\n"
    "s_cmp_ge_u32 s20, 6\n"
    "s_cselect_b32 s23, s41, s23\n"
    "s_cselect_b32 s24, 1, 0\n"
    "s_min_u32 s34, s23, 25\n"
    "s_lshr_b32 s35, s2, 2\n"
    "s_cmp_ge_u32 s2, 4\n"
    "s_cbranch_scc1 .Lxd_bR\n"
    "v_and_b32 v190, 31, v0\n"
    "v_min_u32 v190, 25, v190\n"
    "v_mul_u32_u24 v190, 52, v190\n"
    "v_bfe_u32 v191, v0, 5, 1\n"
    "v_lshl_add_u32 v190, v191, 4, v190\n"
    "s_mul_i32 s40, s34, 1352\n"
    "s_xor_b32 s66, s20, 7\n"
    "s_or_b32 s66, s66, s2\n"
    "s_waitcnt lgkmcnt(0)\n"
    "s_load_dwordx4 s[68:71], s[0:1], 64\n"
    "s_add_u32 s38, s12, s40\n"
    "s_addc_u32 s39, s13, 0\n"
    "s_cmp_lg_u32 s66, 0\n"
    "s_cbranch_scc1 .Lxd_on45\n"
    "v_and_b32 v188, 31, v0\n"
    "v_lshlrev_b32 v188, 7, v188\n"
    "v_add_u32 v188, 32768, v188\n"
    "v_mov_b32 v176, 114\n"
    "global_store_dword v188, v176, s[16:17] sc1\n"
    "global_load_dwordx4 v[180:183], v42, s[16:17] sc1\n"
    ".Lxd_on45:\n"
    "global_load_dwordx4 v[80:83], v190, s[38:39] offset:0\n"
    "global_load_dwordx4 v[84:87], v190, s[38:39] offset:32\n"
    "s_getpc_b64 s[60:61]\n"
    "s_add_u32 s60, s60, xd_tab@rel32@lo+4\n"
    "s_addc_u32 s61, s61, xd_tab@rel32@hi+12\n"
    "v_lshl_add_u32 v188, s24, 10, v42\n"
    "s_lshl1_add_u32 s40, s24, s35\n"
    "v_lshl_add_u32 v189, s40, 11, v42\n"
    "s_load_dword s25, s[16:17], 0\n"
    "global_load_dwordx4 v[44:47], v188, s[60:61]\n"
    "global_load_dwordx4 v[48:51], v189, s[60:61] offset:2048\n"
    "global_load_dwordx4 v[52:55], v189, s[60:61] offset:3072\n"
    "s_setprio 0\n"
    "v_mov_b64 v[188:189], 0\n"
    "v_lshlrev_b32 v190, 3, v0\n"
    "v_lshrrev_b32 v186, 5, v1\n"
    "ds_write_b64 v190, v[188:189] offset:36864\n"
    "v_bfe_u32 v190, v0, 1, 4\n"
    "v_lshlrev_b32 v72, 2, v186\n"
    "v_sub_u32 v190, v190, v72\n"
    "v_and_b32 v72, 16, v42\n"
    "s_movk_i32 s40, 0x3f80\n"
    "v_lshlrev_b32_e64 v191, v72, s40\n"
    "v_cmp_eq_u32 s[60:61], 0, v190\n"
    "v_cmp_eq_u32 s[62:63], 1, v190\n"
    "v_cmp_eq_u32 s[64:65], 2, v190\n"
    "v_cmp_eq_u32 s[50:51], 3, v190\n"
    "v_cndmask_b32 v72, 0, v191, s[60:61]\n"
    "v_cndmask_b32 v73, 0, v191, s[62:63]\n"
    "v_cndmask_b32 v74, 0, v191, s[64:65]\n"
    "v_cndmask_b32 v75, 0, v191, s[50:51]\n"
    "v_cmp_eq_u32 s[60:61], 8, v190\n"
    "v_cmp_eq_u32 s[62:63], 9, v190\n"
    "v_cmp_eq_u32 s[64:65], 10, v190\n"
    "v_cmp_eq_u32 s[50:51], 11, v190\n"
    "v_cndmask_b32 v76, 0, v191, s[60:61]\n"
    "v_cndmask_b32 v77, 0, v191, s[62:63]\n"
    "v_cndmask_b32 v78, 0, v191, s[64:65]\n"
    "v_cndmask_b32 v79, 0, v191, s[50:51]\n"
    "s_lshl_b32 s30, s20, 12\n"
    "s_add_u32 s30, s30, 65536\n"
    "v_and_b32 v188, 31, v0\n"
    "v_mul_u32_u24 v192, 528, v186\n"
    "v_lshl_add_u32 v192, v188, 2, v192\n"
    "v_add_u32 v192, s30, v192\n"
    "s_lshl_b32 s40, s35, 6\n"
    "s_add_u32 s40, s40, s20\n"
    "s_lshl1_add_u32 s41, s20, s35\n"
    "s_add_u32 s41, s41, 120\n"
    "s_cmp_ge_u32 s20, 4\n"
    "s_cselect_b32 s40, s41, s40\n"
    "s_lshl1_add_u32 s41, s35, 384\n"
    "s_cmp_ge_u32 s20, 6\n"
    "s_cselect_b32 s40, s41, s40\n"
    "s_cselect_b32 s42, 1, 192\n"
    "s_and_b32 s41, s2, 3\n"
    "s_lshl2_add_u32 s40, s41, s40\n"
    "v_lshrrev_b32 v188, 3, v1\n"
    "v_mul_u32_u24 v188, 448, v188\n"
    "v_bfe_u32 v189, v0, 2, 1\n"
    "v_mad_u32_u24 v188, v189, s42, v188\n"
    "v_and_b32 v189, 48, v42\n"
    "v_add3_u32 v188, v188, v189, s40\n"
    "v_lshlrev_b32 v193, 4, v188\n"
    "v_mov_b32 v177, 0\n"
    "s_waitcnt vmcnt(0) lgkmcnt(0)\n"
    "s_cmp_lg_u32 s66, 0\n"
    "s_cbranch_scc1 .Lxd_on46\n"
    "s_mov_b32 s40, 0x9e3779b1\n"
    "v_mul_lo_u32 v144, v180, s40\n"
    "v_xor_b32 v145, 0xa5c30f96, v180\n"
    "v_alignbit_b32 v144, s25, v144, 24\n"
    "v_mul_lo_u32 v146, v181, s40\n"
    "v_xor_b32 v147, 0xa5c30f96, v181\n"
    "v_alignbit_b32 v146, s25, v146, 24\n"
    "v_mul_lo_u32 v148, v182, s40\n"
    "v_xor_b32 v149, 0xa5c30f96, v182\n"
    "v_alignbit_b32 v148, s25, v148, 24\n"
    "v_mul_lo_u32 v150, v183, s40\n"
    "v_xor_b32 v151, 0xa5c30f96, v183\n"
    "v_alignbit_b32 v150, s25, v150, 24\n"
    "v_lshlrev_b32 v180, 1, v42\n"
    "s_add_u32 s40, s16, 4096\n"
    "s_addc_u32 s41, s17, 0\n"
    "global_store_dwordx4 v180, v[144:147], s[40:41] sc1\n"
    "global_store_dwordx4 v180, v[148:151], s[40:41] offset:16 sc1\n"
    ".Lxd_on46:\n"
    "s_barrier\n"
    "s_branch .Lxd_bD\n"
    ".Lxd_bR:\n"
    "s_mul_i32 s40, s20, 4480\n"
    "s_min_u32 s40, s40, 30672\n"
    "s_mov_b32 m0, s40\n"
    "s_add_u32 s40, s40, 70304\n"
    "v_lshrrev_b32 v190, 2, v1\n"
    "v_and_b32 v188, 48, v42\n"
    "s_movk_i32 s42, 1352\n"
    "v_min_u32 v191, 9, v190\n"
    "v_mad_u32_u24 v190, v190, s42, v188\n"
    "v_mad_u32_u24 v191, v191, s42, v188\n"
    "v_add_u32 v191, 20608, v191\n"
    "s_mul_i32 s41, s34, 52\n"
    "s_add_u32 s41, s41, 35152\n"
    "s_waitcnt lgkmcnt(0)\n"
    "s_load_dwordx4 s[68:71], s[0:1], 64\n"
    "s_add_u32 s38, s12, s40\n"
    "s_addc_u32 s39, s13, 0\n"
    "global_load_lds_dwordx4 v42, s[38:39] offset:0\n"
    "global_load_lds_dwordx4 v42, s[38:39] offset:1024\n"
    "global_load_lds_dwordx4 v42, s[38:39] offset:2048\n"
    "global_load_lds_dwordx4 v42, s[38:39] offset:3072\n"
    "global_load_lds_dwordx4 v42, s[38:39] offset:3456\n"
    "s_cmp_lg_u32 s20, 7\n"
    "s_cbranch_scc1 .Lxd_on47\n"
    "v_min_u32 v188, 38, v1\n"
    "s_mov_b32 m0, 40960\n"
    "v_lshlrev_b32 v188, 2, v188\n"
    "global_load_lds_dword v188, s[14:15]\n"
    ".Lxd_on47:\n"
    "s_lshl_b32 s28, s20, 11\n"
    "s_add_u32 s28, s28, 49152\n"
    "s_mov_b32 m0, s28\n"
    "s_add_u32 s38, s12, s41\n"
    "s_addc_u32 s39, s13, 0\n"
    "global_load_lds_dwordx4 v190, s[38:39] offset:0\n"
    "global_load_lds_dwordx4 v191, s[38:39] offset:1024\n"
    "s_getpc_b64 s[60:61]\n"
    "s_add_u32 s60, s60, xd_tab@rel32@lo+4\n"
    "s_addc_u32 s61, s61, xd_tab@rel32@hi+12\n"
    "v_lshl_add_u32 v188, s24, 10, v42\n"
    "s_lshl1_add_u32 s40, s24, s35\n"
    "v_lshl_add_u32 v189, s40, 11, v42\n"
    "s_load_dword s25, s[16:17], 0\n"
    "global_load_dwordx4 v[44:47], v188, s[60:61]\n"
    "global_load_dwordx4 v[48:51], v189, s[60:61] offset:2048\n"
    "global_load_dwordx4 v[52:55], v189, s[60:61] offset:3072\n"
    "s_setprio 0\n"
    "v_mov_b64 v[188:189], 0\n"
    "v_lshlrev_b32 v190, 3, v0\n"
    "v_lshrrev_b32 v186, 5, v1\n"
    "ds_write_b64 v190, v[188:189] offset:36864\n"
    "v_lshrrev_b32 v184, 4, v1\n"
    "v_and_b32 v185, 15, v0\n"
    "v_lshrrev_b32 v190, 2, v185\n"
    "v_lshl_add_u32 v64, v184, 3, v190\n"
    "v_add_u32 v65, 4, v64\n"
    "s_lshr_b32 s21, s20, 2\n"
    "s_and_b32 s43, s20, 3\n"
    "s_mul_i32 s40, s21, 13\n"
    "s_lshl2_add_u32 s40, s43, s40\n"
    "v_and_b32 v191, 3, v0\n"
    "v_add_lshl_u32 v188, v191, s40, 3\n"
    "v_min_u32 v64, 25, v64\n"
    "v_mad_u32_u24 v64, v64, s42, v188\n"
    "v_min_u32 v65, 25, v65\n"
    "v_mad_u32_u24 v65, v65, s42, v188\n"
    "v_lshl_add_u32 v188, s43, 4, v185\n"
    "v_cmp_lt_u32 vcc, 25, v188\n"
    "v_cmp_lt_u32 s[60:61], 51, v188\n"
    "v_lshl_add_u32 v66, s21, 6, v188\n"
    "v_cndmask_b32_e64 v189, 0, 6, vcc\n"
    "v_add_lshl_u32 v66, v66, v189, 1\n"
    "v_mov_b32 v189, 0x40000\n"
    "v_cmp_gt_u32 vcc, 26, v1\n"
    "v_cndmask_b32 v66, v66, v189, s[60:61]\n"
    "v_lshlrev_b32 v67, 6, v1\n"
    "v_cndmask_b32 v67, v189, v67, vcc\n"
    "v_lshlrev_b32 v68, 4, v184\n"
    "v_add_u32 v68, 40960, v68\n"
    "v_lshlrev_b32 v69, 1, v1\n"
    "v_lshl_add_u32 v190, v186, 3, v190\n"
    "v_and_b32 v188, 1, v184\n"
    "v_lshl_add_u32 v188, v188, 2, v191\n"
    "v_cmp_eq_u32 vcc, 7, v188\n"
    "v_lshlrev_b32 v188, 3, v188\n"
    "v_lshl_add_u32 v70, v190, 6, v188\n"
    "v_add_u32 v71, s28, v70\n"
    "v_mov_b32 v188, 38912\n"
    "v_cndmask_b32 v71, v71, v188, vcc\n"
    "v_cmp_gt_u32 vcc, 3, v184\n"
    "s_waitcnt vmcnt(5) lgkmcnt(0)\n"
    "s_barrier\n"
    "ds_read2_b64 v[176:179], v68 offset0:13 offset1:14\n"
    "ds_read_u16 v68, v69 offset:41012\n"
    "ds_read_u16 v69, v69 offset:40960\n"
    "ds_read_b64_tr_b16 v[112:113], v64 offset:0\n"
    "ds_read_b64_tr_b16 v[114:115], v65 offset:0\n"
    "ds_read_b64_tr_b16 v[116:117], v64 offset:208\n"
    "ds_read_b64_tr_b16 v[118:119], v65 offset:208\n"
    "ds_read_b64_tr_b16 v[120:121], v64 offset:416\n"
    "ds_read_b64_tr_b16 v[122:123], v65 offset:416\n"
    "ds_read_b64_tr_b16 v[124:125], v64 offset:624\n"
    "ds_read_b64_tr_b16 v[126:127], v65 offset:624\n"
    "ds_read_b64_tr_b16 v[128:129], v64 offset:832\n"
    "ds_read_b64_tr_b16 v[130:131], v65 offset:832\n"
    "ds_read_b64_tr_b16 v[132:133], v64 offset:1040\n"
    "ds_read_b64_tr_b16 v[134:135], v65 offset:1040\n"
    "ds_read_b64_tr_b16 v[136:137], v64 offset:1248\n"
    "ds_read_b64_tr_b16 v[138:139], v65 offset:1248\n"
    "s_waitcnt lgkmcnt(14)\n"
    "v_cndmask_b32 v177, 0, v177, vcc\n"
    "v_cndmask_b32 v178, 0, v178, vcc\n"
    "v_cndmask_b32 v179, 0, v179, vcc\n"
    "ds_write_b16 v67, v68 offset:36920\n"
    "s_waitcnt lgkmcnt(13)\n"
    "v_mfma_f32_16x16x32_bf16 v[144:147], v[176:179], v[112:115], 0\n"
    "s_waitcnt lgkmcnt(11)\n"
    "v_mfma_f32_16x16x32_bf16 v[148:151], v[176:179], v[116:119], 0\n"
    "s_waitcnt lgkmcnt(9)\n"
    "v_mfma_f32_16x16x32_bf16 v[152:155], v[176:179], v[120:123], 0\n"
    "s_waitcnt lgkmcnt(7)\n"
    "v_mfma_f32_16x16x32_bf16 v[156:159], v[176:179], v[124:127], 0\n"
    "s_waitcnt lgkmcnt(5)\n"
    "v_mfma_f32_16x16x32_bf16 v[160:163], v[176:179], v[128:131], 0\n"
    "s_waitcnt lgkmcnt(3)\n"
    "v_mfma_f32_16x16x32_bf16 v[164:167], v[176:179], v[132:135], 0\n"
    "s_waitcnt lgkmcnt(1)\n"
    "v_mfma_f32_16x16x32_bf16 v[168:171], v[176:179], v[136:139], 0\n"
    "s_waitcnt vmcnt(3)\n"
    "ds_read_b64_tr_b16 v[80:81], v71 offset:0\n"
    "ds_read_b64_tr_b16 v[82:83], v71 offset:256\n"
    "ds_read_b64_tr_b16 v[84:85], v71 offset:1024\n"
    "ds_read_b64_tr_b16 v[86:87], v71 offset:1280\n"
    "v_cvt_pk_bf16_f32 v144, v144, v144\n"
    "ds_write_b16 v66, v144 offset:36864\n"
    "v_cvt_pk_bf16_f32 v148, v148, v148\n"
    "ds_write_b16 v66, v148 offset:37120\n"
    "v_cvt_pk_bf16_f32 v152, v152, v152\n"
    "ds_write_b16 v66, v152 offset:37376\n"
    "v_cvt_pk_bf16_f32 v156, v156, v156\n"
    "ds_write_b16 v66, v156 offset:37632\n"
    "v_cvt_pk_bf16_f32 v160, v160, v160\n"
    "ds_write_b16 v66, v160 offset:37888\n"
    "v_cvt_pk_bf16_f32 v164, v164, v164\n"
    "ds_write_b16 v66, v164 offset:38144\n"
    "s_cmp_lg_u32 s21, 0\n"
    "s_cbranch_scc1 .Lxd_on48\n"
    "v_cvt_pk_bf16_f32 v168, v168, v168\n"
    "ds_write_b16 v66, v168 offset:38400\n"
    ".Lxd_on48:\n"
    "v_lshlrev_b32 v177, 16, v69\n"
    "s_waitcnt lgkmcnt(0)\n"
    "s_barrier\n"
    "ds_read_b64_tr_b16 v[72:73], v70 offset:36864\n"
    "ds_read_b64_tr_b16 v[74:75], v70 offset:37120\n"
    "ds_read_b64_tr_b16 v[76:77], v70 offset:37888\n"
    "ds_read_b64_tr_b16 v[78:79], v70 offset:38144\n"
    "s_lshl_b32 s30, s20, 12\n"
    "s_add_u32 s30, s30, 65536\n"
    "v_and_b32 v188, 31, v0\n"
    "v_mul_u32_u24 v192, 528, v186\n"
    "v_lshl_add_u32 v192, v188, 2, v192\n"
    "v_add_u32 v192, s30, v192\n"
    "s_lshl_b32 s40, s35, 6\n"
    "s_add_u32 s40, s40, s20\n"
    "s_lshl1_add_u32 s41, s20, s35\n"
    "s_add_u32 s41, s41, 120\n"
    "s_cmp_ge_u32 s20, 4\n"
    "s_cselect_b32 s40, s41, s40\n"
    "s_lshl1_add_u32 s41, s35, 384\n"
    "s_cmp_ge_u32 s20, 6\n"
    "s_cselect_b32 s40, s41, s40\n"
    "s_cselect_b32 s42, 1, 192\n"
    "s_and_b32 s41, s2, 3\n"
    "s_lshl2_add_u32 s40, s41, s40\n"
    "v_lshrrev_b32 v188, 3, v1\n"
    "v_mul_u32_u24 v188, 448, v188\n"
    "v_bfe_u32 v189, v0, 2, 1\n"
    "v_mad_u32_u24 v188, v189, s42, v188\n"
    "v_and_b32 v189, 48, v42\n"
    "v_add3_u32 v188, v188, v189, s40\n"
    "v_lshlrev_b32 v193, 4, v188\n"
    "s_waitcnt vmcnt(0) lgkmcnt(0)\n"
    ".Lxd_bD:\n"
    "v_mfma_f32_32x32x16_bf16 v[96:111], v[72:75], v[80:83], 0\n"
    "v_mfma_f32_32x32x16_bf16 v[96:111], v[80:83], v[72:75], v[96:111]\n"
    "v_mfma_f32_32x32x16_bf16 v[96:111], v[76:79], v[84:87], v[96:111]\n"
    "v_mfma_f32_32x32x16_bf16 v[96:111], v[84:87], v[76:79], v[96:111]\n"
    "v_readlane_b32 s43, v177, s34\n"
    "v_mad_u32_u16 v64, v44, 4, s30 op_sel:[0,0,0,0]\n"
    "v_mad_u32_u16 v65, v44, 4, s30 op_sel:[1,0,0,0]\n"
    "v_mad_u32_u16 v66, v45, 4, s30 op_sel:[0,0,0,0]\n"
    "v_mad_u32_u16 v67, v45, 4, s30 op_sel:[1,0,0,0]\n"
    "v_mad_u32_u16 v68, v46, 4, s30 op_sel:[0,0,0,0]\n"
    "v_mad_u32_u16 v69, v46, 4, s30 op_sel:[1,0,0,0]\n"
    "v_mad_u32_u16 v70, v47, 4, s30 op_sel:[0,0,0,0]\n"
    "v_mad_u32_u16 v71, v47, 4, s30 op_sel:[1,0,0,0]\n"
    "v_mov_b32 v184, 40944\n"
    "s_and_b32 s25, s25, 0xffffff\n"
    "s_mul_i32 s40, s25, 0x9e3779b1\n"
    "s_xor_b32 s41, s25, 0xa5c30f96\n"
    "s_lshr_b32 s40, s40, 24\n"
    "v_writelane_b32 v108, s43, 60\n"
    "ds_write2_b32 v192, v96, v97 offset0:0 offset1:33\n"
    "ds_write2_b32 v192, v98, v99 offset0:66 offset1:99\n"
    "v_add_u32 v192, 1056, v192\n"
    "ds_write2_b32 v192, v100, v101 offset0:0 offset1:33\n"
    "ds_write2_b32 v192, v102, v103 offset0:66 offset1:99\n"
    "v_add_u32 v192, 1056, v192\n"
    "ds_write2_b32 v192, v104, v105 offset0:0 offset1:33\n"
    "ds_write2_b32 v192, v106, v107 offset0:66 offset1:99\n"
    "v_add_u32 v192, 1056, v192\n"
    "ds_write2_b32 v192, v108, v109 offset0:0 offset1:33\n"
    "ds_read_b32 v56, v64\n"
    "ds_read_b32 v57, v65\n"
    "ds_read_b32 v58, v66\n"
    "ds_read_b32 v59, v67\n"
    "ds_read_b32 v60, v68\n"
    "ds_read_b32 v61, v69\n"
    "ds_read_b32 v62, v70\n"
    "ds_read_b32 v63, v71\n"
    "s_waitcnt lgkmcnt(6)\n"
    "v_pk_mul_f32 v[56:57], v[56:57], v[48:49]\n"
    "v_max3_f32 v188, |v56|, |v57|, 0\n"
    "s_waitcnt lgkmcnt(4)\n"
    "v_pk_mul_f32 v[58:59], v[58:59], v[50:51]\n"
    "v_max3_f32 v188, |v58|, |v59|, v188\n"
    "s_waitcnt lgkmcnt(2)\n"
    "v_pk_mul_f32 v[60:61], v[60:61], v[52:53]\n"
    "v_max3_f32 v188, |v60|, |v61|, v188\n"
    "s_waitcnt lgkmcnt(0)\n"
    "v_pk_mul_f32 v[62:63], v[62:63], v[54:55]\n"
    "v_max3_f32 v188, |v62|, |v63|, v188\n"
    "s_nop 1\n"
    "v_max_u32_dpp v188, v188, v188 quad_perm:[1,0,3,2] row_mask:0xf bank_mask:0xf\n"
    "s_nop 1\n"
    "v_max_u32_dpp v188, v188, v188 quad_perm:[2,3,0,1] row_mask:0xf bank_mask:0xf\n"
    "s_nop 1\n"
    "v_max_u32_dpp v188, v188, v188 row_half_mirror row_mask:0xf bank_mask:0xf\n"
    "s_nop 1\n"
    "v_max_u32_dpp v188, v188, v188 row_mirror row_mask:0xf bank_mask:0xf\n"
    "s_mov_b32 exec_lo, 0x10001\n"
    "s_mov_b32 exec_hi, 1\n"
    "ds_max_u32 v184, v188\n"
    "s_mov_b64 exec, -1\n"
    "s_add_u32 s38, s16, 1047552\n"
    "s_addc_u32 s39, s17, 0\n"
    "s_cmp_lt_u32 s23, 26\n"
    "s_cselect_b32 s43, 0xfff8fff8, 0\n"
    "s_waitcnt lgkmcnt(0)\n"
    "s_barrier\n"
    "ds_read_b32 v188, v184\n"
    "v_cmp_le_u32 vcc, 0x400, v193\n"
    "s_cmp_eq_u32 s20, 7\n"
    "s_cselect_b32 s42, 0, 48\n"
    "s_bfm_b64 s[60:61], s42, 0\n"
    "s_and_b64 s[60:61], s[60:61], vcc\n"
    "s_waitcnt lgkmcnt(0)\n"
    "v_lshrrev_b32 v188, 23, v188\n"
    "v_max_u32 v188, 40, v188\n"
    "v_min_u32 v188, 250, v188\n"
    "v_sub_u32 v190, 268, v188\n"
    "v_lshlrev_b32 v190, 23, v190\n"
    "v_pk_mul_f32 v[56:57], v[56:57], v[190:191] op_sel_hi:[1,0]\n"
    "v_cvt_pkrtz_f16_f32 v56, v56, v57\n"
    "v_add_u32 v56, 0x40004, v56\n"
    "v_and_b32 v56, s43, v56\n"
    "v_pk_mul_f32 v[58:59], v[58:59], v[190:191] op_sel_hi:[1,0]\n"
    "v_cvt_pkrtz_f16_f32 v57, v58, v59\n"
    "v_add_u32 v57, 0x40004, v57\n"
    "v_and_b32 v57, s43, v57\n"
    "v_pk_mul_f32 v[60:61], v[60:61], v[190:191] op_sel_hi:[1,0]\n"
    "v_cvt_pkrtz_f16_f32 v58, v60, v61\n"
    "v_add_u32 v58, 0x40004, v58\n"
    "v_and_b32 v58, s43, v58\n"
    "v_pk_mul_f32 v[62:63], v[62:63], v[190:191] op_sel_hi:[1,0]\n"
    "v_cvt_pkrtz_f16_f32 v59, v62, v63\n"
    "v_add_u32 v59, 0x40004, v59\n"
    "v_and_b32 v59, s43, v59\n"
    "v_lshl_or_b32 v188, v188, 8, s40\n"
    "v_mov_b32 v189, s41\n"
    "s_lshl3_add_u32 s40, s2, 40960\n"
    "v_mov_b32 v190, s40\n"
    "s_cmp_eq_u32 s20, 7\n"
    "s_cbranch_scc1 .Lxd_b3\n"
    "s_mov_b64 exec, s[60:61]\n"
    "global_store_dwordx4 v193, v[56:59], s[38:39] sc1\n"
    "s_mov_b64 exec, -1\n"
    ".Lxd_b3:\n"
    "s_waitcnt vmcnt(0)\n"
    "s_barrier\n"
    "s_cmp_lg_u32 s20, 0\n"
    "s_cbranch_scc1 .Lxd_on49\n"
    "s_mov_b64 exec, 1\n"
    "global_store_dwordx2 v190, v[188:189], s[16:17] sc1\n"
    "s_mov_b64 exec, -1\n"
    ".Lxd_on49:\n"
    "s_branch .Lxd_h1\n"
    ".Lxd_help:\n"
    "s_lshr_b32 s41, s2, 3\n"
    "s_sub_u32 s41, s41, 1\n"
    "s_cmp_lg_u32 s20, 7\n"
    "s_cbranch_scc1 .Lxd_on51\n"
    "s_mul_i32 s40, s41, 60\n"
    "v_min_u32 v7, 59, v1\n"
    "v_add_lshl_u32 v7, v7, s40, 7\n"
    "s_getpc_b64 s[60:61]\n"
    ".Lxd_pc50:\n"
    "s_sub_u32 s60, s60, .Lxd_pc50-.Lxd_top\n"
    "s_subb_u32 s61, s61, 0\n"
    "v_min_u32 v7, .Lxd_end-.Lxd_top-4, v7\n"
    "global_load_dword v7, v7, s[60:61]\n"
    ".Lxd_on51:\n"
    "v_mul_u32_u24 v68, 52, v1\n"
    "v_lshrrev_b32 v68, 8, v68\n"
    "v_mad_i32_i24 v69, v68, -5, v1\n"
    "s_and_b32 s40, s2, 0xf8\n"
    "s_add_u32 s40, s40, s20\n"
    "s_mul_i32 s40, s40, 13\n"
    "s_sub_u32 s40, s40, 104\n"
    "v_add_u32 v68, s40, v68\n"
    "v_min_u32 v68, 0x1ff, v68\n"
    "v_lshrrev_b32 v70, 8, v68\n"
    "s_movk_i32 s40, 1792\n"
    "v_mad_u32_u24 v68, v70, s40, v68\n"
    "s_and_b32 s40, s2, 7\n"
    "v_lshl_add_u32 v68, s40, 8, v68\n"
    "v_mul_u32_u24 v68, 104, v68\n"
    "s_lshl_b32 s40, s2, 2\n"
    "s_lshr_b32 s40, 0xa8864220, s40\n"
    "s_and_b32 s40, s40, 14\n"
    "s_lshl_b32 s40, s40, 1\n"
    "v_add3_u32 v70, v69, s40, 2\n"
    "v_subrev_u32 v71, 24, v70\n"
    "v_min_u32 v70, v70, v71\n"
    "v_lshl_add_u32 v70, v70, 2, v68\n"
    "s_waitcnt lgkmcnt(0)\n"
    "s_load_dwordx4 s[68:71], s[0:1], 64\n"
    "global_load_dword v60, v70, s[4:5]\n"
    "v_add3_u32 v70, v69, s40, 7\n"
    "v_subrev_u32 v71, 24, v70\n"
    "v_min_u32 v70, v70, v71\n"
    "v_lshl_add_u32 v70, v70, 2, v68\n"
    "global_load_dword v61, v70, s[4:5]\n"
    "v_add3_u32 v70, v69, s40, 12\n"
    "v_subrev_u32 v71, 24, v70\n"
    "v_min_u32 v70, v70, v71\n"
    "v_lshl_add_u32 v70, v70, 2, v68\n"
    "global_load_dword v62, v70, s[4:5]\n"
    "v_add3_u32 v70, v69, s40, 17\n"
    "v_subrev_u32 v71, 24, v70\n"
    "v_min_u32 v70, v70, v71\n"
    "v_lshl_add_u32 v70, v70, 2, v68\n"
    "global_load_dword v63, v70, s[4:5]\n"
    "v_add3_u32 v70, v69, s40, 22\n"
    "v_subrev_u32 v71, 24, v70\n"
    "v_min_u32 v70, v70, v71\n"
    "v_add_u32 v71, 22, v69\n"
    "v_cmp_lt_u32 vcc, 23, v71\n"
    "v_min_u32 v71, 25, v71\n"
    "s_nop 0\n"
    "v_cndmask_b32 v70, v70, v71, vcc\n"
    "v_lshl_add_u32 v70, v70, 2, v68\n"
    "global_load_dword v64, v70, s[4:5]\n"
    ".Lxd_h1:\n"
    "s_lshr_b32 s3, s2, 4\n"
    "s_mul_i32 s40, s3, 3\n"
    "s_add_u32 s47, s2, s40\n"
    "s_and_b32 s47, s47, 15\n"
    "s_lshr_b32 s21, s20, 2\n"
    "s_lshl1_add_u32 s22, s21, s20\n"
    "s_and_b32 s22, s22, 3\n"
    "s_lshl_b32 s58, s21, 8\n"
    "s_add_u32 s58, s58, 150528\n"
    "s_mov_b32 s40, s47\n"
    "s_mul_i32 s54, s40, 512\n"
    "s_add_u32 s54, s54, 2\n"
    "s_lshl_b32 s40, s40, 7\n"
    "s_add_u32 s40, s40, 32768\n"
    "s_add_u32 s52, s16, s40\n"
    "s_addc_u32 s53, s17, 0\n"
    "s_mov_b32 s57, 0x7fff7fff\n"
    "s_mov_b32 s56, 0\n"
    "s_mov_b32 s27, 0\n"
    "s_mov_b32 s55, 9\n"
    "s_mov_b32 s44, 0\n"
    "s_mov_b32 s46, 0\n"
    "v_lshrrev_b32 v17, 3, v1\n"
    "v_mul_u32_u24 v17, 12, v17\n"
    "v_and_b32 v19, 7, v1\n"
    "v_lshlrev_b32 v19, 4, v19\n"
    "v_lshrrev_b32 v18, 5, v1\n"
    "v_lshlrev_b32 v18, 2, v18\n"
    "v_and_b32 v20, 31, v1\n"
    "v_lshlrev_b32 v20, 2, v20\n"
    "s_lshl2_add_u32 s40, s22, s58\n"
    "s_sub_u32 s40, s40, 20\n"
    "v_mov_b32 v25, s40\n"
    "s_lshl1_add_u32 s23, s3, s21\n"
    "s_mul_i32 s23, s23, 6\n"
    "s_add_u32 s23, s23, s22\n"
    "s_add_u32 s23, s23, 78\n"
    "v_mov_b32 v24, s23\n"
    "v_lshlrev_b32 v13, 2, v0\n"
    "v_readfirstlane_b32 s40, v24\n"
    "s_or_b32 s40, s40, s44\n"
    "s_lshl1_add_u32 s41, s40, s54\n"
    "s_cmpk_lt_u32 s40, 256\n"
    "s_cselect_b32 s41, s41, 2\n"
    "v_mov_b32 v12, s41\n"
    "s_mul_i32 s41, s41, 52\n"
    "s_add_u32 s38, s4, s41\n"
    "s_addc_u32 s39, s5, 0\n"
    "global_load_dwordx3 v[36:38], v17, s[38:39] offset:-104\n"
    "global_load_dword v41, v18, s[38:39] offset:-8\n"
    "s_setprio 0\n"
    "v_mov_b32 v21, 0\n"
    "v_add_u32 v13, 149504, v13\n"
    "ds_write_b32 v13, v21\n"
    "s_cmp_lt_u32 s2, 8\n"
    "s_cbranch_scc1 .Lxd_hp52\n"
    "s_lshl_b32 s40, s2, 2\n"
    "s_lshr_b32 s40, 0xa8864220, s40\n"
    "s_and_b32 s40, s40, 14\n"
    "s_mul_i32 s40, s40, 3328\n"
    "s_and_b32 s41, s2, 0xf8\n"
    "s_add_u32 s41, s41, s20\n"
    "s_mul_i32 s41, s41, 42\n"
    "s_add_u32 s40, s40, s41\n"
    "s_add_u32 s40, s40, 4656\n"
    "v_min_u32 v68, 41, v1\n"
    "v_add_lshl_u32 v68, v68, s40, 7\n"
    "s_mov_b32 m0, 159744\n"
    "s_nop 0\n"
    "global_load_lds_dword v68, s[6:7]\n"
    "s_waitcnt vmcnt(3)\n"
    "s_mov_b32 m0, 159744\n"
    "v_lshlrev_b32 v60, 7, v60\n"
    "global_load_lds_dword v60, s[8:9]\n"
    "s_waitcnt vmcnt(2)\n"
    "s_branch .Lxd_h3\n"
    ".Lxd_hp52:\n"
    "s_waitcnt vmcnt(0)\n"
    ".Lxd_h3:\n"
    "v_lshl_or_b32 v36, v36, 7, v19\n"
    "v_lshl_or_b32 v37, v37, 7, v19\n"
    "v_lshl_or_b32 v38, v38, 7, v19\n"
    "v_lshl_or_b32 v41, v41, 7, v20\n"
    "global_load_dwordx4 v[28:31], v36, s[8:9]\n"
    "global_load_dwordx4 v[32:35], v37, s[8:9]\n"
    "global_load_dword v40, v41, s[8:9]\n"
    "global_load_dwordx4 v[36:39], v38, s[8:9]\n"
    "s_waitcnt lgkmcnt(0)\n"
    "s_barrier\n"
    "ds_write_b32 v25, v12 offset:20\n"
    "s_load_dword s26, s[68:69], 0\n"
    "s_lshl_b32 s43, s21, 16\n"
    "v_lshrrev_b32 v12, 4, v1\n"
    "v_bfe_u32 v13, v1, 2, 2\n"
    "v_and_b32 v21, 3, v1\n"
    "s_lshl_b32 s40, s22, 5\n"
    "s_add_u32 s40, s40, s43\n"
    "v_lshl_add_u32 v21, v21, 3, s40\n"
    "s_movk_i32 s41, 144\n"
    "v_add_u32 v22, v12, v13\n"
    "v_and_b32 v22, 3, v22\n"
    "v_mad_u32_u24 v4, v22, s41, v21\n"
    "v_cmp_gt_u32 vcc, 2, v13\n"
    "v_add_u32 v24, 22, v13\n"
    "v_lshlrev_b32 v13, 3, v13\n"
    "v_cndmask_b32 v22, v24, v22, vcc\n"
    "v_mad_u32_u24 v3, v22, s41, v21\n"
    "v_cmp_eq_u32 vcc, 3, v12\n"
    "v_xor_b32 v22, 0, v12\n"
    "v_and_b32 v22, 1, v22\n"
    "v_lshlrev_b32 v22, 1, v22\n"
    "v_lshl_add_u32 v22, v12, 3, v22\n"
    "s_mov_b32 s40, 0x05040100\n"
    "v_bfe_u32 v24, s40, v13, 8\n"
    "v_add_u32 v22, v22, v24\n"
    "s_mov_b32 s40, 0x1a191818\n"
    "v_bfe_u32 v24, s40, v13, 8\n"
    "v_cndmask_b32 v22, v22, v24, vcc\n"
    "v_mad_u32_u24 v5, v22, s41, v21\n"
    "v_xor_b32 v22, 1, v12\n"
    "v_and_b32 v22, 1, v22\n"
    "s_cmp_lt_u32 s2, 8\n"
    "s_cbranch_scc1 .Lxd_hp53\n"
    "s_mov_b32 m0, 159744\n"
    "v_lshlrev_b32 v61, 7, v61\n"
    "global_load_lds_dword v61, s[8:9]\n"
    "s_mov_b32 m0, 159744\n"
    "v_lshlrev_b32 v62, 7, v62\n"
    "global_load_lds_dword v62, s[8:9]\n"
    ".Lxd_hp53:\n"
    "v_lshlrev_b32 v22, 1, v22\n"
    "v_lshl_add_u32 v22, v12, 3, v22\n"
    "s_mov_b32 s40, 0x05040100\n"
    "v_bfe_u32 v24, s40, v13, 8\n"
    "v_add_u32 v22, v22, v24\n"
    "s_mov_b32 s40, 0x1a191918\n"
    "v_bfe_u32 v24, s40, v13, 8\n"
    "v_cndmask_b32 v22, v22, v24, vcc\n"
    "v_mad_u32_u24 v6, v22, s41, v21\n"
    "v_bfrev_b32 v14, v12\n"
    "v_lshrrev_b32 v14, 26, v14\n"
    "s_add_u32 s40, s22, 3\n"
    "s_and_b32 s40, s40, 3\n"
    "s_lshl2_add_u32 s40, s40, s22\n"
    "s_add_u32 s40, s40, s58\n"
    "s_lshl_b32 s40, s40, 2\n"
    "s_add_u32 s40, s40, -450048\n"
    "v_add_u32 v14, s40, v14\n"
    "v_and_b32 v2, 1, v1\n"
    "v_mul_u32_u24 v2, 0xaaaa, v2\n"
    "v_add_u32 v2, 0x4444, v2\n"
    "v_mad_u32_u24 v15, v17, 36, v19\n"
    "v_mad_u32_u24 v16, v18, 36, v20\n"
    "s_lshl4_add_u32 s41, s21, s22\n"
    "v_lshrrev_b32 v12, 5, v19\n"
    "v_sub_u32 v12, 1, v12\n"
    "v_and_b32 v12, 3, v12\n"
    "v_add_lshl_u32 v12, v12, s41, 12\n"
    "v_add_u32 v15, v12, v15\n"
    "v_lshrrev_b32 v12, 5, v20\n"
    "v_sub_u32 v12, 1, v12\n"
    "v_and_b32 v12, 3, v12\n"
    "v_add_lshl_u32 v12, v12, s41, 12\n"
    "v_add_u32 v16, v12, v16\n"
    "s_lshl3_add_u32 s40, s2, 4096\n"
    "s_lshl_b32 s41, s2, 2\n"
    "v_lshlrev_b32 v11, 3, v1\n"
    "v_add_u32 v11, 40960, v11\n"
    "v_writelane_b32 v11, s40, 8\n"
    "v_writelane_b32 v11, s41, 9\n"
    "v_add_u32 v43, 4096, v42\n"
    "s_cmp_lt_u32 s2, 8\n"
    "s_cbranch_scc1 .Lxd_hp54\n"
    "s_waitcnt vmcnt(2)\n"
    "s_branch .Lxd_h2\n"
    ".Lxd_hp54:\n"
    "s_waitcnt vmcnt(0)\n"
    ".Lxd_h2:\n"
    "s_cmp_lg_u32 s20, 7\n"
    "s_cbranch_scc1 .Lxd_on55\n"
    "s_mov_b64 exec, 0x3ff\n"
    "global_load_dwordx2 v[26:27], v11, s[16:17] sc1\n"
    "s_mov_b64 exec, -1\n"
    ".Lxd_on55:\n"
    "s_movk_i32 s43, 15744\n"
    "v_pk_maximum3_f16 v21, v28, v28, s43 op_sel_hi:[1,1,0] neg_lo:[0,1,0] neg_hi:[0,1,0]\n"
    "v_pk_maximum3_f16 v21, v29, v29, v21 neg_lo:[0,1,0] neg_hi:[0,1,0]\n"
    "v_pk_maximum3_f16 v21, v30, v30, v21 neg_lo:[0,1,0] neg_hi:[0,1,0]\n"
    "v_pk_maximum3_f16 v21, v31, v31, v21 neg_lo:[0,1,0] neg_hi:[0,1,0]\n"
    "v_pk_maximum3_f16 v21, v32, v32, v21 neg_lo:[0,1,0] neg_hi:[0,1,0]\n"
    "v_pk_maximum3_f16 v21, v33, v33, v21 neg_lo:[0,1,0] neg_hi:[0,1,0]\n"
    "v_pk_maximum3_f16 v21, v34, v34, v21 neg_lo:[0,1,0] neg_hi:[0,1,0]\n"
    "v_pk_maximum3_f16 v21, v35, v35, v21 neg_lo:[0,1,0] neg_hi:[0,1,0]\n"
    "v_pk_maximum3_f16 v21, v36, v36, v21 neg_lo:[0,1,0] neg_hi:[0,1,0]\n"
    "v_pk_maximum3_f16 v21, v37, v37, v21 neg_lo:[0,1,0] neg_hi:[0,1,0]\n"
    "v_pk_maximum3_f16 v21, v38, v38, v21 neg_lo:[0,1,0] neg_hi:[0,1,0]\n"
    "v_pk_maximum3_f16 v21, v39, v39, v21 neg_lo:[0,1,0] neg_hi:[0,1,0]\n"
    "v_pk_maximum3_f16 v21, v40, v40, v21 neg_lo:[0,1,0] neg_hi:[0,1,0]\n"
    "v_pk_max_u16 v21, v21, v21 op_sel:[0,1] op_sel_hi:[1,0]\n"
    "s_nop 1\n"
    "v_max_u32_dpp v21, v21, v21 quad_perm:[1,0,3,2] row_mask:0xf bank_mask:0xf\n"
    "s_nop 1\n"
    "v_max_u32_dpp v21, v21, v21 quad_perm:[2,3,0,1] row_mask:0xf bank_mask:0xf\n"
    "s_nop 1\n"
    "v_max_u32_dpp v21, v21, v21 row_half_mirror row_mask:0xf bank_mask:0xf\n"
    "s_nop 1\n"
    "v_max_u32_dpp v21, v21, v21 row_mirror row_mask:0xf bank_mask:0xf\n"
    "s_nop 1\n"
    "v_max_u32_dpp v21, v21, v21 row_bcast:15 row_mask:0xa bank_mask:0xf\n"
    "s_nop 1\n"
    "v_max_u32_dpp v21, v21, v21 row_bcast:31 row_mask:0xc bank_mask:0xf\n"
    "s_nop 1\n"
    "v_readlane_b32 s47, v21, 63\n"
    "s_and_b32 s50, s47, 0x7f80\n"
    "s_lshr_b32 s47, s50, 3\n"
    "s_sub_u32 s50, s50, 2816\n"
    "s_mul_i32 s51, s50, -8\n"
    "v_pk_maximum3_f16 v21, v28, v28, s50 op_sel_hi:[1,1,0] neg_lo:[0,1,0] neg_hi:[0,1,0]\n"
    "v_pk_mad_u16 v21, v21, 8, s51 op_sel_hi:[1,0,0]\n"
    "v_bfi_b32 v28, s57, v21, v28\n"
    "v_pk_maximum3_f16 v12, v29, v29, s50 op_sel_hi:[1,1,0] neg_lo:[0,1,0] neg_hi:[0,1,0]\n"
    "v_pk_mad_u16 v12, v12, 8, s51 op_sel_hi:[1,0,0]\n"
    "v_bfi_b32 v29, s57, v12, v29\n"
    "v_pk_maximum3_f16 v21, v30, v30, s50 op_sel_hi:[1,1,0] neg_lo:[0,1,0] neg_hi:[0,1,0]\n"
    "v_pk_mad_u16 v21, v21, 8, s51 op_sel_hi:[1,0,0]\n"
    "v_bfi_b32 v30, s57, v21, v30\n"
    "v_pk_maximum3_f16 v12, v31, v31, s50 op_sel_hi:[1,1,0] neg_lo:[0,1,0] neg_hi:[0,1,0]\n"
    "v_pk_mad_u16 v12, v12, 8, s51 op_sel_hi:[1,0,0]\n"
    "v_bfi_b32 v31, s57, v12, v31\n"
    "v_pk_maximum3_f16 v21, v32, v32, s50 op_sel_hi:[1,1,0] neg_lo:[0,1,0] neg_hi:[0,1,0]\n"
    "v_pk_mad_u16 v21, v21, 8, s51 op_sel_hi:[1,0,0]\n"
    "v_bfi_b32 v32, s57, v21, v32\n"
    "v_pk_maximum3_f16 v12, v33, v33, s50 op_sel_hi:[1,1,0] neg_lo:[0,1,0] neg_hi:[0,1,0]\n"
    "v_pk_mad_u16 v12, v12, 8, s51 op_sel_hi:[1,0,0]\n"
    "v_bfi_b32 v33, s57, v12, v33\n"
    "v_pk_maximum3_f16 v21, v34, v34, s50 op_sel_hi:[1,1,0] neg_lo:[0,1,0] neg_hi:[0,1,0]\n"
    "v_pk_mad_u16 v21, v21, 8, s51 op_sel_hi:[1,0,0]\n"
    "v_bfi_b32 v34, s57, v21, v34\n"
    "v_pk_maximum3_f16 v12, v35, v35, s50 op_sel_hi:[1,1,0] neg_lo:[0,1,0] neg_hi:[0,1,0]\n"
    "v_pk_mad_u16 v12, v12, 8, s51 op_sel_hi:[1,0,0]\n"
    "v_bfi_b32 v35, s57, v12, v35\n"
    "v_pk_maximum3_f16 v21, v36, v36, s50 op_sel_hi:[1,1,0] neg_lo:[0,1,0] neg_hi:[0,1,0]\n"
    "v_pk_mad_u16 v21, v21, 8, s51 op_sel_hi:[1,0,0]\n"
    "v_bfi_b32 v36, s57, v21, v36\n"
    "v_pk_maximum3_f16 v12, v37, v37, s50 op_sel_hi:[1,1,0] neg_lo:[0,1,0] neg_hi:[0,1,0]\n"
    "v_pk_mad_u16 v12, v12, 8, s51 op_sel_hi:[1,0,0]\n"
    "v_bfi_b32 v37, s57, v12, v37\n"
    "v_pk_maximum3_f16 v21, v38, v38, s50 op_sel_hi:[1,1,0] neg_lo:[0,1,0] neg_hi:[0,1,0]\n"
    "v_pk_mad_u16 v21, v21, 8, s51 op_sel_hi:[1,0,0]\n"
    "v_bfi_b32 v38, s57, v21, v38\n"
    "v_pk_maximum3_f16 v12, v39, v39, s50 op_sel_hi:[1,1,0] neg_lo:[0,1,0] neg_hi:[0,1,0]\n"
    "v_pk_mad_u16 v12, v12, 8, s51 op_sel_hi:[1,0,0]\n"
    "v_bfi_b32 v39, s57, v12, v39\n"
    "v_pk_maximum3_f16 v21, v40, v40, s50 op_sel_hi:[1,1,0] neg_lo:[0,1,0] neg_hi:[0,1,0]\n"
    "v_pk_mad_u16 v21, v21, 8, s51 op_sel_hi:[1,0,0]\n"
    "v_bfi_b32 v40, s57, v21, v40\n"
    "s_sub_u32 s43, 4112, s47\n"
    "s_lshl_b32 s43, s43, 19\n"
    "v_cvt_pkrtz_f16_f32 v12, s43, s43\n"
    "v_lshl_or_b32 v21, v12, 16, s47\n"
    "ds_write_b128 v15, v[28:31] offset:0\n"
    "ds_write_b128 v15, v[32:35] offset:144\n"
    "ds_write_b128 v15, v[36:39] offset:288\n"
    "ds_write_b32 v16, v40 offset:3456\n"
    "ds_write_b32 v16, v12 offset:3744\n"
    "s_mov_b64 exec, 1\n"
    "ds_or_b32 v25, v21 offset:532\n"
    "s_mov_b64 exec, -1\n"
    "s_cmp_lt_u32 s22, 2\n"
    "s_cselect_b32 s40, 16, 0\n"
    "v_add_u32 v25, s40, v25\n"
    "s_add_u32 s23, s23, 4\n"
    "v_mov_b32 v24, s23\n"
    "s_mov_b32 s36, 0\n"
    "v_mov_b32 v69, v43\n"
    "v_mov_b32 v68, v42\n"
    "s_cmp_lg_u32 s22, 0\n"
    "s_cbranch_scc1 .Lxd_on56\n"
    "v_readfirstlane_b32 s40, v24\n"
    "s_or_b32 s40, s40, s44\n"
    "s_lshl1_add_u32 s41, s40, s54\n"
    "s_cmpk_lt_u32 s40, 256\n"
    "s_cselect_b32 s41, s41, 2\n"
    "v_mov_b32 v12, s41\n"
    "s_mul_i32 s41, s41, 52\n"
    "s_add_u32 s38, s4, s41\n"
    "s_addc_u32 s39, s5, 0\n"
    "ds_write_b32 v25, v12 offset:20\n"
    "v_xor_b32 v15, 0x4000, v15\n"
    "v_xor_b32 v16, 0x4000, v16\n"
    "global_load_dwordx3 v[36:38], v17, s[38:39] offset:-104\n"
    "global_load_dword v41, v18, s[38:39] offset:-8\n"
    ".Lxd_on56:\n"
    "s_cmp_lg_u32 s20, 7\n"
    "s_cbranch_scc1 .Lxd_on58\n"
    "s_waitcnt vmcnt(0)\n"
    "v_readlane_b32 s25, v26, 9\n"
    "v_readlane_b32 s40, v26, 8\n"
    "v_readlane_b32 s41, v27, 8\n"
    "s_mul_i32 s42, s25, 0x9e3779b1\n"
    "s_lshr_b32 s42, s42, 24\n"
    "s_xor_b32 s42, s42, s40\n"
    "s_and_b32 s42, s42, 0xff\n"
    "s_xor_b32 s41, s41, s25\n"
    "s_xor_b32 s41, s41, 0xa5c30f96\n"
    "s_or_b32 s42, s41, s42\n"
    "s_lshr_b32 s41, s40, 8\n"
    "s_mul_i32 s40, s41, 0x9e3779b1\n"
    "s_xor_b32 s41, s41, 0xa5c30f96\n"
    "s_lshr_b32 s40, s40, 24\n"
    "v_xor_b32 v13, s40, v26\n"
    "v_xor_b32 v22, s41, v27\n"
    "v_lshl_or_b32 v13, v13, 24, v22\n"
    "v_cmp_ne_u32 s[66:67], 0, v13\n"
    "s_and_b32 s66, s66, 0xff\n"
    "s_or_b32 s42, s42, s66\n"
    "s_cmp_eq_u32 s20, 7\n"
    "s_cselect_b32 s42, s42, 0\n"
    "s_cmp_lg_u32 s42, 0\n"
    "s_cbranch_scc1 .Lxd_pv57\n"
    ".Lxd_pv57r:\n"
    "v_lshrrev_b32 v22, 8, v26\n"
    "v_mov_b32 v26, s40\n"
    "v_mov_b32 v27, s41\n"
    "v_add_u32_dpp v22, v22, v22 row_shl:4 row_mask:0xf bank_mask:0xf\n"
    "v_add_u32_e64 v43, s25, 1\n"
    "v_lshlrev_b32 v8, 2, v1\n"
    "v_max_u32_dpp v13, v22, v22 quad_perm:[1,0,3,2] row_mask:0xf bank_mask:0xf\n"
    "v_add_u32 v8, 154112, v8\n"
    "s_nop 0\n"
    "v_max_u32_dpp v13, v13, v13 quad_perm:[2,3,0,1] row_mask:0xf bank_mask:0xf\n"
    "v_sub_u32 v22, v22, v13\n"
    "v_subrev_u32 v13, 818, v13\n"
    "v_ldexp_f32 v22, 1.0, v22\n"
    "s_mov_b64 exec, 1\n"
    "ds_write_b64 v8, v[26:27]\n"
    "s_mov_b64 exec, 15\n"
    "ds_write2_b32 v8, v22, v13 offset0:4 offset1:8\n"
    "s_mov_b64 exec, 0x200\n"
    "global_store_dword v11, v43, s[16:17] sc1\n"
    "s_mov_b64 exec, -1\n"
    ".Lxd_on58:\n"
    "s_waitcnt lgkmcnt(0)\n"
    "s_barrier\n"
    "s_mul_i32 s42, s20, 5376\n"
    "s_add_u32 s40, s42, 1048576\n"
    "s_add_u32 s38, s16, s40\n"
    "s_addc_u32 s39, s17, 0\n"
    "s_add_u32 m0, s42, 98304\n"
    "s_nop 0\n"
    "global_load_lds_dwordx4 v68, s[38:39] offset:0 sc1\n"
    "global_load_lds_dwordx4 v68, s[38:39] offset:1024 sc1\n"
    "global_load_lds_dwordx4 v68, s[38:39] offset:2048 sc1\n"
    "global_load_lds_dwordx4 v68, s[38:39] offset:3072 sc1\n"
    "s_add_u32 m0, s42, 102400\n"
    "s_nop 0\n"
    "global_load_lds_dwordx4 v69, s[38:39] offset:0 sc1\n"
    "global_load_lds_dwordx4 v69, s[38:39] offset:256 sc1\n"
    "s_getpc_b64 s[10:11]\n"
    ".Lxd_en59:\n"
    "s_mov_b32 s40, .Lxd_ph1-.Lxd_en59\n"
    "s_cmp_eq_u32 s22, 1\n"
    "s_cselect_b32 s40, .Lxd_ph0-.Lxd_en59, s40\n"
    "s_cmp_eq_u32 s22, 2\n"
    "s_cselect_b32 s40, .Lxd_pl3-.Lxd_en59, s40\n"
    "s_cmp_eq_u32 s22, 3\n"
    "s_cselect_b32 s40, .Lxd_pl2-.Lxd_en59, s40\n"
    "s_ashr_i32 s41, s40, 31\n"
    "s_add_u32 s10, s10, s40\n"
    "s_addc_u32 s11, s11, s41\n"
    "v_mov_b32 v13, 154112\n"
    "ds_read_b64 v[26:27], v13\n"
    "v_add_u32 v42, 98304, v42\n"
    "v_mov_b32 v13, s58\n"
    "s_sub_u32 s40, 1, s22\n"
    "s_and_b32 s40, s40, 3\n"
    "v_lshl_add_u32 v7, s40, 12, v3\n"
    "v_lshl_add_u32 v8, s40, 12, v4\n"
    "v_lshl_add_u32 v9, s40, 12, v5\n"
    "v_lshl_add_u32 v10, s40, 12, v6\n"
    "v_bfe_u32 v21, v1, 4, 1\n"
    "v_lshlrev_b32 v21, 2, v21\n"
    "v_add_u32 v21, 154112, v21\n"
    "ds_read_b32 v0, v21 offset:16\n"
    "ds_read_b32 v23, v21 offset:24\n"
    "ds_read_b32 v22, v13 offset:512\n"
    "ds_read_b64_tr_b16 v[44:45], v9 offset:0\n"
    "ds_read_b64_tr_b16 v[46:47], v10 offset:0\n"
    "ds_read_b64_tr_b16 v[48:49], v7 offset:0\n"
    "ds_read_b64_tr_b16 v[50:51], v8 offset:576\n"
    "ds_read_b64_tr_b16 v[52:53], v8 offset:1152\n"
    "ds_read_b64_tr_b16 v[54:55], v8 offset:1728\n"
    "ds_read_b64_tr_b16 v[56:57], v8 offset:2304\n"
    "ds_read_b64_tr_b16 v[58:59], v8 offset:2880\n"
    "v_mov_b64 v[88:89], 0\n"
    "v_mov_b64 v[90:91], 0\n"
    "s_waitcnt vmcnt(0)\n"
    "s_barrier\n"
    "ds_read_b128 v[92:95], v42 offset:0\n"
    "ds_read_b128 v[96:99], v42 offset:1024\n"
    "ds_read_b128 v[100:103], v42 offset:2048\n"
    "ds_read_b128 v[104:107], v42 offset:3072\n"
    "ds_read_b128 v[108:111], v42 offset:4096\n"
    "ds_read_b128 v[112:115], v42 offset:5120\n"
    "ds_read_b128 v[116:119], v42 offset:6144\n"
    "ds_read_b128 v[120:123], v42 offset:7168\n"
    "ds_read_b128 v[124:127], v42 offset:8192\n"
    "ds_read_b128 v[128:131], v42 offset:9216\n"
    "ds_read_b128 v[132:135], v42 offset:10240\n"
    "ds_read_b128 v[136:139], v42 offset:11264\n"
    "ds_read_b128 v[140:143], v42 offset:12288\n"
    "ds_read_b128 v[144:147], v42 offset:13312\n"
    "ds_read_b128 v[148:151], v42 offset:14336\n"
    "ds_read_b128 v[152:155], v42 offset:15360\n"
    "ds_read_b128 v[156:159], v42 offset:16384\n"
    "ds_read_b128 v[160:163], v42 offset:17408\n"
    "ds_read_b128 v[164:167], v42 offset:18432\n"
    "ds_read_b128 v[168:171], v42 offset:19456\n"
    "ds_read_b128 v[172:175], v42 offset:20480\n"
    "ds_read_b128 v[176:179], v42 offset:21504\n"
    "ds_read_b128 v[180:183], v42 offset:22528\n"
    "ds_read_b128 v[184:187], v42 offset:23552\n"
    "ds_read_b128 v[188:191], v42 offset:24576\n"
    "ds_read_b128 v[192:195], v42 offset:25600\n"
    "ds_read_b128 v[196:199], v42 offset:26624\n"
    "ds_read_b128 v[200:203], v42 offset:27648\n"
    "ds_read_b128 v[204:207], v42 offset:28672\n"
    "ds_read_b128 v[208:211], v42 offset:29696\n"
    "ds_read_b128 v[212:215], v42 offset:30720\n"
    "ds_read_b128 v[216:219], v42 offset:31744\n"
    "ds_read_b128 v[220:223], v42 offset:32768\n"
    "ds_read_b128 v[224:227], v42 offset:33792\n"
    "ds_read_b128 v[228:231], v42 offset:34816\n"
    "ds_read_b128 v[232:235], v42 offset:35840\n"
    "ds_read_b128 v[236:239], v42 offset:36864\n"
    "ds_read_b128 v[240:243], v42 offset:37888\n"
    "ds_read_b128 v[244:247], v42 offset:38912\n"
    "ds_read_b128 v[248:251], v42 offset:39936\n"
    "ds_read_b128 v[252:255], v42 offset:40960\n"
    "s_waitcnt lgkmcnt(15)\n"
    "v_readfirstlane_b32 s45, v22\n"
    "v_readfirstlane_b32 s28, v26\n"
    "v_readfirstlane_b32 s29, v27\n"
    "s_cmp_lt_u32 s2, 8\n"
    "s_cbranch_scc1 .Lxd_hp61\n"
    "s_mov_b32 m0, 159744\n"
    "s_mov_b32 s46, 2\n"
    "v_lshlrev_b32 v26, 7, v63\n"
    "v_lshlrev_b32 v27, 7, v64\n"
    ".Lxd_hp61:\n"
    "v_pk_mul_f16 v64, v44, v46\n"
    "v_pk_mul_f16 v65, v45, v47\n"
    "v_pk_mul_f16 v66, v48, v48 op_sel_hi:[0,1]\n"
    "v_pk_mul_f16 v67, v48, v49 op_sel_hi:[0,1]\n"
    "v_pk_mul_f16 v60, v44, s45 op_sel:[0,1]\n"
    "v_pk_mul_f16 v61, v45, s45 op_sel:[0,1]\n"
    "v_pk_mul_f16 v62, v46, s45 op_sel:[0,1]\n"
    "v_pk_mul_f16 v63, v47, s45 op_sel:[0,1]\n"
    "s_waitcnt lgkmcnt(0)\n"
    "s_branch .Lxd_enter\n"
    ".Lxd_slow:\n"
    "s_cmp_eq_u32 s45, -1\n"
    "s_cbranch_scc1 .Lxd_sl7\n"
    "s_mov_b32 s59, 0x100000\n"
    "s_and_b32 s40, s45, 0xff0\n"
    "s_cbranch_scc0 .Lxd_sl1\n"
    ".Lxd_sl2:\n"
    "s_cmp_ge_i32 s45, -16\n"
    "s_cbranch_scc1 .Lxd_sl3\n"
    "s_bitset0_b32 s45, 31\n"
    ".Lxd_sl6:\n"
    "s_cmp_lg_u32 s56, 0\n"
    "s_cbranch_scc1 .Lxd_sl9\n"
    "v_readfirstlane_b32 s40, v13\n"
    "s_lshl4_add_u32 s41, s22, s58\n"
    "s_mov_b32 s56, 2\n"
    "s_cmp_lt_u32 s40, s41\n"
    "s_cbranch_scc0 .Lxd_gran\n"
    ".Lxd_sl9:\n"
    "s_setpc_b64 s[60:61]\n"
    ".Lxd_sl1:\n"
    "s_sleep 1\n"
    "ds_read_b32 v22, v13 offset:512\n"
    "s_waitcnt lgkmcnt(0)\n"
    "v_readfirstlane_b32 s45, v22\n"
    "s_sub_u32 s59, s59, 1\n"
    "s_cbranch_scc1 .Lxd_sl5\n"
    "s_and_b32 s40, s45, 0xff0\n"
    "s_cbranch_scc0 .Lxd_sl1\n"
    "s_cmp_gt_i32 s45, 0\n"
    "s_cbranch_scc0 .Lxd_sl2\n"
    "s_setpc_b64 s[60:61]\n"
    ".Lxd_sl3:\n"
    "v_lshl_add_u32 v13, v1, 2, v13\n"
    "s_mov_b64 exec, 7\n"
    ".Lxd_sl4:\n"
    "ds_read_b32 v22, v13 offset:4\n"
    "s_waitcnt lgkmcnt(0)\n"
    "v_cmp_eq_u32 s[40:41], 0, v22\n"
    "v_cmp_ne_u32 s[42:43], -1, v22\n"
    "s_sub_u32 s59, s59, 1\n"
    "s_cbranch_scc1 .Lxd_sl5\n"
    "s_cmp_lg_u32 s40, 0\n"
    "s_cbranch_scc1 .Lxd_sl4\n"
    "s_cmp_eq_u32 s42, 0\n"
    "s_cbranch_scc1 .Lxd_sl5\n"
    "s_mov_b64 exec, -1\n"
    "s_mov_b32 s45, 1\n"
    "s_mov_b32 s56, 0\n"
    "s_setpc_b64 s[60:61]\n"
    ".Lxd_sl5:\n"
    "s_mov_b64 exec, -1\n"
    "s_mov_b32 s45, -1\n"
    ".Lxd_sl7:\n"
    "s_cmp_lg_u32 s56, 1\n"
    "s_cbranch_scc1 .Lxd_sl6\n"
    "s_branch .Lxd_dsum\n"
    ".Lxd_gran:\n"
    "s_mov_b32 s46, 0\n"
    "s_lshl4_add_u32 s40, s22, s58\n"
    "v_lshrrev_b32 v13, 4, v1\n"
    "v_lshl_add_u32 v13, v13, 2, s40\n"
    "s_waitcnt vmcnt(0)\n"
    "ds_read2_b32 v[26:27], v13 offset1:16\n"
    "v_and_b32 v13, 15, v1\n"
    "v_lshlrev_b32 v13, 15, v13\n"
    "s_mov_b32 s42, 0x1fff1fff\n"
    "s_mov_b32 s43, s42\n"
    "s_add_u32 s40, s16, 65528\n"
    "s_addc_u32 s41, s17, 0\n"
    "v_mov_b32 v42, 0\n"
    "s_mov_b32 s56, 2\n"
    "s_waitcnt lgkmcnt(0)\n"
    "v_cmp_lt_i32 s[30:31], 0, v26\n"
    "v_cmp_lt_i32 s[64:65], 0, v27\n"
    "v_lshl_add_u32 v43, v26, 2, v13\n"
    "v_lshl_add_u32 v27, v27, 2, v13\n"
    "v_mov_b32 v26, 0\n"
    "s_and_b64 s[30:31], s[30:31], s[42:43]\n"
    "s_cbranch_scc0 .Lxd_gr30\n"
    "s_mov_b64 exec, s[30:31]\n"
    "global_load_dwordx2 v[42:43], v43, s[40:41] sc1\n"
    "s_mov_b64 exec, -1\n"
    "s_mov_b32 s56, 1\n"
    ".Lxd_gr30:\n"
    "s_and_b64 s[64:65], s[64:65], s[42:43]\n"
    "s_cbranch_scc0 .Lxd_gr64\n"
    "s_mov_b64 exec, s[64:65]\n"
    "global_load_dwordx2 v[26:27], v27, s[40:41] sc1\n"
    "s_mov_b64 exec, -1\n"
    "s_mov_b32 s56, 1\n"
    ".Lxd_gr64:\n"
    "s_setpc_b64 s[60:61]\n"
    ".Lxd_dsum:\n"
    "s_lshl4_add_u32 s42, s22, s58\n"
    "s_waitcnt vmcnt(0)\n"
    "s_cmp_eq_u64 s[30:31], 0\n"
    "s_cbranch_scc1 .Lxd_ds30\n"
    "v_xor_b32 v13, s28, v42\n"
    "v_xor_b32 v22, s29, v43\n"
    "v_lshl_or_b32 v13, v13, 24, v22\n"
    "v_and_b32 v22, 0xffffff00, v42\n"
    "v_cmp_ne_u32 s[40:41], 0, v13\n"
    "v_lshrrev_b32 v13, 4, v1\n"
    "v_add_f32_dpp v22, v22, v22 quad_perm:[1,0,3,2] row_mask:0xf bank_mask:0xf\n"
    "s_and_b64 s[40:41], s[40:41], s[30:31]\n"
    "s_cbranch_scc1 .Lxd_ds9\n"
    "v_add_f32_dpp v22, v22, v22 quad_perm:[2,3,0,1] row_mask:0xf bank_mask:0xf\n"
    "v_lshl_add_u32 v13, v13, 2, s42\n"
    "s_nop 0\n"
    "v_add_f32_dpp v22, v22, v22 row_half_mirror row_mask:0xf bank_mask:0xf\n"
    "s_nop 1\n"
    "v_add_f32_dpp v22, v22, v22 row_mirror row_mask:0xf bank_mask:0xf\n"
    "ds_write_b32 v13, v22 offset:1024\n"
    ".Lxd_ds30:\n"
    "s_cmp_eq_u64 s[64:65], 0\n"
    "s_cbranch_scc1 .Lxd_ds64\n"
    "v_xor_b32 v13, s28, v26\n"
    "v_xor_b32 v22, s29, v27\n"
    "v_lshl_or_b32 v13, v13, 24, v22\n"
    "v_and_b32 v22, 0xffffff00, v26\n"
    "v_cmp_ne_u32 s[40:41], 0, v13\n"
    "v_lshrrev_b32 v13, 4, v1\n"
    "v_add_f32_dpp v22, v22, v22 quad_perm:[1,0,3,2] row_mask:0xf bank_mask:0xf\n"
    "s_and_b64 s[40:41], s[40:41], s[64:65]\n"
    "s_cbranch_scc1 .Lxd_ds9\n"
    "v_add_f32_dpp v22, v22, v22 quad_perm:[2,3,0,1] row_mask:0xf bank_mask:0xf\n"
    "v_lshl_add_u32 v13, v13, 2, s42\n"
    "s_nop 0\n"
    "v_add_f32_dpp v22, v22, v22 row_half_mirror row_mask:0xf bank_mask:0xf\n"
    "s_nop 1\n"
    "v_add_f32_dpp v22, v22, v22 row_mirror row_mask:0xf bank_mask:0xf\n"
    "ds_write_b32 v13, v22 offset:1088\n"
    ".Lxd_ds64:\n"
    "s_mov_b32 s56, 2\n"
    ".Lxd_ds9:\n"
    "s_setpc_b64 s[60:61]\n"
    ".Lxd_mark:\n"
    "v_lshl_add_u32 v13, v1, 2, v13\n"
    "s_add_u32 s40, s58, 248\n"
    "v_mov_b32 v24, -1\n"
    "v_cmp_gt_u32 s[42:43], s40, v13\n"
    "s_mov_b32 s62, 0x88888888\n"
    "s_mov_b32 s63, s62\n"
    "s_and_b64 exec, s[42:43], s[62:63]\n"
    "ds_write_b32 v13, v24 offset:8\n"
    "s_mov_b64 exec, s[42:43]\n"
    "v_cndmask_b32 v24, 0, -16, s[62:63]\n"
    "s_brev_b32 s40, 1\n"
    "v_writelane_b32 v24, s40, 2\n"
    "s_lshl_b32 s40, 1, s22\n"
    "v_or_b32 v24, s40, v24\n"
    "s_mov_b32 s36, 1\n"
    "ds_or_b32 v13, v24 offset:520\n"
    "s_mov_b64 exec, -1\n"
    "s_setpc_b64 s[60:61]\n"
    ".Lxd_tf15:\n"
    "v_mov_b32 v12, -1\n"
    "v_mov_b32 v13, v25\n"
    "s_call_b64 s[60:61], .Lxd_mark\n"
    "s_branch .Lxd_tf15r\n"
    ".Lxd_ck18:\n"
    "v_add_u32 v13, 4, v25\n"
    "s_call_b64 s[60:61], .Lxd_slow\n"
    "s_branch .Lxd_ck18r\n"
    ".Lxd_ck21:\n"
    "v_add_u32 v13, 8, v25\n"
    "s_call_b64 s[60:61], .Lxd_slow\n"
    "s_branch .Lxd_ck21r\n"
    ".Lxd_ht24:\n"
    "global_load_lds_dword v26, s[8:9]\n"
    "v_mov_b32 v26, v27\n"
    "v_mov_b32 v27, v42\n"
    "s_sub_u32 s46, s46, 1\n"
    "s_branch .Lxd_ht24r\n"
    ".Lxd_ck25:\n"
    "v_add_u32 v13, 12, v25\n"
    "s_call_b64 s[60:61], .Lxd_slow\n"
    "s_branch .Lxd_ck25r\n"
    ".Lxd_ck28:\n"
    "v_add_u32 v13, 16, v25\n"
    "s_call_b64 s[60:61], .Lxd_slow\n"
    "s_branch .Lxd_ck28r\n"
    ".Lxd_dr29:\n"
    "v_add_u32 v13, 16, v25\n"
    "s_call_b64 s[60:61], .Lxd_mark\n"
    "s_branch .Lxd_dr29r\n"
    ".Lxd_ck32:\n"
    "v_add_u32 v13, 4, v25\n"
    "s_call_b64 s[60:61], .Lxd_slow\n"
    "s_branch .Lxd_ck32r\n"
    ".Lxd_ck35:\n"
    "v_add_u32 v13, 8, v25\n"
    "s_call_b64 s[60:61], .Lxd_slow\n"
    "s_branch .Lxd_ck35r\n"
    ".Lxd_ht38:\n"
    "global_load_lds_dword v26, s[8:9]\n"
    "v_mov_b32 v26, v27\n"
    "v_mov_b32 v27, v42\n"
    "s_sub_u32 s46, s46, 1\n"
    "s_branch .Lxd_ht38r\n"
    ".Lxd_ck39:\n"
    "v_add_u32 v13, 12, v25\n"
    "s_call_b64 s[60:61], .Lxd_slow\n"
    "s_branch .Lxd_ck39r\n"
    ".Lxd_dr43:\n"
    "v_add_u32 v13, 16, v25\n"
    "s_call_b64 s[60:61], .Lxd_mark\n"
    "s_branch .Lxd_dr43r\n"
    ".Lxd_on42:\n"
    "s_and_b32 s40, s45, 15\n"
    "s_cbranch_scc1 .Lxd_dr43\n"
    "v_mov_b32 v24, 1\n"
    "s_mov_b64 exec, 1\n"
    "global_atomic_add v24, v17, v24, s[52:53] sc0 sc1\n"
    "s_mov_b64 exec, -1\n"
    ".Lxd_dr43r:\n"
    "s_branch .Lxd_on42r\n"
    ".Lxd_ck44:\n"
    "v_add_u32 v13, 16, v25\n"
    "s_call_b64 s[60:61], .Lxd_slow\n"
    "s_branch .Lxd_ck44r\n"
    ".Lxd_f2:\n"
    "s_mov_b32 s59, 0x20000\n"
    ".Lxd_f4:\n"
    "s_call_b64 s[60:61], .Lxd_dsum\n"
    "s_cmp_eq_u32 s56, 2\n"
    "s_cbranch_scc1 .Lxd_f3\n"
    "s_sleep 2\n"
    "s_call_b64 s[60:61], .Lxd_gran\n"
    "s_sub_u32 s59, s59, 1\n"
    "s_cbranch_scc0 .Lxd_f4\n"
    "s_waitcnt vmcnt(0)\n"
    "s_branch .Lxd_f3\n"
    ".Lxd_on2:\n"
    "s_mov_b64 exec, 0x3ff\n"
    "global_load_dwordx2 v[26:27], v11, s[16:17] sc1\n"
    "s_mov_b64 exec, -1\n"
    "s_branch .Lxd_on2r\n"
    ".Lxd_pv3:\n"
    "s_mov_b32 s59, 0x20000\n"
    ".Lxd_pv3l:\n"
    "s_sleep 2\n"
    "s_mov_b64 exec, 0x3ff\n"
    "global_load_dwordx2 v[26:27], v11, s[16:17] sc1\n"
    "s_mov_b64 exec, -1\n"
    "s_waitcnt vmcnt(0)\n"
    "v_readlane_b32 s25, v26, 9\n"
    "v_readlane_b32 s40, v26, 8\n"
    "v_readlane_b32 s41, v27, 8\n"
    "s_mul_i32 s42, s25, 0x9e3779b1\n"
    "s_lshr_b32 s42, s42, 24\n"
    "s_xor_b32 s42, s42, s40\n"
    "s_and_b32 s42, s42, 0xff\n"
    "s_xor_b32 s41, s41, s25\n"
    "s_xor_b32 s41, s41, 0xa5c30f96\n"
    "s_or_b32 s42, s41, s42\n"
    "s_lshr_b32 s41, s40, 8\n"
    "s_mul_i32 s40, s41, 0x9e3779b1\n"
    "s_xor_b32 s41, s41, 0xa5c30f96\n"
    "s_lshr_b32 s40, s40, 24\n"
    "v_xor_b32 v13, s40, v26\n"
    "v_xor_b32 v22, s41, v27\n"
    "v_lshl_or_b32 v13, v13, 24, v22\n"
    "v_cmp_ne_u32 s[66:67], 0, v13\n"
    "s_and_b32 s66, s66, 0xff\n"
    "s_or_b32 s42, s42, s66\n"
    "s_cmp_eq_u32 s42, 0\n"
    "s_cbranch_scc1 .Lxd_pv3r\n"
    "s_sub_u32 s59, s59, 1\n"
    "s_cbranch_scc0 .Lxd_pv3l\n"
    "s_branch .Lxd_pv3r\n"
    ".Lxd_on6:\n"
    "v_mov_b32 v24, 1\n"
    "s_mov_b64 exec, 1\n"
    "global_atomic_add v24, v17, v24, s[52:53] sc0 sc1\n"
    "s_mov_b64 exec, -1\n"
    "s_branch .Lxd_on6r\n"
    ".Lxd_ck7:\n"
    "v_mov_b32 v13, s58\n"
    "s_call_b64 s[60:61], .Lxd_slow\n"
    "s_branch .Lxd_ck7r\n"
    ".Lxd_tf8:\n"
    "v_mov_b32 v12, -1\n"
    "v_mov_b32 v13, v25\n"
    "s_call_b64 s[60:61], .Lxd_mark\n"
    "s_branch .Lxd_tf8r\n"
    ".Lxd_on9:\n"
    "s_waitcnt vmcnt(0)\n"
    "v_readfirstlane_b32 s40, v24\n"
    "s_or_b32 s40, s40, s44\n"
    "s_lshl1_add_u32 s41, s40, s54\n"
    "s_cmpk_lt_u32 s40, 256\n"
    "s_cselect_b32 s41, s41, 2\n"
    "v_mov_b32 v12, s41\n"
    "s_cbranch_scc0 .Lxd_tf8\n"
    ".Lxd_tf8r:\n"
    "s_mul_i32 s41, s41, 52\n"
    "s_add_u32 s38, s4, s41\n"
    "s_addc_u32 s39, s5, 0\n"
    "ds_write_b32 v25, v12 offset:20\n"
    "global_load_dwordx3 v[36:38], v17, s[38:39] offset:-104\n"
    "global_load_dword v41, v18, s[38:39] offset:-8\n"
    "v_mov_b32 v13, s58\n"
    "s_branch .Lxd_on9r\n"
    ".Lxd_on10:\n"
    "v_mov_b32 v24, 1\n"
    "s_mov_b64 exec, 1\n"
    "global_atomic_add v24, v17, v24, s[52:53] sc0 sc1\n"
    "s_mov_b64 exec, -1\n"
    "s_branch .Lxd_on10r\n"
    ".Lxd_on11:\n"
    "s_waitcnt vmcnt(0)\n"
    "v_lshl_or_b32 v36, v36, 7, v19\n"
    "v_lshl_or_b32 v37, v37, 7, v19\n"
    "v_lshl_or_b32 v38, v38, 7, v19\n"
    "v_lshl_or_b32 v41, v41, 7, v20\n"
    "global_load_dwordx4 v[28:31], v36, s[8:9]\n"
    "global_load_dwordx4 v[32:35], v37, s[8:9]\n"
    "global_load_dword v40, v41, s[8:9]\n"
    "global_load_dwordx4 v[36:39], v38, s[8:9]\n"
    "s_branch .Lxd_on11r\n"
    ".Lxd_tf12:\n"
    "v_mov_b32 v12, -1\n"
    "v_mov_b32 v13, v25\n"
    "s_call_b64 s[60:61], .Lxd_mark\n"
    "s_branch .Lxd_tf12r\n"
    ".Lxd_on13:\n"
    "s_waitcnt vmcnt(0)\n"
    "v_readfirstlane_b32 s40, v24\n"
    "s_or_b32 s40, s40, s44\n"
    "s_lshl1_add_u32 s41, s40, s54\n"
    "s_cmpk_lt_u32 s40, 256\n"
    "s_cselect_b32 s41, s41, 2\n"
    "v_mov_b32 v12, s41\n"
    "s_cbranch_scc0 .Lxd_tf12\n"
    ".Lxd_tf12r:\n"
    "s_mul_i32 s41, s41, 52\n"
    "s_add_u32 s38, s4, s41\n"
    "s_addc_u32 s39, s5, 0\n"
    "ds_write_b32 v25, v12 offset:20\n"
    "v_xor_b32 v15, 0x4000, v15\n"
    "v_xor_b32 v16, 0x4000, v16\n"
    "global_load_dwordx3 v[36:38], v17, s[38:39] offset:-104\n"
    "global_load_dword v41, v18, s[38:39] offset:-8\n"
    "s_branch .Lxd_on13r\n"
    ".Lxd_on14:\n"
    "v_mov_b32 v24, 1\n"
    "s_mov_b64 exec, 1\n"
    "global_atomic_add v24, v17, v24, s[52:53] sc0 sc1\n"
    "s_mov_b64 exec, -1\n"
    "s_branch .Lxd_on14r\n"
    ".Lxd_pv57:\n"
    "s_mov_b32 s59, 0x20000\n"
    ".Lxd_pv57l:\n"
    "s_sleep 2\n"
    "s_mov_b64 exec, 0x3ff\n"
    "global_load_dwordx2 v[26:27], v11, s[16:17] sc1\n"
    "s_mov_b64 exec, -1\n"
    "s_waitcnt vmcnt(0)\n"
    "v_readlane_b32 s25, v26, 9\n"
    "v_readlane_b32 s40, v26, 8\n"
    "v_readlane_b32 s41, v27, 8\n"
    "s_mul_i32 s42, s25, 0x9e3779b1\n"
    "s_lshr_b32 s42, s42, 24\n"
    "s_xor_b32 s42, s42, s40\n"
    "s_and_b32 s42, s42, 0xff\n"
    "s_xor_b32 s41, s41, s25\n"
    "s_xor_b32 s41, s41, 0xa5c30f96\n"
    "s_or_b32 s42, s41, s42\n"
    "s_lshr_b32 s41, s40, 8\n"
    "s_mul_i32 s40, s41, 0x9e3779b1\n"
    "s_xor_b32 s41, s41, 0xa5c30f96\n"
    "s_lshr_b32 s40, s40, 24\n"
    "v_xor_b32 v13, s40, v26\n"
    "v_xor_b32 v22, s41, v27\n"
    "v_lshl_or_b32 v13, v13, 24, v22\n"
    "v_cmp_ne_u32 s[66:67], 0, v13\n"
    "s_and_b32 s66, s66, 0xff\n"
    "s_or_b32 s42, s42, s66\n"
    "s_cmp_eq_u32 s42, 0\n"
    "s_cbranch_scc1 .Lxd_pv57r\n"
    "s_sub_u32 s59, s59, 1\n"
    "s_cbranch_scc0 .Lxd_pv57l\n"
    "s_branch .Lxd_pv57r\n"
    ".Lxd_end:\n"
    : "+{v0}"(tid) : "{s[0:1]}"(__builtin_amdgcn_kernarg_segment_ptr()), "{s2}"(blockIdx.x)
    : "memory", "vcc", "scc", "v1", "v2", "v3", "v4", "v5", "v6", "v7", "v8", "v9", "v10", "v11", "v12", "v13", "v14", "v15", "v16", "v17", "v18", "v19", "v20", "v21", "v22", "v23", "v24", "v25", "v26", "v27", "v28", "v29", "v30", "v31", "v32", "v33", "v34", "v35", "v36", "v37", "v38", "v39", "v40", "v41", "v42", "v43", "v44", "v45", "v46", "v47", "v48", "v49", "v50", "v51", "v52", "v53", "v54", "v55", "v56", "v57", "v58", "v59", "v60", "v61", "v62", "v63", "v64", "v65", "v66", "v67", "v68", "v69", "v70", "v71", "v72", "v73", "v74", "v75", "v76", "v77", "v78", "v79", "v80", "v81", "v82", "v83", "v84", "v85", "v86", "v87", "v88", "v89", "v90", "v91", "v92", "v93", "v94", "v95", "v96", "v97", "v98", "v99", "v100", "v101", "v102", "v103", "v104", "v105", "v106", "v107", "v108", "v109", "v110", "v111", "v112", "v113", "v114", "v115", "v116", "v117", "v118", "v119", "v120", "v121", "v122", "v123", "v124", "v125", "v126", "v127", "v128", "v129", "v130", "v131", "v132", "v133", "v134", "v135", "v136", "v137", "v138", "v139", "v140", "v141", "v142", "v143", "v144", "v145", "v146", "v147", "v148", "v149", "v150", "v151", "v152", "v153", "v154", "v155", "v156", "v157", "v158", "v159", "v160", "v161", "v162", "v163", "v164", "v165", "v166", "v167", "v168", "v169", "v170", "v171", "v172", "v173", "v174", "v175", "v176", "v177", "v178", "v179", "v180", "v181", "v182", "v183", "v184", "v185", "v186", "v187", "v188", "v189", "v190", "v191", "v192", "v193", "v194", "v195", "v196", "v197", "v198", "v199", "v200", "v201", "v202", "v203", "v204", "v205", "v206", "v207", "v208", "v209", "v210", "v211", "v212", "v213", "v214", "v215", "v216", "v217", "v218", "v219", "v220", "v221", "v222", "v223", "v224", "v225", "v226", "v227", "v228", "v229", "v230", "v231", "v232", "v233", "v234", "v235", "v236", "v237", "v238", "v239", "v240", "v241", "v242", "v243", "v244", "v245", "v246", "v247", "v248", "v249", "v250", "v251", "v252", "v253", "v254", "v255", "s3", "s4", "s5", "s6", "s7", "s8", "s9", "s10", "s11", "s12", "s13", "s14", "s15", "s16", "s17", "s18", "s19", "s20", "s21", "s22", "s23", "s24", "s25", "s26", "s27", "s28", "s29", "s30", "s31", "s34", "s35", "s36", "s37", "s38", "s39", "s40", "s41", "s42", "s43", "s44", "s45", "s46", "s47", "s48", "s49", "s50", "s51", "s52", "s53", "s54", "s55", "s56", "s57", "s58", "s59", "s60", "s61", "s62", "s63", "s64", "s65", "s66", "s67", "s68", "s69", "s70", "s71");
  asm volatile("" :: "v"((__attribute__((address_space(3))) char*)shm));
}

extern "C" void kernel_launch(void* const* d_in, const int* in_sizes, int n_in, void* d_out, int out_size,
                              void* d_ws, size_t ws_size, hipStream_t stream) {
  xd_kernel<<<256, 512, 0, stream>>>((const int*)d_in[0], (const u16*)d_in[4], (const u16*)d_in[1], (const u16*)d_in[5], (const u16*)d_in[2], (const u16*)d_in[3], (char*)d_ws,
                                     (const u16*)d_in[6], (const u16*)d_in[7], (u16*)d_out);
}
```

```cpp
#include <hip/hip_runtime.h>
typedef unsigned short u16;

__device__ const __attribute__((aligned(16), used, visibility("hidden"))) u16 xd_tab[5120] asm("xd_tab") = {924, 925, 928, 929, 926, 927, 930, 931, 934, 935, 938, 939, 932, 933, 936, 937, 940, 941, 944, 945, 942, 943, 946, 947, 948, 0, 949, 0, 0, 0, 0, 0, 2, 36, 138, 172, 0, 1, 24, 25, 338, 372, 474, 508, 34, 35, 57, 58, 546, 580, 682, 716, 68, 69, 90, 91, 816, 817, 850, 952, 102, 99, 123, 124, 680, 681, 684, 685, 20, 21, 22, 23, 714, 715, 717, 718, 54, 55, 56, 53, 748, 749, 750, 751, 88, 89, 86, 87, 782, 779, 783, 784, 122, 119, 120, 121, 152, 153, 154, 155, 284, 285, 286, 287, 186, 187, 188, 185, 318, 319, 320, 317, 220, 221, 218, 219, 352, 353, 350, 351, 254, 251, 252, 253, 386, 383, 384, 385, 416, 417, 418, 419, 548, 549, 550, 551, 450, 451, 452, 449, 582, 583, 584, 581, 484, 485, 482, 483, 616, 617, 614, 615, 518, 515, 516, 517, 650, 647, 648, 649, 544, 545, 552, 553, 16, 17, 18, 19, 578, 579, 585, 586, 50, 51, 52, 49, 612, 613, 618, 619, 84, 85, 82, 83, 646, 643, 651, 652, 118, 115, 116, 117, 148, 149, 150, 151, 280, 281, 282, 283, 182, 183, 184, 181, 314, 315, 316, 313, 216, 217, 214, 215, 348, 349, 346, 347, 250, 247, 248, 249, 382, 379, 380, 381, 412, 413, 414, 415, 408, 409, 420, 421, 446, 447, 448, 445, 442, 443, 453, 454, 480, 481, 478, 479, 476, 477, 486, 487, 514, 511, 512, 513, 510, 507, 519, 520, 12, 13, 14, 15, 144, 145, 146, 147, 46, 47, 48, 45, 178, 179, 180, 177, 80, 81, 78, 79, 212, 213, 210, 211, 114, 111, 112, 113, 246, 243, 244, 245, 276, 277, 278, 279, 272, 273, 288, 289, 310, 311, 312, 309, 306, 307, 321, 322, 344, 345, 342, 343, 340, 341, 354, 355, 378, 375, 376, 377, 374, 371, 387, 388, 8, 9, 10, 11, 140, 141, 142, 143, 42, 43, 44, 41, 174, 175, 176, 173, 76, 77, 74, 75, 208, 209, 206, 207, 110, 107, 108, 109, 242, 239, 240, 241, 136, 137, 156, 157, 4, 5, 6, 7, 170, 171, 189, 190, 38, 39, 40, 37, 204, 205, 222, 223, 72, 73, 70, 71, 238, 235, 255, 256, 106, 103, 104, 105, 0, 0, 0, 0, 0, 0, 0, 0, 0, 0, 0, 0, 0, 0, 0, 0, 0, 0, 0, 0, 0, 0, 0, 0, 0, 0, 0, 0, 0, 0, 0, 0, 0, 0, 0, 0, 0, 0, 0, 0, 0, 0, 0, 0, 0, 0, 0, 0, 0, 0, 0, 0, 0, 0, 0, 0, 0, 0, 0, 0, 0, 0, 0, 0, 0, 0, 0, 0, 0, 0, 0, 0, 0, 0, 0, 0, 0, 0, 0, 0, 0, 0, 0, 0, 0, 0, 0, 0, 0, 0, 0, 0, 0, 0, 0, 0, 0, 0, 0, 0, 0, 0, 0, 0, 0, 0, 0, 0, 0, 0, 0, 0, 0, 0, 0, 0, 0, 0, 0, 0, 0, 0, 0, 0, 0, 0, 0, 0, 924, 925, 926, 927, 934, 935, 932, 933, 940, 941, 942, 943, 948, 0, 0, 0, 2, 36, 0, 1, 338, 372, 34, 35, 546, 580, 68, 69, 816, 817, 102, 99, 928, 929, 930, 931, 938, 939, 936, 937, 944, 945, 946, 947, 949, 0, 0, 0, 138, 172, 24, 25, 474, 508, 57, 58, 682, 716, 90, 91, 850, 952, 123, 124, 680, 681, 20, 21, 714, 715, 54, 55, 748, 749, 88, 89, 782, 779, 122, 119, 152, 153, 284, 285, 186, 187, 318, 319, 220, 221, 352, 353, 254, 251, 386, 383, 684, 685, 22, 23, 717, 718, 56, 53, 750, 751, 86, 87, 783, 784, 120, 121, 154, 155, 286, 287, 188, 185, 320, 317, 218, 219, 350, 351, 252, 253, 384, 385, 416, 417, 548, 549, 450, 451, 582, 583, 484, 485, 616, 617, 518, 515, 650, 647, 544, 545, 16, 17, 578, 579, 50, 51, 612, 613, 84, 85, 646, 643, 118, 115, 418, 419, 550, 551, 452, 449, 584, 581, 482, 483, 614, 615, 516, 517, 648, 649, 552, 553, 18, 19, 585, 586, 52, 49, 618, 619, 82, 83, 651, 652, 116, 117, 148, 149, 280, 281, 182, 183, 314, 315, 216, 217, 348, 349, 250, 247, 382, 379, 412, 413, 408, 409, 446, 447, 442, 443, 480, 481, 476, 477, 514, 511, 510, 507, 150, 151, 282, 283, 184, 181, 316, 313, 214, 215, 346, 347, 248, 249, 380, 381, 414, 415, 420, 421, 448, 445, 453, 454, 478, 479, 486, 487, 512, 513, 519, 520, 12, 13, 144, 145, 46, 47, 178, 179, 80, 81, 212, 213, 114, 111, 246, 243, 276, 277, 272, 273, 310, 311, 306, 307, 344, 345, 340, 341, 378, 375, 374, 371, 14, 15, 146, 147, 48, 45, 180, 177, 78, 79, 210, 211, 112, 113, 244, 245, 278, 279, 288, 289, 312, 309, 321, 322, 342, 343, 354, 355, 376, 377, 387, 388, 8, 9, 140, 141, 42, 43, 174, 175, 76, 77, 208, 209, 110, 107, 242, 239, 136, 137, 4, 5, 170, 171, 38, 39, 204, 205, 72, 73, 238, 235, 106, 103, 10, 11, 142, 143, 44, 41, 176, 173, 74, 75, 206, 207, 108, 109, 240, 241, 156, 157, 6, 7, 189, 190, 40, 37, 222, 223, 70, 71, 255, 256, 104, 105, 0, 0, 0, 0, 0, 0, 0, 0, 0, 0, 0, 0, 0, 0, 0, 0, 0, 0, 0, 0, 0, 0, 0, 0, 0, 0, 0, 0, 0, 0, 0, 0, 0, 0, 0, 0, 0, 0, 0, 0, 0, 0, 0, 0, 0, 0, 0, 0, 0, 0, 0, 0, 0, 0, 0, 0, 0, 0, 0, 0, 0, 0, 0, 0, 0, 0, 0, 0, 0, 0, 0, 0, 0, 0, 0, 0, 0, 0, 0, 0, 0, 0, 0, 0, 0, 0, 0, 0, 0, 0, 0, 0, 0, 0, 0, 0, 0, 0, 0, 0, 0, 0, 0, 0, 0, 0, 0, 0, 0, 0, 0, 0, 0, 0, 0, 0, 0, 0, 0, 0, 0, 0, 0, 0, 0, 0, 0, 0, 0, 0, 0, 0, 0, 0, 0, 0, 0, 0, 0, 0, 0, 0, 0, 0, 0, 0, 0, 0, 0, 0, 0, 0, 0, 0, 0, 0, 0, 0, 0, 0, 0, 16256, 0, 16256, 0, 16256, 0, 16256, 0, 16256, 0, 16256, 0, 16256, 0, 16256, 0, 16256, 0, 16256, 0, 16256, 0, 16256, 0, 16128, 0, 16256, 0, 16128, 0, 0, 0, 16128, 0, 16256, 0, 16256, 0, 16256, 0, 16128, 0, 16256, 0, 16256, 0, 16256, 0, 16128, 0, 16256, 0, 16256, 0, 16256, 0, 16128, 0, 16256, 0, 16256, 0, 16256, 0, 16256, 0, 16256, 0, 16256, 0, 16256, 0, 16256, 0, 16256, 0, 16256, 0, 16256, 0, 16256, 0, 16256, 0, 16256, 0, 16256, 0, 16256, 0, 16256, 0, 16256, 0, 16256, 0, 16256, 0, 16256, 0, 16256, 0, 16256, 0, 16256, 0, 16256, 0, 16256, 0, 16256, 0, 16256, 0, 16256, 0, 16256, 0, 16256, 0, 16256, 0, 16256, 0, 16256, 0, 16256, 0, 16128, 0, 16256, 0, 16256, 0, 16256, 0, 16128, 0, 16256, 0, 16256, 0, 16256, 0, 16128, 0, 16256, 0, 16256, 0, 16256, 0, 16128, 0, 16256, 0, 16256, 0, 16256, 0, 16256, 0, 16256, 0, 16256, 0, 16256, 0, 16256, 0, 16256, 0, 16256, 0, 16256, 0, 16256, 0, 16256, 0, 16256, 0, 16256, 0, 16256, 0, 16256, 0, 16256, 0, 16256, 0, 16256, 0, 16256, 0, 16256, 0, 16256, 0, 16256, 0, 16256, 0, 16256, 0, 16256, 0, 16256, 0, 16256, 0, 16256, 0, 16256, 0, 16256, 0, 16256, 0, 16256, 0, 16256, 0, 16256, 0, 16256, 0, 16256, 0, 16256, 0, 16256, 0, 16256, 0, 16256, 0, 16256, 0, 16256, 0, 16256, 0, 16256, 0, 16256, 0, 16256, 0, 16256, 0, 16256, 0, 16256, 0, 16256, 0, 16256, 0, 16256, 0, 16256, 0, 16256, 0, 16256, 0, 16256, 0, 16256, 0, 16256, 0, 16256, 0, 16256, 0, 16256, 0, 16256, 0, 16256, 0, 16256, 0, 16256, 0, 16256, 0, 16256, 0, 16256, 0, 16256, 0, 16256, 0, 16256, 0, 16256, 0, 16256, 0, 16256, 0, 16256, 0, 16256, 0, 16256, 0, 16256, 0, 16256, 0, 16256, 0, 16256, 0, 16128, 0, 16256, 0, 16256, 0, 16256, 0, 16128, 0, 16256, 0, 16256, 0, 16256, 0, 16128, 0, 16256, 0, 16256, 0, 16256, 0, 16128, 0, 16256, 0, 16256, 0, 16256, 0, 0, 0, 0, 0, 0, 0, 0, 0, 0, 0, 0, 0, 0, 0, 0, 0, 0, 0, 0, 0, 0, 0, 0, 0, 0, 0, 0, 0, 0, 0, 0, 0, 0, 0, 0, 0, 0, 0, 0, 0, 0, 0, 0, 0, 0, 0, 0, 0, 0, 0, 0, 0, 0, 0, 0, 0, 0, 0, 0, 0, 0, 0, 0, 0, 0, 0, 0, 0, 0, 0, 0, 0, 0, 0, 0, 0, 0, 0, 0, 0, 0, 0, 0, 0, 0, 0, 0, 0, 0, 0, 0, 0, 0, 0, 0, 0, 0, 0, 0, 0, 0, 0, 0, 0, 0, 0, 0, 0, 0, 0, 0, 0, 0, 0, 0, 0, 0, 0, 0, 0, 0, 0, 0, 0, 0, 0, 0, 0, 0, 0, 0, 0, 0, 0, 0, 0, 0, 0, 0, 0, 0, 0, 0, 0, 0, 0, 0, 0, 0, 0, 0, 0, 0, 0, 0, 0, 0, 0, 0, 0, 16128, 0, 16256, 0, 16256, 0, 16256, 0, 16128, 0, 16256, 0, 16256, 0, 16256, 0, 16128, 0, 16256, 0, 16256, 0, 16256, 0, 16128, 0, 16256, 0, 16256, 0, 16256, 0, 16256, 0, 16256, 0, 16256, 0, 16256, 0, 16256, 0, 16256, 0, 16256, 0, 16256, 0, 16256, 0, 16256, 0, 16256, 0, 16256, 0, 16256, 0, 16256, 0, 16256, 0, 16256, 0, 16256, 0, 16256, 0, 16256, 0, 16256, 0, 16256, 0, 16256, 0, 16256, 0, 16256, 0, 16256, 0, 16256, 0, 16256, 0, 16256, 0, 16256, 0, 16256, 0, 16256, 0, 16256, 0, 16256, 0, 16256, 0, 16256, 0, 16256, 0, 16256, 0, 16256, 0, 16256, 0, 16256, 0, 16256, 0, 16256, 0, 16256, 0, 16256, 0, 16256, 0, 16256, 0, 16256, 0, 16256, 0, 16256, 0, 16256, 0, 16256, 0, 16256, 0, 16256, 0, 16256, 0, 16256, 0, 16256, 0, 16256, 0, 16256, 0, 16256, 0, 16256, 0, 16256, 0, 16256, 0, 16256, 0, 16256, 0, 16256, 0, 16256, 0, 16256, 0, 16256, 0, 16256, 0, 16256, 0, 16256, 0, 16256, 0, 16256, 0, 16256, 0, 16256, 0, 16256, 0, 16256, 0, 16256, 0, 16256, 0, 16256, 0, 16128, 0, 16256, 0, 16256, 0, 16256, 0, 16128, 0, 16256, 0, 16256, 0, 16256, 0, 16128, 0, 16256, 0, 16256, 0, 16256, 0, 16128, 0, 16256, 0, 16256, 0, 16256, 0, 16256, 0, 16256, 0, 16256, 0, 16256, 0, 16256, 0, 16256, 0, 16256, 0, 16256, 0, 16256, 0, 16256, 0, 16256, 0, 16256, 0, 16256, 0, 16256, 0, 16256, 0, 16256, 0, 16128, 0, 16256, 0, 16256, 0, 16256, 0, 16128, 0, 16256, 0, 16256, 0, 16256, 0, 16128, 0, 16256, 0, 16256, 0, 16256, 0, 16128, 0, 16256, 0, 16256, 0, 16256, 0, 16256, 0, 16256, 0, 16256, 0, 16256, 0, 16256, 0, 16256, 0, 16256, 0, 16256, 0, 16256, 0, 16256, 0, 16256, 0, 16256, 0, 16256, 0, 16256, 0, 16256, 0, 16256, 0, 16256, 0, 16256, 0, 16256, 0, 16256, 0, 16256, 0, 16256, 0, 16256, 0, 16256, 0, 16256, 0, 16256, 0, 16256, 0, 16256, 0, 16256, 0, 16256, 0, 16256, 0, 16256, 0, 0, 0, 0, 0, 0, 0, 0, 0, 0, 0, 0, 0, 0, 0, 0, 0, 0, 0, 0, 0, 0, 0, 0, 0, 0, 0, 0, 0, 0, 0, 0, 0, 0, 0, 0, 0, 0, 0, 0, 0, 0, 0, 0, 0, 0, 0, 0, 0, 0, 0, 0, 0, 0, 0, 0, 0, 0, 0, 0, 0, 0, 0, 0, 0, 0, 0, 0, 0, 0, 0, 0, 0, 0, 0, 0, 0, 0, 0, 0, 0, 0, 0, 0, 0, 0, 0, 0, 0, 0, 0, 0, 0, 0, 0, 0, 0, 0, 0, 0, 0, 0, 0, 0, 0, 0, 0, 0, 0, 0, 0, 0, 0, 0, 0, 0, 0, 0, 0, 0, 0, 0, 0, 0, 0, 0, 0, 0, 0, 16768, 0, 16768, 0, 16768, 0, 16768, 0, 16768, 0, 16768, 0, 16768, 0, 16768, 0, 16768, 0, 16768, 0, 16768, 0, 16768, 0, 16768, 0, 0, 0, 16768, 0, 0, 0, 16256, 0, 16256, 0, 16256, 0, 16256, 0, 16256, 0, 16256, 0, 16256, 0, 16256, 0, 16256, 0, 16256, 0, 16256, 0, 16256, 0, 16128, 0, 16256, 0, 16128, 0, 17280, 0, 16128, 0, 16256, 0, 16256, 0, 16256, 0, 16128, 0, 16256, 0, 16256, 0, 16256, 0, 16128, 0, 16256, 0, 16256, 0, 16256, 0, 16128, 0, 16256, 0, 16256, 0, 16256, 0, 16256, 0, 16256, 0, 16256, 0, 16256, 0, 16256, 0, 16256, 0, 16256, 0, 16256, 0, 16256, 0, 16256, 0, 16256, 0, 16256, 0, 16256, 0, 16256, 0, 16256, 0, 16256, 0, 16256, 0, 16256, 0, 16256, 0, 16256, 0, 16256, 0, 16256, 0, 16256, 0, 16256, 0, 16256, 0, 16256, 0, 16256, 0, 16256, 0, 16256, 0, 16256, 0, 16256, 0, 16256, 0, 16128, 0, 16256, 0, 16256, 0, 16256, 0, 16128, 0, 16256, 0, 16256, 0, 16256, 0, 16128, 0, 16256, 0, 16256, 0, 16256, 0, 16128, 0, 16256, 0, 16256, 0, 16256, 0, 16256, 0, 16256, 0, 16256, 0, 16256, 0, 16256, 0, 16256, 0, 16256, 0, 16256, 0, 16256, 0, 16256, 0, 16256, 0, 16256, 0, 16256, 0, 16256, 0, 16256, 0, 16256, 0, 16256, 0, 16256, 0, 16256, 0, 16256, 0, 16256, 0, 16256, 0, 16256, 0, 16256, 0, 16256, 0, 16256, 0, 16256, 0, 16256, 0, 16256, 0, 16256, 0, 16256, 0, 16256, 0, 16256, 0, 16256, 0, 16256, 0, 16256, 0, 16256, 0, 16256, 0, 16256, 0, 16256, 0, 16256, 0, 16256, 0, 16256, 0, 16256, 0, 16256, 0, 16256, 0, 16256, 0, 16256, 0, 16256, 0, 16256, 0, 16256, 0, 16256, 0, 16256, 0, 16256, 0, 16256, 0, 16256, 0, 16256, 0, 16256, 0, 16256, 0, 16256, 0, 16256, 0, 16256, 0, 16256, 0, 16256, 0, 16256, 0, 16256, 0, 16256, 0, 16256, 0, 16256, 0, 16256, 0, 16256, 0, 16256, 0, 16256, 0, 16256, 0, 16256, 0, 16256, 0, 16256, 0, 16256, 0, 16256, 0, 16256, 0, 16128, 0, 16256, 0, 16256, 0, 16256, 0, 16128, 0, 16256, 0, 16256, 0, 16256, 0, 16128, 0, 16256, 0, 16256, 0, 16256, 0, 16128, 0, 16256, 0, 16256, 0, 16256, 0, 0, 0, 0, 0, 0, 0, 0, 0, 0, 0, 0, 0, 0, 0, 0, 0, 0, 0, 0, 0, 0, 0, 0, 0, 0, 0, 0, 0, 0, 0, 0, 0, 0, 0, 0, 0, 0, 0, 0, 0, 0, 0, 0, 0, 0, 0, 0, 0, 0, 0, 0, 0, 0, 0, 0, 0, 0, 0, 0, 0, 0, 0, 0, 0, 0, 0, 0, 0, 0, 0, 0, 0, 0, 0, 0, 0, 0, 0, 0, 0, 0, 0, 0, 0, 0, 0, 0, 0, 0, 0, 0, 0, 0, 0, 0, 0, 0, 0, 0, 0, 0, 0, 0, 0, 0, 0, 0, 0, 0, 0, 0, 0, 0, 0, 0, 0, 0, 0, 0, 0, 0, 0, 0, 0, 0, 0, 0, 0, 16768, 0, 16768, 0, 16768, 0, 16768, 0, 16768, 0, 16768, 0, 16768, 0, 16768, 0, 16768, 0, 16768, 0, 16768, 0, 16768, 0, 0, 0, 0, 0, 0, 0, 0, 0, 16128, 0, 16256, 0, 16256, 0, 16256, 0, 16128, 0, 16256, 0, 16256, 0, 16256, 0, 16128, 0, 16256, 0, 16256, 0, 16256, 0, 16128, 0, 16256, 0, 16256, 0, 16256, 0, 16256, 0, 16256, 0, 16256, 0, 16256, 0, 16256, 0, 16256, 0, 16256, 0, 16256, 0, 16256, 0, 16256, 0, 16256, 0, 16256, 0, 16256, 0, 16256, 0, 16256, 0, 16256, 0, 16256, 0, 16256, 0, 16256, 0, 16256, 0, 16256, 0, 16256, 0, 16256, 0, 16256, 0, 16256, 0, 16256, 0, 16256, 0, 16256, 0, 16256, 0, 16256, 0, 16256, 0, 16256, 0, 16256, 0, 16256, 0, 16256, 0, 16256, 0, 16256, 0, 16256, 0, 16256, 0, 16256, 0, 16256, 0, 16256, 0, 16256, 0, 16256, 0, 16256, 0, 16256, 0, 16256, 0, 16256, 0, 16256, 0, 16256, 0, 16256, 0, 16256, 0, 16256, 0, 16256, 0, 16256, 0, 16256, 0, 16256, 0, 16256, 0, 16256, 0, 16256, 0, 16256, 0, 16256, 0, 16256, 0, 16256, 0, 16256, 0, 16256, 0, 16256, 0, 16256, 0, 16256, 0, 16256, 0, 16256, 0, 16256, 0, 16256, 0, 16256, 0, 16256, 0, 16256, 0, 16256, 0, 16256, 0, 16256, 0, 16256, 0, 16128, 0, 16256, 0, 16256, 0, 16256, 0, 16128, 0, 16256, 0, 16256, 0, 16256, 0, 16128, 0, 16256, 0, 16256, 0, 16256, 0, 16128, 0, 16256, 0, 16256, 0, 16256, 0, 16256, 0, 16256, 0, 16256, 0, 16256, 0, 16256, 0, 16256, 0, 16256, 0, 16256, 0, 16256, 0, 16256, 0, 16256, 0, 16256, 0, 16256, 0, 16256, 0, 16256, 0, 16256, 0, 16128, 0, 16256, 0, 16256, 0, 16256, 0, 16128, 0, 16256, 0, 16256, 0, 16256, 0, 16128, 0, 16256, 0, 16256, 0, 16256, 0, 16128, 0, 16256, 0, 16256, 0, 16256, 0, 16256, 0, 16256, 0, 16256, 0, 16256, 0, 16256, 0, 16256, 0, 16256, 0, 16256, 0, 16256, 0, 16256, 0, 16256, 0, 16256, 0, 16256, 0, 16256, 0, 16256, 0, 16256, 0, 16256, 0, 16256, 0, 16256, 0, 16256, 0, 16256, 0, 16256, 0, 16256, 0, 16256, 0, 16256, 0, 16256, 0, 16256, 0, 16256, 0, 16256, 0, 16256, 0, 16256, 0, 16256, 0, 0, 0, 0, 0, 0, 0, 0, 0, 0, 0, 0, 0, 0, 0, 0, 0, 0, 0, 0, 0, 0, 0, 0, 0, 0, 0, 0, 0, 0, 0, 0, 0, 0, 0, 0, 0, 0, 0, 0, 0, 0, 0, 0, 0, 0, 0, 0, 0, 0, 0, 0, 0, 0, 0, 0, 0, 0, 0, 0, 0, 0, 0, 0, 0, 0, 0, 0, 0, 0, 0, 0, 0, 0, 0, 0, 0, 0, 0, 0, 0, 0, 0, 0, 0, 0, 0, 0, 0, 0, 0, 0, 0, 0, 0, 0, 0, 0, 0, 0, 0, 0, 0, 0, 0, 0, 0, 0, 0, 0, 0, 0, 0, 0, 0, 0, 0, 0, 0, 0, 0, 0, 0, 0, 0, 0, 0, 0, 0, 0, 0, 0, 0, 0, 0, 0, 0, 0, 0, 0, 0, 0, 0, 0, 0, 16256, 0, 16256, 0, 16128, 0, 16256, 0, 16256, 0, 16256, 0, 16128, 0, 16256, 0, 0, 0, 0, 0, 0, 0, 0, 0, 0, 0, 0, 0, 0, 0, 0, 0, 16256, 0, 16256, 0, 16256, 0, 16256, 0, 16256, 0, 16256, 0, 16256, 0, 16256, 0, 16128, 0, 16256, 0, 16256, 0, 16256, 0, 16128, 0, 16256, 0, 16256, 0, 16256, 0, 16256, 0, 16256, 0, 16256, 0, 16256, 0, 16256, 0, 16256, 0, 16256, 0, 16256, 0, 16256, 0, 16256, 0, 16256, 0, 16256, 0, 16256, 0, 16256, 0, 16256, 0, 16256, 0, 16256, 0, 16256, 0, 16256, 0, 16256, 0, 16256, 0, 16256, 0, 16256, 0, 16256, 0, 16256, 0, 16256, 0, 16256, 0, 16256, 0, 16256, 0, 16256, 0, 16256, 0, 16256, 0, 16128, 0, 16256, 0, 16256, 0, 16256, 0, 16128, 0, 16256, 0, 16256, 0, 16256, 0, 16256, 0, 16256, 0, 16256, 0, 16256, 0, 16256, 0, 16256, 0, 16256, 0, 16256, 0, 16256, 0, 16256, 0, 16256, 0, 16256, 0, 16256, 0, 16256, 0, 16256, 0, 16256, 0, 16256, 0, 16256, 0, 16256, 0, 16256, 0, 16256, 0, 16256, 0, 16256, 0, 16256, 0, 16256, 0, 16256, 0, 16128, 0, 16256, 0, 16256, 0, 16256, 0, 16128, 0, 16256, 0, 16256, 0, 16256, 0, 16256, 0, 16256, 0, 16256, 0, 16256, 0, 16256, 0, 16256, 0, 16256, 0, 16256, 0, 16256, 0, 16256, 0, 16256, 0, 16256, 0, 16256, 0, 16256, 0, 16256, 0, 16256, 0, 16256, 0, 16256, 0, 16256, 0, 16256, 0, 16256, 0, 16256, 0, 16256, 0, 16256, 0, 16128, 0, 16256, 0, 16256, 0, 16256, 0, 16128, 0, 16256, 0, 16256, 0, 16256, 0, 16256, 0, 16256, 0, 16256, 0, 16256, 0, 16256, 0, 16256, 0, 16256, 0, 16256, 0, 16256, 0, 16256, 0, 16256, 0, 16256, 0, 16256, 0, 16256, 0, 16256, 0, 16256, 0, 16256, 0, 16256, 0, 16256, 0, 16256, 0, 16256, 0, 16256, 0, 16128, 0, 16256, 0, 16256, 0, 16256, 0, 16128, 0, 16256, 0, 16256, 0, 16256, 0, 16256, 0, 16256, 0, 16256, 0, 16256, 0, 16256, 0, 16256, 0, 16256, 0, 16256, 0, 16256, 0, 16256, 0, 16256, 0, 16256, 0, 16256, 0, 16256, 0, 16256, 0, 16256, 0, 0, 0, 0, 0, 0, 0, 0, 0, 0, 0, 0, 0, 0, 0, 0, 0, 0, 0, 0, 0, 0, 0, 0, 0, 0, 0, 0, 0, 0, 0, 0, 0, 0, 0, 0, 0, 0, 0, 0, 0, 0, 0, 0, 0, 0, 0, 0, 0, 0, 0, 0, 0, 0, 0, 0, 0, 0, 0, 0, 0, 0, 0, 0, 0, 0, 0, 0, 0, 0, 0, 0, 0, 0, 0, 0, 0, 0, 0, 0, 0, 0, 0, 0, 0, 0, 0, 0, 0, 0, 0, 0, 0, 0, 0, 0, 0, 0, 0, 0, 0, 0, 0, 0, 0, 0, 0, 0, 0, 0, 0, 0, 0, 0, 0, 0, 0, 0, 0, 0, 0, 0, 0, 0, 0, 0, 0, 0, 0, 0, 0, 0, 0, 0, 0, 0, 0, 0, 0, 0, 0, 0, 0, 0, 0, 16256, 0, 16256, 0, 16128, 0, 16256, 0, 16128, 0, 16256, 0, 16128, 0, 16256, 0, 0, 0, 0, 0, 0, 0, 0, 0, 0, 0, 0, 0, 0, 0, 0, 0, 16256, 0, 16256, 0, 16256, 0, 16256, 0, 16128, 0, 0, 0, 16256, 0, 16256, 0, 16128, 0, 16256, 0, 16256, 0, 16256, 0, 16128, 0, 16256, 0, 16256, 0, 16256, 0, 16256, 0, 16256, 0, 16256, 0, 16256, 0, 16256, 0, 16256, 0, 16256, 0, 16256, 0, 16256, 0, 16256, 0, 16256, 0, 16256, 0, 16256, 0, 16256, 0, 16256, 0, 16256, 0, 16256, 0, 16256, 0, 16256, 0, 16256, 0, 16256, 0, 16256, 0, 16256, 0, 16256, 0, 16256, 0, 16256, 0, 16256, 0, 16256, 0, 16256, 0, 16256, 0, 16256, 0, 16256, 0, 16128, 0, 16256, 0, 16256, 0, 16256, 0, 16128, 0, 16256, 0, 16256, 0, 16256, 0, 16256, 0, 16256, 0, 16256, 0, 16256, 0, 16256, 0, 16256, 0, 16256, 0, 16256, 0, 16256, 0, 16256, 0, 16256, 0, 16256, 0, 16256, 0, 16256, 0, 16256, 0, 16256, 0, 16256, 0, 16256, 0, 16256, 0, 16256, 0, 16256, 0, 16256, 0, 16256, 0, 16256, 0, 16256, 0, 16256, 0, 16128, 0, 16256, 0, 16256, 0, 16256, 0, 16128, 0, 16256, 0, 16256, 0, 16256, 0, 16256, 0, 16256, 0, 16256, 0, 16256, 0, 16256, 0, 16256, 0, 16256, 0, 16256, 0, 16256, 0, 16256, 0, 16256, 0, 16256, 0, 16256, 0, 16256, 0, 16256, 0, 16256, 0, 16256, 0, 16256, 0, 16256, 0, 16256, 0, 16256, 0, 16256, 0, 16256, 0, 16256, 0, 16128, 0, 16256, 0, 16256, 0, 16256, 0, 16128, 0, 16256, 0, 16256, 0, 16256, 0, 16256, 0, 16256, 0, 16256, 0, 16256, 0, 16256, 0, 16256, 0, 16256, 0, 16256, 0, 16256, 0, 16256, 0, 16256, 0, 16256, 0, 16256, 0, 16256, 0, 16256, 0, 16256, 0, 16256, 0, 16256, 0, 16256, 0, 16256, 0, 16256, 0, 16256, 0, 16128, 0, 16256, 0, 16256, 0, 16256, 0, 16128, 0, 16256, 0, 16256, 0, 16256, 0, 16256, 0, 16256, 0, 16256, 0, 16256, 0, 16256, 0, 16256, 0, 16256, 0, 16256, 0, 16256, 0, 16256, 0, 16256, 0, 16256, 0, 16256, 0, 16256, 0, 16256, 0, 16256, 0, 0, 0, 0, 0, 0, 0, 0, 0, 0, 0, 0, 0, 0, 0, 0, 0, 0, 0, 0, 0, 0, 0, 0, 0, 0, 0, 0, 0, 0, 0, 0, 0, 0, 0, 0, 0, 0, 0, 0, 0, 0, 0, 0, 0, 0, 0, 0, 0, 0, 0, 0, 0, 0, 0, 0, 0, 0, 0, 0, 0, 0, 0, 0, 0, 0, 0, 0, 0, 0, 0, 0, 0, 0, 0, 0, 0, 0, 0, 0, 0, 0, 0, 0, 0, 0, 0, 0, 0, 0, 0, 0, 0, 0, 0, 0, 0, 0, 0, 0, 0, 0, 0, 0, 0, 0, 0, 0, 0, 0, 0, 0, 0, 0, 0, 0, 0, 0, 0, 0, 0, 0, 0, 0, 0, 0, 0, 0, 0, 16768, 0, 16768, 0, 16768, 0, 16768, 0, 16768, 0, 16768, 0, 16768, 0, 16768, 0, 16256, 0, 16256, 0, 16128, 0, 16256, 0, 16256, 0, 16256, 0, 16128, 0, 16256, 0, 16768, 0, 16768, 0, 16768, 0, 16768, 0, 16768, 0, 16768, 0, 16768, 0, 16768, 0, 16256, 0, 16256, 0, 16256, 0, 16256, 0, 16256, 0, 16256, 0, 16256, 0, 16256, 0, 16128, 0, 16256, 0, 16256, 0, 16256, 0, 16128, 0, 16256, 0, 16256, 0, 16256, 0, 16256, 0, 16256, 0, 16256, 0, 16256, 0, 16256, 0, 16256, 0, 16256, 0, 16256, 0, 16256, 0, 16256, 0, 16256, 0, 16256, 0, 16256, 0, 16256, 0, 16256, 0, 16256, 0, 16256, 0, 16256, 0, 16256, 0, 16256, 0, 16256, 0, 16256, 0, 16256, 0, 16256, 0, 16256, 0, 16256, 0, 16256, 0, 16256, 0, 16256, 0, 16256, 0, 16256, 0, 16256, 0, 16128, 0, 16256, 0, 16256, 0, 16256, 0, 16128, 0, 16256, 0, 16256, 0, 16256, 0, 16256, 0, 16256, 0, 16256, 0, 16256, 0, 16256, 0, 16256, 0, 16256, 0, 16256, 0, 16256, 0, 16256, 0, 16256, 0, 16256, 0, 16256, 0, 16256, 0, 16256, 0, 16256, 0, 16256, 0, 16256, 0, 16256, 0, 16256, 0, 16256, 0, 16256, 0, 16256, 0, 16256, 0, 16256, 0, 16256, 0, 16128, 0, 16256, 0, 16256, 0, 16256, 0, 16128, 0, 16256, 0, 16256, 0, 16256, 0, 16256, 0, 16256, 0, 16256, 0, 16256, 0, 16256, 0, 16256, 0, 16256, 0, 16256, 0, 16256, 0, 16256, 0, 16256, 0, 16256, 0, 16256, 0, 16256, 0, 16256, 0, 16256, 0, 16256, 0, 16256, 0, 16256, 0, 16256, 0, 16256, 0, 16256, 0, 16256, 0, 16256, 0, 16128, 0, 16256, 0, 16256, 0, 16256, 0, 16128, 0, 16256, 0, 16256, 0, 16256, 0, 16256, 0, 16256, 0, 16256, 0, 16256, 0, 16256, 0, 16256, 0, 16256, 0, 16256, 0, 16256, 0, 16256, 0, 16256, 0, 16256, 0, 16256, 0, 16256, 0, 16256, 0, 16256, 0, 16256, 0, 16256, 0, 16256, 0, 16256, 0, 16256, 0, 16256, 0, 16128, 0, 16256, 0, 16256, 0, 16256, 0, 16128, 0, 16256, 0, 16256, 0, 16256, 0, 16256, 0, 16256, 0, 16256, 0, 16256, 0, 16256, 0, 16256, 0, 16256, 0, 16256, 0, 16256, 0, 16256, 0, 16256, 0, 16256, 0, 16256, 0, 16256, 0, 16256, 0, 16256, 0, 0, 0, 0, 0, 0, 0, 0, 0, 0, 0, 0, 0, 0, 0, 0, 0, 0, 0, 0, 0, 0, 0, 0, 0, 0, 0, 0, 0, 0, 0, 0, 0, 0, 0, 0, 0, 0, 0, 0, 0, 0, 0, 0, 0, 0, 0, 0, 0, 0, 0, 0, 0, 0, 0, 0, 0, 0, 0, 0, 0, 0, 0, 0, 0, 0, 0, 0, 0, 0, 0, 0, 0, 0, 0, 0, 0, 0, 0, 0, 0, 0, 0, 0, 0, 0, 0, 0, 0, 0, 0, 0, 0, 0, 0, 0, 0, 0, 0, 0, 0, 0, 0, 0, 0, 0, 0, 0, 0, 0, 0, 0, 0, 0, 0, 0, 0, 0, 0, 0, 0, 0, 0, 0, 0, 0, 0, 0, 0, 16768, 0, 16768, 0, 16768, 0, 16768, 0, 16768, 0, 0, 0, 0, 0, 0, 0, 16256, 0, 16256, 0, 16128, 0, 16256, 0, 16128, 0, 16256, 0, 16128, 0, 16256, 0, 16768, 0, 16768, 0, 16768, 0, 16768, 0, 16768, 0, 0, 0, 0, 0, 0, 0, 16256, 0, 16256, 0, 16256, 0, 16256, 0, 16128, 0, 17280, 0, 16256, 0, 16256, 0, 16128, 0, 16256, 0, 16256, 0, 16256, 0, 16128, 0, 16256, 0, 16256, 0, 16256, 0, 16256, 0, 16256, 0, 16256, 0, 16256, 0, 16256, 0, 16256, 0, 16256, 0, 16256, 0, 16256, 0, 16256, 0, 16256, 0, 16256, 0, 16256, 0, 16256, 0, 16256, 0, 16256, 0, 16256, 0, 16256, 0, 16256, 0, 16256, 0, 16256, 0, 16256, 0, 16256, 0, 16256, 0, 16256, 0, 16256, 0, 16256, 0, 16256, 0, 16256, 0, 16256, 0, 16256, 0, 16256, 0, 16128, 0, 16256, 0, 16256, 0, 16256, 0, 16128, 0, 16256, 0, 16256, 0, 16256, 0, 16256, 0, 16256, 0, 16256, 0, 16256, 0, 16256, 0, 16256, 0, 16256, 0, 16256, 0, 16256, 0, 16256, 0, 16256, 0, 16256, 0, 16256, 0, 16256, 0, 16256, 0, 16256, 0, 16256, 0, 16256, 0, 16256, 0, 16256, 0, 16256, 0, 16256, 0, 16256, 0, 16256, 0, 16256, 0, 16256, 0, 16128, 0, 16256, 0, 16256, 0, 16256, 0, 16128, 0, 16256, 0, 16256, 0, 16256, 0, 16256, 0, 16256, 0, 16256, 0, 16256, 0, 16256, 0, 16256, 0, 16256, 0, 16256, 0, 16256, 0, 16256, 0, 16256, 0, 16256, 0, 16256, 0, 16256, 0, 16256, 0, 16256, 0, 16256, 0, 16256, 0, 16256, 0, 16256, 0, 16256, 0, 16256, 0, 16256, 0, 16256, 0, 16128, 0, 16256, 0, 16256, 0, 16256, 0, 16128, 0, 16256, 0, 16256, 0, 16256, 0, 16256, 0, 16256, 0, 16256, 0, 16256, 0, 16256, 0, 16256, 0, 16256, 0, 16256, 0, 16256, 0, 16256, 0, 16256, 0, 16256, 0, 16256, 0, 16256, 0, 16256, 0, 16256, 0, 16256, 0, 16256, 0, 16256, 0, 16256, 0, 16256, 0, 16256, 0, 16128, 0, 16256, 0, 16256, 0, 16256, 0, 16128, 0, 16256, 0, 16256, 0, 16256, 0, 16256, 0, 16256, 0, 16256, 0, 16256, 0, 16256, 0, 16256, 0, 16256, 0, 16256, 0, 16256, 0, 16256, 0, 16256, 0, 16256, 0, 16256, 0, 16256, 0, 16256, 0, 16256, 0, 0, 0, 0, 0, 0, 0, 0, 0, 0, 0, 0, 0, 0, 0, 0, 0, 0, 0, 0, 0, 0, 0, 0, 0, 0, 0, 0, 0, 0, 0, 0, 0, 0, 0, 0, 0, 0, 0, 0, 0, 0, 0, 0, 0, 0, 0, 0, 0, 0, 0, 0, 0, 0, 0, 0, 0, 0, 0, 0, 0, 0, 0, 0, 0, 0, 0, 0, 0, 0, 0, 0, 0, 0, 0, 0, 0, 0, 0, 0, 0, 0, 0, 0, 0, 0, 0, 0, 0, 0, 0, 0, 0, 0, 0, 0, 0, 0, 0, 0, 0, 0, 0, 0, 0, 0, 0, 0, 0, 0, 0, 0, 0, 0, 0, 0, 0, 0, 0, 0, 0, 0, 0, 0, 0, 0, 0, 0};

__global__ __launch_bounds__(512) void xd_kernel(const int* idx, const u16* w1, const u16* table, const u16* b1, const u16* ws_cin, const u16* wout, char* ws, const u16* w2, const u16* b_out, u16* out) {
  __shared__ __attribute__((aligned(1024))) char shm[163840];
  unsigned tid = threadIdx.x;
  asm volatile(
    "s_load_dwordx16 s[4:19], s[0:1], 0\n"
    ".Lxd_top:\n"
    "v_readfirstlane_b32 s43, v0\n"
    "s_setprio 3\n"
    "s_cmp_lt_u32 s2, 8\n"
    "s_cbranch_scc1 .Lxd_s2\n"
    "s_cmp_lt_u32 s2, 48\n"
    "s_cbranch_scc1 .Lxd_help\n"
    "s_and_b32 s24, s2, 15\n"
    "s_mul_i32 s40, s43, 36\n"
    "s_mul_i32 s41, s24, 26624\n"
    "s_add_u32 s38, s40, s41\n"
    "v_lshlrev_b32 v86, 4, v0\n"
    "s_lshl_b32 s34, s43, 6\n"
    "s_add_u32 m0, s34, 98304\n"
    "s_waitcnt lgkmcnt(0)\n"
    "s_add_u32 s38, s4, s38\n"
    "s_addc_u32 s39, s5, 0\n"
    "global_load_lds_dwordx4 v86, s[38:39] offset:0\n"
    "global_load_lds_dwordx4 v86, s[38:39] offset:1024\n"
    "global_load_lds_dwordx4 v86, s[38:39] offset:2048\n"
    "global_load_lds_dwordx4 v86, s[38:39] offset:2304\n"
    "s_sub_u32 s3, s2, 48\n"
    "s_lshl_b32 s40, s2, 2\n"
    "s_lshr_b32 s23, s3, 4\n"
    "s_lshr_b32 s40, 0xa8864220, s40\n"
    "s_and_b32 s42, s40, 14\n"
    "v_bfe_u32 v85, v0, 4, 2\n"
    "v_lshlrev_b32 v87, 1, v0\n"
    "v_and_b32 v87, 0xe0, v87\n"
    "v_and_b32 v88, 0xf0, v86\n"
    "v_xor_b32 v87, v87, v88\n"
    "s_movk_i32 s41, 3328\n"
    "v_mad_u32_u24 v46, v85, s41, v87\n"
    "v_add_u32 v47, 12288, v46\n"
    "s_mul_i32 s41, s43, 416\n"
    "s_lshl_b32 s40, s23, 8\n"
    "s_add_u32 s41, s41, s40\n"
    "s_mul_i32 s30, s42, 425984\n"
    "s_lshl_b32 s35, s43, 5\n"
    "s_bitset1_b32 s35, 15\n"
    "s_add_u32 s6, s6, s41\n"
    "s_addc_u32 s7, s7, 0\n"
    "s_add_u32 s30, s6, s30\n"
    "s_addc_u32 s31, s7, 0\n"
    "s_add_u32 m0, s35, 0\n"
    "s_nop 0\n"
    "global_load_lds_dwordx4 v46, s[30:31] offset:0\n"
    "global_load_lds_dwordx4 v47, s[30:31] offset:1024\n"
    "s_add_u32 s30, s30, 212992\n"
    "s_addc_u32 s31, s31, 0\n"
    "s_setprio 2\n"
    "v_and_b32 v84, 3, v0\n"
    "v_bfe_u32 v44, v0, 3, 3\n"
    "v_lshl_add_u32 v44, v84, 3, v44\n"
    "s_add_u32 s40, s34, 98304\n"
    "v_mul_u32_u24 v44, 104, v44\n"
    "v_add_u32 v44, s40, v44\n"
    "v_lshl_add_u32 v45, s42, 3, v44\n"
    "v_xor_b32 v51, v0, v86\n"
    "v_and_b32 v86, 0x70, v86\n"
    "v_bfi_b32 v86, 48, v51, v86\n"
    "v_xor_b32 v51, 64, v86\n"
    "v_add_u32 v48, 3072, v86\n"
    "v_add_u32 v49, 2048, v51\n"
    "v_add_u32 v50, 1024, v86\n"
    "s_sub_u32 s36, s8, 3072\n"
    "s_subb_u32 s37, s9, 0\n"
    "s_add_u32 m0, s35, 49152\n"
    "s_nop 0\n"
    "global_load_lds_dwordx4 v46, s[30:31] offset:0\n"
    "global_load_lds_dwordx4 v47, s[30:31] offset:1024\n"
    "s_add_u32 s30, s30, 212992\n"
    "s_addc_u32 s31, s31, 0\n"
    "s_add_u32 m0, s35, 98304\n"
    "s_nop 0\n"
    "global_load_lds_dwordx4 v46, s[30:31] offset:0\n"
    "global_load_lds_dwordx4 v47, s[30:31] offset:1024\n"
    "s_add_u32 s30, s30, 212992\n"
    "s_addc_u32 s31, s31, 0\n"
    "s_waitcnt vmcnt(6)\n"
    "ds_read2_b64 v[96:99], v45 offset1:1\n"
    "s_lshr_b32 s20, s43, 6\n"
    "s_lshr_b32 s21, s43, 8\n"
    "v_and_b32 v1, 63, v0\n"
    "s_waitcnt lgkmcnt(0)\n"
    "v_lshlrev_b32 v96, 7, v96\n"
    "v_lshlrev_b32 v97, 7, v97\n"
    "v_lshlrev_b32 v98, 7, v98\n"
    "v_add_u32_dpp v52, v96, v48 quad_perm:[0,0,0,0] row_mask:0xf bank_mask:0xf\n"
    "v_add_u32_dpp v53, v96, v49 quad_perm:[1,1,1,1] row_mask:0xf bank_mask:0xf\n"
    "v_add_u32_dpp v54, v96, v50 quad_perm:[2,2,2,2] row_mask:0xf bank_mask:0xf\n"
    "v_add_u32_dpp v55, v96, v51 quad_perm:[3,3,3,3] row_mask:0xf bank_mask:0xf\n"
    "s_add_u32 m0, s34, 0\n"
    "s_nop 0\n"
    "global_load_lds_dwordx4 v52, s[36:37] offset:0\n"
    "global_load_lds_dwordx4 v53, s[36:37] offset:1024\n"
    "global_load_lds_dwordx4 v54, s[36:37] offset:2048\n"
    "global_load_lds_dwordx4 v55, s[36:37] offset:3072\n"
    "s_setprio 1\n"
    "v_add_u32_dpp v52, v97, v48 quad_perm:[0,0,0,0] row_mask:0xf bank_mask:0xf\n"
    "v_add_u32_dpp v53, v97, v49 quad_perm:[1,1,1,1] row_mask:0xf bank_mask:0xf\n"
    "v_add_u32_dpp v54, v97, v50 quad_perm:[2,2,2,2] row_mask:0xf bank_mask:0xf\n"
    "v_add_u32_dpp v55, v97, v51 quad_perm:[3,3,3,3] row_mask:0xf bank_mask:0xf\n"
    "s_load_dwordx4 s[68:71], s[0:1], 64\n"
    "s_lshl2_add_u32 s41, s42, 8\n"
    "s_mov_b32 s28, 0x30201000\n"
    "s_movk_i32 s29, 0x5040\n"
    "s_lshr_b64 s[42:43], s[28:29], s41\n"
    "s_sub_u32 s41, 48, s41\n"
    "s_lshl_b64 s[28:29], s[28:29], s41\n"
    "s_or_b64 s[28:29], s[28:29], s[42:43]\n"
    "s_and_b32 s29, s29, 0xff\n"
    "s_or_b32 s29, s29, 0x6000\n"
    "s_bfe_u32 s40, s28, 0x80000\n"
    "v_add_u32 v45, s40, v44\n"
    "ds_read2_b64 v[100:103], v45 offset1:1\n"
    "s_bfe_u32 s40, s28, 0x80008\n"
    "v_add_u32 v45, s40, v44\n"
    "ds_read2_b64 v[104:107], v45 offset1:1\n"
    "s_bfe_u32 s40, s28, 0x80010\n"
    "v_add_u32 v45, s40, v44\n"
    "ds_read2_b64 v[108:111], v45 offset1:1\n"
    "s_bfe_u32 s40, s28, 0x80018\n"
    "v_add_u32 v45, s40, v44\n"
    "ds_read2_b64 v[112:115], v45 offset1:1\n"
    "s_bfe_u32 s40, s29, 0x80000\n"
    "v_add_u32 v45, s40, v44\n"
    "ds_read2_b64 v[116:119], v45 offset1:1\n"
    "s_bfe_u32 s40, s29, 0x80008\n"
    "v_add_u32 v45, s40, v44\n"
    "ds_read_b64 v[120:121], v45\n"
    "v_lshrrev_b32 v86, 5, v88\n"
    "v_xor_b32 v86, v86, v85\n"
    "v_lshlrev_b32 v86, 4, v86\n"
    "v_lshl_add_u32 v86, v88, 3, v86\n"
    "s_and_b32 s41, s20, 1\n"
    "s_lshl1_add_u32 s40, s21, s41\n"
    "v_lshl_add_u32 v56, s40, 13, v86\n"
    "v_xor_b32 v57, 64, v56\n"
    "v_lshrrev_b32 v86, 6, v88\n"
    "v_lshl_add_u32 v88, v85, 3, v86\n"
    "v_bfe_u32 v86, v0, 2, 3\n"
    "s_and_b32 s40, s20, 2\n"
    "s_lshl1_add_u32 s40, s40, 1024\n"
    "v_xor_b32 v86, s40, v86\n"
    "v_lshlrev_b32 v86, 5, v86\n"
    "v_lshl_add_u32 v86, v88, 8, v86\n"
    "v_lshl_add_u32 v76, v84, 3, v86\n"
    "v_xor_b32 v77, 32, v76\n"
    "v_xor_b32 v78, 64, v76\n"
    "v_xor_b32 v79, 96, v76\n"
    "v_lshlrev_b32 v92, 2, v0\n"
    "v_add_u32_e64 v93, s21, -1\n"
    "v_add_u32 v92, 149504, v92\n"
    "ds_write_b32 v92, v93\n"
    "v_lshlrev_b32 v99, 7, v99\n"
    "s_setprio 0\n"
    "s_cmp_eq_u32 s21, 0\n"
    "s_cbranch_scc1 .Lxd_g0\n"
    "s_waitcnt vmcnt(8)\n"
    "s_barrier\n"
    ".Lxd_g0:\n"
    "s_add_u32 m0, s34, 49152\n"
    "s_nop 0\n"
    "global_load_lds_dwordx4 v52, s[36:37] offset:0\n"
    "global_load_lds_dwordx4 v53, s[36:37] offset:1024\n"
    "global_load_lds_dwordx4 v54, s[36:37] offset:2048\n"
    "global_load_lds_dwordx4 v55, s[36:37] offset:3072\n"
    "v_add_u32_dpp v52, v98, v48 quad_perm:[0,0,0,0] row_mask:0xf bank_mask:0xf\n"
    "v_add_u32_dpp v53, v98, v49 quad_perm:[1,1,1,1] row_mask:0xf bank_mask:0xf\n"
    "v_add_u32_dpp v54, v98, v50 quad_perm:[2,2,2,2] row_mask:0xf bank_mask:0xf\n"
    "v_add_u32_dpp v55, v98, v51 quad_perm:[3,3,3,3] row_mask:0xf bank_mask:0xf\n"
    "s_waitcnt vmcnt(4) lgkmcnt(0)\n"
    "s_barrier\n"
    "ds_read_b128 v[128:131], v56 offset:0\n"
    "ds_read_b128 v[132:135], v56 offset:2048\n"
    "ds_read_b128 v[136:139], v56 offset:4096\n"
    "ds_read_b128 v[140:143], v56 offset:6144\n"
    "ds_read_b64_tr_b16 v[144:145], v76 offset:0\n"
    "ds_read_b64_tr_b16 v[146:147], v76 offset:1024\n"
    "ds_read_b64_tr_b16 v[148:149], v77 offset:0\n"
    "ds_read_b64_tr_b16 v[150:151], v77 offset:1024\n"
    "ds_read_b64_tr_b16 v[152:153], v78 offset:0\n"
    "ds_read_b64_tr_b16 v[154:155], v78 offset:1024\n"
    "ds_read_b64_tr_b16 v[156:157], v79 offset:0\n"
    "ds_read_b64_tr_b16 v[158:159], v79 offset:1024\n"
    "ds_read_b128 v[160:163], v57 offset:0\n"
    "ds_read_b128 v[164:167], v57 offset:2048\n"
    "ds_read_b128 v[168:171], v57 offset:4096\n"
    "ds_read_b128 v[172:175], v57 offset:6144\n"
    "ds_read_b64_tr_b16 v[176:177], v76 offset:8192\n"
    "ds_read_b64_tr_b16 v[178:179], v76 offset:9216\n"
    "ds_read_b64_tr_b16 v[180:181], v77 offset:8192\n"
    "ds_read_b64_tr_b16 v[182:183], v77 offset:9216\n"
    "ds_read_b64_tr_b16 v[184:185], v78 offset:8192\n"
    "ds_read_b64_tr_b16 v[186:187], v78 offset:9216\n"
    "ds_read_b64_tr_b16 v[188:189], v79 offset:8192\n"
    "ds_read_b64_tr_b16 v[190:191], v79 offset:9216\n"
    "s_add_u32 m0, s34, 98304\n"
    "s_nop 0\n"
    "global_load_lds_dwordx4 v52, s[36:37] offset:0\n"
    "global_load_lds_dwordx4 v53, s[36:37] offset:1024\n"
    "global_load_lds_dwordx4 v54, s[36:37] offset:2048\n"
    "global_load_lds_dwordx4 v55, s[36:37] offset:3072\n"
    "s_waitcnt lgkmcnt(0)\n"
    "s_barrier\n"
    "v_mfma_f32_16x16x32_bf16 v[192:195], v[144:147], v[128:131], 0\n"
    "v_lshlrev_b32 v100, 7, v100\n"
    "v_mfma_f32_16x16x32_bf16 v[192:195], v[176:179], v[160:163], v[192:195]\n"
    "v_add_u32_dpp v52, v99, v48 quad_perm:[0,0,0,0] row_mask:0xf bank_mask:0xf\n"
    "v_mfma_f32_16x16x32_bf16 v[196:199], v[176:179], v[164:167], 0\n"
    "v_mfma_f32_16x16x32_bf16 v[196:199], v[144:147], v[132:135], v[196:199]\n"
    "v_add_u32_dpp v53, v99, v49 quad_perm:[1,1,1,1] row_mask:0xf bank_mask:0xf\n"
    "v_mfma_f32_16x16x32_bf16 v[200:203], v[144:147], v[136:139], 0\n"
    "v_mfma_f32_16x16x32_bf16 v[200:203], v[176:179], v[168:171], v[200:203]\n"
    "v_add_u32_dpp v54, v99, v50 quad_perm:[2,2,2,2] row_mask:0xf bank_mask:0xf\n"
    "v_mfma_f32_16x16x32_bf16 v[204:207], v[176:179], v[172:175], 0\n"
    "v_mfma_f32_16x16x32_bf16 v[204:207], v[144:147], v[140:143], v[204:207]\n"
    "v_add_u32_dpp v55, v99, v51 quad_perm:[3,3,3,3] row_mask:0xf bank_mask:0xf\n"
    "v_mfma_f32_16x16x32_bf16 v[220:223], v[148:151], v[140:143], 0\n"
    "v_mfma_f32_16x16x32_bf16 v[220:223], v[180:183], v[172:175], v[220:223]\n"
    "s_and_b32 s40, s20, 2\n"
    "v_mfma_f32_16x16x32_bf16 v[216:219], v[180:183], v[168:171], 0\n"
    "v_mfma_f32_16x16x32_bf16 v[216:219], v[148:151], v[136:139], v[216:219]\n"
    "s_lshl2_add_u32 s40, s23, s40\n"
    "v_mfma_f32_16x16x32_bf16 v[212:215], v[148:151], v[132:135], 0\n"
    "v_mfma_f32_16x16x32_bf16 v[212:215], v[180:183], v[164:167], v[212:215]\n"
    "s_lshl_b32 s40, s40, 6\n"
    "v_mfma_f32_16x16x32_bf16 v[208:211], v[180:183], v[160:163], 0\n"
    "v_mfma_f32_16x16x32_bf16 v[208:211], v[148:151], v[128:131], v[208:211]\n"
    "v_lshl_add_u32 v42, v85, 3, s40\n"
    "v_mfma_f32_16x16x32_bf16 v[224:227], v[152:155], v[128:131], 0\n"
    "v_mfma_f32_16x16x32_bf16 v[224:227], v[184:187], v[160:163], v[224:227]\n"
    "s_lshl_b32 s24, s24, 8\n"
    "v_mfma_f32_16x16x32_bf16 v[228:231], v[184:187], v[164:167], 0\n"
    "v_mfma_f32_16x16x32_bf16 v[228:231], v[152:155], v[132:135], v[228:231]\n"
    "v_add_u32 v58, 98304, v56\n"
    "v_mfma_f32_16x16x32_bf16 v[232:235], v[152:155], v[136:139], 0\n"
    "v_mfma_f32_16x16x32_bf16 v[232:235], v[184:187], v[168:171], v[232:235]\n"
    "v_add_u32 v59, 98304, v57\n"
    "v_mfma_f32_16x16x32_bf16 v[236:239], v[184:187], v[172:175], 0\n"
    "v_mfma_f32_16x16x32_bf16 v[236:239], v[152:155], v[140:143], v[236:239]\n"
    "v_add_u32 v80, 98304, v76\n"
    "v_mfma_f32_16x16x32_bf16 v[252:255], v[156:159], v[140:143], 0\n"
    "v_mfma_f32_16x16x32_bf16 v[252:255], v[188:191], v[172:175], v[252:255]\n"
    "v_add_u32 v81, 98304, v77\n"
    "v_mfma_f32_16x16x32_bf16 v[248:251], v[188:191], v[168:171], 0\n"
    "v_mfma_f32_16x16x32_bf16 v[248:251], v[156:159], v[136:139], v[248:251]\n"
    "v_add_u32 v82, 98304, v78\n"
    "v_mfma_f32_16x16x32_bf16 v[244:247], v[156:159], v[132:135], 0\n"
    "v_mfma_f32_16x16x32_bf16 v[244:247], v[188:191], v[164:167], v[244:247]\n"
    "v_add_u32 v83, 98304, v79\n"
    "v_mfma_f32_16x16x32_bf16 v[240:243], v[188:191], v[160:163], 0\n"
    "v_mfma_f32_16x16x32_bf16 v[240:243], v[156:159], v[128:131], v[240:243]\n"
    "s_waitcnt vmcnt(4)\n"
    "s_barrier\n"
    "s_add_u32 m0, s35, 0\n"
    "s_nop 0\n"
    "global_load_lds_dwordx4 v46, s[30:31] offset:0\n"
    "ds_read_b128 v[128:131], v56 offset:49152\n"
    "ds_read_b128 v[132:135], v56 offset:51200\n"
    "ds_read_b128 v[136:139], v56 offset:53248\n"
    "ds_read_b128 v[140:143], v56 offset:55296\n"
    "global_load_lds_dwordx4 v47, s[30:31] offset:1024\n"
    "s_bfe_u32 s66, s28, 0x80000\n"
    "s_mul_i32 s66, s66, 53248\n"
    "s_add_u32 s30, s6, s66\n"
    "s_addc_u32 s31, s7, 0\n"
    "ds_read_b64_tr_b16 v[144:145], v76 offset:49152\n"
    "ds_read_b64_tr_b16 v[146:147], v76 offset:50176\n"
    "ds_read_b64_tr_b16 v[148:149], v77 offset:49152\n"
    "ds_read_b64_tr_b16 v[150:151], v77 offset:50176\n"
    "s_add_u32 m0, s34, 0\n"
    "s_nop 0\n"
    "global_load_lds_dwordx4 v52, s[36:37] offset:0\n"
    "ds_read_b64_tr_b16 v[152:153], v78 offset:49152\n"
    "ds_read_b64_tr_b16 v[154:155], v78 offset:50176\n"
    "ds_read_b64_tr_b16 v[156:157], v79 offset:49152\n"
    "ds_read_b64_tr_b16 v[158:159], v79 offset:50176\n"
    "global_load_lds_dwordx4 v53, s[36:37] offset:1024\n"
    "ds_read_b128 v[160:163], v57 offset:49152\n"
    "ds_read_b128 v[164:167], v57 offset:51200\n"
    "ds_read_b128 v[168:171], v57 offset:53248\n"
    "ds_read_b128 v[172:175], v57 offset:55296\n"
    "global_load_lds_dwordx4 v54, s[36:37] offset:2048\n"
    "ds_read_b64_tr_b16 v[176:177], v76 offset:57344\n"
    "ds_read_b64_tr_b16 v[178:179], v76 offset:58368\n"
    "ds_read_b64_tr_b16 v[180:181], v77 offset:57344\n"
    "ds_read_b64_tr_b16 v[182:183], v77 offset:58368\n"
    "global_load_lds_dwordx4 v55, s[36:37] offset:3072\n"
    "ds_read_b64_tr_b16 v[184:185], v78 offset:57344\n"
    "ds_read_b64_tr_b16 v[186:187], v78 offset:58368\n"
    "ds_read_b64_tr_b16 v[188:189], v79 offset:57344\n"
    "ds_read_b64_tr_b16 v[190:191], v79 offset:58368\n"
    "s_waitcnt lgkmcnt(0)\n"
    "s_barrier\n"
    "v_mfma_f32_16x16x32_bf16 v[192:195], v[144:147], v[128:131], v[192:195]\n"
    "v_lshlrev_b32 v101, 7, v101\n"
    "v_mfma_f32_16x16x32_bf16 v[192:195], v[176:179], v[160:163], v[192:195]\n"
    "v_mfma_f32_16x16x32_bf16 v[196:199], v[176:179], v[164:167], v[196:199]\n"
    "v_add_u32_dpp v52, v100, v48 quad_perm:[0,0,0,0] row_mask:0xf bank_mask:0xf\n"
    "v_mfma_f32_16x16x32_bf16 v[196:199], v[144:147], v[132:135], v[196:199]\n"
    "v_mfma_f32_16x16x32_bf16 v[200:203], v[144:147], v[136:139], v[200:203]\n"
    "v_add_u32_dpp v53, v100, v49 quad_perm:[1,1,1,1] row_mask:0xf bank_mask:0xf\n"
    "v_mfma_f32_16x16x32_bf16 v[200:203], v[176:179], v[168:171], v[200:203]\n"
    "v_mfma_f32_16x16x32_bf16 v[204:207], v[176:179], v[172:175], v[204:207]\n"
    "v_add_u32_dpp v54, v100, v50 quad_perm:[2,2,2,2] row_mask:0xf bank_mask:0xf\n"
    "v_mfma_f32_16x16x32_bf16 v[204:207], v[144:147], v[140:143], v[204:207]\n"
    "v_mfma_f32_16x16x32_bf16 v[220:223], v[148:151], v[140:143], v[220:223]\n"
    "v_add_u32_dpp v55, v100, v51 quad_perm:[3,3,3,3] row_mask:0xf bank_mask:0xf\n"
    "v_mfma_f32_16x16x32_bf16 v[220:223], v[180:183], v[172:175], v[220:223]\n"
    "v_mfma_f32_16x16x32_bf16 v[216:219], v[180:183], v[168:171], v[216:219]\n"
    "s_mul_i32 s47, s3, 5042\n"
    "v_mfma_f32_16x16x32_bf16 v[216:219], v[148:151], v[136:139], v[216:219]\n"
    "v_mfma_f32_16x16x32_bf16 v[212:215], v[148:151], v[132:135], v[212:215]\n"
    "s_lshr_b32 s47, s47, 16\n"
    "v_mfma_f32_16x16x32_bf16 v[212:215], v[180:183], v[164:167], v[212:215]\n"
    "v_mfma_f32_16x16x32_bf16 v[208:211], v[180:183], v[160:163], v[208:211]\n"
    "s_lshr_b32 s21, s20, 2\n"
    "v_mfma_f32_16x16x32_bf16 v[208:211], v[148:151], v[128:131], v[208:211]\n"
    "v_mfma_f32_16x16x32_bf16 v[224:227], v[152:155], v[128:131], v[224:227]\n"
    "s_lshl1_add_u32 s22, s21, s20\n"
    "v_mfma_f32_16x16x32_bf16 v[224:227], v[184:187], v[160:163], v[224:227]\n"
    "v_mfma_f32_16x16x32_bf16 v[228:231], v[184:187], v[164:167], v[228:231]\n"
    "s_and_b32 s22, s22, 3\n"
    "v_mfma_f32_16x16x32_bf16 v[228:231], v[152:155], v[132:135], v[228:231]\n"
    "v_mfma_f32_16x16x32_bf16 v[232:235], v[152:155], v[136:139], v[232:235]\n"
    "s_lshl_b32 s58, s21, 8\n"
    "v_mfma_f32_16x16x32_bf16 v[232:235], v[184:187], v[168:171], v[232:235]\n"
    "v_mfma_f32_16x16x32_bf16 v[236:239], v[184:187], v[172:175], v[236:239]\n"
    "s_add_u32 s58, s58, 150528\n"
    "v_mfma_f32_16x16x32_bf16 v[236:239], v[152:155], v[140:143], v[236:239]\n"
    "v_mfma_f32_16x16x32_bf16 v[252:255], v[156:159], v[140:143], v[252:255]\n"
    "s_mov_b32 s40, s47\n"
    "v_mfma_f32_16x16x32_bf16 v[252:255], v[188:191], v[172:175], v[252:255]\n"
    "v_mfma_f32_16x16x32_bf16 v[248:251], v[188:191], v[168:171], v[248:251]\n"
    "s_mul_i32 s54, s40, 512\n"
    "v_mfma_f32_16x16x32_bf16 v[248:251], v[156:159], v[136:139], v[248:251]\n"
    "v_mfma_f32_16x16x32_bf16 v[244:247], v[156:159], v[132:135], v[244:247]\n"
    "s_add_u32 s54, s54, 2\n"
    "v_mfma_f32_16x16x32_bf16 v[244:247], v[188:191], v[164:167], v[244:247]\n"
    "v_mfma_f32_16x16x32_bf16 v[240:243], v[188:191], v[160:163], v[240:243]\n"
    "v_mfma_f32_16x16x32_bf16 v[240:243], v[156:159], v[128:131], v[240:243]\n"
    "s_waitcnt vmcnt(6)\n"
    "s_barrier\n"
    "s_add_u32 m0, s35, 49152\n"
    "s_nop 0\n"
    "global_load_lds_dwordx4 v46, s[30:31] offset:0\n"
    "ds_read_b128 v[128:131], v58 offset:0\n"
    "ds_read_b128 v[132:135], v58 offset:2048\n"
    "ds_read_b128 v[136:139], v58 offset:4096\n"
    "ds_read_b128 v[140:143], v58 offset:6144\n"
    "global_load_lds_dwordx4 v47, s[30:31] offset:1024\n"
    "s_add_u32 s30, s30, 212992\n"
    "s_addc_u32 s31, s31, 0\n"
    "ds_read_b64_tr_b16 v[144:145], v80 offset:0\n"
    "ds_read_b64_tr_b16 v[146:147], v80 offset:1024\n"
    "ds_read_b64_tr_b16 v[148:149], v81 offset:0\n"
    "ds_read_b64_tr_b16 v[150:151], v81 offset:1024\n"
    "s_add_u32 m0, s34, 49152\n"
    "s_nop 0\n"
    "global_load_lds_dwordx4 v52, s[36:37] offset:0\n"
    "ds_read_b64_tr_b16 v[152:153], v82 offset:0\n"
    "ds_read_b64_tr_b16 v[154:155], v82 offset:1024\n"
    "ds_read_b64_tr_b16 v[156:157], v83 offset:0\n"
    "ds_read_b64_tr_b16 v[158:159], v83 offset:1024\n"
    "global_load_lds_dwordx4 v53, s[36:37] offset:1024\n"
    "ds_read_b128 v[160:163], v59 offset:0\n"
    "ds_read_b128 v[164:167], v59 offset:2048\n"
    "ds_read_b128 v[168:171], v59 offset:4096\n"
    "ds_read_b128 v[172:175], v59 offset:6144\n"
    "global_load_lds_dwordx4 v54, s[36:37] offset:2048\n"
    "ds_read_b64_tr_b16 v[176:177], v80 offset:8192\n"
    "ds_read_b64_tr_b16 v[178:179], v80 offset:9216\n"
    "ds_read_b64_tr_b16 v[180:181], v81 offset:8192\n"
    "ds_read_b64_tr_b16 v[182:183], v81 offset:9216\n"
    "global_load_lds_dwordx4 v55, s[36:37] offset:3072\n"
    "ds_read_b64_tr_b16 v[184:185], v82 offset:8192\n"
    "ds_read_b64_tr_b16 v[186:187], v82 offset:9216\n"
    "ds_read_b64_tr_b16 v[188:189], v83 offset:8192\n"
    "ds_read_b64_tr_b16 v[190:191], v83 offset:9216\n"
    "s_waitcnt vmcnt(10) lgkmcnt(0)\n"
    "s_barrier\n"
    "v_mfma_f32_16x16x32_bf16 v[192:195], v[144:147], v[128:131], v[192:195]\n"
    "v_lshlrev_b32 v102, 7, v102\n"
    "v_mfma_f32_16x16x32_bf16 v[192:195], v[176:179], v[160:163], v[192:195]\n"
    "v_mfma_f32_16x16x32_bf16 v[196:199], v[176:179], v[164:167], v[196:199]\n"
    "v_add_u32_dpp v52, v101, v48 quad_perm:[0,0,0,0] row_mask:0xf bank_mask:0xf\n"
    "v_mfma_f32_16x16x32_bf16 v[196:199], v[144:147], v[132:135], v[196:199]\n"
    "v_mfma_f32_16x16x32_bf16 v[200:203], v[144:147], v[136:139], v[200:203]\n"
    "v_add_u32_dpp v53, v101, v49 quad_perm:[1,1,1,1] row_mask:0xf bank_mask:0xf\n"
    "v_mfma_f32_16x16x32_bf16 v[200:203], v[176:179], v[168:171], v[200:203]\n"
    "v_mfma_f32_16x16x32_bf16 v[204:207], v[176:179], v[172:175], v[204:207]\n"
    "v_add_u32_dpp v54, v101, v50 quad_perm:[2,2,2,2] row_mask:0xf bank_mask:0xf\n"
    "v_mfma_f32_16x16x32_bf16 v[204:207], v[144:147], v[140:143], v[204:207]\n"
    "v_mfma_f32_16x16x32_bf16 v[220:223], v[148:151], v[140:143], v[220:223]\n"
    "v_add_u32_dpp v55, v101, v51 quad_perm:[3,3,3,3] row_mask:0xf bank_mask:0xf\n"
    "v_mfma_f32_16x16x32_bf16 v[220:223], v[180:183], v[172:175], v[220:223]\n"
    "v_mfma_f32_16x16x32_bf16 v[216:219], v[180:183], v[168:171], v[216:219]\n"
    "s_lshl_b32 s40, s40, 7\n"
    "v_mfma_f32_16x16x32_bf16 v[216:219], v[148:151], v[136:139], v[216:219]\n"
    "v_mfma_f32_16x16x32_bf16 v[212:215], v[148:151], v[132:135], v[212:215]\n"
    "s_add_u32 s40, s40, 32768\n"
    "v_mfma_f32_16x16x32_bf16 v[212:215], v[180:183], v[164:167], v[212:215]\n"
    "v_mfma_f32_16x16x32_bf16 v[208:211], v[180:183], v[160:163], v[208:211]\n"
    "s_add_u32 s52, s16, s40\n"
    "s_addc_u32 s53, s17, 0\n"
    "v_mfma_f32_16x16x32_bf16 v[208:211], v[148:151], v[128:131], v[208:211]\n"
    "v_mfma_f32_16x16x32_bf16 v[224:227], v[152:155], v[128:131], v[224:227]\n"
    "s_mov_b32 s57, 0x7fff7fff\n"
    "v_mfma_f32_16x16x32_bf16 v[224:227], v[184:187], v[160:163], v[224:227]\n"
    "v_mfma_f32_16x16x32_bf16 v[228:231], v[184:187], v[164:167], v[228:231]\n"
    "s_mov_b32 s56, 0\n"
    "v_mfma_f32_16x16x32_bf16 v[228:231], v[152:155], v[132:135], v[228:231]\n"
    "v_mfma_f32_16x16x32_bf16 v[232:235], v[152:155], v[136:139], v[232:235]\n"
    "s_mov_b32 s27, 0\n"
    "v_mfma_f32_16x16x32_bf16 v[232:235], v[184:187], v[168:171], v[232:235]\n"
    "v_mfma_f32_16x16x32_bf16 v[236:239], v[184:187], v[172:175], v[236:239]\n"
    "s_mov_b32 s55, 9\n"
    "v_mfma_f32_16x16x32_bf16 v[236:239], v[152:155], v[140:143], v[236:239]\n"
    "v_mfma_f32_16x16x32_bf16 v[252:255], v[156:159], v[140:143], v[252:255]\n"
    "s_mov_b32 s44, 0\n"
    "v_mfma_f32_16x16x32_bf16 v[252:255], v[188:191], v[172:175], v[252:255]\n"
    "v_mfma_f32_16x16x32_bf16 v[248:251], v[188:191], v[168:171], v[248:251]\n"
    "s_mov_b32 s46, 0\n"
    "v_mfma_f32_16x16x32_bf16 v[248:251], v[156:159], v[136:139], v[248:251]\n"
    "v_mfma_f32_16x16x32_bf16 v[244:247], v[156:159], v[132:135], v[244:247]\n"
    "v_lshrrev_b32 v17, 3, v1\n"
    "v_mfma_f32_16x16x32_bf16 v[244:247], v[188:191], v[164:167], v[244:247]\n"
    "v_mfma_f32_16x16x32_bf16 v[240:243], v[188:191], v[160:163], v[240:243]\n"
    "v_mfma_f32_16x16x32_bf16 v[240:243], v[156:159], v[128:131], v[240:243]\n"
    "s_waitcnt vmcnt(6)\n"
    "s_barrier\n"
    "s_add_u32 m0, s35, 98304\n"
    "s_nop 0\n"
    "global_load_lds_dwordx4 v46, s[30:31] offset:0\n"
    "ds_read_b128 v[128:131], v56 offset:0\n"
    "ds_read_b128 v[132:135], v56 offset:2048\n"
    "ds_read_b128 v[136:139], v56 offset:4096\n"
    "ds_read_b128 v[140:143], v56 offset:6144\n"
    "global_load_lds_dwordx4 v47, s[30:31] offset:1024\n"
    "s_add_u32 s30, s30, 212992\n"
    "s_addc_u32 s31, s31, 0\n"
    "ds_read_b64_tr_b16 v[144:145], v76 offset:0\n"
    "ds_read_b64_tr_b16 v[146:147], v76 offset:1024\n"
    "ds_read_b64_tr_b16 v[148:149], v77 offset:0\n"
    "ds_read_b64_tr_b16 v[150:151], v77 offset:1024\n"
    "s_add_u32 m0, s34, 98304\n"
    "s_nop 0\n"
    "global_load_lds_dwordx4 v52, s[36:37] offset:0\n"
    "ds_read_b64_tr_b16 v[152:153], v78 offset:0\n"
    "ds_read_b64_tr_b16 v[154:155], v78 offset:1024\n"
    "ds_read_b64_tr_b16 v[156:157], v79 offset:0\n"
    "ds_read_b64_tr_b16 v[158:159], v79 offset:1024\n"
    "global_load_lds_dwordx4 v53, s[36:37] offset:1024\n"
    "ds_read_b128 v[160:163], v57 offset:0\n"
    "ds_read_b128 v[164:167], v57 offset:2048\n"
    "ds_read_b128 v[168:171], v57 offset:4096\n"
    "ds_read_b128 v[172:175], v57 offset:6144\n"
    "global_load_lds_dwordx4 v54, s[36:37] offset:2048\n"
    "ds_read_b64_tr_b16 v[176:177], v76 offset:8192\n"
    "ds_read_b64_tr_b16 v[178:179], v76 offset:9216\n"
    "ds_read_b64_tr_b16 v[180:181], v77 offset:8192\n"
    "ds_read_b64_tr_b16 v[182:183], v77 offset:9216\n"
    "global_load_lds_dwordx4 v55, s[36:37] offset:3072\n"
    "ds_read_b64_tr_b16 v[184:185], v78 offset:8192\n"
    "ds_read_b64_tr_b16 v[186:187], v78 offset:9216\n"
    "ds_read_b64_tr_b16 v[188:189], v79 offset:8192\n"
    "ds_read_b64_tr_b16 v[190:191], v79 offset:9216\n"
    "s_waitcnt vmcnt(10) lgkmcnt(0)\n"
    "s_barrier\n"
    "v_mfma_f32_16x16x32_bf16 v[192:195], v[144:147], v[128:131], v[192:195]\n"
    "v_lshlrev_b32 v103, 7, v103\n"
    "v_mfma_f32_16x16x32_bf16 v[192:195], v[176:179], v[160:163], v[192:195]\n"
    "v_mfma_f32_16x16x32_bf16 v[196:199], v[176:179], v[164:167], v[196:199]\n"
    "v_add_u32_dpp v52, v102, v48 quad_perm:[0,0,0,0] row_mask:0xf bank_mask:0xf\n"
    "v_mfma_f32_16x16x32_bf16 v[196:199], v[144:147], v[132:135], v[196:199]\n"
    "v_mfma_f32_16x16x32_bf16 v[200:203], v[144:147], v[136:139], v[200:203]\n"
    "v_add_u32_dpp v53, v102, v49 quad_perm:[1,1,1,1] row_mask:0xf bank_mask:0xf\n"
    "v_mfma_f32_16x16x32_bf16 v[200:203], v[176:179], v[168:171], v[200:203]\n"
    "v_mfma_f32_16x16x32_bf16 v[204:207], v[176:179], v[172:175], v[204:207]\n"
    "v_add_u32_dpp v54, v102, v50 quad_perm:[2,2,2,2] row_mask:0xf bank_mask:0xf\n"
    "v_mfma_f32_16x16x32_bf16 v[204:207], v[144:147], v[140:143], v[204:207]\n"
    "v_mfma_f32_16x16x32_bf16 v[220:223], v[148:151], v[140:143], v[220:223]\n"
    "v_add_u32_dpp v55, v102, v51 quad_perm:[3,3,3,3] row_mask:0xf bank_mask:0xf\n"
    "v_mfma_f32_16x16x32_bf16 v[220:223], v[180:183], v[172:175], v[220:223]\n"
    "v_mfma_f32_16x16x32_bf16 v[216:219], v[180:183], v[168:171], v[216:219]\n"
    "v_mul_u32_u24 v17, 12, v17\n"
    "v_mfma_f32_16x16x32_bf16 v[216:219], v[148:151], v[136:139], v[216:219]\n"
    "v_mfma_f32_16x16x32_bf16 v[212:215], v[148:151], v[132:135], v[212:215]\n"
    "v_and_b32 v19, 7, v1\n"
    "v_mfma_f32_16x16x32_bf16 v[212:215], v[180:183], v[164:167], v[212:215]\n"
    "v_mfma_f32_16x16x32_bf16 v[208:211], v[180:183], v[160:163], v[208:211]\n"
    "v_lshlrev_b32 v19, 4, v19\n"
    "v_mfma_f32_16x16x32_bf16 v[208:211], v[148:151], v[128:131], v[208:211]\n"
    "v_mfma_f32_16x16x32_bf16 v[224:227], v[152:155], v[128:131], v[224:227]\n"
    "v_lshrrev_b32 v18, 5, v1\n"
    "v_mfma_f32_16x16x32_bf16 v[224:227], v[184:187], v[160:163], v[224:227]\n"
    "v_mfma_f32_16x16x32_bf16 v[228:231], v[184:187], v[164:167], v[228:231]\n"
    "v_lshlrev_b32 v18, 2, v18\n"
    "v_mfma_f32_16x16x32_bf16 v[228:231], v[152:155], v[132:135], v[228:231]\n"
    "v_mfma_f32_16x16x32_bf16 v[232:235], v[152:155], v[136:139], v[232:235]\n"
    "v_and_b32 v20, 31, v1\n"
    "v_mfma_f32_16x16x32_bf16 v[232:235], v[184:187], v[168:171], v[232:235]\n"
    "v_mfma_f32_16x16x32_bf16 v[236:239], v[184:187], v[172:175], v[236:239]\n"
    "v_lshlrev_b32 v20, 2, v20\n"
    "v_mfma_f32_16x16x32_bf16 v[236:239], v[152:155], v[140:143], v[236:239]\n"
    "v_mfma_f32_16x16x32_bf16 v[252:255], v[156:159], v[140:143], v[252:255]\n"
    "s_lshl2_add_u32 s40, s22, s58\n"
    "v_mfma_f32_16x16x32_bf16 v[252:255], v[188:191], v[172:175], v[252:255]\n"
    "v_mfma_f32_16x16x32_bf16 v[248:251], v[188:191], v[168:171], v[248:251]\n"
    "s_sub_u32 s40, s40, 20\n"
    "v_mfma_f32_16x16x32_bf16 v[248:251], v[156:159], v[136:139], v[248:251]\n"
    "v_mfma_f32_16x16x32_bf16 v[244:247], v[156:159], v[132:135], v[244:247]\n"
    "v_mov_b32 v25, s40\n"
    "v_mfma_f32_16x16x32_bf16 v[244:247], v[188:191], v[164:167], v[244:247]\n"
    "v_mfma_f32_16x16x32_bf16 v[240:243], v[188:191], v[160:163], v[240:243]\n"
    "v_mfma_f32_16x16x32_bf16 v[240:243], v[156:159], v[128:131], v[240:243]\n"
    "s_waitcnt vmcnt(6)\n"
    "s_barrier\n"
    "s_add_u32 m0, s35, 0\n"
    "s_nop 0\n"
    "global_load_lds_dwordx4 v46, s[30:31] offset:0\n"
    "ds_read_b128 v[128:131], v56 offset:49152\n"
    "ds_read_b128 v[132:135], v56 offset:51200\n"
    "ds_read_b128 v[136:139], v56 offset:53248\n"
    "global_load_lds_dwordx4 v47, s[30:31] offset:1024\n"
    "s_add_u32 s30, s30, 212992\n"
    "s_addc_u32 s31, s31, 0\n"
    "ds_read_b128 v[140:143], v56 offset:55296\n"
    "ds_read_b64_tr_b16 v[144:145], v76 offset:49152\n"
    "ds_read_b64_tr_b16 v[146:147], v76 offset:50176\n"
    "s_add_u32 m0, s34, 0\n"
    "s_nop 0\n"
    "global_load_lds_dwordx4 v52, s[36:37] offset:0\n"
    "ds_read_b64_tr_b16 v[148:149], v77 offset:49152\n"
    "ds_read_b64_tr_b16 v[150:151], v77 offset:50176\n"
    "ds_read_b64_tr_b16 v[152:153], v78 offset:49152\n"
    "global_load_lds_dwordx4 v53, s[36:37] offset:1024\n"
    "ds_read_b64_tr_b16 v[154:155], v78 offset:50176\n"
    "ds_read_b64_tr_b16 v[156:157], v79 offset:49152\n"
    "ds_read_b64_tr_b16 v[158:159], v79 offset:50176\n"
    "global_load_lds_dwordx4 v54, s[36:37] offset:2048\n"
    "ds_read_b128 v[160:163], v57 offset:49152\n"
    "ds_read_b128 v[164:167], v57 offset:51200\n"
    "ds_read_b128 v[168:171], v57 offset:53248\n"
    "global_load_lds_dwordx4 v55, s[36:37] offset:3072\n"
    "ds_read_b128 v[172:175], v57 offset:55296\n"
    "ds_read_b64_tr_b16 v[176:177], v76 offset:57344\n"
    "ds_read_b64_tr_b16 v[178:179], v76 offset:58368\n"
    "global_load_dwordx2 v[84:85], v42, s[10:11] offset:0\n"
    "ds_read_b64_tr_b16 v[180:181], v77 offset:57344\n"
    "ds_read_b64_tr_b16 v[182:183], v77 offset:58368\n"
    "ds_read_b64_tr_b16 v[184:185], v78 offset:57344\n"
    "ds_read_b64_tr_b16 v[186:187], v78 offset:58368\n"
    "ds_read_b64_tr_b16 v[188:189], v79 offset:57344\n"
    "ds_read_b64_tr_b16 v[190:191], v79 offset:58368\n"
    "s_waitcnt vmcnt(11) lgkmcnt(0)\n"
    "s_barrier\n"
    "v_mfma_f32_16x16x32_bf16 v[192:195], v[144:147], v[128:131], v[192:195]\n"
    "v_lshlrev_b32 v104, 7, v104\n"
    "v_mfma_f32_16x16x32_bf16 v[192:195], v[176:179], v[160:163], v[192:195]\n"
    "v_mfma_f32_16x16x32_bf16 v[196:199], v[176:179], v[164:167], v[196:199]\n"
    "v_add_u32_dpp v52, v103, v48 quad_perm:[0,0,0,0] row_mask:0xf bank_mask:0xf\n"
    "v_mfma_f32_16x16x32_bf16 v[196:199], v[144:147], v[132:135], v[196:199]\n"
    "v_mfma_f32_16x16x32_bf16 v[200:203], v[144:147], v[136:139], v[200:203]\n"
    "v_add_u32_dpp v53, v103, v49 quad_perm:[1,1,1,1] row_mask:0xf bank_mask:0xf\n"
    "v_mfma_f32_16x16x32_bf16 v[200:203], v[176:179], v[168:171], v[200:203]\n"
    "v_mfma_f32_16x16x32_bf16 v[204:207], v[176:179], v[172:175], v[204:207]\n"
    "v_add_u32_dpp v54, v103, v50 quad_perm:[2,2,2,2] row_mask:0xf bank_mask:0xf\n"
    "v_mfma_f32_16x16x32_bf16 v[204:207], v[144:147], v[140:143], v[204:207]\n"
    "v_mfma_f32_16x16x32_bf16 v[220:223], v[148:151], v[140:143], v[220:223]\n"
    "v_add_u32_dpp v55, v103, v51 quad_perm:[3,3,3,3] row_mask:0xf bank_mask:0xf\n"
    "v_mfma_f32_16x16x32_bf16 v[220:223], v[180:183], v[172:175], v[220:223]\n"
    "v_mfma_f32_16x16x32_bf16 v[216:219], v[180:183], v[168:171], v[216:219]\n"
    "s_lshl_b32 s43, s21, 16\n"
    "v_mfma_f32_16x16x32_bf16 v[216:219], v[148:151], v[136:139], v[216:219]\n"
    "v_mfma_f32_16x16x32_bf16 v[212:215], v[148:151], v[132:135], v[212:215]\n"
    "v_lshrrev_b32 v12, 4, v1\n"
    "v_mfma_f32_16x16x32_bf16 v[212:215], v[180:183], v[164:167], v[212:215]\n"
    "v_mfma_f32_16x16x32_bf16 v[208:211], v[180:183], v[160:163], v[208:211]\n"
    "v_bfe_u32 v13, v1, 2, 2\n"
    "v_mfma_f32_16x16x32_bf16 v[208:211], v[148:151], v[128:131], v[208:211]\n"
    "v_mfma_f32_16x16x32_bf16 v[224:227], v[152:155], v[128:131], v[224:227]\n"
    "v_and_b32 v21, 3, v1\n"
    "v_mfma_f32_16x16x32_bf16 v[224:227], v[184:187], v[160:163], v[224:227]\n"
    "v_mfma_f32_16x16x32_bf16 v[228:231], v[184:187], v[164:167], v[228:231]\n"
    "s_lshl_b32 s40, s22, 5\n"
    "v_mfma_f32_16x16x32_bf16 v[228:231], v[152:155], v[132:135], v[228:231]\n"
    "v_mfma_f32_16x16x32_bf16 v[232:235], v[152:155], v[136:139], v[232:235]\n"
    "s_add_u32 s40, s40, s43\n"
    "v_mfma_f32_16x16x32_bf16 v[232:235], v[184:187], v[168:171], v[232:235]\n"
    "v_mfma_f32_16x16x32_bf16 v[236:239], v[184:187], v[172:175], v[236:239]\n"
    "v_lshl_add_u32 v21, v21, 3, s40\n"
    "v_mfma_f32_16x16x32_bf16 v[236:239], v[152:155], v[140:143], v[236:239]\n"
    "v_mfma_f32_16x16x32_bf16 v[252:255], v[156:159], v[140:143], v[252:255]\n"
    "s_movk_i32 s41, 144\n"
    "v_mfma_f32_16x16x32_bf16 v[252:255], v[188:191], v[172:175], v[252:255]\n"
    "v_mfma_f32_16x16x32_bf16 v[248:251], v[188:191], v[168:171], v[248:251]\n"
    "v_add_u32 v22, v12, v13\n"
    "v_mfma_f32_16x16x32_bf16 v[248:251], v[156:159], v[136:139], v[248:251]\n"
    "v_mfma_f32_16x16x32_bf16 v[244:247], v[156:159], v[132:135], v[244:247]\n"
    "v_and_b32 v22, 3, v22\n"
    "v_mfma_f32_16x16x32_bf16 v[244:247], v[188:191], v[164:167], v[244:247]\n"
    "v_mfma_f32_16x16x32_bf16 v[240:243], v[188:191], v[160:163], v[240:243]\n"
    "v_mfma_f32_16x16x32_bf16 v[240:243], v[156:159], v[128:131], v[240:243]\n"
    "s_waitcnt vmcnt(7)\n"
    "s_barrier\n"
    "s_add_u32 m0, s35, 49152\n"
    "s_nop 0\n"
    "global_load_lds_dwordx4 v46, s[30:31] offset:0\n"
    "ds_read_b128 v[128:131], v58 offset:0\n"
    "ds_read_b128 v[132:135], v58 offset:2048\n"
    "ds_read_b128 v[136:139], v58 offset:4096\n"
    "global_load_lds_dwordx4 v47, s[30:31] offset:1024\n"
    "s_bfe_u32 s66, s28, 0x80008\n"
    "s_mul_i32 s66, s66, 53248\n"
    "s_add_u32 s30, s6, s66\n"
    "s_addc_u32 s31, s7, 0\n"
    "ds_read_b128 v[140:143], v58 offset:6144\n"
    "ds_read_b64_tr_b16 v[144:145], v80 offset:0\n"
    "ds_read_b64_tr_b16 v[146:147], v80 offset:1024\n"
    "s_add_u32 m0, s34, 49152\n"
    "s_nop 0\n"
    "global_load_lds_dwordx4 v52, s[36:37] offset:0\n"
    "ds_read_b64_tr_b16 v[148:149], v81 offset:0\n"
    "ds_read_b64_tr_b16 v[150:151], v81 offset:1024\n"
    "ds_read_b64_tr_b16 v[152:153], v82 offset:0\n"
    "global_load_lds_dwordx4 v53, s[36:37] offset:1024\n"
    "ds_read_b64_tr_b16 v[154:155], v82 offset:1024\n"
    "ds_read_b64_tr_b16 v[156:157], v83 offset:0\n"
    "ds_read_b64_tr_b16 v[158:159], v83 offset:1024\n"
    "global_load_lds_dwordx4 v54, s[36:37] offset:2048\n"
    "ds_read_b128 v[160:163], v59 offset:0\n"
    "ds_read_b128 v[164:167], v59 offset:2048\n"
    "ds_read_b128 v[168:171], v59 offset:4096\n"
    "global_load_lds_dwordx4 v55, s[36:37] offset:3072\n"
    "ds_read_b128 v[172:175], v59 offset:6144\n"
    "ds_read_b64_tr_b16 v[176:177], v80 offset:8192\n"
    "ds_read_b64_tr_b16 v[178:179], v80 offset:9216\n"
    "global_load_dwordx2 v[86:87], v42, s[10:11] offset:32\n"
    "ds_read_b64_tr_b16 v[180:181], v81 offset:8192\n"
    "ds_read_b64_tr_b16 v[182:183], v81 offset:9216\n"
    "ds_read_b64_tr_b16 v[184:185], v82 offset:8192\n"
    "ds_read_b64_tr_b16 v[186:187], v82 offset:9216\n"
    "ds_read_b64_tr_b16 v[188:189], v83 offset:8192\n"
    "ds_read_b64_tr_b16 v[190:191], v83 offset:9216\n"
    "s_waitcnt vmcnt(12) lgkmcnt(0)\n"
    "s_barrier\n"
    "v_mfma_f32_16x16x32_bf16 v[192:195], v[144:147], v[128:131], v[192:195]\n"
    "v_lshlrev_b32 v105, 7, v105\n"
    "v_mfma_f32_16x16x32_bf16 v[192:195], v[176:179], v[160:163], v[192:195]\n"
    "v_mfma_f32_16x16x32_bf16 v[196:199], v[176:179], v[164:167], v[196:199]\n"
    "v_add_u32_dpp v52, v104, v48 quad_perm:[0,0,0,0] row_mask:0xf bank_mask:0xf\n"
    "v_mfma_f32_16x16x32_bf16 v[196:199], v[144:147], v[132:135], v[196:199]\n"
    "v_mfma_f32_16x16x32_bf16 v[200:203], v[144:147], v[136:139], v[200:203]\n"
    "v_add_u32_dpp v53, v104, v49 quad_perm:[1,1,1,1] row_mask:0xf bank_mask:0xf\n"
    "v_mfma_f32_16x16x32_bf16 v[200:203], v[176:179], v[168:171], v[200:203]\n"
    "v_mfma_f32_16x16x32_bf16 v[204:207], v[176:179], v[172:175], v[204:207]\n"
    "v_add_u32_dpp v54, v104, v50 quad_perm:[2,2,2,2] row_mask:0xf bank_mask:0xf\n"
    "v_mfma_f32_16x16x32_bf16 v[204:207], v[144:147], v[140:143], v[204:207]\n"
    "v_mfma_f32_16x16x32_bf16 v[220:223], v[148:151], v[140:143], v[220:223]\n"
    "v_add_u32_dpp v55, v104, v51 quad_perm:[3,3,3,3] row_mask:0xf bank_mask:0xf\n"
    "v_mfma_f32_16x16x32_bf16 v[220:223], v[180:183], v[172:175], v[220:223]\n"
    "v_mfma_f32_16x16x32_bf16 v[216:219], v[180:183], v[168:171], v[216:219]\n"
    "v_mad_u32_u24 v4, v22, s41, v21\n"
    "v_mfma_f32_16x16x32_bf16 v[216:219], v[148:151], v[136:139], v[216:219]\n"
    "v_mfma_f32_16x16x32_bf16 v[212:215], v[148:151], v[132:135], v[212:215]\n"
    "v_cmp_gt_u32 vcc, 2, v13\n"
    "v_mfma_f32_16x16x32_bf16 v[212:215], v[180:183], v[164:167], v[212:215]\n"
    "v_mfma_f32_16x16x32_bf16 v[208:211], v[180:183], v[160:163], v[208:211]\n"
    "v_add_u32 v24, 22, v13\n"
    "v_mfma_f32_16x16x32_bf16 v[208:211], v[148:151], v[128:131], v[208:211]\n"
    "v_mfma_f32_16x16x32_bf16 v[224:227], v[152:155], v[128:131], v[224:227]\n"
    "v_lshlrev_b32 v13, 3, v13\n"
    "v_mfma_f32_16x16x32_bf16 v[224:227], v[184:187], v[160:163], v[224:227]\n"
    "v_mfma_f32_16x16x32_bf16 v[228:231], v[184:187], v[164:167], v[228:231]\n"
    "v_cndmask_b32 v22, v24, v22, vcc\n"
    "v_mfma_f32_16x16x32_bf16 v[228:231], v[152:155], v[132:135], v[228:231]\n"
    "v_mfma_f32_16x16x32_bf16 v[232:235], v[152:155], v[136:139], v[232:235]\n"
    "v_mad_u32_u24 v3, v22, s41, v21\n"
    "v_mfma_f32_16x16x32_bf16 v[232:235], v[184:187], v[168:171], v[232:235]\n"
    "v_mfma_f32_16x16x32_bf16 v[236:239], v[184:187], v[172:175], v[236:239]\n"
    "v_cmp_eq_u32 vcc, 3, v12\n"
    "v_mfma_f32_16x16x32_bf16 v[236:239], v[152:155], v[140:143], v[236:239]\n"
    "v_mfma_f32_16x16x32_bf16 v[252:255], v[156:159], v[140:143], v[252:255]\n"
    "v_xor_b32 v22, 0, v12\n"
    "v_mfma_f32_16x16x32_bf16 v[252:255], v[188:191], v[172:175], v[252:255]\n"
    "v_mfma_f32_16x16x32_bf16 v[248:251], v[188:191], v[168:171], v[248:251]\n"
    "v_and_b32 v22, 1, v22\n"
    "v_mfma_f32_16x16x32_bf16 v[248:251], v[156:159], v[136:139], v[248:251]\n"
    "v_mfma_f32_16x16x32_bf16 v[244:247], v[156:159], v[132:135], v[244:247]\n"
    "v_lshlrev_b32 v22, 1, v22\n"
    "v_mfma_f32_16x16x32_bf16 v[244:247], v[188:191], v[164:167], v[244:247]\n"
    "v_mfma_f32_16x16x32_bf16 v[240:243], v[188:191], v[160:163], v[240:243]\n"
    "v_mfma_f32_16x16x32_bf16 v[240:243], v[156:159], v[128:131], v[240:243]\n"
    "s_waitcnt vmcnt(8)\n"
    "s_barrier\n"
    "s_add_u32 m0, s35, 98304\n"
    "s_nop 0\n"
    "global_load_lds_dwordx4 v46, s[30:31] offset:0\n"
    "ds_read_b128 v[128:131], v56 offset:0\n"
    "ds_read_b128 v[132:135], v56 offset:2048\n"
    "ds_read_b128 v[136:139], v56 offset:4096\n"
    "global_load_lds_dwordx4 v47, s[30:31] offset:1024\n"
    "s_add_u32 s30, s30, 212992\n"
    "s_addc_u32 s31, s31, 0\n"
    "ds_read_b128 v[140:143], v56 offset:6144\n"
    "ds_read_b64_tr_b16 v[144:145], v76 offset:0\n"
    "ds_read_b64_tr_b16 v[146:147], v76 offset:1024\n"
    "s_add_u32 m0, s34, 98304\n"
    "s_nop 0\n"
    "global_load_lds_dwordx4 v52, s[36:37] offset:0\n"
    "ds_read_b64_tr_b16 v[148:149], v77 offset:0\n"
    "ds_read_b64_tr_b16 v[150:151], v77 offset:1024\n"
    "ds_read_b64_tr_b16 v[152:153], v78 offset:0\n"
    "global_load_lds_dwordx4 v53, s[36:37] offset:1024\n"
    "ds_read_b64_tr_b16 v[154:155], v78 offset:1024\n"
    "ds_read_b64_tr_b16 v[156:157], v79 offset:0\n"
    "ds_read_b64_tr_b16 v[158:159], v79 offset:1024\n"
    "global_load_lds_dwordx4 v54, s[36:37] offset:2048\n"
    "ds_read_b128 v[160:163], v57 offset:0\n"
    "ds_read_b128 v[164:167], v57 offset:2048\n"
    "ds_read_b128 v[168:171], v57 offset:4096\n"
    "global_load_lds_dwordx4 v55, s[36:37] offset:3072\n"
    "ds_read_b128 v[172:175], v57 offset:6144\n"
    "ds_read_b64_tr_b16 v[176:177], v76 offset:8192\n"
    "ds_read_b64_tr_b16 v[178:179], v76 offset:9216\n"
    "global_load_dwordx2 v[88:89], v42, s[10:11] offset:64\n"
    "ds_read_b64_tr_b16 v[180:181], v77 offset:8192\n"
    "ds_read_b64_tr_b16 v[182:183], v77 offset:9216\n"
    "ds_read_b64_tr_b16 v[184:185], v78 offset:8192\n"
    "ds_read_b64_tr_b16 v[186:187], v78 offset:9216\n"
    "ds_read_b64_tr_b16 v[188:189], v79 offset:8192\n"
    "ds_read_b64_tr_b16 v[190:191], v79 offset:9216\n"
    "s_waitcnt vmcnt(12) lgkmcnt(0)\n"
    "s_barrier\n"
    "v_mfma_f32_16x16x32_bf16 v[192:195], v[144:147], v[128:131], v[192:195]\n"
    "v_lshlrev_b32 v106, 7, v106\n"
    "v_mfma_f32_16x16x32_bf16 v[192:195], v[176:179], v[160:163], v[192:195]\n"
    "v_mfma_f32_16x16x32_bf16 v[196:199], v[176:179], v[164:167], v[196:199]\n"
    "v_add_u32_dpp v52, v105, v48 quad_perm:[0,0,0,0] row_mask:0xf bank_mask:0xf\n"
    "v_mfma_f32_16x16x32_bf16 v[196:199], v[144:147], v[132:135], v[196:199]\n"
    "v_mfma_f32_16x16x32_bf16 v[200:203], v[144:147], v[136:139], v[200:203]\n"
    "v_add_u32_dpp v53, v105, v49 quad_perm:[1,1,1,1] row_mask:0xf bank_mask:0xf\n"
    "v_mfma_f32_16x16x32_bf16 v[200:203], v[176:179], v[168:171], v[200:203]\n"
    "v_mfma_f32_16x16x32_bf16 v[204:207], v[176:179], v[172:175], v[204:207]\n"
    "v_add_u32_dpp v54, v105, v50 quad_perm:[2,2,2,2] row_mask:0xf bank_mask:0xf\n"
    "v_mfma_f32_16x16x32_bf16 v[204:207], v[144:147], v[140:143], v[204:207]\n"
    "v_mfma_f32_16x16x32_bf16 v[220:223], v[148:151], v[140:143], v[220:223]\n"
    "v_add_u32_dpp v55, v105, v51 quad_perm:[3,3,3,3] row_mask:0xf bank_mask:0xf\n"
    "v_mfma_f32_16x16x32_bf16 v[220:223], v[180:183], v[172:175], v[220:223]\n"
    "v_mfma_f32_16x16x32_bf16 v[216:219], v[180:183], v[168:171], v[216:219]\n"
    "v_lshl_add_u32 v22, v12, 3, v22\n"
    "v_mfma_f32_16x16x32_bf16 v[216:219], v[148:151], v[136:139], v[216:219]\n"
    "v_mfma_f32_16x16x32_bf16 v[212:215], v[148:151], v[132:135], v[212:215]\n"
    "s_mov_b32 s40, 0x05040100\n"
    "v_mfma_f32_16x16x32_bf16 v[212:215], v[180:183], v[164:167], v[212:215]\n"
    "v_mfma_f32_16x16x32_bf16 v[208:211], v[180:183], v[160:163], v[208:211]\n"
    "v_bfe_u32 v24, s40, v13, 8\n"
    "v_mfma_f32_16x16x32_bf16 v[208:211], v[148:151], v[128:131], v[208:211]\n"
    "v_mfma_f32_16x16x32_bf16 v[224:227], v[152:155], v[128:131], v[224:227]\n"
    "v_add_u32 v22, v22, v24\n"
    "v_mfma_f32_16x16x32_bf16 v[224:227], v[184:187], v[160:163], v[224:227]\n"
    "v_mfma_f32_16x16x32_bf16 v[228:231], v[184:187], v[164:167], v[228:231]\n"
    "s_mov_b32 s40, 0x1a191818\n"
    "v_mfma_f32_16x16x32_bf16 v[228:231], v[152:155], v[132:135], v[228:231]\n"
    "v_mfma_f32_16x16x32_bf16 v[232:235], v[152:155], v[136:139], v[232:235]\n"
    "v_bfe_u32 v24, s40, v13, 8\n"
    "v_mfma_f32_16x16x32_bf16 v[232:235], v[184:187], v[168:171], v[232:235]\n"
    "v_mfma_f32_16x16x32_bf16 v[236:239], v[184:187], v[172:175], v[236:239]\n"
    "v_cndmask_b32 v22, v22, v24, vcc\n"
    "v_mfma_f32_16x16x32_bf16 v[236:239], v[152:155], v[140:143], v[236:239]\n"
    "v_mfma_f32_16x16x32_bf16 v[252:255], v[156:159], v[140:143], v[252:255]\n"
    "v_mad_u32_u24 v5, v22, s41, v21\n"
    "v_mfma_f32_16x16x32_bf16 v[252:255], v[188:191], v[172:175], v[252:255]\n"
    "v_mfma_f32_16x16x32_bf16 v[248:251], v[188:191], v[168:171], v[248:251]\n"
    "v_xor_b32 v22, 1, v12\n"
    "v_mfma_f32_16x16x32_bf16 v[248:251], v[156:159], v[136:139], v[248:251]\n"
    "v_mfma_f32_16x16x32_bf16 v[244:247], v[156:159], v[132:135], v[244:247]\n"
    "v_and_b32 v22, 1, v22\n"
    "v_mfma_f32_16x16x32_bf16 v[244:247], v[188:191], v[164:167], v[244:247]\n"
    "v_mfma_f32_16x16x32_bf16 v[240:243], v[188:191], v[160:163], v[240:243]\n"
    "v_mfma_f32_16x16x32_bf16 v[240:243], v[156:159], v[128:131], v[240:243]\n"
    "s_waitcnt vmcnt(8)\n"
    "s_barrier\n"
    "s_add_u32 m0, s35, 0\n"
    "s_nop 0\n"
    "global_load_lds_dwordx4 v46, s[30:31] offset:0\n"
    "ds_read_b128 v[128:131], v56 offset:49152\n"
    "ds_read_b128 v[132:135], v56 offset:51200\n"
    "ds_read_b128 v[136:139], v56 offset:53248\n"
    "global_load_lds_dwordx4 v47, s[30:31] offset:1024\n"
    "s_add_u32 s30, s30, 212992\n"
    "s_addc_u32 s31, s31, 0\n"
    "ds_read_b128 v[140:143], v56 offset:55296\n"
    "ds_read_b64_tr_b16 v[144:145], v76 offset:49152\n"
    "ds_read_b64_tr_b16 v[146:147], v76 offset:50176\n"
    "s_add_u32 m0, s34, 0\n"
    "s_nop 0\n"
    "global_load_lds_dwordx4 v52, s[36:37] offset:0\n"
    "ds_read_b64_tr_b16 v[148:149], v77 offset:49152\n"
    "ds_read_b64_tr_b16 v[150:151], v77 offset:50176\n"
    "ds_read_b64_tr_b16 v[152:153], v78 offset:49152\n"
    "global_load_lds_dwordx4 v53, s[36:37] offset:1024\n"
    "ds_read_b64_tr_b16 v[154:155], v78 offset:50176\n"
    "ds_read_b64_tr_b16 v[156:157], v79 offset:49152\n"
    "ds_read_b64_tr_b16 v[158:159], v79 offset:50176\n"
    "global_load_lds_dwordx4 v54, s[36:37] offset:2048\n"
    "ds_read_b128 v[160:163], v57 offset:49152\n"
    "ds_read_b128 v[164:167], v57 offset:51200\n"
    "ds_read_b128 v[168:171], v57 offset:53248\n"
    "global_load_lds_dwordx4 v55, s[36:37] offset:3072\n"
    "ds_read_b128 v[172:175], v57 offset:55296\n"
    "ds_read_b64_tr_b16 v[176:177], v76 offset:57344\n"
    "ds_read_b64_tr_b16 v[178:179], v76 offset:58368\n"
    "global_load_dwordx2 v[90:91], v42, s[10:11] offset:96\n"
    "ds_read_b64_tr_b16 v[180:181], v77 offset:57344\n"
    "ds_read_b64_tr_b16 v[182:183], v77 offset:58368\n"
    "ds_read_b64_tr_b16 v[184:185], v78 offset:57344\n"
    "ds_read_b64_tr_b16 v[186:187], v78 offset:58368\n"
    "ds_read_b64_tr_b16 v[188:189], v79 offset:57344\n"
    "ds_read_b64_tr_b16 v[190:191], v79 offset:58368\n"
    "s_waitcnt vmcnt(12) lgkmcnt(0)\n"
    "s_barrier\n"
    "v_mfma_f32_16x16x32_bf16 v[192:195], v[144:147], v[128:131], v[192:195]\n"
    "v_lshlrev_b32 v107, 7, v107\n"
    "v_mfma_f32_16x16x32_bf16 v[192:195], v[176:179], v[160:163], v[192:195]\n"
    "v_mfma_f32_16x16x32_bf16 v[196:199], v[176:179], v[164:167], v[196:199]\n"
    "v_add_u32_dpp v52, v106, v48 quad_perm:[0,0,0,0] row_mask:0xf bank_mask:0xf\n"
    "v_mfma_f32_16x16x32_bf16 v[196:199], v[144:147], v[132:135], v[196:199]\n"
    "v_mfma_f32_16x16x32_bf16 v[200:203], v[144:147], v[136:139], v[200:203]\n"
    "v_add_u32_dpp v53, v106, v49 quad_perm:[1,1,1,1] row_mask:0xf bank_mask:0xf\n"
    "v_mfma_f32_16x16x32_bf16 v[200:203], v[176:179], v[168:171], v[200:203]\n"
    "v_mfma_f32_16x16x32_bf16 v[204:207], v[176:179], v[172:175], v[204:207]\n"
    "v_add_u32_dpp v54, v106, v50 quad_perm:[2,2,2,2] row_mask:0xf bank_mask:0xf\n"
    "v_mfma_f32_16x16x32_bf16 v[204:207], v[144:147], v[140:143], v[204:207]\n"
    "v_mfma_f32_16x16x32_bf16 v[220:223], v[148:151], v[140:143], v[220:223]\n"
    "v_add_u32_dpp v55, v106, v51 quad_perm:[3,3,3,3] row_mask:0xf bank_mask:0xf\n"
    "v_mfma_f32_16x16x32_bf16 v[220:223], v[180:183], v[172:175], v[220:223]\n"
    "v_mfma_f32_16x16x32_bf16 v[216:219], v[180:183], v[168:171], v[216:219]\n"
    "v_lshlrev_b32 v22, 1, v22\n"
    "v_mfma_f32_16x16x32_bf16 v[216:219], v[148:151], v[136:139], v[216:219]\n"
    "v_mfma_f32_16x16x32_bf16 v[212:215], v[148:151], v[132:135], v[212:215]\n"
    "v_lshl_add_u32 v22, v12, 3, v22\n"
    "v_mfma_f32_16x16x32_bf16 v[212:215], v[180:183], v[164:167], v[212:215]\n"
    "v_mfma_f32_16x16x32_bf16 v[208:211], v[180:183], v[160:163], v[208:211]\n"
    "s_mov_b32 s40, 0x05040100\n"
    "v_mfma_f32_16x16x32_bf16 v[208:211], v[148:151], v[128:131], v[208:211]\n"
    "v_mfma_f32_16x16x32_bf16 v[224:227], v[152:155], v[128:131], v[224:227]\n"
    "v_bfe_u32 v24, s40, v13, 8\n"
    "v_mfma_f32_16x16x32_bf16 v[224:227], v[184:187], v[160:163], v[224:227]\n"
    "v_mfma_f32_16x16x32_bf16 v[228:231], v[184:187], v[164:167], v[228:231]\n"
    "v_add_u32 v22, v22, v24\n"
    "v_mfma_f32_16x16x32_bf16 v[228:231], v[152:155], v[132:135], v[228:231]\n"
    "v_mfma_f32_16x16x32_bf16 v[232:235], v[152:155], v[136:139], v[232:235]\n"
    "s_mov_b32 s40, 0x1a191918\n"
    "v_mfma_f32_16x16x32_bf16 v[232:235], v[184:187], v[168:171], v[232:235]\n"
    "v_mfma_f32_16x16x32_bf16 v[236:239], v[184:187], v[172:175], v[236:239]\n"
    "v_bfe_u32 v24, s40, v13, 8\n"
    "v_mfma_f32_16x16x32_bf16 v[236:239], v[152:155], v[140:143], v[236:239]\n"
    "v_mfma_f32_16x16x32_bf16 v[252:255], v[156:159], v[140:143], v[252:255]\n"
    "v_cndmask_b32 v22, v22, v24, vcc\n"
    "v_mfma_f32_16x16x32_bf16 v[252:255], v[188:191], v[172:175], v[252:255]\n"
    "v_mfma_f32_16x16x32_bf16 v[248:251], v[188:191], v[168:171], v[248:251]\n"
    "v_mad_u32_u24 v6, v22, s41, v21\n"
    "v_mfma_f32_16x16x32_bf16 v[248:251], v[156:159], v[136:139], v[248:251]\n"
    "v_mfma_f32_16x16x32_bf16 v[244:247], v[156:159], v[132:135], v[244:247]\n"
    "v_bfrev_b32 v14, v12\n"
    "v_mfma_f32_16x16x32_bf16 v[244:247], v[188:191], v[164:167], v[244:247]\n"
    "v_mfma_f32_16x16x32_bf16 v[240:243], v[188:191], v[160:163], v[240:243]\n"
    "v_mfma_f32_16x16x32_bf16 v[240:243], v[156:159], v[128:131], v[240:243]\n"
    "s_waitcnt vmcnt(8)\n"
    "s_barrier\n"
    "s_add_u32 m0, s35, 49152\n"
    "s_nop 0\n"
    "global_load_lds_dwordx4 v46, s[30:31] offset:0\n"
    "ds_read_b128 v[128:131], v58 offset:0\n"
    "ds_read_b128 v[132:135], v58 offset:2048\n"
    "ds_read_b128 v[136:139], v58 offset:4096\n"
    "global_load_lds_dwordx4 v47, s[30:31] offset:1024\n"
    "s_add_u32 s30, s30, 212992\n"
    "s_addc_u32 s31, s31, 0\n"
    "ds_read_b128 v[140:143], v58 offset:6144\n"
    "ds_read_b64_tr_b16 v[144:145], v80 offset:0\n"
    "ds_read_b64_tr_b16 v[146:147], v80 offset:1024\n"
    "s_add_u32 m0, s34, 49152\n"
    "s_nop 0\n"
    "global_load_lds_dwordx4 v52, s[36:37] offset:0\n"
    "ds_read_b64_tr_b16 v[148:149], v81 offset:0\n"
    "ds_read_b64_tr_b16 v[150:151], v81 offset:1024\n"
    "ds_read_b64_tr_b16 v[152:153], v82 offset:0\n"
    "global_load_lds_dwordx4 v53, s[36:37] offset:1024\n"
    "ds_read_b64_tr_b16 v[154:155], v82 offset:1024\n"
    "ds_read_b64_tr_b16 v[156:157], v83 offset:0\n"
    "ds_read_b64_tr_b16 v[158:159], v83 offset:1024\n"
    "global_load_lds_dwordx4 v54, s[36:37] offset:2048\n"
    "ds_read_b128 v[160:163], v59 offset:0\n"
    "ds_read_b128 v[164:167], v59 offset:2048\n"
    "ds_read_b128 v[168:171], v59 offset:4096\n"
    "global_load_lds_dwordx4 v55, s[36:37] offset:3072\n"
    "ds_read_b128 v[172:175], v59 offset:6144\n"
    "ds_read_b64_tr_b16 v[176:177], v80 offset:8192\n"
    "ds_read_b64_tr_b16 v[178:179], v80 offset:9216\n"
    "global_load_dwordx2 v[92:93], v42, s[18:19] offset:0\n"
    "ds_read_b64_tr_b16 v[180:181], v81 offset:8192\n"
    "ds_read_b64_tr_b16 v[182:183], v81 offset:9216\n"
    "ds_read_b64_tr_b16 v[184:185], v82 offset:8192\n"
    "ds_read_b64_tr_b16 v[186:187], v82 offset:9216\n"
    "ds_read_b64_tr_b16 v[188:189], v83 offset:8192\n"
    "ds_read_b64_tr_b16 v[190:191], v83 offset:9216\n"
    "s_waitcnt vmcnt(12) lgkmcnt(0)\n"
    "s_barrier\n"
    "v_mfma_f32_16x16x32_bf16 v[192:195], v[144:147], v[128:131], v[192:195]\n"
    "v_lshlrev_b32 v108, 7, v108\n"
    "v_mfma_f32_16x16x32_bf16 v[192:195], v[176:179], v[160:163], v[192:195]\n"
    "v_mfma_f32_16x16x32_bf16 v[196:199], v[176:179], v[164:167], v[196:199]\n"
    "v_add_u32_dpp v52, v107, v48 quad_perm:[0,0,0,0] row_mask:0xf bank_mask:0xf\n"
    "v_mfma_f32_16x16x32_bf16 v[196:199], v[144:147], v[132:135], v[196:199]\n"
    "v_mfma_f32_16x16x32_bf16 v[200:203], v[144:147], v[136:139], v[200:203]\n"
    "v_add_u32_dpp v53, v107, v49 quad_perm:[1,1,1,1] row_mask:0xf bank_mask:0xf\n"
    "v_mfma_f32_16x16x32_bf16 v[200:203], v[176:179], v[168:171], v[200:203]\n"
    "v_mfma_f32_16x16x32_bf16 v[204:207], v[176:179], v[172:175], v[204:207]\n"
    "v_add_u32_dpp v54, v107, v50 quad_perm:[2,2,2,2] row_mask:0xf bank_mask:0xf\n"
    "v_mfma_f32_16x16x32_bf16 v[204:207], v[144:147], v[140:143], v[204:207]\n"
    "v_mfma_f32_16x16x32_bf16 v[220:223], v[148:151], v[140:143], v[220:223]\n"
    "v_add_u32_dpp v55, v107, v51 quad_perm:[3,3,3,3] row_mask:0xf bank_mask:0xf\n"
    "v_mfma_f32_16x16x32_bf16 v[220:223], v[180:183], v[172:175], v[220:223]\n"
    "v_mfma_f32_16x16x32_bf16 v[216:219], v[180:183], v[168:171], v[216:219]\n"
    "v_lshrrev_b32 v14, 26, v14\n"
    "v_mfma_f32_16x16x32_bf16 v[216:219], v[148:151], v[136:139], v[216:219]\n"
    "v_mfma_f32_16x16x32_bf16 v[212:215], v[148:151], v[132:135], v[212:215]\n"
    "s_add_u32 s40, s22, 3\n"
    "v_mfma_f32_16x16x32_bf16 v[212:215], v[180:183], v[164:167], v[212:215]\n"
    "v_mfma_f32_16x16x32_bf16 v[208:211], v[180:183], v[160:163], v[208:211]\n"
    "s_and_b32 s40, s40, 3\n"
    "v_mfma_f32_16x16x32_bf16 v[208:211], v[148:151], v[128:131], v[208:211]\n"
    "v_mfma_f32_16x16x32_bf16 v[224:227], v[152:155], v[128:131], v[224:227]\n"
    "s_lshl2_add_u32 s40, s40, s22\n"
    "v_mfma_f32_16x16x32_bf16 v[224:227], v[184:187], v[160:163], v[224:227]\n"
    "v_mfma_f32_16x16x32_bf16 v[228:231], v[184:187], v[164:167], v[228:231]\n"
    "s_add_u32 s40, s40, s58\n"
    "v_mfma_f32_16x16x32_bf16 v[228:231], v[152:155], v[132:135], v[228:231]\n"
    "v_mfma_f32_16x16x32_bf16 v[232:235], v[152:155], v[136:139], v[232:235]\n"
    "s_lshl_b32 s40, s40, 2\n"
    "v_mfma_f32_16x16x32_bf16 v[232:235], v[184:187], v[168:171], v[232:235]\n"
    "v_mfma_f32_16x16x32_bf16 v[236:239], v[184:187], v[172:175], v[236:239]\n"
    "s_add_u32 s40, s40, -450048\n"
    "v_mfma_f32_16x16x32_bf16 v[236:239], v[152:155], v[140:143], v[236:239]\n"
    "v_mfma_f32_16x16x32_bf16 v[252:255], v[156:159], v[140:143], v[252:255]\n"
    "v_add_u32 v14, s40, v14\n"
    "v_mfma_f32_16x16x32_bf16 v[252:255], v[188:191], v[172:175], v[252:255]\n"
    "v_mfma_f32_16x16x32_bf16 v[248:251], v[188:191], v[168:171], v[248:251]\n"
    "v_and_b32 v2, 1, v1\n"
    "v_mfma_f32_16x16x32_bf16 v[248:251], v[156:159], v[136:139], v[248:251]\n"
    "v_mfma_f32_16x16x32_bf16 v[244:247], v[156:159], v[132:135], v[244:247]\n"
    "v_mul_u32_u24 v2, 0xaaaa, v2\n"
    "v_mfma_f32_16x16x32_bf16 v[244:247], v[188:191], v[164:167], v[244:247]\n"
    "v_mfma_f32_16x16x32_bf16 v[240:243], v[188:191], v[160:163], v[240:243]\n"
    "v_mfma_f32_16x16x32_bf16 v[240:243], v[156:159], v[128:131], v[240:243]\n"
    "s_waitcnt vmcnt(8)\n"
    "s_barrier\n"
    "s_add_u32 m0, s35, 98304\n"
    "s_nop 0\n"
    "global_load_lds_dwordx4 v46, s[30:31] offset:0\n"
    "ds_read_b128 v[128:131], v56 offset:0\n"
    "ds_read_b128 v[132:135], v56 offset:2048\n"
    "ds_read_b128 v[136:139], v56 offset:4096\n"
    "global_load_lds_dwordx4 v47, s[30:31] offset:1024\n"
    "s_bfe_u32 s66, s28, 0x80010\n"
    "s_mul_i32 s66, s66, 53248\n"
    "s_add_u32 s30, s6, s66\n"
    "s_addc_u32 s31, s7, 0\n"
    "ds_read_b128 v[140:143], v56 offset:6144\n"
    "ds_read_b64_tr_b16 v[144:145], v76 offset:0\n"
    "ds_read_b64_tr_b16 v[146:147], v76 offset:1024\n"
    "s_add_u32 m0, s34, 98304\n"
    "s_nop 0\n"
    "global_load_lds_dwordx4 v52, s[36:37] offset:0\n"
    "ds_read_b64_tr_b16 v[148:149], v77 offset:0\n"
    "ds_read_b64_tr_b16 v[150:151], v77 offset:1024\n"
    "ds_read_b64_tr_b16 v[152:153], v78 offset:0\n"
    "global_load_lds_dwordx4 v53, s[36:37] offset:1024\n"
    "ds_read_b64_tr_b16 v[154:155], v78 offset:1024\n"
    "ds_read_b64_tr_b16 v[156:157], v79 offset:0\n"
    "ds_read_b64_tr_b16 v[158:159], v79 offset:1024\n"
    "global_load_lds_dwordx4 v54, s[36:37] offset:2048\n"
    "ds_read_b128 v[160:163], v57 offset:0\n"
    "ds_read_b128 v[164:167], v57 offset:2048\n"
    "ds_read_b128 v[168:171], v57 offset:4096\n"
    "global_load_lds_dwordx4 v55, s[36:37] offset:3072\n"
    "ds_read_b128 v[172:175], v57 offset:6144\n"
    "ds_read_b64_tr_b16 v[176:177], v76 offset:8192\n"
    "ds_read_b64_tr_b16 v[178:179], v76 offset:9216\n"
    "global_load_dwordx2 v[94:95], v42, s[18:19] offset:32\n"
    "ds_read_b64_tr_b16 v[180:181], v77 offset:8192\n"
    "ds_read_b64_tr_b16 v[182:183], v77 offset:9216\n"
    "ds_read_b64_tr_b16 v[184:185], v78 offset:8192\n"
    "ds_read_b64_tr_b16 v[186:187], v78 offset:9216\n"
    "ds_read_b64_tr_b16 v[188:189], v79 offset:8192\n"
    "ds_read_b64_tr_b16 v[190:191], v79 offset:9216\n"
    "s_waitcnt vmcnt(12) lgkmcnt(0)\n"
    "s_barrier\n"
    "v_mfma_f32_16x16x32_bf16 v[192:195], v[144:147], v[128:131], v[192:195]\n"
    "v_lshlrev_b32 v109, 7, v109\n"
    "v_mfma_f32_16x16x32_bf16 v[192:195], v[176:179], v[160:163], v[192:195]\n"
    "v_mfma_f32_16x16x32_bf16 v[196:199], v[176:179], v[164:167], v[196:199]\n"
    "v_add_u32_dpp v52, v108, v48 quad_perm:[0,0,0,0] row_mask:0xf bank_mask:0xf\n"
    "v_mfma_f32_16x16x32_bf16 v[196:199], v[144:147], v[132:135], v[196:199]\n"
    "v_mfma_f32_16x16x32_bf16 v[200:203], v[144:147], v[136:139], v[200:203]\n"
    "v_add_u32_dpp v53, v108, v49 quad_perm:[1,1,1,1] row_mask:0xf bank_mask:0xf\n"
    "v_mfma_f32_16x16x32_bf16 v[200:203], v[176:179], v[168:171], v[200:203]\n"
    "v_mfma_f32_16x16x32_bf16 v[204:207], v[176:179], v[172:175], v[204:207]\n"
    "v_add_u32_dpp v54, v108, v50 quad_perm:[2,2,2,2] row_mask:0xf bank_mask:0xf\n"
    "v_mfma_f32_16x16x32_bf16 v[204:207], v[144:147], v[140:143], v[204:207]\n"
    "v_mfma_f32_16x16x32_bf16 v[220:223], v[148:151], v[140:143], v[220:223]\n"
    "v_add_u32_dpp v55, v108, v51 quad_perm:[3,3,3,3] row_mask:0xf bank_mask:0xf\n"
    "v_mfma_f32_16x16x32_bf16 v[220:223], v[180:183], v[172:175], v[220:223]\n"
    "v_mfma_f32_16x16x32_bf16 v[216:219], v[180:183], v[168:171], v[216:219]\n"
    "v_add_u32 v2, 0x4444, v2\n"
    "v_mfma_f32_16x16x32_bf16 v[216:219], v[148:151], v[136:139], v[216:219]\n"
    "v_mfma_f32_16x16x32_bf16 v[212:215], v[148:151], v[132:135], v[212:215]\n"
    "v_mad_u32_u24 v15, v17, 36, v19\n"
    "v_mfma_f32_16x16x32_bf16 v[212:215], v[180:183], v[164:167], v[212:215]\n"
    "v_mfma_f32_16x16x32_bf16 v[208:211], v[180:183], v[160:163], v[208:211]\n"
    "v_mad_u32_u24 v16, v18, 36, v20\n"
    "v_mfma_f32_16x16x32_bf16 v[208:211], v[148:151], v[128:131], v[208:211]\n"
    "v_mfma_f32_16x16x32_bf16 v[224:227], v[152:155], v[128:131], v[224:227]\n"
    "s_lshl4_add_u32 s41, s21, s22\n"
    "v_mfma_f32_16x16x32_bf16 v[224:227], v[184:187], v[160:163], v[224:227]\n"
    "v_mfma_f32_16x16x32_bf16 v[228:231], v[184:187], v[164:167], v[228:231]\n"
    "v_lshrrev_b32 v12, 5, v19\n"
    "v_mfma_f32_16x16x32_bf16 v[228:231], v[152:155], v[132:135], v[228:231]\n"
    "v_mfma_f32_16x16x32_bf16 v[232:235], v[152:155], v[136:139], v[232:235]\n"
    "v_sub_u32 v12, 1, v12\n"
    "v_mfma_f32_16x16x32_bf16 v[232:235], v[184:187], v[168:171], v[232:235]\n"
    "v_mfma_f32_16x16x32_bf16 v[236:239], v[184:187], v[172:175], v[236:239]\n"
    "v_and_b32 v12, 3, v12\n"
    "v_mfma_f32_16x16x32_bf16 v[236:239], v[152:155], v[140:143], v[236:239]\n"
    "v_mfma_f32_16x16x32_bf16 v[252:255], v[156:159], v[140:143], v[252:255]\n"
    "v_add_lshl_u32 v12, v12, s41, 12\n"
    "v_mfma_f32_16x16x32_bf16 v[252:255], v[188:191], v[172:175], v[252:255]\n"
    "v_mfma_f32_16x16x32_bf16 v[248:251], v[188:191], v[168:171], v[248:251]\n"
    "v_add_u32 v15, v12, v15\n"
    "v_mfma_f32_16x16x32_bf16 v[248:251], v[156:159], v[136:139], v[248:251]\n"
    "v_mfma_f32_16x16x32_bf16 v[244:247], v[156:159], v[132:135], v[244:247]\n"
    "v_lshrrev_b32 v12, 5, v20\n"
    "v_mfma_f32_16x16x32_bf16 v[244:247], v[188:191], v[164:167], v[244:247]\n"
    "v_mfma_f32_16x16x32_bf16 v[240:243], v[188:191], v[160:163], v[240:243]\n"
    "v_mfma_f32_16x16x32_bf16 v[240:243], v[156:159], v[128:131], v[240:243]\n"
    "s_waitcnt vmcnt(8)\n"
    "s_barrier\n"
    "s_add_u32 m0, s35, 0\n"
    "s_nop 0\n"
    "global_load_lds_dwordx4 v46, s[30:31] offset:0\n"
    "ds_read_b128 v[128:131], v56 offset:49152\n"
    "ds_read_b128 v[132:135], v56 offset:51200\n"
    "ds_read_b128 v[136:139], v56 offset:53248\n"
    "global_load_lds_dwordx4 v47, s[30:31] offset:1024\n"
    "s_add_u32 s30, s30, 212992\n"
    "s_addc_u32 s31, s31, 0\n"
    "ds_read_b128 v[140:143], v56 offset:55296\n"
    "ds_read_b64_tr_b16 v[144:145], v76 offset:49152\n"
    "ds_read_b64_tr_b16 v[146:147], v76 offset:50176\n"
    "s_add_u32 m0, s34, 0\n"
    "s_nop 0\n"
    "global_load_lds_dwordx4 v52, s[36:37] offset:0\n"
    "ds_read_b64_tr_b16 v[148:149], v77 offset:49152\n"
    "ds_read_b64_tr_b16 v[150:151], v77 offset:50176\n"
    "ds_read_b64_tr_b16 v[152:153], v78 offset:49152\n"
    "global_load_lds_dwordx4 v53, s[36:37] offset:1024\n"
    "ds_read_b64_tr_b16 v[154:155], v78 offset:50176\n"
    "ds_read_b64_tr_b16 v[156:157], v79 offset:49152\n"
    "ds_read_b64_tr_b16 v[158:159], v79 offset:50176\n"
    "global_load_lds_dwordx4 v54, s[36:37] offset:2048\n"
    "ds_read_b128 v[160:163], v57 offset:49152\n"
    "ds_read_b128 v[164:167], v57 offset:51200\n"
    "ds_read_b128 v[168:171], v57 offset:53248\n"
    "global_load_lds_dwordx4 v55, s[36:37] offset:3072\n"
    "ds_read_b128 v[172:175], v57 offset:55296\n"
    "ds_read_b64_tr_b16 v[176:177], v76 offset:57344\n"
    "ds_read_b64_tr_b16 v[178:179], v76 offset:58368\n"
    "global_load_dwordx2 v[122:123], v42, s[18:19] offset:64\n"
    "ds_read_b64_tr_b16 v[180:181], v77 offset:57344\n"
    "ds_read_b64_tr_b16 v[182:183], v77 offset:58368\n"
    "ds_read_b64_tr_b16 v[184:185], v78 offset:57344\n"
    "ds_read_b64_tr_b16 v[186:187], v78 offset:58368\n"
    "ds_read_b64_tr_b16 v[188:189], v79 offset:57344\n"
    "ds_read_b64_tr_b16 v[190:191], v79 offset:58368\n"
    "s_waitcnt vmcnt(12) lgkmcnt(0)\n"
    "s_barrier\n"
    "v_mfma_f32_16x16x32_bf16 v[192:195], v[144:147], v[128:131], v[192:195]\n"
    "v_lshlrev_b32 v110, 7, v110\n"
    "v_mfma_f32_16x16x32_bf16 v[192:195], v[176:179], v[160:163], v[192:195]\n"
    "v_add_u32_dpp v52, v109, v48 quad_perm:[0,0,0,0] row_mask:0xf bank_mask:0xf\n"
    "v_mfma_f32_16x16x32_bf16 v[196:199], v[176:179], v[164:167], v[196:199]\n"
    "v_mfma_f32_16x16x32_bf16 v[196:199], v[144:147], v[132:135], v[196:199]\n"
    "v_add_u32_dpp v53, v109, v49 quad_perm:[1,1,1,1] row_mask:0xf bank_mask:0xf\n"
    "v_mfma_f32_16x16x32_bf16 v[200:203], v[144:147], v[136:139], v[200:203]\n"
    "v_mfma_f32_16x16x32_bf16 v[200:203], v[176:179], v[168:171], v[200:203]\n"
    "v_add_u32_dpp v54, v109, v50 quad_perm:[2,2,2,2] row_mask:0xf bank_mask:0xf\n"
    "v_mfma_f32_16x16x32_bf16 v[204:207], v[176:179], v[172:175], v[204:207]\n"
    "v_mfma_f32_16x16x32_bf16 v[204:207], v[144:147], v[140:143], v[204:207]\n"
    "v_add_u32_dpp v55, v109, v51 quad_perm:[3,3,3,3] row_mask:0xf bank_mask:0xf\n"
    "v_mfma_f32_16x16x32_bf16 v[220:223], v[148:151], v[140:143], v[220:223]\n"
    "v_mfma_f32_16x16x32_bf16 v[220:223], v[180:183], v[172:175], v[220:223]\n"
    "v_sub_u32 v12, 1, v12\n"
    "v_mfma_f32_16x16x32_bf16 v[216:219], v[180:183], v[168:171], v[216:219]\n"
    "v_mfma_f32_16x16x32_bf16 v[216:219], v[148:151], v[136:139], v[216:219]\n"
    "v_and_b32 v12, 3, v12\n"
    "v_mfma_f32_16x16x32_bf16 v[212:215], v[148:151], v[132:135], v[212:215]\n"
    "v_mfma_f32_16x16x32_bf16 v[212:215], v[180:183], v[164:167], v[212:215]\n"
    "v_add_lshl_u32 v12, v12, s41, 12\n"
    "v_mfma_f32_16x16x32_bf16 v[208:211], v[180:183], v[160:163], v[208:211]\n"
    "v_mfma_f32_16x16x32_bf16 v[208:211], v[148:151], v[128:131], v[208:211]\n"
    "v_add_u32 v16, v12, v16\n"
    "v_mfma_f32_16x16x32_bf16 v[224:227], v[152:155], v[128:131], v[224:227]\n"
    "v_mfma_f32_16x16x32_bf16 v[224:227], v[184:187], v[160:163], v[224:227]\n"
    "v_add_u32 v16, 3456, v16\n"
    "v_mfma_f32_16x16x32_bf16 v[228:231], v[184:187], v[164:167], v[228:231]\n"
    "v_mfma_f32_16x16x32_bf16 v[228:231], v[152:155], v[132:135], v[228:231]\n"
    "s_mul_i32 s41, s47, 13\n"
    "v_mfma_f32_16x16x32_bf16 v[232:235], v[152:155], v[136:139], v[232:235]\n"
    "v_mfma_f32_16x16x32_bf16 v[232:235], v[184:187], v[168:171], v[232:235]\n"
    "s_sub_u32 s41, s3, s41\n"
    "v_mfma_f32_16x16x32_bf16 v[236:239], v[184:187], v[172:175], v[236:239]\n"
    "v_mfma_f32_16x16x32_bf16 v[236:239], v[152:155], v[140:143], v[236:239]\n"
    "s_lshl1_add_u32 s41, s41, s21\n"
    "v_mfma_f32_16x16x32_bf16 v[252:255], v[156:159], v[140:143], v[252:255]\n"
    "v_mfma_f32_16x16x32_bf16 v[252:255], v[188:191], v[172:175], v[252:255]\n"
    "s_mul_i32 s41, s41, 3\n"
    "v_mfma_f32_16x16x32_bf16 v[248:251], v[188:191], v[168:171], v[248:251]\n"
    "v_mfma_f32_16x16x32_bf16 v[248:251], v[156:159], v[136:139], v[248:251]\n"
    "s_add_u32 s41, s41, s22\n"
    "v_mfma_f32_16x16x32_bf16 v[244:247], v[156:159], v[132:135], v[244:247]\n"
    "v_mfma_f32_16x16x32_bf16 v[244:247], v[188:191], v[164:167], v[244:247]\n"
    "v_mov_b32 v24, s41\n"
    "v_mfma_f32_16x16x32_bf16 v[240:243], v[188:191], v[160:163], v[240:243]\n"
    "v_mfma_f32_16x16x32_bf16 v[240:243], v[156:159], v[128:131], v[240:243]\n"
    "s_waitcnt vmcnt(8)\n"
    "s_barrier\n"
    "s_add_u32 m0, s35, 49152\n"
    "s_nop 0\n"
    "global_load_lds_dwordx4 v46, s[30:31] offset:0\n"
    "ds_read_b128 v[128:131], v58 offset:0\n"
    "ds_read_b128 v[132:135], v58 offset:2048\n"
    "ds_read_b128 v[136:139], v58 offset:4096\n"
    "global_load_lds_dwordx4 v47, s[30:31] offset:1024\n"
    "s_add_u32 s30, s30, 212992\n"
    "s_addc_u32 s31, s31, 0\n"
    "ds_read_b128 v[140:143], v58 offset:6144\n"
    "ds_read_b64_tr_b16 v[144:145], v80 offset:0\n"
    "ds_read_b64_tr_b16 v[146:147], v80 offset:1024\n"
    "s_add_u32 m0, s34, 49152\n"
    "s_nop 0\n"
    "global_load_lds_dwordx4 v52, s[36:37] offset:0\n"
    "ds_read_b64_tr_b16 v[148:149], v81 offset:0\n"
    "ds_read_b64_tr_b16 v[150:151], v81 offset:1024\n"
    "ds_read_b64_tr_b16 v[152:153], v82 offset:0\n"
    "global_load_lds_dwordx4 v53, s[36:37] offset:1024\n"
    "ds_read_b64_tr_b16 v[154:155], v82 offset:1024\n"
    "ds_read_b64_tr_b16 v[156:157], v83 offset:0\n"
    "ds_read_b64_tr_b16 v[158:159], v83 offset:1024\n"
    "global_load_lds_dwordx4 v54, s[36:37] offset:2048\n"
    "ds_read_b128 v[160:163], v59 offset:0\n"
    "ds_read_b128 v[164:167], v59 offset:2048\n"
    "ds_read_b128 v[168:171], v59 offset:4096\n"
    "global_load_lds_dwordx4 v55, s[36:37] offset:3072\n"
    "ds_read_b128 v[172:175], v59 offset:6144\n"
    "ds_read_b64_tr_b16 v[176:177], v80 offset:8192\n"
    "ds_read_b64_tr_b16 v[178:179], v80 offset:9216\n"
    "global_load_dwordx2 v[124:125], v42, s[18:19] offset:96\n"
    "ds_read_b64_tr_b16 v[180:181], v81 offset:8192\n"
    "ds_read_b64_tr_b16 v[182:183], v81 offset:9216\n"
    "ds_read_b64_tr_b16 v[184:185], v82 offset:8192\n"
    "ds_read_b64_tr_b16 v[186:187], v82 offset:9216\n"
    "ds_read_b64_tr_b16 v[188:189], v83 offset:8192\n"
    "ds_read_b64_tr_b16 v[190:191], v83 offset:9216\n"
    "s_waitcnt vmcnt(12) lgkmcnt(0)\n"
    "s_barrier\n"
    "v_mfma_f32_16x16x32_bf16 v[192:195], v[144:147], v[128:131], v[192:195]\n"
    "v_lshlrev_b32 v111, 7, v111\n"
    "v_mfma_f32_16x16x32_bf16 v[192:195], v[176:179], v[160:163], v[192:195]\n"
    "v_mfma_f32_16x16x32_bf16 v[196:199], v[176:179], v[164:167], v[196:199]\n"
    "v_add_u32_dpp v52, v110, v48 quad_perm:[0,0,0,0] row_mask:0xf bank_mask:0xf\n"
    "v_mfma_f32_16x16x32_bf16 v[196:199], v[144:147], v[132:135], v[196:199]\n"
    "v_mfma_f32_16x16x32_bf16 v[200:203], v[144:147], v[136:139], v[200:203]\n"
    "v_add_u32_dpp v53, v110, v49 quad_perm:[1,1,1,1] row_mask:0xf bank_mask:0xf\n"
    "v_mfma_f32_16x16x32_bf16 v[200:203], v[176:179], v[168:171], v[200:203]\n"
    "v_mfma_f32_16x16x32_bf16 v[204:207], v[176:179], v[172:175], v[204:207]\n"
    "v_add_u32_dpp v54, v110, v50 quad_perm:[2,2,2,2] row_mask:0xf bank_mask:0xf\n"
    "v_mfma_f32_16x16x32_bf16 v[204:207], v[144:147], v[140:143], v[204:207]\n"
    "v_mfma_f32_16x16x32_bf16 v[220:223], v[148:151], v[140:143], v[220:223]\n"
    "v_add_u32_dpp v55, v110, v51 quad_perm:[3,3,3,3] row_mask:0xf bank_mask:0xf\n"
    "v_mfma_f32_16x16x32_bf16 v[220:223], v[180:183], v[172:175], v[220:223]\n"
    "v_mfma_f32_16x16x32_bf16 v[216:219], v[180:183], v[168:171], v[216:219]\n"
    "v_mfma_f32_16x16x32_bf16 v[216:219], v[148:151], v[136:139], v[216:219]\n"
    "v_readfirstlane_b32 s40, v24\n"
    "v_mfma_f32_16x16x32_bf16 v[212:215], v[148:151], v[132:135], v[212:215]\n"
    "v_mfma_f32_16x16x32_bf16 v[212:215], v[180:183], v[164:167], v[212:215]\n"
    "s_or_b32 s40, s40, s44\n"
    "v_mfma_f32_16x16x32_bf16 v[208:211], v[180:183], v[160:163], v[208:211]\n"
    "v_mfma_f32_16x16x32_bf16 v[208:211], v[148:151], v[128:131], v[208:211]\n"
    "s_lshl1_add_u32 s41, s40, s54\n"
    "v_mfma_f32_16x16x32_bf16 v[224:227], v[152:155], v[128:131], v[224:227]\n"
    "v_mfma_f32_16x16x32_bf16 v[224:227], v[184:187], v[160:163], v[224:227]\n"
    "s_cmpk_lt_u32 s40, 256\n"
    "v_mfma_f32_16x16x32_bf16 v[228:231], v[184:187], v[164:167], v[228:231]\n"
    "v_mfma_f32_16x16x32_bf16 v[228:231], v[152:155], v[132:135], v[228:231]\n"
    "s_cselect_b32 s41, s41, 2\n"
    "v_mfma_f32_16x16x32_bf16 v[232:235], v[152:155], v[136:139], v[232:235]\n"
    "v_mfma_f32_16x16x32_bf16 v[232:235], v[184:187], v[168:171], v[232:235]\n"
    "v_mfma_f32_16x16x32_bf16 v[236:239], v[184:187], v[172:175], v[236:239]\n"
    "v_mov_b32 v24, s41\n"
    "v_mfma_f32_16x16x32_bf16 v[236:239], v[152:155], v[140:143], v[236:239]\n"
    "v_mfma_f32_16x16x32_bf16 v[252:255], v[156:159], v[140:143], v[252:255]\n"
    "s_mul_i32 s41, s41, 52\n"
    "v_mfma_f32_16x16x32_bf16 v[252:255], v[188:191], v[172:175], v[252:255]\n"
    "v_mfma_f32_16x16x32_bf16 v[248:251], v[188:191], v[168:171], v[248:251]\n"
    "s_add_u32 s38, s4, s41\n"
    "v_mfma_f32_16x16x32_bf16 v[248:251], v[156:159], v[136:139], v[248:251]\n"
    "v_mfma_f32_16x16x32_bf16 v[244:247], v[156:159], v[132:135], v[244:247]\n"
    "s_addc_u32 s39, s5, 0\n"
    "v_mfma_f32_16x16x32_bf16 v[244:247], v[188:191], v[164:167], v[244:247]\n"
    "v_mfma_f32_16x16x32_bf16 v[240:243], v[188:191], v[160:163], v[240:243]\n"
    "v_mfma_f32_16x16x32_bf16 v[240:243], v[156:159], v[128:131], v[240:243]\n"
    "s_waitcnt vmcnt(8)\n"
    "s_barrier\n"
    "s_add_u32 m0, s35, 98304\n"
    "s_nop 0\n"
    "global_load_lds_dwordx4 v46, s[30:31] offset:0\n"
    "ds_read_b128 v[128:131], v56 offset:0\n"
    "ds_read_b128 v[132:135], v56 offset:2048\n"
    "ds_read_b128 v[136:139], v56 offset:4096\n"
    "global_load_lds_dwordx4 v47, s[30:31] offset:1024\n"
    "s_add_u32 s30, s30, 212992\n"
    "s_addc_u32 s31, s31, 0\n"
    "ds_read_b128 v[140:143], v56 offset:6144\n"
    "ds_read_b64_tr_b16 v[144:145], v76 offset:0\n"
    "ds_read_b64_tr_b16 v[146:147], v76 offset:1024\n"
    "s_add_u32 m0, s34, 98304\n"
    "s_nop 0\n"
    "global_load_lds_dwordx4 v52, s[36:37] offset:0\n"
    "ds_read_b64_tr_b16 v[148:149], v77 offset:0\n"
    "ds_read_b64_tr_b16 v[150:151], v77 offset:1024\n"
    "ds_read_b64_tr_b16 v[152:153], v78 offset:0\n"
    "global_load_lds_dwordx4 v53, s[36:37] offset:1024\n"
    "ds_read_b64_tr_b16 v[154:155], v78 offset:1024\n"
    "ds_read_b64_tr_b16 v[156:157], v79 offset:0\n"
    "ds_read_b64_tr_b16 v[158:159], v79 offset:1024\n"
    "global_load_lds_dwordx4 v54, s[36:37] offset:2048\n"
    "ds_read_b128 v[160:163], v57 offset:0\n"
    "ds_read_b128 v[164:167], v57 offset:2048\n"
    "ds_read_b128 v[168:171], v57 offset:4096\n"
    "global_load_lds_dwordx4 v55, s[36:37] offset:3072\n"
    "ds_read_b128 v[172:175], v57 offset:6144\n"
    "ds_read_b64_tr_b16 v[176:177], v76 offset:8192\n"
    "ds_read_b64_tr_b16 v[178:179], v76 offset:9216\n"
    "global_load_dwordx3 v[36:38], v17, s[38:39] offset:-104\n"
    "ds_read_b64_tr_b16 v[180:181], v77 offset:8192\n"
    "ds_read_b64_tr_b16 v[182:183], v77 offset:9216\n"
    "ds_read_b64_tr_b16 v[184:185], v78 offset:8192\n"
    "ds_read_b64_tr_b16 v[186:187], v78 offset:9216\n"
    "ds_read_b64_tr_b16 v[188:189], v79 offset:8192\n"
    "ds_read_b64_tr_b16 v[190:191], v79 offset:9216\n"
    "s_waitcnt vmcnt(12) lgkmcnt(0)\n"
    "s_barrier\n"
    "v_mfma_f32_16x16x32_bf16 v[192:195], v[144:147], v[128:131], v[192:195]\n"
    "v_lshlrev_b32 v112, 7, v112\n"
    "v_mfma_f32_16x16x32_bf16 v[192:195], v[176:179], v[160:163], v[192:195]\n"
    "v_add_u32_dpp v52, v111, v48 quad_perm:[0,0,0,0] row_mask:0xf bank_mask:0xf\n"
    "v_mfma_f32_16x16x32_bf16 v[196:199], v[176:179], v[164:167], v[196:199]\n"
    "v_add_u32_dpp v53, v111, v49 quad_perm:[1,1,1,1] row_mask:0xf bank_mask:0xf\n"
    "v_mfma_f32_16x16x32_bf16 v[196:199], v[144:147], v[132:135], v[196:199]\n"
    "v_add_u32_dpp v54, v111, v50 quad_perm:[2,2,2,2] row_mask:0xf bank_mask:0xf\n"
    "v_mfma_f32_16x16x32_bf16 v[200:203], v[144:147], v[136:139], v[200:203]\n"
    "v_add_u32_dpp v55, v111, v51 quad_perm:[3,3,3,3] row_mask:0xf bank_mask:0xf\n"
    "v_mfma_f32_16x16x32_bf16 v[200:203], v[176:179], v[168:171], v[200:203]\n"
    "v_lshlrev_b32 v60, 16, v84\n"
    "v_mfma_f32_16x16x32_bf16 v[204:207], v[176:179], v[172:175], v[204:207]\n"
    "v_and_b32 v61, 0xffff0000, v84\n"
    "v_mfma_f32_16x16x32_bf16 v[204:207], v[144:147], v[140:143], v[204:207]\n"
    "v_mfma_f32_16x16x32_bf16 v[220:223], v[148:151], v[140:143], v[220:223]\n"
    "v_lshlrev_b32 v62, 16, v85\n"
    "v_mfma_f32_16x16x32_bf16 v[220:223], v[180:183], v[172:175], v[220:223]\n"
    "v_and_b32 v63, 0xffff0000, v85\n"
    "v_mfma_f32_16x16x32_bf16 v[216:219], v[180:183], v[168:171], v[216:219]\n"
    "v_lshlrev_b32 v64, 16, v86\n"
    "v_mfma_f32_16x16x32_bf16 v[216:219], v[148:151], v[136:139], v[216:219]\n"
    "v_and_b32 v65, 0xffff0000, v86\n"
    "v_mfma_f32_16x16x32_bf16 v[212:215], v[148:151], v[132:135], v[212:215]\n"
    "v_lshlrev_b32 v66, 16, v87\n"
    "v_mfma_f32_16x16x32_bf16 v[212:215], v[180:183], v[164:167], v[212:215]\n"
    "v_and_b32 v67, 0xffff0000, v87\n"
    "v_mfma_f32_16x16x32_bf16 v[208:211], v[180:183], v[160:163], v[208:211]\n"
    "v_lshlrev_b32 v68, 16, v88\n"
    "v_mfma_f32_16x16x32_bf16 v[208:211], v[148:151], v[128:131], v[208:211]\n"
    "v_mfma_f32_16x16x32_bf16 v[224:227], v[152:155], v[128:131], v[224:227]\n"
    "v_and_b32 v69, 0xffff0000, v88\n"
    "v_mfma_f32_16x16x32_bf16 v[224:227], v[184:187], v[160:163], v[224:227]\n"
    "v_lshlrev_b32 v70, 16, v89\n"
    "v_mfma_f32_16x16x32_bf16 v[228:231], v[184:187], v[164:167], v[228:231]\n"
    "v_and_b32 v71, 0xffff0000, v89\n"
    "v_mfma_f32_16x16x32_bf16 v[228:231], v[152:155], v[132:135], v[228:231]\n"
    "v_lshlrev_b32 v72, 16, v90\n"
    "v_mfma_f32_16x16x32_bf16 v[232:235], v[152:155], v[136:139], v[232:235]\n"
    "v_and_b32 v73, 0xffff0000, v90\n"
    "v_mfma_f32_16x16x32_bf16 v[232:235], v[184:187], v[168:171], v[232:235]\n"
    "v_lshlrev_b32 v74, 16, v91\n"
    "v_mfma_f32_16x16x32_bf16 v[236:239], v[184:187], v[172:175], v[236:239]\n"
    "v_and_b32 v75, 0xffff0000, v91\n"
    "v_mfma_f32_16x16x32_bf16 v[236:239], v[152:155], v[140:143], v[236:239]\n"
    "v_mfma_f32_16x16x32_bf16 v[252:255], v[156:159], v[140:143], v[252:255]\n"
    "s_lshl3_add_u32 s40, s2, 4096\n"
    "v_mfma_f32_16x16x32_bf16 v[252:255], v[188:191], v[172:175], v[252:255]\n"
    "s_lshl_b32 s41, s2, 2\n"
    "v_mfma_f32_16x16x32_bf16 v[248:251], v[188:191], v[168:171], v[248:251]\n"
    "v_lshlrev_b32 v11, 3, v1\n"
    "v_mfma_f32_16x16x32_bf16 v[248:251], v[156:159], v[136:139], v[248:251]\n"
    "v_add_u32 v11, 40960, v11\n"
    "v_mfma_f32_16x16x32_bf16 v[244:247], v[156:159], v[132:135], v[244:247]\n"
    "v_writelane_b32 v11, s40, 8\n"
    "v_mfma_f32_16x16x32_bf16 v[244:247], v[188:191], v[164:167], v[244:247]\n"
    "v_writelane_b32 v11, s41, 9\n"
    "v_mfma_f32_16x16x32_bf16 v[240:243], v[188:191], v[160:163], v[240:243]\n"
    "v_mfma_f32_16x16x32_bf16 v[240:243], v[156:159], v[128:131], v[240:243]\n"
    "s_waitcnt vmcnt(8)\n"
    "s_barrier\n"
    "s_add_u32 m0, s35, 0\n"
    "s_nop 0\n"
    "global_load_lds_dwordx4 v46, s[30:31] offset:0\n"
    "ds_read_b128 v[128:131], v56 offset:49152\n"
    "ds_read_b128 v[132:135], v56 offset:51200\n"
    "ds_read_b128 v[136:139], v56 offset:53248\n"
    "global_load_lds_dwordx4 v47, s[30:31] offset:1024\n"
    "s_bfe_u32 s66, s28, 0x80018\n"
    "s_mul_i32 s66, s66, 53248\n"
    "s_add_u32 s30, s6, s66\n"
    "s_addc_u32 s31, s7, 0\n"
    "ds_read_b128 v[140:143], v56 offset:55296\n"
    "ds_read_b64_tr_b16 v[144:145], v76 offset:49152\n"
    "ds_read_b64_tr_b16 v[146:147], v76 offset:50176\n"
    "s_add_u32 m0, s34, 0\n"
    "s_nop 0\n"
    "global_load_lds_dwordx4 v52, s[36:37] offset:0\n"
    "ds_read_b64_tr_b16 v[148:149], v77 offset:49152\n"
    "ds_read_b64_tr_b16 v[150:151], v77 offset:50176\n"
    "ds_read_b64_tr_b16 v[152:153], v78 offset:49152\n"
    "global_load_lds_dwordx4 v53, s[36:37] offset:1024\n"
    "ds_read_b64_tr_b16 v[154:155], v78 offset:50176\n"
    "ds_read_b64_tr_b16 v[156:157], v79 offset:49152\n"
    "ds_read_b64_tr_b16 v[158:159], v79 offset:50176\n"
    "global_load_lds_dwordx4 v54, s[36:37] offset:2048\n"
    "ds_read_b128 v[160:163], v57 offset:49152\n"
    "ds_read_b128 v[164:167], v57 offset:51200\n"
    "ds_read_b128 v[168:171], v57 offset:53248\n"
    "global_load_lds_dwordx4 v55, s[36:37] offset:3072\n"
    "ds_read_b128 v[172:175], v57 offset:55296\n"
    "ds_read_b64_tr_b16 v[176:177], v76 offset:57344\n"
    "ds_read_b64_tr_b16 v[178:179], v76 offset:58368\n"
    "global_load_dword v41, v18, s[38:39] offset:-8\n"
    "ds_read_b64_tr_b16 v[180:181], v77 offset:57344\n"
    "ds_read_b64_tr_b16 v[182:183], v77 offset:58368\n"
    "ds_read_b64_tr_b16 v[184:185], v78 offset:57344\n"
    "ds_read_b64_tr_b16 v[186:187], v78 offset:58368\n"
    "ds_read_b64_tr_b16 v[188:189], v79 offset:57344\n"
    "ds_read_b64_tr_b16 v[190:191], v79 offset:58368\n"
    "s_waitcnt vmcnt(12) lgkmcnt(0)\n"
    "s_barrier\n"
    "v_mfma_f32_16x16x32_bf16 v[192:195], v[144:147], v[128:131], v[192:195]\n"
    "v_mfma_f32_16x16x32_bf16 v[192:195], v[176:179], v[160:163], v[192:195]\n"
    "v_lshlrev_b32 v113, 7, v113\n"
    "v_mfma_f32_16x16x32_bf16 v[196:199], v[176:179], v[164:167], v[196:199]\n"
    "v_mfma_f32_16x16x32_bf16 v[196:199], v[144:147], v[132:135], v[196:199]\n"
    "v_add_u32_dpp v52, v112, v48 quad_perm:[0,0,0,0] row_mask:0xf bank_mask:0xf\n"
    "v_mfma_f32_16x16x32_bf16 v[200:203], v[144:147], v[136:139], v[200:203]\n"
    "v_mfma_f32_16x16x32_bf16 v[200:203], v[176:179], v[168:171], v[200:203]\n"
    "v_add_u32_dpp v53, v112, v49 quad_perm:[1,1,1,1] row_mask:0xf bank_mask:0xf\n"
    "v_mfma_f32_16x16x32_bf16 v[204:207], v[176:179], v[172:175], v[204:207]\n"
    "v_mfma_f32_16x16x32_bf16 v[204:207], v[144:147], v[140:143], v[204:207]\n"
    "v_add_u32_dpp v54, v112, v50 quad_perm:[2,2,2,2] row_mask:0xf bank_mask:0xf\n"
    "v_mfma_f32_16x16x32_bf16 v[220:223], v[148:151], v[140:143], v[220:223]\n"
    "v_mfma_f32_16x16x32_bf16 v[220:223], v[180:183], v[172:175], v[220:223]\n"
    "v_add_u32_dpp v55, v112, v51 quad_perm:[3,3,3,3] row_mask:0xf bank_mask:0xf\n"
    "v_mfma_f32_16x16x32_bf16 v[216:219], v[180:183], v[168:171], v[216:219]\n"
    "v_mfma_f32_16x16x32_bf16 v[216:219], v[148:151], v[136:139], v[216:219]\n"
    "v_mfma_f32_16x16x32_bf16 v[212:215], v[148:151], v[132:135], v[212:215]\n"
    "v_mfma_f32_16x16x32_bf16 v[212:215], v[180:183], v[164:167], v[212:215]\n"
    "v_mfma_f32_16x16x32_bf16 v[208:211], v[180:183], v[160:163], v[208:211]\n"
    "v_add_f32 v192, v192, v60\n"
    "v_mfma_f32_16x16x32_bf16 v[208:211], v[148:151], v[128:131], v[208:211]\n"
    "v_add_f32 v193, v193, v61\n"
    "v_mfma_f32_16x16x32_bf16 v[224:227], v[152:155], v[128:131], v[224:227]\n"
    "v_add_f32 v194, v194, v62\n"
    "v_mfma_f32_16x16x32_bf16 v[224:227], v[184:187], v[160:163], v[224:227]\n"
    "v_add_f32 v195, v195, v63\n"
    "v_mfma_f32_16x16x32_bf16 v[228:231], v[184:187], v[164:167], v[228:231]\n"
    "v_add_f32 v196, v196, v60\n"
    "v_mfma_f32_16x16x32_bf16 v[228:231], v[152:155], v[132:135], v[228:231]\n"
    "v_add_f32 v197, v197, v61\n"
    "v_mfma_f32_16x16x32_bf16 v[232:235], v[152:155], v[136:139], v[232:235]\n"
    "v_add_f32 v198, v198, v62\n"
    "v_mfma_f32_16x16x32_bf16 v[232:235], v[184:187], v[168:171], v[232:235]\n"
    "v_add_f32 v199, v199, v63\n"
    "v_mfma_f32_16x16x32_bf16 v[236:239], v[184:187], v[172:175], v[236:239]\n"
    "v_add_f32 v200, v200, v60\n"
    "v_mfma_f32_16x16x32_bf16 v[236:239], v[152:155], v[140:143], v[236:239]\n"
    "v_add_f32 v201, v201, v61\n"
    "v_mfma_f32_16x16x32_bf16 v[252:255], v[156:159], v[140:143], v[252:255]\n"
    "v_add_f32 v202, v202, v62\n"
    "v_mfma_f32_16x16x32_bf16 v[252:255], v[188:191], v[172:175], v[252:255]\n"
    "v_add_f32 v203, v203, v63\n"
    "v_mfma_f32_16x16x32_bf16 v[248:251], v[188:191], v[168:171], v[248:251]\n"
    "v_add_f32 v204, v204, v60\n"
    "v_mfma_f32_16x16x32_bf16 v[248:251], v[156:159], v[136:139], v[248:251]\n"
    "v_add_f32 v205, v205, v61\n"
    "v_mfma_f32_16x16x32_bf16 v[244:247], v[156:159], v[132:135], v[244:247]\n"
    "v_add_f32 v206, v206, v62\n"
    "v_mfma_f32_16x16x32_bf16 v[244:247], v[188:191], v[164:167], v[244:247]\n"
    "v_add_f32 v207, v207, v63\n"
    "v_mfma_f32_16x16x32_bf16 v[240:243], v[188:191], v[160:163], v[240:243]\n"
    "v_mfma_f32_16x16x32_bf16 v[240:243], v[156:159], v[128:131], v[240:243]\n"
    "s_waitcnt vmcnt(8)\n"
    "s_barrier\n"
    "s_cmp_eq_u32 s20, 3\n"
    "s_cbranch_scc1 .Lxd_on1\n"
    ".Lxd_on1r:\n"
    "s_add_u32 m0, s35, 49152\n"
    "s_nop 0\n"
    "global_load_lds_dwordx4 v46, s[30:31] offset:0\n"
    "ds_read_b128 v[128:131], v58 offset:0\n"
    "ds_read_b128 v[132:135], v58 offset:2048\n"
    "ds_read_b128 v[136:139], v58 offset:4096\n"
    "ds_read_b128 v[140:143], v58 offset:6144\n"
    "global_load_lds_dwordx4 v47, s[30:31] offset:1024\n"
    "s_add_u32 s30, s30, 212992\n"
    "s_addc_u32 s31, s31, 0\n"
    "ds_read_b64_tr_b16 v[144:145], v80 offset:0\n"
    "ds_read_b64_tr_b16 v[146:147], v80 offset:1024\n"
    "ds_read_b64_tr_b16 v[148:149], v81 offset:0\n"
    "ds_read_b64_tr_b16 v[150:151], v81 offset:1024\n"
    "s_add_u32 m0, s34, 49152\n"
    "s_nop 0\n"
    "global_load_lds_dwordx4 v52, s[36:37] offset:0\n"
    "ds_read_b64_tr_b16 v[152:153], v82 offset:0\n"
    "ds_read_b64_tr_b16 v[154:155], v82 offset:1024\n"
    "ds_read_b64_tr_b16 v[156:157], v83 offset:0\n"
    "ds_read_b64_tr_b16 v[158:159], v83 offset:1024\n"
    "global_load_lds_dwordx4 v53, s[36:37] offset:1024\n"
    "ds_read_b128 v[160:163], v59 offset:0\n"
    "ds_read_b128 v[164:167], v59 offset:2048\n"
    "ds_read_b128 v[168:171], v59 offset:4096\n"
    "ds_read_b128 v[172:175], v59 offset:6144\n"
    "global_load_lds_dwordx4 v54, s[36:37] offset:2048\n"
    "ds_read_b64_tr_b16 v[176:177], v80 offset:8192\n"
    "ds_read_b64_tr_b16 v[178:179], v80 offset:9216\n"
    "ds_read_b64_tr_b16 v[180:181], v81 offset:8192\n"
    "ds_read_b64_tr_b16 v[182:183], v81 offset:9216\n"
    "global_load_lds_dwordx4 v55, s[36:37] offset:3072\n"
    "ds_read_b64_tr_b16 v[184:185], v82 offset:8192\n"
    "ds_read_b64_tr_b16 v[186:187], v82 offset:9216\n"
    "ds_read_b64_tr_b16 v[188:189], v83 offset:8192\n"
    "ds_read_b64_tr_b16 v[190:191], v83 offset:9216\n"
    "s_waitcnt vmcnt(11) lgkmcnt(0)\n"
    "s_barrier\n"
    "v_mfma_f32_16x16x32_bf16 v[192:195], v[144:147], v[128:131], v[192:195]\n"
    "v_mfma_f32_16x16x32_bf16 v[192:195], v[176:179], v[160:163], v[192:195]\n"
    "v_lshlrev_b32 v114, 7, v114\n"
    "v_mfma_f32_16x16x32_bf16 v[196:199], v[176:179], v[164:167], v[196:199]\n"
    "v_mfma_f32_16x16x32_bf16 v[196:199], v[144:147], v[132:135], v[196:199]\n"
    "v_add_u32_dpp v52, v113, v48 quad_perm:[0,0,0,0] row_mask:0xf bank_mask:0xf\n"
    "v_mfma_f32_16x16x32_bf16 v[200:203], v[144:147], v[136:139], v[200:203]\n"
    "v_mfma_f32_16x16x32_bf16 v[200:203], v[176:179], v[168:171], v[200:203]\n"
    "v_add_u32_dpp v53, v113, v49 quad_perm:[1,1,1,1] row_mask:0xf bank_mask:0xf\n"
    "v_mfma_f32_16x16x32_bf16 v[204:207], v[176:179], v[172:175], v[204:207]\n"
    "v_mfma_f32_16x16x32_bf16 v[204:207], v[144:147], v[140:143], v[204:207]\n"
    "v_add_u32_dpp v54, v113, v50 quad_perm:[2,2,2,2] row_mask:0xf bank_mask:0xf\n"
    "v_mfma_f32_16x16x32_bf16 v[220:223], v[148:151], v[140:143], v[220:223]\n"
    "v_mfma_f32_16x16x32_bf16 v[220:223], v[180:183], v[172:175], v[220:223]\n"
    "v_add_u32_dpp v55, v113, v51 quad_perm:[3,3,3,3] row_mask:0xf bank_mask:0xf\n"
    "v_mfma_f32_16x16x32_bf16 v[216:219], v[180:183], v[168:171], v[216:219]\n"
    "v_mfma_f32_16x16x32_bf16 v[216:219], v[148:151], v[136:139], v[216:219]\n"
    "v_mfma_f32_16x16x32_bf16 v[212:215], v[148:151], v[132:135], v[212:215]\n"
    "v_mfma_f32_16x16x32_bf16 v[212:215], v[180:183], v[164:167], v[212:215]\n"
    "v_mfma_f32_16x16x32_bf16 v[208:211], v[180:183], v[160:163], v[208:211]\n"
    "v_mfma_f32_16x16x32_bf16 v[208:211], v[148:151], v[128:131], v[208:211]\n"
    "v_mfma_f32_16x16x32_bf16 v[224:227], v[152:155], v[128:131], v[224:227]\n"
    "v_mfma_f32_16x16x32_bf16 v[224:227], v[184:187], v[160:163], v[224:227]\n"
    "v_mfma_f32_16x16x32_bf16 v[228:231], v[184:187], v[164:167], v[228:231]\n"
    "v_add_f32 v222, v222, v66\n"
    "v_add_f32 v223, v223, v67\n"
    "v_mfma_f32_16x16x32_bf16 v[228:231], v[152:155], v[132:135], v[228:231]\n"
    "v_add_f32 v221, v221, v65\n"
    "v_mfma_f32_16x16x32_bf16 v[232:235], v[152:155], v[136:139], v[232:235]\n"
    "v_add_f32 v220, v220, v64\n"
    "v_mfma_f32_16x16x32_bf16 v[232:235], v[184:187], v[168:171], v[232:235]\n"
    "v_add_f32 v219, v219, v67\n"
    "v_mfma_f32_16x16x32_bf16 v[236:239], v[184:187], v[172:175], v[236:239]\n"
    "v_add_f32 v217, v217, v65\n"
    "v_add_f32 v218, v218, v66\n"
    "v_mfma_f32_16x16x32_bf16 v[236:239], v[152:155], v[140:143], v[236:239]\n"
    "v_add_f32 v216, v216, v64\n"
    "v_mfma_f32_16x16x32_bf16 v[252:255], v[156:159], v[140:143], v[252:255]\n"
    "v_add_f32 v215, v215, v67\n"
    "v_mfma_f32_16x16x32_bf16 v[252:255], v[188:191], v[172:175], v[252:255]\n"
    "v_add_f32 v214, v214, v66\n"
    "v_mfma_f32_16x16x32_bf16 v[248:251], v[188:191], v[168:171], v[248:251]\n"
    "v_add_f32 v212, v212, v64\n"
    "v_add_f32 v213, v213, v65\n"
    "v_mfma_f32_16x16x32_bf16 v[248:251], v[156:159], v[136:139], v[248:251]\n"
    "v_add_f32 v211, v211, v67\n"
    "v_mfma_f32_16x16x32_bf16 v[244:247], v[156:159], v[132:135], v[244:247]\n"
    "v_add_f32 v210, v210, v66\n"
    "v_mfma_f32_16x16x32_bf16 v[244:247], v[188:191], v[164:167], v[244:247]\n"
    "v_add_f32 v209, v209, v65\n"
    "v_mfma_f32_16x16x32_bf16 v[240:243], v[188:191], v[160:163], v[240:243]\n"
    "v_add_f32 v208, v208, v64\n"
    "v_mfma_f32_16x16x32_bf16 v[240:243], v[156:159], v[128:131], v[240:243]\n"
    "s_waitcnt vmcnt(7)\n"
    "s_barrier\n"
    "s_add_u32 m0, s35, 98304\n"
    "s_nop 0\n"
    "global_load_lds_dwordx4 v46, s[30:31] offset:0\n"
    "ds_read_b128 v[128:131], v56 offset:0\n"
    "ds_read_b128 v[132:135], v56 offset:2048\n"
    "ds_read_b128 v[136:139], v56 offset:4096\n"
    "ds_read_b128 v[140:143], v56 offset:6144\n"
    "global_load_lds_dwordx4 v47, s[30:31] offset:1024\n"
    "s_add_u32 s30, s30, 212992\n"
    "s_addc_u32 s31, s31, 0\n"
    "ds_read_b64_tr_b16 v[144:145], v76 offset:0\n"
    "ds_read_b64_tr_b16 v[146:147], v76 offset:1024\n"
    "ds_read_b64_tr_b16 v[148:149], v77 offset:0\n"
    "ds_read_b64_tr_b16 v[150:151], v77 offset:1024\n"
    "s_add_u32 m0, s34, 98304\n"
    "s_nop 0\n"
    "global_load_lds_dwordx4 v52, s[36:37] offset:0\n"
    "ds_read_b64_tr_b16 v[152:153], v78 offset:0\n"
    "ds_read_b64_tr_b16 v[154:155], v78 offset:1024\n"
    "ds_read_b64_tr_b16 v[156:157], v79 offset:0\n"
    "ds_read_b64_tr_b16 v[158:159], v79 offset:1024\n"
    "global_load_lds_dwordx4 v53, s[36:37] offset:1024\n"
    "ds_read_b128 v[160:163], v57 offset:0\n"
    "ds_read_b128 v[164:167], v57 offset:2048\n"
    "ds_read_b128 v[168:171], v57 offset:4096\n"
    "ds_read_b128 v[172:175], v57 offset:6144\n"
    "global_load_lds_dwordx4 v54, s[36:37] offset:2048\n"
    "ds_read_b64_tr_b16 v[176:177], v76 offset:8192\n"
    "ds_read_b64_tr_b16 v[178:179], v76 offset:9216\n"
    "ds_read_b64_tr_b16 v[180:181], v77 offset:8192\n"
    "ds_read_b64_tr_b16 v[182:183], v77 offset:9216\n"
    "global_load_lds_dwordx4 v55, s[36:37] offset:3072\n"
    "ds_read_b64_tr_b16 v[184:185], v78 offset:8192\n"
    "ds_read_b64_tr_b16 v[186:187], v78 offset:9216\n"
    "ds_read_b64_tr_b16 v[188:189], v79 offset:8192\n"
    "ds_read_b64_tr_b16 v[190:191], v79 offset:9216\n"
    "s_waitcnt vmcnt(10) lgkmcnt(0)\n"
    "s_barrier\n"
    "v_mfma_f32_16x16x32_bf16 v[192:195], v[144:147], v[128:131], v[192:195]\n"
    "v_lshlrev_b32 v115, 7, v115\n"
    "v_mfma_f32_16x16x32_bf16 v[192:195], v[176:179], v[160:163], v[192:195]\n"
    "v_add_u32_dpp v52, v114, v48 quad_perm:[0,0,0,0] row_mask:0xf bank_mask:0xf\n"
    "v_mfma_f32_16x16x32_bf16 v[196:199], v[176:179], v[164:167], v[196:199]\n"
    "v_add_u32_dpp v53, v114, v49 quad_perm:[1,1,1,1] row_mask:0xf bank_mask:0xf\n"
    "v_mfma_f32_16x16x32_bf16 v[196:199], v[144:147], v[132:135], v[196:199]\n"
    "v_add_u32_dpp v54, v114, v50 quad_perm:[2,2,2,2] row_mask:0xf bank_mask:0xf\n"
    "v_mfma_f32_16x16x32_bf16 v[200:203], v[144:147], v[136:139], v[200:203]\n"
    "v_add_u32_dpp v55, v114, v51 quad_perm:[3,3,3,3] row_mask:0xf bank_mask:0xf\n"
    "v_mfma_f32_16x16x32_bf16 v[200:203], v[176:179], v[168:171], v[200:203]\n"
    "v_lshl_or_b32 v36, v36, 7, v19\n"
    "v_mfma_f32_16x16x32_bf16 v[204:207], v[176:179], v[172:175], v[204:207]\n"
    "v_mfma_f32_16x16x32_bf16 v[204:207], v[144:147], v[140:143], v[204:207]\n"
    "v_lshl_or_b32 v37, v37, 7, v19\n"
    "v_mfma_f32_16x16x32_bf16 v[220:223], v[148:151], v[140:143], v[220:223]\n"
    "v_lshl_or_b32 v38, v38, 7, v19\n"
    "v_mfma_f32_16x16x32_bf16 v[220:223], v[180:183], v[172:175], v[220:223]\n"
    "v_lshl_or_b32 v41, v41, 7, v20\n"
    "v_mfma_f32_16x16x32_bf16 v[216:219], v[180:183], v[168:171], v[216:219]\n"
    "s_mov_b32 s64, 0xbdd2d3e8\n"
    "v_mfma_f32_16x16x32_bf16 v[216:219], v[148:151], v[136:139], v[216:219]\n"
    "s_mov_b32 s65, 0xc0135761\n"
    "v_mfma_f32_16x16x32_bf16 v[212:215], v[148:151], v[132:135], v[212:215]\n"
    "v_mfma_f32_16x16x32_bf16 v[212:215], v[180:183], v[164:167], v[212:215]\n"
    "s_and_b32 s41, s20, 1\n"
    "v_mfma_f32_16x16x32_bf16 v[208:211], v[180:183], v[160:163], v[208:211]\n"
    "s_lshl1_add_u32 s40, s21, s41\n"
    "v_mfma_f32_16x16x32_bf16 v[208:211], v[148:151], v[128:131], v[208:211]\n"
    "s_lshl_b32 s41, s40, 8\n"
    "v_mfma_f32_16x16x32_bf16 v[224:227], v[152:155], v[128:131], v[224:227]\n"
    "s_add_u32 s3, s41, 149504\n"
    "v_mfma_f32_16x16x32_bf16 v[224:227], v[184:187], v[160:163], v[224:227]\n"
    "s_lshl_b32 s40, s40, 6\n"
    "v_mfma_f32_16x16x32_bf16 v[228:231], v[184:187], v[164:167], v[228:231]\n"
    "v_mfma_f32_16x16x32_bf16 v[228:231], v[152:155], v[132:135], v[228:231]\n"
    "s_add_u32 s40, s40, s24\n"
    "v_mfma_f32_16x16x32_bf16 v[232:235], v[152:155], v[136:139], v[232:235]\n"
    "s_lshl_b32 s41, s23, 12\n"
    "v_mfma_f32_16x16x32_bf16 v[232:235], v[184:187], v[168:171], v[232:235]\n"
    "s_add_u32 s40, s40, s41\n"
    "v_mfma_f32_16x16x32_bf16 v[236:239], v[184:187], v[172:175], v[236:239]\n"
    "s_lshl3_add_u32 s40, s40, 65536\n"
    "v_mfma_f32_16x16x32_bf16 v[236:239], v[152:155], v[140:143], v[236:239]\n"
    "s_add_u32 s48, s40, s16\n"
    "v_mfma_f32_16x16x32_bf16 v[252:255], v[156:159], v[140:143], v[252:255]\n"
    "v_mfma_f32_16x16x32_bf16 v[252:255], v[188:191], v[172:175], v[252:255]\n"
    "s_addc_u32 s49, s17, 0\n"
    "v_mfma_f32_16x16x32_bf16 v[248:251], v[188:191], v[168:171], v[248:251]\n"
    "s_cmp_lt_u32 s22, 2\n"
    "v_mfma_f32_16x16x32_bf16 v[248:251], v[156:159], v[136:139], v[248:251]\n"
    "s_cselect_b32 s24, 16, 0\n"
    "v_mfma_f32_16x16x32_bf16 v[244:247], v[156:159], v[132:135], v[244:247]\n"
    "s_sub_u32 s23, 1, s22\n"
    "v_mfma_f32_16x16x32_bf16 v[244:247], v[188:191], v[164:167], v[244:247]\n"
    "s_and_b32 s23, s23, 3\n"
    "v_mfma_f32_16x16x32_bf16 v[240:243], v[188:191], v[160:163], v[240:243]\n"
    "v_mfma_f32_16x16x32_bf16 v[240:243], v[156:159], v[128:131], v[240:243]\n"
    "s_waitcnt vmcnt(6)\n"
    "s_barrier\n"
    "s_add_u32 m0, s35, 0\n"
    "s_nop 0\n"
    "global_load_lds_dwordx4 v46, s[30:31] offset:0\n"
    "ds_read_b128 v[128:131], v56 offset:49152\n"
    "ds_read_b128 v[132:135], v56 offset:51200\n"
    "ds_read_b128 v[136:139], v56 offset:53248\n"
    "global_load_lds_dwordx4 v47, s[30:31] offset:1024\n"
    "s_add_u32 s30, s30, 212992\n"
    "s_addc_u32 s31, s31, 0\n"
    "ds_read_b128 v[140:143], v56 offset:55296\n"
    "ds_read_b64_tr_b16 v[144:145], v76 offset:49152\n"
    "ds_read_b64_tr_b16 v[146:147], v76 offset:50176\n"
    "s_add_u32 m0, s34, 0\n"
    "s_nop 0\n"
    "global_load_lds_dwordx4 v52, s[36:37] offset:0\n"
    "ds_read_b64_tr_b16 v[148:149], v77 offset:49152\n"
    "ds_read_b64_tr_b16 v[150:151], v77 offset:50176\n"
    "ds_read_b64_tr_b16 v[152:153], v78 offset:49152\n"
    "global_load_lds_dwordx4 v53, s[36:37] offset:1024\n"
    "ds_read_b64_tr_b16 v[154:155], v78 offset:50176\n"
    "ds_read_b64_tr_b16 v[156:157], v79 offset:49152\n"
    "ds_read_b64_tr_b16 v[158:159], v79 offset:50176\n"
    "global_load_lds_dwordx4 v54, s[36:37] offset:2048\n"
    "ds_read_b128 v[160:163], v57 offset:49152\n"
    "ds_read_b128 v[164:167], v57 offset:51200\n"
    "ds_read_b128 v[168:171], v57 offset:53248\n"
    "global_load_lds_dwordx4 v55, s[36:37] offset:3072\n"
    "ds_read_b128 v[172:175], v57 offset:55296\n"
    "ds_read_b64_tr_b16 v[176:177], v76 offset:57344\n"
    "ds_read_b64_tr_b16 v[178:179], v76 offset:58368\n"
    "global_load_dwordx4 v[28:31], v36, s[8:9]\n"
    "ds_read_b64_tr_b16 v[180:181], v77 offset:57344\n"
    "ds_read_b64_tr_b16 v[182:183], v77 offset:58368\n"
    "ds_read_b64_tr_b16 v[184:185], v78 offset:57344\n"
    "ds_read_b64_tr_b16 v[186:187], v78 offset:58368\n"
    "ds_read_b64_tr_b16 v[188:189], v79 offset:57344\n"
    "ds_read_b64_tr_b16 v[190:191], v79 offset:58368\n"
    "s_waitcnt vmcnt(11) lgkmcnt(0)\n"
    "s_barrier\n"
    "v_mfma_f32_16x16x32_bf16 v[192:195], v[144:147], v[128:131], v[192:195]\n"
    "v_mfma_f32_16x16x32_bf16 v[192:195], v[176:179], v[160:163], v[192:195]\n"
    "v_add_f32 v224, v224, v68\n"
    "v_add_f32 v225, v225, v69\n"
    "v_mfma_f32_16x16x32_bf16 v[196:199], v[176:179], v[164:167], v[196:199]\n"
    "v_add_f32 v226, v226, v70\n"
    "v_mfma_f32_16x16x32_bf16 v[196:199], v[144:147], v[132:135], v[196:199]\n"
    "v_add_f32 v227, v227, v71\n"
    "v_mfma_f32_16x16x32_bf16 v[200:203], v[144:147], v[136:139], v[200:203]\n"
    "v_add_f32 v228, v228, v68\n"
    "v_mfma_f32_16x16x32_bf16 v[200:203], v[176:179], v[168:171], v[200:203]\n"
    "v_add_f32 v229, v229, v69\n"
    "v_add_f32 v230, v230, v70\n"
    "v_mfma_f32_16x16x32_bf16 v[204:207], v[176:179], v[172:175], v[204:207]\n"
    "v_add_f32 v231, v231, v71\n"
    "v_mfma_f32_16x16x32_bf16 v[204:207], v[144:147], v[140:143], v[204:207]\n"
    "v_add_f32 v232, v232, v68\n"
    "v_mfma_f32_16x16x32_bf16 v[220:223], v[148:151], v[140:143], v[220:223]\n"
    "v_add_f32 v233, v233, v69\n"
    "v_mfma_f32_16x16x32_bf16 v[220:223], v[180:183], v[172:175], v[220:223]\n"
    "v_add_f32 v234, v234, v70\n"
    "v_add_f32 v235, v235, v71\n"
    "v_mfma_f32_16x16x32_bf16 v[216:219], v[180:183], v[168:171], v[216:219]\n"
    "v_add_f32 v236, v236, v68\n"
    "v_mfma_f32_16x16x32_bf16 v[216:219], v[148:151], v[136:139], v[216:219]\n"
    "v_add_f32 v237, v237, v69\n"
    "v_mfma_f32_16x16x32_bf16 v[212:215], v[148:151], v[132:135], v[212:215]\n"
    "v_add_f32 v238, v238, v70\n"
    "v_mfma_f32_16x16x32_bf16 v[212:215], v[180:183], v[164:167], v[212:215]\n"
    "v_add_f32 v239, v239, v71\n"
    "v_mfma_f32_16x16x32_bf16 v[208:211], v[180:183], v[160:163], v[208:211]\n"
    "v_mfma_f32_16x16x32_bf16 v[208:211], v[148:151], v[128:131], v[208:211]\n"
    "v_mfma_f32_16x16x32_bf16 v[224:227], v[152:155], v[128:131], v[224:227]\n"
    "v_mfma_f32_16x16x32_bf16 v[224:227], v[184:187], v[160:163], v[224:227]\n"
    "v_mfma_f32_16x16x32_bf16 v[228:231], v[184:187], v[164:167], v[228:231]\n"
    "v_lshlrev_b32 v116, 7, v116\n"
    "v_mfma_f32_16x16x32_bf16 v[228:231], v[152:155], v[132:135], v[228:231]\n"
    "v_add_u32_dpp v52, v115, v48 quad_perm:[0,0,0,0] row_mask:0xf bank_mask:0xf\n"
    "v_mfma_f32_16x16x32_bf16 v[232:235], v[152:155], v[136:139], v[232:235]\n"
    "v_add_u32_dpp v53, v115, v49 quad_perm:[1,1,1,1] row_mask:0xf bank_mask:0xf\n"
    "v_mfma_f32_16x16x32_bf16 v[232:235], v[184:187], v[168:171], v[232:235]\n"
    "v_add_u32_dpp v54, v115, v50 quad_perm:[2,2,2,2] row_mask:0xf bank_mask:0xf\n"
    "v_mfma_f32_16x16x32_bf16 v[236:239], v[184:187], v[172:175], v[236:239]\n"
    "v_add_u32_dpp v55, v115, v51 quad_perm:[3,3,3,3] row_mask:0xf bank_mask:0xf\n"
    "v_mfma_f32_16x16x32_bf16 v[236:239], v[152:155], v[140:143], v[236:239]\n"
    "s_getpc_b64 s[10:11]\n"
    ".Lxd_en2:\n"
    "v_mfma_f32_16x16x32_bf16 v[252:255], v[156:159], v[140:143], v[252:255]\n"
    "s_mov_b32 s40, .Lxd_ph1-.Lxd_en2\n"
    "v_mfma_f32_16x16x32_bf16 v[252:255], v[188:191], v[172:175], v[252:255]\n"
    "s_cmp_eq_u32 s22, 1\n"
    "s_cselect_b32 s40, .Lxd_ph0-.Lxd_en2, s40\n"
    "v_mfma_f32_16x16x32_bf16 v[248:251], v[188:191], v[168:171], v[248:251]\n"
    "s_cmp_eq_u32 s22, 2\n"
    "s_cselect_b32 s40, .Lxd_pl3-.Lxd_en2, s40\n"
    "v_mfma_f32_16x16x32_bf16 v[248:251], v[156:159], v[136:139], v[248:251]\n"
    "s_cmp_eq_u32 s22, 3\n"
    "s_cselect_b32 s40, .Lxd_ph2-.Lxd_en2, s40\n"
    "v_mfma_f32_16x16x32_bf16 v[244:247], v[156:159], v[132:135], v[244:247]\n"
    "s_ashr_i32 s41, s40, 31\n"
    "v_mfma_f32_16x16x32_bf16 v[244:247], v[188:191], v[164:167], v[244:247]\n"
    "s_add_u32 s10, s10, s40\n"
    "s_addc_u32 s11, s11, s41\n"
    "v_mfma_f32_16x16x32_bf16 v[240:243], v[188:191], v[160:163], v[240:243]\n"
    "v_mfma_f32_16x16x32_bf16 v[240:243], v[156:159], v[128:131], v[240:243]\n"
    "s_waitcnt vmcnt(7)\n"
    "s_barrier\n"
    "s_add_u32 m0, s35, 49152\n"
    "s_nop 0\n"
    "global_load_lds_dwordx4 v46, s[30:31] offset:0\n"
    "ds_read_b128 v[128:131], v58 offset:0\n"
    "ds_read_b128 v[132:135], v58 offset:2048\n"
    "ds_read_b128 v[136:139], v58 offset:4096\n"
    "global_load_lds_dwordx4 v47, s[30:31] offset:1024\n"
    "s_bfe_u32 s66, s29, 0x80000\n"
    "s_mul_i32 s66, s66, 53248\n"
    "s_add_u32 s30, s6, s66\n"
    "s_addc_u32 s31, s7, 0\n"
    "ds_read_b128 v[140:143], v58 offset:6144\n"
    "ds_read_b64_tr_b16 v[144:145], v80 offset:0\n"
    "ds_read_b64_tr_b16 v[146:147], v80 offset:1024\n"
    "s_add_u32 m0, s34, 49152\n"
    "s_nop 0\n"
    "global_load_lds_dwordx4 v52, s[36:37] offset:0\n"
    "ds_read_b64_tr_b16 v[148:149], v81 offset:0\n"
    "ds_read_b64_tr_b16 v[150:151], v81 offset:1024\n"
    "ds_read_b64_tr_b16 v[152:153], v82 offset:0\n"
    "global_load_lds_dwordx4 v53, s[36:37] offset:1024\n"
    "ds_read_b64_tr_b16 v[154:155], v82 offset:1024\n"
    "ds_read_b64_tr_b16 v[156:157], v83 offset:0\n"
    "ds_read_b64_tr_b16 v[158:159], v83 offset:1024\n"
    "global_load_lds_dwordx4 v54, s[36:37] offset:2048\n"
    "ds_read_b128 v[160:163], v59 offset:0\n"
    "ds_read_b128 v[164:167], v59 offset:2048\n"
    "ds_read_b128 v[168:171], v59 offset:4096\n"
    "global_load_lds_dwordx4 v55, s[36:37] offset:3072\n"
    "ds_read_b128 v[172:175], v59 offset:6144\n"
    "ds_read_b64_tr_b16 v[176:177], v80 offset:8192\n"
    "ds_read_b64_tr_b16 v[178:179], v80 offset:9216\n"
    "global_load_dwordx4 v[32:35], v37, s[8:9]\n"
    "ds_read_b64_tr_b16 v[180:181], v81 offset:8192\n"
    "ds_read_b64_tr_b16 v[182:183], v81 offset:9216\n"
    "ds_read_b64_tr_b16 v[184:185], v82 offset:8192\n"
    "ds_read_b64_tr_b16 v[186:187], v82 offset:9216\n"
    "ds_read_b64_tr_b16 v[188:189], v83 offset:8192\n"
    "ds_read_b64_tr_b16 v[190:191], v83 offset:9216\n"
    "s_waitcnt vmcnt(12) lgkmcnt(0)\n"
    "s_barrier\n"
    "v_mfma_f32_16x16x32_bf16 v[192:195], v[144:147], v[128:131], v[192:195]\n"
    "v_mfma_f32_16x16x32_bf16 v[192:195], v[176:179], v[160:163], v[192:195]\n"
    "v_mfma_f32_16x16x32_bf16 v[196:199], v[176:179], v[164:167], v[196:199]\n"
    "v_mfma_f32_16x16x32_bf16 v[196:199], v[144:147], v[132:135], v[196:199]\n"
    "v_mfma_f32_16x16x32_bf16 v[200:203], v[144:147], v[136:139], v[200:203]\n"
    "v_add_f32 v240, v240, v72\n"
    "v_mfma_f32_16x16x32_bf16 v[200:203], v[176:179], v[168:171], v[200:203]\n"
    "v_add_f32 v241, v241, v73\n"
    "v_mfma_f32_16x16x32_bf16 v[204:207], v[176:179], v[172:175], v[204:207]\n"
    "v_add_f32 v242, v242, v74\n"
    "v_mfma_f32_16x16x32_bf16 v[204:207], v[144:147], v[140:143], v[204:207]\n"
    "v_add_f32 v243, v243, v75\n"
    "v_mfma_f32_16x16x32_bf16 v[220:223], v[148:151], v[140:143], v[220:223]\n"
    "v_add_f32 v244, v244, v72\n"
    "v_mfma_f32_16x16x32_bf16 v[220:223], v[180:183], v[172:175], v[220:223]\n"
    "v_add_f32 v245, v245, v73\n"
    "v_mfma_f32_16x16x32_bf16 v[216:219], v[180:183], v[168:171], v[216:219]\n"
    "v_add_f32 v246, v246, v74\n"
    "v_mfma_f32_16x16x32_bf16 v[216:219], v[148:151], v[136:139], v[216:219]\n"
    "v_add_f32 v247, v247, v75\n"
    "v_mfma_f32_16x16x32_bf16 v[212:215], v[148:151], v[132:135], v[212:215]\n"
    "v_add_f32 v248, v248, v72\n"
    "v_mfma_f32_16x16x32_bf16 v[212:215], v[180:183], v[164:167], v[212:215]\n"
    "v_add_f32 v249, v249, v73\n"
    "v_mfma_f32_16x16x32_bf16 v[208:211], v[180:183], v[160:163], v[208:211]\n"
    "v_add_f32 v250, v250, v74\n"
    "v_mfma_f32_16x16x32_bf16 v[208:211], v[148:151], v[128:131], v[208:211]\n"
    "v_add_f32 v251, v251, v75\n"
    "v_mfma_f32_16x16x32_bf16 v[224:227], v[152:155], v[128:131], v[224:227]\n"
    "v_add_f32 v252, v252, v72\n"
    "v_mfma_f32_16x16x32_bf16 v[224:227], v[184:187], v[160:163], v[224:227]\n"
    "v_add_f32 v253, v253, v73\n"
    "v_mfma_f32_16x16x32_bf16 v[228:231], v[184:187], v[164:167], v[228:231]\n"
    "v_lshlrev_b32 v117, 7, v117\n"
    "v_add_f32 v254, v254, v74\n"
    "v_mfma_f32_16x16x32_bf16 v[228:231], v[152:155], v[132:135], v[228:231]\n"
    "v_add_f32 v255, v255, v75\n"
    "v_mfma_f32_16x16x32_bf16 v[232:235], v[152:155], v[136:139], v[232:235]\n"
    "v_add_u32_dpp v52, v116, v48 quad_perm:[0,0,0,0] row_mask:0xf bank_mask:0xf\n"
    "v_mfma_f32_16x16x32_bf16 v[232:235], v[184:187], v[168:171], v[232:235]\n"
    "v_mfma_f32_16x16x32_bf16 v[236:239], v[184:187], v[172:175], v[236:239]\n"
    "v_mfma_f32_16x16x32_bf16 v[236:239], v[152:155], v[140:143], v[236:239]\n"
    "v_add_u32_dpp v53, v116, v49 quad_perm:[1,1,1,1] row_mask:0xf bank_mask:0xf\n"
    "v_mfma_f32_16x16x32_bf16 v[252:255], v[156:159], v[140:143], v[252:255]\n"
    "v_mfma_f32_16x16x32_bf16 v[252:255], v[188:191], v[172:175], v[252:255]\n"
    "v_add_u32_dpp v54, v116, v50 quad_perm:[2,2,2,2] row_mask:0xf bank_mask:0xf\n"
    "v_mfma_f32_16x16x32_bf16 v[248:251], v[188:191], v[168:171], v[248:251]\n"
    "v_mfma_f32_16x16x32_bf16 v[248:251], v[156:159], v[136:139], v[248:251]\n"
    "v_mfma_f32_16x16x32_bf16 v[244:247], v[156:159], v[132:135], v[244:247]\n"
    "v_add_u32_dpp v55, v116, v51 quad_perm:[3,3,3,3] row_mask:0xf bank_mask:0xf\n"
    "v_mfma_f32_16x16x32_bf16 v[244:247], v[188:191], v[164:167], v[244:247]\n"
    "v_mfma_f32_16x16x32_bf16 v[240:243], v[188:191], v[160:163], v[240:243]\n"
    "v_mfma_f32_16x16x32_bf16 v[240:243], v[156:159], v[128:131], v[240:243]\n"
    "s_waitcnt vmcnt(8)\n"
    "s_barrier\n"
    "s_add_u32 m0, s35, 98304\n"
    "s_nop 0\n"
    "global_load_lds_dwordx4 v46, s[30:31] offset:0\n"
    "ds_read_b128 v[128:131], v56 offset:0\n"
    "ds_read_b128 v[132:135], v56 offset:2048\n"
    "ds_read_b128 v[136:139], v56 offset:4096\n"
    "global_load_lds_dwordx4 v47, s[30:31] offset:1024\n"
    "s_add_u32 s30, s30, 212992\n"
    "s_addc_u32 s31, s31, 0\n"
    "ds_read_b128 v[140:143], v56 offset:6144\n"
    "ds_read_b64_tr_b16 v[144:145], v76 offset:0\n"
    "ds_read_b64_tr_b16 v[146:147], v76 offset:1024\n"
    "s_add_u32 m0, s34, 98304\n"
    "s_nop 0\n"
    "global_load_lds_dwordx4 v52, s[36:37] offset:0\n"
    "ds_read_b64_tr_b16 v[148:149], v77 offset:0\n"
    "ds_read_b64_tr_b16 v[150:151], v77 offset:1024\n"
    "ds_read_b64_tr_b16 v[152:153], v78 offset:0\n"
    "global_load_lds_dwordx4 v53, s[36:37] offset:1024\n"
    "ds_read_b64_tr_b16 v[154:155], v78 offset:1024\n"
    "ds_read_b64_tr_b16 v[156:157], v79 offset:0\n"
    "ds_read_b64_tr_b16 v[158:159], v79 offset:1024\n"
    "global_load_lds_dwordx4 v54, s[36:37] offset:2048\n"
    "ds_read_b128 v[160:163], v57 offset:0\n"
    "ds_read_b128 v[164:167], v57 offset:2048\n"
    "ds_read_b128 v[168:171], v57 offset:4096\n"
    "global_load_lds_dwordx4 v55, s[36:37] offset:3072\n"
    "ds_read_b128 v[172:175], v57 offset:6144\n"
    "ds_read_b64_tr_b16 v[176:177], v76 offset:8192\n"
    "ds_read_b64_tr_b16 v[178:179], v76 offset:9216\n"
    "global_load_dword v40, v41, s[8:9]\n"
    "ds_read_b64_tr_b16 v[180:181], v77 offset:8192\n"
    "ds_read_b64_tr_b16 v[182:183], v77 offset:9216\n"
    "ds_read_b64_tr_b16 v[184:185], v78 offset:8192\n"
    "ds_read_b64_tr_b16 v[186:187], v78 offset:9216\n"
    "ds_read_b64_tr_b16 v[188:189], v79 offset:8192\n"
    "ds_read_b64_tr_b16 v[190:191], v79 offset:9216\n"
    "s_waitcnt vmcnt(12) lgkmcnt(0)\n"
    "s_barrier\n"
    "v_mfma_f32_16x16x32_bf16 v[192:195], v[144:147], v[128:131], v[192:195]\n"
    "v_lshlrev_b32 v118, 7, v118\n"
    "v_mfma_f32_16x16x32_bf16 v[192:195], v[176:179], v[160:163], v[192:195]\n"
    "v_add_u32_dpp v52, v117, v48 quad_perm:[0,0,0,0] row_mask:0xf bank_mask:0xf\n"
    "v_mfma_f32_16x16x32_bf16 v[196:199], v[176:179], v[164:167], v[196:199]\n"
    "v_add_u32_dpp v53, v117, v49 quad_perm:[1,1,1,1] row_mask:0xf bank_mask:0xf\n"
    "v_mfma_f32_16x16x32_bf16 v[196:199], v[144:147], v[132:135], v[196:199]\n"
    "v_add_u32_dpp v54, v117, v50 quad_perm:[2,2,2,2] row_mask:0xf bank_mask:0xf\n"
    "v_mfma_f32_16x16x32_bf16 v[200:203], v[144:147], v[136:139], v[200:203]\n"
    "v_add_u32_dpp v55, v117, v51 quad_perm:[3,3,3,3] row_mask:0xf bank_mask:0xf\n"
    "v_mfma_f32_16x16x32_bf16 v[200:203], v[176:179], v[168:171], v[200:203]\n"
    "v_readlane_b32 s25, v26, 9\n"
    "v_mfma_f32_16x16x32_bf16 v[204:207], v[176:179], v[172:175], v[204:207]\n"
    "v_readlane_b32 s40, v26, 8\n"
    "v_mfma_f32_16x16x32_bf16 v[204:207], v[144:147], v[140:143], v[204:207]\n"
    "v_readlane_b32 s41, v27, 8\n"
    "v_mfma_f32_16x16x32_bf16 v[220:223], v[148:151], v[140:143], v[220:223]\n"
    "s_mul_i32 s42, s25, 0x9e3779b1\n"
    "v_mfma_f32_16x16x32_bf16 v[220:223], v[180:183], v[172:175], v[220:223]\n"
    "s_lshr_b32 s42, s42, 24\n"
    "v_mfma_f32_16x16x32_bf16 v[216:219], v[180:183], v[168:171], v[216:219]\n"
    "s_xor_b32 s42, s42, s40\n"
    "v_mfma_f32_16x16x32_bf16 v[216:219], v[148:151], v[136:139], v[216:219]\n"
    "s_and_b32 s42, s42, 0xff\n"
    "v_mfma_f32_16x16x32_bf16 v[212:215], v[148:151], v[132:135], v[212:215]\n"
    "s_xor_b32 s41, s41, s25\n"
    "v_mfma_f32_16x16x32_bf16 v[212:215], v[180:183], v[164:167], v[212:215]\n"
    "s_xor_b32 s41, s41, 0xa5c30f96\n"
    "v_mfma_f32_16x16x32_bf16 v[208:211], v[180:183], v[160:163], v[208:211]\n"
    "s_or_b32 s42, s41, s42\n"
    "v_mfma_f32_16x16x32_bf16 v[208:211], v[148:151], v[128:131], v[208:211]\n"
    "v_mfma_f32_16x16x32_bf16 v[224:227], v[152:155], v[128:131], v[224:227]\n"
    "s_lshr_b32 s41, s40, 8\n"
    "v_mfma_f32_16x16x32_bf16 v[224:227], v[184:187], v[160:163], v[224:227]\n"
    "s_mul_i32 s40, s41, 0x9e3779b1\n"
    "v_mfma_f32_16x16x32_bf16 v[228:231], v[184:187], v[164:167], v[228:231]\n"
    "s_xor_b32 s41, s41, 0xa5c30f96\n"
    "v_mfma_f32_16x16x32_bf16 v[228:231], v[152:155], v[132:135], v[228:231]\n"
    "s_lshr_b32 s40, s40, 24\n"
    "v_mfma_f32_16x16x32_bf16 v[232:235], v[152:155], v[136:139], v[232:235]\n"
    "v_xor_b32 v13, s40, v26\n"
    "v_mfma_f32_16x16x32_bf16 v[232:235], v[184:187], v[168:171], v[232:235]\n"
    "v_xor_b32 v22, s41, v27\n"
    "v_mfma_f32_16x16x32_bf16 v[236:239], v[184:187], v[172:175], v[236:239]\n"
    "v_lshl_or_b32 v13, v13, 24, v22\n"
    "v_mfma_f32_16x16x32_bf16 v[236:239], v[152:155], v[140:143], v[236:239]\n"
    "v_cmp_ne_u32 s[66:67], 0, v13\n"
    "v_mfma_f32_16x16x32_bf16 v[252:255], v[156:159], v[140:143], v[252:255]\n"
    "s_and_b32 s66, s66, 0xff\n"
    "v_mfma_f32_16x16x32_bf16 v[252:255], v[188:191], v[172:175], v[252:255]\n"
    "s_or_b32 s42, s42, s66\n"
    "v_mfma_f32_16x16x32_bf16 v[248:251], v[188:191], v[168:171], v[248:251]\n"
    "s_cmp_eq_u32 s20, 3\n"
    "v_mfma_f32_16x16x32_bf16 v[248:251], v[156:159], v[136:139], v[248:251]\n"
    "s_cselect_b32 s42, s42, 0\n"
    "v_mfma_f32_16x16x32_bf16 v[244:247], v[156:159], v[132:135], v[244:247]\n"
    "s_cmp_lg_u32 s42, 0\n"
    "v_mfma_f32_16x16x32_bf16 v[244:247], v[188:191], v[164:167], v[244:247]\n"
    "s_cbranch_scc1 .Lxd_pv3\n"
    ".Lxd_pv3r:\n"
    "v_mfma_f32_16x16x32_bf16 v[240:243], v[188:191], v[160:163], v[240:243]\n"
    "v_mfma_f32_16x16x32_bf16 v[240:243], v[156:159], v[128:131], v[240:243]\n"
    "s_waitcnt vmcnt(8)\n"
    "s_barrier\n"
    "s_add_u32 m0, s35, 0\n"
    "s_nop 0\n"
    "global_load_lds_dwordx4 v46, s[30:31] offset:0\n"
    "ds_read_b128 v[128:131], v56 offset:49152\n"
    "ds_read_b128 v[132:135], v56 offset:51200\n"
    "ds_read_b128 v[136:139], v56 offset:53248\n"
    "global_load_lds_dwordx4 v47, s[30:31] offset:1024\n"
    "s_add_u32 s30, s30, 212992\n"
    "s_addc_u32 s31, s31, 0\n"
    "ds_read_b128 v[140:143], v56 offset:55296\n"
    "ds_read_b64_tr_b16 v[144:145], v76 offset:49152\n"
    "ds_read_b64_tr_b16 v[146:147], v76 offset:50176\n"
    "s_add_u32 m0, s34, 0\n"
    "s_nop 0\n"
    "global_load_lds_dwordx4 v52, s[36:37] offset:0\n"
    "ds_read_b64_tr_b16 v[148:149], v77 offset:49152\n"
    "ds_read_b64_tr_b16 v[150:151], v77 offset:50176\n"
    "ds_read_b64_tr_b16 v[152:153], v78 offset:49152\n"
    "global_load_lds_dwordx4 v53, s[36:37] offset:1024\n"
    "ds_read_b64_tr_b16 v[154:155], v78 offset:50176\n"
    "ds_read_b64_tr_b16 v[156:157], v79 offset:49152\n"
    "ds_read_b64_tr_b16 v[158:159], v79 offset:50176\n"
    "global_load_lds_dwordx4 v54, s[36:37] offset:2048\n"
    "ds_read_b128 v[160:163], v57 offset:49152\n"
    "ds_read_b128 v[164:167], v57 offset:51200\n"
    "ds_read_b128 v[168:171], v57 offset:53248\n"
    "global_load_lds_dwordx4 v55, s[36:37] offset:3072\n"
    "ds_read_b128 v[172:175], v57 offset:55296\n"
    "ds_read_b64_tr_b16 v[176:177], v76 offset:57344\n"
    "ds_read_b64_tr_b16 v[178:179], v76 offset:58368\n"
    "global_load_dwordx4 v[36:39], v38, s[8:9]\n"
    "ds_read_b64_tr_b16 v[180:181], v77 offset:57344\n"
    "ds_read_b64_tr_b16 v[182:183], v77 offset:58368\n"
    "ds_read_b64_tr_b16 v[184:185], v78 offset:57344\n"
    "ds_read_b64_tr_b16 v[186:187], v78 offset:58368\n"
    "ds_read_b64_tr_b16 v[188:189], v79 offset:57344\n"
    "ds_read_b64_tr_b16 v[190:191], v79 offset:58368\n"
    "s_waitcnt vmcnt(12) lgkmcnt(0)\n"
    "s_barrier\n"
    "v_mfma_f32_16x16x32_bf16 v[192:195], v[144:147], v[128:131], v[192:195]\n"
    "v_lshlrev_b32 v119, 7, v119\n"
    "v_mfma_f32_16x16x32_bf16 v[192:195], v[176:179], v[160:163], v[192:195]\n"
    "v_add_u32_dpp v52, v118, v48 quad_perm:[0,0,0,0] row_mask:0xf bank_mask:0xf\n"
    "v_mfma_f32_16x16x32_bf16 v[196:199], v[176:179], v[164:167], v[196:199]\n"
    "v_add_u32_dpp v53, v118, v49 quad_perm:[1,1,1,1] row_mask:0xf bank_mask:0xf\n"
    "v_mfma_f32_16x16x32_bf16 v[196:199], v[144:147], v[132:135], v[196:199]\n"
    "v_mfma_f32_16x16x32_bf16 v[200:203], v[144:147], v[136:139], v[200:203]\n"
    "v_add_u32_dpp v54, v118, v50 quad_perm:[2,2,2,2] row_mask:0xf bank_mask:0xf\n"
    "v_mfma_f32_16x16x32_bf16 v[200:203], v[176:179], v[168:171], v[200:203]\n"
    "v_add_u32_dpp v55, v118, v51 quad_perm:[3,3,3,3] row_mask:0xf bank_mask:0xf\n"
    "v_mfma_f32_16x16x32_bf16 v[204:207], v[176:179], v[172:175], v[204:207]\n"
    "v_mfma_f32_16x16x32_bf16 v[204:207], v[144:147], v[140:143], v[204:207]\n"
    "v_lshlrev_b32 v60, 16, v92\n"
    "v_mfma_f32_16x16x32_bf16 v[220:223], v[148:151], v[140:143], v[220:223]\n"
    "v_and_b32 v61, 0xffff0000, v92\n"
    "v_mfma_f32_16x16x32_bf16 v[220:223], v[180:183], v[172:175], v[220:223]\n"
    "v_mfma_f32_16x16x32_bf16 v[216:219], v[180:183], v[168:171], v[216:219]\n"
    "v_lshlrev_b32 v62, 16, v93\n"
    "v_mfma_f32_16x16x32_bf16 v[216:219], v[148:151], v[136:139], v[216:219]\n"
    "v_and_b32 v63, 0xffff0000, v93\n"
    "v_mfma_f32_16x16x32_bf16 v[212:215], v[148:151], v[132:135], v[212:215]\n"
    "v_mfma_f32_16x16x32_bf16 v[212:215], v[180:183], v[164:167], v[212:215]\n"
    "v_lshlrev_b32 v64, 16, v94\n"
    "v_mfma_f32_16x16x32_bf16 v[208:211], v[180:183], v[160:163], v[208:211]\n"
    "v_and_b32 v65, 0xffff0000, v94\n"
    "v_mfma_f32_16x16x32_bf16 v[208:211], v[148:151], v[128:131], v[208:211]\n"
    "v_mfma_f32_16x16x32_bf16 v[224:227], v[152:155], v[128:131], v[224:227]\n"
    "v_lshlrev_b32 v66, 16, v95\n"
    "v_mfma_f32_16x16x32_bf16 v[224:227], v[184:187], v[160:163], v[224:227]\n"
    "v_and_b32 v67, 0xffff0000, v95\n"
    "v_mfma_f32_16x16x32_bf16 v[228:231], v[184:187], v[164:167], v[228:231]\n"
    "v_mfma_f32_16x16x32_bf16 v[228:231], v[152:155], v[132:135], v[228:231]\n"
    "v_lshlrev_b32 v68, 16, v122\n"
    "v_mfma_f32_16x16x32_bf16 v[232:235], v[152:155], v[136:139], v[232:235]\n"
    "v_and_b32 v69, 0xffff0000, v122\n"
    "v_mfma_f32_16x16x32_bf16 v[232:235], v[184:187], v[168:171], v[232:235]\n"
    "v_mfma_f32_16x16x32_bf16 v[236:239], v[184:187], v[172:175], v[236:239]\n"
    "v_lshlrev_b32 v70, 16, v123\n"
    "v_mfma_f32_16x16x32_bf16 v[236:239], v[152:155], v[140:143], v[236:239]\n"
    "v_and_b32 v71, 0xffff0000, v123\n"
    "v_mfma_f32_16x16x32_bf16 v[252:255], v[156:159], v[140:143], v[252:255]\n"
    "v_mfma_f32_16x16x32_bf16 v[252:255], v[188:191], v[172:175], v[252:255]\n"
    "v_lshlrev_b32 v72, 16, v124\n"
    "v_mfma_f32_16x16x32_bf16 v[248:251], v[188:191], v[168:171], v[248:251]\n"
    "v_and_b32 v73, 0xffff0000, v124\n"
    "v_mfma_f32_16x16x32_bf16 v[248:251], v[156:159], v[136:139], v[248:251]\n"
    "v_mfma_f32_16x16x32_bf16 v[244:247], v[156:159], v[132:135], v[244:247]\n"
    "v_lshlrev_b32 v74, 16, v125\n"
    "v_mfma_f32_16x16x32_bf16 v[244:247], v[188:191], v[164:167], v[244:247]\n"
    "v_and_b32 v75, 0xffff0000, v125\n"
    "v_mfma_f32_16x16x32_bf16 v[240:243], v[188:191], v[160:163], v[240:243]\n"
    "v_mfma_f32_16x16x32_bf16 v[240:243], v[156:159], v[128:131], v[240:243]\n"
    "s_waitcnt vmcnt(8)\n"
    "s_barrier\n"
    "s_add_u32 m0, s35, 49152\n"
    "s_nop 0\n"
    "global_load_lds_dwordx4 v46, s[30:31] offset:0\n"
    "ds_read_b128 v[128:131], v58 offset:0\n"
    "ds_read_b128 v[132:135], v58 offset:2048\n"
    "ds_read_b128 v[136:139], v58 offset:4096\n"
    "ds_read_b128 v[140:143], v58 offset:6144\n"
    "global_load_lds_dwordx4 v47, s[30:31] offset:1024\n"
    "s_add_u32 s30, s30, 212992\n"
    "s_addc_u32 s31, s31, 0\n"
    "ds_read_b64_tr_b16 v[144:145], v80 offset:0\n"
    "ds_read_b64_tr_b16 v[146:147], v80 offset:1024\n"
    "ds_read_b64_tr_b16 v[148:149], v81 offset:0\n"
    "ds_read_b64_tr_b16 v[150:151], v81 offset:1024\n"
    "s_add_u32 m0, s34, 49152\n"
    "s_nop 0\n"
    "global_load_lds_dwordx4 v52, s[36:37] offset:0\n"
    "ds_read_b64_tr_b16 v[152:153], v82 offset:0\n"
    "ds_read_b64_tr_b16 v[154:155], v82 offset:1024\n"
    "ds_read_b64_tr_b16 v[156:157], v83 offset:0\n"
    "ds_read_b64_tr_b16 v[158:159], v83 offset:1024\n"
    "global_load_lds_dwordx4 v53, s[36:37] offset:1024\n"
    "ds_read_b128 v[160:163], v59 offset:0\n"
    "ds_read_b128 v[164:167], v59 offset:2048\n"
    "ds_read_b128 v[168:171], v59 offset:4096\n"
    "ds_read_b128 v[172:175], v59 offset:6144\n"
    "global_load_lds_dwordx4 v54, s[36:37] offset:2048\n"
    "ds_read_b64_tr_b16 v[176:177], v80 offset:8192\n"
    "ds_read_b64_tr_b16 v[178:179], v80 offset:9216\n"
    "ds_read_b64_tr_b16 v[180:181], v81 offset:8192\n"
    "ds_read_b64_tr_b16 v[182:183], v81 offset:9216\n"
    "global_load_lds_dwordx4 v55, s[36:37] offset:3072\n"
    "ds_read_b64_tr_b16 v[184:185], v82 offset:8192\n"
    "ds_read_b64_tr_b16 v[186:187], v82 offset:9216\n"
    "ds_read_b64_tr_b16 v[188:189], v83 offset:8192\n"
    "ds_read_b64_tr_b16 v[190:191], v83 offset:9216\n"
    "s_waitcnt vmcnt(11) lgkmcnt(0)\n"
    "s_barrier\n"
    "v_mfma_f32_16x16x32_bf16 v[192:195], v[144:147], v[128:131], v[192:195]\n"
    "v_lshlrev_b32 v120, 7, v120\n"
    "v_mfma_f32_16x16x32_bf16 v[192:195], v[176:179], v[160:163], v[192:195]\n"
    "v_add_u32_dpp v52, v119, v48 quad_perm:[0,0,0,0] row_mask:0xf bank_mask:0xf\n"
    "v_mfma_f32_16x16x32_bf16 v[196:199], v[176:179], v[164:167], v[196:199]\n"
    "v_add_u32_dpp v53, v119, v49 quad_perm:[1,1,1,1] row_mask:0xf bank_mask:0xf\n"
    "v_mfma_f32_16x16x32_bf16 v[196:199], v[144:147], v[132:135], v[196:199]\n"
    "v_add_u32_dpp v54, v119, v50 quad_perm:[2,2,2,2] row_mask:0xf bank_mask:0xf\n"
    "v_mfma_f32_16x16x32_bf16 v[200:203], v[144:147], v[136:139], v[200:203]\n"
    "v_add_u32_dpp v55, v119, v51 quad_perm:[3,3,3,3] row_mask:0xf bank_mask:0xf\n"
    "v_mfma_f32_16x16x32_bf16 v[200:203], v[176:179], v[168:171], v[200:203]\n"
    "v_lshrrev_b32 v22, 8, v26\n"
    "v_mfma_f32_16x16x32_bf16 v[204:207], v[176:179], v[172:175], v[204:207]\n"
    "v_mfma_f32_16x16x32_bf16 v[204:207], v[144:147], v[140:143], v[204:207]\n"
    "v_mov_b32 v26, s40\n"
    "v_mfma_f32_16x16x32_bf16 v[220:223], v[148:151], v[140:143], v[220:223]\n"
    "v_mov_b32 v27, s41\n"
    "v_mfma_f32_16x16x32_bf16 v[220:223], v[180:183], v[172:175], v[220:223]\n"
    "v_add_u32_dpp v22, v22, v22 row_shl:4 row_mask:0xf bank_mask:0xf\n"
    "v_mfma_f32_16x16x32_bf16 v[216:219], v[180:183], v[168:171], v[216:219]\n"
    "v_add_u32_e64 v43, s25, 1\n"
    "v_mfma_f32_16x16x32_bf16 v[216:219], v[148:151], v[136:139], v[216:219]\n"
    "v_lshlrev_b32 v8, 2, v1\n"
    "v_mfma_f32_16x16x32_bf16 v[212:215], v[148:151], v[132:135], v[212:215]\n"
    "v_mfma_f32_16x16x32_bf16 v[212:215], v[180:183], v[164:167], v[212:215]\n"
    "v_max_u32_dpp v13, v22, v22 quad_perm:[1,0,3,2] row_mask:0xf bank_mask:0xf\n"
    "v_mfma_f32_16x16x32_bf16 v[208:211], v[180:183], v[160:163], v[208:211]\n"
    "v_add_u32 v8, 154112, v8\n"
    "v_mfma_f32_16x16x32_bf16 v[208:211], v[148:151], v[128:131], v[208:211]\n"
    "s_nop 0\n"
    "v_mfma_f32_16x16x32_bf16 v[224:227], v[152:155], v[128:131], v[224:227]\n"
    "v_max_u32_dpp v13, v13, v13 quad_perm:[2,3,0,1] row_mask:0xf bank_mask:0xf\n"
    "v_mfma_f32_16x16x32_bf16 v[224:227], v[184:187], v[160:163], v[224:227]\n"
    "v_sub_u32 v22, v22, v13\n"
    "v_mfma_f32_16x16x32_bf16 v[228:231], v[184:187], v[164:167], v[228:231]\n"
    "v_mfma_f32_16x16x32_bf16 v[228:231], v[152:155], v[132:135], v[228:231]\n"
    "v_subrev_u32 v13, 818, v13\n"
    "v_mfma_f32_16x16x32_bf16 v[232:235], v[152:155], v[136:139], v[232:235]\n"
    "v_ldexp_f32 v22, 1.0, v22\n"
    "v_mfma_f32_16x16x32_bf16 v[232:235], v[184:187], v[168:171], v[232:235]\n"
    "v_rcp_f32 v60, v60\n"
    "v_mfma_f32_16x16x32_bf16 v[236:239], v[184:187], v[172:175], v[236:239]\n"
    "v_rcp_f32 v61, v61\n"
    "v_mfma_f32_16x16x32_bf16 v[236:239], v[152:155], v[140:143], v[236:239]\n"
    "v_rcp_f32 v62, v62\n"
    "v_mfma_f32_16x16x32_bf16 v[252:255], v[156:159], v[140:143], v[252:255]\n"
    "v_mfma_f32_16x16x32_bf16 v[252:255], v[188:191], v[172:175], v[252:255]\n"
    "v_rcp_f32 v63, v63\n"
    "v_mfma_f32_16x16x32_bf16 v[248:251], v[188:191], v[168:171], v[248:251]\n"
    "v_rcp_f32 v64, v64\n"
    "v_mfma_f32_16x16x32_bf16 v[248:251], v[156:159], v[136:139], v[248:251]\n"
    "v_rcp_f32 v65, v65\n"
    "v_mfma_f32_16x16x32_bf16 v[244:247], v[156:159], v[132:135], v[244:247]\n"
    "v_rcp_f32 v66, v66\n"
    "v_mfma_f32_16x16x32_bf16 v[244:247], v[188:191], v[164:167], v[244:247]\n"
    "v_rcp_f32 v67, v67\n"
    "v_mfma_f32_16x16x32_bf16 v[240:243], v[188:191], v[160:163], v[240:243]\n"
    "v_mfma_f32_16x16x32_bf16 v[240:243], v[156:159], v[128:131], v[240:243]\n"
    "s_waitcnt vmcnt(7)\n"
    "s_barrier\n"
    "s_add_u32 m0, s35, 98304\n"
    "s_nop 0\n"
    "global_load_lds_dwordx4 v46, s[30:31] offset:0\n"
    "ds_read_b128 v[128:131], v56 offset:0\n"
    "ds_read_b128 v[132:135], v56 offset:2048\n"
    "ds_read_b128 v[136:139], v56 offset:4096\n"
    "ds_read_b128 v[140:143], v56 offset:6144\n"
    "global_load_lds_dwordx4 v47, s[30:31] offset:1024\n"
    "s_bfe_u32 s66, s29, 0x80008\n"
    "s_mul_i32 s66, s66, 53248\n"
    "s_add_u32 s30, s6, s66\n"
    "s_addc_u32 s31, s7, 0\n"
    "ds_read_b64_tr_b16 v[144:145], v76 offset:0\n"
    "ds_read_b64_tr_b16 v[146:147], v76 offset:1024\n"
    "ds_read_b64_tr_b16 v[148:149], v77 offset:0\n"
    "ds_read_b64_tr_b16 v[150:151], v77 offset:1024\n"
    "s_add_u32 m0, s34, 98304\n"
    "s_nop 0\n"
    "global_load_lds_dwordx4 v52, s[36:37] offset:0\n"
    "ds_read_b64_tr_b16 v[152:153], v78 offset:0\n"
    "ds_read_b64_tr_b16 v[154:155], v78 offset:1024\n"
    "ds_read_b64_tr_b16 v[156:157], v79 offset:0\n"
    "ds_read_b64_tr_b16 v[158:159], v79 offset:1024\n"
    "global_load_lds_dwordx4 v53, s[36:37] offset:1024\n"
    "ds_read_b128 v[160:163], v57 offset:0\n"
    "ds_read_b128 v[164:167], v57 offset:2048\n"
    "ds_read_b128 v[168:171], v57 offset:4096\n"
    "ds_read_b128 v[172:175], v57 offset:6144\n"
    "global_load_lds_dwordx4 v54, s[36:37] offset:2048\n"
    "ds_read_b64_tr_b16 v[176:177], v76 offset:8192\n"
    "ds_read_b64_tr_b16 v[178:179], v76 offset:9216\n"
    "ds_read_b64_tr_b16 v[180:181], v77 offset:8192\n"
    "ds_read_b64_tr_b16 v[182:183], v77 offset:9216\n"
    "global_load_lds_dwordx4 v55, s[36:37] offset:3072\n"
    "ds_read_b64_tr_b16 v[184:185], v78 offset:8192\n"
    "ds_read_b64_tr_b16 v[186:187], v78 offset:9216\n"
    "ds_read_b64_tr_b16 v[188:189], v79 offset:8192\n"
    "ds_read_b64_tr_b16 v[190:191], v79 offset:9216\n"
    "s_waitcnt vmcnt(10) lgkmcnt(0)\n"
    "s_barrier\n"
    "v_mfma_f32_16x16x32_bf16 v[192:195], v[144:147], v[128:131], v[192:195]\n"
    "v_lshlrev_b32 v121, 7, v121\n"
    "v_mfma_f32_16x16x32_bf16 v[192:195], v[176:179], v[160:163], v[192:195]\n"
    "v_add_u32_dpp v52, v120, v48 quad_perm:[0,0,0,0] row_mask:0xf bank_mask:0xf\n"
    "v_mfma_f32_16x16x32_bf16 v[196:199], v[176:179], v[164:167], v[196:199]\n"
    "v_add_u32_dpp v53, v120, v49 quad_perm:[1,1,1,1] row_mask:0xf bank_mask:0xf\n"
    "v_mfma_f32_16x16x32_bf16 v[196:199], v[144:147], v[132:135], v[196:199]\n"
    "v_add_u32_dpp v54, v120, v50 quad_perm:[2,2,2,2] row_mask:0xf bank_mask:0xf\n"
    "v_mfma_f32_16x16x32_bf16 v[200:203], v[144:147], v[136:139], v[200:203]\n"
    "v_add_u32_dpp v55, v120, v51 quad_perm:[3,3,3,3] row_mask:0xf bank_mask:0xf\n"
    "v_mfma_f32_16x16x32_bf16 v[200:203], v[176:179], v[168:171], v[200:203]\n"
    "s_movk_i32 s43, 15744\n"
    "v_pk_maximum3_f16 v21, v28, v28, s43 op_sel_hi:[1,1,0] neg_lo:[0,1,0] neg_hi:[0,1,0]\n"
    "v_mfma_f32_16x16x32_bf16 v[204:207], v[176:179], v[172:175], v[204:207]\n"
    "v_pk_maximum3_f16 v21, v29, v29, v21 neg_lo:[0,1,0] neg_hi:[0,1,0]\n"
    "v_mfma_f32_16x16x32_bf16 v[204:207], v[144:147], v[140:143], v[204:207]\n"
    "v_mfma_f32_16x16x32_bf16 v[220:223], v[148:151], v[140:143], v[220:223]\n"
    "v_pk_maximum3_f16 v21, v30, v30, v21 neg_lo:[0,1,0] neg_hi:[0,1,0]\n"
    "v_mfma_f32_16x16x32_bf16 v[220:223], v[180:183], v[172:175], v[220:223]\n"
    "v_pk_maximum3_f16 v21, v31, v31, v21 neg_lo:[0,1,0] neg_hi:[0,1,0]\n"
    "v_mfma_f32_16x16x32_bf16 v[216:219], v[180:183], v[168:171], v[216:219]\n"
    "v_pk_maximum3_f16 v21, v32, v32, v21 neg_lo:[0,1,0] neg_hi:[0,1,0]\n"
    "v_mfma_f32_16x16x32_bf16 v[216:219], v[148:151], v[136:139], v[216:219]\n"
    "v_pk_maximum3_f16 v21, v33, v33, v21 neg_lo:[0,1,0] neg_hi:[0,1,0]\n"
    "v_mfma_f32_16x16x32_bf16 v[212:215], v[148:151], v[132:135], v[212:215]\n"
    "v_pk_maximum3_f16 v21, v34, v34, v21 neg_lo:[0,1,0] neg_hi:[0,1,0]\n"
    "v_mfma_f32_16x16x32_bf16 v[212:215], v[180:183], v[164:167], v[212:215]\n"
    "v_pk_maximum3_f16 v21, v35, v35, v21 neg_lo:[0,1,0] neg_hi:[0,1,0]\n"
    "v_mfma_f32_16x16x32_bf16 v[208:211], v[180:183], v[160:163], v[208:211]\n"
    "v_pk_maximum3_f16 v21, v36, v36, v21 neg_lo:[0,1,0] neg_hi:[0,1,0]\n"
    "v_mfma_f32_16x16x32_bf16 v[208:211], v[148:151], v[128:131], v[208:211]\n"
    "v_mfma_f32_16x16x32_bf16 v[224:227], v[152:155], v[128:131], v[224:227]\n"
    "v_pk_maximum3_f16 v21, v37, v37, v21 neg_lo:[0,1,0] neg_hi:[0,1,0]\n"
    "v_mfma_f32_16x16x32_bf16 v[224:227], v[184:187], v[160:163], v[224:227]\n"
    "v_pk_maximum3_f16 v21, v38, v38, v21 neg_lo:[0,1,0] neg_hi:[0,1,0]\n"
    "v_mfma_f32_16x16x32_bf16 v[228:231], v[184:187], v[164:167], v[228:231]\n"
    "v_pk_maximum3_f16 v21, v39, v39, v21 neg_lo:[0,1,0] neg_hi:[0,1,0]\n"
    "v_mfma_f32_16x16x32_bf16 v[228:231], v[152:155], v[132:135], v[228:231]\n"
    "v_pk_maximum3_f16 v21, v40, v40, v21 neg_lo:[0,1,0] neg_hi:[0,1,0]\n"
    "v_mfma_f32_16x16x32_bf16 v[232:235], v[152:155], v[136:139], v[232:235]\n"
    "v_pk_max_u16 v21, v21, v21 op_sel:[0,1] op_sel_hi:[1,0]\n"
    "v_mfma_f32_16x16x32_bf16 v[232:235], v[184:187], v[168:171], v[232:235]\n"
    "v_rcp_f32 v68, v68\n"
    "v_mfma_f32_16x16x32_bf16 v[236:239], v[184:187], v[172:175], v[236:239]\n"
    "v_rcp_f32 v69, v69\n"
    "v_mfma_f32_16x16x32_bf16 v[236:239], v[152:155], v[140:143], v[236:239]\n"
    "v_mfma_f32_16x16x32_bf16 v[252:255], v[156:159], v[140:143], v[252:255]\n"
    "v_rcp_f32 v70, v70\n"
    "v_mfma_f32_16x16x32_bf16 v[252:255], v[188:191], v[172:175], v[252:255]\n"
    "v_rcp_f32 v71, v71\n"
    "v_mfma_f32_16x16x32_bf16 v[248:251], v[188:191], v[168:171], v[248:251]\n"
    "v_rcp_f32 v72, v72\n"
    "v_mfma_f32_16x16x32_bf16 v[248:251], v[156:159], v[136:139], v[248:251]\n"
    "v_rcp_f32 v73, v73\n"
    "v_mfma_f32_16x16x32_bf16 v[244:247], v[156:159], v[132:135], v[244:247]\n"
    "v_rcp_f32 v74, v74\n"
    "v_mfma_f32_16x16x32_bf16 v[244:247], v[188:191], v[164:167], v[244:247]\n"
    "v_rcp_f32 v75, v75\n"
    "v_mfma_f32_16x16x32_bf16 v[240:243], v[188:191], v[160:163], v[240:243]\n"
    "v_mfma_f32_16x16x32_bf16 v[240:243], v[156:159], v[128:131], v[240:243]\n"
    "s_waitcnt vmcnt(6)\n"
    "s_barrier\n"
    "s_add_u32 m0, s35, 0\n"
    "s_nop 0\n"
    "global_load_lds_dwordx4 v46, s[30:31] offset:0\n"
    "ds_read_b128 v[128:131], v56 offset:49152\n"
    "ds_read_b128 v[132:135], v56 offset:51200\n"
    "ds_read_b128 v[136:139], v56 offset:53248\n"
    "ds_read_b128 v[140:143], v56 offset:55296\n"
    "global_load_lds_dwordx4 v47, s[30:31] offset:1024\n"
    "s_add_u32 s30, s30, 212992\n"
    "s_addc_u32 s31, s31, 0\n"
    "ds_read_b64_tr_b16 v[144:145], v76 offset:49152\n"
    "ds_read_b64_tr_b16 v[146:147], v76 offset:50176\n"
    "ds_read_b64_tr_b16 v[148:149], v77 offset:49152\n"
    "ds_read_b64_tr_b16 v[150:151], v77 offset:50176\n"
    "s_add_u32 m0, s34, 0\n"
    "s_nop 0\n"
    "global_load_lds_dwordx4 v52, s[36:37] offset:0\n"
    "ds_read_b64_tr_b16 v[152:153], v78 offset:49152\n"
    "ds_read_b64_tr_b16 v[154:155], v78 offset:50176\n"
    "ds_read_b64_tr_b16 v[156:157], v79 offset:49152\n"
    "ds_read_b64_tr_b16 v[158:159], v79 offset:50176\n"
    "global_load_lds_dwordx4 v53, s[36:37] offset:1024\n"
    "ds_read_b128 v[160:163], v57 offset:49152\n"
    "ds_read_b128 v[164:167], v57 offset:51200\n"
    "ds_read_b128 v[168:171], v57 offset:53248\n"
    "ds_read_b128 v[172:175], v57 offset:55296\n"
    "global_load_lds_dwordx4 v54, s[36:37] offset:2048\n"
    "ds_read_b64_tr_b16 v[176:177], v76 offset:57344\n"
    "ds_read_b64_tr_b16 v[178:179], v76 offset:58368\n"
    "ds_read_b64_tr_b16 v[180:181], v77 offset:57344\n"
    "ds_read_b64_tr_b16 v[182:183], v77 offset:58368\n"
    "global_load_lds_dwordx4 v55, s[36:37] offset:3072\n"
    "ds_read_b64_tr_b16 v[184:185], v78 offset:57344\n"
    "ds_read_b64_tr_b16 v[186:187], v78 offset:58368\n"
    "ds_read_b64_tr_b16 v[188:189], v79 offset:57344\n"
    "ds_read_b64_tr_b16 v[190:191], v79 offset:58368\n"
    "s_waitcnt vmcnt(10) lgkmcnt(0)\n"
    "s_barrier\n"
    "v_mfma_f32_16x16x32_bf16 v[192:195], v[144:147], v[128:131], v[192:195]\n"
    "v_add_u32_dpp v52, v121, v48 quad_perm:[0,0,0,0] row_mask:0xf bank_mask:0xf\n"
    "s_mov_b32 s42, 0x71800000\n"
    "v_mfma_f32_16x16x32_bf16 v[192:195], v[176:179], v[160:163], v[192:195]\n"
    "v_add_u32_dpp v53, v121, v49 quad_perm:[1,1,1,1] row_mask:0xf bank_mask:0xf\n"
    "v_mfma_f32_16x16x32_bf16 v[196:199], v[176:179], v[164:167], v[196:199]\n"
    "v_add_u32_dpp v54, v121, v50 quad_perm:[2,2,2,2] row_mask:0xf bank_mask:0xf\n"
    "v_mfma_f32_16x16x32_bf16 v[196:199], v[144:147], v[132:135], v[196:199]\n"
    "v_add_u32_dpp v55, v121, v51 quad_perm:[3,3,3,3] row_mask:0xf bank_mask:0xf\n"
    "v_mfma_f32_16x16x32_bf16 v[200:203], v[144:147], v[136:139], v[200:203]\n"
    "v_bfe_u32 v41, v1, 4, 1\n"
    "v_mfma_f32_16x16x32_bf16 v[200:203], v[176:179], v[168:171], v[200:203]\n"
    "v_lshlrev_b32 v41, 2, v41\n"
    "v_mfma_f32_16x16x32_bf16 v[204:207], v[176:179], v[172:175], v[204:207]\n"
    "v_add_u32 v41, 154112, v41\n"
    "v_mfma_f32_16x16x32_bf16 v[204:207], v[144:147], v[140:143], v[204:207]\n"
    "v_med3_f32 v60, v60, s42, -s42\n"
    "v_mfma_f32_16x16x32_bf16 v[220:223], v[148:151], v[140:143], v[220:223]\n"
    "v_max_u32_dpp v21, v21, v21 quad_perm:[1,0,3,2] row_mask:0xf bank_mask:0xf\n"
    "v_mfma_f32_16x16x32_bf16 v[220:223], v[180:183], v[172:175], v[220:223]\n"
    "v_med3_f32 v61, v61, s42, -s42\n"
    "v_mfma_f32_16x16x32_bf16 v[216:219], v[180:183], v[168:171], v[216:219]\n"
    "v_max_u32_dpp v21, v21, v21 quad_perm:[2,3,0,1] row_mask:0xf bank_mask:0xf\n"
    "v_mfma_f32_16x16x32_bf16 v[216:219], v[148:151], v[136:139], v[216:219]\n"
    "v_med3_f32 v62, v62, s42, -s42\n"
    "v_mfma_f32_16x16x32_bf16 v[212:215], v[148:151], v[132:135], v[212:215]\n"
    "v_max_u32_dpp v21, v21, v21 row_half_mirror row_mask:0xf bank_mask:0xf\n"
    "v_mfma_f32_16x16x32_bf16 v[212:215], v[180:183], v[164:167], v[212:215]\n"
    "v_med3_f32 v63, v63, s42, -s42\n"
    "v_mfma_f32_16x16x32_bf16 v[208:211], v[180:183], v[160:163], v[208:211]\n"
    "v_max_u32_dpp v21, v21, v21 row_mirror row_mask:0xf bank_mask:0xf\n"
    "v_mfma_f32_16x16x32_bf16 v[208:211], v[148:151], v[128:131], v[208:211]\n"
    "v_med3_f32 v64, v64, s42, -s42\n"
    "v_mfma_f32_16x16x32_bf16 v[224:227], v[152:155], v[128:131], v[224:227]\n"
    "v_max_u32_dpp v21, v21, v21 row_bcast:15 row_mask:0xa bank_mask:0xf\n"
    "v_mfma_f32_16x16x32_bf16 v[224:227], v[184:187], v[160:163], v[224:227]\n"
    "v_med3_f32 v65, v65, s42, -s42\n"
    "v_mfma_f32_16x16x32_bf16 v[228:231], v[184:187], v[164:167], v[228:231]\n"
    "v_max_u32_dpp v21, v21, v21 row_bcast:31 row_mask:0xc bank_mask:0xf\n"
    "v_mfma_f32_16x16x32_bf16 v[228:231], v[152:155], v[132:135], v[228:231]\n"
    "v_med3_f32 v66, v66, s42, -s42\n"
    "v_mfma_f32_16x16x32_bf16 v[232:235], v[152:155], v[136:139], v[232:235]\n"
    "v_readlane_b32 s47, v21, 63\n"
    "v_mfma_f32_16x16x32_bf16 v[232:235], v[184:187], v[168:171], v[232:235]\n"
    "v_med3_f32 v67, v67, s42, -s42\n"
    "v_mfma_f32_16x16x32_bf16 v[236:239], v[184:187], v[172:175], v[236:239]\n"
    "s_and_b32 s50, s47, 0x7f80\n"
    "v_mfma_f32_16x16x32_bf16 v[236:239], v[152:155], v[140:143], v[236:239]\n"
    "s_lshr_b32 s47, s50, 3\n"
    "v_mfma_f32_16x16x32_bf16 v[252:255], v[156:159], v[140:143], v[252:255]\n"
    "s_sub_u32 s50, s50, 2816\n"
    "v_med3_f32 v68, v68, s42, -s42\n"
    "v_mfma_f32_16x16x32_bf16 v[252:255], v[188:191], v[172:175], v[252:255]\n"
    "s_mul_i32 s51, s50, -8\n"
    "v_med3_f32 v69, v69, s42, -s42\n"
    "v_mfma_f32_16x16x32_bf16 v[248:251], v[188:191], v[168:171], v[248:251]\n"
    "v_med3_f32 v70, v70, s42, -s42\n"
    "v_mfma_f32_16x16x32_bf16 v[248:251], v[156:159], v[136:139], v[248:251]\n"
    "v_med3_f32 v71, v71, s42, -s42\n"
    "v_mfma_f32_16x16x32_bf16 v[244:247], v[156:159], v[132:135], v[244:247]\n"
    "v_med3_f32 v72, v72, s42, -s42\n"
    "v_mfma_f32_16x16x32_bf16 v[244:247], v[188:191], v[164:167], v[244:247]\n"
    "v_med3_f32 v73, v73, s42, -s42\n"
    "v_mfma_f32_16x16x32_bf16 v[240:243], v[188:191], v[160:163], v[240:243]\n"
    "v_med3_f32 v74, v74, s42, -s42\n"
    "v_mfma_f32_16x16x32_bf16 v[240:243], v[156:159], v[128:131], v[240:243]\n"
    "v_med3_f32 v75, v75, s42, -s42\n"
    "s_waitcnt vmcnt(6)\n"
    "s_barrier\n"
    "s_add_u32 m0, s35, 49152\n"
    "s_nop 0\n"
    "global_load_lds_dwordx4 v46, s[30:31] offset:0\n"
    "ds_read_b128 v[128:131], v58 offset:0\n"
    "ds_read_b128 v[132:135], v58 offset:2048\n"
    "ds_read_b128 v[136:139], v58 offset:4096\n"
    "ds_read_b128 v[140:143], v58 offset:6144\n"
    "global_load_lds_dwordx4 v47, s[30:31] offset:1024\n"
    "ds_read_b64_tr_b16 v[144:145], v80 offset:0\n"
    "ds_read_b64_tr_b16 v[146:147], v80 offset:1024\n"
    "ds_read_b64_tr_b16 v[148:149], v81 offset:0\n"
    "ds_read_b64_tr_b16 v[150:151], v81 offset:1024\n"
    "s_add_u32 m0, s34, 49152\n"
    "s_nop 0\n"
    "global_load_lds_dwordx4 v52, s[36:37] offset:0\n"
    "ds_read_b64_tr_b16 v[152:153], v82 offset:0\n"
    "ds_read_b64_tr_b16 v[154:155], v82 offset:1024\n"
    "ds_read_b64_tr_b16 v[156:157], v83 offset:0\n"
    "ds_read_b64_tr_b16 v[158:159], v83 offset:1024\n"
    "global_load_lds_dwordx4 v53, s[36:37] offset:1024\n"
    "ds_read_b128 v[160:163], v59 offset:0\n"
    "ds_read_b128 v[164:167], v59 offset:2048\n"
    "ds_read_b128 v[168:171], v59 offset:4096\n"
    "ds_read_b128 v[172:175], v59 offset:6144\n"
    "global_load_lds_dwordx4 v54, s[36:37] offset:2048\n"
    "ds_read_b64_tr_b16 v[176:177], v80 offset:8192\n"
    "ds_read_b64_tr_b16 v[178:179], v80 offset:9216\n"
    "ds_read_b64_tr_b16 v[180:181], v81 offset:8192\n"
    "ds_read_b64_tr_b16 v[182:183], v81 offset:9216\n"
    "global_load_lds_dwordx4 v55, s[36:37] offset:3072\n"
    "ds_read_b64_tr_b16 v[184:185], v82 offset:8192\n"
    "ds_read_b64_tr_b16 v[186:187], v82 offset:9216\n"
    "ds_read_b64_tr_b16 v[188:189], v83 offset:8192\n"
    "ds_read_b64_tr_b16 v[190:191], v83 offset:9216\n"
    "s_waitcnt vmcnt(10) lgkmcnt(0)\n"
    "s_barrier\n"
    "v_mfma_f32_16x16x32_bf16 v[192:195], v[144:147], v[128:131], v[192:195]\n"
    "v_pk_maximum3_f16 v21, v28, v28, s50 op_sel_hi:[1,1,0] neg_lo:[0,1,0] neg_hi:[0,1,0]\n"
    "v_mfma_f32_16x16x32_bf16 v[192:195], v[176:179], v[160:163], v[192:195]\n"
    "v_pk_mad_u16 v21, v21, 8, s51 op_sel_hi:[1,0,0]\n"
    "v_mfma_f32_16x16x32_bf16 v[196:199], v[176:179], v[164:167], v[196:199]\n"
    "v_bfi_b32 v28, s57, v21, v28\n"
    "v_mfma_f32_16x16x32_bf16 v[196:199], v[144:147], v[132:135], v[196:199]\n"
    "v_mfma_f32_16x16x32_bf16 v[200:203], v[144:147], v[136:139], v[200:203]\n"
    "v_pk_maximum3_f16 v12, v29, v29, s50 op_sel_hi:[1,1,0] neg_lo:[0,1,0] neg_hi:[0,1,0]\n"
    "v_mfma_f32_16x16x32_bf16 v[200:203], v[176:179], v[168:171], v[200:203]\n"
    "v_pk_mad_u16 v12, v12, 8, s51 op_sel_hi:[1,0,0]\n"
    "v_mfma_f32_16x16x32_bf16 v[204:207], v[176:179], v[172:175], v[204:207]\n"
    "v_bfi_b32 v29, s57, v12, v29\n"
    "v_mfma_f32_16x16x32_bf16 v[204:207], v[144:147], v[140:143], v[204:207]\n"
    "v_mfma_f32_16x16x32_bf16 v[220:223], v[148:151], v[140:143], v[220:223]\n"
    "v_pk_maximum3_f16 v21, v30, v30, s50 op_sel_hi:[1,1,0] neg_lo:[0,1,0] neg_hi:[0,1,0]\n"
    "v_mfma_f32_16x16x32_bf16 v[220:223], v[180:183], v[172:175], v[220:223]\n"
    "v_pk_mad_u16 v21, v21, 8, s51 op_sel_hi:[1,0,0]\n"
    "v_mfma_f32_16x16x32_bf16 v[216:219], v[180:183], v[168:171], v[216:219]\n"
    "v_bfi_b32 v30, s57, v21, v30\n"
    "v_mfma_f32_16x16x32_bf16 v[216:219], v[148:151], v[136:139], v[216:219]\n"
    "v_mfma_f32_16x16x32_bf16 v[212:215], v[148:151], v[132:135], v[212:215]\n"
    "v_pk_maximum3_f16 v12, v31, v31, s50 op_sel_hi:[1,1,0] neg_lo:[0,1,0] neg_hi:[0,1,0]\n"
    "v_mfma_f32_16x16x32_bf16 v[212:215], v[180:183], v[164:167], v[212:215]\n"
    "v_pk_mad_u16 v12, v12, 8, s51 op_sel_hi:[1,0,0]\n"
    "v_mfma_f32_16x16x32_bf16 v[208:211], v[180:183], v[160:163], v[208:211]\n"
    "v_bfi_b32 v31, s57, v12, v31\n"
    "v_mfma_f32_16x16x32_bf16 v[208:211], v[148:151], v[128:131], v[208:211]\n"
    "v_mfma_f32_16x16x32_bf16 v[224:227], v[152:155], v[128:131], v[224:227]\n"
    "v_pk_maximum3_f16 v21, v32, v32, s50 op_sel_hi:[1,1,0] neg_lo:[0,1,0] neg_hi:[0,1,0]\n"
    "v_mfma_f32_16x16x32_bf16 v[224:227], v[184:187], v[160:163], v[224:227]\n"
    "v_pk_mad_u16 v21, v21, 8, s51 op_sel_hi:[1,0,0]\n"
    "v_mfma_f32_16x16x32_bf16 v[228:231], v[184:187], v[164:167], v[228:231]\n"
    "v_bfi_b32 v32, s57, v21, v32\n"
    "v_mfma_f32_16x16x32_bf16 v[228:231], v[152:155], v[132:135], v[228:231]\n"
    "v_mfma_f32_16x16x32_bf16 v[232:235], v[152:155], v[136:139], v[232:235]\n"
    "v_pk_maximum3_f16 v12, v33, v33, s50 op_sel_hi:[1,1,0] neg_lo:[0,1,0] neg_hi:[0,1,0]\n"
    "v_mfma_f32_16x16x32_bf16 v[232:235], v[184:187], v[168:171], v[232:235]\n"
    "v_pk_mad_u16 v12, v12, 8, s51 op_sel_hi:[1,0,0]\n"
    "v_mfma_f32_16x16x32_bf16 v[236:239], v[184:187], v[172:175], v[236:239]\n"
    "v_bfi_b32 v33, s57, v12, v33\n"
    "v_mfma_f32_16x16x32_bf16 v[236:239], v[152:155], v[140:143], v[236:239]\n"
    "v_mfma_f32_16x16x32_bf16 v[252:255], v[156:159], v[140:143], v[252:255]\n"
    "v_pk_maximum3_f16 v21, v34, v34, s50 op_sel_hi:[1,1,0] neg_lo:[0,1,0] neg_hi:[0,1,0]\n"
    "v_mfma_f32_16x16x32_bf16 v[252:255], v[188:191], v[172:175], v[252:255]\n"
    "v_pk_mad_u16 v21, v21, 8, s51 op_sel_hi:[1,0,0]\n"
    "v_mfma_f32_16x16x32_bf16 v[248:251], v[188:191], v[168:171], v[248:251]\n"
    "v_bfi_b32 v34, s57, v21, v34\n"
    "v_mfma_f32_16x16x32_bf16 v[248:251], v[156:159], v[136:139], v[248:251]\n"
    "v_mfma_f32_16x16x32_bf16 v[244:247], v[156:159], v[132:135], v[244:247]\n"
    "v_lshlrev_b32 v52, 4, v1\n"
    "v_mfma_f32_16x16x32_bf16 v[244:247], v[188:191], v[164:167], v[244:247]\n"
    "v_add_u32 v53, 4096, v52\n"
    "v_mfma_f32_16x16x32_bf16 v[240:243], v[188:191], v[160:163], v[240:243]\n"
    "v_mfma_f32_16x16x32_bf16 v[240:243], v[156:159], v[128:131], v[240:243]\n"
    "s_waitcnt vmcnt(6)\n"
    "s_barrier\n"
    "s_mul_i32 s42, s20, 5376\n"
    "s_add_u32 s40, s42, 1048576\n"
    "s_add_u32 s38, s16, s40\n"
    "s_addc_u32 s39, s17, 0\n"
    "s_add_u32 m0, s42, 98304\n"
    "s_nop 0\n"
    "global_load_lds_dwordx4 v52, s[38:39] offset:0 sc1\n"
    "ds_read_b128 v[128:131], v56 offset:0\n"
    "ds_read_b128 v[132:135], v56 offset:2048\n"
    "ds_read_b128 v[136:139], v56 offset:4096\n"
    "ds_read_b128 v[140:143], v56 offset:6144\n"
    "global_load_lds_dwordx4 v52, s[38:39] offset:1024 sc1\n"
    "ds_read_b64_tr_b16 v[144:145], v76 offset:0\n"
    "ds_read_b64_tr_b16 v[146:147], v76 offset:1024\n"
    "ds_read_b64_tr_b16 v[148:149], v77 offset:0\n"
    "ds_read_b64_tr_b16 v[150:151], v77 offset:1024\n"
    "global_load_lds_dwordx4 v52, s[38:39] offset:2048 sc1\n"
    "ds_read_b64_tr_b16 v[152:153], v78 offset:0\n"
    "ds_read_b64_tr_b16 v[154:155], v78 offset:1024\n"
    "ds_read_b64_tr_b16 v[156:157], v79 offset:0\n"
    "ds_read_b64_tr_b16 v[158:159], v79 offset:1024\n"
    "global_load_lds_dwordx4 v52, s[38:39] offset:3072 sc1\n"
    "ds_read_b128 v[160:163], v57 offset:0\n"
    "ds_read_b128 v[164:167], v57 offset:2048\n"
    "ds_read_b128 v[168:171], v57 offset:4096\n"
    "ds_read_b128 v[172:175], v57 offset:6144\n"
    "s_add_u32 m0, s42, 102400\n"
    "s_nop 0\n"
    "global_load_lds_dwordx4 v53, s[38:39] offset:0 sc1\n"
    "ds_read_b64_tr_b16 v[176:177], v76 offset:8192\n"
    "ds_read_b64_tr_b16 v[178:179], v76 offset:9216\n"
    "ds_read_b64_tr_b16 v[180:181], v77 offset:8192\n"
    "ds_read_b64_tr_b16 v[182:183], v77 offset:9216\n"
    "global_load_lds_dwordx4 v53, s[38:39] offset:256 sc1\n"
    "ds_read_b64_tr_b16 v[184:185], v78 offset:8192\n"
    "ds_read_b64_tr_b16 v[186:187], v78 offset:9216\n"
    "ds_read_b64_tr_b16 v[188:189], v79 offset:8192\n"
    "ds_read_b64_tr_b16 v[190:191], v79 offset:9216\n"
    "s_waitcnt vmcnt(10) lgkmcnt(0)\n"
    "s_barrier\n"
    "v_mfma_f32_16x16x32_bf16 v[192:195], v[144:147], v[128:131], v[192:195]\n"
    "v_pk_maximum3_f16 v12, v35, v35, s50 op_sel_hi:[1,1,0] neg_lo:[0,1,0] neg_hi:[0,1,0]\n"
    "v_mfma_f32_16x16x32_bf16 v[192:195], v[176:179], v[160:163], v[192:195]\n"
    "v_pk_mad_u16 v12, v12, 8, s51 op_sel_hi:[1,0,0]\n"
    "v_mfma_f32_16x16x32_bf16 v[196:199], v[176:179], v[164:167], v[196:199]\n"
    "v_mfma_f32_16x16x32_bf16 v[196:199], v[144:147], v[132:135], v[196:199]\n"
    "v_bfi_b32 v35, s57, v12, v35\n"
    "v_mfma_f32_16x16x32_bf16 v[200:203], v[144:147], v[136:139], v[200:203]\n"
    "v_pk_maximum3_f16 v21, v36, v36, s50 op_sel_hi:[1,1,0] neg_lo:[0,1,0] neg_hi:[0,1,0]\n"
    "v_mfma_f32_16x16x32_bf16 v[200:203], v[176:179], v[168:171], v[200:203]\n"
    "v_mfma_f32_16x16x32_bf16 v[204:207], v[176:179], v[172:175], v[204:207]\n"
    "v_pk_mad_u16 v21, v21, 8, s51 op_sel_hi:[1,0,0]\n"
    "v_mfma_f32_16x16x32_bf16 v[204:207], v[144:147], v[140:143], v[204:207]\n"
    "v_bfi_b32 v36, s57, v21, v36\n"
    "v_mfma_f32_16x16x32_bf16 v[220:223], v[148:151], v[140:143], v[220:223]\n"
    "v_mfma_f32_16x16x32_bf16 v[220:223], v[180:183], v[172:175], v[220:223]\n"
    "v_pk_maximum3_f16 v12, v37, v37, s50 op_sel_hi:[1,1,0] neg_lo:[0,1,0] neg_hi:[0,1,0]\n"
    "v_mfma_f32_16x16x32_bf16 v[216:219], v[180:183], v[168:171], v[216:219]\n"
    "v_pk_mad_u16 v12, v12, 8, s51 op_sel_hi:[1,0,0]\n"
    "v_mfma_f32_16x16x32_bf16 v[216:219], v[148:151], v[136:139], v[216:219]\n"
    "v_mfma_f32_16x16x32_bf16 v[212:215], v[148:151], v[132:135], v[212:215]\n"
    "v_bfi_b32 v37, s57, v12, v37\n"
    "v_mfma_f32_16x16x32_bf16 v[212:215], v[180:183], v[164:167], v[212:215]\n"
    "v_pk_maximum3_f16 v21, v38, v38, s50 op_sel_hi:[1,1,0] neg_lo:[0,1,0] neg_hi:[0,1,0]\n"
    "v_mfma_f32_16x16x32_bf16 v[208:211], v[180:183], v[160:163], v[208:211]\n"
    "v_mfma_f32_16x16x32_bf16 v[208:211], v[148:151], v[128:131], v[208:211]\n"
    "v_pk_mad_u16 v21, v21, 8, s51 op_sel_hi:[1,0,0]\n"
    "v_mfma_f32_16x16x32_bf16 v[224:227], v[152:155], v[128:131], v[224:227]\n"
    "v_mfma_f32_16x16x32_bf16 v[224:227], v[184:187], v[160:163], v[224:227]\n"
    "v_bfi_b32 v38, s57, v21, v38\n"
    "v_mfma_f32_16x16x32_bf16 v[228:231], v[184:187], v[164:167], v[228:231]\n"
    "v_pk_maximum3_f16 v12, v39, v39, s50 op_sel_hi:[1,1,0] neg_lo:[0,1,0] neg_hi:[0,1,0]\n"
    "v_mfma_f32_16x16x32_bf16 v[228:231], v[152:155], v[132:135], v[228:231]\n"
    "v_mfma_f32_16x16x32_bf16 v[232:235], v[152:155], v[136:139], v[232:235]\n"
    "v_pk_mad_u16 v12, v12, 8, s51 op_sel_hi:[1,0,0]\n"
    "v_mfma_f32_16x16x32_bf16 v[232:235], v[184:187], v[168:171], v[232:235]\n"
    "v_bfi_b32 v39, s57, v12, v39\n"
    "v_mfma_f32_16x16x32_bf16 v[236:239], v[184:187], v[172:175], v[236:239]\n"
    "v_mfma_f32_16x16x32_bf16 v[236:239], v[152:155], v[140:143], v[236:239]\n"
    "v_pk_maximum3_f16 v21, v40, v40, s50 op_sel_hi:[1,1,0] neg_lo:[0,1,0] neg_hi:[0,1,0]\n"
    "v_mfma_f32_16x16x32_bf16 v[252:255], v[156:159], v[140:143], v[252:255]\n"
    "v_pk_mad_u16 v21, v21, 8, s51 op_sel_hi:[1,0,0]\n"
    "v_mfma_f32_16x16x32_bf16 v[252:255], v[188:191], v[172:175], v[252:255]\n"
    "v_mfma_f32_16x16x32_bf16 v[248:251], v[188:191], v[168:171], v[248:251]\n"
    "v_bfi_b32 v40, s57, v21, v40\n"
    "v_mfma_f32_16x16x32_bf16 v[248:251], v[156:159], v[136:139], v[248:251]\n"
    "s_sub_u32 s43, 4112, s47\n"
    "s_lshl_b32 s43, s43, 19\n"
    "v_cvt_pkrtz_f16_f32 v12, s43, s43\n"
    "v_mfma_f32_16x16x32_bf16 v[244:247], v[156:159], v[132:135], v[244:247]\n"
    "v_mfma_f32_16x16x32_bf16 v[244:247], v[188:191], v[164:167], v[244:247]\n"
    "v_lshl_or_b32 v21, v12, 16, s47\n"
    "v_mfma_f32_16x16x32_bf16 v[240:243], v[188:191], v[160:163], v[240:243]\n"
    "v_mfma_f32_16x16x32_bf16 v[240:243], v[156:159], v[128:131], v[240:243]\n"
    "s_waitcnt vmcnt(6)\n"
    "s_barrier\n"
    "ds_read_b128 v[128:131], v56 offset:49152\n"
    "ds_read_b128 v[132:135], v56 offset:51200\n"
    "ds_read_b128 v[136:139], v56 offset:53248\n"
    "ds_read_b128 v[140:143], v56 offset:55296\n"
    "ds_read_b64_tr_b16 v[144:145], v76 offset:49152\n"
    "ds_read_b64_tr_b16 v[146:147], v76 offset:50176\n"
    "ds_read_b64_tr_b16 v[148:149], v77 offset:49152\n"
    "ds_read_b64_tr_b16 v[150:151], v77 offset:50176\n"
    "ds_read_b64_tr_b16 v[152:153], v78 offset:49152\n"
    "ds_read_b64_tr_b16 v[154:155], v78 offset:50176\n"
    "ds_read_b64_tr_b16 v[156:157], v79 offset:49152\n"
    "ds_read_b64_tr_b16 v[158:159], v79 offset:50176\n"
    "ds_read_b128 v[160:163], v57 offset:49152\n"
    "ds_read_b128 v[164:167], v57 offset:51200\n"
    "ds_read_b128 v[168:171], v57 offset:53248\n"
    "ds_read_b128 v[172:175], v57 offset:55296\n"
    "ds_read_b64_tr_b16 v[176:177], v76 offset:57344\n"
    "ds_read_b64_tr_b16 v[178:179], v76 offset:58368\n"
    "ds_read_b64_tr_b16 v[180:181], v77 offset:57344\n"
    "ds_read_b64_tr_b16 v[182:183], v77 offset:58368\n"
    "ds_read_b64_tr_b16 v[184:185], v78 offset:57344\n"
    "ds_read_b64_tr_b16 v[186:187], v78 offset:58368\n"
    "ds_read_b64_tr_b16 v[188:189], v79 offset:57344\n"
    "ds_read_b64_tr_b16 v[190:191], v79 offset:58368\n"
    "s_cmp_lg_u32 s20, 3\n"
    "s_cbranch_scc1 .Lxd_on4\n"
    "s_mov_b64 exec, 1\n"
    "ds_write_b64 v8, v[26:27]\n"
    "s_mov_b64 exec, 15\n"
    "ds_write2_b32 v8, v22, v13 offset0:4 offset1:8\n"
    "s_mov_b64 exec, -1\n"
    ".Lxd_on4:\n"
    "s_waitcnt vmcnt(0) lgkmcnt(0)\n"
    "s_cmp_lg_u32 s20, 3\n"
    "s_cbranch_scc1 .Lxd_on5\n"
    "s_mov_b64 exec, 0x200\n"
    "global_store_dword v11, v43, s[16:17] sc1\n"
    "s_mov_b64 exec, -1\n"
    ".Lxd_on5:\n"
    "s_barrier\n"
    "s_setprio 3\n"
    "v_mfma_f32_16x16x32_bf16 v[192:195], v[144:147], v[128:131], v[192:195]\n"
    "v_add_u32 v42, 98304, v52\n"
    "v_mfma_f32_16x16x32_bf16 v[192:195], v[176:179], v[160:163], v[192:195]\n"
    "v_lshl_add_u32 v11, v1, 2, s3\n"
    "v_mfma_f32_16x16x32_bf16 v[196:199], v[176:179], v[164:167], v[196:199]\n"
    "v_mov_b32 v13, s58\n"
    "v_mfma_f32_16x16x32_bf16 v[196:199], v[144:147], v[132:135], v[196:199]\n"
    "s_mov_b32 s36, 0\n"
    "v_mfma_f32_16x16x32_bf16 v[200:203], v[144:147], v[136:139], v[200:203]\n"
    "v_mov_b32 v84, s65\n"
    "v_mfma_f32_16x16x32_bf16 v[200:203], v[176:179], v[168:171], v[200:203]\n"
    "v_lshl_add_u32 v7, s23, 12, v3\n"
    "v_mfma_f32_16x16x32_bf16 v[204:207], v[176:179], v[172:175], v[204:207]\n"
    "v_lshl_add_u32 v8, s23, 12, v4\n"
    "v_mfma_f32_16x16x32_bf16 v[204:207], v[144:147], v[140:143], v[204:207]\n"
    "v_lshl_add_u32 v9, s23, 12, v5\n"
    "v_mfma_f32_16x16x32_bf16 v[220:223], v[148:151], v[140:143], v[220:223]\n"
    "v_lshl_add_u32 v10, s23, 12, v6\n"
    "v_mfma_f32_16x16x32_bf16 v[220:223], v[180:183], v[172:175], v[220:223]\n"
    "v_mfma_f32_16x16x32_bf16 v[216:219], v[180:183], v[168:171], v[216:219]\n"
    "v_mul_f32 v76, v192, v192\n"
    "v_mul_f32 v77, v193, v193\n"
    "v_mfma_f32_16x16x32_bf16 v[216:219], v[148:151], v[136:139], v[216:219]\n"
    "v_mul_f32 v78, v196, v196\n"
    "v_mul_f32 v79, v197, v197\n"
    "v_mfma_f32_16x16x32_bf16 v[212:215], v[148:151], v[132:135], v[212:215]\n"
    "v_mul_f32 v80, v200, v200\n"
    "v_mul_f32 v81, v201, v201\n"
    "v_mfma_f32_16x16x32_bf16 v[212:215], v[180:183], v[164:167], v[212:215]\n"
    "v_mul_f32 v82, v204, v204\n"
    "v_mul_f32 v83, v205, v205\n"
    "v_mfma_f32_16x16x32_bf16 v[208:211], v[180:183], v[160:163], v[208:211]\n"
    "v_fma_f32 v76, v76, s64, v84\n"
    "v_fma_f32 v77, v77, s64, v84\n"
    "v_mfma_f32_16x16x32_bf16 v[208:211], v[148:151], v[128:131], v[208:211]\n"
    "v_fma_f32 v78, v78, s64, v84\n"
    "v_fma_f32 v79, v79, s64, v84\n"
    "v_mfma_f32_16x16x32_bf16 v[224:227], v[152:155], v[128:131], v[224:227]\n"
    "v_fma_f32 v80, v80, s64, v84\n"
    "v_fma_f32 v81, v81, s64, v84\n"
    "v_mfma_f32_16x16x32_bf16 v[224:227], v[184:187], v[160:163], v[224:227]\n"
    "v_fma_f32 v82, v82, s64, v84\n"
    "v_fma_f32 v83, v83, s64, v84\n"
    "v_mfma_f32_16x16x32_bf16 v[228:231], v[184:187], v[164:167], v[228:231]\n"
    "v_mul_f32 v76, v76, v192\n"
    "v_mul_f32 v77, v77, v193\n"
    "v_mfma_f32_16x16x32_bf16 v[228:231], v[152:155], v[132:135], v[228:231]\n"
    "v_mul_f32 v78, v78, v196\n"
    "v_mul_f32 v79, v79, v197\n"
    "v_mfma_f32_16x16x32_bf16 v[232:235], v[152:155], v[136:139], v[232:235]\n"
    "v_mul_f32 v80, v80, v200\n"
    "v_mul_f32 v81, v81, v201\n"
    "v_mfma_f32_16x16x32_bf16 v[232:235], v[184:187], v[168:171], v[232:235]\n"
    "v_mul_f32 v82, v82, v204\n"
    "v_mul_f32 v83, v83, v205\n"
    "v_mfma_f32_16x16x32_bf16 v[236:239], v[184:187], v[172:175], v[236:239]\n"
    "v_exp_f32 v76, v76\n"
    "v_mfma_f32_16x16x32_bf16 v[236:239], v[152:155], v[140:143], v[236:239]\n"
    "v_exp_f32 v77, v77\n"
    "v_mfma_f32_16x16x32_bf16 v[252:255], v[156:159], v[140:143], v[252:255]\n"
    "v_exp_f32 v78, v78\n"
    "v_mfma_f32_16x16x32_bf16 v[252:255], v[188:191], v[172:175], v[252:255]\n"
    "v_exp_f32 v79, v79\n"
    "v_mfma_f32_16x16x32_bf16 v[248:251], v[188:191], v[168:171], v[248:251]\n"
    "v_exp_f32 v80, v80\n"
    "v_mfma_f32_16x16x32_bf16 v[248:251], v[156:159], v[136:139], v[248:251]\n"
    "v_exp_f32 v81, v81\n"
    "v_mfma_f32_16x16x32_bf16 v[244:247], v[156:159], v[132:135], v[244:247]\n"
    "v_exp_f32 v82, v82\n"
    "v_mfma_f32_16x16x32_bf16 v[244:247], v[188:191], v[164:167], v[244:247]\n"
    "v_exp_f32 v83, v83\n"
    "v_mfma_f32_16x16x32_bf16 v[240:243], v[188:191], v[160:163], v[240:243]\n"
    "v_mfma_f32_16x16x32_bf16 v[240:243], v[156:159], v[128:131], v[240:243]\n"
    "s_cmp_lg_u32 s21, 0\n"
    "s_cbranch_scc1 .Lxd_g1\n"
    "s_barrier\n"
    ".Lxd_g1:\n"
    "s_setprio 2\n"
    "s_cmp_eq_u32 s22, 3\n"
    "s_cbranch_scc1 .Lxd_np\n"
    "ds_write_b32 v25, v24 offset:20\n"
    "ds_write_b128 v15, v[28:31] offset:0\n"
    "ds_write_b128 v15, v[32:35] offset:144\n"
    "ds_write_b128 v15, v[36:39] offset:288\n"
    "ds_write2_b32 v16, v40, v12 offset1:72\n"
    "s_mov_b64 exec, 1\n"
    "ds_or_b32 v25, v21 offset:532\n"
    "s_mov_b64 exec, -1\n"
    ".Lxd_np:\n"
    "ds_read2_b32 v[26:27], v41 offset1:1\n"
    "ds_read_b32 v0, v41 offset:16\n"
    "ds_read_b32 v23, v41 offset:24\n"
    "s_load_dword s26, s[68:69], 0\n"
    "v_add_u32 v25, s24, v25\n"
    "ds_read_b128 v[92:95], v42 offset:0\n"
    "ds_read_b128 v[96:99], v42 offset:1024\n"
    "ds_read_b128 v[100:103], v42 offset:2048\n"
    "ds_read_b128 v[104:107], v42 offset:3072\n"
    "ds_read_b128 v[108:111], v42 offset:4096\n"
    "ds_read_b128 v[112:115], v42 offset:5120\n"
    "ds_read_b128 v[116:119], v42 offset:6144\n"
    "ds_read_b128 v[120:123], v42 offset:7168\n"
    "ds_read_b128 v[124:127], v42 offset:8192\n"
    "s_cmp_eq_u32 s22, 3\n"
    "s_cbranch_scc1 .Lxd_on6\n"
    ".Lxd_on6r:\n"
    "v_pk_fma_f32 v[76:77], v[76:77], v[60:61], v[60:61]\n"
    "v_pk_fma_f32 v[78:79], v[78:79], v[60:61], v[60:61]\n"
    "v_pk_fma_f32 v[80:81], v[80:81], v[60:61], v[60:61]\n"
    "v_pk_fma_f32 v[82:83], v[82:83], v[60:61], v[60:61]\n"
    "v_rcp_f32 v76, v76\n"
    "v_rcp_f32 v77, v77\n"
    "v_rcp_f32 v78, v78\n"
    "v_rcp_f32 v79, v79\n"
    "v_rcp_f32 v80, v80\n"
    "v_rcp_f32 v81, v81\n"
    "v_rcp_f32 v82, v82\n"
    "v_rcp_f32 v83, v83\n"
    "v_pk_mul_f32 v[84:85], v[192:193], v[76:77]\n"
    "v_pk_mul_f32 v[86:87], v[196:197], v[78:79]\n"
    "v_pk_mul_f32 v[88:89], v[200:201], v[80:81]\n"
    "v_pk_mul_f32 v[90:91], v[204:205], v[82:83]\n"
    "s_setprio 3\n"
    "v_pk_mul_f32 v[76:77], v[194:195], v[194:195]\n"
    "v_pk_mul_f32 v[78:79], v[198:199], v[198:199]\n"
    "v_pk_mul_f32 v[80:81], v[202:203], v[202:203]\n"
    "v_pk_mul_f32 v[82:83], v[206:207], v[206:207]\n"
    "v_pk_fma_f32 v[76:77], v[76:77], s[64:65], s[64:65] op_sel:[0,0,1] op_sel_hi:[1,0,1]\n"
    "v_pk_fma_f32 v[78:79], v[78:79], s[64:65], s[64:65] op_sel:[0,0,1] op_sel_hi:[1,0,1]\n"
    "v_pk_fma_f32 v[80:81], v[80:81], s[64:65], s[64:65] op_sel:[0,0,1] op_sel_hi:[1,0,1]\n"
    "v_pk_fma_f32 v[82:83], v[82:83], s[64:65], s[64:65] op_sel:[0,0,1] op_sel_hi:[1,0,1]\n"
    "v_pk_mul_f32 v[76:77], v[76:77], v[194:195]\n"
    "v_pk_mul_f32 v[78:79], v[78:79], v[198:199]\n"
    "v_pk_mul_f32 v[80:81], v[80:81], v[202:203]\n"
    "v_pk_mul_f32 v[82:83], v[82:83], v[206:207]\n"
    "v_exp_f32 v76, v76\n"
    "v_exp_f32 v77, v77\n"
    "v_exp_f32 v78, v78\n"
    "v_exp_f32 v79, v79\n"
    "v_exp_f32 v80, v80\n"
    "v_exp_f32 v81, v81\n"
    "v_exp_f32 v82, v82\n"
    "v_exp_f32 v83, v83\n"
    "v_pk_fma_f32 v[76:77], v[76:77], v[62:63], v[62:63]\n"
    "v_pk_fma_f32 v[78:79], v[78:79], v[62:63], v[62:63]\n"
    "v_pk_fma_f32 v[80:81], v[80:81], v[62:63], v[62:63]\n"
    "v_pk_fma_f32 v[82:83], v[82:83], v[62:63], v[62:63]\n"
    "v_rcp_f32 v76, v76\n"
    "v_rcp_f32 v77, v77\n"
    "v_rcp_f32 v78, v78\n"
    "v_rcp_f32 v79, v79\n"
    "v_rcp_f32 v80, v80\n"
    "v_rcp_f32 v81, v81\n"
    "v_rcp_f32 v82, v82\n"
    "v_rcp_f32 v83, v83\n"
    "v_pk_fma_f32 v[84:85], v[194:195], v[76:77], v[84:85]\n"
    "v_pk_fma_f32 v[86:87], v[198:199], v[78:79], v[86:87]\n"
    "v_pk_fma_f32 v[88:89], v[202:203], v[80:81], v[88:89]\n"
    "v_pk_fma_f32 v[90:91], v[206:207], v[82:83], v[90:91]\n"
    "ds_read_b32 v22, v13 offset:512\n"
    "v_pk_mul_f32 v[76:77], v[208:209], v[208:209]\n"
    "v_pk_mul_f32 v[78:79], v[212:213], v[212:213]\n"
    "v_pk_mul_f32 v[80:81], v[216:217], v[216:217]\n"
    "v_pk_mul_f32 v[82:83], v[220:221], v[220:221]\n"
    "v_pk_fma_f32 v[76:77], v[76:77], s[64:65], s[64:65] op_sel:[0,0,1] op_sel_hi:[1,0,1]\n"
    "v_pk_fma_f32 v[78:79], v[78:79], s[64:65], s[64:65] op_sel:[0,0,1] op_sel_hi:[1,0,1]\n"
    "v_pk_fma_f32 v[80:81], v[80:81], s[64:65], s[64:65] op_sel:[0,0,1] op_sel_hi:[1,0,1]\n"
    "v_pk_fma_f32 v[82:83], v[82:83], s[64:65], s[64:65] op_sel:[0,0,1] op_sel_hi:[1,0,1]\n"
    "v_pk_mul_f32 v[76:77], v[76:77], v[208:209]\n"
    "v_pk_mul_f32 v[78:79], v[78:79], v[212:213]\n"
    "v_pk_mul_f32 v[80:81], v[80:81], v[216:217]\n"
    "v_pk_mul_f32 v[82:83], v[82:83], v[220:221]\n"
    "v_exp_f32 v76, v76\n"
    "v_exp_f32 v77, v77\n"
    "ds_read_b128 v[128:131], v42 offset:9216\n"
    "v_exp_f32 v78, v78\n"
    "v_exp_f32 v79, v79\n"
    "v_exp_f32 v80, v80\n"
    "ds_read_b128 v[132:135], v42 offset:10240\n"
    "v_exp_f32 v81, v81\n"
    "v_exp_f32 v82, v82\n"
    "v_exp_f32 v83, v83\n"
    "ds_read_b128 v[136:139], v42 offset:11264\n"
    "v_pk_fma_f32 v[76:77], v[76:77], v[64:65], v[64:65]\n"
    "v_pk_fma_f32 v[78:79], v[78:79], v[64:65], v[64:65]\n"
    "v_pk_fma_f32 v[80:81], v[80:81], v[64:65], v[64:65]\n"
    "v_pk_fma_f32 v[82:83], v[82:83], v[64:65], v[64:65]\n"
    "v_rcp_f32 v76, v76\n"
    "v_rcp_f32 v77, v77\n"
    "ds_read_b128 v[140:143], v42 offset:12288\n"
    "v_rcp_f32 v78, v78\n"
    "v_rcp_f32 v79, v79\n"
    "v_rcp_f32 v80, v80\n"
    "ds_read_b128 v[144:147], v42 offset:13312\n"
    "v_rcp_f32 v81, v81\n"
    "v_rcp_f32 v82, v82\n"
    "v_rcp_f32 v83, v83\n"
    "ds_read_b128 v[148:151], v42 offset:14336\n"
    "v_pk_fma_f32 v[84:85], v[208:209], v[76:77], v[84:85]\n"
    "v_pk_fma_f32 v[86:87], v[212:213], v[78:79], v[86:87]\n"
    "v_pk_fma_f32 v[88:89], v[216:217], v[80:81], v[88:89]\n"
    "v_pk_fma_f32 v[90:91], v[220:221], v[82:83], v[90:91]\n"
    "s_waitcnt lgkmcnt(6)\n"
    "v_readfirstlane_b32 s45, v22\n"
    "s_cmp_lt_i32 s45, 16\n"
    "s_cbranch_scc1 .Lxd_ck7\n"
    ".Lxd_ck7r:\n"
    "ds_read_b64_tr_b16 v[44:45], v9 offset:0\n"
    "ds_read_b64_tr_b16 v[46:47], v10 offset:0\n"
    "ds_read_b64_tr_b16 v[48:49], v7 offset:0\n"
    "ds_read_b64_tr_b16 v[50:51], v8 offset:576\n"
    "ds_read_b64_tr_b16 v[52:53], v8 offset:1152\n"
    "ds_read_b64_tr_b16 v[54:55], v8 offset:1728\n"
    "ds_read_b64_tr_b16 v[56:57], v8 offset:2304\n"
    "ds_read_b64_tr_b16 v[58:59], v8 offset:2880\n"
    "v_readfirstlane_b32 s28, v26\n"
    "v_readfirstlane_b32 s29, v27\n"
    "s_cmp_eq_u32 s22, 3\n"
    "s_cbranch_scc1 .Lxd_on9\n"
    ".Lxd_on9r:\n"
    "s_cmp_eq_u32 s22, 0\n"
    "s_cbranch_scc1 .Lxd_on10\n"
    ".Lxd_on10r:\n"
    "s_setprio 2\n"
    "v_pk_mul_f32 v[76:77], v[210:211], v[210:211]\n"
    "v_pk_mul_f32 v[78:79], v[214:215], v[214:215]\n"
    "v_pk_mul_f32 v[80:81], v[218:219], v[218:219]\n"
    "v_pk_mul_f32 v[82:83], v[222:223], v[222:223]\n"
    "v_pk_fma_f32 v[76:77], v[76:77], s[64:65], s[64:65] op_sel:[0,0,1] op_sel_hi:[1,0,1]\n"
    "v_pk_fma_f32 v[78:79], v[78:79], s[64:65], s[64:65] op_sel:[0,0,1] op_sel_hi:[1,0,1]\n"
    "v_pk_fma_f32 v[80:81], v[80:81], s[64:65], s[64:65] op_sel:[0,0,1] op_sel_hi:[1,0,1]\n"
    "v_pk_fma_f32 v[82:83], v[82:83], s[64:65], s[64:65] op_sel:[0,0,1] op_sel_hi:[1,0,1]\n"
    "v_pk_mul_f32 v[76:77], v[76:77], v[210:211]\n"
    "v_pk_mul_f32 v[78:79], v[78:79], v[214:215]\n"
    "v_pk_mul_f32 v[80:81], v[80:81], v[218:219]\n"
    "v_pk_mul_f32 v[82:83], v[82:83], v[222:223]\n"
    "v_exp_f32 v76, v76\n"
    "v_exp_f32 v77, v77\n"
    "ds_read_b128 v[152:155], v42 offset:15360\n"
    "v_exp_f32 v78, v78\n"
    "v_exp_f32 v79, v79\n"
    "v_exp_f32 v80, v80\n"
    "ds_read_b128 v[156:159], v42 offset:16384\n"
    "v_exp_f32 v81, v81\n"
    "v_exp_f32 v82, v82\n"
    "v_exp_f32 v83, v83\n"
    "ds_read_b128 v[160:163], v42 offset:17408\n"
    "v_pk_fma_f32 v[76:77], v[76:77], v[66:67], v[66:67]\n"
    "v_pk_fma_f32 v[78:79], v[78:79], v[66:67], v[66:67]\n"
    "v_pk_fma_f32 v[80:81], v[80:81], v[66:67], v[66:67]\n"
    "v_pk_fma_f32 v[82:83], v[82:83], v[66:67], v[66:67]\n"
    "v_rcp_f32 v76, v76\n"
    "v_rcp_f32 v77, v77\n"
    "ds_read_b128 v[164:167], v42 offset:18432\n"
    "v_rcp_f32 v78, v78\n"
    "v_rcp_f32 v79, v79\n"
    "v_rcp_f32 v80, v80\n"
    "ds_read_b128 v[168:171], v42 offset:19456\n"
    "v_rcp_f32 v81, v81\n"
    "v_rcp_f32 v82, v82\n"
    "v_rcp_f32 v83, v83\n"
    "ds_read_b128 v[172:175], v42 offset:20480\n"
    "v_pk_fma_f32 v[84:85], v[210:211], v[76:77], v[84:85]\n"
    "v_pk_fma_f32 v[86:87], v[214:215], v[78:79], v[86:87]\n"
    "v_pk_fma_f32 v[88:89], v[218:219], v[80:81], v[88:89]\n"
    "v_pk_fma_f32 v[90:91], v[222:223], v[82:83], v[90:91]\n"
    "v_pk_mul_f32 v[76:77], v[224:225], v[224:225]\n"
    "v_pk_mul_f32 v[78:79], v[228:229], v[228:229]\n"
    "v_pk_mul_f32 v[80:81], v[232:233], v[232:233]\n"
    "v_pk_mul_f32 v[82:83], v[236:237], v[236:237]\n"
    "v_pk_fma_f32 v[76:77], v[76:77], s[64:65], s[64:65] op_sel:[0,0,1] op_sel_hi:[1,0,1]\n"
    "v_pk_fma_f32 v[78:79], v[78:79], s[64:65], s[64:65] op_sel:[0,0,1] op_sel_hi:[1,0,1]\n"
    "v_pk_fma_f32 v[80:81], v[80:81], s[64:65], s[64:65] op_sel:[0,0,1] op_sel_hi:[1,0,1]\n"
    "v_pk_fma_f32 v[82:83], v[82:83], s[64:65], s[64:65] op_sel:[0,0,1] op_sel_hi:[1,0,1]\n"
    "v_pk_mul_f32 v[76:77], v[76:77], v[224:225]\n"
    "v_pk_mul_f32 v[78:79], v[78:79], v[228:229]\n"
    "v_pk_mul_f32 v[80:81], v[80:81], v[232:233]\n"
    "v_pk_mul_f32 v[82:83], v[82:83], v[236:237]\n"
    "v_exp_f32 v76, v76\n"
    "v_exp_f32 v77, v77\n"
    "ds_read_b128 v[176:179], v42 offset:21504\n"
    "v_exp_f32 v78, v78\n"
    "v_exp_f32 v79, v79\n"
    "v_exp_f32 v80, v80\n"
    "ds_read_b128 v[180:183], v42 offset:22528\n"
    "v_exp_f32 v81, v81\n"
    "v_exp_f32 v82, v82\n"
    "v_exp_f32 v83, v83\n"
    "ds_read_b128 v[184:187], v42 offset:23552\n"
    "v_pk_fma_f32 v[76:77], v[76:77], v[68:69], v[68:69]\n"
    "v_pk_fma_f32 v[78:79], v[78:79], v[68:69], v[68:69]\n"
    "v_pk_fma_f32 v[80:81], v[80:81], v[68:69], v[68:69]\n"
    "v_pk_fma_f32 v[82:83], v[82:83], v[68:69], v[68:69]\n"
    "v_rcp_f32 v76, v76\n"
    "v_rcp_f32 v77, v77\n"
    "ds_read_b128 v[188:191], v42 offset:24576\n"
    "v_rcp_f32 v78, v78\n"
    "v_rcp_f32 v79, v79\n"
    "v_rcp_f32 v80, v80\n"
    "ds_read_b128 v[192:195], v42 offset:25600\n"
    "v_rcp_f32 v81, v81\n"
    "v_rcp_f32 v82, v82\n"
    "v_rcp_f32 v83, v83\n"
    "ds_read_b128 v[196:199], v42 offset:26624\n"
    "v_pk_fma_f32 v[84:85], v[224:225], v[76:77], v[84:85]\n"
    "v_pk_fma_f32 v[86:87], v[228:229], v[78:79], v[86:87]\n"
    "v_pk_fma_f32 v[88:89], v[232:233], v[80:81], v[88:89]\n"
    "v_pk_fma_f32 v[90:91], v[236:237], v[82:83], v[90:91]\n"
    "s_setprio 1\n"
    "v_pk_mul_f32 v[76:77], v[226:227], v[226:227]\n"
    "v_pk_mul_f32 v[78:79], v[230:231], v[230:231]\n"
    "v_pk_mul_f32 v[80:81], v[234:235], v[234:235]\n"
    "v_pk_mul_f32 v[82:83], v[238:239], v[238:239]\n"
    "v_pk_fma_f32 v[76:77], v[76:77], s[64:65], s[64:65] op_sel:[0,0,1] op_sel_hi:[1,0,1]\n"
    "v_pk_fma_f32 v[78:79], v[78:79], s[64:65], s[64:65] op_sel:[0,0,1] op_sel_hi:[1,0,1]\n"
    "v_pk_fma_f32 v[80:81], v[80:81], s[64:65], s[64:65] op_sel:[0,0,1] op_sel_hi:[1,0,1]\n"
    "v_pk_fma_f32 v[82:83], v[82:83], s[64:65], s[64:65] op_sel:[0,0,1] op_sel_hi:[1,0,1]\n"
    "v_pk_mul_f32 v[76:77], v[76:77], v[226:227]\n"
    "v_pk_mul_f32 v[78:79], v[78:79], v[230:231]\n"
    "v_pk_mul_f32 v[80:81], v[80:81], v[234:235]\n"
    "v_pk_mul_f32 v[82:83], v[82:83], v[238:239]\n"
    "v_exp_f32 v76, v76\n"
    "v_exp_f32 v77, v77\n"
    "ds_read_b128 v[200:203], v42 offset:27648\n"
    "v_exp_f32 v78, v78\n"
    "v_exp_f32 v79, v79\n"
    "v_exp_f32 v80, v80\n"
    "ds_read_b128 v[204:207], v42 offset:28672\n"
    "v_exp_f32 v81, v81\n"
    "v_exp_f32 v82, v82\n"
    "v_exp_f32 v83, v83\n"
    "ds_read_b128 v[208:211], v42 offset:29696\n"
    "v_pk_fma_f32 v[76:77], v[76:77], v[70:71], v[70:71]\n"
    "v_pk_fma_f32 v[78:79], v[78:79], v[70:71], v[70:71]\n"
    "v_pk_fma_f32 v[80:81], v[80:81], v[70:71], v[70:71]\n"
    "v_pk_fma_f32 v[82:83], v[82:83], v[70:71], v[70:71]\n"
    "v_rcp_f32 v76, v76\n"
    "v_rcp_f32 v77, v77\n"
    "ds_read_b128 v[212:215], v42 offset:30720\n"
    "v_rcp_f32 v78, v78\n"
    "v_rcp_f32 v79, v79\n"
    "v_rcp_f32 v80, v80\n"
    "ds_read_b128 v[216:219], v42 offset:31744\n"
    "v_rcp_f32 v81, v81\n"
    "v_rcp_f32 v82, v82\n"
    "v_rcp_f32 v83, v83\n"
    "ds_read_b128 v[220:223], v42 offset:32768\n"
    "v_pk_fma_f32 v[84:85], v[226:227], v[76:77], v[84:85]\n"
    "v_pk_fma_f32 v[86:87], v[230:231], v[78:79], v[86:87]\n"
    "v_pk_fma_f32 v[88:89], v[234:235], v[80:81], v[88:89]\n"
    "v_pk_fma_f32 v[90:91], v[238:239], v[82:83], v[90:91]\n"
    "s_cmp_eq_u32 s22, 3\n"
    "s_cbranch_scc1 .Lxd_on11\n"
    ".Lxd_on11r:\n"
    "s_cmp_eq_u32 s22, 0\n"
    "s_cbranch_scc1 .Lxd_on13\n"
    ".Lxd_on13r:\n"
    "s_cmp_eq_u32 s22, 1\n"
    "s_cbranch_scc1 .Lxd_on14\n"
    ".Lxd_on14r:\n"
    "v_pk_mul_f32 v[76:77], v[240:241], v[240:241]\n"
    "v_pk_mul_f32 v[78:79], v[244:245], v[244:245]\n"
    "v_pk_mul_f32 v[80:81], v[248:249], v[248:249]\n"
    "v_pk_mul_f32 v[82:83], v[252:253], v[252:253]\n"
    "v_pk_fma_f32 v[76:77], v[76:77], s[64:65], s[64:65] op_sel:[0,0,1] op_sel_hi:[1,0,1]\n"
    "v_pk_fma_f32 v[78:79], v[78:79], s[64:65], s[64:65] op_sel:[0,0,1] op_sel_hi:[1,0,1]\n"
    "v_pk_fma_f32 v[80:81], v[80:81], s[64:65], s[64:65] op_sel:[0,0,1] op_sel_hi:[1,0,1]\n"
    "v_pk_fma_f32 v[82:83], v[82:83], s[64:65], s[64:65] op_sel:[0,0,1] op_sel_hi:[1,0,1]\n"
    "v_pk_mul_f32 v[76:77], v[76:77], v[240:241]\n"
    "v_pk_mul_f32 v[78:79], v[78:79], v[244:245]\n"
    "v_pk_mul_f32 v[80:81], v[80:81], v[248:249]\n"
    "v_pk_mul_f32 v[82:83], v[82:83], v[252:253]\n"
    "v_exp_f32 v76, v76\n"
    "v_exp_f32 v77, v77\n"
    "v_exp_f32 v78, v78\n"
    "v_exp_f32 v79, v79\n"
    "v_exp_f32 v80, v80\n"
    "v_exp_f32 v81, v81\n"
    "v_exp_f32 v82, v82\n"
    "v_exp_f32 v83, v83\n"
    "v_pk_fma_f32 v[76:77], v[76:77], v[72:73], v[72:73]\n"
    "v_pk_fma_f32 v[78:79], v[78:79], v[72:73], v[72:73]\n"
    "v_pk_fma_f32 v[80:81], v[80:81], v[72:73], v[72:73]\n"
    "v_pk_fma_f32 v[82:83], v[82:83], v[72:73], v[72:73]\n"
    "v_rcp_f32 v76, v76\n"
    "v_rcp_f32 v77, v77\n"
    "v_rcp_f32 v78, v78\n"
    "v_rcp_f32 v79, v79\n"
    "v_rcp_f32 v80, v80\n"
    "v_rcp_f32 v81, v81\n"
    "v_rcp_f32 v82, v82\n"
    "v_rcp_f32 v83, v83\n"
    "v_pk_fma_f32 v[84:85], v[240:241], v[76:77], v[84:85]\n"
    "v_pk_fma_f32 v[86:87], v[244:245], v[78:79], v[86:87]\n"
    "v_pk_fma_f32 v[88:89], v[248:249], v[80:81], v[88:89]\n"
    "v_pk_fma_f32 v[90:91], v[252:253], v[82:83], v[90:91]\n"
    "s_setprio 0\n"
    "v_pk_mul_f32 v[76:77], v[242:243], v[242:243]\n"
    "v_pk_mul_f32 v[78:79], v[246:247], v[246:247]\n"
    "v_pk_mul_f32 v[80:81], v[250:251], v[250:251]\n"
    "v_pk_mul_f32 v[82:83], v[254:255], v[254:255]\n"
    "v_pk_fma_f32 v[76:77], v[76:77], s[64:65], s[64:65] op_sel:[0,0,1] op_sel_hi:[1,0,1]\n"
    "v_pk_fma_f32 v[78:79], v[78:79], s[64:65], s[64:65] op_sel:[0,0,1] op_sel_hi:[1,0,1]\n"
    "v_pk_fma_f32 v[80:81], v[80:81], s[64:65], s[64:65] op_sel:[0,0,1] op_sel_hi:[1,0,1]\n"
    "v_pk_fma_f32 v[82:83], v[82:83], s[64:65], s[64:65] op_sel:[0,0,1] op_sel_hi:[1,0,1]\n"
    "v_pk_mul_f32 v[76:77], v[76:77], v[242:243]\n"
    "v_pk_mul_f32 v[78:79], v[78:79], v[246:247]\n"
    "v_pk_mul_f32 v[80:81], v[80:81], v[250:251]\n"
    "v_pk_mul_f32 v[82:83], v[82:83], v[254:255]\n"
    "v_exp_f32 v76, v76\n"
    "v_exp_f32 v77, v77\n"
    "v_exp_f32 v78, v78\n"
    "v_exp_f32 v79, v79\n"
    "v_exp_f32 v80, v80\n"
    "v_exp_f32 v81, v81\n"
    "v_exp_f32 v82, v82\n"
    "v_exp_f32 v83, v83\n"
    "v_pk_fma_f32 v[76:77], v[76:77], v[74:75], v[74:75]\n"
    "v_pk_fma_f32 v[78:79], v[78:79], v[74:75], v[74:75]\n"
    "v_pk_fma_f32 v[80:81], v[80:81], v[74:75], v[74:75]\n"
    "v_pk_fma_f32 v[82:83], v[82:83], v[74:75], v[74:75]\n"
    "v_rcp_f32 v76, v76\n"
    "v_rcp_f32 v77, v77\n"
    "v_rcp_f32 v78, v78\n"
    "v_rcp_f32 v79, v79\n"
    "v_rcp_f32 v80, v80\n"
    "v_rcp_f32 v81, v81\n"
    "v_rcp_f32 v82, v82\n"
    "v_rcp_f32 v83, v83\n"
    "v_pk_fma_f32 v[84:85], v[242:243], v[76:77], v[84:85]\n"
    "v_pk_fma_f32 v[86:87], v[246:247], v[78:79], v[86:87]\n"
    "v_pk_fma_f32 v[88:89], v[250:251], v[80:81], v[88:89]\n"
    "v_pk_fma_f32 v[90:91], v[254:255], v[82:83], v[90:91]\n"
    "s_cmp_eq_u32 s21, 0\n"
    "s_cbranch_scc1 .Lxd_gb0\n"
    "s_barrier\n"
    "s_setprio 3\n"
    ".Lxd_gb0:\n"
    "v_add_f32 v84, v84, v85\n"
    "v_add_f32 v86, v86, v87\n"
    "v_add_f32 v88, v88, v89\n"
    "v_add_f32 v90, v90, v91\n"
    "s_waitcnt lgkmcnt(15)\n"
    "v_pk_mul_f16 v60, v44, s45 op_sel:[0,1]\n"
    "v_pk_mul_f16 v61, v45, s45 op_sel:[0,1]\n"
    "v_permlane32_swap_b32 v84, v88\n"
    "v_permlane32_swap_b32 v86, v90\n"
    "v_add_f32 v84, v84, v88\n"
    "v_add_f32 v86, v86, v90\n"
    "v_pk_mul_f16 v62, v46, s45 op_sel:[0,1]\n"
    "v_pk_mul_f16 v63, v47, s45 op_sel:[0,1]\n"
    "v_permlane16_swap_b32 v84, v86\n"
    "v_add_f32 v84, v84, v86\n"
    "v_and_b32 v84, -2, v84\n"
    "ds_wrxchg_rtn_b32 v85, v11, v84\n"
    "ds_read_b128 v[224:227], v42 offset:33792\n"
    "ds_read_b128 v[228:231], v42 offset:34816\n"
    "ds_read_b128 v[232:235], v42 offset:35840\n"
    "ds_read_b128 v[236:239], v42 offset:36864\n"
    "ds_read_b128 v[240:243], v42 offset:37888\n"
    "ds_read_b128 v[244:247], v42 offset:38912\n"
    "ds_read_b128 v[248:251], v42 offset:39936\n"
    "ds_read_b128 v[252:255], v42 offset:40960\n"
    "v_pk_mul_f16 v64, v44, v46\n"
    "v_pk_mul_f16 v65, v45, v47\n"
    "v_pk_mul_f16 v66, v48, v48 op_sel_hi:[0,1]\n"
    "v_pk_mul_f16 v67, v48, v49 op_sel_hi:[0,1]\n"
    "v_lshlrev_b32 v86, 3, v1\n"
    "v_mov_b64 v[88:89], 0\n"
    "v_mov_b64 v[90:91], 0\n"
    "s_waitcnt lgkmcnt(8)\n"
    "v_cmp_ne_u32 vcc, -1, v85\n"
    "v_add_f32 v84, v84, v85\n"
    "v_add_u32 v84, 0x80, v84\n"
    "v_and_b32 v84, 0xffffff00, v84\n"
    "v_or_b32 v26, s28, v84\n"
    "v_mov_b32 v27, s29\n"
    "s_cbranch_vccz .Lxd_nost\n"
    "s_mov_b64 exec, vcc\n"
    "global_store_dwordx2 v86, v[26:27], s[48:49] sc1\n"
    "s_mov_b64 exec, -1\n"
    ".Lxd_nost:\n"
    "s_cmp_lg_u32 s21, 0\n"
    "s_cbranch_scc1 .Lxd_gb1\n"
    "s_barrier\n"
    ".Lxd_gb1:\n"
    ".Lxd_enter:\n"
    "s_cmp_eq_u32 s21, 0\n"
    "s_cselect_b64 vcc, 0, -1\n"
    "s_cbranch_scc1 .Lxd_pr\n"
    "s_setprio 1\n"
    ".Lxd_pr:\n"
    "s_cmp_lg_u32 s36, 0\n"
    "s_cbranch_scc1 .Lxd_el\n"
    "s_setpc_b64 s[10:11]\n"
    ".Lxd_el:\n"
    "s_cmp_eq_u32 s22, 0\n"
    "s_cbranch_scc1 .Lxd_pl1\n"
    "s_branch .Lxd_pl2\n"
    ".Lxd_ph0:\n"
    "s_cbranch_vccnz .Lxd_pr16\n"
    "s_setprio 2\n"
    ".Lxd_pr16:\n"
    "v_smfmac_f32_16x16x64_f16 v[88:91], v[112:115], v[60:67], v2\n"
    "v_fmac_f32 v11, v76, v80\n"
    "v_fmac_f32 v11, v77, v81\n"
    "v_fmac_f32 v11, v78, v82\n"
    "v_fmac_f32 v11, v79, v83\n"
    "v_mfma_f32_16x16x32_f16 v[80:83], v[92:95], v[60:63], 0\n"
    "v_pk_mul_f16 v68, v58, v58 op_sel_hi:[0,1]\n"
    "v_pk_mul_f16 v69, v58, v49 op_sel_hi:[0,1]\n"
    "v_mfma_f32_16x16x32_f16 v[80:83], v[104:107], v[64:67], v[80:83]\n"
    "v_pk_mul_f16 v70, v48, v58 op_sel_hi:[0,1]\n"
    "v_pk_mul_f16 v71, v48, v59 op_sel_hi:[0,1]\n"
    "v_mfma_f32_16x16x32_f16 v[76:79], v[100:103], v[64:67], 0\n"
    "v_fmac_f32 v11, v84, v85\n"
    "v_fma_f32 v10, v86, v87, v11\n"
    "v_mfma_f32_16x16x32_f16 v[84:87], v[108:111], v[64:67], 0\n"
    "v_pk_mul_f16 v72, v50, v58 op_sel_hi:[0,1]\n"
    "v_pk_mul_f16 v73, v50, v59 op_sel_hi:[0,1]\n"
    "v_mfma_f32_16x16x32_f16 v[84:87], v[96:99], v[60:63], v[84:87]\n"
    "v_pk_mul_f16 v74, v52, v58 op_sel_hi:[0,1]\n"
    "v_pk_mul_f16 v75, v52, v59 op_sel_hi:[0,1]\n"
    "v_permlane32_swap_b32 v7, v8\n"
    "v_permlane32_swap_b32 v9, v10\n"
    "s_waitcnt vmcnt(0)\n"
    "v_mfma_f32_16x16x32_f16 v[76:79], v[116:119], v[68:71], v[76:79]\n"
    "v_pk_mul_f16 v60, v54, v58 op_sel_hi:[0,1]\n"
    "v_pk_mul_f16 v61, v54, v59 op_sel_hi:[0,1]\n"
    "v_mfma_f32_16x16x32_f16 v[76:79], v[128:131], v[72:75], v[76:79]\n"
    "v_pk_mul_f16 v62, v56, v58 op_sel_hi:[0,1]\n"
    "v_readfirstlane_b32 s40, v24\n"
    "s_or_b32 s40, s40, s44\n"
    "s_lshl1_add_u32 s41, s40, s54\n"
    "s_cmpk_lt_u32 s40, 256\n"
    "s_cselect_b32 s41, s41, 2\n"
    "v_mov_b32 v12, s41\n"
    "s_cbranch_scc0 .Lxd_tf15\n"
    ".Lxd_tf15r:\n"
    "s_mul_i32 s41, s41, 52\n"
    "s_add_u32 s38, s4, s41\n"
    "s_addc_u32 s39, s5, 0\n"
    "v_mfma_f32_16x16x32_f16 v[80:83], v[132:135], v[72:75], v[80:83]\n"
    "v_pk_mul_f16 v63, v56, v59 op_sel_hi:[0,1]\n"
    "ds_write_b32 v25, v12 offset:20\n"
    "global_load_dwordx3 v[36:38], v17, s[38:39] offset:-104\n"
    "global_load_dword v41, v18, s[38:39] offset:-8\n"
    "v_mfma_f32_16x16x32_f16 v[80:83], v[120:123], v[68:71], v[80:83]\n"
    "v_pk_mul_f16 v64, v56, v56 op_sel_hi:[0,1]\n"
    "v_mul_f32 v7, v0, v7\n"
    "v_mul_f32 v9, v0, v9\n"
    "v_mfma_f32_16x16x32_f16 v[84:87], v[124:127], v[68:71], v[84:87]\n"
    "v_pk_mul_f16 v65, v56, v49 op_sel_hi:[0,1]\n"
    "v_fmac_f32 v7, v23, v8\n"
    "v_fmac_f32 v9, v23, v10\n"
    "v_mfma_f32_16x16x32_f16 v[84:87], v[136:139], v[72:75], v[84:87]\n"
    "v_pk_mul_f16 v66, v48, v56 op_sel_hi:[0,1]\n"
    "v_permlane16_swap_b32 v7, v9\n"
    "v_smfmac_f32_16x16x64_f16 v[88:91], v[140:143], v[68:75], v2\n"
    "v_pk_mul_f16 v67, v48, v57 op_sel_hi:[0,1]\n"
    "v_add_f32 v7, v7, v9\n"
    "v_mfma_f32_16x16x32_f16 v[76:79], v[144:147], v[60:63], v[76:79]\n"
    "v_pk_mul_f16 v68, v50, v56 op_sel_hi:[0,1]\n"
    "v_pk_mul_f16 v69, v50, v57 op_sel_hi:[0,1]\n"
    "v_mfma_f32_16x16x32_f16 v[76:79], v[156:159], v[64:67], v[76:79]\n"
    "v_pk_mul_f16 v70, v52, v56 op_sel_hi:[0,1]\n"
    "v_add_f32_dpp v7, v7, v7 quad_perm:[1,0,3,2] row_mask:0xf bank_mask:0xf\n"
    "v_mfma_f32_16x16x32_f16 v[80:83], v[160:163], v[64:67], v[80:83]\n"
    "v_pk_mul_f16 v71, v52, v57 op_sel_hi:[0,1]\n"
    "v_add_f32_dpp v7, v7, v7 quad_perm:[2,3,0,1] row_mask:0xf bank_mask:0xf\n"
    "v_mfma_f32_16x16x32_f16 v[80:83], v[148:151], v[60:63], v[80:83]\n"
    "v_pk_mul_f16 v72, v54, v56 op_sel_hi:[0,1]\n"
    "ds_read_b32 v22, v25 offset:516\n"
    "v_mfma_f32_16x16x32_f16 v[84:87], v[152:155], v[60:63], v[84:87]\n"
    "v_pk_mul_f16 v73, v54, v57 op_sel_hi:[0,1]\n"
    "v_add_f32_dpp v7, v7, v7 row_half_mirror row_mask:0xf bank_mask:0xf\n"
    "v_mfma_f32_16x16x32_f16 v[84:87], v[164:167], v[64:67], v[84:87]\n"
    "v_pk_mul_f16 v74, v54, v54 op_sel_hi:[0,1]\n"
    "v_add_f32_dpp v7, v7, v7 row_mirror row_mask:0xf bank_mask:0xf\n"
    "v_smfmac_f32_16x16x64_f16 v[88:91], v[168:171], v[60:67], v2\n"
    "v_pk_mul_f16 v75, v54, v49 op_sel_hi:[0,1]\n"
    "ds_write_b32 v14, v7\n"
    "v_add_u32 v14, 64, v14\n"
    "v_mfma_f32_16x16x32_f16 v[76:79], v[172:175], v[68:71], v[76:79]\n"
    "v_pk_mul_f16 v60, v48, v54 op_sel_hi:[0,1]\n"
    "v_pk_mul_f16 v61, v48, v55 op_sel_hi:[0,1]\n"
    "v_mfma_f32_16x16x32_f16 v[76:79], v[184:187], v[72:75], v[76:79]\n"
    "v_pk_mul_f16 v62, v50, v54 op_sel_hi:[0,1]\n"
    "v_mfma_f32_16x16x32_f16 v[80:83], v[188:191], v[72:75], v[80:83]\n"
    "v_pk_mul_f16 v63, v50, v55 op_sel_hi:[0,1]\n"
    "s_waitcnt lgkmcnt(1)\n"
    "v_readfirstlane_b32 s45, v22\n"
    "s_cmp_lt_i32 s45, 16\n"
    "s_cbranch_scc1 .Lxd_ck18\n"
    ".Lxd_ck18r:\n"
    "v_mfma_f32_16x16x32_f16 v[80:83], v[176:179], v[68:71], v[80:83]\n"
    "v_pk_mul_f16 v64, v52, v54 op_sel_hi:[0,1]\n"
    "ds_read_b64_tr_b16 v[44:45], v5 offset:4096\n"
    "v_mfma_f32_16x16x32_f16 v[84:87], v[180:183], v[68:71], v[84:87]\n"
    "v_pk_mul_f16 v65, v52, v55 op_sel_hi:[0,1]\n"
    "ds_read_b64_tr_b16 v[46:47], v6 offset:4096\n"
    "v_mfma_f32_16x16x32_f16 v[84:87], v[192:195], v[72:75], v[84:87]\n"
    "v_pk_mul_f16 v66, v52, v52 op_sel_hi:[0,1]\n"
    "ds_read_b64_tr_b16 v[58:59], v4 offset:6976\n"
    "s_cbranch_vccnz .Lxd_pr17\n"
    "s_setprio 0\n"
    ".Lxd_pr17:\n"
    "v_smfmac_f32_16x16x64_f16 v[88:91], v[196:199], v[68:75], v2\n"
    "v_pk_mul_f16 v67, v52, v49 op_sel_hi:[0,1]\n"
    "ds_read_b64_tr_b16 v[56:57], v4 offset:6400\n"
    "v_mfma_f32_16x16x32_f16 v[76:79], v[200:203], v[60:63], v[76:79]\n"
    "v_pk_mul_f16 v68, v48, v52 op_sel_hi:[0,1]\n"
    "v_pk_mul_f16 v69, v48, v53 op_sel_hi:[0,1]\n"
    "v_mfma_f32_16x16x32_f16 v[76:79], v[212:215], v[64:67], v[76:79]\n"
    "v_pk_mul_f16 v70, v50, v52 op_sel_hi:[0,1]\n"
    "ds_read_b64_tr_b16 v[54:55], v4 offset:5824\n"
    "v_mfma_f32_16x16x32_f16 v[80:83], v[216:219], v[64:67], v[80:83]\n"
    "v_pk_mul_f16 v71, v50, v53 op_sel_hi:[0,1]\n"
    "v_xor_b32 v15, 0x4000, v15\n"
    "v_mfma_f32_16x16x32_f16 v[80:83], v[204:207], v[60:63], v[80:83]\n"
    "v_pk_mul_f16 v72, v50, v50 op_sel_hi:[0,1]\n"
    "v_xor_b32 v16, 0x4000, v16\n"
    "v_mfma_f32_16x16x32_f16 v[84:87], v[208:211], v[60:63], v[84:87]\n"
    "v_pk_mul_f16 v73, v50, v49 op_sel_hi:[0,1]\n"
    "v_pk_mul_f16 v74, v48, v50 op_sel_hi:[0,1]\n"
    "v_mfma_f32_16x16x32_f16 v[84:87], v[220:223], v[64:67], v[84:87]\n"
    "v_pk_mul_f16 v75, v48, v51 op_sel_hi:[0,1]\n"
    "v_smfmac_f32_16x16x64_f16 v[88:91], v[224:227], v[60:67], v2\n"
    "ds_read_b64_tr_b16 v[48:49], v3 offset:4096\n"
    "ds_read_b64_tr_b16 v[50:51], v4 offset:4672\n"
    "ds_read_b64_tr_b16 v[52:53], v4 offset:5248\n"
    "v_smfmac_f32_16x16x64_f16 v[88:91], v[252:255], v[68:75], v2\n"
    "s_waitcnt lgkmcnt(6)\n"
    "v_pk_mul_f16 v60, v44, s45 op_sel:[0,1]\n"
    "v_pk_mul_f16 v61, v45, s45 op_sel:[0,1]\n"
    "v_mfma_f32_16x16x32_f16 v[76:79], v[228:231], v[68:71], v[76:79]\n"
    "v_pk_mul_f16 v62, v46, s45 op_sel:[0,1]\n"
    "v_pk_mul_f16 v63, v47, s45 op_sel:[0,1]\n"
    "v_mfma_f32_16x16x32_f16 v[76:79], v[240:243], v[72:75], v[76:79]\n"
    "v_pk_mul_f16 v64, v44, v46\n"
    "v_pk_mul_f16 v65, v45, v47\n"
    "v_mfma_f32_16x16x32_f16 v[80:83], v[244:247], v[72:75], v[80:83]\n"
    "v_add_f32 v11, v88, v89\n"
    "v_add_f32 v13, v90, v91\n"
    "v_mfma_f32_16x16x32_f16 v[80:83], v[232:235], v[68:71], v[80:83]\n"
    "v_mov_b64 v[88:89], 0\n"
    "v_mov_b64 v[90:91], 0\n"
    "v_mfma_f32_16x16x32_f16 v[84:87], v[236:239], v[68:71], v[84:87]\n"
    "s_waitcnt lgkmcnt(0)\n"
    "v_pk_mul_f16 v66, v48, v48 op_sel_hi:[0,1]\n"
    "v_pk_mul_f16 v67, v48, v49 op_sel_hi:[0,1]\n"
    "v_mfma_f32_16x16x32_f16 v[84:87], v[248:251], v[72:75], v[84:87]\n"
    "v_mul_f32 v11, v11, v13\n"
    "s_cmp_lg_u32 s36, 0\n"
    "s_cbranch_scc1 .Lxd_pl1\n"
    ".Lxd_ph1:\n"
    "s_cbranch_vccnz .Lxd_pr19\n"
    "s_setprio 2\n"
    ".Lxd_pr19:\n"
    "v_smfmac_f32_16x16x64_f16 v[88:91], v[112:115], v[60:67], v2\n"
    "v_fmac_f32 v11, v76, v80\n"
    "v_fmac_f32 v11, v77, v81\n"
    "v_fmac_f32 v11, v78, v82\n"
    "v_fmac_f32 v11, v79, v83\n"
    "v_mfma_f32_16x16x32_f16 v[80:83], v[92:95], v[60:63], 0\n"
    "v_pk_mul_f16 v68, v58, v58 op_sel_hi:[0,1]\n"
    "v_pk_mul_f16 v69, v58, v49 op_sel_hi:[0,1]\n"
    "v_mfma_f32_16x16x32_f16 v[80:83], v[104:107], v[64:67], v[80:83]\n"
    "v_pk_mul_f16 v70, v48, v58 op_sel_hi:[0,1]\n"
    "v_pk_mul_f16 v71, v48, v59 op_sel_hi:[0,1]\n"
    "v_mfma_f32_16x16x32_f16 v[76:79], v[100:103], v[64:67], 0\n"
    "v_fmac_f32 v11, v84, v85\n"
    "v_fma_f32 v7, v86, v87, v11\n"
    "v_mfma_f32_16x16x32_f16 v[84:87], v[108:111], v[64:67], 0\n"
    "v_pk_mul_f16 v72, v50, v58 op_sel_hi:[0,1]\n"
    "v_pk_mul_f16 v73, v50, v59 op_sel_hi:[0,1]\n"
    "v_mfma_f32_16x16x32_f16 v[84:87], v[96:99], v[60:63], v[84:87]\n"
    "v_pk_mul_f16 v74, v52, v58 op_sel_hi:[0,1]\n"
    "v_pk_mul_f16 v75, v52, v59 op_sel_hi:[0,1]\n"
    "s_waitcnt vmcnt(0)\n"
    "v_mfma_f32_16x16x32_f16 v[76:79], v[116:119], v[68:71], v[76:79]\n"
    "v_pk_mul_f16 v60, v54, v58 op_sel_hi:[0,1]\n"
    "v_pk_mul_f16 v61, v54, v59 op_sel_hi:[0,1]\n"
    "v_mfma_f32_16x16x32_f16 v[76:79], v[128:131], v[72:75], v[76:79]\n"
    "v_pk_mul_f16 v62, v56, v58 op_sel_hi:[0,1]\n"
    "v_lshl_or_b32 v36, v36, 7, v19\n"
    "global_load_dwordx4 v[28:31], v36, s[8:9]\n"
    "v_mfma_f32_16x16x32_f16 v[80:83], v[132:135], v[72:75], v[80:83]\n"
    "v_pk_mul_f16 v63, v56, v59 op_sel_hi:[0,1]\n"
    "v_lshl_or_b32 v37, v37, 7, v19\n"
    "global_load_dwordx4 v[32:35], v37, s[8:9]\n"
    "v_mfma_f32_16x16x32_f16 v[80:83], v[120:123], v[68:71], v[80:83]\n"
    "v_pk_mul_f16 v64, v56, v56 op_sel_hi:[0,1]\n"
    "v_lshl_or_b32 v41, v41, 7, v20\n"
    "global_load_dword v40, v41, s[8:9]\n"
    "v_mfma_f32_16x16x32_f16 v[84:87], v[124:127], v[68:71], v[84:87]\n"
    "v_pk_mul_f16 v65, v56, v49 op_sel_hi:[0,1]\n"
    "v_lshl_or_b32 v38, v38, 7, v19\n"
    "global_load_dwordx4 v[36:39], v38, s[8:9]\n"
    "v_mfma_f32_16x16x32_f16 v[84:87], v[136:139], v[72:75], v[84:87]\n"
    "v_pk_mul_f16 v66, v48, v56 op_sel_hi:[0,1]\n"
    "v_smfmac_f32_16x16x64_f16 v[88:91], v[140:143], v[68:75], v2\n"
    "v_pk_mul_f16 v67, v48, v57 op_sel_hi:[0,1]\n"
    "v_mfma_f32_16x16x32_f16 v[76:79], v[144:147], v[60:63], v[76:79]\n"
    "v_pk_mul_f16 v68, v50, v56 op_sel_hi:[0,1]\n"
    "v_pk_mul_f16 v69, v50, v57 op_sel_hi:[0,1]\n"
    "v_mfma_f32_16x16x32_f16 v[76:79], v[156:159], v[64:67], v[76:79]\n"
    "v_pk_mul_f16 v70, v52, v56 op_sel_hi:[0,1]\n"
    "v_mfma_f32_16x16x32_f16 v[80:83], v[160:163], v[64:67], v[80:83]\n"
    "v_pk_mul_f16 v71, v52, v57 op_sel_hi:[0,1]\n"
    "v_mfma_f32_16x16x32_f16 v[80:83], v[148:151], v[60:63], v[80:83]\n"
    "v_pk_mul_f16 v72, v54, v56 op_sel_hi:[0,1]\n"
    "ds_read_b32 v22, v25 offset:520\n"
    "v_mfma_f32_16x16x32_f16 v[84:87], v[152:155], v[60:63], v[84:87]\n"
    "v_pk_mul_f16 v73, v54, v57 op_sel_hi:[0,1]\n"
    "v_mfma_f32_16x16x32_f16 v[84:87], v[164:167], v[64:67], v[84:87]\n"
    "v_pk_mul_f16 v74, v54, v54 op_sel_hi:[0,1]\n"
    "v_smfmac_f32_16x16x64_f16 v[88:91], v[168:171], v[60:67], v2\n"
    "v_pk_mul_f16 v75, v54, v49 op_sel_hi:[0,1]\n"
    "v_mfma_f32_16x16x32_f16 v[76:79], v[172:175], v[68:71], v[76:79]\n"
    "v_pk_mul_f16 v60, v48, v54 op_sel_hi:[0,1]\n"
    "v_pk_mul_f16 v61, v48, v55 op_sel_hi:[0,1]\n"
    "v_mfma_f32_16x16x32_f16 v[76:79], v[184:187], v[72:75], v[76:79]\n"
    "v_pk_mul_f16 v62, v50, v54 op_sel_hi:[0,1]\n"
    "v_mfma_f32_16x16x32_f16 v[80:83], v[188:191], v[72:75], v[80:83]\n"
    "v_pk_mul_f16 v63, v50, v55 op_sel_hi:[0,1]\n"
    "s_waitcnt lgkmcnt(0)\n"
    "v_readfirstlane_b32 s45, v22\n"
    "s_cmp_lt_i32 s45, 16\n"
    "s_cbranch_scc1 .Lxd_ck21\n"
    ".Lxd_ck21r:\n"
    "v_mfma_f32_16x16x32_f16 v[80:83], v[176:179], v[68:71], v[80:83]\n"
    "v_pk_mul_f16 v64, v52, v54 op_sel_hi:[0,1]\n"
    "ds_read_b64_tr_b16 v[44:45], v5 offset:8192\n"
    "v_mfma_f32_16x16x32_f16 v[84:87], v[180:183], v[68:71], v[84:87]\n"
    "v_pk_mul_f16 v65, v52, v55 op_sel_hi:[0,1]\n"
    "ds_read_b64_tr_b16 v[46:47], v6 offset:8192\n"
    "v_mfma_f32_16x16x32_f16 v[84:87], v[192:195], v[72:75], v[84:87]\n"
    "v_pk_mul_f16 v66, v52, v52 op_sel_hi:[0,1]\n"
    "ds_read_b64_tr_b16 v[58:59], v4 offset:11072\n"
    "s_cbranch_vccnz .Lxd_pr20\n"
    "s_setprio 0\n"
    ".Lxd_pr20:\n"
    "v_smfmac_f32_16x16x64_f16 v[88:91], v[196:199], v[68:75], v2\n"
    "v_pk_mul_f16 v67, v52, v49 op_sel_hi:[0,1]\n"
    "ds_read_b64_tr_b16 v[56:57], v4 offset:10496\n"
    "v_mfma_f32_16x16x32_f16 v[76:79], v[200:203], v[60:63], v[76:79]\n"
    "v_pk_mul_f16 v68, v48, v52 op_sel_hi:[0,1]\n"
    "v_pk_mul_f16 v69, v48, v53 op_sel_hi:[0,1]\n"
    "v_mfma_f32_16x16x32_f16 v[76:79], v[212:215], v[64:67], v[76:79]\n"
    "v_pk_mul_f16 v70, v50, v52 op_sel_hi:[0,1]\n"
    "ds_read_b64_tr_b16 v[54:55], v4 offset:9920\n"
    "v_mfma_f32_16x16x32_f16 v[80:83], v[216:219], v[64:67], v[80:83]\n"
    "v_pk_mul_f16 v71, v50, v53 op_sel_hi:[0,1]\n"
    "v_mfma_f32_16x16x32_f16 v[80:83], v[204:207], v[60:63], v[80:83]\n"
    "v_pk_mul_f16 v72, v50, v50 op_sel_hi:[0,1]\n"
    "v_mfma_f32_16x16x32_f16 v[84:87], v[208:211], v[60:63], v[84:87]\n"
    "v_pk_mul_f16 v73, v50, v49 op_sel_hi:[0,1]\n"
    "v_pk_mul_f16 v74, v48, v50 op_sel_hi:[0,1]\n"
    "v_mfma_f32_16x16x32_f16 v[84:87], v[220:223], v[64:67], v[84:87]\n"
    "v_pk_mul_f16 v75, v48, v51 op_sel_hi:[0,1]\n"
    "v_smfmac_f32_16x16x64_f16 v[88:91], v[224:227], v[60:67], v2\n"
    "ds_read_b64_tr_b16 v[48:49], v3 offset:8192\n"
    "ds_read_b64_tr_b16 v[50:51], v4 offset:8768\n"
    "ds_read_b64_tr_b16 v[52:53], v4 offset:9344\n"
    "v_smfmac_f32_16x16x64_f16 v[88:91], v[252:255], v[68:75], v2\n"
    "s_waitcnt lgkmcnt(6)\n"
    "v_pk_mul_f16 v60, v44, s45 op_sel:[0,1]\n"
    "v_pk_mul_f16 v61, v45, s45 op_sel:[0,1]\n"
    "v_mfma_f32_16x16x32_f16 v[76:79], v[228:231], v[68:71], v[76:79]\n"
    "v_pk_mul_f16 v62, v46, s45 op_sel:[0,1]\n"
    "v_pk_mul_f16 v63, v47, s45 op_sel:[0,1]\n"
    "v_mfma_f32_16x16x32_f16 v[76:79], v[240:243], v[72:75], v[76:79]\n"
    "v_pk_mul_f16 v64, v44, v46\n"
    "v_pk_mul_f16 v65, v45, v47\n"
    "v_mfma_f32_16x16x32_f16 v[80:83], v[244:247], v[72:75], v[80:83]\n"
    "v_add_f32 v11, v88, v89\n"
    "v_add_f32 v13, v90, v91\n"
    "v_mfma_f32_16x16x32_f16 v[80:83], v[232:235], v[68:71], v[80:83]\n"
    "v_mov_b64 v[88:89], 0\n"
    "v_mov_b64 v[90:91], 0\n"
    "v_mfma_f32_16x16x32_f16 v[84:87], v[236:239], v[68:71], v[84:87]\n"
    "s_waitcnt lgkmcnt(0)\n"
    "v_pk_mul_f16 v66, v48, v48 op_sel_hi:[0,1]\n"
    "v_pk_mul_f16 v67, v48, v49 op_sel_hi:[0,1]\n"
    "v_mfma_f32_16x16x32_f16 v[84:87], v[248:251], v[72:75], v[84:87]\n"
    "v_mul_f32 v11, v11, v13\n"
    ".Lxd_ph2:\n"
    "s_cbranch_vccnz .Lxd_pr22\n"
    "s_setprio 2\n"
    ".Lxd_pr22:\n"
    "v_smfmac_f32_16x16x64_f16 v[88:91], v[112:115], v[60:67], v2\n"
    "v_fmac_f32 v11, v76, v80\n"
    "v_fmac_f32 v11, v77, v81\n"
    "v_fmac_f32 v11, v78, v82\n"
    "v_fmac_f32 v11, v79, v83\n"
    "v_mfma_f32_16x16x32_f16 v[80:83], v[92:95], v[60:63], 0\n"
    "v_pk_mul_f16 v68, v58, v58 op_sel_hi:[0,1]\n"
    "v_pk_mul_f16 v69, v58, v49 op_sel_hi:[0,1]\n"
    "v_mfma_f32_16x16x32_f16 v[80:83], v[104:107], v[64:67], v[80:83]\n"
    "v_pk_mul_f16 v70, v48, v58 op_sel_hi:[0,1]\n"
    "v_pk_mul_f16 v71, v48, v59 op_sel_hi:[0,1]\n"
    "v_mfma_f32_16x16x32_f16 v[76:79], v[100:103], v[64:67], 0\n"
    "v_fmac_f32 v11, v84, v85\n"
    "v_fma_f32 v8, v86, v87, v11\n"
    "v_mfma_f32_16x16x32_f16 v[84:87], v[108:111], v[64:67], 0\n"
    "v_pk_mul_f16 v72, v50, v58 op_sel_hi:[0,1]\n"
    "v_pk_mul_f16 v73, v50, v59 op_sel_hi:[0,1]\n"
    "v_mfma_f32_16x16x32_f16 v[84:87], v[96:99], v[60:63], v[84:87]\n"
    "v_pk_mul_f16 v74, v52, v58 op_sel_hi:[0,1]\n"
    "v_pk_mul_f16 v75, v52, v59 op_sel_hi:[0,1]\n"
    "s_waitcnt vmcnt(0)\n"
    "v_mfma_f32_16x16x32_f16 v[76:79], v[116:119], v[68:71], v[76:79]\n"
    "v_pk_mul_f16 v60, v54, v58 op_sel_hi:[0,1]\n"
    "v_pk_mul_f16 v61, v54, v59 op_sel_hi:[0,1]\n"
    "s_movk_i32 s43, 15744\n"
    "v_pk_maximum3_f16 v21, v28, v28, s43 op_sel_hi:[1,1,0] neg_lo:[0,1,0] neg_hi:[0,1,0]\n"
    "v_mfma_f32_16x16x32_f16 v[76:79], v[128:131], v[72:75], v[76:79]\n"
    "v_pk_mul_f16 v62, v56, v58 op_sel_hi:[0,1]\n"
    "v_pk_maximum3_f16 v21, v29, v29, v21 neg_lo:[0,1,0] neg_hi:[0,1,0]\n"
    "v_mfma_f32_16x16x32_f16 v[80:83], v[132:135], v[72:75], v[80:83]\n"
    "v_pk_mul_f16 v63, v56, v59 op_sel_hi:[0,1]\n"
    "v_pk_maximum3_f16 v21, v30, v30, v21 neg_lo:[0,1,0] neg_hi:[0,1,0]\n"
    "v_mfma_f32_16x16x32_f16 v[80:83], v[120:123], v[68:71], v[80:83]\n"
    "v_pk_mul_f16 v64, v56, v56 op_sel_hi:[0,1]\n"
    "v_pk_maximum3_f16 v21, v31, v31, v21 neg_lo:[0,1,0] neg_hi:[0,1,0]\n"
    "v_mfma_f32_16x16x32_f16 v[84:87], v[124:127], v[68:71], v[84:87]\n"
    "v_pk_mul_f16 v65, v56, v49 op_sel_hi:[0,1]\n"
    "v_pk_maximum3_f16 v21, v32, v32, v21 neg_lo:[0,1,0] neg_hi:[0,1,0]\n"
    "v_mfma_f32_16x16x32_f16 v[84:87], v[136:139], v[72:75], v[84:87]\n"
    "v_pk_mul_f16 v66, v48, v56 op_sel_hi:[0,1]\n"
    "v_pk_maximum3_f16 v21, v33, v33, v21 neg_lo:[0,1,0] neg_hi:[0,1,0]\n"
    "v_smfmac_f32_16x16x64_f16 v[88:91], v[140:143], v[68:75], v2\n"
    "v_pk_mul_f16 v67, v48, v57 op_sel_hi:[0,1]\n"
    "s_cmp_lg_u32 s46, 0\n"
    "s_cbranch_scc1 .Lxd_ht24\n"
    ".Lxd_ht24r:\n"
    "v_pk_maximum3_f16 v21, v34, v34, v21 neg_lo:[0,1,0] neg_hi:[0,1,0]\n"
    "v_mfma_f32_16x16x32_f16 v[76:79], v[144:147], v[60:63], v[76:79]\n"
    "v_pk_mul_f16 v68, v50, v56 op_sel_hi:[0,1]\n"
    "v_pk_mul_f16 v69, v50, v57 op_sel_hi:[0,1]\n"
    "v_pk_maximum3_f16 v21, v35, v35, v21 neg_lo:[0,1,0] neg_hi:[0,1,0]\n"
    "v_mfma_f32_16x16x32_f16 v[76:79], v[156:159], v[64:67], v[76:79]\n"
    "v_pk_mul_f16 v70, v52, v56 op_sel_hi:[0,1]\n"
    "v_pk_maximum3_f16 v21, v36, v36, v21 neg_lo:[0,1,0] neg_hi:[0,1,0]\n"
    "v_mfma_f32_16x16x32_f16 v[80:83], v[160:163], v[64:67], v[80:83]\n"
    "v_pk_mul_f16 v71, v52, v57 op_sel_hi:[0,1]\n"
    "v_pk_maximum3_f16 v21, v37, v37, v21 neg_lo:[0,1,0] neg_hi:[0,1,0]\n"
    "v_mfma_f32_16x16x32_f16 v[80:83], v[148:151], v[60:63], v[80:83]\n"
    "v_pk_mul_f16 v72, v54, v56 op_sel_hi:[0,1]\n"
    "ds_read_b32 v22, v25 offset:524\n"
    "v_pk_maximum3_f16 v21, v38, v38, v21 neg_lo:[0,1,0] neg_hi:[0,1,0]\n"
    "v_mfma_f32_16x16x32_f16 v[84:87], v[152:155], v[60:63], v[84:87]\n"
    "v_pk_mul_f16 v73, v54, v57 op_sel_hi:[0,1]\n"
    "v_pk_maximum3_f16 v21, v39, v39, v21 neg_lo:[0,1,0] neg_hi:[0,1,0]\n"
    "v_mfma_f32_16x16x32_f16 v[84:87], v[164:167], v[64:67], v[84:87]\n"
    "v_pk_mul_f16 v74, v54, v54 op_sel_hi:[0,1]\n"
    "v_pk_maximum3_f16 v21, v40, v40, v21 neg_lo:[0,1,0] neg_hi:[0,1,0]\n"
    "v_smfmac_f32_16x16x64_f16 v[88:91], v[168:171], v[60:67], v2\n"
    "v_pk_mul_f16 v75, v54, v49 op_sel_hi:[0,1]\n"
    "v_pk_max_u16 v21, v21, v21 op_sel:[0,1] op_sel_hi:[1,0]\n"
    "v_mfma_f32_16x16x32_f16 v[76:79], v[172:175], v[68:71], v[76:79]\n"
    "v_pk_mul_f16 v60, v48, v54 op_sel_hi:[0,1]\n"
    "v_pk_mul_f16 v61, v48, v55 op_sel_hi:[0,1]\n"
    "v_max_u32_dpp v21, v21, v21 quad_perm:[1,0,3,2] row_mask:0xf bank_mask:0xf\n"
    "v_mfma_f32_16x16x32_f16 v[76:79], v[184:187], v[72:75], v[76:79]\n"
    "v_pk_mul_f16 v62, v50, v54 op_sel_hi:[0,1]\n"
    "v_max_u32_dpp v21, v21, v21 quad_perm:[2,3,0,1] row_mask:0xf bank_mask:0xf\n"
    "v_mfma_f32_16x16x32_f16 v[80:83], v[188:191], v[72:75], v[80:83]\n"
    "v_pk_mul_f16 v63, v50, v55 op_sel_hi:[0,1]\n"
    "s_waitcnt lgkmcnt(0)\n"
    "v_readfirstlane_b32 s45, v22\n"
    "s_cmp_lt_i32 s45, 16\n"
    "s_cbranch_scc1 .Lxd_ck25\n"
    ".Lxd_ck25r:\n"
    "v_max_u32_dpp v21, v21, v21 row_half_mirror row_mask:0xf bank_mask:0xf\n"
    "v_mfma_f32_16x16x32_f16 v[80:83], v[176:179], v[68:71], v[80:83]\n"
    "v_pk_mul_f16 v64, v52, v54 op_sel_hi:[0,1]\n"
    "ds_read_b64_tr_b16 v[44:45], v5 offset:12288\n"
    "v_max_u32_dpp v21, v21, v21 row_mirror row_mask:0xf bank_mask:0xf\n"
    "v_mfma_f32_16x16x32_f16 v[84:87], v[180:183], v[68:71], v[84:87]\n"
    "v_pk_mul_f16 v65, v52, v55 op_sel_hi:[0,1]\n"
    "ds_read_b64_tr_b16 v[46:47], v6 offset:12288\n"
    "v_max_u32_dpp v21, v21, v21 row_bcast:15 row_mask:0xa bank_mask:0xf\n"
    "v_mfma_f32_16x16x32_f16 v[84:87], v[192:195], v[72:75], v[84:87]\n"
    "v_pk_mul_f16 v66, v52, v52 op_sel_hi:[0,1]\n"
    "ds_read_b64_tr_b16 v[58:59], v4 offset:15168\n"
    "v_max_u32_dpp v21, v21, v21 row_bcast:31 row_mask:0xc bank_mask:0xf\n"
    "s_cbranch_vccnz .Lxd_pr23\n"
    "s_setprio 0\n"
    ".Lxd_pr23:\n"
    "v_smfmac_f32_16x16x64_f16 v[88:91], v[196:199], v[68:75], v2\n"
    "v_pk_mul_f16 v67, v52, v49 op_sel_hi:[0,1]\n"
    "ds_read_b64_tr_b16 v[56:57], v4 offset:14592\n"
    "v_readlane_b32 s47, v21, 63\n"
    "v_mfma_f32_16x16x32_f16 v[76:79], v[200:203], v[60:63], v[76:79]\n"
    "v_pk_mul_f16 v68, v48, v52 op_sel_hi:[0,1]\n"
    "v_pk_mul_f16 v69, v48, v53 op_sel_hi:[0,1]\n"
    "s_and_b32 s50, s47, 0x7f80\n"
    "s_lshr_b32 s47, s50, 3\n"
    "s_sub_u32 s50, s50, 2816\n"
    "s_mul_i32 s51, s50, -8\n"
    "v_mfma_f32_16x16x32_f16 v[76:79], v[212:215], v[64:67], v[76:79]\n"
    "v_pk_mul_f16 v70, v50, v52 op_sel_hi:[0,1]\n"
    "ds_read_b64_tr_b16 v[54:55], v4 offset:14016\n"
    "v_pk_maximum3_f16 v21, v28, v28, s50 op_sel_hi:[1,1,0] neg_lo:[0,1,0] neg_hi:[0,1,0]\n"
    "v_mfma_f32_16x16x32_f16 v[80:83], v[216:219], v[64:67], v[80:83]\n"
    "v_pk_mul_f16 v71, v50, v53 op_sel_hi:[0,1]\n"
    "v_pk_mad_u16 v21, v21, 8, s51 op_sel_hi:[1,0,0]\n"
    "v_mfma_f32_16x16x32_f16 v[80:83], v[204:207], v[60:63], v[80:83]\n"
    "v_pk_mul_f16 v72, v50, v50 op_sel_hi:[0,1]\n"
    "v_bfi_b32 v28, s57, v21, v28\n"
    "v_mfma_f32_16x16x32_f16 v[84:87], v[208:211], v[60:63], v[84:87]\n"
    "v_pk_mul_f16 v73, v50, v49 op_sel_hi:[0,1]\n"
    "v_pk_mul_f16 v74, v48, v50 op_sel_hi:[0,1]\n"
    "v_pk_maximum3_f16 v12, v29, v29, s50 op_sel_hi:[1,1,0] neg_lo:[0,1,0] neg_hi:[0,1,0]\n"
    "v_mfma_f32_16x16x32_f16 v[84:87], v[220:223], v[64:67], v[84:87]\n"
    "v_pk_mul_f16 v75, v48, v51 op_sel_hi:[0,1]\n"
    "v_pk_mad_u16 v12, v12, 8, s51 op_sel_hi:[1,0,0]\n"
    "v_smfmac_f32_16x16x64_f16 v[88:91], v[224:227], v[60:67], v2\n"
    "ds_read_b64_tr_b16 v[48:49], v3 offset:12288\n"
    "ds_read_b64_tr_b16 v[50:51], v4 offset:12864\n"
    "ds_read_b64_tr_b16 v[52:53], v4 offset:13440\n"
    "v_bfi_b32 v29, s57, v12, v29\n"
    "v_smfmac_f32_16x16x64_f16 v[88:91], v[252:255], v[68:75], v2\n"
    "s_waitcnt lgkmcnt(6)\n"
    "v_pk_mul_f16 v60, v44, s45 op_sel:[0,1]\n"
    "v_pk_mul_f16 v61, v45, s45 op_sel:[0,1]\n"
    "v_mfma_f32_16x16x32_f16 v[76:79], v[228:231], v[68:71], v[76:79]\n"
    "v_pk_mul_f16 v62, v46, s45 op_sel:[0,1]\n"
    "v_pk_mul_f16 v63, v47, s45 op_sel:[0,1]\n"
    "v_mfma_f32_16x16x32_f16 v[76:79], v[240:243], v[72:75], v[76:79]\n"
    "v_pk_mul_f16 v64, v44, v46\n"
    "v_pk_mul_f16 v65, v45, v47\n"
    "v_mfma_f32_16x16x32_f16 v[80:83], v[244:247], v[72:75], v[80:83]\n"
    "v_add_f32 v11, v88, v89\n"
    "v_add_f32 v13, v90, v91\n"
    "v_mfma_f32_16x16x32_f16 v[80:83], v[232:235], v[68:71], v[80:83]\n"
    "v_mov_b64 v[88:89], 0\n"
    "v_mov_b64 v[90:91], 0\n"
    "v_mfma_f32_16x16x32_f16 v[84:87], v[236:239], v[68:71], v[84:87]\n"
    "s_waitcnt lgkmcnt(0)\n"
    "v_pk_mul_f16 v66, v48, v48 op_sel_hi:[0,1]\n"
    "v_pk_mul_f16 v67, v48, v49 op_sel_hi:[0,1]\n"
    "v_mfma_f32_16x16x32_f16 v[84:87], v[248:251], v[72:75], v[84:87]\n"
    "v_mul_f32 v11, v11, v13\n"
    ".Lxd_ph3:\n"
    "s_cbranch_vccnz .Lxd_pr26\n"
    "s_setprio 2\n"
    ".Lxd_pr26:\n"
    "v_smfmac_f32_16x16x64_f16 v[88:91], v[112:115], v[60:67], v2\n"
    "v_fmac_f32 v11, v76, v80\n"
    "v_fmac_f32 v11, v77, v81\n"
    "v_fmac_f32 v11, v78, v82\n"
    "v_fmac_f32 v11, v79, v83\n"
    "v_mfma_f32_16x16x32_f16 v[80:83], v[92:95], v[60:63], 0\n"
    "v_pk_mul_f16 v68, v58, v58 op_sel_hi:[0,1]\n"
    "v_pk_mul_f16 v69, v58, v49 op_sel_hi:[0,1]\n"
    "v_xor_b32 v3, 0x4000, v3\n"
    "v_xor_b32 v4, 0x4000, v4\n"
    "v_mfma_f32_16x16x32_f16 v[80:83], v[104:107], v[64:67], v[80:83]\n"
    "v_pk_mul_f16 v70, v48, v58 op_sel_hi:[0,1]\n"
    "v_pk_mul_f16 v71, v48, v59 op_sel_hi:[0,1]\n"
    "v_xor_b32 v5, 0x4000, v5\n"
    "v_mfma_f32_16x16x32_f16 v[76:79], v[100:103], v[64:67], 0\n"
    "v_fmac_f32 v11, v84, v85\n"
    "v_fma_f32 v9, v86, v87, v11\n"
    "v_xor_b32 v6, 0x4000, v6\n"
    "v_mfma_f32_16x16x32_f16 v[84:87], v[108:111], v[64:67], 0\n"
    "v_pk_mul_f16 v72, v50, v58 op_sel_hi:[0,1]\n"
    "v_pk_mul_f16 v73, v50, v59 op_sel_hi:[0,1]\n"
    "v_pk_maximum3_f16 v21, v30, v30, s50 op_sel_hi:[1,1,0] neg_lo:[0,1,0] neg_hi:[0,1,0]\n"
    "v_pk_mad_u16 v21, v21, 8, s51 op_sel_hi:[1,0,0]\n"
    "v_mfma_f32_16x16x32_f16 v[84:87], v[96:99], v[60:63], v[84:87]\n"
    "v_pk_mul_f16 v74, v52, v58 op_sel_hi:[0,1]\n"
    "v_pk_mul_f16 v75, v52, v59 op_sel_hi:[0,1]\n"
    "v_bfi_b32 v30, s57, v21, v30\n"
    "v_mfma_f32_16x16x32_f16 v[76:79], v[116:119], v[68:71], v[76:79]\n"
    "v_pk_mul_f16 v60, v54, v58 op_sel_hi:[0,1]\n"
    "v_pk_mul_f16 v61, v54, v59 op_sel_hi:[0,1]\n"
    "v_pk_maximum3_f16 v12, v31, v31, s50 op_sel_hi:[1,1,0] neg_lo:[0,1,0] neg_hi:[0,1,0]\n"
    "v_mfma_f32_16x16x32_f16 v[76:79], v[128:131], v[72:75], v[76:79]\n"
    "v_pk_mul_f16 v62, v56, v58 op_sel_hi:[0,1]\n"
    "v_pk_mad_u16 v12, v12, 8, s51 op_sel_hi:[1,0,0]\n"
    "v_mfma_f32_16x16x32_f16 v[80:83], v[132:135], v[72:75], v[80:83]\n"
    "v_pk_mul_f16 v63, v56, v59 op_sel_hi:[0,1]\n"
    "v_bfi_b32 v31, s57, v12, v31\n"
    "v_pk_maximum3_f16 v21, v32, v32, s50 op_sel_hi:[1,1,0] neg_lo:[0,1,0] neg_hi:[0,1,0]\n"
    "v_mfma_f32_16x16x32_f16 v[80:83], v[120:123], v[68:71], v[80:83]\n"
    "v_pk_mul_f16 v64, v56, v56 op_sel_hi:[0,1]\n"
    "v_pk_mad_u16 v21, v21, 8, s51 op_sel_hi:[1,0,0]\n"
    "v_mfma_f32_16x16x32_f16 v[84:87], v[124:127], v[68:71], v[84:87]\n"
    "v_pk_mul_f16 v65, v56, v49 op_sel_hi:[0,1]\n"
    "v_bfi_b32 v32, s57, v21, v32\n"
    "v_mfma_f32_16x16x32_f16 v[84:87], v[136:139], v[72:75], v[84:87]\n"
    "v_pk_mul_f16 v66, v48, v56 op_sel_hi:[0,1]\n"
    "v_pk_maximum3_f16 v12, v33, v33, s50 op_sel_hi:[1,1,0] neg_lo:[0,1,0] neg_hi:[0,1,0]\n"
    "v_pk_mad_u16 v12, v12, 8, s51 op_sel_hi:[1,0,0]\n"
    "v_smfmac_f32_16x16x64_f16 v[88:91], v[140:143], v[68:75], v2\n"
    "v_pk_mul_f16 v67, v48, v57 op_sel_hi:[0,1]\n"
    "v_bfi_b32 v33, s57, v12, v33\n"
    "v_mfma_f32_16x16x32_f16 v[76:79], v[144:147], v[60:63], v[76:79]\n"
    "v_pk_mul_f16 v68, v50, v56 op_sel_hi:[0,1]\n"
    "v_pk_mul_f16 v69, v50, v57 op_sel_hi:[0,1]\n"
    "v_pk_maximum3_f16 v21, v34, v34, s50 op_sel_hi:[1,1,0] neg_lo:[0,1,0] neg_hi:[0,1,0]\n"
    "v_mfma_f32_16x16x32_f16 v[76:79], v[156:159], v[64:67], v[76:79]\n"
    "v_pk_mul_f16 v70, v52, v56 op_sel_hi:[0,1]\n"
    "v_pk_mad_u16 v21, v21, 8, s51 op_sel_hi:[1,0,0]\n"
    "v_mfma_f32_16x16x32_f16 v[80:83], v[160:163], v[64:67], v[80:83]\n"
    "v_pk_mul_f16 v71, v52, v57 op_sel_hi:[0,1]\n"
    "v_bfi_b32 v34, s57, v21, v34\n"
    "v_pk_maximum3_f16 v12, v35, v35, s50 op_sel_hi:[1,1,0] neg_lo:[0,1,0] neg_hi:[0,1,0]\n"
    "v_mfma_f32_16x16x32_f16 v[80:83], v[148:151], v[60:63], v[80:83]\n"
    "v_pk_mul_f16 v72, v54, v56 op_sel_hi:[0,1]\n"
    "ds_read_b32 v22, v25 offset:528\n"
    "v_pk_mad_u16 v12, v12, 8, s51 op_sel_hi:[1,0,0]\n"
    "v_mfma_f32_16x16x32_f16 v[84:87], v[152:155], v[60:63], v[84:87]\n"
    "v_pk_mul_f16 v73, v54, v57 op_sel_hi:[0,1]\n"
    "v_bfi_b32 v35, s57, v12, v35\n"
    "v_mfma_f32_16x16x32_f16 v[84:87], v[164:167], v[64:67], v[84:87]\n"
    "v_pk_mul_f16 v74, v54, v54 op_sel_hi:[0,1]\n"
    "v_pk_maximum3_f16 v21, v36, v36, s50 op_sel_hi:[1,1,0] neg_lo:[0,1,0] neg_hi:[0,1,0]\n"
    "v_pk_mad_u16 v21, v21, 8, s51 op_sel_hi:[1,0,0]\n"
    "v_smfmac_f32_16x16x64_f16 v[88:91], v[168:171], v[60:67], v2\n"
    "v_pk_mul_f16 v75, v54, v49 op_sel_hi:[0,1]\n"
    "v_bfi_b32 v36, s57, v21, v36\n"
    "v_mfma_f32_16x16x32_f16 v[76:79], v[172:175], v[68:71], v[76:79]\n"
    "v_pk_mul_f16 v60, v48, v54 op_sel_hi:[0,1]\n"
    "v_pk_mul_f16 v61, v48, v55 op_sel_hi:[0,1]\n"
    "v_pk_maximum3_f16 v12, v37, v37, s50 op_sel_hi:[1,1,0] neg_lo:[0,1,0] neg_hi:[0,1,0]\n"
    "v_mfma_f32_16x16x32_f16 v[76:79], v[184:187], v[72:75], v[76:79]\n"
    "v_pk_mul_f16 v62, v50, v54 op_sel_hi:[0,1]\n"
    "v_pk_mad_u16 v12, v12, 8, s51 op_sel_hi:[1,0,0]\n"
    "v_mfma_f32_16x16x32_f16 v[80:83], v[188:191], v[72:75], v[80:83]\n"
    "v_pk_mul_f16 v63, v50, v55 op_sel_hi:[0,1]\n"
    "s_waitcnt lgkmcnt(0)\n"
    "v_readfirstlane_b32 s45, v22\n"
    "s_cmp_lt_i32 s45, 16\n"
    "s_cbranch_scc1 .Lxd_ck28\n"
    ".Lxd_ck28r:\n"
    "v_bfi_b32 v37, s57, v12, v37\n"
    "v_pk_maximum3_f16 v21, v38, v38, s50 op_sel_hi:[1,1,0] neg_lo:[0,1,0] neg_hi:[0,1,0]\n"
    "v_mfma_f32_16x16x32_f16 v[80:83], v[176:179], v[68:71], v[80:83]\n"
    "v_pk_mul_f16 v64, v52, v54 op_sel_hi:[0,1]\n"
    "ds_read_b64_tr_b16 v[44:45], v5 offset:0\n"
    "s_and_b32 s40, s45, 15\n"
    "s_cbranch_scc1 .Lxd_dr29\n"
    "s_cmp_lt_u32 s55, 32\n"
    "s_cselect_b32 s44, 0, -1\n"
    "s_add_u32 s55, s55, 4\n"
    "v_add_u32_e64 v24, s44, 1\n"
    "s_mov_b64 exec, 1\n"
    "global_atomic_add v24, v17, v24, s[52:53] sc0 sc1\n"
    "s_mov_b64 exec, -1\n"
    ".Lxd_dr29r:\n"
    "v_pk_mad_u16 v21, v21, 8, s51 op_sel_hi:[1,0,0]\n"
    "v_mfma_f32_16x16x32_f16 v[84:87], v[180:183], v[68:71], v[84:87]\n"
    "v_pk_mul_f16 v65, v52, v55 op_sel_hi:[0,1]\n"
    "ds_read_b64_tr_b16 v[46:47], v6 offset:0\n"
    "v_bfi_b32 v38, s57, v21, v38\n"
    "v_mfma_f32_16x16x32_f16 v[84:87], v[192:195], v[72:75], v[84:87]\n"
    "v_pk_mul_f16 v66, v52, v52 op_sel_hi:[0,1]\n"
    "ds_read_b64_tr_b16 v[58:59], v4 offset:2880\n"
    "v_pk_maximum3_f16 v12, v39, v39, s50 op_sel_hi:[1,1,0] neg_lo:[0,1,0] neg_hi:[0,1,0]\n"
    "v_pk_mad_u16 v12, v12, 8, s51 op_sel_hi:[1,0,0]\n"
    "s_cbranch_vccnz .Lxd_pr27\n"
    "s_setprio 0\n"
    ".Lxd_pr27:\n"
    "v_smfmac_f32_16x16x64_f16 v[88:91], v[196:199], v[68:75], v2\n"
    "v_pk_mul_f16 v67, v52, v49 op_sel_hi:[0,1]\n"
    "ds_read_b64_tr_b16 v[56:57], v4 offset:2304\n"
    "v_bfi_b32 v39, s57, v12, v39\n"
    "v_mfma_f32_16x16x32_f16 v[76:79], v[200:203], v[60:63], v[76:79]\n"
    "v_pk_mul_f16 v68, v48, v52 op_sel_hi:[0,1]\n"
    "v_pk_mul_f16 v69, v48, v53 op_sel_hi:[0,1]\n"
    "v_pk_maximum3_f16 v21, v40, v40, s50 op_sel_hi:[1,1,0] neg_lo:[0,1,0] neg_hi:[0,1,0]\n"
    "v_mfma_f32_16x16x32_f16 v[76:79], v[212:215], v[64:67], v[76:79]\n"
    "v_pk_mul_f16 v70, v50, v52 op_sel_hi:[0,1]\n"
    "ds_read_b64_tr_b16 v[54:55], v4 offset:1728\n"
    "v_pk_mad_u16 v21, v21, 8, s51 op_sel_hi:[1,0,0]\n"
    "v_mfma_f32_16x16x32_f16 v[80:83], v[216:219], v[64:67], v[80:83]\n"
    "v_pk_mul_f16 v71, v50, v53 op_sel_hi:[0,1]\n"
    "v_bfi_b32 v40, s57, v21, v40\n"
    "s_sub_u32 s43, 4112, s47\n"
    "s_lshl_b32 s43, s43, 19\n"
    "v_cvt_pkrtz_f16_f32 v12, s43, s43\n"
    "v_mfma_f32_16x16x32_f16 v[80:83], v[204:207], v[60:63], v[80:83]\n"
    "v_pk_mul_f16 v72, v50, v50 op_sel_hi:[0,1]\n"
    "v_lshl_or_b32 v21, v12, 16, s47\n"
    "v_mfma_f32_16x16x32_f16 v[84:87], v[208:211], v[60:63], v[84:87]\n"
    "v_pk_mul_f16 v73, v50, v49 op_sel_hi:[0,1]\n"
    "v_pk_mul_f16 v74, v48, v50 op_sel_hi:[0,1]\n"
    "ds_write_b128 v15, v[28:31] offset:0\n"
    "ds_write_b128 v15, v[32:35] offset:144\n"
    "ds_write_b128 v15, v[36:39] offset:288\n"
    "v_mfma_f32_16x16x32_f16 v[84:87], v[220:223], v[64:67], v[84:87]\n"
    "v_pk_mul_f16 v75, v48, v51 op_sel_hi:[0,1]\n"
    "ds_write2_b32 v16, v40, v12 offset1:72\n"
    "s_mov_b64 exec, 1\n"
    "ds_or_b32 v25, v21 offset:532\n"
    "s_mov_b64 exec, -1\n"
    "v_smfmac_f32_16x16x64_f16 v[88:91], v[224:227], v[60:67], v2\n"
    "ds_read_b64_tr_b16 v[48:49], v3 offset:0\n"
    "ds_read_b64_tr_b16 v[50:51], v4 offset:576\n"
    "ds_read_b64_tr_b16 v[52:53], v4 offset:1152\n"
    "v_smfmac_f32_16x16x64_f16 v[88:91], v[252:255], v[68:75], v2\n"
    "s_waitcnt lgkmcnt(11)\n"
    "v_pk_mul_f16 v60, v44, s45 op_sel:[0,1]\n"
    "v_pk_mul_f16 v61, v45, s45 op_sel:[0,1]\n"
    "v_mfma_f32_16x16x32_f16 v[76:79], v[228:231], v[68:71], v[76:79]\n"
    "v_pk_mul_f16 v62, v46, s45 op_sel:[0,1]\n"
    "v_pk_mul_f16 v63, v47, s45 op_sel:[0,1]\n"
    "v_mfma_f32_16x16x32_f16 v[76:79], v[240:243], v[72:75], v[76:79]\n"
    "v_pk_mul_f16 v64, v44, v46\n"
    "v_pk_mul_f16 v65, v45, v47\n"
    "v_mfma_f32_16x16x32_f16 v[80:83], v[244:247], v[72:75], v[80:83]\n"
    "v_add_f32 v11, v88, v89\n"
    "v_add_f32 v13, v90, v91\n"
    "v_mfma_f32_16x16x32_f16 v[80:83], v[232:235], v[68:71], v[80:83]\n"
    "v_mov_b64 v[88:89], 0\n"
    "v_mov_b64 v[90:91], 0\n"
    "v_mfma_f32_16x16x32_f16 v[84:87], v[236:239], v[68:71], v[84:87]\n"
    "s_waitcnt lgkmcnt(0)\n"
    "v_pk_mul_f16 v66, v48, v48 op_sel_hi:[0,1]\n"
    "v_pk_mul_f16 v67, v48, v49 op_sel_hi:[0,1]\n"
    "v_mfma_f32_16x16x32_f16 v[84:87], v[248:251], v[72:75], v[84:87]\n"
    "v_mul_f32 v11, v11, v13\n"
    "v_add_u32 v25, 16, v25\n"
    "s_cmp_eq_u32 s36, 0\n"
    "s_cbranch_scc1 .Lxd_ph0\n"
    ".Lxd_pl0:\n"
    "s_cbranch_vccnz .Lxd_pr30\n"
    "s_setprio 2\n"
    ".Lxd_pr30:\n"
    "v_smfmac_f32_16x16x64_f16 v[88:91], v[112:115], v[60:67], v2\n"
    "v_fmac_f32 v11, v76, v80\n"
    "v_fmac_f32 v11, v77, v81\n"
    "v_fmac_f32 v11, v78, v82\n"
    "v_fmac_f32 v11, v79, v83\n"
    "v_mfma_f32_16x16x32_f16 v[80:83], v[92:95], v[60:63], 0\n"
    "v_pk_mul_f16 v68, v58, v58 op_sel_hi:[0,1]\n"
    "v_pk_mul_f16 v69, v58, v49 op_sel_hi:[0,1]\n"
    "v_mfma_f32_16x16x32_f16 v[80:83], v[104:107], v[64:67], v[80:83]\n"
    "v_pk_mul_f16 v70, v48, v58 op_sel_hi:[0,1]\n"
    "v_pk_mul_f16 v71, v48, v59 op_sel_hi:[0,1]\n"
    "v_mfma_f32_16x16x32_f16 v[76:79], v[100:103], v[64:67], 0\n"
    "v_fmac_f32 v11, v84, v85\n"
    "v_fma_f32 v10, v86, v87, v11\n"
    "v_mfma_f32_16x16x32_f16 v[84:87], v[108:111], v[64:67], 0\n"
    "v_pk_mul_f16 v72, v50, v58 op_sel_hi:[0,1]\n"
    "v_pk_mul_f16 v73, v50, v59 op_sel_hi:[0,1]\n"
    "v_mfma_f32_16x16x32_f16 v[84:87], v[96:99], v[60:63], v[84:87]\n"
    "v_pk_mul_f16 v74, v52, v58 op_sel_hi:[0,1]\n"
    "v_pk_mul_f16 v75, v52, v59 op_sel_hi:[0,1]\n"
    "v_permlane32_swap_b32 v7, v8\n"
    "v_permlane32_swap_b32 v9, v10\n"
    "v_mfma_f32_16x16x32_f16 v[76:79], v[116:119], v[68:71], v[76:79]\n"
    "v_pk_mul_f16 v60, v54, v58 op_sel_hi:[0,1]\n"
    "v_pk_mul_f16 v61, v54, v59 op_sel_hi:[0,1]\n"
    "v_mfma_f32_16x16x32_f16 v[76:79], v[128:131], v[72:75], v[76:79]\n"
    "v_pk_mul_f16 v62, v56, v58 op_sel_hi:[0,1]\n"
    "v_mfma_f32_16x16x32_f16 v[80:83], v[132:135], v[72:75], v[80:83]\n"
    "v_pk_mul_f16 v63, v56, v59 op_sel_hi:[0,1]\n"
    "v_mfma_f32_16x16x32_f16 v[80:83], v[120:123], v[68:71], v[80:83]\n"
    "v_pk_mul_f16 v64, v56, v56 op_sel_hi:[0,1]\n"
    "v_mul_f32 v7, v0, v7\n"
    "v_mul_f32 v9, v0, v9\n"
    "v_mfma_f32_16x16x32_f16 v[84:87], v[124:127], v[68:71], v[84:87]\n"
    "v_pk_mul_f16 v65, v56, v49 op_sel_hi:[0,1]\n"
    "v_fmac_f32 v7, v23, v8\n"
    "v_fmac_f32 v9, v23, v10\n"
    "v_mfma_f32_16x16x32_f16 v[84:87], v[136:139], v[72:75], v[84:87]\n"
    "v_pk_mul_f16 v66, v48, v56 op_sel_hi:[0,1]\n"
    "v_permlane16_swap_b32 v7, v9\n"
    "v_smfmac_f32_16x16x64_f16 v[88:91], v[140:143], v[68:75], v2\n"
    "v_pk_mul_f16 v67, v48, v57 op_sel_hi:[0,1]\n"
    "v_add_f32 v7, v7, v9\n"
    "v_mfma_f32_16x16x32_f16 v[76:79], v[144:147], v[60:63], v[76:79]\n"
    "v_pk_mul_f16 v68, v50, v56 op_sel_hi:[0,1]\n"
    "v_pk_mul_f16 v69, v50, v57 op_sel_hi:[0,1]\n"
    "v_mfma_f32_16x16x32_f16 v[76:79], v[156:159], v[64:67], v[76:79]\n"
    "v_pk_mul_f16 v70, v52, v56 op_sel_hi:[0,1]\n"
    "v_add_f32_dpp v7, v7, v7 quad_perm:[1,0,3,2] row_mask:0xf bank_mask:0xf\n"
    "v_mfma_f32_16x16x32_f16 v[80:83], v[160:163], v[64:67], v[80:83]\n"
    "v_pk_mul_f16 v71, v52, v57 op_sel_hi:[0,1]\n"
    "v_add_f32_dpp v7, v7, v7 quad_perm:[2,3,0,1] row_mask:0xf bank_mask:0xf\n"
    "v_mfma_f32_16x16x32_f16 v[80:83], v[148:151], v[60:63], v[80:83]\n"
    "v_pk_mul_f16 v72, v54, v56 op_sel_hi:[0,1]\n"
    "ds_read_b32 v22, v25 offset:516\n"
    "v_mfma_f32_16x16x32_f16 v[84:87], v[152:155], v[60:63], v[84:87]\n"
    "v_pk_mul_f16 v73, v54, v57 op_sel_hi:[0,1]\n"
    "v_add_f32_dpp v7, v7, v7 row_half_mirror row_mask:0xf bank_mask:0xf\n"
    "v_mfma_f32_16x16x32_f16 v[84:87], v[164:167], v[64:67], v[84:87]\n"
    "v_pk_mul_f16 v74, v54, v54 op_sel_hi:[0,1]\n"
    "v_add_f32_dpp v7, v7, v7 row_mirror row_mask:0xf bank_mask:0xf\n"
    "v_smfmac_f32_16x16x64_f16 v[88:91], v[168:171], v[60:67], v2\n"
    "v_pk_mul_f16 v75, v54, v49 op_sel_hi:[0,1]\n"
    "ds_write_b32 v14, v7\n"
    "v_add_u32 v14, 64, v14\n"
    "v_mfma_f32_16x16x32_f16 v[76:79], v[172:175], v[68:71], v[76:79]\n"
    "v_pk_mul_f16 v60, v48, v54 op_sel_hi:[0,1]\n"
    "v_pk_mul_f16 v61, v48, v55 op_sel_hi:[0,1]\n"
    "v_mfma_f32_16x16x32_f16 v[76:79], v[184:187], v[72:75], v[76:79]\n"
    "v_pk_mul_f16 v62, v50, v54 op_sel_hi:[0,1]\n"
    "v_mfma_f32_16x16x32_f16 v[80:83], v[188:191], v[72:75], v[80:83]\n"
    "v_pk_mul_f16 v63, v50, v55 op_sel_hi:[0,1]\n"
    "s_waitcnt lgkmcnt(1)\n"
    "v_readfirstlane_b32 s45, v22\n"
    "s_cmp_lt_i32 s45, 16\n"
    "s_cbranch_scc1 .Lxd_ck32\n"
    ".Lxd_ck32r:\n"
    "v_mfma_f32_16x16x32_f16 v[80:83], v[176:179], v[68:71], v[80:83]\n"
    "v_pk_mul_f16 v64, v52, v54 op_sel_hi:[0,1]\n"
    "ds_read_b64_tr_b16 v[44:45], v5 offset:4096\n"
    "v_mfma_f32_16x16x32_f16 v[84:87], v[180:183], v[68:71], v[84:87]\n"
    "v_pk_mul_f16 v65, v52, v55 op_sel_hi:[0,1]\n"
    "ds_read_b64_tr_b16 v[46:47], v6 offset:4096\n"
    "v_mfma_f32_16x16x32_f16 v[84:87], v[192:195], v[72:75], v[84:87]\n"
    "v_pk_mul_f16 v66, v52, v52 op_sel_hi:[0,1]\n"
    "ds_read_b64_tr_b16 v[58:59], v4 offset:6976\n"
    "s_cbranch_vccnz .Lxd_pr31\n"
    "s_setprio 0\n"
    ".Lxd_pr31:\n"
    "v_smfmac_f32_16x16x64_f16 v[88:91], v[196:199], v[68:75], v2\n"
    "v_pk_mul_f16 v67, v52, v49 op_sel_hi:[0,1]\n"
    "ds_read_b64_tr_b16 v[56:57], v4 offset:6400\n"
    "v_mfma_f32_16x16x32_f16 v[76:79], v[200:203], v[60:63], v[76:79]\n"
    "v_pk_mul_f16 v68, v48, v52 op_sel_hi:[0,1]\n"
    "v_pk_mul_f16 v69, v48, v53 op_sel_hi:[0,1]\n"
    "v_mfma_f32_16x16x32_f16 v[76:79], v[212:215], v[64:67], v[76:79]\n"
    "v_pk_mul_f16 v70, v50, v52 op_sel_hi:[0,1]\n"
    "ds_read_b64_tr_b16 v[54:55], v4 offset:5824\n"
    "v_mfma_f32_16x16x32_f16 v[80:83], v[216:219], v[64:67], v[80:83]\n"
    "v_pk_mul_f16 v71, v50, v53 op_sel_hi:[0,1]\n"
    "v_mfma_f32_16x16x32_f16 v[80:83], v[204:207], v[60:63], v[80:83]\n"
    "v_pk_mul_f16 v72, v50, v50 op_sel_hi:[0,1]\n"
    "v_mfma_f32_16x16x32_f16 v[84:87], v[208:211], v[60:63], v[84:87]\n"
    "v_pk_mul_f16 v73, v50, v49 op_sel_hi:[0,1]\n"
    "v_pk_mul_f16 v74, v48, v50 op_sel_hi:[0,1]\n"
    "v_mfma_f32_16x16x32_f16 v[84:87], v[220:223], v[64:67], v[84:87]\n"
    "v_pk_mul_f16 v75, v48, v51 op_sel_hi:[0,1]\n"
    "v_smfmac_f32_16x16x64_f16 v[88:91], v[224:227], v[60:67], v2\n"
    "ds_read_b64_tr_b16 v[48:49], v3 offset:4096\n"
    "ds_read_b64_tr_b16 v[50:51], v4 offset:4672\n"
    "ds_read_b64_tr_b16 v[52:53], v4 offset:5248\n"
    "v_smfmac_f32_16x16x64_f16 v[88:91], v[252:255], v[68:75], v2\n"
    "s_waitcnt lgkmcnt(6)\n"
    "v_pk_mul_f16 v60, v44, s45 op_sel:[0,1]\n"
    "v_pk_mul_f16 v61, v45, s45 op_sel:[0,1]\n"
    "v_mfma_f32_16x16x32_f16 v[76:79], v[228:231], v[68:71], v[76:79]\n"
    "v_pk_mul_f16 v62, v46, s45 op_sel:[0,1]\n"
    "v_pk_mul_f16 v63, v47, s45 op_sel:[0,1]\n"
    "v_mfma_f32_16x16x32_f16 v[76:79], v[240:243], v[72:75], v[76:79]\n"
    "v_pk_mul_f16 v64, v44, v46\n"
    "v_pk_mul_f16 v65, v45, v47\n"
    "v_mfma_f32_16x16x32_f16 v[80:83], v[244:247], v[72:75], v[80:83]\n"
    "v_add_f32 v11, v88, v89\n"
    "v_add_f32 v13, v90, v91\n"
    "v_mfma_f32_16x16x32_f16 v[80:83], v[232:235], v[68:71], v[80:83]\n"
    "v_mov_b64 v[88:89], 0\n"
    "v_mov_b64 v[90:91], 0\n"
    "v_mfma_f32_16x16x32_f16 v[84:87], v[236:239], v[68:71], v[84:87]\n"
    "s_waitcnt lgkmcnt(0)\n"
    "v_pk_mul_f16 v66, v48, v48 op_sel_hi:[0,1]\n"
    "v_pk_mul_f16 v67, v48, v49 op_sel_hi:[0,1]\n"
    "v_mfma_f32_16x16x32_f16 v[84:87], v[248:251], v[72:75], v[84:87]\n"
    "v_mul_f32 v11, v11, v13\n"
    ".Lxd_pl1:\n"
    "s_cbranch_vccnz .Lxd_pr33\n"
    "s_setprio 2\n"
    ".Lxd_pr33:\n"
    "v_smfmac_f32_16x16x64_f16 v[88:91], v[112:115], v[60:67], v2\n"
    "v_fmac_f32 v11, v76, v80\n"
    "v_fmac_f32 v11, v77, v81\n"
    "v_fmac_f32 v11, v78, v82\n"
    "v_fmac_f32 v11, v79, v83\n"
    "v_mfma_f32_16x16x32_f16 v[80:83], v[92:95], v[60:63], 0\n"
    "v_pk_mul_f16 v68, v58, v58 op_sel_hi:[0,1]\n"
    "v_pk_mul_f16 v69, v58, v49 op_sel_hi:[0,1]\n"
    "v_mfma_f32_16x16x32_f16 v[80:83], v[104:107], v[64:67], v[80:83]\n"
    "v_pk_mul_f16 v70, v48, v58 op_sel_hi:[0,1]\n"
    "v_pk_mul_f16 v71, v48, v59 op_sel_hi:[0,1]\n"
    "v_mfma_f32_16x16x32_f16 v[76:79], v[100:103], v[64:67], 0\n"
    "v_fmac_f32 v11, v84, v85\n"
    "v_fma_f32 v7, v86, v87, v11\n"
    "v_mfma_f32_16x16x32_f16 v[84:87], v[108:111], v[64:67], 0\n"
    "v_pk_mul_f16 v72, v50, v58 op_sel_hi:[0,1]\n"
    "v_pk_mul_f16 v73, v50, v59 op_sel_hi:[0,1]\n"
    "v_mfma_f32_16x16x32_f16 v[84:87], v[96:99], v[60:63], v[84:87]\n"
    "v_pk_mul_f16 v74, v52, v58 op_sel_hi:[0,1]\n"
    "v_pk_mul_f16 v75, v52, v59 op_sel_hi:[0,1]\n"
    "v_mfma_f32_16x16x32_f16 v[76:79], v[116:119], v[68:71], v[76:79]\n"
    "v_pk_mul_f16 v60, v54, v58 op_sel_hi:[0,1]\n"
    "v_pk_mul_f16 v61, v54, v59 op_sel_hi:[0,1]\n"
    "v_mfma_f32_16x16x32_f16 v[76:79], v[128:131], v[72:75], v[76:79]\n"
    "v_pk_mul_f16 v62, v56, v58 op_sel_hi:[0,1]\n"
    "v_mfma_f32_16x16x32_f16 v[80:83], v[132:135], v[72:75], v[80:83]\n"
    "v_pk_mul_f16 v63, v56, v59 op_sel_hi:[0,1]\n"
    "v_mfma_f32_16x16x32_f16 v[80:83], v[120:123], v[68:71], v[80:83]\n"
    "v_pk_mul_f16 v64, v56, v56 op_sel_hi:[0,1]\n"
    "v_mfma_f32_16x16x32_f16 v[84:87], v[124:127], v[68:71], v[84:87]\n"
    "v_pk_mul_f16 v65, v56, v49 op_sel_hi:[0,1]\n"
    "v_mfma_f32_16x16x32_f16 v[84:87], v[136:139], v[72:75], v[84:87]\n"
    "v_pk_mul_f16 v66, v48, v56 op_sel_hi:[0,1]\n"
    "v_smfmac_f32_16x16x64_f16 v[88:91], v[140:143], v[68:75], v2\n"
    "v_pk_mul_f16 v67, v48, v57 op_sel_hi:[0,1]\n"
    "v_mfma_f32_16x16x32_f16 v[76:79], v[144:147], v[60:63], v[76:79]\n"
    "v_pk_mul_f16 v68, v50, v56 op_sel_hi:[0,1]\n"
    "v_pk_mul_f16 v69, v50, v57 op_sel_hi:[0,1]\n"
    "v_mfma_f32_16x16x32_f16 v[76:79], v[156:159], v[64:67], v[76:79]\n"
    "v_pk_mul_f16 v70, v52, v56 op_sel_hi:[0,1]\n"
    "v_mfma_f32_16x16x32_f16 v[80:83], v[160:163], v[64:67], v[80:83]\n"
    "v_pk_mul_f16 v71, v52, v57 op_sel_hi:[0,1]\n"
    "v_mfma_f32_16x16x32_f16 v[80:83], v[148:151], v[60:63], v[80:83]\n"
    "v_pk_mul_f16 v72, v54, v56 op_sel_hi:[0,1]\n"
    "ds_read_b32 v22, v25 offset:520\n"
    "v_mfma_f32_16x16x32_f16 v[84:87], v[152:155], v[60:63], v[84:87]\n"
    "v_pk_mul_f16 v73, v54, v57 op_sel_hi:[0,1]\n"
    "v_mfma_f32_16x16x32_f16 v[84:87], v[164:167], v[64:67], v[84:87]\n"
    "v_pk_mul_f16 v74, v54, v54 op_sel_hi:[0,1]\n"
    "v_smfmac_f32_16x16x64_f16 v[88:91], v[168:171], v[60:67], v2\n"
    "v_pk_mul_f16 v75, v54, v49 op_sel_hi:[0,1]\n"
    "v_mfma_f32_16x16x32_f16 v[76:79], v[172:175], v[68:71], v[76:79]\n"
    "v_pk_mul_f16 v60, v48, v54 op_sel_hi:[0,1]\n"
    "v_pk_mul_f16 v61, v48, v55 op_sel_hi:[0,1]\n"
    "v_mfma_f32_16x16x32_f16 v[76:79], v[184:187], v[72:75], v[76:79]\n"
    "v_pk_mul_f16 v62, v50, v54 op_sel_hi:[0,1]\n"
    "v_mfma_f32_16x16x32_f16 v[80:83], v[188:191], v[72:75], v[80:83]\n"
    "v_pk_mul_f16 v63, v50, v55 op_sel_hi:[0,1]\n"
    "s_waitcnt lgkmcnt(0)\n"
    "v_readfirstlane_b32 s45, v22\n"
    "s_cmp_lt_i32 s45, 16\n"
    "s_cbranch_scc1 .Lxd_ck35\n"
    ".Lxd_ck35r:\n"
    "v_mfma_f32_16x16x32_f16 v[80:83], v[176:179], v[68:71], v[80:83]\n"
    "v_pk_mul_f16 v64, v52, v54 op_sel_hi:[0,1]\n"
    "ds_read_b64_tr_b16 v[44:45], v5 offset:8192\n"
    "v_mfma_f32_16x16x32_f16 v[84:87], v[180:183], v[68:71], v[84:87]\n"
    "v_pk_mul_f16 v65, v52, v55 op_sel_hi:[0,1]\n"
    "ds_read_b64_tr_b16 v[46:47], v6 offset:8192\n"
    "v_mfma_f32_16x16x32_f16 v[84:87], v[192:195], v[72:75], v[84:87]\n"
    "v_pk_mul_f16 v66, v52, v52 op_sel_hi:[0,1]\n"
    "ds_read_b64_tr_b16 v[58:59], v4 offset:11072\n"
    "s_cbranch_vccnz .Lxd_pr34\n"
    "s_setprio 0\n"
    ".Lxd_pr34:\n"
    "v_smfmac_f32_16x16x64_f16 v[88:91], v[196:199], v[68:75], v2\n"
    "v_pk_mul_f16 v67, v52, v49 op_sel_hi:[0,1]\n"
    "ds_read_b64_tr_b16 v[56:57], v4 offset:10496\n"
    "v_mfma_f32_16x16x32_f16 v[76:79], v[200:203], v[60:63], v[76:79]\n"
    "v_pk_mul_f16 v68, v48, v52 op_sel_hi:[0,1]\n"
    "v_pk_mul_f16 v69, v48, v53 op_sel_hi:[0,1]\n"
    "v_mfma_f32_16x16x32_f16 v[76:79], v[212:215], v[64:67], v[76:79]\n"
    "v_pk_mul_f16 v70, v50, v52 op_sel_hi:[0,1]\n"
    "ds_read_b64_tr_b16 v[54:55], v4 offset:9920\n"
    "v_mfma_f32_16x16x32_f16 v[80:83], v[216:219], v[64:67], v[80:83]\n"
    "v_pk_mul_f16 v71, v50, v53 op_sel_hi:[0,1]\n"
    "v_mfma_f32_16x16x32_f16 v[80:83], v[204:207], v[60:63], v[80:83]\n"
    "v_pk_mul_f16 v72, v50, v50 op_sel_hi:[0,1]\n"
    "v_mfma_f32_16x16x32_f16 v[84:87], v[208:211], v[60:63], v[84:87]\n"
    "v_pk_mul_f16 v73, v50, v49 op_sel_hi:[0,1]\n"
    "v_pk_mul_f16 v74, v48, v50 op_sel_hi:[0,1]\n"
    "v_mfma_f32_16x16x32_f16 v[84:87], v[220:223], v[64:67], v[84:87]\n"
    "v_pk_mul_f16 v75, v48, v51 op_sel_hi:[0,1]\n"
    "v_smfmac_f32_16x16x64_f16 v[88:91], v[224:227], v[60:67], v2\n"
    "ds_read_b64_tr_b16 v[48:49], v3 offset:8192\n"
    "ds_read_b64_tr_b16 v[50:51], v4 offset:8768\n"
    "ds_read_b64_tr_b16 v[52:53], v4 offset:9344\n"
    "v_smfmac_f32_16x16x64_f16 v[88:91], v[252:255], v[68:75], v2\n"
    "s_waitcnt lgkmcnt(6)\n"
    "v_pk_mul_f16 v60, v44, s45 op_sel:[0,1]\n"
    "v_pk_mul_f16 v61, v45, s45 op_sel:[0,1]\n"
    "v_mfma_f32_16x16x32_f16 v[76:79], v[228:231], v[68:71], v[76:79]\n"
    "v_pk_mul_f16 v62, v46, s45 op_sel:[0,1]\n"
    "v_pk_mul_f16 v63, v47, s45 op_sel:[0,1]\n"
    "v_mfma_f32_16x16x32_f16 v[76:79], v[240:243], v[72:75], v[76:79]\n"
    "v_pk_mul_f16 v64, v44, v46\n"
    "v_pk_mul_f16 v65, v45, v47\n"
    "v_mfma_f32_16x16x32_f16 v[80:83], v[244:247], v[72:75], v[80:83]\n"
    "v_add_f32 v11, v88, v89\n"
    "v_add_f32 v13, v90, v91\n"
    "v_mfma_f32_16x16x32_f16 v[80:83], v[232:235], v[68:71], v[80:83]\n"
    "v_mov_b64 v[88:89], 0\n"
    "v_mov_b64 v[90:91], 0\n"
    "v_mfma_f32_16x16x32_f16 v[84:87], v[236:239], v[68:71], v[84:87]\n"
    "s_waitcnt lgkmcnt(0)\n"
    "v_pk_mul_f16 v66, v48, v48 op_sel_hi:[0,1]\n"
    "v_pk_mul_f16 v67, v48, v49 op_sel_hi:[0,1]\n"
    "v_mfma_f32_16x16x32_f16 v[84:87], v[248:251], v[72:75], v[84:87]\n"
    "v_mul_f32 v11, v11, v13\n"
    ".Lxd_pl2:\n"
    "s_cbranch_vccnz .Lxd_pr36\n"
    "s_setprio 2\n"
    ".Lxd_pr36:\n"
    "v_smfmac_f32_16x16x64_f16 v[88:91], v[112:115], v[60:67], v2\n"
    "v_fmac_f32 v11, v76, v80\n"
    "v_fmac_f32 v11, v77, v81\n"
    "v_fmac_f32 v11, v78, v82\n"
    "v_fmac_f32 v11, v79, v83\n"
    "v_mfma_f32_16x16x32_f16 v[80:83], v[92:95], v[60:63], 0\n"
    "v_pk_mul_f16 v68, v58, v58 op_sel_hi:[0,1]\n"
    "v_pk_mul_f16 v69, v58, v49 op_sel_hi:[0,1]\n"
    "v_mfma_f32_16x16x32_f16 v[80:83], v[104:107], v[64:67], v[80:83]\n"
    "v_pk_mul_f16 v70, v48, v58 op_sel_hi:[0,1]\n"
    "v_pk_mul_f16 v71, v48, v59 op_sel_hi:[0,1]\n"
    "v_mfma_f32_16x16x32_f16 v[76:79], v[100:103], v[64:67], 0\n"
    "v_fmac_f32 v11, v84, v85\n"
    "v_fma_f32 v8, v86, v87, v11\n"
    "v_mfma_f32_16x16x32_f16 v[84:87], v[108:111], v[64:67], 0\n"
    "v_pk_mul_f16 v72, v50, v58 op_sel_hi:[0,1]\n"
    "v_pk_mul_f16 v73, v50, v59 op_sel_hi:[0,1]\n"
    "v_mfma_f32_16x16x32_f16 v[84:87], v[96:99], v[60:63], v[84:87]\n"
    "v_pk_mul_f16 v74, v52, v58 op_sel_hi:[0,1]\n"
    "v_pk_mul_f16 v75, v52, v59 op_sel_hi:[0,1]\n"
    "v_mfma_f32_16x16x32_f16 v[76:79], v[116:119], v[68:71], v[76:79]\n"
    "v_pk_mul_f16 v60, v54, v58 op_sel_hi:[0,1]\n"
    "v_pk_mul_f16 v61, v54, v59 op_sel_hi:[0,1]\n"
    "v_mfma_f32_16x16x32_f16 v[76:79], v[128:131], v[72:75], v[76:79]\n"
    "v_pk_mul_f16 v62, v56, v58 op_sel_hi:[0,1]\n"
    "v_mfma_f32_16x16x32_f16 v[80:83], v[132:135], v[72:75], v[80:83]\n"
    "v_pk_mul_f16 v63, v56, v59 op_sel_hi:[0,1]\n"
    "v_mfma_f32_16x16x32_f16 v[80:83], v[120:123], v[68:71], v[80:83]\n"
    "v_pk_mul_f16 v64, v56, v56 op_sel_hi:[0,1]\n"
    "v_mfma_f32_16x16x32_f16 v[84:87], v[124:127], v[68:71], v[84:87]\n"
    "v_pk_mul_f16 v65, v56, v49 op_sel_hi:[0,1]\n"
    "v_mfma_f32_16x16x32_f16 v[84:87], v[136:139], v[72:75], v[84:87]\n"
    "v_pk_mul_f16 v66, v48, v56 op_sel_hi:[0,1]\n"
    "v_smfmac_f32_16x16x64_f16 v[88:91], v[140:143], v[68:75], v2\n"
    "v_pk_mul_f16 v67, v48, v57 op_sel_hi:[0,1]\n"
    "s_cmp_lg_u32 s46, 0\n"
    "s_cbranch_scc1 .Lxd_ht38\n"
    ".Lxd_ht38r:\n"
    "v_mfma_f32_16x16x32_f16 v[76:79], v[144:147], v[60:63], v[76:79]\n"
    "v_pk_mul_f16 v68, v50, v56 op_sel_hi:[0,1]\n"
    "v_pk_mul_f16 v69, v50, v57 op_sel_hi:[0,1]\n"
    "v_mfma_f32_16x16x32_f16 v[76:79], v[156:159], v[64:67], v[76:79]\n"
    "v_pk_mul_f16 v70, v52, v56 op_sel_hi:[0,1]\n"
    "v_mfma_f32_16x16x32_f16 v[80:83], v[160:163], v[64:67], v[80:83]\n"
    "v_pk_mul_f16 v71, v52, v57 op_sel_hi:[0,1]\n"
    "v_mfma_f32_16x16x32_f16 v[80:83], v[148:151], v[60:63], v[80:83]\n"
    "v_pk_mul_f16 v72, v54, v56 op_sel_hi:[0,1]\n"
    "ds_read_b32 v22, v25 offset:524\n"
    "v_mfma_f32_16x16x32_f16 v[84:87], v[152:155], v[60:63], v[84:87]\n"
    "v_pk_mul_f16 v73, v54, v57 op_sel_hi:[0,1]\n"
    "v_mfma_f32_16x16x32_f16 v[84:87], v[164:167], v[64:67], v[84:87]\n"
    "v_pk_mul_f16 v74, v54, v54 op_sel_hi:[0,1]\n"
    "v_smfmac_f32_16x16x64_f16 v[88:91], v[168:171], v[60:67], v2\n"
    "v_pk_mul_f16 v75, v54, v49 op_sel_hi:[0,1]\n"
    "v_mfma_f32_16x16x32_f16 v[76:79], v[172:175], v[68:71], v[76:79]\n"
    "v_pk_mul_f16 v60, v48, v54 op_sel_hi:[0,1]\n"
    "v_pk_mul_f16 v61, v48, v55 op_sel_hi:[0,1]\n"
    "v_mfma_f32_16x16x32_f16 v[76:79], v[184:187], v[72:75], v[76:79]\n"
    "v_pk_mul_f16 v62, v50, v54 op_sel_hi:[0,1]\n"
    "v_mfma_f32_16x16x32_f16 v[80:83], v[188:191], v[72:75], v[80:83]\n"
    "v_pk_mul_f16 v63, v50, v55 op_sel_hi:[0,1]\n"
    "s_waitcnt lgkmcnt(0)\n"
    "v_readfirstlane_b32 s45, v22\n"
    "s_cmp_lt_i32 s45, 16\n"
    "s_cbranch_scc1 .Lxd_ck39\n"
    ".Lxd_ck39r:\n"
    "v_mfma_f32_16x16x32_f16 v[80:83], v[176:179], v[68:71], v[80:83]\n"
    "v_pk_mul_f16 v64, v52, v54 op_sel_hi:[0,1]\n"
    "ds_read_b64_tr_b16 v[44:45], v5 offset:12288\n"
    "v_mfma_f32_16x16x32_f16 v[84:87], v[180:183], v[68:71], v[84:87]\n"
    "v_pk_mul_f16 v65, v52, v55 op_sel_hi:[0,1]\n"
    "ds_read_b64_tr_b16 v[46:47], v6 offset:12288\n"
    "v_mfma_f32_16x16x32_f16 v[84:87], v[192:195], v[72:75], v[84:87]\n"
    "v_pk_mul_f16 v66, v52, v52 op_sel_hi:[0,1]\n"
    "ds_read_b64_tr_b16 v[58:59], v4 offset:15168\n"
    "s_cbranch_vccnz .Lxd_pr37\n"
    "s_setprio 0\n"
    ".Lxd_pr37:\n"
    "v_smfmac_f32_16x16x64_f16 v[88:91], v[196:199], v[68:75], v2\n"
    "v_pk_mul_f16 v67, v52, v49 op_sel_hi:[0,1]\n"
    "ds_read_b64_tr_b16 v[56:57], v4 offset:14592\n"
    "v_mfma_f32_16x16x32_f16 v[76:79], v[200:203], v[60:63], v[76:79]\n"
    "v_pk_mul_f16 v68, v48, v52 op_sel_hi:[0,1]\n"
    "v_pk_mul_f16 v69, v48, v53 op_sel_hi:[0,1]\n"
    "v_mfma_f32_16x16x32_f16 v[76:79], v[212:215], v[64:67], v[76:79]\n"
    "v_pk_mul_f16 v70, v50, v52 op_sel_hi:[0,1]\n"
    "ds_read_b64_tr_b16 v[54:55], v4 offset:14016\n"
    "v_mfma_f32_16x16x32_f16 v[80:83], v[216:219], v[64:67], v[80:83]\n"
    "v_pk_mul_f16 v71, v50, v53 op_sel_hi:[0,1]\n"
    "v_mfma_f32_16x16x32_f16 v[80:83], v[204:207], v[60:63], v[80:83]\n"
    "v_pk_mul_f16 v72, v50, v50 op_sel_hi:[0,1]\n"
    "v_mfma_f32_16x16x32_f16 v[84:87], v[208:211], v[60:63], v[84:87]\n"
    "v_pk_mul_f16 v73, v50, v49 op_sel_hi:[0,1]\n"
    "v_pk_mul_f16 v74, v48, v50 op_sel_hi:[0,1]\n"
    "v_mfma_f32_16x16x32_f16 v[84:87], v[220:223], v[64:67], v[84:87]\n"
    "v_pk_mul_f16 v75, v48, v51 op_sel_hi:[0,1]\n"
    "v_smfmac_f32_16x16x64_f16 v[88:91], v[224:227], v[60:67], v2\n"
    "ds_read_b64_tr_b16 v[48:49], v3 offset:12288\n"
    "ds_read_b64_tr_b16 v[50:51], v4 offset:12864\n"
    "ds_read_b64_tr_b16 v[52:53], v4 offset:13440\n"
    "v_smfmac_f32_16x16x64_f16 v[88:91], v[252:255], v[68:75], v2\n"
    "s_waitcnt lgkmcnt(6)\n"
    "v_pk_mul_f16 v60, v44, s45 op_sel:[0,1]\n"
    "v_pk_mul_f16 v61, v45, s45 op_sel:[0,1]\n"
    "v_mfma_f32_16x16x32_f16 v[76:79], v[228:231], v[68:71], v[76:79]\n"
    "v_pk_mul_f16 v62, v46, s45 op_sel:[0,1]\n"
    "v_pk_mul_f16 v63, v47, s45 op_sel:[0,1]\n"
    "v_mfma_f32_16x16x32_f16 v[76:79], v[240:243], v[72:75], v[76:79]\n"
    "v_pk_mul_f16 v64, v44, v46\n"
    "v_pk_mul_f16 v65, v45, v47\n"
    "v_mfma_f32_16x16x32_f16 v[80:83], v[244:247], v[72:75], v[80:83]\n"
    "v_add_f32 v11, v88, v89\n"
    "v_add_f32 v13, v90, v91\n"
    "v_mfma_f32_16x16x32_f16 v[80:83], v[232:235], v[68:71], v[80:83]\n"
    "v_mov_b64 v[88:89], 0\n"
    "v_mov_b64 v[90:91], 0\n"
    "v_mfma_f32_16x16x32_f16 v[84:87], v[236:239], v[68:71], v[84:87]\n"
    "s_waitcnt lgkmcnt(0)\n"
    "v_pk_mul_f16 v66, v48, v48 op_sel_hi:[0,1]\n"
    "v_pk_mul_f16 v67, v48, v49 op_sel_hi:[0,1]\n"
    "v_mfma_f32_16x16x32_f16 v[84:87], v[248:251], v[72:75], v[84:87]\n"
    "v_mul_f32 v11, v11, v13\n"
    ".Lxd_pl3:\n"
    "s_cbranch_vccnz .Lxd_pr40\n"
    "s_setprio 2\n"
    ".Lxd_pr40:\n"
    "v_smfmac_f32_16x16x64_f16 v[88:91], v[112:115], v[60:67], v2\n"
    "v_fmac_f32 v11, v76, v80\n"
    "v_fmac_f32 v11, v77, v81\n"
    "v_fmac_f32 v11, v78, v82\n"
    "v_fmac_f32 v11, v79, v83\n"
    "v_mfma_f32_16x16x32_f16 v[80:83], v[92:95], v[60:63], 0\n"
    "v_pk_mul_f16 v68, v58, v58 op_sel_hi:[0,1]\n"
    "v_pk_mul_f16 v69, v58, v49 op_sel_hi:[0,1]\n"
    "v_mfma_f32_16x16x32_f16 v[80:83], v[104:107], v[64:67], v[80:83]\n"
    "v_pk_mul_f16 v70, v48, v58 op_sel_hi:[0,1]\n"
    "v_pk_mul_f16 v71, v48, v59 op_sel_hi:[0,1]\n"
    "v_mfma_f32_16x16x32_f16 v[76:79], v[100:103], v[64:67], 0\n"
    "v_fmac_f32 v11, v84, v85\n"
    "v_fma_f32 v9, v86, v87, v11\n"
    "v_mfma_f32_16x16x32_f16 v[84:87], v[108:111], v[64:67], 0\n"
    "v_pk_mul_f16 v72, v50, v58 op_sel_hi:[0,1]\n"
    "v_pk_mul_f16 v73, v50, v59 op_sel_hi:[0,1]\n"
    "v_mfma_f32_16x16x32_f16 v[84:87], v[96:99], v[60:63], v[84:87]\n"
    "v_pk_mul_f16 v74, v52, v58 op_sel_hi:[0,1]\n"
    "v_pk_mul_f16 v75, v52, v59 op_sel_hi:[0,1]\n"
    "v_mfma_f32_16x16x32_f16 v[76:79], v[116:119], v[68:71], v[76:79]\n"
    "v_pk_mul_f16 v60, v54, v58 op_sel_hi:[0,1]\n"
    "v_pk_mul_f16 v61, v54, v59 op_sel_hi:[0,1]\n"
    "v_mfma_f32_16x16x32_f16 v[76:79], v[128:131], v[72:75], v[76:79]\n"
    "v_pk_mul_f16 v62, v56, v58 op_sel_hi:[0,1]\n"
    "s_cmp_eq_u32 s36, 0\n"
    "s_cbranch_scc1 .Lxd_on42\n"
    ".Lxd_on42r:\n"
    "v_mfma_f32_16x16x32_f16 v[80:83], v[132:135], v[72:75], v[80:83]\n"
    "v_pk_mul_f16 v63, v56, v59 op_sel_hi:[0,1]\n"
    "v_xor_b32 v3, 0x4000, v3\n"
    "v_mfma_f32_16x16x32_f16 v[80:83], v[120:123], v[68:71], v[80:83]\n"
    "v_pk_mul_f16 v64, v56, v56 op_sel_hi:[0,1]\n"
    "v_xor_b32 v4, 0x4000, v4\n"
    "v_mfma_f32_16x16x32_f16 v[84:87], v[124:127], v[68:71], v[84:87]\n"
    "v_pk_mul_f16 v65, v56, v49 op_sel_hi:[0,1]\n"
    "v_xor_b32 v5, 0x4000, v5\n"
    "v_mfma_f32_16x16x32_f16 v[84:87], v[136:139], v[72:75], v[84:87]\n"
    "v_pk_mul_f16 v66, v48, v56 op_sel_hi:[0,1]\n"
    "v_xor_b32 v6, 0x4000, v6\n"
    "v_smfmac_f32_16x16x64_f16 v[88:91], v[140:143], v[68:75], v2\n"
    "v_pk_mul_f16 v67, v48, v57 op_sel_hi:[0,1]\n"
    "v_mfma_f32_16x16x32_f16 v[76:79], v[144:147], v[60:63], v[76:79]\n"
    "v_pk_mul_f16 v68, v50, v56 op_sel_hi:[0,1]\n"
    "v_pk_mul_f16 v69, v50, v57 op_sel_hi:[0,1]\n"
    "v_mfma_f32_16x16x32_f16 v[76:79], v[156:159], v[64:67], v[76:79]\n"
    "v_pk_mul_f16 v70, v52, v56 op_sel_hi:[0,1]\n"
    "v_mfma_f32_16x16x32_f16 v[80:83], v[160:163], v[64:67], v[80:83]\n"
    "v_pk_mul_f16 v71, v52, v57 op_sel_hi:[0,1]\n"
    "v_mfma_f32_16x16x32_f16 v[80:83], v[148:151], v[60:63], v[80:83]\n"
    "v_pk_mul_f16 v72, v54, v56 op_sel_hi:[0,1]\n"
    "ds_read_b32 v22, v25 offset:528\n"
    "v_mfma_f32_16x16x32_f16 v[84:87], v[152:155], v[60:63], v[84:87]\n"
    "v_pk_mul_f16 v73, v54, v57 op_sel_hi:[0,1]\n"
    "v_mfma_f32_16x16x32_f16 v[84:87], v[164:167], v[64:67], v[84:87]\n"
    "v_pk_mul_f16 v74, v54, v54 op_sel_hi:[0,1]\n"
    "v_smfmac_f32_16x16x64_f16 v[88:91], v[168:171], v[60:67], v2\n"
    "v_pk_mul_f16 v75, v54, v49 op_sel_hi:[0,1]\n"
    "v_mfma_f32_16x16x32_f16 v[76:79], v[172:175], v[68:71], v[76:79]\n"
    "v_pk_mul_f16 v60, v48, v54 op_sel_hi:[0,1]\n"
    "v_pk_mul_f16 v61, v48, v55 op_sel_hi:[0,1]\n"
    "v_mfma_f32_16x16x32_f16 v[76:79], v[184:187], v[72:75], v[76:79]\n"
    "v_pk_mul_f16 v62, v50, v54 op_sel_hi:[0,1]\n"
    "v_mfma_f32_16x16x32_f16 v[80:83], v[188:191], v[72:75], v[80:83]\n"
    "v_pk_mul_f16 v63, v50, v55 op_sel_hi:[0,1]\n"
    "s_waitcnt lgkmcnt(0)\n"
    "v_readfirstlane_b32 s45, v22\n"
    "s_cmp_lt_i32 s45, 16\n"
    "s_cbranch_scc1 .Lxd_ck44\n"
    ".Lxd_ck44r:\n"
    "v_mfma_f32_16x16x32_f16 v[80:83], v[176:179], v[68:71], v[80:83]\n"
    "v_pk_mul_f16 v64, v52, v54 op_sel_hi:[0,1]\n"
    "ds_read_b64_tr_b16 v[44:45], v5 offset:0\n"
    "v_mfma_f32_16x16x32_f16 v[84:87], v[180:183], v[68:71], v[84:87]\n"
    "v_pk_mul_f16 v65, v52, v55 op_sel_hi:[0,1]\n"
    "ds_read_b64_tr_b16 v[46:47], v6 offset:0\n"
    "v_mfma_f32_16x16x32_f16 v[84:87], v[192:195], v[72:75], v[84:87]\n"
    "v_pk_mul_f16 v66, v52, v52 op_sel_hi:[0,1]\n"
    "ds_read_b64_tr_b16 v[58:59], v4 offset:2880\n"
    "s_cbranch_vccnz .Lxd_pr41\n"
    "s_setprio 0\n"
    ".Lxd_pr41:\n"
    "v_smfmac_f32_16x16x64_f16 v[88:91], v[196:199], v[68:75], v2\n"
    "v_pk_mul_f16 v67, v52, v49 op_sel_hi:[0,1]\n"
    "ds_read_b64_tr_b16 v[56:57], v4 offset:2304\n"
    "v_mfma_f32_16x16x32_f16 v[76:79], v[200:203], v[60:63], v[76:79]\n"
    "v_pk_mul_f16 v68, v48, v52 op_sel_hi:[0,1]\n"
    "v_pk_mul_f16 v69, v48, v53 op_sel_hi:[0,1]\n"
    "v_mfma_f32_16x16x32_f16 v[76:79], v[212:215], v[64:67], v[76:79]\n"
    "v_pk_mul_f16 v70, v50, v52 op_sel_hi:[0,1]\n"
    "ds_read_b64_tr_b16 v[54:55], v4 offset:1728\n"
    "v_mfma_f32_16x16x32_f16 v[80:83], v[216:219], v[64:67], v[80:83]\n"
    "v_pk_mul_f16 v71, v50, v53 op_sel_hi:[0,1]\n"
    "v_mfma_f32_16x16x32_f16 v[80:83], v[204:207], v[60:63], v[80:83]\n"
    "v_pk_mul_f16 v72, v50, v50 op_sel_hi:[0,1]\n"
    "v_mfma_f32_16x16x32_f16 v[84:87], v[208:211], v[60:63], v[84:87]\n"
    "v_pk_mul_f16 v73, v50, v49 op_sel_hi:[0,1]\n"
    "v_pk_mul_f16 v74, v48, v50 op_sel_hi:[0,1]\n"
    "v_mfma_f32_16x16x32_f16 v[84:87], v[220:223], v[64:67], v[84:87]\n"
    "v_pk_mul_f16 v75, v48, v51 op_sel_hi:[0,1]\n"
    "v_smfmac_f32_16x16x64_f16 v[88:91], v[224:227], v[60:67], v2\n"
    "ds_read_b64_tr_b16 v[48:49], v3 offset:0\n"
    "ds_read_b64_tr_b16 v[50:51], v4 offset:576\n"
    "ds_read_b64_tr_b16 v[52:53], v4 offset:1152\n"
    "v_smfmac_f32_16x16x64_f16 v[88:91], v[252:255], v[68:75], v2\n"
    "s_waitcnt lgkmcnt(6)\n"
    "v_pk_mul_f16 v60, v44, s45 op_sel:[0,1]\n"
    "v_pk_mul_f16 v61, v45, s45 op_sel:[0,1]\n"
    "v_mfma_f32_16x16x32_f16 v[76:79], v[228:231], v[68:71], v[76:79]\n"
    "v_pk_mul_f16 v62, v46, s45 op_sel:[0,1]\n"
    "v_pk_mul_f16 v63, v47, s45 op_sel:[0,1]\n"
    "v_mfma_f32_16x16x32_f16 v[76:79], v[240:243], v[72:75], v[76:79]\n"
    "v_pk_mul_f16 v64, v44, v46\n"
    "v_pk_mul_f16 v65, v45, v47\n"
    "v_mfma_f32_16x16x32_f16 v[80:83], v[244:247], v[72:75], v[80:83]\n"
    "v_add_f32 v11, v88, v89\n"
    "v_add_f32 v13, v90, v91\n"
    "v_mfma_f32_16x16x32_f16 v[80:83], v[232:235], v[68:71], v[80:83]\n"
    "v_mov_b64 v[88:89], 0\n"
    "v_mov_b64 v[90:91], 0\n"
    "v_mfma_f32_16x16x32_f16 v[84:87], v[236:239], v[68:71], v[84:87]\n"
    "s_waitcnt lgkmcnt(0)\n"
    "v_pk_mul_f16 v66, v48, v48 op_sel_hi:[0,1]\n"
    "v_pk_mul_f16 v67, v48, v49 op_sel_hi:[0,1]\n"
    "v_mfma_f32_16x16x32_f16 v[84:87], v[248:251], v[72:75], v[84:87]\n"
    "v_mul_f32 v11, v11, v13\n"
    "v_add_u32 v25, 16, v25\n"
    "s_cmp_eq_u32 s36, 0\n"
    "s_cbranch_scc1 .Lxd_ph0\n"
    "s_branch .Lxd_pl0\n"
    ".Lxd_last:\n"
    "s_cbranch_vccnz .Lxd_pr45\n"
    "s_setprio 0\n"
    ".Lxd_pr45:\n"
    "v_mfma_f32_16x16x32_f16 v[80:83], v[176:179], v[68:71], v[80:83]\n"
    "v_pk_mul_f16 v64, v52, v54 op_sel_hi:[0,1]\n"
    "v_mfma_f32_16x16x32_f16 v[84:87], v[180:183], v[68:71], v[84:87]\n"
    "v_pk_mul_f16 v65, v52, v55 op_sel_hi:[0,1]\n"
    "v_mfma_f32_16x16x32_f16 v[84:87], v[192:195], v[72:75], v[84:87]\n"
    "v_pk_mul_f16 v66, v52, v52 op_sel_hi:[0,1]\n"
    "v_smfmac_f32_16x16x64_f16 v[88:91], v[196:199], v[68:75], v2\n"
    "v_pk_mul_f16 v67, v52, v49 op_sel_hi:[0,1]\n"
    "v_mfma_f32_16x16x32_f16 v[76:79], v[200:203], v[60:63], v[76:79]\n"
    "v_pk_mul_f16 v68, v48, v52 op_sel_hi:[0,1]\n"
    "v_pk_mul_f16 v69, v48, v53 op_sel_hi:[0,1]\n"
    "v_mfma_f32_16x16x32_f16 v[76:79], v[212:215], v[64:67], v[76:79]\n"
    "v_pk_mul_f16 v70, v50, v52 op_sel_hi:[0,1]\n"
    "v_mfma_f32_16x16x32_f16 v[80:83], v[216:219], v[64:67], v[80:83]\n"
    "v_pk_mul_f16 v71, v50, v53 op_sel_hi:[0,1]\n"
    "v_mfma_f32_16x16x32_f16 v[80:83], v[204:207], v[60:63], v[80:83]\n"
    "v_pk_mul_f16 v72, v50, v50 op_sel_hi:[0,1]\n"
    "v_mfma_f32_16x16x32_f16 v[84:87], v[208:211], v[60:63], v[84:87]\n"
    "v_pk_mul_f16 v73, v50, v49 op_sel_hi:[0,1]\n"
    "v_pk_mul_f16 v74, v48, v50 op_sel_hi:[0,1]\n"
    "v_mfma_f32_16x16x32_f16 v[84:87], v[220:223], v[64:67], v[84:87]\n"
    "v_pk_mul_f16 v75, v48, v51 op_sel_hi:[0,1]\n"
    "v_smfmac_f32_16x16x64_f16 v[88:91], v[224:227], v[60:67], v2\n"
    "s_nop 0\n"
    "v_smfmac_f32_16x16x64_f16 v[88:91], v[252:255], v[68:75], v2\n"
    "v_mfma_f32_16x16x32_f16 v[76:79], v[228:231], v[68:71], v[76:79]\n"
    "v_mfma_f32_16x16x32_f16 v[76:79], v[240:243], v[72:75], v[76:79]\n"
    "v_mfma_f32_16x16x32_f16 v[80:83], v[244:247], v[72:75], v[80:83]\n"
    "v_mfma_f32_16x16x32_f16 v[80:83], v[232:235], v[68:71], v[80:83]\n"
    "v_mfma_f32_16x16x32_f16 v[84:87], v[236:239], v[68:71], v[84:87]\n"
    "v_mfma_f32_16x16x32_f16 v[84:87], v[248:251], v[72:75], v[84:87]\n"
    "s_nop 1\n"
    "v_add_f32 v11, v88, v89\n"
    "v_add_f32 v13, v90, v91\n"
    "v_mul_f32 v11, v11, v13\n"
    "s_nop 0\n"
    "v_fmac_f32 v11, v76, v80\n"
    "v_fmac_f32 v11, v77, v81\n"
    "v_fmac_f32 v11, v78, v82\n"
    "v_fmac_f32 v11, v79, v83\n"
    "v_fmac_f32 v11, v84, v85\n"
    "s_setpc_b64 s[60:61]\n"
    ".Lxd_fin:\n"
    "v_lshl_add_u32 v68, v18, 6, v20\n"
    "v_mov_b32 v67, 154144\n"
    "v_permlane32_swap_b32 v7, v8\n"
    "v_permlane32_swap_b32 v9, v10\n"
    "v_mul_f32 v7, v0, v7\n"
    "v_mul_f32 v9, v0, v9\n"
    "v_fmac_f32 v7, v23, v8\n"
    "v_fmac_f32 v9, v23, v10\n"
    "ds_read_b32 v67, v67\n"
    "v_lshlrev_b32 v69, 2, v68\n"
    "v_permlane16_swap_b32 v7, v9\n"
    "v_add_f32 v7, v7, v9\n"
    "v_add_u32 v68, 150528, v68\n"
    "v_add_u32 v69, 152128, v69\n"
    "v_add_f32_dpp v7, v7, v7 quad_perm:[1,0,3,2] row_mask:0xf bank_mask:0xf\n"
    "s_lshl_b32 s26, s26, 16\n"
    "s_nop 0\n"
    "v_add_f32_dpp v7, v7, v7 quad_perm:[2,3,0,1] row_mask:0xf bank_mask:0xf\n"
    "s_nop 1\n"
    "v_add_f32_dpp v7, v7, v7 row_half_mirror row_mask:0xf bank_mask:0xf\n"
    "s_nop 1\n"
    "v_add_f32_dpp v7, v7, v7 row_mirror row_mask:0xf bank_mask:0xf\n"
    "ds_write_b32 v14, v7\n"
    "s_cmp_lg_u32 s56, 2\n"
    "s_cbranch_scc1 .Lxd_f2\n"
    ".Lxd_f3:\n"
    "s_waitcnt vmcnt(0) lgkmcnt(0)\n"
    "s_barrier\n"
    "s_cmp_lg_u32 s20, 0\n"
    "s_cbranch_scc1 .Lxd_done\n"
    "ds_read_b128 v[60:63], v69\n"
    "ds_read_b32 v64, v68\n"
    "ds_read_u16 v65, v68 offset:512\n"
    "ds_read_b32 v66, v68 offset:1024\n"
    "s_waitcnt lgkmcnt(3)\n"
    "v_pk_add_f32 v[60:61], v[60:61], v[62:63]\n"
    "s_waitcnt lgkmcnt(0)\n"
    "v_cmp_lt_i32 vcc, 0, v64\n"
    "v_lshrrev_b32 v65, 4, v65\n"
    "v_add_f32 v60, v60, v61\n"
    "v_lshl_add_u32 v65, v65, 2, v67\n"
    "v_add_f32 v66, s26, v66\n"
    "v_ldexp_f32 v60, v60, v65\n"
    "s_mov_b64 exec, vcc\n"
    "v_add_f32 v60, v60, v66\n"
    "v_cvt_pk_bf16_f32 v60, v60, v60\n"
    "global_store_short v64, v60, s[70:71] offset:-2\n"
    "s_mov_b64 exec, -1\n"
    ".Lxd_done:\n"
    "s_endpgm\n"
    ".Lxd_cin:\n"
    "s_lshr_b32 s20, s43, 6\n"
    "v_and_b32 v1, 63, v0\n"
    "v_lshlrev_b32 v42, 4, v1\n"
    "s_and_b32 s40, s2, 3\n"
    "s_lshl2_add_u32 s23, s40, s20\n"
    "s_lshl1_add_u32 s41, s40, s20\n"
    "s_add_u32 s41, s41, 12\n"
    "s_cmp_ge_u32 s20, 4\n"
    "s_cselect_b32 s23, s41, s23\n"
    "s_add_u32 s41, s40, 24\n"
    "s_cmp_ge_u32 s20, 6\n"
    "s_cselect_b32 s23, s41, s23\n"
    "s_cselect_b32 s24, 1, 0\n"
    "s_min_u32 s34, s23, 25\n"
    "s_lshr_b32 s35, s2, 2\n"
    "v_and_b32 v190, 31, v0\n"
    "v_min_u32 v190, 25, v190\n"
    "v_mul_u32_u24 v190, 52, v190\n"
    "v_bfe_u32 v191, v0, 5, 1\n"
    "v_lshl_add_u32 v190, v191, 4, v190\n"
    "s_mul_i32 s40, s34, 1352\n"
    "s_xor_b32 s66, s20, 7\n"
    "s_or_b32 s66, s66, s2\n"
    "s_waitcnt lgkmcnt(0)\n"
    "s_load_dwordx4 s[68:71], s[0:1], 64\n"
    "s_add_u32 s38, s12, s40\n"
    "s_addc_u32 s39, s13, 0\n"
    "s_cmp_lg_u32 s66, 0\n"
    "s_cbranch_scc1 .Lxd_on46\n"
    "v_and_b32 v188, 31, v0\n"
    "v_lshlrev_b32 v188, 7, v188\n"
    "v_add_u32 v188, 32768, v188\n"
    "v_mov_b32 v176, 114\n"
    "global_store_dword v188, v176, s[16:17] sc1\n"
    "global_load_dwordx4 v[180:183], v42, s[16:17] sc1\n"
    ".Lxd_on46:\n"
    "global_load_dwordx4 v[80:83], v190, s[38:39] offset:0\n"
    "global_load_dwordx4 v[84:87], v190, s[38:39] offset:32\n"
    "s_getpc_b64 s[60:61]\n"
    "s_add_u32 s60, s60, xd_tab@rel32@lo+4\n"
    "s_addc_u32 s61, s61, xd_tab@rel32@hi+12\n"
    "v_lshl_add_u32 v188, s24, 10, v42\n"
    "s_lshl1_add_u32 s40, s24, s35\n"
    "v_lshl_add_u32 v189, s40, 11, v42\n"
    "s_load_dword s25, s[16:17], 0\n"
    "global_load_dwordx4 v[44:47], v188, s[60:61]\n"
    "global_load_dwordx4 v[48:51], v189, s[60:61] offset:2048\n"
    "global_load_dwordx4 v[52:55], v189, s[60:61] offset:3072\n"
    "s_setprio 0\n"
    "v_mov_b64 v[188:189], 0\n"
    "v_lshlrev_b32 v190, 3, v0\n"
    "v_lshrrev_b32 v186, 5, v1\n"
    "ds_write_b64 v190, v[188:189] offset:36864\n"
    "v_bfe_u32 v190, v0, 1, 4\n"
    "v_lshlrev_b32 v72, 2, v186\n"
    "v_sub_u32 v190, v190, v72\n"
    "v_and_b32 v72, 16, v42\n"
    "s_movk_i32 s40, 0x3f80\n"
    "v_lshlrev_b32_e64 v191, v72, s40\n"
    "v_cmp_eq_u32 s[60:61], 0, v190\n"
    "v_cmp_eq_u32 s[62:63], 1, v190\n"
    "v_cmp_eq_u32 s[64:65], 2, v190\n"
    "v_cmp_eq_u32 s[50:51], 3, v190\n"
    "v_cndmask_b32 v72, 0, v191, s[60:61]\n"
    "v_cndmask_b32 v73, 0, v191, s[62:63]\n"
    "v_cndmask_b32 v74, 0, v191, s[64:65]\n"
    "v_cndmask_b32 v75, 0, v191, s[50:51]\n"
    "v_cmp_eq_u32 s[60:61], 8, v190\n"
    "v_cmp_eq_u32 s[62:63], 9, v190\n"
    "v_cmp_eq_u32 s[64:65], 10, v190\n"
    "v_cmp_eq_u32 s[50:51], 11, v190\n"
    "v_cndmask_b32 v76, 0, v191, s[60:61]\n"
    "v_cndmask_b32 v77, 0, v191, s[62:63]\n"
    "v_cndmask_b32 v78, 0, v191, s[64:65]\n"
    "v_cndmask_b32 v79, 0, v191, s[50:51]\n"
    "s_lshl_b32 s30, s20, 12\n"
    "s_add_u32 s30, s30, 65536\n"
    "v_and_b32 v188, 31, v0\n"
    "v_mul_u32_u24 v192, 528, v186\n"
    "v_lshl_add_u32 v192, v188, 2, v192\n"
    "v_add_u32 v192, s30, v192\n"
    "s_lshl_b32 s40, s35, 6\n"
    "s_add_u32 s40, s40, s20\n"
    "s_lshl1_add_u32 s41, s20, s35\n"
    "s_add_u32 s41, s41, 120\n"
    "s_cmp_ge_u32 s20, 4\n"
    "s_cselect_b32 s40, s41, s40\n"
    "s_lshl1_add_u32 s41, s35, 384\n"
    "s_cmp_ge_u32 s20, 6\n"
    "s_cselect_b32 s40, s41, s40\n"
    "s_cselect_b32 s42, 1, 192\n"
    "s_and_b32 s41, s2, 3\n"
    "s_lshl2_add_u32 s40, s41, s40\n"
    "v_lshrrev_b32 v188, 3, v1\n"
    "v_mul_u32_u24 v188, 448, v188\n"
    "v_bfe_u32 v189, v0, 2, 1\n"
    "v_mad_u32_u24 v188, v189, s42, v188\n"
    "v_and_b32 v189, 48, v42\n"
    "v_add3_u32 v188, v188, v189, s40\n"
    "v_lshlrev_b32 v193, 4, v188\n"
    "v_mov_b32 v177, 0\n"
    "s_waitcnt vmcnt(0) lgkmcnt(0)\n"
    "s_cmp_lg_u32 s66, 0\n"
    "s_cbranch_scc1 .Lxd_on47\n"
    "s_mov_b32 s40, 0x9e3779b1\n"
    "v_mul_lo_u32 v144, v180, s40\n"
    "v_xor_b32 v145, 0xa5c30f96, v180\n"
    "v_alignbit_b32 v144, s25, v144, 24\n"
    "v_mul_lo_u32 v146, v181, s40\n"
    "v_xor_b32 v147, 0xa5c30f96, v181\n"
    "v_alignbit_b32 v146, s25, v146, 24\n"
    "v_mul_lo_u32 v148, v182, s40\n"
    "v_xor_b32 v149, 0xa5c30f96, v182\n"
    "v_alignbit_b32 v148, s25, v148, 24\n"
    "v_mul_lo_u32 v150, v183, s40\n"
    "v_xor_b32 v151, 0xa5c30f96, v183\n"
    "v_alignbit_b32 v150, s25, v150, 24\n"
    "v_lshlrev_b32 v180, 1, v42\n"
    "s_add_u32 s40, s16, 4096\n"
    "s_addc_u32 s41, s17, 0\n"
    "global_store_dwordx4 v180, v[144:147], s[40:41] sc1\n"
    "global_store_dwordx4 v180, v[148:151], s[40:41] offset:16 sc1\n"
    ".Lxd_on47:\n"
    "s_barrier\n"
    "s_branch .Lxd_bD\n"
    ".Lxd_s2:\n"
    "s_cmp_lt_u32 s2, 4\n"
    "s_cbranch_scc1 .Lxd_cin\n"
    ".Lxd_bR:\n"
    "s_lshr_b32 s20, s43, 6\n"
    "v_and_b32 v1, 63, v0\n"
    "v_lshlrev_b32 v42, 4, v1\n"
    "s_mul_i32 s40, s20, 4480\n"
    "s_min_u32 s40, s40, 30672\n"
    "s_mov_b32 m0, s40\n"
    "s_add_u32 s40, s40, 70304\n"
    "s_waitcnt lgkmcnt(0)\n"
    "s_load_dwordx4 s[68:71], s[0:1], 64\n"
    "s_add_u32 s38, s12, s40\n"
    "s_addc_u32 s39, s13, 0\n"
    "global_load_lds_dwordx4 v42, s[38:39] offset:0\n"
    "global_load_lds_dwordx4 v42, s[38:39] offset:1024\n"
    "global_load_lds_dwordx4 v42, s[38:39] offset:2048\n"
    "global_load_lds_dwordx4 v42, s[38:39] offset:3072\n"
    "global_load_lds_dwordx4 v42, s[38:39] offset:3456\n"
    "s_and_b32 s40, s2, 3\n"
    "s_lshl2_add_u32 s23, s40, s20\n"
    "s_lshl1_add_u32 s41, s40, s20\n"
    "s_add_u32 s41, s41, 12\n"
    "s_cmp_ge_u32 s20, 4\n"
    "s_cselect_b32 s23, s41, s23\n"
    "s_add_u32 s41, s40, 24\n"
    "s_cmp_ge_u32 s20, 6\n"
    "s_cselect_b32 s23, s41, s23\n"
    "s_cselect_b32 s24, 1, 0\n"
    "s_min_u32 s34, s23, 25\n"
    "s_lshr_b32 s35, s2, 2\n"
    "v_lshrrev_b32 v190, 2, v1\n"
    "v_and_b32 v188, 48, v42\n"
    "s_movk_i32 s42, 1352\n"
    "v_min_u32 v191, 9, v190\n"
    "v_mad_u32_u24 v190, v190, s42, v188\n"
    "v_mad_u32_u24 v191, v191, s42, v188\n"
    "v_add_u32 v191, 20608, v191\n"
    "s_mul_i32 s41, s34, 52\n"
    "s_add_u32 s41, s41, 35152\n"
    "s_cmp_lg_u32 s20, 7\n"
    "s_cbranch_scc1 .Lxd_on48\n"
    "v_min_u32 v188, 38, v1\n"
    "s_mov_b32 m0, 40960\n"
    "v_lshlrev_b32 v188, 2, v188\n"
    "global_load_lds_dword v188, s[14:15]\n"
    ".Lxd_on48:\n"
    "s_lshl_b32 s28, s20, 11\n"
    "s_add_u32 s28, s28, 49152\n"
    "s_mov_b32 m0, s28\n"
    "s_add_u32 s38, s12, s41\n"
    "s_addc_u32 s39, s13, 0\n"
    "global_load_lds_dwordx4 v190, s[38:39] offset:0\n"
    "global_load_lds_dwordx4 v191, s[38:39] offset:1024\n"
    "s_getpc_b64 s[60:61]\n"
    "s_add_u32 s60, s60, xd_tab@rel32@lo+4\n"
    "s_addc_u32 s61, s61, xd_tab@rel32@hi+12\n"
    "v_lshl_add_u32 v188, s24, 10, v42\n"
    "s_lshl1_add_u32 s40, s24, s35\n"
    "v_lshl_add_u32 v189, s40, 11, v42\n"
    "s_load_dword s25, s[16:17], 0\n"
    "global_load_dwordx4 v[44:47], v188, s[60:61]\n"
    "global_load_dwordx4 v[48:51], v189, s[60:61] offset:2048\n"
    "global_load_dwordx4 v[52:55], v189, s[60:61] offset:3072\n"
    "s_setprio 0\n"
    "v_mov_b64 v[188:189], 0\n"
    "v_lshlrev_b32 v190, 3, v0\n"
    "v_lshrrev_b32 v186, 5, v1\n"
    "ds_write_b64 v190, v[188:189] offset:36864\n"
    "v_lshrrev_b32 v184, 4, v1\n"
    "v_and_b32 v185, 15, v0\n"
    "v_lshrrev_b32 v190, 2, v185\n"
    "v_lshl_add_u32 v64, v184, 3, v190\n"
    "v_add_u32 v65, 4, v64\n"
    "s_lshr_b32 s21, s20, 2\n"
    "s_and_b32 s43, s20, 3\n"
    "s_mul_i32 s40, s21, 13\n"
    "s_lshl2_add_u32 s40, s43, s40\n"
    "v_and_b32 v191, 3, v0\n"
    "v_add_lshl_u32 v188, v191, s40, 3\n"
    "v_min_u32 v64, 25, v64\n"
    "v_mad_u32_u24 v64, v64, s42, v188\n"
    "v_min_u32 v65, 25, v65\n"
    "v_mad_u32_u24 v65, v65, s42, v188\n"
    "v_lshl_add_u32 v188, s43, 4, v185\n"
    "v_cmp_lt_u32 vcc, 25, v188\n"
    "v_cmp_lt_u32 s[60:61], 51, v188\n"
    "v_lshl_add_u32 v66, s21, 6, v188\n"
    "v_cndmask_b32_e64 v189, 0, 6, vcc\n"
    "v_add_lshl_u32 v66, v66, v189, 1\n"
    "v_mov_b32 v189, 0x40000\n"
    "v_cmp_gt_u32 vcc, 26, v1\n"
    "v_cndmask_b32 v66, v66, v189, s[60:61]\n"
    "v_lshlrev_b32 v67, 6, v1\n"
    "v_cndmask_b32 v67, v189, v67, vcc\n"
    "v_lshlrev_b32 v68, 4, v184\n"
    "v_add_u32 v68, 40960, v68\n"
    "v_lshlrev_b32 v69, 1, v1\n"
    "v_lshl_add_u32 v190, v186, 3, v190\n"
    "v_and_b32 v188, 1, v184\n"
    "v_lshl_add_u32 v188, v188, 2, v191\n"
    "v_cmp_eq_u32 vcc, 7, v188\n"
    "v_lshlrev_b32 v188, 3, v188\n"
    "v_lshl_add_u32 v70, v190, 6, v188\n"
    "v_add_u32 v71, s28, v70\n"
    "v_mov_b32 v188, 38912\n"
    "v_cndmask_b32 v71, v71, v188, vcc\n"
    "v_cmp_gt_u32 vcc, 3, v184\n"
    "s_waitcnt vmcnt(5) lgkmcnt(0)\n"
    "s_barrier\n"
    "ds_read2_b64 v[176:179], v68 offset0:13 offset1:14\n"
    "ds_read_u16 v68, v69 offset:41012\n"
    "ds_read_u16 v69, v69 offset:40960\n"
    "ds_read_b64_tr_b16 v[112:113], v64 offset:0\n"
    "ds_read_b64_tr_b16 v[114:115], v65 offset:0\n"
    "ds_read_b64_tr_b16 v[116:117], v64 offset:208\n"
    "ds_read_b64_tr_b16 v[118:119], v65 offset:208\n"
    "ds_read_b64_tr_b16 v[120:121], v64 offset:416\n"
    "ds_read_b64_tr_b16 v[122:123], v65 offset:416\n"
    "ds_read_b64_tr_b16 v[124:125], v64 offset:624\n"
    "ds_read_b64_tr_b16 v[126:127], v65 offset:624\n"
    "ds_read_b64_tr_b16 v[128:129], v64 offset:832\n"
    "ds_read_b64_tr_b16 v[130:131], v65 offset:832\n"
    "ds_read_b64_tr_b16 v[132:133], v64 offset:1040\n"
    "ds_read_b64_tr_b16 v[134:135], v65 offset:1040\n"
    "ds_read_b64_tr_b16 v[136:137], v64 offset:1248\n"
    "ds_read_b64_tr_b16 v[138:139], v65 offset:1248\n"
    "s_waitcnt lgkmcnt(14)\n"
    "v_cndmask_b32 v177, 0, v177, vcc\n"
    "v_cndmask_b32 v178, 0, v178, vcc\n"
    "v_cndmask_b32 v179, 0, v179, vcc\n"
    "ds_write_b16 v67, v68 offset:36920\n"
    "s_waitcnt lgkmcnt(13)\n"
    "v_mfma_f32_16x16x32_bf16 v[144:147], v[176:179], v[112:115], 0\n"
    "s_waitcnt lgkmcnt(11)\n"
    "v_mfma_f32_16x16x32_bf16 v[148:151], v[176:179], v[116:119], 0\n"
    "s_waitcnt lgkmcnt(9)\n"
    "v_mfma_f32_16x16x32_bf16 v[152:155], v[176:179], v[120:123], 0\n"
    "s_waitcnt lgkmcnt(7)\n"
    "v_mfma_f32_16x16x32_bf16 v[156:159], v[176:179], v[124:127], 0\n"
    "s_waitcnt lgkmcnt(5)\n"
    "v_mfma_f32_16x16x32_bf16 v[160:163], v[176:179], v[128:131], 0\n"
    "s_waitcnt lgkmcnt(3)\n"
    "v_mfma_f32_16x16x32_bf16 v[164:167], v[176:179], v[132:135], 0\n"
    "s_waitcnt lgkmcnt(1)\n"
    "v_mfma_f32_16x16x32_bf16 v[168:171], v[176:179], v[136:139], 0\n"
    "s_waitcnt vmcnt(3)\n"
    "ds_read_b64_tr_b16 v[80:81], v71 offset:0\n"
    "ds_read_b64_tr_b16 v[82:83], v71 offset:256\n"
    "ds_read_b64_tr_b16 v[84:85], v71 offset:1024\n"
    "ds_read_b64_tr_b16 v[86:87], v71 offset:1280\n"
    "v_cvt_pk_bf16_f32 v144, v144, v144\n"
    "ds_write_b16 v66, v144 offset:36864\n"
    "v_cvt_pk_bf16_f32 v148, v148, v148\n"
    "ds_write_b16 v66, v148 offset:37120\n"
    "v_cvt_pk_bf16_f32 v152, v152, v152\n"
    "ds_write_b16 v66, v152 offset:37376\n"
    "v_cvt_pk_bf16_f32 v156, v156, v156\n"
    "ds_write_b16 v66, v156 offset:37632\n"
    "v_cvt_pk_bf16_f32 v160, v160, v160\n"
    "ds_write_b16 v66, v160 offset:37888\n"
    "v_cvt_pk_bf16_f32 v164, v164, v164\n"
    "ds_write_b16 v66, v164 offset:38144\n"
    "s_cmp_lg_u32 s21, 0\n"
    "s_cbranch_scc1 .Lxd_on49\n"
    "v_cvt_pk_bf16_f32 v168, v168, v168\n"
    "ds_write_b16 v66, v168 offset:38400\n"
    ".Lxd_on49:\n"
    "v_lshlrev_b32 v177, 16, v69\n"
    "s_waitcnt lgkmcnt(0)\n"
    "s_barrier\n"
    "ds_read_b64_tr_b16 v[72:73], v70 offset:36864\n"
    "ds_read_b64_tr_b16 v[74:75], v70 offset:37120\n"
    "ds_read_b64_tr_b16 v[76:77], v70 offset:37888\n"
    "ds_read_b64_tr_b16 v[78:79], v70 offset:38144\n"
    "s_lshl_b32 s30, s20, 12\n"
    "s_add_u32 s30, s30, 65536\n"
    "v_and_b32 v188, 31, v0\n"
    "v_mul_u32_u24 v192, 528, v186\n"
    "v_lshl_add_u32 v192, v188, 2, v192\n"
    "v_add_u32 v192, s30, v192\n"
    "s_lshl_b32 s40, s35, 6\n"
    "s_add_u32 s40, s40, s20\n"
    "s_lshl1_add_u32 s41, s20, s35\n"
    "s_add_u32 s41, s41, 120\n"
    "s_cmp_ge_u32 s20, 4\n"
    "s_cselect_b32 s40, s41, s40\n"
    "s_lshl1_add_u32 s41, s35, 384\n"
    "s_cmp_ge_u32 s20, 6\n"
    "s_cselect_b32 s40, s41, s40\n"
    "s_cselect_b32 s42, 1, 192\n"
    "s_and_b32 s41, s2, 3\n"
    "s_lshl2_add_u32 s40, s41, s40\n"
    "v_lshrrev_b32 v188, 3, v1\n"
    "v_mul_u32_u24 v188, 448, v188\n"
    "v_bfe_u32 v189, v0, 2, 1\n"
    "v_mad_u32_u24 v188, v189, s42, v188\n"
    "v_and_b32 v189, 48, v42\n"
    "v_add3_u32 v188, v188, v189, s40\n"
    "v_lshlrev_b32 v193, 4, v188\n"
    "s_waitcnt vmcnt(0) lgkmcnt(0)\n"
    ".Lxd_bD:\n"
    "v_mfma_f32_32x32x16_bf16 v[96:111], v[72:75], v[80:83], 0\n"
    "v_mfma_f32_32x32x16_bf16 v[96:111], v[80:83], v[72:75], v[96:111]\n"
    "v_mfma_f32_32x32x16_bf16 v[96:111], v[76:79], v[84:87], v[96:111]\n"
    "v_mfma_f32_32x32x16_bf16 v[96:111], v[84:87], v[76:79], v[96:111]\n"
    "v_readlane_b32 s43, v177, s34\n"
    "v_mad_u32_u16 v64, v44, 4, s30 op_sel:[0,0,0,0]\n"
    "v_mad_u32_u16 v65, v44, 4, s30 op_sel:[1,0,0,0]\n"
    "v_mad_u32_u16 v66, v45, 4, s30 op_sel:[0,0,0,0]\n"
    "v_mad_u32_u16 v67, v45, 4, s30 op_sel:[1,0,0,0]\n"
    "v_mad_u32_u16 v68, v46, 4, s30 op_sel:[0,0,0,0]\n"
    "v_mad_u32_u16 v69, v46, 4, s30 op_sel:[1,0,0,0]\n"
    "v_mad_u32_u16 v70, v47, 4, s30 op_sel:[0,0,0,0]\n"
    "v_mad_u32_u16 v71, v47, 4, s30 op_sel:[1,0,0,0]\n"
    "v_mov_b32 v184, 40944\n"
    "s_and_b32 s25, s25, 0xffffff\n"
    "s_mul_i32 s40, s25, 0x9e3779b1\n"
    "s_xor_b32 s41, s25, 0xa5c30f96\n"
    "s_lshr_b32 s40, s40, 24\n"
    "v_writelane_b32 v108, s43, 60\n"
    "ds_write2_b32 v192, v96, v97 offset0:0 offset1:33\n"
    "ds_write2_b32 v192, v98, v99 offset0:66 offset1:99\n"
    "v_add_u32 v192, 1056, v192\n"
    "ds_write2_b32 v192, v100, v101 offset0:0 offset1:33\n"
    "ds_write2_b32 v192, v102, v103 offset0:66 offset1:99\n"
    "v_add_u32 v192, 1056, v192\n"
    "ds_write2_b32 v192, v104, v105 offset0:0 offset1:33\n"
    "ds_write2_b32 v192, v106, v107 offset0:66 offset1:99\n"
    "v_add_u32 v192, 1056, v192\n"
    "ds_write2_b32 v192, v108, v109 offset0:0 offset1:33\n"
    "ds_read_b32 v56, v64\n"
    "ds_read_b32 v57, v65\n"
    "ds_read_b32 v58, v66\n"
    "ds_read_b32 v59, v67\n"
    "ds_read_b32 v60, v68\n"
    "ds_read_b32 v61, v69\n"
    "ds_read_b32 v62, v70\n"
    "ds_read_b32 v63, v71\n"
    "s_waitcnt lgkmcnt(6)\n"
    "v_pk_mul_f32 v[56:57], v[56:57], v[48:49]\n"
    "v_max3_f32 v188, |v56|, |v57|, 0\n"
    "s_waitcnt lgkmcnt(4)\n"
    "v_pk_mul_f32 v[58:59], v[58:59], v[50:51]\n"
    "v_max3_f32 v188, |v58|, |v59|, v188\n"
    "s_waitcnt lgkmcnt(2)\n"
    "v_pk_mul_f32 v[60:61], v[60:61], v[52:53]\n"
    "v_max3_f32 v188, |v60|, |v61|, v188\n"
    "s_waitcnt lgkmcnt(0)\n"
    "v_pk_mul_f32 v[62:63], v[62:63], v[54:55]\n"
    "v_max3_f32 v188, |v62|, |v63|, v188\n"
    "s_nop 1\n"
    "v_max_u32_dpp v188, v188, v188 quad_perm:[1,0,3,2] row_mask:0xf bank_mask:0xf\n"
    "s_nop 1\n"
    "v_max_u32_dpp v188, v188, v188 quad_perm:[2,3,0,1] row_mask:0xf bank_mask:0xf\n"
    "s_nop 1\n"
    "v_max_u32_dpp v188, v188, v188 row_half_mirror row_mask:0xf bank_mask:0xf\n"
    "s_nop 1\n"
    "v_max_u32_dpp v188, v188, v188 row_mirror row_mask:0xf bank_mask:0xf\n"
    "s_mov_b32 exec_lo, 0x10001\n"
    "s_mov_b32 exec_hi, 1\n"
    "ds_max_u32 v184, v188\n"
    "s_mov_b64 exec, -1\n"
    "s_add_u32 s38, s16, 1047552\n"
    "s_addc_u32 s39, s17, 0\n"
    "s_cmp_lt_u32 s23, 26\n"
    "s_cselect_b32 s43, 0xfff8fff8, 0\n"
    "s_waitcnt lgkmcnt(0)\n"
    "s_barrier\n"
    "ds_read_b32 v188, v184\n"
    "v_cmp_le_u32 vcc, 0x400, v193\n"
    "s_cmp_eq_u32 s20, 7\n"
    "s_cselect_b32 s42, 0, 48\n"
    "s_bfm_b64 s[60:61], s42, 0\n"
    "s_and_b64 s[60:61], s[60:61], vcc\n"
    "s_waitcnt lgkmcnt(0)\n"
    "v_lshrrev_b32 v188, 23, v188\n"
    "v_max_u32 v188, 40, v188\n"
    "v_min_u32 v188, 250, v188\n"
    "v_sub_u32 v190, 268, v188\n"
    "v_lshlrev_b32 v190, 23, v190\n"
    "v_pk_mul_f32 v[56:57], v[56:57], v[190:191] op_sel_hi:[1,0]\n"
    "v_cvt_pkrtz_f16_f32 v56, v56, v57\n"
    "v_add_u32 v56, 0x40004, v56\n"
    "v_and_b32 v56, s43, v56\n"
    "v_pk_mul_f32 v[58:59], v[58:59], v[190:191] op_sel_hi:[1,0]\n"
    "v_cvt_pkrtz_f16_f32 v57, v58, v59\n"
    "v_add_u32 v57, 0x40004, v57\n"
    "v_and_b32 v57, s43, v57\n"
    "v_pk_mul_f32 v[60:61], v[60:61], v[190:191] op_sel_hi:[1,0]\n"
    "v_cvt_pkrtz_f16_f32 v58, v60, v61\n"
    "v_add_u32 v58, 0x40004, v58\n"
    "v_and_b32 v58, s43, v58\n"
    "v_pk_mul_f32 v[62:63], v[62:63], v[190:191] op_sel_hi:[1,0]\n"
    "v_cvt_pkrtz_f16_f32 v59, v62, v63\n"
    "v_add_u32 v59, 0x40004, v59\n"
    "v_and_b32 v59, s43, v59\n"
    "v_lshl_or_b32 v188, v188, 8, s40\n"
    "v_mov_b32 v189, s41\n"
    "s_lshl3_add_u32 s40, s2, 40960\n"
    "v_mov_b32 v190, s40\n"
    "s_cmp_eq_u32 s20, 7\n"
    "s_cbranch_scc1 .Lxd_b3\n"
    "s_mov_b64 exec, s[60:61]\n"
    "global_store_dwordx4 v193, v[56:59], s[38:39] sc1\n"
    "s_mov_b64 exec, -1\n"
    ".Lxd_b3:\n"
    "s_waitcnt vmcnt(0)\n"
    "s_barrier\n"
    "s_cmp_lg_u32 s20, 0\n"
    "s_cbranch_scc1 .Lxd_on50\n"
    "s_mov_b64 exec, 1\n"
    "global_store_dwordx2 v190, v[188:189], s[16:17] sc1\n"
    "s_mov_b64 exec, -1\n"
    ".Lxd_on50:\n"
    "s_branch .Lxd_h1\n"
    ".Lxd_help:\n"
    "s_lshr_b32 s20, s43, 6\n"
    "v_and_b32 v1, 63, v0\n"
    "v_lshlrev_b32 v42, 4, v1\n"
    "s_lshr_b32 s41, s2, 3\n"
    "s_sub_u32 s41, s41, 1\n"
    "s_cmp_lg_u32 s20, 7\n"
    "s_cbranch_scc1 .Lxd_on52\n"
    "s_mul_i32 s40, s41, 60\n"
    "v_min_u32 v7, 59, v1\n"
    "v_add_lshl_u32 v7, v7, s40, 7\n"
    "s_getpc_b64 s[60:61]\n"
    ".Lxd_pc51:\n"
    "s_sub_u32 s60, s60, .Lxd_pc51-.Lxd_top\n"
    "s_subb_u32 s61, s61, 0\n"
    "v_min_u32 v7, .Lxd_end-.Lxd_top-4, v7\n"
    "global_load_dword v7, v7, s[60:61]\n"
    ".Lxd_on52:\n"
    "v_mul_u32_u24 v68, 52, v1\n"
    "v_lshrrev_b32 v68, 8, v68\n"
    "v_mad_i32_i24 v69, v68, -5, v1\n"
    "s_and_b32 s40, s2, 0xf8\n"
    "s_add_u32 s40, s40, s20\n"
    "s_mul_i32 s40, s40, 13\n"
    "s_sub_u32 s40, s40, 104\n"
    "v_add_u32 v68, s40, v68\n"
    "v_min_u32 v68, 0x1ff, v68\n"
    "v_lshrrev_b32 v70, 8, v68\n"
    "s_movk_i32 s40, 1792\n"
    "v_mad_u32_u24 v68, v70, s40, v68\n"
    "s_and_b32 s40, s2, 7\n"
    "v_lshl_add_u32 v68, s40, 8, v68\n"
    "v_mul_u32_u24 v68, 104, v68\n"
    "s_lshl_b32 s40, s2, 2\n"
    "s_lshr_b32 s40, 0xa8864220, s40\n"
    "s_and_b32 s40, s40, 14\n"
    "s_lshl_b32 s40, s40, 1\n"
    "v_add3_u32 v70, v69, s40, 2\n"
    "v_subrev_u32 v71, 24, v70\n"
    "v_min_u32 v70, v70, v71\n"
    "v_lshl_add_u32 v70, v70, 2, v68\n"
    "s_waitcnt lgkmcnt(0)\n"
    "s_load_dwordx4 s[68:71], s[0:1], 64\n"
    "global_load_dword v60, v70, s[4:5]\n"
    "v_add3_u32 v70, v69, s40, 7\n"
    "v_subrev_u32 v71, 24, v70\n"
    "v_min_u32 v70, v70, v71\n"
    "v_lshl_add_u32 v70, v70, 2, v68\n"
    "global_load_dword v61, v70, s[4:5]\n"
    "v_add3_u32 v70, v69, s40, 12\n"
    "v_subrev_u32 v71, 24, v70\n"
    "v_min_u32 v70, v70, v71\n"
    "v_lshl_add_u32 v70, v70, 2, v68\n"
    "global_load_dword v62, v70, s[4:5]\n"
    "v_add3_u32 v70, v69, s40, 17\n"
    "v_subrev_u32 v71, 24, v70\n"
    "v_min_u32 v70, v70, v71\n"
    "v_lshl_add_u32 v70, v70, 2, v68\n"
    "global_load_dword v63, v70, s[4:5]\n"
    "v_add3_u32 v70, v69, s40, 22\n"
    "v_subrev_u32 v71, 24, v70\n"
    "v_min_u32 v70, v70, v71\n"
    "v_add_u32 v71, 22, v69\n"
    "v_cmp_lt_u32 vcc, 23, v71\n"
    "v_min_u32 v71, 25, v71\n"
    "s_nop 0\n"
    "v_cndmask_b32 v70, v70, v71, vcc\n"
    "v_lshl_add_u32 v70, v70, 2, v68\n"
    "global_load_dword v64, v70, s[4:5]\n"
    "s_cmp_lg_u32 s20, 6\n"
    "s_cbranch_scc1 .Lxd_on53\n"
    "v_min_u32 v12, 25, v1\n"
    "s_mov_b32 m0, 159744\n"
    "v_lshlrev_b32 v12, 7, v12\n"
    "global_load_lds_dword v12, s[10:11]\n"
    "global_load_lds_dword v12, s[18:19]\n"
    ".Lxd_on53:\n"
    ".Lxd_h1:\n"
    "s_lshr_b32 s3, s2, 4\n"
    "s_mul_i32 s40, s3, 3\n"
    "s_add_u32 s47, s2, s40\n"
    "s_and_b32 s47, s47, 15\n"
    "s_lshr_b32 s21, s20, 2\n"
    "s_lshl1_add_u32 s22, s21, s20\n"
    "s_and_b32 s22, s22, 3\n"
    "s_lshl_b32 s58, s21, 8\n"
    "s_add_u32 s58, s58, 150528\n"
    "s_mov_b32 s40, s47\n"
    "s_mul_i32 s54, s40, 512\n"
    "s_add_u32 s54, s54, 2\n"
    "s_lshl_b32 s40, s40, 7\n"
    "s_add_u32 s40, s40, 32768\n"
    "s_add_u32 s52, s16, s40\n"
    "s_addc_u32 s53, s17, 0\n"
    "s_mov_b32 s57, 0x7fff7fff\n"
    "s_mov_b32 s56, 0\n"
    "s_mov_b32 s27, 0\n"
    "s_mov_b32 s55, 9\n"
    "s_mov_b32 s44, 0\n"
    "s_mov_b32 s46, 0\n"
    "v_lshrrev_b32 v17, 3, v1\n"
    "v_mul_u32_u24 v17, 12, v17\n"
    "v_and_b32 v19, 7, v1\n"
    "v_lshlrev_b32 v19, 4, v19\n"
    "v_lshrrev_b32 v18, 5, v1\n"
    "v_lshlrev_b32 v18, 2, v18\n"
    "v_and_b32 v20, 31, v1\n"
    "v_lshlrev_b32 v20, 2, v20\n"
    "s_lshl2_add_u32 s40, s22, s58\n"
    "s_sub_u32 s40, s40, 20\n"
    "v_mov_b32 v25, s40\n"
    "s_lshl1_add_u32 s23, s3, s21\n"
    "s_mul_i32 s23, s23, 6\n"
    "s_add_u32 s23, s23, s22\n"
    "s_add_u32 s23, s23, 78\n"
    "v_mov_b32 v24, s23\n"
    "v_lshlrev_b32 v13, 2, v0\n"
    "v_readfirstlane_b32 s40, v24\n"
    "s_or_b32 s40, s40, s44\n"
    "s_lshl1_add_u32 s41, s40, s54\n"
    "s_cmpk_lt_u32 s40, 256\n"
    "s_cselect_b32 s41, s41, 2\n"
    "v_mov_b32 v12, s41\n"
    "s_mul_i32 s41, s41, 52\n"
    "s_add_u32 s38, s4, s41\n"
    "s_addc_u32 s39, s5, 0\n"
    "global_load_dwordx3 v[36:38], v17, s[38:39] offset:-104\n"
    "global_load_dword v41, v18, s[38:39] offset:-8\n"
    "s_setprio 0\n"
    "v_mov_b32 v21, 0\n"
    "v_add_u32 v13, 149504, v13\n"
    "ds_write_b32 v13, v21\n"
    "s_cmp_lt_u32 s2, 8\n"
    "s_cbranch_scc1 .Lxd_hp54\n"
    "s_lshl_b32 s40, s2, 2\n"
    "s_lshr_b32 s40, 0xa8864220, s40\n"
    "s_and_b32 s40, s40, 14\n"
    "s_mul_i32 s40, s40, 3328\n"
    "s_and_b32 s41, s2, 0xf8\n"
    "s_add_u32 s41, s41, s20\n"
    "s_mul_i32 s41, s41, 42\n"
    "s_add_u32 s40, s40, s41\n"
    "s_add_u32 s40, s40, 4656\n"
    "v_min_u32 v68, 41, v1\n"
    "v_add_lshl_u32 v68, v68, s40, 7\n"
    "s_mov_b32 m0, 159744\n"
    "s_nop 0\n"
    "global_load_lds_dword v68, s[6:7]\n"
    "s_waitcnt vmcnt(3)\n"
    "s_mov_b32 m0, 159744\n"
    "v_lshlrev_b32 v60, 7, v60\n"
    "global_load_lds_dword v60, s[8:9]\n"
    "s_waitcnt vmcnt(2)\n"
    "s_branch .Lxd_h3\n"
    ".Lxd_hp54:\n"
    "s_waitcnt vmcnt(0)\n"
    ".Lxd_h3:\n"
    "s_setprio 0\n"
    "v_lshl_or_b32 v36, v36, 7, v19\n"
    "v_lshl_or_b32 v37, v37, 7, v19\n"
    "v_lshl_or_b32 v38, v38, 7, v19\n"
    "v_lshl_or_b32 v41, v41, 7, v20\n"
    "global_load_dwordx4 v[28:31], v36, s[8:9]\n"
    "global_load_dwordx4 v[32:35], v37, s[8:9]\n"
    "global_load_dword v40, v41, s[8:9]\n"
    "global_load_dwordx4 v[36:39], v38, s[8:9]\n"
    "s_waitcnt lgkmcnt(0)\n"
    "s_barrier\n"
    "ds_write_b32 v25, v12 offset:20\n"
    "s_load_dword s26, s[68:69], 0\n"
    "s_lshl_b32 s43, s21, 16\n"
    "v_lshrrev_b32 v12, 4, v1\n"
    "v_bfe_u32 v13, v1, 2, 2\n"
    "v_and_b32 v21, 3, v1\n"
    "s_lshl_b32 s40, s22, 5\n"
    "s_add_u32 s40, s40, s43\n"
    "v_lshl_add_u32 v21, v21, 3, s40\n"
    "s_movk_i32 s41, 144\n"
    "v_add_u32 v22, v12, v13\n"
    "v_and_b32 v22, 3, v22\n"
    "v_mad_u32_u24 v4, v22, s41, v21\n"
    "v_cmp_gt_u32 vcc, 2, v13\n"
    "v_add_u32 v24, 22, v13\n"
    "v_lshlrev_b32 v13, 3, v13\n"
    "v_cndmask_b32 v22, v24, v22, vcc\n"
    "v_mad_u32_u24 v3, v22, s41, v21\n"
    "v_cmp_eq_u32 vcc, 3, v12\n"
    "v_xor_b32 v22, 0, v12\n"
    "v_and_b32 v22, 1, v22\n"
    "v_lshlrev_b32 v22, 1, v22\n"
    "v_lshl_add_u32 v22, v12, 3, v22\n"
    "s_mov_b32 s40, 0x05040100\n"
    "v_bfe_u32 v24, s40, v13, 8\n"
    "v_add_u32 v22, v22, v24\n"
    "s_mov_b32 s40, 0x1a191818\n"
    "v_bfe_u32 v24, s40, v13, 8\n"
    "v_cndmask_b32 v22, v22, v24, vcc\n"
    "v_mad_u32_u24 v5, v22, s41, v21\n"
    "v_xor_b32 v22, 1, v12\n"
    "v_and_b32 v22, 1, v22\n"
    "s_cmp_lt_u32 s2, 8\n"
    "s_cbranch_scc1 .Lxd_hp55\n"
    "s_mov_b32 m0, 159744\n"
    "v_lshlrev_b32 v61, 7, v61\n"
    "global_load_lds_dword v61, s[8:9]\n"
    "s_mov_b32 m0, 159744\n"
    "v_lshlrev_b32 v62, 7, v62\n"
    "global_load_lds_dword v62, s[8:9]\n"
    ".Lxd_hp55:\n"
    "v_lshlrev_b32 v22, 1, v22\n"
    "v_lshl_add_u32 v22, v12, 3, v22\n"
    "s_mov_b32 s40, 0x05040100\n"
    "v_bfe_u32 v24, s40, v13, 8\n"
    "v_add_u32 v22, v22, v24\n"
    "s_mov_b32 s40, 0x1a191918\n"
    "v_bfe_u32 v24, s40, v13, 8\n"
    "v_cndmask_b32 v22, v22, v24, vcc\n"
    "v_mad_u32_u24 v6, v22, s41, v21\n"
    "v_bfrev_b32 v14, v12\n"
    "v_lshrrev_b32 v14, 26, v14\n"
    "s_add_u32 s40, s22, 3\n"
    "s_and_b32 s40, s40, 3\n"
    "s_lshl2_add_u32 s40, s40, s22\n"
    "s_add_u32 s40, s40, s58\n"
    "s_lshl_b32 s40, s40, 2\n"
    "s_add_u32 s40, s40, -450048\n"
    "v_add_u32 v14, s40, v14\n"
    "v_and_b32 v2, 1, v1\n"
    "v_mul_u32_u24 v2, 0xaaaa, v2\n"
    "v_add_u32 v2, 0x4444, v2\n"
    "v_mad_u32_u24 v15, v17, 36, v19\n"
    "v_mad_u32_u24 v16, v18, 36, v20\n"
    "s_lshl4_add_u32 s41, s21, s22\n"
    "v_lshrrev_b32 v12, 5, v19\n"
    "v_sub_u32 v12, 1, v12\n"
    "v_and_b32 v12, 3, v12\n"
    "v_add_lshl_u32 v12, v12, s41, 12\n"
    "v_add_u32 v15, v12, v15\n"
    "v_lshrrev_b32 v12, 5, v20\n"
    "v_sub_u32 v12, 1, v12\n"
    "v_and_b32 v12, 3, v12\n"
    "v_add_lshl_u32 v12, v12, s41, 12\n"
    "v_add_u32 v16, v12, v16\n"
    "v_add_u32 v16, 3456, v16\n"
    "s_lshl3_add_u32 s40, s2, 4096\n"
    "s_lshl_b32 s41, s2, 2\n"
    "v_lshlrev_b32 v11, 3, v1\n"
    "v_add_u32 v11, 40960, v11\n"
    "v_writelane_b32 v11, s40, 8\n"
    "v_writelane_b32 v11, s41, 9\n"
    "v_add_u32 v43, 4096, v42\n"
    "s_cmp_lt_u32 s2, 8\n"
    "s_cbranch_scc1 .Lxd_hp56\n"
    "s_waitcnt vmcnt(2)\n"
    "s_branch .Lxd_h2\n"
    ".Lxd_hp56:\n"
    "s_waitcnt vmcnt(0)\n"
    ".Lxd_h2:\n"
    "s_cmp_lg_u32 s20, 7\n"
    "s_cbranch_scc1 .Lxd_on57\n"
    "s_mov_b64 exec, 0x3ff\n"
    "global_load_dwordx2 v[26:27], v11, s[16:17] sc1\n"
    "s_mov_b64 exec, -1\n"
    ".Lxd_on57:\n"
    "s_movk_i32 s43, 15744\n"
    "v_pk_maximum3_f16 v21, v28, v28, s43 op_sel_hi:[1,1,0] neg_lo:[0,1,0] neg_hi:[0,1,0]\n"
    "v_pk_maximum3_f16 v21, v29, v29, v21 neg_lo:[0,1,0] neg_hi:[0,1,0]\n"
    "v_pk_maximum3_f16 v21, v30, v30, v21 neg_lo:[0,1,0] neg_hi:[0,1,0]\n"
    "v_pk_maximum3_f16 v21, v31, v31, v21 neg_lo:[0,1,0] neg_hi:[0,1,0]\n"
    "v_pk_maximum3_f16 v21, v32, v32, v21 neg_lo:[0,1,0] neg_hi:[0,1,0]\n"
    "v_pk_maximum3_f16 v21, v33, v33, v21 neg_lo:[0,1,0] neg_hi:[0,1,0]\n"
    "v_pk_maximum3_f16 v21, v34, v34, v21 neg_lo:[0,1,0] neg_hi:[0,1,0]\n"
    "v_pk_maximum3_f16 v21, v35, v35, v21 neg_lo:[0,1,0] neg_hi:[0,1,0]\n"
    "v_pk_maximum3_f16 v21, v36, v36, v21 neg_lo:[0,1,0] neg_hi:[0,1,0]\n"
    "v_pk_maximum3_f16 v21, v37, v37, v21 neg_lo:[0,1,0] neg_hi:[0,1,0]\n"
    "v_pk_maximum3_f16 v21, v38, v38, v21 neg_lo:[0,1,0] neg_hi:[0,1,0]\n"
    "v_pk_maximum3_f16 v21, v39, v39, v21 neg_lo:[0,1,0] neg_hi:[0,1,0]\n"
    "v_pk_maximum3_f16 v21, v40, v40, v21 neg_lo:[0,1,0] neg_hi:[0,1,0]\n"
    "v_pk_max_u16 v21, v21, v21 op_sel:[0,1] op_sel_hi:[1,0]\n"
    "s_nop 1\n"
    "v_max_u32_dpp v21, v21, v21 quad_perm:[1,0,3,2] row_mask:0xf bank_mask:0xf\n"
    "s_nop 1\n"
    "v_max_u32_dpp v21, v21, v21 quad_perm:[2,3,0,1] row_mask:0xf bank_mask:0xf\n"
    "s_nop 1\n"
    "v_max_u32_dpp v21, v21, v21 row_half_mirror row_mask:0xf bank_mask:0xf\n"
    "s_nop 1\n"
    "v_max_u32_dpp v21, v21, v21 row_mirror row_mask:0xf bank_mask:0xf\n"
    "s_nop 1\n"
    "v_max_u32_dpp v21, v21, v21 row_bcast:15 row_mask:0xa bank_mask:0xf\n"
    "s_nop 1\n"
    "v_max_u32_dpp v21, v21, v21 row_bcast:31 row_mask:0xc bank_mask:0xf\n"
    "s_nop 1\n"
    "v_readlane_b32 s47, v21, 63\n"
    "s_and_b32 s50, s47, 0x7f80\n"
    "s_lshr_b32 s47, s50, 3\n"
    "s_sub_u32 s50, s50, 2816\n"
    "s_mul_i32 s51, s50, -8\n"
    "v_pk_maximum3_f16 v21, v28, v28, s50 op_sel_hi:[1,1,0] neg_lo:[0,1,0] neg_hi:[0,1,0]\n"
    "v_pk_mad_u16 v21, v21, 8, s51 op_sel_hi:[1,0,0]\n"
    "v_bfi_b32 v28, s57, v21, v28\n"
    "v_pk_maximum3_f16 v12, v29, v29, s50 op_sel_hi:[1,1,0] neg_lo:[0,1,0] neg_hi:[0,1,0]\n"
    "v_pk_mad_u16 v12, v12, 8, s51 op_sel_hi:[1,0,0]\n"
    "v_bfi_b32 v29, s57, v12, v29\n"
    "v_pk_maximum3_f16 v21, v30, v30, s50 op_sel_hi:[1,1,0] neg_lo:[0,1,0] neg_hi:[0,1,0]\n"
    "v_pk_mad_u16 v21, v21, 8, s51 op_sel_hi:[1,0,0]\n"
    "v_bfi_b32 v30, s57, v21, v30\n"
    "v_pk_maximum3_f16 v12, v31, v31, s50 op_sel_hi:[1,1,0] neg_lo:[0,1,0] neg_hi:[0,1,0]\n"
    "v_pk_mad_u16 v12, v12, 8, s51 op_sel_hi:[1,0,0]\n"
    "v_bfi_b32 v31, s57, v12, v31\n"
    "v_pk_maximum3_f16 v21, v32, v32, s50 op_sel_hi:[1,1,0] neg_lo:[0,1,0] neg_hi:[0,1,0]\n"
    "v_pk_mad_u16 v21, v21, 8, s51 op_sel_hi:[1,0,0]\n"
    "v_bfi_b32 v32, s57, v21, v32\n"
    "v_pk_maximum3_f16 v12, v33, v33, s50 op_sel_hi:[1,1,0] neg_lo:[0,1,0] neg_hi:[0,1,0]\n"
    "v_pk_mad_u16 v12, v12, 8, s51 op_sel_hi:[1,0,0]\n"
    "v_bfi_b32 v33, s57, v12, v33\n"
    "v_pk_maximum3_f16 v21, v34, v34, s50 op_sel_hi:[1,1,0] neg_lo:[0,1,0] neg_hi:[0,1,0]\n"
    "v_pk_mad_u16 v21, v21, 8, s51 op_sel_hi:[1,0,0]\n"
    "v_bfi_b32 v34, s57, v21, v34\n"
    "v_pk_maximum3_f16 v12, v35, v35, s50 op_sel_hi:[1,1,0] neg_lo:[0,1,0] neg_hi:[0,1,0]\n"
    "v_pk_mad_u16 v12, v12, 8, s51 op_sel_hi:[1,0,0]\n"
    "v_bfi_b32 v35, s57, v12, v35\n"
    "v_pk_maximum3_f16 v21, v36, v36, s50 op_sel_hi:[1,1,0] neg_lo:[0,1,0] neg_hi:[0,1,0]\n"
    "v_pk_mad_u16 v21, v21, 8, s51 op_sel_hi:[1,0,0]\n"
    "v_bfi_b32 v36, s57, v21, v36\n"
    "v_pk_maximum3_f16 v12, v37, v37, s50 op_sel_hi:[1,1,0] neg_lo:[0,1,0] neg_hi:[0,1,0]\n"
    "v_pk_mad_u16 v12, v12, 8, s51 op_sel_hi:[1,0,0]\n"
    "v_bfi_b32 v37, s57, v12, v37\n"
    "v_pk_maximum3_f16 v21, v38, v38, s50 op_sel_hi:[1,1,0] neg_lo:[0,1,0] neg_hi:[0,1,0]\n"
    "v_pk_mad_u16 v21, v21, 8, s51 op_sel_hi:[1,0,0]\n"
    "v_bfi_b32 v38, s57, v21, v38\n"
    "v_pk_maximum3_f16 v12, v39, v39, s50 op_sel_hi:[1,1,0] neg_lo:[0,1,0] neg_hi:[0,1,0]\n"
    "v_pk_mad_u16 v12, v12, 8, s51 op_sel_hi:[1,0,0]\n"
    "v_bfi_b32 v39, s57, v12, v39\n"
    "v_pk_maximum3_f16 v21, v40, v40, s50 op_sel_hi:[1,1,0] neg_lo:[0,1,0] neg_hi:[0,1,0]\n"
    "v_pk_mad_u16 v21, v21, 8, s51 op_sel_hi:[1,0,0]\n"
    "v_bfi_b32 v40, s57, v21, v40\n"
    "s_sub_u32 s43, 4112, s47\n"
    "s_lshl_b32 s43, s43, 19\n"
    "v_cvt_pkrtz_f16_f32 v12, s43, s43\n"
    "v_lshl_or_b32 v21, v12, 16, s47\n"
    "ds_write_b128 v15, v[28:31] offset:0\n"
    "ds_write_b128 v15, v[32:35] offset:144\n"
    "ds_write_b128 v15, v[36:39] offset:288\n"
    "ds_write2_b32 v16, v40, v12 offset1:72\n"
    "s_mov_b64 exec, 1\n"
    "ds_or_b32 v25, v21 offset:532\n"
    "s_mov_b64 exec, -1\n"
    "s_cmp_lt_u32 s22, 2\n"
    "s_cselect_b32 s40, 16, 0\n"
    "v_add_u32 v25, s40, v25\n"
    "s_add_u32 s23, s23, 4\n"
    "v_mov_b32 v24, s23\n"
    "s_mov_b32 s36, 0\n"
    "v_mov_b32 v69, v43\n"
    "v_mov_b32 v68, v42\n"
    "s_cmp_lg_u32 s22, 0\n"
    "s_cbranch_scc1 .Lxd_on58\n"
    "v_readfirstlane_b32 s40, v24\n"
    "s_or_b32 s40, s40, s44\n"
    "s_lshl1_add_u32 s41, s40, s54\n"
    "s_cmpk_lt_u32 s40, 256\n"
    "s_cselect_b32 s41, s41, 2\n"
    "v_mov_b32 v12, s41\n"
    "s_mul_i32 s41, s41, 52\n"
    "s_add_u32 s38, s4, s41\n"
    "s_addc_u32 s39, s5, 0\n"
    "ds_write_b32 v25, v12 offset:20\n"
    "v_xor_b32 v15, 0x4000, v15\n"
    "v_xor_b32 v16, 0x4000, v16\n"
    "global_load_dwordx3 v[36:38], v17, s[38:39] offset:-104\n"
    "global_load_dword v41, v18, s[38:39] offset:-8\n"
    ".Lxd_on58:\n"
    "s_cmp_lg_u32 s20, 7\n"
    "s_cbranch_scc1 .Lxd_on60\n"
    "s_waitcnt vmcnt(0)\n"
    "v_readlane_b32 s25, v26, 9\n"
    "v_readlane_b32 s40, v26, 8\n"
    "v_readlane_b32 s41, v27, 8\n"
    "s_mul_i32 s42, s25, 0x9e3779b1\n"
    "s_lshr_b32 s42, s42, 24\n"
    "s_xor_b32 s42, s42, s40\n"
    "s_and_b32 s42, s42, 0xff\n"
    "s_xor_b32 s41, s41, s25\n"
    "s_xor_b32 s41, s41, 0xa5c30f96\n"
    "s_or_b32 s42, s41, s42\n"
    "s_lshr_b32 s41, s40, 8\n"
    "s_mul_i32 s40, s41, 0x9e3779b1\n"
    "s_xor_b32 s41, s41, 0xa5c30f96\n"
    "s_lshr_b32 s40, s40, 24\n"
    "v_xor_b32 v13, s40, v26\n"
    "v_xor_b32 v22, s41, v27\n"
    "v_lshl_or_b32 v13, v13, 24, v22\n"
    "v_cmp_ne_u32 s[66:67], 0, v13\n"
    "s_and_b32 s66, s66, 0xff\n"
    "s_or_b32 s42, s42, s66\n"
    "s_cmp_eq_u32 s20, 7\n"
    "s_cselect_b32 s42, s42, 0\n"
    "s_cmp_lg_u32 s42, 0\n"
    "s_cbranch_scc1 .Lxd_pv59\n"
    ".Lxd_pv59r:\n"
    "v_lshrrev_b32 v22, 8, v26\n"
    "v_mov_b32 v26, s40\n"
    "v_mov_b32 v27, s41\n"
    "v_add_u32_dpp v22, v22, v22 row_shl:4 row_mask:0xf bank_mask:0xf\n"
    "v_add_u32_e64 v43, s25, 1\n"
    "v_lshlrev_b32 v8, 2, v1\n"
    "v_max_u32_dpp v13, v22, v22 quad_perm:[1,0,3,2] row_mask:0xf bank_mask:0xf\n"
    "v_add_u32 v8, 154112, v8\n"
    "s_nop 0\n"
    "v_max_u32_dpp v13, v13, v13 quad_perm:[2,3,0,1] row_mask:0xf bank_mask:0xf\n"
    "v_sub_u32 v22, v22, v13\n"
    "v_subrev_u32 v13, 818, v13\n"
    "v_ldexp_f32 v22, 1.0, v22\n"
    "s_mov_b64 exec, 1\n"
    "ds_write_b64 v8, v[26:27]\n"
    "s_mov_b64 exec, 15\n"
    "ds_write2_b32 v8, v22, v13 offset0:4 offset1:8\n"
    "s_mov_b64 exec, 0x200\n"
    "global_store_dword v11, v43, s[16:17] sc1\n"
    "s_mov_b64 exec, -1\n"
    ".Lxd_on60:\n"
    "s_waitcnt lgkmcnt(0)\n"
    "s_barrier\n"
    "s_mul_i32 s42, s20, 5376\n"
    "s_add_u32 s40, s42, 1048576\n"
    "s_add_u32 s38, s16, s40\n"
    "s_addc_u32 s39, s17, 0\n"
    "s_add_u32 m0, s42, 98304\n"
    "s_nop 0\n"
    "global_load_lds_dwordx4 v68, s[38:39] offset:0 sc1\n"
    "global_load_lds_dwordx4 v68, s[38:39] offset:1024 sc1\n"
    "global_load_lds_dwordx4 v68, s[38:39] offset:2048 sc1\n"
    "global_load_lds_dwordx4 v68, s[38:39] offset:3072 sc1\n"
    "s_add_u32 m0, s42, 102400\n"
    "s_nop 0\n"
    "global_load_lds_dwordx4 v69, s[38:39] offset:0 sc1\n"
    "global_load_lds_dwordx4 v69, s[38:39] offset:256 sc1\n"
    "s_getpc_b64 s[10:11]\n"
    ".Lxd_en61:\n"
    "s_mov_b32 s40, .Lxd_ph1-.Lxd_en61\n"
    "s_cmp_eq_u32 s22, 1\n"
    "s_cselect_b32 s40, .Lxd_ph0-.Lxd_en61, s40\n"
    "s_cmp_eq_u32 s22, 2\n"
    "s_cselect_b32 s40, .Lxd_pl3-.Lxd_en61, s40\n"
    "s_cmp_eq_u32 s22, 3\n"
    "s_cselect_b32 s40, .Lxd_pl2-.Lxd_en61, s40\n"
    "s_ashr_i32 s41, s40, 31\n"
    "s_add_u32 s10, s10, s40\n"
    "s_addc_u32 s11, s11, s41\n"
    "v_mov_b32 v13, 154112\n"
    "ds_read_b64 v[26:27], v13\n"
    "v_add_u32 v42, 98304, v42\n"
    "v_mov_b32 v13, s58\n"
    "s_sub_u32 s40, 1, s22\n"
    "s_and_b32 s40, s40, 3\n"
    "v_lshl_add_u32 v7, s40, 12, v3\n"
    "v_lshl_add_u32 v8, s40, 12, v4\n"
    "v_lshl_add_u32 v9, s40, 12, v5\n"
    "v_lshl_add_u32 v10, s40, 12, v6\n"
    "v_bfe_u32 v21, v1, 4, 1\n"
    "v_lshlrev_b32 v21, 2, v21\n"
    "v_add_u32 v21, 154112, v21\n"
    "ds_read_b32 v0, v21 offset:16\n"
    "ds_read_b32 v23, v21 offset:24\n"
    "ds_read_b32 v22, v13 offset:512\n"
    "ds_read_b64_tr_b16 v[44:45], v9 offset:0\n"
    "ds_read_b64_tr_b16 v[46:47], v10 offset:0\n"
    "ds_read_b64_tr_b16 v[48:49], v7 offset:0\n"
    "ds_read_b64_tr_b16 v[50:51], v8 offset:576\n"
    "ds_read_b64_tr_b16 v[52:53], v8 offset:1152\n"
    "ds_read_b64_tr_b16 v[54:55], v8 offset:1728\n"
    "ds_read_b64_tr_b16 v[56:57], v8 offset:2304\n"
    "ds_read_b64_tr_b16 v[58:59], v8 offset:2880\n"
    "v_mov_b64 v[88:89], 0\n"
    "v_mov_b64 v[90:91], 0\n"
    "s_waitcnt vmcnt(0)\n"
    "s_barrier\n"
    "ds_read_b128 v[92:95], v42 offset:0\n"
    "ds_read_b128 v[96:99], v42 offset:1024\n"
    "ds_read_b128 v[100:103], v42 offset:2048\n"
    "ds_read_b128 v[104:107], v42 offset:3072\n"
    "ds_read_b128 v[108:111], v42 offset:4096\n"
    "ds_read_b128 v[112:115], v42 offset:5120\n"
    "ds_read_b128 v[116:119], v42 offset:6144\n"
    "ds_read_b128 v[120:123], v42 offset:7168\n"
    "ds_read_b128 v[124:127], v42 offset:8192\n"
    "ds_read_b128 v[128:131], v42 offset:9216\n"
    "ds_read_b128 v[132:135], v42 offset:10240\n"
    "ds_read_b128 v[136:139], v42 offset:11264\n"
    "ds_read_b128 v[140:143], v42 offset:12288\n"
    "ds_read_b128 v[144:147], v42 offset:13312\n"
    "ds_read_b128 v[148:151], v42 offset:14336\n"
    "ds_read_b128 v[152:155], v42 offset:15360\n"
    "ds_read_b128 v[156:159], v42 offset:16384\n"
    "ds_read_b128 v[160:163], v42 offset:17408\n"
    "ds_read_b128 v[164:167], v42 offset:18432\n"
    "ds_read_b128 v[168:171], v42 offset:19456\n"
    "ds_read_b128 v[172:175], v42 offset:20480\n"
    "ds_read_b128 v[176:179], v42 offset:21504\n"
    "ds_read_b128 v[180:183], v42 offset:22528\n"
    "ds_read_b128 v[184:187], v42 offset:23552\n"
    "ds_read_b128 v[188:191], v42 offset:24576\n"
    "ds_read_b128 v[192:195], v42 offset:25600\n"
    "ds_read_b128 v[196:199], v42 offset:26624\n"
    "ds_read_b128 v[200:203], v42 offset:27648\n"
    "ds_read_b128 v[204:207], v42 offset:28672\n"
    "ds_read_b128 v[208:211], v42 offset:29696\n"
    "ds_read_b128 v[212:215], v42 offset:30720\n"
    "ds_read_b128 v[216:219], v42 offset:31744\n"
    "ds_read_b128 v[220:223], v42 offset:32768\n"
    "ds_read_b128 v[224:227], v42 offset:33792\n"
    "ds_read_b128 v[228:231], v42 offset:34816\n"
    "ds_read_b128 v[232:235], v42 offset:35840\n"
    "ds_read_b128 v[236:239], v42 offset:36864\n"
    "ds_read_b128 v[240:243], v42 offset:37888\n"
    "ds_read_b128 v[244:247], v42 offset:38912\n"
    "ds_read_b128 v[248:251], v42 offset:39936\n"
    "ds_read_b128 v[252:255], v42 offset:40960\n"
    "s_waitcnt lgkmcnt(15)\n"
    "v_readfirstlane_b32 s45, v22\n"
    "v_readfirstlane_b32 s28, v26\n"
    "v_readfirstlane_b32 s29, v27\n"
    "s_cmp_lt_u32 s2, 8\n"
    "s_cbranch_scc1 .Lxd_hp63\n"
    "s_mov_b32 m0, 159744\n"
    "s_mov_b32 s46, 2\n"
    "v_lshlrev_b32 v26, 7, v63\n"
    "v_lshlrev_b32 v27, 7, v64\n"
    ".Lxd_hp63:\n"
    "v_pk_mul_f16 v64, v44, v46\n"
    "v_pk_mul_f16 v65, v45, v47\n"
    "v_pk_mul_f16 v66, v48, v48 op_sel_hi:[0,1]\n"
    "v_pk_mul_f16 v67, v48, v49 op_sel_hi:[0,1]\n"
    "v_pk_mul_f16 v60, v44, s45 op_sel:[0,1]\n"
    "v_pk_mul_f16 v61, v45, s45 op_sel:[0,1]\n"
    "v_pk_mul_f16 v62, v46, s45 op_sel:[0,1]\n"
    "v_pk_mul_f16 v63, v47, s45 op_sel:[0,1]\n"
    "s_waitcnt lgkmcnt(0)\n"
    "s_branch .Lxd_enter\n"
    ".Lxd_slow:\n"
    "s_cmp_eq_u32 s45, -1\n"
    "s_cbranch_scc1 .Lxd_sl7\n"
    "s_mov_b32 s59, 0x100000\n"
    "s_and_b32 s40, s45, 0xff0\n"
    "s_cbranch_scc0 .Lxd_sl1\n"
    ".Lxd_sl2:\n"
    "s_cmp_ge_i32 s45, -16\n"
    "s_cbranch_scc1 .Lxd_sl3\n"
    "s_bitset0_b32 s45, 31\n"
    ".Lxd_sl6:\n"
    "s_cmp_lg_u32 s56, 0\n"
    "s_cbranch_scc1 .Lxd_sl9\n"
    "v_readfirstlane_b32 s40, v13\n"
    "s_lshl4_add_u32 s41, s22, s58\n"
    "s_mov_b32 s56, 2\n"
    "s_cmp_lt_u32 s40, s41\n"
    "s_cbranch_scc0 .Lxd_gran\n"
    ".Lxd_sl9:\n"
    "s_setpc_b64 s[60:61]\n"
    ".Lxd_sl1:\n"
    "s_sleep 1\n"
    "ds_read_b32 v22, v13 offset:512\n"
    "s_waitcnt lgkmcnt(0)\n"
    "v_readfirstlane_b32 s45, v22\n"
    "s_sub_u32 s59, s59, 1\n"
    "s_cbranch_scc1 .Lxd_sl5\n"
    "s_and_b32 s40, s45, 0xff0\n"
    "s_cbranch_scc0 .Lxd_sl1\n"
    "s_cmp_gt_i32 s45, 0\n"
    "s_cbranch_scc0 .Lxd_sl2\n"
    "s_setpc_b64 s[60:61]\n"
    ".Lxd_sl3:\n"
    "v_lshl_add_u32 v13, v1, 2, v13\n"
    "s_mov_b64 exec, 7\n"
    ".Lxd_sl4:\n"
    "ds_read_b32 v22, v13 offset:4\n"
    "s_waitcnt lgkmcnt(0)\n"
    "v_cmp_eq_u32 s[40:41], 0, v22\n"
    "v_cmp_ne_u32 s[42:43], -1, v22\n"
    "s_sub_u32 s59, s59, 1\n"
    "s_cbranch_scc1 .Lxd_sl5\n"
    "s_cmp_lg_u32 s40, 0\n"
    "s_cbranch_scc1 .Lxd_sl4\n"
    "s_cmp_eq_u32 s42, 0\n"
    "s_cbranch_scc1 .Lxd_sl5\n"
    "s_mov_b64 exec, -1\n"
    "s_mov_b32 s45, 1\n"
    "s_mov_b32 s56, 0\n"
    "s_setpc_b64 s[60:61]\n"
    ".Lxd_sl5:\n"
    "s_mov_b64 exec, -1\n"
    "s_mov_b32 s45, -1\n"
    ".Lxd_sl7:\n"
    "s_cmp_lg_u32 s56, 1\n"
    "s_cbranch_scc1 .Lxd_sl6\n"
    "s_branch .Lxd_dsum\n"
    ".Lxd_gran:\n"
    "s_mov_b32 s46, 0\n"
    "s_lshl4_add_u32 s40, s22, s58\n"
    "v_lshrrev_b32 v13, 4, v1\n"
    "v_lshl_add_u32 v13, v13, 2, s40\n"
    "s_waitcnt vmcnt(0)\n"
    "ds_read2_b32 v[26:27], v13 offset1:16\n"
    "v_and_b32 v13, 15, v1\n"
    "v_lshlrev_b32 v13, 15, v13\n"
    "s_mov_b32 s42, 0x1fff1fff\n"
    "s_mov_b32 s43, s42\n"
    "s_add_u32 s40, s16, 65528\n"
    "s_addc_u32 s41, s17, 0\n"
    "v_mov_b32 v42, 0\n"
    "s_mov_b32 s56, 2\n"
    "s_waitcnt lgkmcnt(0)\n"
    "v_cmp_lt_i32 s[30:31], 0, v26\n"
    "v_cmp_lt_i32 s[64:65], 0, v27\n"
    "v_lshl_add_u32 v43, v26, 2, v13\n"
    "v_lshl_add_u32 v27, v27, 2, v13\n"
    "v_mov_b32 v26, 0\n"
    "s_and_b64 s[30:31], s[30:31], s[42:43]\n"
    "s_cbranch_scc0 .Lxd_gr30\n"
    "s_mov_b64 exec, s[30:31]\n"
    "global_load_dwordx2 v[42:43], v43, s[40:41] sc1\n"
    "s_mov_b64 exec, -1\n"
    "s_mov_b32 s56, 1\n"
    ".Lxd_gr30:\n"
    "s_and_b64 s[64:65], s[64:65], s[42:43]\n"
    "s_cbranch_scc0 .Lxd_gr64\n"
    "s_mov_b64 exec, s[64:65]\n"
    "global_load_dwordx2 v[26:27], v27, s[40:41] sc1\n"
    "s_mov_b64 exec, -1\n"
    "s_mov_b32 s56, 1\n"
    ".Lxd_gr64:\n"
    "s_setpc_b64 s[60:61]\n"
    ".Lxd_dsum:\n"
    "s_lshl4_add_u32 s42, s22, s58\n"
    "s_waitcnt vmcnt(0)\n"
    "s_cmp_eq_u64 s[30:31], 0\n"
    "s_cbranch_scc1 .Lxd_ds30\n"
    "v_xor_b32 v13, s28, v42\n"
    "v_xor_b32 v22, s29, v43\n"
    "v_lshl_or_b32 v13, v13, 24, v22\n"
    "v_and_b32 v22, 0xffffff00, v42\n"
    "v_cmp_ne_u32 s[40:41], 0, v13\n"
    "v_lshrrev_b32 v13, 4, v1\n"
    "v_add_f32_dpp v22, v22, v22 quad_perm:[1,0,3,2] row_mask:0xf bank_mask:0xf\n"
    "s_and_b64 s[40:41], s[40:41], s[30:31]\n"
    "s_cbranch_scc1 .Lxd_ds9\n"
    "v_add_f32_dpp v22, v22, v22 quad_perm:[2,3,0,1] row_mask:0xf bank_mask:0xf\n"
    "v_lshl_add_u32 v13, v13, 2, s42\n"
    "s_nop 0\n"
    "v_add_f32_dpp v22, v22, v22 row_half_mirror row_mask:0xf bank_mask:0xf\n"
    "s_nop 1\n"
    "v_add_f32_dpp v22, v22, v22 row_mirror row_mask:0xf bank_mask:0xf\n"
    "ds_write_b32 v13, v22 offset:1024\n"
    ".Lxd_ds30:\n"
    "s_cmp_eq_u64 s[64:65], 0\n"
    "s_cbranch_scc1 .Lxd_ds64\n"
    "v_xor_b32 v13, s28, v26\n"
    "v_xor_b32 v22, s29, v27\n"
    "v_lshl_or_b32 v13, v13, 24, v22\n"
    "v_and_b32 v22, 0xffffff00, v26\n"
    "v_cmp_ne_u32 s[40:41], 0, v13\n"
    "v_lshrrev_b32 v13, 4, v1\n"
    "v_add_f32_dpp v22, v22, v22 quad_perm:[1,0,3,2] row_mask:0xf bank_mask:0xf\n"
    "s_and_b64 s[40:41], s[40:41], s[64:65]\n"
    "s_cbranch_scc1 .Lxd_ds9\n"
    "v_add_f32_dpp v22, v22, v22 quad_perm:[2,3,0,1] row_mask:0xf bank_mask:0xf\n"
    "v_lshl_add_u32 v13, v13, 2, s42\n"
    "s_nop 0\n"
    "v_add_f32_dpp v22, v22, v22 row_half_mirror row_mask:0xf bank_mask:0xf\n"
    "s_nop 1\n"
    "v_add_f32_dpp v22, v22, v22 row_mirror row_mask:0xf bank_mask:0xf\n"
    "ds_write_b32 v13, v22 offset:1088\n"
    ".Lxd_ds64:\n"
    "s_mov_b32 s56, 2\n"
    ".Lxd_ds9:\n"
    "s_setpc_b64 s[60:61]\n"
    ".Lxd_mark:\n"
    "v_lshl_add_u32 v13, v1, 2, v13\n"
    "s_add_u32 s40, s58, 248\n"
    "v_mov_b32 v24, -1\n"
    "v_cmp_gt_u32 s[42:43], s40, v13\n"
    "s_mov_b32 s62, 0x88888888\n"
    "s_mov_b32 s63, s62\n"
    "s_and_b64 exec, s[42:43], s[62:63]\n"
    "ds_write_b32 v13, v24 offset:8\n"
    "s_mov_b64 exec, s[42:43]\n"
    "v_cndmask_b32 v24, 0, -16, s[62:63]\n"
    "s_brev_b32 s40, 1\n"
    "v_writelane_b32 v24, s40, 2\n"
    "s_lshl_b32 s40, 1, s22\n"
    "v_or_b32 v24, s40, v24\n"
    "s_mov_b32 s36, 1\n"
    "ds_or_b32 v13, v24 offset:520\n"
    "s_mov_b64 exec, -1\n"
    "s_setpc_b64 s[60:61]\n"
    ".Lxd_tf15:\n"
    "v_mov_b32 v12, -1\n"
    "v_mov_b32 v13, v25\n"
    "s_call_b64 s[60:61], .Lxd_mark\n"
    "s_branch .Lxd_tf15r\n"
    ".Lxd_ck18:\n"
    "v_add_u32 v13, 4, v25\n"
    "s_call_b64 s[60:61], .Lxd_slow\n"
    "s_cmp_eq_u32 s45, -1\n"
    "s_cbranch_scc0 .Lxd_ck18r\n"
    "s_call_b64 s[60:61], .Lxd_last\n"
    "v_fma_f32 v7, v86, v87, v11\n"
    "s_branch .Lxd_fin\n"
    ".Lxd_ck21:\n"
    "v_add_u32 v13, 8, v25\n"
    "s_call_b64 s[60:61], .Lxd_slow\n"
    "s_cmp_eq_u32 s45, -1\n"
    "s_cbranch_scc0 .Lxd_ck21r\n"
    "s_call_b64 s[60:61], .Lxd_last\n"
    "v_fma_f32 v8, v86, v87, v11\n"
    "s_branch .Lxd_fin\n"
    ".Lxd_ht24:\n"
    "global_load_lds_dword v26, s[8:9]\n"
    "v_mov_b32 v26, v27\n"
    "v_mov_b32 v27, v42\n"
    "s_sub_u32 s46, s46, 1\n"
    "s_branch .Lxd_ht24r\n"
    ".Lxd_ck25:\n"
    "v_add_u32 v13, 12, v25\n"
    "s_call_b64 s[60:61], .Lxd_slow\n"
    "s_cmp_eq_u32 s45, -1\n"
    "s_cbranch_scc0 .Lxd_ck25r\n"
    "s_call_b64 s[60:61], .Lxd_last\n"
    "v_fma_f32 v9, v86, v87, v11\n"
    "s_branch .Lxd_fin\n"
    ".Lxd_ck28:\n"
    "v_add_u32 v13, 16, v25\n"
    "s_call_b64 s[60:61], .Lxd_slow\n"
    "s_cmp_eq_u32 s45, -1\n"
    "s_cbranch_scc0 .Lxd_ck28r\n"
    "s_call_b64 s[60:61], .Lxd_last\n"
    "v_fma_f32 v10, v86, v87, v11\n"
    "s_branch .Lxd_fin\n"
    ".Lxd_dr29:\n"
    "v_add_u32 v13, 16, v25\n"
    "s_call_b64 s[60:61], .Lxd_mark\n"
    "s_branch .Lxd_dr29r\n"
    ".Lxd_ck32:\n"
    "v_add_u32 v13, 4, v25\n"
    "s_call_b64 s[60:61], .Lxd_slow\n"
    "s_cmp_eq_u32 s45, -1\n"
    "s_cbranch_scc0 .Lxd_ck32r\n"
    "s_call_b64 s[60:61], .Lxd_last\n"
    "v_fma_f32 v7, v86, v87, v11\n"
    "s_branch .Lxd_fin\n"
    ".Lxd_ck35:\n"
    "v_add_u32 v13, 8, v25\n"
    "s_call_b64 s[60:61], .Lxd_slow\n"
    "s_cmp_eq_u32 s45, -1\n"
    "s_cbranch_scc0 .Lxd_ck35r\n"
    "s_call_b64 s[60:61], .Lxd_last\n"
    "v_fma_f32 v8, v86, v87, v11\n"
    "s_branch .Lxd_fin\n"
    ".Lxd_ht38:\n"
    "global_load_lds_dword v26, s[8:9]\n"
    "v_mov_b32 v26, v27\n"
    "v_mov_b32 v27, v42\n"
    "s_sub_u32 s46, s46, 1\n"
    "s_branch .Lxd_ht38r\n"
    ".Lxd_ck39:\n"
    "v_add_u32 v13, 12, v25\n"
    "s_call_b64 s[60:61], .Lxd_slow\n"
    "s_cmp_eq_u32 s45, -1\n"
    "s_cbranch_scc0 .Lxd_ck39r\n"
    "s_call_b64 s[60:61], .Lxd_last\n"
    "v_fma_f32 v9, v86, v87, v11\n"
    "s_branch .Lxd_fin\n"
    ".Lxd_dr43:\n"
    "v_add_u32 v13, 16, v25\n"
    "s_call_b64 s[60:61], .Lxd_mark\n"
    "s_branch .Lxd_dr43r\n"
    ".Lxd_on42:\n"
    "s_and_b32 s40, s45, 15\n"
    "s_cbranch_scc1 .Lxd_dr43\n"
    "v_mov_b32 v24, 1\n"
    "s_mov_b64 exec, 1\n"
    "global_atomic_add v24, v17, v24, s[52:53] sc0 sc1\n"
    "s_mov_b64 exec, -1\n"
    ".Lxd_dr43r:\n"
    "s_branch .Lxd_on42r\n"
    ".Lxd_ck44:\n"
    "v_add_u32 v13, 16, v25\n"
    "s_call_b64 s[60:61], .Lxd_slow\n"
    "s_cmp_eq_u32 s45, -1\n"
    "s_cbranch_scc0 .Lxd_ck44r\n"
    "s_call_b64 s[60:61], .Lxd_last\n"
    "v_fma_f32 v10, v86, v87, v11\n"
    "s_branch .Lxd_fin\n"
    ".Lxd_f2:\n"
    "s_mov_b32 s59, 0x20000\n"
    ".Lxd_f4:\n"
    "s_call_b64 s[60:61], .Lxd_dsum\n"
    "s_cmp_eq_u32 s56, 2\n"
    "s_cbranch_scc1 .Lxd_f3\n"
    "s_sleep 2\n"
    "s_call_b64 s[60:61], .Lxd_gran\n"
    "s_sub_u32 s59, s59, 1\n"
    "s_cbranch_scc0 .Lxd_f4\n"
    "s_waitcnt vmcnt(0)\n"
    "s_branch .Lxd_f3\n"
    ".Lxd_on1:\n"
    "s_mov_b64 exec, 0x3ff\n"
    "global_load_dwordx2 v[26:27], v11, s[16:17] sc1\n"
    "s_mov_b64 exec, -1\n"
    "s_branch .Lxd_on1r\n"
    ".Lxd_pv3:\n"
    "s_mov_b32 s59, 0x20000\n"
    ".Lxd_pv3l:\n"
    "s_sleep 2\n"
    "s_mov_b64 exec, 0x3ff\n"
    "global_load_dwordx2 v[26:27], v11, s[16:17] sc1\n"
    "s_mov_b64 exec, -1\n"
    "s_waitcnt vmcnt(0)\n"
    "v_readlane_b32 s25, v26, 9\n"
    "v_readlane_b32 s40, v26, 8\n"
    "v_readlane_b32 s41, v27, 8\n"
    "s_mul_i32 s42, s25, 0x9e3779b1\n"
    "s_lshr_b32 s42, s42, 24\n"
    "s_xor_b32 s42, s42, s40\n"
    "s_and_b32 s42, s42, 0xff\n"
    "s_xor_b32 s41, s41, s25\n"
    "s_xor_b32 s41, s41, 0xa5c30f96\n"
    "s_or_b32 s42, s41, s42\n"
    "s_lshr_b32 s41, s40, 8\n"
    "s_mul_i32 s40, s41, 0x9e3779b1\n"
    "s_xor_b32 s41, s41, 0xa5c30f96\n"
    "s_lshr_b32 s40, s40, 24\n"
    "v_xor_b32 v13, s40, v26\n"
    "v_xor_b32 v22, s41, v27\n"
    "v_lshl_or_b32 v13, v13, 24, v22\n"
    "v_cmp_ne_u32 s[66:67], 0, v13\n"
    "s_and_b32 s66, s66, 0xff\n"
    "s_or_b32 s42, s42, s66\n"
    "s_cmp_eq_u32 s42, 0\n"
    "s_cbranch_scc1 .Lxd_pv3r\n"
    "s_sub_u32 s59, s59, 1\n"
    "s_cbranch_scc0 .Lxd_pv3l\n"
    "s_branch .Lxd_pv3r\n"
    ".Lxd_on6:\n"
    "v_mov_b32 v24, 1\n"
    "s_mov_b64 exec, 1\n"
    "global_atomic_add v24, v17, v24, s[52:53] sc0 sc1\n"
    "s_mov_b64 exec, -1\n"
    "s_branch .Lxd_on6r\n"
    ".Lxd_ck7:\n"
    "v_mov_b32 v13, s58\n"
    "s_call_b64 s[60:61], .Lxd_slow\n"
    "s_branch .Lxd_ck7r\n"
    ".Lxd_tf8:\n"
    "v_mov_b32 v12, -1\n"
    "v_mov_b32 v13, v25\n"
    "s_call_b64 s[60:61], .Lxd_mark\n"
    "s_branch .Lxd_tf8r\n"
    ".Lxd_on9:\n"
    "s_waitcnt vmcnt(0)\n"
    "v_readfirstlane_b32 s40, v24\n"
    "s_or_b32 s40, s40, s44\n"
    "s_lshl1_add_u32 s41, s40, s54\n"
    "s_cmpk_lt_u32 s40, 256\n"
    "s_cselect_b32 s41, s41, 2\n"
    "v_mov_b32 v12, s41\n"
    "s_cbranch_scc0 .Lxd_tf8\n"
    ".Lxd_tf8r:\n"
    "s_mul_i32 s41, s41, 52\n"
    "s_add_u32 s38, s4, s41\n"
    "s_addc_u32 s39, s5, 0\n"
    "ds_write_b32 v25, v12 offset:20\n"
    "global_load_dwordx3 v[36:38], v17, s[38:39] offset:-104\n"
    "global_load_dword v41, v18, s[38:39] offset:-8\n"
    "v_mov_b32 v13, s58\n"
    "s_branch .Lxd_on9r\n"
    ".Lxd_on10:\n"
    "v_mov_b32 v24, 1\n"
    "s_mov_b64 exec, 1\n"
    "global_atomic_add v24, v17, v24, s[52:53] sc0 sc1\n"
    "s_mov_b64 exec, -1\n"
    "s_branch .Lxd_on10r\n"
    ".Lxd_on11:\n"
    "s_waitcnt vmcnt(0)\n"
    "v_lshl_or_b32 v36, v36, 7, v19\n"
    "v_lshl_or_b32 v37, v37, 7, v19\n"
    "v_lshl_or_b32 v38, v38, 7, v19\n"
    "v_lshl_or_b32 v41, v41, 7, v20\n"
    "global_load_dwordx4 v[28:31], v36, s[8:9]\n"
    "global_load_dwordx4 v[32:35], v37, s[8:9]\n"
    "global_load_dword v40, v41, s[8:9]\n"
    "global_load_dwordx4 v[36:39], v38, s[8:9]\n"
    "s_branch .Lxd_on11r\n"
    ".Lxd_tf12:\n"
    "v_mov_b32 v12, -1\n"
    "v_mov_b32 v13, v25\n"
    "s_call_b64 s[60:61], .Lxd_mark\n"
    "s_branch .Lxd_tf12r\n"
    ".Lxd_on13:\n"
    "s_waitcnt vmcnt(0)\n"
    "v_readfirstlane_b32 s40, v24\n"
    "s_or_b32 s40, s40, s44\n"
    "s_lshl1_add_u32 s41, s40, s54\n"
    "s_cmpk_lt_u32 s40, 256\n"
    "s_cselect_b32 s41, s41, 2\n"
    "v_mov_b32 v12, s41\n"
    "s_cbranch_scc0 .Lxd_tf12\n"
    ".Lxd_tf12r:\n"
    "s_mul_i32 s41, s41, 52\n"
    "s_add_u32 s38, s4, s41\n"
    "s_addc_u32 s39, s5, 0\n"
    "ds_write_b32 v25, v12 offset:20\n"
    "v_xor_b32 v15, 0x4000, v15\n"
    "v_xor_b32 v16, 0x4000, v16\n"
    "global_load_dwordx3 v[36:38], v17, s[38:39] offset:-104\n"
    "global_load_dword v41, v18, s[38:39] offset:-8\n"
    "s_branch .Lxd_on13r\n"
    ".Lxd_on14:\n"
    "v_mov_b32 v24, 1\n"
    "s_mov_b64 exec, 1\n"
    "global_atomic_add v24, v17, v24, s[52:53] sc0 sc1\n"
    "s_mov_b64 exec, -1\n"
    "s_branch .Lxd_on14r\n"
    ".Lxd_pv59:\n"
    "s_mov_b32 s59, 0x20000\n"
    ".Lxd_pv59l:\n"
    "s_sleep 2\n"
    "s_mov_b64 exec, 0x3ff\n"
    "global_load_dwordx2 v[26:27], v11, s[16:17] sc1\n"
    "s_mov_b64 exec, -1\n"
    "s_waitcnt vmcnt(0)\n"
    "v_readlane_b32 s25, v26, 9\n"
    "v_readlane_b32 s40, v26, 8\n"
    "v_readlane_b32 s41, v27, 8\n"
    "s_mul_i32 s42, s25, 0x9e3779b1\n"
    "s_lshr_b32 s42, s42, 24\n"
    "s_xor_b32 s42, s42, s40\n"
    "s_and_b32 s42, s42, 0xff\n"
    "s_xor_b32 s41, s41, s25\n"
    "s_xor_b32 s41, s41, 0xa5c30f96\n"
    "s_or_b32 s42, s41, s42\n"
    "s_lshr_b32 s41, s40, 8\n"
    "s_mul_i32 s40, s41, 0x9e3779b1\n"
    "s_xor_b32 s41, s41, 0xa5c30f96\n"
    "s_lshr_b32 s40, s40, 24\n"
    "v_xor_b32 v13, s40, v26\n"
    "v_xor_b32 v22, s41, v27\n"
    "v_lshl_or_b32 v13, v13, 24, v22\n"
    "v_cmp_ne_u32 s[66:67], 0, v13\n"
    "s_and_b32 s66, s66, 0xff\n"
    "s_or_b32 s42, s42, s66\n"
    "s_cmp_eq_u32 s42, 0\n"
    "s_cbranch_scc1 .Lxd_pv59r\n"
    "s_sub_u32 s59, s59, 1\n"
    "s_cbranch_scc0 .Lxd_pv59l\n"
    "s_branch .Lxd_pv59r\n"
    ".Lxd_end:\n"
    : "+{v0}"(tid) : "{s[0:1]}"(__builtin_amdgcn_kernarg_segment_ptr()), "{s2}"(blockIdx.x)
    : "memory", "vcc", "scc", "v1", "v2", "v3", "v4", "v5", "v6", "v7", "v8", "v9", "v10", "v11", "v12", "v13", "v14", "v15", "v16", "v17", "v18", "v19", "v20", "v21", "v22", "v23", "v24", "v25", "v26", "v27", "v28", "v29", "v30", "v31", "v32", "v33", "v34", "v35", "v36", "v37", "v38", "v39", "v40", "v41", "v42", "v43", "v44", "v45", "v46", "v47", "v48", "v49", "v50", "v51", "v52", "v53", "v54", "v55", "v56", "v57", "v58", "v59", "v60", "v61", "v62", "v63", "v64", "v65", "v66", "v67", "v68", "v69", "v70", "v71", "v72", "v73", "v74", "v75", "v76", "v77", "v78", "v79", "v80", "v81", "v82", "v83", "v84", "v85", "v86", "v87", "v88", "v89", "v90", "v91", "v92", "v93", "v94", "v95", "v96", "v97", "v98", "v99", "v100", "v101", "v102", "v103", "v104", "v105", "v106", "v107", "v108", "v109", "v110", "v111", "v112", "v113", "v114", "v115", "v116", "v117", "v118", "v119", "v120", "v121", "v122", "v123", "v124", "v125", "v126", "v127", "v128", "v129", "v130", "v131", "v132", "v133", "v134", "v135", "v136", "v137", "v138", "v139", "v140", "v141", "v142", "v143", "v144", "v145", "v146", "v147", "v148", "v149", "v150", "v151", "v152", "v153", "v154", "v155", "v156", "v157", "v158", "v159", "v160", "v161", "v162", "v163", "v164", "v165", "v166", "v167", "v168", "v169", "v170", "v171", "v172", "v173", "v174", "v175", "v176", "v177", "v178", "v179", "v180", "v181", "v182", "v183", "v184", "v185", "v186", "v187", "v188", "v189", "v190", "v191", "v192", "v193", "v194", "v195", "v196", "v197", "v198", "v199", "v200", "v201", "v202", "v203", "v204", "v205", "v206", "v207", "v208", "v209", "v210", "v211", "v212", "v213", "v214", "v215", "v216", "v217", "v218", "v219", "v220", "v221", "v222", "v223", "v224", "v225", "v226", "v227", "v228", "v229", "v230", "v231", "v232", "v233", "v234", "v235", "v236", "v237", "v238", "v239", "v240", "v241", "v242", "v243", "v244", "v245", "v246", "v247", "v248", "v249", "v250", "v251", "v252", "v253", "v254", "v255", "s3", "s4", "s5", "s6", "s7", "s8", "s9", "s10", "s11", "s12", "s13", "s14", "s15", "s16", "s17", "s18", "s19", "s20", "s21", "s22", "s23", "s24", "s25", "s26", "s27", "s28", "s29", "s30", "s31", "s34", "s35", "s36", "s37", "s38", "s39", "s40", "s41", "s42", "s43", "s44", "s45", "s46", "s47", "s48", "s49", "s50", "s51", "s52", "s53", "s54", "s55", "s56", "s57", "s58", "s59", "s60", "s61", "s62", "s63", "s64", "s65", "s66", "s67", "s68", "s69", "s70", "s71");
  asm volatile("" :: "v"((__attribute__((address_space(3))) char*)shm));
}

extern "C" void kernel_launch(void* const* d_in, const int* in_sizes, int n_in, void* d_out, int out_size,
                              void* d_ws, size_t ws_size, hipStream_t stream) {
  xd_kernel<<<256, 512, 0, stream>>>((const int*)d_in[0], (const u16*)d_in[4], (const u16*)d_in[1], (const u16*)d_in[5], (const u16*)d_in[2], (const u16*)d_in[3], (char*)d_ws,
                                     (const u16*)d_in[6], (const u16*)d_in[7], (u16*)d_out);
}
```

```cpp
#include <hip/hip_runtime.h>
typedef unsigned short u16;

__device__ const __attribute__((aligned(16), used, visibility("hidden"))) u16 xd_tab[5120] asm("xd_tab") = {924, 925, 928, 929, 926, 927, 930, 931, 934, 935, 938, 939, 932, 933, 936, 937, 940, 941, 944, 945, 942, 943, 946, 947, 948, 0, 949, 0, 0, 0, 0, 0, 2, 36, 138, 172, 0, 1, 24, 25, 338, 372, 474, 508, 34, 35, 57, 58, 546, 580, 682, 716, 68, 69, 90, 91, 816, 817, 850, 952, 102, 99, 123, 124, 680, 681, 684, 685, 20, 21, 22, 23, 714, 715, 717, 718, 54, 55, 56, 53, 748, 749, 750, 751, 88, 89, 86, 87, 782, 779, 783, 784, 122, 119, 120, 121, 152, 153, 154, 155, 284, 285, 286, 287, 186, 187, 188, 185, 318, 319, 320, 317, 220, 221, 218, 219, 352, 353, 350, 351, 254, 251, 252, 253, 386, 383, 384, 385, 416, 417, 418, 419, 548, 549, 550, 551, 450, 451, 452, 449, 582, 583, 584, 581, 484, 485, 482, 483, 616, 617, 614, 615, 518, 515, 516, 517, 650, 647, 648, 649, 544, 545, 552, 553, 16, 17, 18, 19, 578, 579, 585, 586, 50, 51, 52, 49, 612, 613, 618, 619, 84, 85, 82, 83, 646, 643, 651, 652, 118, 115, 116, 117, 148, 149, 150, 151, 280, 281, 282, 283, 182, 183, 184, 181, 314, 315, 316, 313, 216, 217, 214, 215, 348, 349, 346, 347, 250, 247, 248, 249, 382, 379, 380, 381, 412, 413, 414, 415, 408, 409, 420, 421, 446, 447, 448, 445, 442, 443, 453, 454, 480, 481, 478, 479, 476, 477, 486, 487, 514, 511, 512, 513, 510, 507, 519, 520, 12, 13, 14, 15, 144, 145, 146, 147, 46, 47, 48, 45, 178, 179, 180, 177, 80, 81, 78, 79, 212, 213, 210, 211, 114, 111, 112, 113, 246, 243, 244, 245, 276, 277, 278, 279, 272, 273, 288, 289, 310, 311, 312, 309, 306, 307, 321, 322, 344, 345, 342, 343, 340, 341, 354, 355, 378, 375, 376, 377, 374, 371, 387, 388, 8, 9, 10, 11, 140, 141, 142, 143, 42, 43, 44, 41, 174, 175, 176, 173, 76, 77, 74, 75, 208, 209, 206, 207, 110, 107, 108, 109, 242, 239, 240, 241, 136, 137, 156, 157, 4, 5, 6, 7, 170, 171, 189, 190, 38, 39, 40, 37, 204, 205, 222, 223, 72, 73, 70, 71, 238, 235, 255, 256, 106, 103, 104, 105, 0, 0, 0, 0, 0, 0, 0, 0, 0, 0, 0, 0, 0, 0, 0, 0, 0, 0, 0, 0, 0, 0, 0, 0, 0, 0, 0, 0, 0, 0, 0, 0, 0, 0, 0, 0, 0, 0, 0, 0, 0, 0, 0, 0, 0, 0, 0, 0, 0, 0, 0, 0, 0, 0, 0, 0, 0, 0, 0, 0, 0, 0, 0, 0, 0, 0, 0, 0, 0, 0, 0, 0, 0, 0, 0, 0, 0, 0, 0, 0, 0, 0, 0, 0, 0, 0, 0, 0, 0, 0, 0, 0, 0, 0, 0, 0, 0, 0, 0, 0, 0, 0, 0, 0, 0, 0, 0, 0, 0, 0, 0, 0, 0, 0, 0, 0, 0, 0, 0, 0, 0, 0, 0, 0, 0, 0, 0, 0, 924, 925, 926, 927, 934, 935, 932, 933, 940, 941, 942, 943, 948, 0, 0, 0, 2, 36, 0, 1, 338, 372, 34, 35, 546, 580, 68, 69, 816, 817, 102, 99, 928, 929, 930, 931, 938, 939, 936, 937, 944, 945, 946, 947, 949, 0, 0, 0, 138, 172, 24, 25, 474, 508, 57, 58, 682, 716, 90, 91, 850, 952, 123, 124, 680, 681, 20, 21, 714, 715, 54, 55, 748, 749, 88, 89, 782, 779, 122, 119, 152, 153, 284, 285, 186, 187, 318, 319, 220, 221, 352, 353, 254, 251, 386, 383, 684, 685, 22, 23, 717, 718, 56, 53, 750, 751, 86, 87, 783, 784, 120, 121, 154, 155, 286, 287, 188, 185, 320, 317, 218, 219, 350, 351, 252, 253, 384, 385, 416, 417, 548, 549, 450, 451, 582, 583, 484, 485, 616, 617, 518, 515, 650, 647, 544, 545, 16, 17, 578, 579, 50, 51, 612, 613, 84, 85, 646, 643, 118, 115, 418, 419, 550, 551, 452, 449, 584, 581, 482, 483, 614, 615, 516, 517, 648, 649, 552, 553, 18, 19, 585, 586, 52, 49, 618, 619, 82, 83, 651, 652, 116, 117, 148, 149, 280, 281, 182, 183, 314, 315, 216, 217, 348, 349, 250, 247, 382, 379, 412, 413, 408, 409, 446, 447, 442, 443, 480, 481, 476, 477, 514, 511, 510, 507, 150, 151, 282, 283, 184, 181, 316, 313, 214, 215, 346, 347, 248, 249, 380, 381, 414, 415, 420, 421, 448, 445, 453, 454, 478, 479, 486, 487, 512, 513, 519, 520, 12, 13, 144, 145, 46, 47, 178, 179, 80, 81, 212, 213, 114, 111, 246, 243, 276, 277, 272, 273, 310, 311, 306, 307, 344, 345, 340, 341, 378, 375, 374, 371, 14, 15, 146, 147, 48, 45, 180, 177, 78, 79, 210, 211, 112, 113, 244, 245, 278, 279, 288, 289, 312, 309, 321, 322, 342, 343, 354, 355, 376, 377, 387, 388, 8, 9, 140, 141, 42, 43, 174, 175, 76, 77, 208, 209, 110, 107, 242, 239, 136, 137, 4, 5, 170, 171, 38, 39, 204, 205, 72, 73, 238, 235, 106, 103, 10, 11, 142, 143, 44, 41, 176, 173, 74, 75, 206, 207, 108, 109, 240, 241, 156, 157, 6, 7, 189, 190, 40, 37, 222, 223, 70, 71, 255, 256, 104, 105, 0, 0, 0, 0, 0, 0, 0, 0, 0, 0, 0, 0, 0, 0, 0, 0, 0, 0, 0, 0, 0, 0, 0, 0, 0, 0, 0, 0, 0, 0, 0, 0, 0, 0, 0, 0, 0, 0, 0, 0, 0, 0, 0, 0, 0, 0, 0, 0, 0, 0, 0, 0, 0, 0, 0, 0, 0, 0, 0, 0, 0, 0, 0, 0, 0, 0, 0, 0, 0, 0, 0, 0, 0, 0, 0, 0, 0, 0, 0, 0, 0, 0, 0, 0, 0, 0, 0, 0, 0, 0, 0, 0, 0, 0, 0, 0, 0, 0, 0, 0, 0, 0, 0, 0, 0, 0, 0, 0, 0, 0, 0, 0, 0, 0, 0, 0, 0, 0, 0, 0, 0, 0, 0, 0, 0, 0, 0, 0, 0, 0, 0, 0, 0, 0, 0, 0, 0, 0, 0, 0, 0, 0, 0, 0, 0, 0, 0, 0, 0, 0, 0, 0, 0, 0, 0, 0, 0, 0, 0, 0, 0, 16256, 0, 16256, 0, 16256, 0, 16256, 0, 16256, 0, 16256, 0, 16256, 0, 16256, 0, 16256, 0, 16256, 0, 16256, 0, 16256, 0, 16128, 0, 16256, 0, 16128, 0, 0, 0, 16128, 0, 16256, 0, 16256, 0, 16256, 0, 16128, 0, 16256, 0, 16256, 0, 16256, 0, 16128, 0, 16256, 0, 16256, 0, 16256, 0, 16128, 0, 16256, 0, 16256, 0, 16256, 0, 16256, 0, 16256, 0, 16256, 0, 16256, 0, 16256, 0, 16256, 0, 16256, 0, 16256, 0, 16256, 0, 16256, 0, 16256, 0, 16256, 0, 16256, 0, 16256, 0, 16256, 0, 16256, 0, 16256, 0, 16256, 0, 16256, 0, 16256, 0, 16256, 0, 16256, 0, 16256, 0, 16256, 0, 16256, 0, 16256, 0, 16256, 0, 16256, 0, 16256, 0, 16256, 0, 16256, 0, 16256, 0, 16128, 0, 16256, 0, 16256, 0, 16256, 0, 16128, 0, 16256, 0, 16256, 0, 16256, 0, 16128, 0, 16256, 0, 16256, 0, 16256, 0, 16128, 0, 16256, 0, 16256, 0, 16256, 0, 16256, 0, 16256, 0, 16256, 0, 16256, 0, 16256, 0, 16256, 0, 16256, 0, 16256, 0, 16256, 0, 16256, 0, 16256, 0, 16256, 0, 16256, 0, 16256, 0, 16256, 0, 16256, 0, 16256, 0, 16256, 0, 16256, 0, 16256, 0, 16256, 0, 16256, 0, 16256, 0, 16256, 0, 16256, 0, 16256, 0, 16256, 0, 16256, 0, 16256, 0, 16256, 0, 16256, 0, 16256, 0, 16256, 0, 16256, 0, 16256, 0, 16256, 0, 16256, 0, 16256, 0, 16256, 0, 16256, 0, 16256, 0, 16256, 0, 16256, 0, 16256, 0, 16256, 0, 16256, 0, 16256, 0, 16256, 0, 16256, 0, 16256, 0, 16256, 0, 16256, 0, 16256, 0, 16256, 0, 16256, 0, 16256, 0, 16256, 0, 16256, 0, 16256, 0, 16256, 0, 16256, 0, 16256, 0, 16256, 0, 16256, 0, 16256, 0, 16256, 0, 16256, 0, 16256, 0, 16256, 0, 16256, 0, 16256, 0, 16256, 0, 16256, 0, 16256, 0, 16256, 0, 16256, 0, 16256, 0, 16256, 0, 16256, 0, 16256, 0, 16128, 0, 16256, 0, 16256, 0, 16256, 0, 16128, 0, 16256, 0, 16256, 0, 16256, 0, 16128, 0, 16256, 0, 16256, 0, 16256, 0, 16128, 0, 16256, 0, 16256, 0, 16256, 0, 0, 0, 0, 0, 0, 0, 0, 0, 0, 0, 0, 0, 0, 0, 0, 0, 0, 0, 0, 0, 0, 0, 0, 0, 0, 0, 0, 0, 0, 0, 0, 0, 0, 0, 0, 0, 0, 0, 0, 0, 0, 0, 0, 0, 0, 0, 0, 0, 0, 0, 0, 0, 0, 0, 0, 0, 0, 0, 0, 0, 0, 0, 0, 0, 0, 0, 0, 0, 0, 0, 0, 0, 0, 0, 0, 0, 0, 0, 0, 0, 0, 0, 0, 0, 0, 0, 0, 0, 0, 0, 0, 0, 0, 0, 0, 0, 0, 0, 0, 0, 0, 0, 0, 0, 0, 0, 0, 0, 0, 0, 0, 0, 0, 0, 0, 0, 0, 0, 0, 0, 0, 0, 0, 0, 0, 0, 0, 0, 0, 0, 0, 0, 0, 0, 0, 0, 0, 0, 0, 0, 0, 0, 0, 0, 0, 0, 0, 0, 0, 0, 0, 0, 0, 0, 0, 0, 0, 0, 0, 0, 16128, 0, 16256, 0, 16256, 0, 16256, 0, 16128, 0, 16256, 0, 16256, 0, 16256, 0, 16128, 0, 16256, 0, 16256, 0, 16256, 0, 16128, 0, 16256, 0, 16256, 0, 16256, 0, 16256, 0, 16256, 0, 16256, 0, 16256, 0, 16256, 0, 16256, 0, 16256, 0, 16256, 0, 16256, 0, 16256, 0, 16256, 0, 16256, 0, 16256, 0, 16256, 0, 16256, 0, 16256, 0, 16256, 0, 16256, 0, 16256, 0, 16256, 0, 16256, 0, 16256, 0, 16256, 0, 16256, 0, 16256, 0, 16256, 0, 16256, 0, 16256, 0, 16256, 0, 16256, 0, 16256, 0, 16256, 0, 16256, 0, 16256, 0, 16256, 0, 16256, 0, 16256, 0, 16256, 0, 16256, 0, 16256, 0, 16256, 0, 16256, 0, 16256, 0, 16256, 0, 16256, 0, 16256, 0, 16256, 0, 16256, 0, 16256, 0, 16256, 0, 16256, 0, 16256, 0, 16256, 0, 16256, 0, 16256, 0, 16256, 0, 16256, 0, 16256, 0, 16256, 0, 16256, 0, 16256, 0, 16256, 0, 16256, 0, 16256, 0, 16256, 0, 16256, 0, 16256, 0, 16256, 0, 16256, 0, 16256, 0, 16256, 0, 16256, 0, 16256, 0, 16256, 0, 16256, 0, 16256, 0, 16256, 0, 16256, 0, 16256, 0, 16256, 0, 16128, 0, 16256, 0, 16256, 0, 16256, 0, 16128, 0, 16256, 0, 16256, 0, 16256, 0, 16128, 0, 16256, 0, 16256, 0, 16256, 0, 16128, 0, 16256, 0, 16256, 0, 16256, 0, 16256, 0, 16256, 0, 16256, 0, 16256, 0, 16256, 0, 16256, 0, 16256, 0, 16256, 0, 16256, 0, 16256, 0, 16256, 0, 16256, 0, 16256, 0, 16256, 0, 16256, 0, 16256, 0, 16128, 0, 16256, 0, 16256, 0, 16256, 0, 16128, 0, 16256, 0, 16256, 0, 16256, 0, 16128, 0, 16256, 0, 16256, 0, 16256, 0, 16128, 0, 16256, 0, 16256, 0, 16256, 0, 16256, 0, 16256, 0, 16256, 0, 16256, 0, 16256, 0, 16256, 0, 16256, 0, 16256, 0, 16256, 0, 16256, 0, 16256, 0, 16256, 0, 16256, 0, 16256, 0, 16256, 0, 16256, 0, 16256, 0, 16256, 0, 16256, 0, 16256, 0, 16256, 0, 16256, 0, 16256, 0, 16256, 0, 16256, 0, 16256, 0, 16256, 0, 16256, 0, 16256, 0, 16256, 0, 16256, 0, 16256, 0, 0, 0, 0, 0, 0, 0, 0, 0, 0, 0, 0, 0, 0, 0, 0, 0, 0, 0, 0, 0, 0, 0, 0, 0, 0, 0, 0, 0, 0, 0, 0, 0, 0, 0, 0, 0, 0, 0, 0, 0, 0, 0, 0, 0, 0, 0, 0, 0, 0, 0, 0, 0, 0, 0, 0, 0, 0, 0, 0, 0, 0, 0, 0, 0, 0, 0, 0, 0, 0, 0, 0, 0, 0, 0, 0, 0, 0, 0, 0, 0, 0, 0, 0, 0, 0, 0, 0, 0, 0, 0, 0, 0, 0, 0, 0, 0, 0, 0, 0, 0, 0, 0, 0, 0, 0, 0, 0, 0, 0, 0, 0, 0, 0, 0, 0, 0, 0, 0, 0, 0, 0, 0, 0, 0, 0, 0, 0, 0, 16768, 0, 16768, 0, 16768, 0, 16768, 0, 16768, 0, 16768, 0, 16768, 0, 16768, 0, 16768, 0, 16768, 0, 16768, 0, 16768, 0, 16768, 0, 0, 0, 16768, 0, 0, 0, 16256, 0, 16256, 0, 16256, 0, 16256, 0, 16256, 0, 16256, 0, 16256, 0, 16256, 0, 16256, 0, 16256, 0, 16256, 0, 16256, 0, 16128, 0, 16256, 0, 16128, 0, 17280, 0, 16128, 0, 16256, 0, 16256, 0, 16256, 0, 16128, 0, 16256, 0, 16256, 0, 16256, 0, 16128, 0, 16256, 0, 16256, 0, 16256, 0, 16128, 0, 16256, 0, 16256, 0, 16256, 0, 16256, 0, 16256, 0, 16256, 0, 16256, 0, 16256, 0, 16256, 0, 16256, 0, 16256, 0, 16256, 0, 16256, 0, 16256, 0, 16256, 0, 16256, 0, 16256, 0, 16256, 0, 16256, 0, 16256, 0, 16256, 0, 16256, 0, 16256, 0, 16256, 0, 16256, 0, 16256, 0, 16256, 0, 16256, 0, 16256, 0, 16256, 0, 16256, 0, 16256, 0, 16256, 0, 16256, 0, 16256, 0, 16128, 0, 16256, 0, 16256, 0, 16256, 0, 16128, 0, 16256, 0, 16256, 0, 16256, 0, 16128, 0, 16256, 0, 16256, 0, 16256, 0, 16128, 0, 16256, 0, 16256, 0, 16256, 0, 16256, 0, 16256, 0, 16256, 0, 16256, 0, 16256, 0, 16256, 0, 16256, 0, 16256, 0, 16256, 0, 16256, 0, 16256, 0, 16256, 0, 16256, 0, 16256, 0, 16256, 0, 16256, 0, 16256, 0, 16256, 0, 16256, 0, 16256, 0, 16256, 0, 16256, 0, 16256, 0, 16256, 0, 16256, 0, 16256, 0, 16256, 0, 16256, 0, 16256, 0, 16256, 0, 16256, 0, 16256, 0, 16256, 0, 16256, 0, 16256, 0, 16256, 0, 16256, 0, 16256, 0, 16256, 0, 16256, 0, 16256, 0, 16256, 0, 16256, 0, 16256, 0, 16256, 0, 16256, 0, 16256, 0, 16256, 0, 16256, 0, 16256, 0, 16256, 0, 16256, 0, 16256, 0, 16256, 0, 16256, 0, 16256, 0, 16256, 0, 16256, 0, 16256, 0, 16256, 0, 16256, 0, 16256, 0, 16256, 0, 16256, 0, 16256, 0, 16256, 0, 16256, 0, 16256, 0, 16256, 0, 16256, 0, 16256, 0, 16256, 0, 16256, 0, 16256, 0, 16256, 0, 16256, 0, 16256, 0, 16256, 0, 16256, 0, 16256, 0, 16128, 0, 16256, 0, 16256, 0, 16256, 0, 16128, 0, 16256, 0, 16256, 0, 16256, 0, 16128, 0, 16256, 0, 16256, 0, 16256, 0, 16128, 0, 16256, 0, 16256, 0, 16256, 0, 0, 0, 0, 0, 0, 0, 0, 0, 0, 0, 0, 0, 0, 0, 0, 0, 0, 0, 0, 0, 0, 0, 0, 0, 0, 0, 0, 0, 0, 0, 0, 0, 0, 0, 0, 0, 0, 0, 0, 0, 0, 0, 0, 0, 0, 0, 0, 0, 0, 0, 0, 0, 0, 0, 0, 0, 0, 0, 0, 0, 0, 0, 0, 0, 0, 0, 0, 0, 0, 0, 0, 0, 0, 0, 0, 0, 0, 0, 0, 0, 0, 0, 0, 0, 0, 0, 0, 0, 0, 0, 0, 0, 0, 0, 0, 0, 0, 0, 0, 0, 0, 0, 0, 0, 0, 0, 0, 0, 0, 0, 0, 0, 0, 0, 0, 0, 0, 0, 0, 0, 0, 0, 0, 0, 0, 0, 0, 0, 16768, 0, 16768, 0, 16768, 0, 16768, 0, 16768, 0, 16768, 0, 16768, 0, 16768, 0, 16768, 0, 16768, 0, 16768, 0, 16768, 0, 0, 0, 0, 0, 0, 0, 0, 0, 16128, 0, 16256, 0, 16256, 0, 16256, 0, 16128, 0, 16256, 0, 16256, 0, 16256, 0, 16128, 0, 16256, 0, 16256, 0, 16256, 0, 16128, 0, 16256, 0, 16256, 0, 16256, 0, 16256, 0, 16256, 0, 16256, 0, 16256, 0, 16256, 0, 16256, 0, 16256, 0, 16256, 0, 16256, 0, 16256, 0, 16256, 0, 16256, 0, 16256, 0, 16256, 0, 16256, 0, 16256, 0, 16256, 0, 16256, 0, 16256, 0, 16256, 0, 16256, 0, 16256, 0, 16256, 0, 16256, 0, 16256, 0, 16256, 0, 16256, 0, 16256, 0, 16256, 0, 16256, 0, 16256, 0, 16256, 0, 16256, 0, 16256, 0, 16256, 0, 16256, 0, 16256, 0, 16256, 0, 16256, 0, 16256, 0, 16256, 0, 16256, 0, 16256, 0, 16256, 0, 16256, 0, 16256, 0, 16256, 0, 16256, 0, 16256, 0, 16256, 0, 16256, 0, 16256, 0, 16256, 0, 16256, 0, 16256, 0, 16256, 0, 16256, 0, 16256, 0, 16256, 0, 16256, 0, 16256, 0, 16256, 0, 16256, 0, 16256, 0, 16256, 0, 16256, 0, 16256, 0, 16256, 0, 16256, 0, 16256, 0, 16256, 0, 16256, 0, 16256, 0, 16256, 0, 16256, 0, 16256, 0, 16256, 0, 16256, 0, 16256, 0, 16256, 0, 16128, 0, 16256, 0, 16256, 0, 16256, 0, 16128, 0, 16256, 0, 16256, 0, 16256, 0, 16128, 0, 16256, 0, 16256, 0, 16256, 0, 16128, 0, 16256, 0, 16256, 0, 16256, 0, 16256, 0, 16256, 0, 16256, 0, 16256, 0, 16256, 0, 16256, 0, 16256, 0, 16256, 0, 16256, 0, 16256, 0, 16256, 0, 16256, 0, 16256, 0, 16256, 0, 16256, 0, 16256, 0, 16128, 0, 16256, 0, 16256, 0, 16256, 0, 16128, 0, 16256, 0, 16256, 0, 16256, 0, 16128, 0, 16256, 0, 16256, 0, 16256, 0, 16128, 0, 16256, 0, 16256, 0, 16256, 0, 16256, 0, 16256, 0, 16256, 0, 16256, 0, 16256, 0, 16256, 0, 16256, 0, 16256, 0, 16256, 0, 16256, 0, 16256, 0, 16256, 0, 16256, 0, 16256, 0, 16256, 0, 16256, 0, 16256, 0, 16256, 0, 16256, 0, 16256, 0, 16256, 0, 16256, 0, 16256, 0, 16256, 0, 16256, 0, 16256, 0, 16256, 0, 16256, 0, 16256, 0, 16256, 0, 16256, 0, 16256, 0, 0, 0, 0, 0, 0, 0, 0, 0, 0, 0, 0, 0, 0, 0, 0, 0, 0, 0, 0, 0, 0, 0, 0, 0, 0, 0, 0, 0, 0, 0, 0, 0, 0, 0, 0, 0, 0, 0, 0, 0, 0, 0, 0, 0, 0, 0, 0, 0, 0, 0, 0, 0, 0, 0, 0, 0, 0, 0, 0, 0, 0, 0, 0, 0, 0, 0, 0, 0, 0, 0, 0, 0, 0, 0, 0, 0, 0, 0, 0, 0, 0, 0, 0, 0, 0, 0, 0, 0, 0, 0, 0, 0, 0, 0, 0, 0, 0, 0, 0, 0, 0, 0, 0, 0, 0, 0, 0, 0, 0, 0, 0, 0, 0, 0, 0, 0, 0, 0, 0, 0, 0, 0, 0, 0, 0, 0, 0, 0, 0, 0, 0, 0, 0, 0, 0, 0, 0, 0, 0, 0, 0, 0, 0, 0, 16256, 0, 16256, 0, 16128, 0, 16256, 0, 16256, 0, 16256, 0, 16128, 0, 16256, 0, 0, 0, 0, 0, 0, 0, 0, 0, 0, 0, 0, 0, 0, 0, 0, 0, 16256, 0, 16256, 0, 16256, 0, 16256, 0, 16256, 0, 16256, 0, 16256, 0, 16256, 0, 16128, 0, 16256, 0, 16256, 0, 16256, 0, 16128, 0, 16256, 0, 16256, 0, 16256, 0, 16256, 0, 16256, 0, 16256, 0, 16256, 0, 16256, 0, 16256, 0, 16256, 0, 16256, 0, 16256, 0, 16256, 0, 16256, 0, 16256, 0, 16256, 0, 16256, 0, 16256, 0, 16256, 0, 16256, 0, 16256, 0, 16256, 0, 16256, 0, 16256, 0, 16256, 0, 16256, 0, 16256, 0, 16256, 0, 16256, 0, 16256, 0, 16256, 0, 16256, 0, 16256, 0, 16256, 0, 16256, 0, 16128, 0, 16256, 0, 16256, 0, 16256, 0, 16128, 0, 16256, 0, 16256, 0, 16256, 0, 16256, 0, 16256, 0, 16256, 0, 16256, 0, 16256, 0, 16256, 0, 16256, 0, 16256, 0, 16256, 0, 16256, 0, 16256, 0, 16256, 0, 16256, 0, 16256, 0, 16256, 0, 16256, 0, 16256, 0, 16256, 0, 16256, 0, 16256, 0, 16256, 0, 16256, 0, 16256, 0, 16256, 0, 16256, 0, 16256, 0, 16128, 0, 16256, 0, 16256, 0, 16256, 0, 16128, 0, 16256, 0, 16256, 0, 16256, 0, 16256, 0, 16256, 0, 16256, 0, 16256, 0, 16256, 0, 16256, 0, 16256, 0, 16256, 0, 16256, 0, 16256, 0, 16256, 0, 16256, 0, 16256, 0, 16256, 0, 16256, 0, 16256, 0, 16256, 0, 16256, 0, 16256, 0, 16256, 0, 16256, 0, 16256, 0, 16256, 0, 16256, 0, 16128, 0, 16256, 0, 16256, 0, 16256, 0, 16128, 0, 16256, 0, 16256, 0, 16256, 0, 16256, 0, 16256, 0, 16256, 0, 16256, 0, 16256, 0, 16256, 0, 16256, 0, 16256, 0, 16256, 0, 16256, 0, 16256, 0, 16256, 0, 16256, 0, 16256, 0, 16256, 0, 16256, 0, 16256, 0, 16256, 0, 16256, 0, 16256, 0, 16256, 0, 16256, 0, 16128, 0, 16256, 0, 16256, 0, 16256, 0, 16128, 0, 16256, 0, 16256, 0, 16256, 0, 16256, 0, 16256, 0, 16256, 0, 16256, 0, 16256, 0, 16256, 0, 16256, 0, 16256, 0, 16256, 0, 16256, 0, 16256, 0, 16256, 0, 16256, 0, 16256, 0, 16256, 0, 16256, 0, 0, 0, 0, 0, 0, 0, 0, 0, 0, 0, 0, 0, 0, 0, 0, 0, 0, 0, 0, 0, 0, 0, 0, 0, 0, 0, 0, 0, 0, 0, 0, 0, 0, 0, 0, 0, 0, 0, 0, 0, 0, 0, 0, 0, 0, 0, 0, 0, 0, 0, 0, 0, 0, 0, 0, 0, 0, 0, 0, 0, 0, 0, 0, 0, 0, 0, 0, 0, 0, 0, 0, 0, 0, 0, 0, 0, 0, 0, 0, 0, 0, 0, 0, 0, 0, 0, 0, 0, 0, 0, 0, 0, 0, 0, 0, 0, 0, 0, 0, 0, 0, 0, 0, 0, 0, 0, 0, 0, 0, 0, 0, 0, 0, 0, 0, 0, 0, 0, 0, 0, 0, 0, 0, 0, 0, 0, 0, 0, 0, 0, 0, 0, 0, 0, 0, 0, 0, 0, 0, 0, 0, 0, 0, 0, 16256, 0, 16256, 0, 16128, 0, 16256, 0, 16128, 0, 16256, 0, 16128, 0, 16256, 0, 0, 0, 0, 0, 0, 0, 0, 0, 0, 0, 0, 0, 0, 0, 0, 0, 16256, 0, 16256, 0, 16256, 0, 16256, 0, 16128, 0, 0, 0, 16256, 0, 16256, 0, 16128, 0, 16256, 0, 16256, 0, 16256, 0, 16128, 0, 16256, 0, 16256, 0, 16256, 0, 16256, 0, 16256, 0, 16256, 0, 16256, 0, 16256, 0, 16256, 0, 16256, 0, 16256, 0, 16256, 0, 16256, 0, 16256, 0, 16256, 0, 16256, 0, 16256, 0, 16256, 0, 16256, 0, 16256, 0, 16256, 0, 16256, 0, 16256, 0, 16256, 0, 16256, 0, 16256, 0, 16256, 0, 16256, 0, 16256, 0, 16256, 0, 16256, 0, 16256, 0, 16256, 0, 16256, 0, 16256, 0, 16128, 0, 16256, 0, 16256, 0, 16256, 0, 16128, 0, 16256, 0, 16256, 0, 16256, 0, 16256, 0, 16256, 0, 16256, 0, 16256, 0, 16256, 0, 16256, 0, 16256, 0, 16256, 0, 16256, 0, 16256, 0, 16256, 0, 16256, 0, 16256, 0, 16256, 0, 16256, 0, 16256, 0, 16256, 0, 16256, 0, 16256, 0, 16256, 0, 16256, 0, 16256, 0, 16256, 0, 16256, 0, 16256, 0, 16256, 0, 16128, 0, 16256, 0, 16256, 0, 16256, 0, 16128, 0, 16256, 0, 16256, 0, 16256, 0, 16256, 0, 16256, 0, 16256, 0, 16256, 0, 16256, 0, 16256, 0, 16256, 0, 16256, 0, 16256, 0, 16256, 0, 16256, 0, 16256, 0, 16256, 0, 16256, 0, 16256, 0, 16256, 0, 16256, 0, 16256, 0, 16256, 0, 16256, 0, 16256, 0, 16256, 0, 16256, 0, 16256, 0, 16128, 0, 16256, 0, 16256, 0, 16256, 0, 16128, 0, 16256, 0, 16256, 0, 16256, 0, 16256, 0, 16256, 0, 16256, 0, 16256, 0, 16256, 0, 16256, 0, 16256, 0, 16256, 0, 16256, 0, 16256, 0, 16256, 0, 16256, 0, 16256, 0, 16256, 0, 16256, 0, 16256, 0, 16256, 0, 16256, 0, 16256, 0, 16256, 0, 16256, 0, 16256, 0, 16128, 0, 16256, 0, 16256, 0, 16256, 0, 16128, 0, 16256, 0, 16256, 0, 16256, 0, 16256, 0, 16256, 0, 16256, 0, 16256, 0, 16256, 0, 16256, 0, 16256, 0, 16256, 0, 16256, 0, 16256, 0, 16256, 0, 16256, 0, 16256, 0, 16256, 0, 16256, 0, 16256, 0, 0, 0, 0, 0, 0, 0, 0, 0, 0, 0, 0, 0, 0, 0, 0, 0, 0, 0, 0, 0, 0, 0, 0, 0, 0, 0, 0, 0, 0, 0, 0, 0, 0, 0, 0, 0, 0, 0, 0, 0, 0, 0, 0, 0, 0, 0, 0, 0, 0, 0, 0, 0, 0, 0, 0, 0, 0, 0, 0, 0, 0, 0, 0, 0, 0, 0, 0, 0, 0, 0, 0, 0, 0, 0, 0, 0, 0, 0, 0, 0, 0, 0, 0, 0, 0, 0, 0, 0, 0, 0, 0, 0, 0, 0, 0, 0, 0, 0, 0, 0, 0, 0, 0, 0, 0, 0, 0, 0, 0, 0, 0, 0, 0, 0, 0, 0, 0, 0, 0, 0, 0, 0, 0, 0, 0, 0, 0, 0, 16768, 0, 16768, 0, 16768, 0, 16768, 0, 16768, 0, 16768, 0, 16768, 0, 16768, 0, 16256, 0, 16256, 0, 16128, 0, 16256, 0, 16256, 0, 16256, 0, 16128, 0, 16256, 0, 16768, 0, 16768, 0, 16768, 0, 16768, 0, 16768, 0, 16768, 0, 16768, 0, 16768, 0, 16256, 0, 16256, 0, 16256, 0, 16256, 0, 16256, 0, 16256, 0, 16256, 0, 16256, 0, 16128, 0, 16256, 0, 16256, 0, 16256, 0, 16128, 0, 16256, 0, 16256, 0, 16256, 0, 16256, 0, 16256, 0, 16256, 0, 16256, 0, 16256, 0, 16256, 0, 16256, 0, 16256, 0, 16256, 0, 16256, 0, 16256, 0, 16256, 0, 16256, 0, 16256, 0, 16256, 0, 16256, 0, 16256, 0, 16256, 0, 16256, 0, 16256, 0, 16256, 0, 16256, 0, 16256, 0, 16256, 0, 16256, 0, 16256, 0, 16256, 0, 16256, 0, 16256, 0, 16256, 0, 16256, 0, 16256, 0, 16128, 0, 16256, 0, 16256, 0, 16256, 0, 16128, 0, 16256, 0, 16256, 0, 16256, 0, 16256, 0, 16256, 0, 16256, 0, 16256, 0, 16256, 0, 16256, 0, 16256, 0, 16256, 0, 16256, 0, 16256, 0, 16256, 0, 16256, 0, 16256, 0, 16256, 0, 16256, 0, 16256, 0, 16256, 0, 16256, 0, 16256, 0, 16256, 0, 16256, 0, 16256, 0, 16256, 0, 16256, 0, 16256, 0, 16256, 0, 16128, 0, 16256, 0, 16256, 0, 16256, 0, 16128, 0, 16256, 0, 16256, 0, 16256, 0, 16256, 0, 16256, 0, 16256, 0, 16256, 0, 16256, 0, 16256, 0, 16256, 0, 16256, 0, 16256, 0, 16256, 0, 16256, 0, 16256, 0, 16256, 0, 16256, 0, 16256, 0, 16256, 0, 16256, 0, 16256, 0, 16256, 0, 16256, 0, 16256, 0, 16256, 0, 16256, 0, 16256, 0, 16128, 0, 16256, 0, 16256, 0, 16256, 0, 16128, 0, 16256, 0, 16256, 0, 16256, 0, 16256, 0, 16256, 0, 16256, 0, 16256, 0, 16256, 0, 16256, 0, 16256, 0, 16256, 0, 16256, 0, 16256, 0, 16256, 0, 16256, 0, 16256, 0, 16256, 0, 16256, 0, 16256, 0, 16256, 0, 16256, 0, 16256, 0, 16256, 0, 16256, 0, 16256, 0, 16128, 0, 16256, 0, 16256, 0, 16256, 0, 16128, 0, 16256, 0, 16256, 0, 16256, 0, 16256, 0, 16256, 0, 16256, 0, 16256, 0, 16256, 0, 16256, 0, 16256, 0, 16256, 0, 16256, 0, 16256, 0, 16256, 0, 16256, 0, 16256, 0, 16256, 0, 16256, 0, 16256, 0, 0, 0, 0, 0, 0, 0, 0, 0, 0, 0, 0, 0, 0, 0, 0, 0, 0, 0, 0, 0, 0, 0, 0, 0, 0, 0, 0, 0, 0, 0, 0, 0, 0, 0, 0, 0, 0, 0, 0, 0, 0, 0, 0, 0, 0, 0, 0, 0, 0, 0, 0, 0, 0, 0, 0, 0, 0, 0, 0, 0, 0, 0, 0, 0, 0, 0, 0, 0, 0, 0, 0, 0, 0, 0, 0, 0, 0, 0, 0, 0, 0, 0, 0, 0, 0, 0, 0, 0, 0, 0, 0, 0, 0, 0, 0, 0, 0, 0, 0, 0, 0, 0, 0, 0, 0, 0, 0, 0, 0, 0, 0, 0, 0, 0, 0, 0, 0, 0, 0, 0, 0, 0, 0, 0, 0, 0, 0, 0, 16768, 0, 16768, 0, 16768, 0, 16768, 0, 16768, 0, 0, 0, 0, 0, 0, 0, 16256, 0, 16256, 0, 16128, 0, 16256, 0, 16128, 0, 16256, 0, 16128, 0, 16256, 0, 16768, 0, 16768, 0, 16768, 0, 16768, 0, 16768, 0, 0, 0, 0, 0, 0, 0, 16256, 0, 16256, 0, 16256, 0, 16256, 0, 16128, 0, 17280, 0, 16256, 0, 16256, 0, 16128, 0, 16256, 0, 16256, 0, 16256, 0, 16128, 0, 16256, 0, 16256, 0, 16256, 0, 16256, 0, 16256, 0, 16256, 0, 16256, 0, 16256, 0, 16256, 0, 16256, 0, 16256, 0, 16256, 0, 16256, 0, 16256, 0, 16256, 0, 16256, 0, 16256, 0, 16256, 0, 16256, 0, 16256, 0, 16256, 0, 16256, 0, 16256, 0, 16256, 0, 16256, 0, 16256, 0, 16256, 0, 16256, 0, 16256, 0, 16256, 0, 16256, 0, 16256, 0, 16256, 0, 16256, 0, 16256, 0, 16128, 0, 16256, 0, 16256, 0, 16256, 0, 16128, 0, 16256, 0, 16256, 0, 16256, 0, 16256, 0, 16256, 0, 16256, 0, 16256, 0, 16256, 0, 16256, 0, 16256, 0, 16256, 0, 16256, 0, 16256, 0, 16256, 0, 16256, 0, 16256, 0, 16256, 0, 16256, 0, 16256, 0, 16256, 0, 16256, 0, 16256, 0, 16256, 0, 16256, 0, 16256, 0, 16256, 0, 16256, 0, 16256, 0, 16256, 0, 16128, 0, 16256, 0, 16256, 0, 16256, 0, 16128, 0, 16256, 0, 16256, 0, 16256, 0, 16256, 0, 16256, 0, 16256, 0, 16256, 0, 16256, 0, 16256, 0, 16256, 0, 16256, 0, 16256, 0, 16256, 0, 16256, 0, 16256, 0, 16256, 0, 16256, 0, 16256, 0, 16256, 0, 16256, 0, 16256, 0, 16256, 0, 16256, 0, 16256, 0, 16256, 0, 16256, 0, 16256, 0, 16128, 0, 16256, 0, 16256, 0, 16256, 0, 16128, 0, 16256, 0, 16256, 0, 16256, 0, 16256, 0, 16256, 0, 16256, 0, 16256, 0, 16256, 0, 16256, 0, 16256, 0, 16256, 0, 16256, 0, 16256, 0, 16256, 0, 16256, 0, 16256, 0, 16256, 0, 16256, 0, 16256, 0, 16256, 0, 16256, 0, 16256, 0, 16256, 0, 16256, 0, 16256, 0, 16128, 0, 16256, 0, 16256, 0, 16256, 0, 16128, 0, 16256, 0, 16256, 0, 16256, 0, 16256, 0, 16256, 0, 16256, 0, 16256, 0, 16256, 0, 16256, 0, 16256, 0, 16256, 0, 16256, 0, 16256, 0, 16256, 0, 16256, 0, 16256, 0, 16256, 0, 16256, 0, 16256, 0, 0, 0, 0, 0, 0, 0, 0, 0, 0, 0, 0, 0, 0, 0, 0, 0, 0, 0, 0, 0, 0, 0, 0, 0, 0, 0, 0, 0, 0, 0, 0, 0, 0, 0, 0, 0, 0, 0, 0, 0, 0, 0, 0, 0, 0, 0, 0, 0, 0, 0, 0, 0, 0, 0, 0, 0, 0, 0, 0, 0, 0, 0, 0, 0, 0, 0, 0, 0, 0, 0, 0, 0, 0, 0, 0, 0, 0, 0, 0, 0, 0, 0, 0, 0, 0, 0, 0, 0, 0, 0, 0, 0, 0, 0, 0, 0, 0, 0, 0, 0, 0, 0, 0, 0, 0, 0, 0, 0, 0, 0, 0, 0, 0, 0, 0, 0, 0, 0, 0, 0, 0, 0, 0, 0, 0, 0, 0};

__global__ __launch_bounds__(512) void xd_kernel(const int* idx, const u16* w1, const u16* table, const u16* b1, const u16* ws_cin, const u16* wout, char* ws, const u16* w2, const u16* b_out, u16* out) {
  __shared__ __attribute__((aligned(1024))) char shm[163840];
  unsigned tid = threadIdx.x;
  asm volatile(
    "s_load_dwordx16 s[4:19], s[0:1], 0\n"
    ".Lxd_top:\n"
    "v_readfirstlane_b32 s43, v0\n"
    "s_setprio 3\n"
    "s_cmp_lt_u32 s2, 8\n"
    "s_cbranch_scc1 .Lxd_s2\n"
    "s_cmp_lt_u32 s2, 48\n"
    "s_cbranch_scc1 .Lxd_help\n"
    "s_and_b32 s24, s2, 15\n"
    "s_mul_i32 s40, s43, 36\n"
    "s_mul_i32 s41, s24, 26624\n"
    "s_add_u32 s38, s40, s41\n"
    "v_lshlrev_b32 v86, 4, v0\n"
    "s_lshl_b32 s34, s43, 6\n"
    "s_add_u32 m0, s34, 98304\n"
    "s_waitcnt lgkmcnt(0)\n"
    "s_add_u32 s38, s4, s38\n"
    "s_addc_u32 s39, s5, 0\n"
    "global_load_lds_dwordx4 v86, s[38:39] offset:0\n"
    "global_load_lds_dwordx4 v86, s[38:39] offset:1024\n"
    "global_load_lds_dwordx4 v86, s[38:39] offset:2048\n"
    "global_load_lds_dwordx4 v86, s[38:39] offset:2304\n"
    "s_sub_u32 s3, s2, 48\n"
    "s_lshl_b32 s40, s2, 2\n"
    "s_lshr_b32 s23, s3, 4\n"
    "s_lshr_b32 s40, 0xa8864220, s40\n"
    "s_and_b32 s42, s40, 14\n"
    "v_bfe_u32 v85, v0, 4, 2\n"
    "v_lshlrev_b32 v87, 1, v0\n"
    "v_and_b32 v87, 0xe0, v87\n"
    "v_and_b32 v88, 0xf0, v86\n"
    "v_xor_b32 v87, v87, v88\n"
    "s_movk_i32 s41, 3328\n"
    "v_mad_u32_u24 v46, v85, s41, v87\n"
    "v_add_u32 v47, 12288, v46\n"
    "s_mul_i32 s41, s43, 416\n"
    "s_lshl_b32 s40, s23, 8\n"
    "s_add_u32 s41, s41, s40\n"
    "s_mul_i32 s30, s42, 425984\n"
    "s_lshl_b32 s35, s43, 5\n"
    "s_bitset1_b32 s35, 15\n"
    "s_add_u32 s6, s6, s41\n"
    "s_addc_u32 s7, s7, 0\n"
    "s_add_u32 s30, s6, s30\n"
    "s_addc_u32 s31, s7, 0\n"
    "s_add_u32 m0, s35, 0\n"
    "s_nop 0\n"
    "global_load_lds_dwordx4 v46, s[30:31] offset:0\n"
    "global_load_lds_dwordx4 v47, s[30:31] offset:1024\n"
    "s_add_u32 s30, s30, 212992\n"
    "s_addc_u32 s31, s31, 0\n"
    "s_setprio 2\n"
    "v_and_b32 v84, 3, v0\n"
    "v_bfe_u32 v44, v0, 3, 3\n"
    "v_lshl_add_u32 v44, v84, 3, v44\n"
    "s_add_u32 s40, s34, 98304\n"
    "v_mul_u32_u24 v44, 104, v44\n"
    "v_add_u32 v44, s40, v44\n"
    "v_lshl_add_u32 v45, s42, 3, v44\n"
    "v_xor_b32 v51, v0, v86\n"
    "v_and_b32 v86, 0x70, v86\n"
    "v_bfi_b32 v86, 48, v51, v86\n"
    "v_xor_b32 v51, 64, v86\n"
    "v_add_u32 v48, 3072, v86\n"
    "v_add_u32 v49, 2048, v51\n"
    "v_add_u32 v50, 1024, v86\n"
    "s_sub_u32 s36, s8, 3072\n"
    "s_subb_u32 s37, s9, 0\n"
    "s_add_u32 m0, s35, 49152\n"
    "s_nop 0\n"
    "global_load_lds_dwordx4 v46, s[30:31] offset:0\n"
    "global_load_lds_dwordx4 v47, s[30:31] offset:1024\n"
    "s_add_u32 s30, s30, 212992\n"
    "s_addc_u32 s31, s31, 0\n"
    "s_add_u32 m0, s35, 98304\n"
    "s_nop 0\n"
    "global_load_lds_dwordx4 v46, s[30:31] offset:0\n"
    "global_load_lds_dwordx4 v47, s[30:31] offset:1024\n"
    "s_add_u32 s30, s30, 212992\n"
    "s_addc_u32 s31, s31, 0\n"
    "s_waitcnt vmcnt(6)\n"
    "ds_read2_b64 v[96:99], v45 offset1:1\n"
    "s_lshr_b32 s20, s43, 6\n"
    "s_lshr_b32 s21, s43, 8\n"
    "v_and_b32 v1, 63, v0\n"
    "s_waitcnt lgkmcnt(0)\n"
    "v_lshlrev_b32 v96, 7, v96\n"
    "v_lshlrev_b32 v97, 7, v97\n"
    "v_lshlrev_b32 v98, 7, v98\n"
    "v_add_u32_dpp v52, v96, v48 quad_perm:[0,0,0,0] row_mask:0xf bank_mask:0xf\n"
    "v_add_u32_dpp v53, v96, v49 quad_perm:[1,1,1,1] row_mask:0xf bank_mask:0xf\n"
    "v_add_u32_dpp v54, v96, v50 quad_perm:[2,2,2,2] row_mask:0xf bank_mask:0xf\n"
    "v_add_u32_dpp v55, v96, v51 quad_perm:[3,3,3,3] row_mask:0xf bank_mask:0xf\n"
    "s_add_u32 m0, s34, 0\n"
    "s_nop 0\n"
    "global_load_lds_dwordx4 v52, s[36:37] offset:0\n"
    "global_load_lds_dwordx4 v53, s[36:37] offset:1024\n"
    "global_load_lds_dwordx4 v54, s[36:37] offset:2048\n"
    "global_load_lds_dwordx4 v55, s[36:37] offset:3072\n"
    "s_setprio 1\n"
    "v_add_u32_dpp v52, v97, v48 quad_perm:[0,0,0,0] row_mask:0xf bank_mask:0xf\n"
    "v_add_u32_dpp v53, v97, v49 quad_perm:[1,1,1,1] row_mask:0xf bank_mask:0xf\n"
    "v_add_u32_dpp v54, v97, v50 quad_perm:[2,2,2,2] row_mask:0xf bank_mask:0xf\n"
    "v_add_u32_dpp v55, v97, v51 quad_perm:[3,3,3,3] row_mask:0xf bank_mask:0xf\n"
    "s_load_dwordx4 s[68:71], s[0:1], 64\n"
    "s_lshl2_add_u32 s41, s42, 8\n"
    "s_mov_b32 s28, 0x30201000\n"
    "s_movk_i32 s29, 0x5040\n"
    "s_lshr_b64 s[42:43], s[28:29], s41\n"
    "s_sub_u32 s41, 48, s41\n"
    "s_lshl_b64 s[28:29], s[28:29], s41\n"
    "s_or_b64 s[28:29], s[28:29], s[42:43]\n"
    "s_and_b32 s29, s29, 0xff\n"
    "s_or_b32 s29, s29, 0x6000\n"
    "s_bfe_u32 s40, s28, 0x80000\n"
    "v_add_u32 v45, s40, v44\n"
    "ds_read2_b64 v[100:103], v45 offset1:1\n"
    "s_bfe_u32 s40, s28, 0x80008\n"
    "v_add_u32 v45, s40, v44\n"
    "ds_read2_b64 v[104:107], v45 offset1:1\n"
    "s_bfe_u32 s40, s28, 0x80010\n"
    "v_add_u32 v45, s40, v44\n"
    "ds_read2_b64 v[108:111], v45 offset1:1\n"
    "s_bfe_u32 s40, s28, 0x80018\n"
    "v_add_u32 v45, s40, v44\n"
    "ds_read2_b64 v[112:115], v45 offset1:1\n"
    "s_bfe_u32 s40, s29, 0x80000\n"
    "v_add_u32 v45, s40, v44\n"
    "ds_read2_b64 v[116:119], v45 offset1:1\n"
    "s_bfe_u32 s40, s29, 0x80008\n"
    "v_add_u32 v45, s40, v44\n"
    "ds_read_b64 v[120:121], v45\n"
    "v_lshrrev_b32 v86, 5, v88\n"
    "v_xor_b32 v86, v86, v85\n"
    "v_lshlrev_b32 v86, 4, v86\n"
    "v_lshl_add_u32 v86, v88, 3, v86\n"
    "s_and_b32 s41, s20, 1\n"
    "s_lshl1_add_u32 s40, s21, s41\n"
    "v_lshl_add_u32 v56, s40, 13, v86\n"
    "v_xor_b32 v57, 64, v56\n"
    "v_lshrrev_b32 v86, 6, v88\n"
    "v_lshl_add_u32 v88, v85, 3, v86\n"
    "v_bfe_u32 v86, v0, 2, 3\n"
    "s_and_b32 s40, s20, 2\n"
    "s_lshl1_add_u32 s40, s40, 1024\n"
    "v_xor_b32 v86, s40, v86\n"
    "v_lshlrev_b32 v86, 5, v86\n"
    "v_lshl_add_u32 v86, v88, 8, v86\n"
    "v_lshl_add_u32 v76, v84, 3, v86\n"
    "v_xor_b32 v77, 32, v76\n"
    "v_xor_b32 v78, 64, v76\n"
    "v_xor_b32 v79, 96, v76\n"
    "v_lshlrev_b32 v92, 2, v0\n"
    "v_add_u32_e64 v93, s21, -1\n"
    "v_add_u32 v92, 149504, v92\n"
    "ds_write_b32 v92, v93\n"
    "v_lshlrev_b32 v99, 7, v99\n"
    "s_setprio 0\n"
    "s_cmp_eq_u32 s21, 0\n"
    "s_cbranch_scc1 .Lxd_g0\n"
    "s_waitcnt vmcnt(8)\n"
    "s_barrier\n"
    ".Lxd_g0:\n"
    "s_add_u32 m0, s34, 49152\n"
    "s_nop 0\n"
    "global_load_lds_dwordx4 v52, s[36:37] offset:0\n"
    "global_load_lds_dwordx4 v53, s[36:37] offset:1024\n"
    "global_load_lds_dwordx4 v54, s[36:37] offset:2048\n"
    "global_load_lds_dwordx4 v55, s[36:37] offset:3072\n"
    "v_add_u32_dpp v52, v98, v48 quad_perm:[0,0,0,0] row_mask:0xf bank_mask:0xf\n"
    "v_add_u32_dpp v53, v98, v49 quad_perm:[1,1,1,1] row_mask:0xf bank_mask:0xf\n"
    "v_add_u32_dpp v54, v98, v50 quad_perm:[2,2,2,2] row_mask:0xf bank_mask:0xf\n"
    "v_add_u32_dpp v55, v98, v51 quad_perm:[3,3,3,3] row_mask:0xf bank_mask:0xf\n"
    "s_waitcnt vmcnt(4) lgkmcnt(0)\n"
    "s_barrier\n"
    "ds_read_b128 v[128:131], v56 offset:0\n"
    "ds_read_b128 v[132:135], v56 offset:2048\n"
    "ds_read_b128 v[136:139], v56 offset:4096\n"
    "ds_read_b128 v[140:143], v56 offset:6144\n"
    "ds_read_b64_tr_b16 v[144:145], v76 offset:0\n"
    "ds_read_b64_tr_b16 v[146:147], v76 offset:1024\n"
    "ds_read_b64_tr_b16 v[148:149], v77 offset:0\n"
    "ds_read_b64_tr_b16 v[150:151], v77 offset:1024\n"
    "ds_read_b64_tr_b16 v[152:153], v78 offset:0\n"
    "ds_read_b64_tr_b16 v[154:155], v78 offset:1024\n"
    "ds_read_b64_tr_b16 v[156:157], v79 offset:0\n"
    "ds_read_b64_tr_b16 v[158:159], v79 offset:1024\n"
    "ds_read_b128 v[160:163], v57 offset:0\n"
    "ds_read_b128 v[164:167], v57 offset:2048\n"
    "ds_read_b128 v[168:171], v57 offset:4096\n"
    "ds_read_b128 v[172:175], v57 offset:6144\n"
    "ds_read_b64_tr_b16 v[176:177], v76 offset:8192\n"
    "ds_read_b64_tr_b16 v[178:179], v76 offset:9216\n"
    "ds_read_b64_tr_b16 v[180:181], v77 offset:8192\n"
    "ds_read_b64_tr_b16 v[182:183], v77 offset:9216\n"
    "ds_read_b64_tr_b16 v[184:185], v78 offset:8192\n"
    "ds_read_b64_tr_b16 v[186:187], v78 offset:9216\n"
    "ds_read_b64_tr_b16 v[188:189], v79 offset:8192\n"
    "ds_read_b64_tr_b16 v[190:191], v79 offset:9216\n"
    "s_add_u32 m0, s34, 98304\n"
    "s_nop 0\n"
    "global_load_lds_dwordx4 v52, s[36:37] offset:0\n"
    "global_load_lds_dwordx4 v53, s[36:37] offset:1024\n"
    "global_load_lds_dwordx4 v54, s[36:37] offset:2048\n"
    "global_load_lds_dwordx4 v55, s[36:37] offset:3072\n"
    "s_waitcnt lgkmcnt(0)\n"
    "s_barrier\n"
    "v_mfma_f32_16x16x32_bf16 v[192:195], v[144:147], v[128:131], 0\n"
    "v_lshlrev_b32 v100, 7, v100\n"
    "v_mfma_f32_16x16x32_bf16 v[192:195], v[176:179], v[160:163], v[192:195]\n"
    "v_add_u32_dpp v52, v99, v48 quad_perm:[0,0,0,0] row_mask:0xf bank_mask:0xf\n"
    "v_mfma_f32_16x16x32_bf16 v[196:199], v[176:179], v[164:167], 0\n"
    "v_mfma_f32_16x16x32_bf16 v[196:199], v[144:147], v[132:135], v[196:199]\n"
    "v_add_u32_dpp v53, v99, v49 quad_perm:[1,1,1,1] row_mask:0xf bank_mask:0xf\n"
    "v_mfma_f32_16x16x32_bf16 v[200:203], v[144:147], v[136:139], 0\n"
    "v_mfma_f32_16x16x32_bf16 v[200:203], v[176:179], v[168:171], v[200:203]\n"
    "v_add_u32_dpp v54, v99, v50 quad_perm:[2,2,2,2] row_mask:0xf bank_mask:0xf\n"
    "v_mfma_f32_16x16x32_bf16 v[204:207], v[176:179], v[172:175], 0\n"
    "v_mfma_f32_16x16x32_bf16 v[204:207], v[144:147], v[140:143], v[204:207]\n"
    "v_add_u32_dpp v55, v99, v51 quad_perm:[3,3,3,3] row_mask:0xf bank_mask:0xf\n"
    "v_mfma_f32_16x16x32_bf16 v[220:223], v[148:151], v[140:143], 0\n"
    "v_mfma_f32_16x16x32_bf16 v[220:223], v[180:183], v[172:175], v[220:223]\n"
    "s_bfe_u32 s40, s34, 0x1000d\n"
    "v_mfma_f32_16x16x32_bf16 v[216:219], v[180:183], v[168:171], 0\n"
    "v_mfma_f32_16x16x32_bf16 v[216:219], v[148:151], v[136:139], v[216:219]\n"
    "s_lshl1_add_u32 s40, s23, s40\n"
    "v_mfma_f32_16x16x32_bf16 v[212:215], v[148:151], v[132:135], 0\n"
    "v_mfma_f32_16x16x32_bf16 v[212:215], v[180:183], v[164:167], v[212:215]\n"
    "s_lshl_b32 s40, s40, 7\n"
    "v_mfma_f32_16x16x32_bf16 v[208:211], v[180:183], v[160:163], 0\n"
    "v_mfma_f32_16x16x32_bf16 v[208:211], v[148:151], v[128:131], v[208:211]\n"
    "v_lshl_add_u32 v42, v85, 3, s40\n"
    "v_mfma_f32_16x16x32_bf16 v[224:227], v[152:155], v[128:131], 0\n"
    "v_mfma_f32_16x16x32_bf16 v[224:227], v[184:187], v[160:163], v[224:227]\n"
    "s_lshl_b32 s24, s24, 8\n"
    "v_mfma_f32_16x16x32_bf16 v[228:231], v[184:187], v[164:167], 0\n"
    "v_mfma_f32_16x16x32_bf16 v[228:231], v[152:155], v[132:135], v[228:231]\n"
    "v_add_u32 v58, 98304, v56\n"
    "v_mfma_f32_16x16x32_bf16 v[232:235], v[152:155], v[136:139], 0\n"
    "v_mfma_f32_16x16x32_bf16 v[232:235], v[184:187], v[168:171], v[232:235]\n"
    "v_add_u32 v59, 98304, v57\n"
    "v_mfma_f32_16x16x32_bf16 v[236:239], v[184:187], v[172:175], 0\n"
    "v_mfma_f32_16x16x32_bf16 v[236:239], v[152:155], v[140:143], v[236:239]\n"
    "v_add_u32 v80, 98304, v76\n"
    "v_mfma_f32_16x16x32_bf16 v[252:255], v[156:159], v[140:143], 0\n"
    "v_mfma_f32_16x16x32_bf16 v[252:255], v[188:191], v[172:175], v[252:255]\n"
    "v_add_u32 v81, 98304, v77\n"
    "v_mfma_f32_16x16x32_bf16 v[248:251], v[188:191], v[168:171], 0\n"
    "v_mfma_f32_16x16x32_bf16 v[248:251], v[156:159], v[136:139], v[248:251]\n"
    "v_add_u32 v82, 98304, v78\n"
    "v_mfma_f32_16x16x32_bf16 v[244:247], v[156:159], v[132:135], 0\n"
    "v_mfma_f32_16x16x32_bf16 v[244:247], v[188:191], v[164:167], v[244:247]\n"
    "v_add_u32 v83, 98304, v79\n"
    "v_mfma_f32_16x16x32_bf16 v[240:243], v[188:191], v[160:163], 0\n"
    "v_mfma_f32_16x16x32_bf16 v[240:243], v[156:159], v[128:131], v[240:243]\n"
    "s_waitcnt vmcnt(4)\n"
    "s_barrier\n"
    "s_add_u32 m0, s35, 0\n"
    "s_nop 0\n"
    "global_load_lds_dwordx4 v46, s[30:31] offset:0\n"
    "ds_read_b128 v[128:131], v56 offset:49152\n"
    "ds_read_b128 v[132:135], v56 offset:51200\n"
    "ds_read_b128 v[136:139], v56 offset:53248\n"
    "ds_read_b128 v[140:143], v56 offset:55296\n"
    "global_load_lds_dwordx4 v47, s[30:31] offset:1024\n"
    "s_bfe_u32 s66, s28, 0x80000\n"
    "s_mul_i32 s66, s66, 53248\n"
    "s_add_u32 s30, s6, s66\n"
    "s_addc_u32 s31, s7, 0\n"
    "ds_read_b64_tr_b16 v[144:145], v76 offset:49152\n"
    "ds_read_b64_tr_b16 v[146:147], v76 offset:50176\n"
    "ds_read_b64_tr_b16 v[148:149], v77 offset:49152\n"
    "ds_read_b64_tr_b16 v[150:151], v77 offset:50176\n"
    "s_add_u32 m0, s34, 0\n"
    "s_nop 0\n"
    "global_load_lds_dwordx4 v52, s[36:37] offset:0\n"
    "ds_read_b64_tr_b16 v[152:153], v78 offset:49152\n"
    "ds_read_b64_tr_b16 v[154:155], v78 offset:50176\n"
    "ds_read_b64_tr_b16 v[156:157], v79 offset:49152\n"
    "ds_read_b64_tr_b16 v[158:159], v79 offset:50176\n"
    "global_load_lds_dwordx4 v53, s[36:37] offset:1024\n"
    "ds_read_b128 v[160:163], v57 offset:49152\n"
    "ds_read_b128 v[164:167], v57 offset:51200\n"
    "ds_read_b128 v[168:171], v57 offset:53248\n"
    "ds_read_b128 v[172:175], v57 offset:55296\n"
    "global_load_lds_dwordx4 v54, s[36:37] offset:2048\n"
    "ds_read_b64_tr_b16 v[176:177], v76 offset:57344\n"
    "ds_read_b64_tr_b16 v[178:179], v76 offset:58368\n"
    "ds_read_b64_tr_b16 v[180:181], v77 offset:57344\n"
    "ds_read_b64_tr_b16 v[182:183], v77 offset:58368\n"
    "global_load_lds_dwordx4 v55, s[36:37] offset:3072\n"
    "ds_read_b64_tr_b16 v[184:185], v78 offset:57344\n"
    "ds_read_b64_tr_b16 v[186:187], v78 offset:58368\n"
    "ds_read_b64_tr_b16 v[188:189], v79 offset:57344\n"
    "ds_read_b64_tr_b16 v[190:191], v79 offset:58368\n"
    "s_waitcnt lgkmcnt(0)\n"
    "s_barrier\n"
    "v_mfma_f32_16x16x32_bf16 v[192:195], v[144:147], v[128:131], v[192:195]\n"
    "v_lshlrev_b32 v101, 7, v101\n"
    "v_mfma_f32_16x16x32_bf16 v[192:195], v[176:179], v[160:163], v[192:195]\n"
    "v_mfma_f32_16x16x32_bf16 v[196:199], v[176:179], v[164:167], v[196:199]\n"
    "v_add_u32_dpp v52, v100, v48 quad_perm:[0,0,0,0] row_mask:0xf bank_mask:0xf\n"
    "v_mfma_f32_16x16x32_bf16 v[196:199], v[144:147], v[132:135], v[196:199]\n"
    "v_mfma_f32_16x16x32_bf16 v[200:203], v[144:147], v[136:139], v[200:203]\n"
    "v_add_u32_dpp v53, v100, v49 quad_perm:[1,1,1,1] row_mask:0xf bank_mask:0xf\n"
    "v_mfma_f32_16x16x32_bf16 v[200:203], v[176:179], v[168:171], v[200:203]\n"
    "v_mfma_f32_16x16x32_bf16 v[204:207], v[176:179], v[172:175], v[204:207]\n"
    "v_add_u32_dpp v54, v100, v50 quad_perm:[2,2,2,2] row_mask:0xf bank_mask:0xf\n"
    "v_mfma_f32_16x16x32_bf16 v[204:207], v[144:147], v[140:143], v[204:207]\n"
    "v_mfma_f32_16x16x32_bf16 v[220:223], v[148:151], v[140:143], v[220:223]\n"
    "v_add_u32_dpp v55, v100, v51 quad_perm:[3,3,3,3] row_mask:0xf bank_mask:0xf\n"
    "v_mfma_f32_16x16x32_bf16 v[220:223], v[180:183], v[172:175], v[220:223]\n"
    "v_mfma_f32_16x16x32_bf16 v[216:219], v[180:183], v[168:171], v[216:219]\n"
    "s_mul_i32 s47, s3, 5042\n"
    "v_mfma_f32_16x16x32_bf16 v[216:219], v[148:151], v[136:139], v[216:219]\n"
    "v_mfma_f32_16x16x32_bf16 v[212:215], v[148:151], v[132:135], v[212:215]\n"
    "s_lshr_b32 s47, s47, 16\n"
    "v_mfma_f32_16x16x32_bf16 v[212:215], v[180:183], v[164:167], v[212:215]\n"
    "v_mfma_f32_16x16x32_bf16 v[208:211], v[180:183], v[160:163], v[208:211]\n"
    "s_lshr_b32 s21, s20, 2\n"
    "v_mfma_f32_16x16x32_bf16 v[208:211], v[148:151], v[128:131], v[208:211]\n"
    "v_mfma_f32_16x16x32_bf16 v[224:227], v[152:155], v[128:131], v[224:227]\n"
    "s_lshl1_add_u32 s22, s21, s20\n"
    "v_mfma_f32_16x16x32_bf16 v[224:227], v[184:187], v[160:163], v[224:227]\n"
    "v_mfma_f32_16x16x32_bf16 v[228:231], v[184:187], v[164:167], v[228:231]\n"
    "s_and_b32 s22, s22, 3\n"
    "v_mfma_f32_16x16x32_bf16 v[228:231], v[152:155], v[132:135], v[228:231]\n"
    "v_mfma_f32_16x16x32_bf16 v[232:235], v[152:155], v[136:139], v[232:235]\n"
    "s_lshl_b32 s58, s21, 8\n"
    "v_mfma_f32_16x16x32_bf16 v[232:235], v[184:187], v[168:171], v[232:235]\n"
    "v_mfma_f32_16x16x32_bf16 v[236:239], v[184:187], v[172:175], v[236:239]\n"
    "s_add_u32 s58, s58, 150528\n"
    "v_mfma_f32_16x16x32_bf16 v[236:239], v[152:155], v[140:143], v[236:239]\n"
    "v_mfma_f32_16x16x32_bf16 v[252:255], v[156:159], v[140:143], v[252:255]\n"
    "s_mov_b32 s40, s47\n"
    "v_mfma_f32_16x16x32_bf16 v[252:255], v[188:191], v[172:175], v[252:255]\n"
    "v_mfma_f32_16x16x32_bf16 v[248:251], v[188:191], v[168:171], v[248:251]\n"
    "s_mul_i32 s54, s40, 512\n"
    "v_mfma_f32_16x16x32_bf16 v[248:251], v[156:159], v[136:139], v[248:251]\n"
    "v_mfma_f32_16x16x32_bf16 v[244:247], v[156:159], v[132:135], v[244:247]\n"
    "s_add_u32 s54, s54, 2\n"
    "v_mfma_f32_16x16x32_bf16 v[244:247], v[188:191], v[164:167], v[244:247]\n"
    "v_mfma_f32_16x16x32_bf16 v[240:243], v[188:191], v[160:163], v[240:243]\n"
    "v_mfma_f32_16x16x32_bf16 v[240:243], v[156:159], v[128:131], v[240:243]\n"
    "s_waitcnt vmcnt(6)\n"
    "s_barrier\n"
    "s_add_u32 m0, s35, 49152\n"
    "s_nop 0\n"
    "global_load_lds_dwordx4 v46, s[30:31] offset:0\n"
    "ds_read_b128 v[128:131], v58 offset:0\n"
    "ds_read_b128 v[132:135], v58 offset:2048\n"
    "ds_read_b128 v[136:139], v58 offset:4096\n"
    "ds_read_b128 v[140:143], v58 offset:6144\n"
    "global_load_lds_dwordx4 v47, s[30:31] offset:1024\n"
    "s_add_u32 s30, s30, 212992\n"
    "s_addc_u32 s31, s31, 0\n"
    "ds_read_b64_tr_b16 v[144:145], v80 offset:0\n"
    "ds_read_b64_tr_b16 v[146:147], v80 offset:1024\n"
    "ds_read_b64_tr_b16 v[148:149], v81 offset:0\n"
    "ds_read_b64_tr_b16 v[150:151], v81 offset:1024\n"
    "s_add_u32 m0, s34, 49152\n"
    "s_nop 0\n"
    "global_load_lds_dwordx4 v52, s[36:37] offset:0\n"
    "ds_read_b64_tr_b16 v[152:153], v82 offset:0\n"
    "ds_read_b64_tr_b16 v[154:155], v82 offset:1024\n"
    "ds_read_b64_tr_b16 v[156:157], v83 offset:0\n"
    "ds_read_b64_tr_b16 v[158:159], v83 offset:1024\n"
    "global_load_lds_dwordx4 v53, s[36:37] offset:1024\n"
    "ds_read_b128 v[160:163], v59 offset:0\n"
    "ds_read_b128 v[164:167], v59 offset:2048\n"
    "ds_read_b128 v[168:171], v59 offset:4096\n"
    "ds_read_b128 v[172:175], v59 offset:6144\n"
    "global_load_lds_dwordx4 v54, s[36:37] offset:2048\n"
    "ds_read_b64_tr_b16 v[176:177], v80 offset:8192\n"
    "ds_read_b64_tr_b16 v[178:179], v80 offset:9216\n"
    "ds_read_b64_tr_b16 v[180:181], v81 offset:8192\n"
    "ds_read_b64_tr_b16 v[182:183], v81 offset:9216\n"
    "global_load_lds_dwordx4 v55, s[36:37] offset:3072\n"
    "ds_read_b64_tr_b16 v[184:185], v82 offset:8192\n"
    "ds_read_b64_tr_b16 v[186:187], v82 offset:9216\n"
    "ds_read_b64_tr_b16 v[188:189], v83 offset:8192\n"
    "ds_read_b64_tr_b16 v[190:191], v83 offset:9216\n"
    "s_waitcnt vmcnt(10) lgkmcnt(0)\n"
    "s_barrier\n"
    "v_mfma_f32_16x16x32_bf16 v[192:195], v[144:147], v[128:131], v[192:195]\n"
    "v_lshlrev_b32 v102, 7, v102\n"
    "v_mfma_f32_16x16x32_bf16 v[192:195], v[176:179], v[160:163], v[192:195]\n"
    "v_mfma_f32_16x16x32_bf16 v[196:199], v[176:179], v[164:167], v[196:199]\n"
    "v_add_u32_dpp v52, v101, v48 quad_perm:[0,0,0,0] row_mask:0xf bank_mask:0xf\n"
    "v_mfma_f32_16x16x32_bf16 v[196:199], v[144:147], v[132:135], v[196:199]\n"
    "v_mfma_f32_16x16x32_bf16 v[200:203], v[144:147], v[136:139], v[200:203]\n"
    "v_add_u32_dpp v53, v101, v49 quad_perm:[1,1,1,1] row_mask:0xf bank_mask:0xf\n"
    "v_mfma_f32_16x16x32_bf16 v[200:203], v[176:179], v[168:171], v[200:203]\n"
    "v_mfma_f32_16x16x32_bf16 v[204:207], v[176:179], v[172:175], v[204:207]\n"
    "v_add_u32_dpp v54, v101, v50 quad_perm:[2,2,2,2] row_mask:0xf bank_mask:0xf\n"
    "v_mfma_f32_16x16x32_bf16 v[204:207], v[144:147], v[140:143], v[204:207]\n"
    "v_mfma_f32_16x16x32_bf16 v[220:223], v[148:151], v[140:143], v[220:223]\n"
    "v_add_u32_dpp v55, v101, v51 quad_perm:[3,3,3,3] row_mask:0xf bank_mask:0xf\n"
    "v_mfma_f32_16x16x32_bf16 v[220:223], v[180:183], v[172:175], v[220:223]\n"
    "v_mfma_f32_16x16x32_bf16 v[216:219], v[180:183], v[168:171], v[216:219]\n"
    "s_lshl_b32 s40, s40, 7\n"
    "v_mfma_f32_16x16x32_bf16 v[216:219], v[148:151], v[136:139], v[216:219]\n"
    "v_mfma_f32_16x16x32_bf16 v[212:215], v[148:151], v[132:135], v[212:215]\n"
    "s_add_u32 s40, s40, 32768\n"
    "v_mfma_f32_16x16x32_bf16 v[212:215], v[180:183], v[164:167], v[212:215]\n"
    "v_mfma_f32_16x16x32_bf16 v[208:211], v[180:183], v[160:163], v[208:211]\n"
    "s_add_u32 s52, s16, s40\n"
    "s_addc_u32 s53, s17, 0\n"
    "v_mfma_f32_16x16x32_bf16 v[208:211], v[148:151], v[128:131], v[208:211]\n"
    "v_mfma_f32_16x16x32_bf16 v[224:227], v[152:155], v[128:131], v[224:227]\n"
    "s_mov_b32 s57, 0x7fff7fff\n"
    "v_mfma_f32_16x16x32_bf16 v[224:227], v[184:187], v[160:163], v[224:227]\n"
    "v_mfma_f32_16x16x32_bf16 v[228:231], v[184:187], v[164:167], v[228:231]\n"
    "s_mov_b32 s56, 0\n"
    "v_mfma_f32_16x16x32_bf16 v[228:231], v[152:155], v[132:135], v[228:231]\n"
    "v_mfma_f32_16x16x32_bf16 v[232:235], v[152:155], v[136:139], v[232:235]\n"
    "s_mov_b32 s27, 0\n"
    "v_mfma_f32_16x16x32_bf16 v[232:235], v[184:187], v[168:171], v[232:235]\n"
    "v_mfma_f32_16x16x32_bf16 v[236:239], v[184:187], v[172:175], v[236:239]\n"
    "s_mov_b32 s55, 9\n"
    "v_mfma_f32_16x16x32_bf16 v[236:239], v[152:155], v[140:143], v[236:239]\n"
    "v_mfma_f32_16x16x32_bf16 v[252:255], v[156:159], v[140:143], v[252:255]\n"
    "s_mov_b32 s44, 0\n"
    "v_mfma_f32_16x16x32_bf16 v[252:255], v[188:191], v[172:175], v[252:255]\n"
    "v_mfma_f32_16x16x32_bf16 v[248:251], v[188:191], v[168:171], v[248:251]\n"
    "s_mov_b32 s46, 0\n"
    "v_mfma_f32_16x16x32_bf16 v[248:251], v[156:159], v[136:139], v[248:251]\n"
    "v_mfma_f32_16x16x32_bf16 v[244:247], v[156:159], v[132:135], v[244:247]\n"
    "v_lshrrev_b32 v17, 3, v1\n"
    "v_mfma_f32_16x16x32_bf16 v[244:247], v[188:191], v[164:167], v[244:247]\n"
    "v_mfma_f32_16x16x32_bf16 v[240:243], v[188:191], v[160:163], v[240:243]\n"
    "v_mfma_f32_16x16x32_bf16 v[240:243], v[156:159], v[128:131], v[240:243]\n"
    "s_waitcnt vmcnt(6)\n"
    "s_barrier\n"
    "s_add_u32 m0, s35, 98304\n"
    "s_nop 0\n"
    "global_load_lds_dwordx4 v46, s[30:31] offset:0\n"
    "ds_read_b128 v[128:131], v56 offset:0\n"
    "ds_read_b128 v[132:135], v56 offset:2048\n"
    "ds_read_b128 v[136:139], v56 offset:4096\n"
    "ds_read_b128 v[140:143], v56 offset:6144\n"
    "global_load_lds_dwordx4 v47, s[30:31] offset:1024\n"
    "s_add_u32 s30, s30, 212992\n"
    "s_addc_u32 s31, s31, 0\n"
    "ds_read_b64_tr_b16 v[144:145], v76 offset:0\n"
    "ds_read_b64_tr_b16 v[146:147], v76 offset:1024\n"
    "ds_read_b64_tr_b16 v[148:149], v77 offset:0\n"
    "ds_read_b64_tr_b16 v[150:151], v77 offset:1024\n"
    "s_add_u32 m0, s34, 98304\n"
    "s_nop 0\n"
    "global_load_lds_dwordx4 v52, s[36:37] offset:0\n"
    "ds_read_b64_tr_b16 v[152:153], v78 offset:0\n"
    "ds_read_b64_tr_b16 v[154:155], v78 offset:1024\n"
    "ds_read_b64_tr_b16 v[156:157], v79 offset:0\n"
    "ds_read_b64_tr_b16 v[158:159], v79 offset:1024\n"
    "global_load_lds_dwordx4 v53, s[36:37] offset:1024\n"
    "ds_read_b128 v[160:163], v57 offset:0\n"
    "ds_read_b128 v[164:167], v57 offset:2048\n"
    "ds_read_b128 v[168:171], v57 offset:4096\n"
    "ds_read_b128 v[172:175], v57 offset:6144\n"
    "global_load_lds_dwordx4 v54, s[36:37] offset:2048\n"
    "ds_read_b64_tr_b16 v[176:177], v76 offset:8192\n"
    "ds_read_b64_tr_b16 v[178:179], v76 offset:9216\n"
    "ds_read_b64_tr_b16 v[180:181], v77 offset:8192\n"
    "ds_read_b64_tr_b16 v[182:183], v77 offset:9216\n"
    "global_load_lds_dwordx4 v55, s[36:37] offset:3072\n"
    "ds_read_b64_tr_b16 v[184:185], v78 offset:8192\n"
    "ds_read_b64_tr_b16 v[186:187], v78 offset:9216\n"
    "ds_read_b64_tr_b16 v[188:189], v79 offset:8192\n"
    "ds_read_b64_tr_b16 v[190:191], v79 offset:9216\n"
    "s_waitcnt vmcnt(10) lgkmcnt(0)\n"
    "s_barrier\n"
    "v_mfma_f32_16x16x32_bf16 v[192:195], v[144:147], v[128:131], v[192:195]\n"
    "v_lshlrev_b32 v103, 7, v103\n"
    "v_mfma_f32_16x16x32_bf16 v[192:195], v[176:179], v[160:163], v[192:195]\n"
    "v_add_u32_dpp v52, v102, v48 quad_perm:[0,0,0,0] row_mask:0xf bank_mask:0xf\n"
    "v_mfma_f32_16x16x32_bf16 v[196:199], v[176:179], v[164:167], v[196:199]\n"
    "v_mfma_f32_16x16x32_bf16 v[196:199], v[144:147], v[132:135], v[196:199]\n"
    "v_add_u32_dpp v53, v102, v49 quad_perm:[1,1,1,1] row_mask:0xf bank_mask:0xf\n"
    "v_mfma_f32_16x16x32_bf16 v[200:203], v[144:147], v[136:139], v[200:203]\n"
    "v_mfma_f32_16x16x32_bf16 v[200:203], v[176:179], v[168:171], v[200:203]\n"
    "v_add_u32_dpp v54, v102, v50 quad_perm:[2,2,2,2] row_mask:0xf bank_mask:0xf\n"
    "v_mfma_f32_16x16x32_bf16 v[204:207], v[176:179], v[172:175], v[204:207]\n"
    "v_mfma_f32_16x16x32_bf16 v[204:207], v[144:147], v[140:143], v[204:207]\n"
    "v_add_u32_dpp v55, v102, v51 quad_perm:[3,3,3,3] row_mask:0xf bank_mask:0xf\n"
    "v_mfma_f32_16x16x32_bf16 v[220:223], v[148:151], v[140:143], v[220:223]\n"
    "v_mfma_f32_16x16x32_bf16 v[220:223], v[180:183], v[172:175], v[220:223]\n"
    "v_mul_u32_u24 v17, 12, v17\n"
    "v_mfma_f32_16x16x32_bf16 v[216:219], v[180:183], v[168:171], v[216:219]\n"
    "v_mfma_f32_16x16x32_bf16 v[216:219], v[148:151], v[136:139], v[216:219]\n"
    "v_and_b32 v19, 7, v1\n"
    "v_mfma_f32_16x16x32_bf16 v[212:215], v[148:151], v[132:135], v[212:215]\n"
    "v_mfma_f32_16x16x32_bf16 v[212:215], v[180:183], v[164:167], v[212:215]\n"
    "v_lshlrev_b32 v19, 4, v19\n"
    "v_mfma_f32_16x16x32_bf16 v[208:211], v[180:183], v[160:163], v[208:211]\n"
    "v_mfma_f32_16x16x32_bf16 v[208:211], v[148:151], v[128:131], v[208:211]\n"
    "v_lshrrev_b32 v18, 5, v1\n"
    "v_mfma_f32_16x16x32_bf16 v[224:227], v[152:155], v[128:131], v[224:227]\n"
    "v_mfma_f32_16x16x32_bf16 v[224:227], v[184:187], v[160:163], v[224:227]\n"
    "v_lshlrev_b32 v18, 2, v18\n"
    "v_mfma_f32_16x16x32_bf16 v[228:231], v[184:187], v[164:167], v[228:231]\n"
    "v_mfma_f32_16x16x32_bf16 v[228:231], v[152:155], v[132:135], v[228:231]\n"
    "v_and_b32 v20, 31, v1\n"
    "v_mfma_f32_16x16x32_bf16 v[232:235], v[152:155], v[136:139], v[232:235]\n"
    "v_mfma_f32_16x16x32_bf16 v[232:235], v[184:187], v[168:171], v[232:235]\n"
    "v_lshlrev_b32 v20, 2, v20\n"
    "v_mfma_f32_16x16x32_bf16 v[236:239], v[184:187], v[172:175], v[236:239]\n"
    "v_mfma_f32_16x16x32_bf16 v[236:239], v[152:155], v[140:143], v[236:239]\n"
    "s_lshl2_add_u32 s40, s22, s58\n"
    "v_mfma_f32_16x16x32_bf16 v[252:255], v[156:159], v[140:143], v[252:255]\n"
    "v_mfma_f32_16x16x32_bf16 v[252:255], v[188:191], v[172:175], v[252:255]\n"
    "s_sub_u32 s40, s40, 20\n"
    "v_mfma_f32_16x16x32_bf16 v[248:251], v[188:191], v[168:171], v[248:251]\n"
    "v_mfma_f32_16x16x32_bf16 v[248:251], v[156:159], v[136:139], v[248:251]\n"
    "v_lshl_add_u32 v25, v1, 18, s40\n"
    "v_mfma_f32_16x16x32_bf16 v[244:247], v[156:159], v[132:135], v[244:247]\n"
    "v_mfma_f32_16x16x32_bf16 v[244:247], v[188:191], v[164:167], v[244:247]\n"
    "s_lshl_b32 s43, s21, 16\n"
    "v_mfma_f32_16x16x32_bf16 v[240:243], v[188:191], v[160:163], v[240:243]\n"
    "v_mfma_f32_16x16x32_bf16 v[240:243], v[156:159], v[128:131], v[240:243]\n"
    "s_waitcnt vmcnt(6)\n"
    "s_barrier\n"
    "s_add_u32 m0, s35, 0\n"
    "s_nop 0\n"
    "global_load_lds_dwordx4 v46, s[30:31] offset:0\n"
    "ds_read_b128 v[128:131], v56 offset:49152\n"
    "ds_read_b128 v[132:135], v56 offset:51200\n"
    "ds_read_b128 v[136:139], v56 offset:53248\n"
    "global_load_lds_dwordx4 v47, s[30:31] offset:1024\n"
    "s_add_u32 s30, s30, 212992\n"
    "s_addc_u32 s31, s31, 0\n"
    "ds_read_b128 v[140:143], v56 offset:55296\n"
    "ds_read_b64_tr_b16 v[144:145], v76 offset:49152\n"
    "ds_read_b64_tr_b16 v[146:147], v76 offset:50176\n"
    "s_add_u32 m0, s34, 0\n"
    "s_nop 0\n"
    "global_load_lds_dwordx4 v52, s[36:37] offset:0\n"
    "ds_read_b64_tr_b16 v[148:149], v77 offset:49152\n"
    "ds_read_b64_tr_b16 v[150:151], v77 offset:50176\n"
    "ds_read_b64_tr_b16 v[152:153], v78 offset:49152\n"
    "global_load_lds_dwordx4 v53, s[36:37] offset:1024\n"
    "ds_read_b64_tr_b16 v[154:155], v78 offset:50176\n"
    "ds_read_b64_tr_b16 v[156:157], v79 offset:49152\n"
    "ds_read_b64_tr_b16 v[158:159], v79 offset:50176\n"
    "global_load_lds_dwordx4 v54, s[36:37] offset:2048\n"
    "ds_read_b128 v[160:163], v57 offset:49152\n"
    "ds_read_b128 v[164:167], v57 offset:51200\n"
    "ds_read_b128 v[168:171], v57 offset:53248\n"
    "global_load_lds_dwordx4 v55, s[36:37] offset:3072\n"
    "ds_read_b128 v[172:175], v57 offset:55296\n"
    "ds_read_b64_tr_b16 v[176:177], v76 offset:57344\n"
    "ds_read_b64_tr_b16 v[178:179], v76 offset:58368\n"
    "global_load_dwordx2 v[84:85], v42, s[10:11] offset:0\n"
    "ds_read_b64_tr_b16 v[180:181], v77 offset:57344\n"
    "ds_read_b64_tr_b16 v[182:183], v77 offset:58368\n"
    "ds_read_b64_tr_b16 v[184:185], v78 offset:57344\n"
    "ds_read_b64_tr_b16 v[186:187], v78 offset:58368\n"
    "ds_read_b64_tr_b16 v[188:189], v79 offset:57344\n"
    "ds_read_b64_tr_b16 v[190:191], v79 offset:58368\n"
    "s_waitcnt vmcnt(11) lgkmcnt(0)\n"
    "s_barrier\n"
    "v_mfma_f32_16x16x32_bf16 v[192:195], v[144:147], v[128:131], v[192:195]\n"
    "v_lshlrev_b32 v104, 7, v104\n"
    "v_mfma_f32_16x16x32_bf16 v[192:195], v[176:179], v[160:163], v[192:195]\n"
    "v_mfma_f32_16x16x32_bf16 v[196:199], v[176:179], v[164:167], v[196:199]\n"
    "v_add_u32_dpp v52, v103, v48 quad_perm:[0,0,0,0] row_mask:0xf bank_mask:0xf\n"
    "v_mfma_f32_16x16x32_bf16 v[196:199], v[144:147], v[132:135], v[196:199]\n"
    "v_mfma_f32_16x16x32_bf16 v[200:203], v[144:147], v[136:139], v[200:203]\n"
    "v_add_u32_dpp v53, v103, v49 quad_perm:[1,1,1,1] row_mask:0xf bank_mask:0xf\n"
    "v_mfma_f32_16x16x32_bf16 v[200:203], v[176:179], v[168:171], v[200:203]\n"
    "v_mfma_f32_16x16x32_bf16 v[204:207], v[176:179], v[172:175], v[204:207]\n"
    "v_add_u32_dpp v54, v103, v50 quad_perm:[2,2,2,2] row_mask:0xf bank_mask:0xf\n"
    "v_mfma_f32_16x16x32_bf16 v[204:207], v[144:147], v[140:143], v[204:207]\n"
    "v_mfma_f32_16x16x32_bf16 v[220:223], v[148:151], v[140:143], v[220:223]\n"
    "v_add_u32_dpp v55, v103, v51 quad_perm:[3,3,3,3] row_mask:0xf bank_mask:0xf\n"
    "v_mfma_f32_16x16x32_bf16 v[220:223], v[180:183], v[172:175], v[220:223]\n"
    "v_mfma_f32_16x16x32_bf16 v[216:219], v[180:183], v[168:171], v[216:219]\n"
    "v_lshrrev_b32 v12, 4, v1\n"
    "v_mfma_f32_16x16x32_bf16 v[216:219], v[148:151], v[136:139], v[216:219]\n"
    "v_mfma_f32_16x16x32_bf16 v[212:215], v[148:151], v[132:135], v[212:215]\n"
    "v_bfe_u32 v13, v1, 2, 2\n"
    "v_mfma_f32_16x16x32_bf16 v[212:215], v[180:183], v[164:167], v[212:215]\n"
    "v_mfma_f32_16x16x32_bf16 v[208:211], v[180:183], v[160:163], v[208:211]\n"
    "v_and_b32 v21, 3, v1\n"
    "v_mfma_f32_16x16x32_bf16 v[208:211], v[148:151], v[128:131], v[208:211]\n"
    "v_mfma_f32_16x16x32_bf16 v[224:227], v[152:155], v[128:131], v[224:227]\n"
    "s_mul_i32 s40, s22, 0x2020\n"
    "v_mfma_f32_16x16x32_bf16 v[224:227], v[184:187], v[160:163], v[224:227]\n"
    "v_mfma_f32_16x16x32_bf16 v[228:231], v[184:187], v[164:167], v[228:231]\n"
    "s_and_b32 s40, s40, 0x4060\n"
    "v_mfma_f32_16x16x32_bf16 v[228:231], v[152:155], v[132:135], v[228:231]\n"
    "v_mfma_f32_16x16x32_bf16 v[232:235], v[152:155], v[136:139], v[232:235]\n"
    "s_add_u32 s40, s40, s43\n"
    "v_mfma_f32_16x16x32_bf16 v[232:235], v[184:187], v[168:171], v[232:235]\n"
    "v_mfma_f32_16x16x32_bf16 v[236:239], v[184:187], v[172:175], v[236:239]\n"
    "v_lshl_add_u32 v21, v21, 3, s40\n"
    "v_mfma_f32_16x16x32_bf16 v[236:239], v[152:155], v[140:143], v[236:239]\n"
    "v_mfma_f32_16x16x32_bf16 v[252:255], v[156:159], v[140:143], v[252:255]\n"
    "s_movk_i32 s41, 144\n"
    "v_mfma_f32_16x16x32_bf16 v[252:255], v[188:191], v[172:175], v[252:255]\n"
    "v_mfma_f32_16x16x32_bf16 v[248:251], v[188:191], v[168:171], v[248:251]\n"
    "v_add_u32 v22, v12, v13\n"
    "v_mfma_f32_16x16x32_bf16 v[248:251], v[156:159], v[136:139], v[248:251]\n"
    "v_mfma_f32_16x16x32_bf16 v[244:247], v[156:159], v[132:135], v[244:247]\n"
    "v_and_b32 v22, 3, v22\n"
    "v_mfma_f32_16x16x32_bf16 v[244:247], v[188:191], v[164:167], v[244:247]\n"
    "v_mfma_f32_16x16x32_bf16 v[240:243], v[188:191], v[160:163], v[240:243]\n"
    "v_mfma_f32_16x16x32_bf16 v[240:243], v[156:159], v[128:131], v[240:243]\n"
    "s_waitcnt vmcnt(7)\n"
    "s_barrier\n"
    "s_add_u32 m0, s35, 49152\n"
    "s_nop 0\n"
    "global_load_lds_dwordx4 v46, s[30:31] offset:0\n"
    "ds_read_b128 v[128:131], v58 offset:0\n"
    "ds_read_b128 v[132:135], v58 offset:2048\n"
    "ds_read_b128 v[136:139], v58 offset:4096\n"
    "global_load_lds_dwordx4 v47, s[30:31] offset:1024\n"
    "s_bfe_u32 s66, s28, 0x80008\n"
    "s_mul_i32 s66, s66, 53248\n"
    "s_add_u32 s30, s6, s66\n"
    "s_addc_u32 s31, s7, 0\n"
    "ds_read_b128 v[140:143], v58 offset:6144\n"
    "ds_read_b64_tr_b16 v[144:145], v80 offset:0\n"
    "ds_read_b64_tr_b16 v[146:147], v80 offset:1024\n"
    "s_add_u32 m0, s34, 49152\n"
    "s_nop 0\n"
    "global_load_lds_dwordx4 v52, s[36:37] offset:0\n"
    "ds_read_b64_tr_b16 v[148:149], v81 offset:0\n"
    "ds_read_b64_tr_b16 v[150:151], v81 offset:1024\n"
    "ds_read_b64_tr_b16 v[152:153], v82 offset:0\n"
    "global_load_lds_dwordx4 v53, s[36:37] offset:1024\n"
    "ds_read_b64_tr_b16 v[154:155], v82 offset:1024\n"
    "ds_read_b64_tr_b16 v[156:157], v83 offset:0\n"
    "ds_read_b64_tr_b16 v[158:159], v83 offset:1024\n"
    "global_load_lds_dwordx4 v54, s[36:37] offset:2048\n"
    "ds_read_b128 v[160:163], v59 offset:0\n"
    "ds_read_b128 v[164:167], v59 offset:2048\n"
    "ds_read_b128 v[168:171], v59 offset:4096\n"
    "global_load_lds_dwordx4 v55, s[36:37] offset:3072\n"
    "ds_read_b128 v[172:175], v59 offset:6144\n"
    "ds_read_b64_tr_b16 v[176:177], v80 offset:8192\n"
    "ds_read_b64_tr_b16 v[178:179], v80 offset:9216\n"
    "global_load_dwordx2 v[86:87], v42, s[10:11] offset:32\n"
    "ds_read_b64_tr_b16 v[180:181], v81 offset:8192\n"
    "ds_read_b64_tr_b16 v[182:183], v81 offset:9216\n"
    "ds_read_b64_tr_b16 v[184:185], v82 offset:8192\n"
    "ds_read_b64_tr_b16 v[186:187], v82 offset:9216\n"
    "ds_read_b64_tr_b16 v[188:189], v83 offset:8192\n"
    "ds_read_b64_tr_b16 v[190:191], v83 offset:9216\n"
    "s_waitcnt vmcnt(12) lgkmcnt(0)\n"
    "s_barrier\n"
    "v_mfma_f32_16x16x32_bf16 v[192:195], v[144:147], v[128:131], v[192:195]\n"
    "v_lshlrev_b32 v105, 7, v105\n"
    "v_mfma_f32_16x16x32_bf16 v[192:195], v[176:179], v[160:163], v[192:195]\n"
    "v_add_u32_dpp v52, v104, v48 quad_perm:[0,0,0,0] row_mask:0xf bank_mask:0xf\n"
    "v_mfma_f32_16x16x32_bf16 v[196:199], v[176:179], v[164:167], v[196:199]\n"
    "v_mfma_f32_16x16x32_bf16 v[196:199], v[144:147], v[132:135], v[196:199]\n"
    "v_add_u32_dpp v53, v104, v49 quad_perm:[1,1,1,1] row_mask:0xf bank_mask:0xf\n"
    "v_mfma_f32_16x16x32_bf16 v[200:203], v[144:147], v[136:139], v[200:203]\n"
    "v_mfma_f32_16x16x32_bf16 v[200:203], v[176:179], v[168:171], v[200:203]\n"
    "v_add_u32_dpp v54, v104, v50 quad_perm:[2,2,2,2] row_mask:0xf bank_mask:0xf\n"
    "v_mfma_f32_16x16x32_bf16 v[204:207], v[176:179], v[172:175], v[204:207]\n"
    "v_mfma_f32_16x16x32_bf16 v[204:207], v[144:147], v[140:143], v[204:207]\n"
    "v_add_u32_dpp v55, v104, v51 quad_perm:[3,3,3,3] row_mask:0xf bank_mask:0xf\n"
    "v_mfma_f32_16x16x32_bf16 v[220:223], v[148:151], v[140:143], v[220:223]\n"
    "v_mfma_f32_16x16x32_bf16 v[220:223], v[180:183], v[172:175], v[220:223]\n"
    "v_mad_u32_u24 v4, v22, s41, v21\n"
    "v_mfma_f32_16x16x32_bf16 v[216:219], v[180:183], v[168:171], v[216:219]\n"
    "v_mfma_f32_16x16x32_bf16 v[216:219], v[148:151], v[136:139], v[216:219]\n"
    "v_cmp_gt_u32 vcc, 2, v13\n"
    "v_mfma_f32_16x16x32_bf16 v[212:215], v[148:151], v[132:135], v[212:215]\n"
    "v_mfma_f32_16x16x32_bf16 v[212:215], v[180:183], v[164:167], v[212:215]\n"
    "v_add_u32 v24, 22, v13\n"
    "v_mfma_f32_16x16x32_bf16 v[208:211], v[180:183], v[160:163], v[208:211]\n"
    "v_mfma_f32_16x16x32_bf16 v[208:211], v[148:151], v[128:131], v[208:211]\n"
    "v_lshlrev_b32 v13, 3, v13\n"
    "v_mfma_f32_16x16x32_bf16 v[224:227], v[152:155], v[128:131], v[224:227]\n"
    "v_mfma_f32_16x16x32_bf16 v[224:227], v[184:187], v[160:163], v[224:227]\n"
    "v_cndmask_b32 v22, v24, v22, vcc\n"
    "v_mfma_f32_16x16x32_bf16 v[228:231], v[184:187], v[164:167], v[228:231]\n"
    "v_mfma_f32_16x16x32_bf16 v[228:231], v[152:155], v[132:135], v[228:231]\n"
    "v_mad_u32_u24 v3, v22, s41, v21\n"
    "v_mfma_f32_16x16x32_bf16 v[232:235], v[152:155], v[136:139], v[232:235]\n"
    "v_mfma_f32_16x16x32_bf16 v[232:235], v[184:187], v[168:171], v[232:235]\n"
    "v_cmp_eq_u32 vcc, 3, v12\n"
    "v_mfma_f32_16x16x32_bf16 v[236:239], v[184:187], v[172:175], v[236:239]\n"
    "v_mfma_f32_16x16x32_bf16 v[236:239], v[152:155], v[140:143], v[236:239]\n"
    "v_xor_b32 v22, 0, v12\n"
    "v_mfma_f32_16x16x32_bf16 v[252:255], v[156:159], v[140:143], v[252:255]\n"
    "v_mfma_f32_16x16x32_bf16 v[252:255], v[188:191], v[172:175], v[252:255]\n"
    "v_and_b32 v22, 1, v22\n"
    "v_mfma_f32_16x16x32_bf16 v[248:251], v[188:191], v[168:171], v[248:251]\n"
    "v_mfma_f32_16x16x32_bf16 v[248:251], v[156:159], v[136:139], v[248:251]\n"
    "v_lshlrev_b32 v22, 1, v22\n"
    "v_mfma_f32_16x16x32_bf16 v[244:247], v[156:159], v[132:135], v[244:247]\n"
    "v_mfma_f32_16x16x32_bf16 v[244:247], v[188:191], v[164:167], v[244:247]\n"
    "v_lshl_add_u32 v22, v12, 3, v22\n"
    "v_mfma_f32_16x16x32_bf16 v[240:243], v[188:191], v[160:163], v[240:243]\n"
    "v_mfma_f32_16x16x32_bf16 v[240:243], v[156:159], v[128:131], v[240:243]\n"
    "s_waitcnt vmcnt(8)\n"
    "s_barrier\n"
    "s_add_u32 m0, s35, 98304\n"
    "s_nop 0\n"
    "global_load_lds_dwordx4 v46, s[30:31] offset:0\n"
    "ds_read_b128 v[128:131], v56 offset:0\n"
    "ds_read_b128 v[132:135], v56 offset:2048\n"
    "ds_read_b128 v[136:139], v56 offset:4096\n"
    "global_load_lds_dwordx4 v47, s[30:31] offset:1024\n"
    "s_add_u32 s30, s30, 212992\n"
    "s_addc_u32 s31, s31, 0\n"
    "ds_read_b128 v[140:143], v56 offset:6144\n"
    "ds_read_b64_tr_b16 v[144:145], v76 offset:0\n"
    "ds_read_b64_tr_b16 v[146:147], v76 offset:1024\n"
    "s_add_u32 m0, s34, 98304\n"
    "s_nop 0\n"
    "global_load_lds_dwordx4 v52, s[36:37] offset:0\n"
    "ds_read_b64_tr_b16 v[148:149], v77 offset:0\n"
    "ds_read_b64_tr_b16 v[150:151], v77 offset:1024\n"
    "ds_read_b64_tr_b16 v[152:153], v78 offset:0\n"
    "global_load_lds_dwordx4 v53, s[36:37] offset:1024\n"
    "ds_read_b64_tr_b16 v[154:155], v78 offset:1024\n"
    "ds_read_b64_tr_b16 v[156:157], v79 offset:0\n"
    "ds_read_b64_tr_b16 v[158:159], v79 offset:1024\n"
    "global_load_lds_dwordx4 v54, s[36:37] offset:2048\n"
    "ds_read_b128 v[160:163], v57 offset:0\n"
    "ds_read_b128 v[164:167], v57 offset:2048\n"
    "ds_read_b128 v[168:171], v57 offset:4096\n"
    "global_load_lds_dwordx4 v55, s[36:37] offset:3072\n"
    "ds_read_b128 v[172:175], v57 offset:6144\n"
    "ds_read_b64_tr_b16 v[176:177], v76 offset:8192\n"
    "ds_read_b64_tr_b16 v[178:179], v76 offset:9216\n"
    "global_load_dwordx2 v[88:89], v42, s[10:11] offset:64\n"
    "ds_read_b64_tr_b16 v[180:181], v77 offset:8192\n"
    "ds_read_b64_tr_b16 v[182:183], v77 offset:9216\n"
    "ds_read_b64_tr_b16 v[184:185], v78 offset:8192\n"
    "ds_read_b64_tr_b16 v[186:187], v78 offset:9216\n"
    "ds_read_b64_tr_b16 v[188:189], v79 offset:8192\n"
    "ds_read_b64_tr_b16 v[190:191], v79 offset:9216\n"
    "s_waitcnt vmcnt(12) lgkmcnt(0)\n"
    "s_barrier\n"
    "v_mfma_f32_16x16x32_bf16 v[192:195], v[144:147], v[128:131], v[192:195]\n"
    "v_lshlrev_b32 v106, 7, v106\n"
    "v_mfma_f32_16x16x32_bf16 v[192:195], v[176:179], v[160:163], v[192:195]\n"
    "v_mfma_f32_16x16x32_bf16 v[196:199], v[176:179], v[164:167], v[196:199]\n"
    "v_add_u32_dpp v52, v105, v48 quad_perm:[0,0,0,0] row_mask:0xf bank_mask:0xf\n"
    "v_mfma_f32_16x16x32_bf16 v[196:199], v[144:147], v[132:135], v[196:199]\n"
    "v_mfma_f32_16x16x32_bf16 v[200:203], v[144:147], v[136:139], v[200:203]\n"
    "v_add_u32_dpp v53, v105, v49 quad_perm:[1,1,1,1] row_mask:0xf bank_mask:0xf\n"
    "v_mfma_f32_16x16x32_bf16 v[200:203], v[176:179], v[168:171], v[200:203]\n"
    "v_mfma_f32_16x16x32_bf16 v[204:207], v[176:179], v[172:175], v[204:207]\n"
    "v_add_u32_dpp v54, v105, v50 quad_perm:[2,2,2,2] row_mask:0xf bank_mask:0xf\n"
    "v_mfma_f32_16x16x32_bf16 v[204:207], v[144:147], v[140:143], v[204:207]\n"
    "v_mfma_f32_16x16x32_bf16 v[220:223], v[148:151], v[140:143], v[220:223]\n"
    "v_add_u32_dpp v55, v105, v51 quad_perm:[3,3,3,3] row_mask:0xf bank_mask:0xf\n"
    "v_mfma_f32_16x16x32_bf16 v[220:223], v[180:183], v[172:175], v[220:223]\n"
    "v_mfma_f32_16x16x32_bf16 v[216:219], v[180:183], v[168:171], v[216:219]\n"
    "s_mov_b32 s40, 0x05040100\n"
    "v_mfma_f32_16x16x32_bf16 v[216:219], v[148:151], v[136:139], v[216:219]\n"
    "v_mfma_f32_16x16x32_bf16 v[212:215], v[148:151], v[132:135], v[212:215]\n"
    "v_bfe_u32 v24, s40, v13, 8\n"
    "v_mfma_f32_16x16x32_bf16 v[212:215], v[180:183], v[164:167], v[212:215]\n"
    "v_mfma_f32_16x16x32_bf16 v[208:211], v[180:183], v[160:163], v[208:211]\n"
    "v_add_u32 v22, v22, v24\n"
    "v_mfma_f32_16x16x32_bf16 v[208:211], v[148:151], v[128:131], v[208:211]\n"
    "v_mfma_f32_16x16x32_bf16 v[224:227], v[152:155], v[128:131], v[224:227]\n"
    "s_mov_b32 s40, 0x1a191818\n"
    "v_mfma_f32_16x16x32_bf16 v[224:227], v[184:187], v[160:163], v[224:227]\n"
    "v_mfma_f32_16x16x32_bf16 v[228:231], v[184:187], v[164:167], v[228:231]\n"
    "v_bfe_u32 v24, s40, v13, 8\n"
    "v_mfma_f32_16x16x32_bf16 v[228:231], v[152:155], v[132:135], v[228:231]\n"
    "v_mfma_f32_16x16x32_bf16 v[232:235], v[152:155], v[136:139], v[232:235]\n"
    "v_cndmask_b32 v22, v22, v24, vcc\n"
    "v_mfma_f32_16x16x32_bf16 v[232:235], v[184:187], v[168:171], v[232:235]\n"
    "v_mfma_f32_16x16x32_bf16 v[236:239], v[184:187], v[172:175], v[236:239]\n"
    "v_mad_u32_u24 v5, v22, s41, v21\n"
    "v_mfma_f32_16x16x32_bf16 v[236:239], v[152:155], v[140:143], v[236:239]\n"
    "v_mfma_f32_16x16x32_bf16 v[252:255], v[156:159], v[140:143], v[252:255]\n"
    "v_xor_b32 v22, 1, v12\n"
    "v_mfma_f32_16x16x32_bf16 v[252:255], v[188:191], v[172:175], v[252:255]\n"
    "v_mfma_f32_16x16x32_bf16 v[248:251], v[188:191], v[168:171], v[248:251]\n"
    "v_and_b32 v22, 1, v22\n"
    "v_mfma_f32_16x16x32_bf16 v[248:251], v[156:159], v[136:139], v[248:251]\n"
    "v_mfma_f32_16x16x32_bf16 v[244:247], v[156:159], v[132:135], v[244:247]\n"
    "v_lshlrev_b32 v22, 1, v22\n"
    "v_mfma_f32_16x16x32_bf16 v[244:247], v[188:191], v[164:167], v[244:247]\n"
    "v_mfma_f32_16x16x32_bf16 v[240:243], v[188:191], v[160:163], v[240:243]\n"
    "v_mfma_f32_16x16x32_bf16 v[240:243], v[156:159], v[128:131], v[240:243]\n"
    "s_waitcnt vmcnt(8)\n"
    "s_barrier\n"
    "s_add_u32 m0, s35, 0\n"
    "s_nop 0\n"
    "global_load_lds_dwordx4 v46, s[30:31] offset:0\n"
    "ds_read_b128 v[128:131], v56 offset:49152\n"
    "ds_read_b128 v[132:135], v56 offset:51200\n"
    "ds_read_b128 v[136:139], v56 offset:53248\n"
    "global_load_lds_dwordx4 v47, s[30:31] offset:1024\n"
    "s_add_u32 s30, s30, 212992\n"
    "s_addc_u32 s31, s31, 0\n"
    "ds_read_b128 v[140:143], v56 offset:55296\n"
    "ds_read_b64_tr_b16 v[144:145], v76 offset:49152\n"
    "ds_read_b64_tr_b16 v[146:147], v76 offset:50176\n"
    "s_add_u32 m0, s34, 0\n"
    "s_nop 0\n"
    "global_load_lds_dwordx4 v52, s[36:37] offset:0\n"
    "ds_read_b64_tr_b16 v[148:149], v77 offset:49152\n"
    "ds_read_b64_tr_b16 v[150:151], v77 offset:50176\n"
    "ds_read_b64_tr_b16 v[152:153], v78 offset:49152\n"
    "global_load_lds_dwordx4 v53, s[36:37] offset:1024\n"
    "ds_read_b64_tr_b16 v[154:155], v78 offset:50176\n"
    "ds_read_b64_tr_b16 v[156:157], v79 offset:49152\n"
    "ds_read_b64_tr_b16 v[158:159], v79 offset:50176\n"
    "global_load_lds_dwordx4 v54, s[36:37] offset:2048\n"
    "ds_read_b128 v[160:163], v57 offset:49152\n"
    "ds_read_b128 v[164:167], v57 offset:51200\n"
    "ds_read_b128 v[168:171], v57 offset:53248\n"
    "global_load_lds_dwordx4 v55, s[36:37] offset:3072\n"
    "ds_read_b128 v[172:175], v57 offset:55296\n"
    "ds_read_b64_tr_b16 v[176:177], v76 offset:57344\n"
    "ds_read_b64_tr_b16 v[178:179], v76 offset:58368\n"
    "global_load_dwordx2 v[90:91], v42, s[10:11] offset:96\n"
    "ds_read_b64_tr_b16 v[180:181], v77 offset:57344\n"
    "ds_read_b64_tr_b16 v[182:183], v77 offset:58368\n"
    "ds_read_b64_tr_b16 v[184:185], v78 offset:57344\n"
    "ds_read_b64_tr_b16 v[186:187], v78 offset:58368\n"
    "ds_read_b64_tr_b16 v[188:189], v79 offset:57344\n"
    "ds_read_b64_tr_b16 v[190:191], v79 offset:58368\n"
    "s_waitcnt vmcnt(12) lgkmcnt(0)\n"
    "s_barrier\n"
    "v_mfma_f32_16x16x32_bf16 v[192:195], v[144:147], v[128:131], v[192:195]\n"
    "v_lshlrev_b32 v107, 7, v107\n"
    "v_mfma_f32_16x16x32_bf16 v[192:195], v[176:179], v[160:163], v[192:195]\n"
    "v_mfma_f32_16x16x32_bf16 v[196:199], v[176:179], v[164:167], v[196:199]\n"
    "v_add_u32_dpp v52, v106, v48 quad_perm:[0,0,0,0] row_mask:0xf bank_mask:0xf\n"
    "v_mfma_f32_16x16x32_bf16 v[196:199], v[144:147], v[132:135], v[196:199]\n"
    "v_mfma_f32_16x16x32_bf16 v[200:203], v[144:147], v[136:139], v[200:203]\n"
    "v_add_u32_dpp v53, v106, v49 quad_perm:[1,1,1,1] row_mask:0xf bank_mask:0xf\n"
    "v_mfma_f32_16x16x32_bf16 v[200:203], v[176:179], v[168:171], v[200:203]\n"
    "v_mfma_f32_16x16x32_bf16 v[204:207], v[176:179], v[172:175], v[204:207]\n"
    "v_add_u32_dpp v54, v106, v50 quad_perm:[2,2,2,2] row_mask:0xf bank_mask:0xf\n"
    "v_mfma_f32_16x16x32_bf16 v[204:207], v[144:147], v[140:143], v[204:207]\n"
    "v_mfma_f32_16x16x32_bf16 v[220:223], v[148:151], v[140:143], v[220:223]\n"
    "v_add_u32_dpp v55, v106, v51 quad_perm:[3,3,3,3] row_mask:0xf bank_mask:0xf\n"
    "v_mfma_f32_16x16x32_bf16 v[220:223], v[180:183], v[172:175], v[220:223]\n"
    "v_mfma_f32_16x16x32_bf16 v[216:219], v[180:183], v[168:171], v[216:219]\n"
    "v_lshl_add_u32 v22, v12, 3, v22\n"
    "v_mfma_f32_16x16x32_bf16 v[216:219], v[148:151], v[136:139], v[216:219]\n"
    "v_mfma_f32_16x16x32_bf16 v[212:215], v[148:151], v[132:135], v[212:215]\n"
    "s_mov_b32 s40, 0x05040100\n"
    "v_mfma_f32_16x16x32_bf16 v[212:215], v[180:183], v[164:167], v[212:215]\n"
    "v_mfma_f32_16x16x32_bf16 v[208:211], v[180:183], v[160:163], v[208:211]\n"
    "v_bfe_u32 v24, s40, v13, 8\n"
    "v_mfma_f32_16x16x32_bf16 v[208:211], v[148:151], v[128:131], v[208:211]\n"
    "v_mfma_f32_16x16x32_bf16 v[224:227], v[152:155], v[128:131], v[224:227]\n"
    "v_add_u32 v22, v22, v24\n"
    "v_mfma_f32_16x16x32_bf16 v[224:227], v[184:187], v[160:163], v[224:227]\n"
    "v_mfma_f32_16x16x32_bf16 v[228:231], v[184:187], v[164:167], v[228:231]\n"
    "s_mov_b32 s40, 0x1a191918\n"
    "v_mfma_f32_16x16x32_bf16 v[228:231], v[152:155], v[132:135], v[228:231]\n"
    "v_mfma_f32_16x16x32_bf16 v[232:235], v[152:155], v[136:139], v[232:235]\n"
    "v_bfe_u32 v24, s40, v13, 8\n"
    "v_mfma_f32_16x16x32_bf16 v[232:235], v[184:187], v[168:171], v[232:235]\n"
    "v_mfma_f32_16x16x32_bf16 v[236:239], v[184:187], v[172:175], v[236:239]\n"
    "v_cndmask_b32 v22, v22, v24, vcc\n"
    "v_mfma_f32_16x16x32_bf16 v[236:239], v[152:155], v[140:143], v[236:239]\n"
    "v_mfma_f32_16x16x32_bf16 v[252:255], v[156:159], v[140:143], v[252:255]\n"
    "v_mad_u32_u24 v6, v22, s41, v21\n"
    "v_mfma_f32_16x16x32_bf16 v[252:255], v[188:191], v[172:175], v[252:255]\n"
    "v_mfma_f32_16x16x32_bf16 v[248:251], v[188:191], v[168:171], v[248:251]\n"
    "v_bfrev_b32 v14, v12\n"
    "v_mfma_f32_16x16x32_bf16 v[248:251], v[156:159], v[136:139], v[248:251]\n"
    "v_mfma_f32_16x16x32_bf16 v[244:247], v[156:159], v[132:135], v[244:247]\n"
    "v_lshrrev_b32 v14, 26, v14\n"
    "v_mfma_f32_16x16x32_bf16 v[244:247], v[188:191], v[164:167], v[244:247]\n"
    "v_mfma_f32_16x16x32_bf16 v[240:243], v[188:191], v[160:163], v[240:243]\n"
    "v_mfma_f32_16x16x32_bf16 v[240:243], v[156:159], v[128:131], v[240:243]\n"
    "s_waitcnt vmcnt(8)\n"
    "s_barrier\n"
    "s_add_u32 m0, s35, 49152\n"
    "s_nop 0\n"
    "global_load_lds_dwordx4 v46, s[30:31] offset:0\n"
    "ds_read_b128 v[128:131], v58 offset:0\n"
    "ds_read_b128 v[132:135], v58 offset:2048\n"
    "ds_read_b128 v[136:139], v58 offset:4096\n"
    "global_load_lds_dwordx4 v47, s[30:31] offset:1024\n"
    "s_add_u32 s30, s30, 212992\n"
    "s_addc_u32 s31, s31, 0\n"
    "ds_read_b128 v[140:143], v58 offset:6144\n"
    "ds_read_b64_tr_b16 v[144:145], v80 offset:0\n"
    "ds_read_b64_tr_b16 v[146:147], v80 offset:1024\n"
    "s_add_u32 m0, s34, 49152\n"
    "s_nop 0\n"
    "global_load_lds_dwordx4 v52, s[36:37] offset:0\n"
    "ds_read_b64_tr_b16 v[148:149], v81 offset:0\n"
    "ds_read_b64_tr_b16 v[150:151], v81 offset:1024\n"
    "ds_read_b64_tr_b16 v[152:153], v82 offset:0\n"
    "global_load_lds_dwordx4 v53, s[36:37] offset:1024\n"
    "ds_read_b64_tr_b16 v[154:155], v82 offset:1024\n"
    "ds_read_b64_tr_b16 v[156:157], v83 offset:0\n"
    "ds_read_b64_tr_b16 v[158:159], v83 offset:1024\n"
    "global_load_lds_dwordx4 v54, s[36:37] offset:2048\n"
    "ds_read_b128 v[160:163], v59 offset:0\n"
    "ds_read_b128 v[164:167], v59 offset:2048\n"
    "ds_read_b128 v[168:171], v59 offset:4096\n"
    "global_load_lds_dwordx4 v55, s[36:37] offset:3072\n"
    "ds_read_b128 v[172:175], v59 offset:6144\n"
    "ds_read_b64_tr_b16 v[176:177], v80 offset:8192\n"
    "ds_read_b64_tr_b16 v[178:179], v80 offset:9216\n"
    "global_load_dwordx2 v[92:93], v42, s[18:19] offset:0\n"
    "ds_read_b64_tr_b16 v[180:181], v81 offset:8192\n"
    "ds_read_b64_tr_b16 v[182:183], v81 offset:9216\n"
    "ds_read_b64_tr_b16 v[184:185], v82 offset:8192\n"
    "ds_read_b64_tr_b16 v[186:187], v82 offset:9216\n"
    "ds_read_b64_tr_b16 v[188:189], v83 offset:8192\n"
    "ds_read_b64_tr_b16 v[190:191], v83 offset:9216\n"
    "s_waitcnt vmcnt(12) lgkmcnt(0)\n"
    "s_barrier\n"
    "v_mfma_f32_16x16x32_bf16 v[192:195], v[144:147], v[128:131], v[192:195]\n"
    "v_lshlrev_b32 v108, 7, v108\n"
    "v_mfma_f32_16x16x32_bf16 v[192:195], v[176:179], v[160:163], v[192:195]\n"
    "v_add_u32_dpp v52, v107, v48 quad_perm:[0,0,0,0] row_mask:0xf bank_mask:0xf\n"
    "v_mfma_f32_16x16x32_bf16 v[196:199], v[176:179], v[164:167], v[196:199]\n"
    "v_mfma_f32_16x16x32_bf16 v[196:199], v[144:147], v[132:135], v[196:199]\n"
    "v_add_u32_dpp v53, v107, v49 quad_perm:[1,1,1,1] row_mask:0xf bank_mask:0xf\n"
    "v_mfma_f32_16x16x32_bf16 v[200:203], v[144:147], v[136:139], v[200:203]\n"
    "v_mfma_f32_16x16x32_bf16 v[200:203], v[176:179], v[168:171], v[200:203]\n"
    "v_add_u32_dpp v54, v107, v50 quad_perm:[2,2,2,2] row_mask:0xf bank_mask:0xf\n"
    "v_mfma_f32_16x16x32_bf16 v[204:207], v[176:179], v[172:175], v[204:207]\n"
    "v_mfma_f32_16x16x32_bf16 v[204:207], v[144:147], v[140:143], v[204:207]\n"
    "v_add_u32_dpp v55, v107, v51 quad_perm:[3,3,3,3] row_mask:0xf bank_mask:0xf\n"
    "v_mfma_f32_16x16x32_bf16 v[220:223], v[148:151], v[140:143], v[220:223]\n"
    "v_mfma_f32_16x16x32_bf16 v[220:223], v[180:183], v[172:175], v[220:223]\n"
    "s_add_u32 s40, s22, 3\n"
    "v_mfma_f32_16x16x32_bf16 v[216:219], v[180:183], v[168:171], v[216:219]\n"
    "v_mfma_f32_16x16x32_bf16 v[216:219], v[148:151], v[136:139], v[216:219]\n"
    "s_and_b32 s40, s40, 3\n"
    "v_mfma_f32_16x16x32_bf16 v[212:215], v[148:151], v[132:135], v[212:215]\n"
    "v_mfma_f32_16x16x32_bf16 v[212:215], v[180:183], v[164:167], v[212:215]\n"
    "s_lshl2_add_u32 s40, s40, s22\n"
    "v_mfma_f32_16x16x32_bf16 v[208:211], v[180:183], v[160:163], v[208:211]\n"
    "v_mfma_f32_16x16x32_bf16 v[208:211], v[148:151], v[128:131], v[208:211]\n"
    "s_add_u32 s40, s40, s58\n"
    "v_mfma_f32_16x16x32_bf16 v[224:227], v[152:155], v[128:131], v[224:227]\n"
    "v_mfma_f32_16x16x32_bf16 v[224:227], v[184:187], v[160:163], v[224:227]\n"
    "s_lshl_b32 s40, s40, 2\n"
    "v_mfma_f32_16x16x32_bf16 v[228:231], v[184:187], v[164:167], v[228:231]\n"
    "v_mfma_f32_16x16x32_bf16 v[228:231], v[152:155], v[132:135], v[228:231]\n"
    "s_add_u32 s40, s40, -450048\n"
    "v_mfma_f32_16x16x32_bf16 v[232:235], v[152:155], v[136:139], v[232:235]\n"
    "v_mfma_f32_16x16x32_bf16 v[232:235], v[184:187], v[168:171], v[232:235]\n"
    "v_add_u32 v14, s40, v14\n"
    "v_mfma_f32_16x16x32_bf16 v[236:239], v[184:187], v[172:175], v[236:239]\n"
    "v_mfma_f32_16x16x32_bf16 v[236:239], v[152:155], v[140:143], v[236:239]\n"
    "v_and_b32 v2, 1, v1\n"
    "v_mfma_f32_16x16x32_bf16 v[252:255], v[156:159], v[140:143], v[252:255]\n"
    "v_mfma_f32_16x16x32_bf16 v[252:255], v[188:191], v[172:175], v[252:255]\n"
    "v_mul_u32_u24 v2, 0xaaaa, v2\n"
    "v_mfma_f32_16x16x32_bf16 v[248:251], v[188:191], v[168:171], v[248:251]\n"
    "v_mfma_f32_16x16x32_bf16 v[248:251], v[156:159], v[136:139], v[248:251]\n"
    "v_add_u32 v2, 0x4444, v2\n"
    "v_mfma_f32_16x16x32_bf16 v[244:247], v[156:159], v[132:135], v[244:247]\n"
    "v_mfma_f32_16x16x32_bf16 v[244:247], v[188:191], v[164:167], v[244:247]\n"
    "v_mad_u32_u24 v15, v17, 36, v19\n"
    "v_mfma_f32_16x16x32_bf16 v[240:243], v[188:191], v[160:163], v[240:243]\n"
    "v_mfma_f32_16x16x32_bf16 v[240:243], v[156:159], v[128:131], v[240:243]\n"
    "s_waitcnt vmcnt(8)\n"
    "s_barrier\n"
    "s_add_u32 m0, s35, 98304\n"
    "s_nop 0\n"
    "global_load_lds_dwordx4 v46, s[30:31] offset:0\n"
    "ds_read_b128 v[128:131], v56 offset:0\n"
    "ds_read_b128 v[132:135], v56 offset:2048\n"
    "ds_read_b128 v[136:139], v56 offset:4096\n"
    "global_load_lds_dwordx4 v47, s[30:31] offset:1024\n"
    "s_bfe_u32 s66, s28, 0x80010\n"
    "s_mul_i32 s66, s66, 53248\n"
    "s_add_u32 s30, s6, s66\n"
    "s_addc_u32 s31, s7, 0\n"
    "ds_read_b128 v[140:143], v56 offset:6144\n"
    "ds_read_b64_tr_b16 v[144:145], v76 offset:0\n"
    "ds_read_b64_tr_b16 v[146:147], v76 offset:1024\n"
    "s_add_u32 m0, s34, 98304\n"
    "s_nop 0\n"
    "global_load_lds_dwordx4 v52, s[36:37] offset:0\n"
    "ds_read_b64_tr_b16 v[148:149], v77 offset:0\n"
    "ds_read_b64_tr_b16 v[150:151], v77 offset:1024\n"
    "ds_read_b64_tr_b16 v[152:153], v78 offset:0\n"
    "global_load_lds_dwordx4 v53, s[36:37] offset:1024\n"
    "ds_read_b64_tr_b16 v[154:155], v78 offset:1024\n"
    "ds_read_b64_tr_b16 v[156:157], v79 offset:0\n"
    "ds_read_b64_tr_b16 v[158:159], v79 offset:1024\n"
    "global_load_lds_dwordx4 v54, s[36:37] offset:2048\n"
    "ds_read_b128 v[160:163], v57 offset:0\n"
    "ds_read_b128 v[164:167], v57 offset:2048\n"
    "ds_read_b128 v[168:171], v57 offset:4096\n"
    "global_load_lds_dwordx4 v55, s[36:37] offset:3072\n"
    "ds_read_b128 v[172:175], v57 offset:6144\n"
    "ds_read_b64_tr_b16 v[176:177], v76 offset:8192\n"
    "ds_read_b64_tr_b16 v[178:179], v76 offset:9216\n"
    "global_load_dwordx2 v[94:95], v42, s[18:19] offset:32\n"
    "ds_read_b64_tr_b16 v[180:181], v77 offset:8192\n"
    "ds_read_b64_tr_b16 v[182:183], v77 offset:9216\n"
    "ds_read_b64_tr_b16 v[184:185], v78 offset:8192\n"
    "ds_read_b64_tr_b16 v[186:187], v78 offset:9216\n"
    "ds_read_b64_tr_b16 v[188:189], v79 offset:8192\n"
    "ds_read_b64_tr_b16 v[190:191], v79 offset:9216\n"
    "s_waitcnt vmcnt(12) lgkmcnt(0)\n"
    "s_barrier\n"
    "v_mfma_f32_16x16x32_bf16 v[192:195], v[144:147], v[128:131], v[192:195]\n"
    "v_lshlrev_b32 v109, 7, v109\n"
    "v_mfma_f32_16x16x32_bf16 v[192:195], v[176:179], v[160:163], v[192:195]\n"
    "v_mfma_f32_16x16x32_bf16 v[196:199], v[176:179], v[164:167], v[196:199]\n"
    "v_add_u32_dpp v52, v108, v48 quad_perm:[0,0,0,0] row_mask:0xf bank_mask:0xf\n"
    "v_mfma_f32_16x16x32_bf16 v[196:199], v[144:147], v[132:135], v[196:199]\n"
    "v_mfma_f32_16x16x32_bf16 v[200:203], v[144:147], v[136:139], v[200:203]\n"
    "v_add_u32_dpp v53, v108, v49 quad_perm:[1,1,1,1] row_mask:0xf bank_mask:0xf\n"
    "v_mfma_f32_16x16x32_bf16 v[200:203], v[176:179], v[168:171], v[200:203]\n"
    "v_mfma_f32_16x16x32_bf16 v[204:207], v[176:179], v[172:175], v[204:207]\n"
    "v_add_u32_dpp v54, v108, v50 quad_perm:[2,2,2,2] row_mask:0xf bank_mask:0xf\n"
    "v_mfma_f32_16x16x32_bf16 v[204:207], v[144:147], v[140:143], v[204:207]\n"
    "v_mfma_f32_16x16x32_bf16 v[220:223], v[148:151], v[140:143], v[220:223]\n"
    "v_add_u32_dpp v55, v108, v51 quad_perm:[3,3,3,3] row_mask:0xf bank_mask:0xf\n"
    "v_mfma_f32_16x16x32_bf16 v[220:223], v[180:183], v[172:175], v[220:223]\n"
    "v_mfma_f32_16x16x32_bf16 v[216:219], v[180:183], v[168:171], v[216:219]\n"
    "v_mad_u32_u24 v16, v18, 36, v20\n"
    "v_mfma_f32_16x16x32_bf16 v[216:219], v[148:151], v[136:139], v[216:219]\n"
    "v_mfma_f32_16x16x32_bf16 v[212:215], v[148:151], v[132:135], v[212:215]\n"
    "s_add_u32 s41, s22, 2\n"
    "v_mfma_f32_16x16x32_bf16 v[212:215], v[180:183], v[164:167], v[212:215]\n"
    "v_mfma_f32_16x16x32_bf16 v[208:211], v[180:183], v[160:163], v[208:211]\n"
    "v_lshrrev_b32 v12, 5, v19\n"
    "v_mfma_f32_16x16x32_bf16 v[208:211], v[148:151], v[128:131], v[208:211]\n"
    "v_mfma_f32_16x16x32_bf16 v[224:227], v[152:155], v[128:131], v[224:227]\n"
    "v_sub_u32 v12, 1, v12\n"
    "v_mfma_f32_16x16x32_bf16 v[224:227], v[184:187], v[160:163], v[224:227]\n"
    "v_mfma_f32_16x16x32_bf16 v[228:231], v[184:187], v[164:167], v[228:231]\n"
    "v_and_b32 v12, 3, v12\n"
    "v_mfma_f32_16x16x32_bf16 v[228:231], v[152:155], v[132:135], v[228:231]\n"
    "v_mfma_f32_16x16x32_bf16 v[232:235], v[152:155], v[136:139], v[232:235]\n"
    "v_add_lshl_u32 v12, v12, s41, 12\n"
    "v_mfma_f32_16x16x32_bf16 v[232:235], v[184:187], v[168:171], v[232:235]\n"
    "v_mfma_f32_16x16x32_bf16 v[236:239], v[184:187], v[172:175], v[236:239]\n"
    "v_and_b32 v12, 0x7000, v12\n"
    "v_mfma_f32_16x16x32_bf16 v[236:239], v[152:155], v[140:143], v[236:239]\n"
    "v_mfma_f32_16x16x32_bf16 v[252:255], v[156:159], v[140:143], v[252:255]\n"
    "v_add3_u32 v15, v12, v15, s43\n"
    "v_mfma_f32_16x16x32_bf16 v[252:255], v[188:191], v[172:175], v[252:255]\n"
    "v_mfma_f32_16x16x32_bf16 v[248:251], v[188:191], v[168:171], v[248:251]\n"
    "v_lshrrev_b32 v12, 5, v20\n"
    "v_mfma_f32_16x16x32_bf16 v[248:251], v[156:159], v[136:139], v[248:251]\n"
    "v_mfma_f32_16x16x32_bf16 v[244:247], v[156:159], v[132:135], v[244:247]\n"
    "v_sub_u32 v12, 1, v12\n"
    "v_mfma_f32_16x16x32_bf16 v[244:247], v[188:191], v[164:167], v[244:247]\n"
    "v_mfma_f32_16x16x32_bf16 v[240:243], v[188:191], v[160:163], v[240:243]\n"
    "v_mfma_f32_16x16x32_bf16 v[240:243], v[156:159], v[128:131], v[240:243]\n"
    "s_waitcnt vmcnt(8)\n"
    "s_barrier\n"
    "s_add_u32 m0, s35, 0\n"
    "s_nop 0\n"
    "global_load_lds_dwordx4 v46, s[30:31] offset:0\n"
    "ds_read_b128 v[128:131], v56 offset:49152\n"
    "ds_read_b128 v[132:135], v56 offset:51200\n"
    "ds_read_b128 v[136:139], v56 offset:53248\n"
    "global_load_lds_dwordx4 v47, s[30:31] offset:1024\n"
    "s_add_u32 s30, s30, 212992\n"
    "s_addc_u32 s31, s31, 0\n"
    "ds_read_b128 v[140:143], v56 offset:55296\n"
    "ds_read_b64_tr_b16 v[144:145], v76 offset:49152\n"
    "ds_read_b64_tr_b16 v[146:147], v76 offset:50176\n"
    "s_add_u32 m0, s34, 0\n"
    "s_nop 0\n"
    "global_load_lds_dwordx4 v52, s[36:37] offset:0\n"
    "ds_read_b64_tr_b16 v[148:149], v77 offset:49152\n"
    "ds_read_b64_tr_b16 v[150:151], v77 offset:50176\n"
    "ds_read_b64_tr_b16 v[152:153], v78 offset:49152\n"
    "global_load_lds_dwordx4 v53, s[36:37] offset:1024\n"
    "ds_read_b64_tr_b16 v[154:155], v78 offset:50176\n"
    "ds_read_b64_tr_b16 v[156:157], v79 offset:49152\n"
    "ds_read_b64_tr_b16 v[158:159], v79 offset:50176\n"
    "global_load_lds_dwordx4 v54, s[36:37] offset:2048\n"
    "ds_read_b128 v[160:163], v57 offset:49152\n"
    "ds_read_b128 v[164:167], v57 offset:51200\n"
    "ds_read_b128 v[168:171], v57 offset:53248\n"
    "global_load_lds_dwordx4 v55, s[36:37] offset:3072\n"
    "ds_read_b128 v[172:175], v57 offset:55296\n"
    "ds_read_b64_tr_b16 v[176:177], v76 offset:57344\n"
    "ds_read_b64_tr_b16 v[178:179], v76 offset:58368\n"
    "global_load_dwordx2 v[122:123], v42, s[18:19] offset:64\n"
    "ds_read_b64_tr_b16 v[180:181], v77 offset:57344\n"
    "ds_read_b64_tr_b16 v[182:183], v77 offset:58368\n"
    "ds_read_b64_tr_b16 v[184:185], v78 offset:57344\n"
    "ds_read_b64_tr_b16 v[186:187], v78 offset:58368\n"
    "ds_read_b64_tr_b16 v[188:189], v79 offset:57344\n"
    "ds_read_b64_tr_b16 v[190:191], v79 offset:58368\n"
    "s_waitcnt vmcnt(12) lgkmcnt(0)\n"
    "s_barrier\n"
    "v_mfma_f32_16x16x32_bf16 v[192:195], v[144:147], v[128:131], v[192:195]\n"
    "v_lshlrev_b32 v110, 7, v110\n"
    "v_mfma_f32_16x16x32_bf16 v[192:195], v[176:179], v[160:163], v[192:195]\n"
    "v_add_u32_dpp v52, v109, v48 quad_perm:[0,0,0,0] row_mask:0xf bank_mask:0xf\n"
    "v_mfma_f32_16x16x32_bf16 v[196:199], v[176:179], v[164:167], v[196:199]\n"
    "v_mfma_f32_16x16x32_bf16 v[196:199], v[144:147], v[132:135], v[196:199]\n"
    "v_add_u32_dpp v53, v109, v49 quad_perm:[1,1,1,1] row_mask:0xf bank_mask:0xf\n"
    "v_mfma_f32_16x16x32_bf16 v[200:203], v[144:147], v[136:139], v[200:203]\n"
    "v_mfma_f32_16x16x32_bf16 v[200:203], v[176:179], v[168:171], v[200:203]\n"
    "v_add_u32_dpp v54, v109, v50 quad_perm:[2,2,2,2] row_mask:0xf bank_mask:0xf\n"
    "v_mfma_f32_16x16x32_bf16 v[204:207], v[176:179], v[172:175], v[204:207]\n"
    "v_mfma_f32_16x16x32_bf16 v[204:207], v[144:147], v[140:143], v[204:207]\n"
    "v_add_u32_dpp v55, v109, v51 quad_perm:[3,3,3,3] row_mask:0xf bank_mask:0xf\n"
    "v_mfma_f32_16x16x32_bf16 v[220:223], v[148:151], v[140:143], v[220:223]\n"
    "v_mfma_f32_16x16x32_bf16 v[220:223], v[180:183], v[172:175], v[220:223]\n"
    "v_and_b32 v12, 3, v12\n"
    "v_mfma_f32_16x16x32_bf16 v[216:219], v[180:183], v[168:171], v[216:219]\n"
    "v_mfma_f32_16x16x32_bf16 v[216:219], v[148:151], v[136:139], v[216:219]\n"
    "v_add_lshl_u32 v12, v12, s41, 12\n"
    "v_mfma_f32_16x16x32_bf16 v[212:215], v[148:151], v[132:135], v[212:215]\n"
    "v_mfma_f32_16x16x32_bf16 v[212:215], v[180:183], v[164:167], v[212:215]\n"
    "v_and_b32 v12, 0x7000, v12\n"
    "v_mfma_f32_16x16x32_bf16 v[208:211], v[180:183], v[160:163], v[208:211]\n"
    "v_mfma_f32_16x16x32_bf16 v[208:211], v[148:151], v[128:131], v[208:211]\n"
    "v_add3_u32 v16, v12, v16, s43\n"
    "v_mfma_f32_16x16x32_bf16 v[224:227], v[152:155], v[128:131], v[224:227]\n"
    "v_mfma_f32_16x16x32_bf16 v[224:227], v[184:187], v[160:163], v[224:227]\n"
    "v_add_u32 v16, 3456, v16\n"
    "v_mfma_f32_16x16x32_bf16 v[228:231], v[184:187], v[164:167], v[228:231]\n"
    "v_mfma_f32_16x16x32_bf16 v[228:231], v[152:155], v[132:135], v[228:231]\n"
    "s_mul_i32 s41, s47, 13\n"
    "v_mfma_f32_16x16x32_bf16 v[232:235], v[152:155], v[136:139], v[232:235]\n"
    "v_mfma_f32_16x16x32_bf16 v[232:235], v[184:187], v[168:171], v[232:235]\n"
    "s_sub_u32 s41, s3, s41\n"
    "v_mfma_f32_16x16x32_bf16 v[236:239], v[184:187], v[172:175], v[236:239]\n"
    "v_mfma_f32_16x16x32_bf16 v[236:239], v[152:155], v[140:143], v[236:239]\n"
    "s_lshl1_add_u32 s41, s41, s21\n"
    "v_mfma_f32_16x16x32_bf16 v[252:255], v[156:159], v[140:143], v[252:255]\n"
    "v_mfma_f32_16x16x32_bf16 v[252:255], v[188:191], v[172:175], v[252:255]\n"
    "s_mul_i32 s41, s41, 3\n"
    "v_mfma_f32_16x16x32_bf16 v[248:251], v[188:191], v[168:171], v[248:251]\n"
    "v_mfma_f32_16x16x32_bf16 v[248:251], v[156:159], v[136:139], v[248:251]\n"
    "s_add_u32 s41, s41, s22\n"
    "v_mfma_f32_16x16x32_bf16 v[244:247], v[156:159], v[132:135], v[244:247]\n"
    "v_mfma_f32_16x16x32_bf16 v[244:247], v[188:191], v[164:167], v[244:247]\n"
    "v_mov_b32 v24, s41\n"
    "v_mfma_f32_16x16x32_bf16 v[240:243], v[188:191], v[160:163], v[240:243]\n"
    "v_mfma_f32_16x16x32_bf16 v[240:243], v[156:159], v[128:131], v[240:243]\n"
    "s_waitcnt vmcnt(8)\n"
    "s_barrier\n"
    "s_add_u32 m0, s35, 49152\n"
    "s_nop 0\n"
    "global_load_lds_dwordx4 v46, s[30:31] offset:0\n"
    "ds_read_b128 v[128:131], v58 offset:0\n"
    "ds_read_b128 v[132:135], v58 offset:2048\n"
    "ds_read_b128 v[136:139], v58 offset:4096\n"
    "global_load_lds_dwordx4 v47, s[30:31] offset:1024\n"
    "s_add_u32 s30, s30, 212992\n"
    "s_addc_u32 s31, s31, 0\n"
    "ds_read_b128 v[140:143], v58 offset:6144\n"
    "ds_read_b64_tr_b16 v[144:145], v80 offset:0\n"
    "ds_read_b64_tr_b16 v[146:147], v80 offset:1024\n"
    "s_add_u32 m0, s34, 49152\n"
    "s_nop 0\n"
    "global_load_lds_dwordx4 v52, s[36:37] offset:0\n"
    "ds_read_b64_tr_b16 v[148:149], v81 offset:0\n"
    "ds_read_b64_tr_b16 v[150:151], v81 offset:1024\n"
    "ds_read_b64_tr_b16 v[152:153], v82 offset:0\n"
    "global_load_lds_dwordx4 v53, s[36:37] offset:1024\n"
    "ds_read_b64_tr_b16 v[154:155], v82 offset:1024\n"
    "ds_read_b64_tr_b16 v[156:157], v83 offset:0\n"
    "ds_read_b64_tr_b16 v[158:159], v83 offset:1024\n"
    "global_load_lds_dwordx4 v54, s[36:37] offset:2048\n"
    "ds_read_b128 v[160:163], v59 offset:0\n"
    "ds_read_b128 v[164:167], v59 offset:2048\n"
    "ds_read_b128 v[168:171], v59 offset:4096\n"
    "global_load_lds_dwordx4 v55, s[36:37] offset:3072\n"
    "ds_read_b128 v[172:175], v59 offset:6144\n"
    "ds_read_b64_tr_b16 v[176:177], v80 offset:8192\n"
    "ds_read_b64_tr_b16 v[178:179], v80 offset:9216\n"
    "global_load_dwordx2 v[124:125], v42, s[18:19] offset:96\n"
    "ds_read_b64_tr_b16 v[180:181], v81 offset:8192\n"
    "ds_read_b64_tr_b16 v[182:183], v81 offset:9216\n"
    "ds_read_b64_tr_b16 v[184:185], v82 offset:8192\n"
    "ds_read_b64_tr_b16 v[186:187], v82 offset:9216\n"
    "ds_read_b64_tr_b16 v[188:189], v83 offset:8192\n"
    "ds_read_b64_tr_b16 v[190:191], v83 offset:9216\n"
    "s_waitcnt vmcnt(12) lgkmcnt(0)\n"
    "s_barrier\n"
    "v_mfma_f32_16x16x32_bf16 v[192:195], v[144:147], v[128:131], v[192:195]\n"
    "v_lshlrev_b32 v111, 7, v111\n"
    "v_mfma_f32_16x16x32_bf16 v[192:195], v[176:179], v[160:163], v[192:195]\n"
    "v_mfma_f32_16x16x32_bf16 v[196:199], v[176:179], v[164:167], v[196:199]\n"
    "v_add_u32_dpp v52, v110, v48 quad_perm:[0,0,0,0] row_mask:0xf bank_mask:0xf\n"
    "v_mfma_f32_16x16x32_bf16 v[196:199], v[144:147], v[132:135], v[196:199]\n"
    "v_mfma_f32_16x16x32_bf16 v[200:203], v[144:147], v[136:139], v[200:203]\n"
    "v_add_u32_dpp v53, v110, v49 quad_perm:[1,1,1,1] row_mask:0xf bank_mask:0xf\n"
    "v_mfma_f32_16x16x32_bf16 v[200:203], v[176:179], v[168:171], v[200:203]\n"
    "v_mfma_f32_16x16x32_bf16 v[204:207], v[176:179], v[172:175], v[204:207]\n"
    "v_add_u32_dpp v54, v110, v50 quad_perm:[2,2,2,2] row_mask:0xf bank_mask:0xf\n"
    "v_mfma_f32_16x16x32_bf16 v[204:207], v[144:147], v[140:143], v[204:207]\n"
    "v_mfma_f32_16x16x32_bf16 v[220:223], v[148:151], v[140:143], v[220:223]\n"
    "v_add_u32_dpp v55, v110, v51 quad_perm:[3,3,3,3] row_mask:0xf bank_mask:0xf\n"
    "v_mfma_f32_16x16x32_bf16 v[220:223], v[180:183], v[172:175], v[220:223]\n"
    "v_mfma_f32_16x16x32_bf16 v[216:219], v[180:183], v[168:171], v[216:219]\n"
    "v_mfma_f32_16x16x32_bf16 v[216:219], v[148:151], v[136:139], v[216:219]\n"
    "v_readfirstlane_b32 s40, v24\n"
    "v_mfma_f32_16x16x32_bf16 v[212:215], v[148:151], v[132:135], v[212:215]\n"
    "v_mfma_f32_16x16x32_bf16 v[212:215], v[180:183], v[164:167], v[212:215]\n"
    "s_or_b32 s40, s40, s44\n"
    "v_mfma_f32_16x16x32_bf16 v[208:211], v[180:183], v[160:163], v[208:211]\n"
    "v_mfma_f32_16x16x32_bf16 v[208:211], v[148:151], v[128:131], v[208:211]\n"
    "s_lshl1_add_u32 s41, s40, s54\n"
    "v_mfma_f32_16x16x32_bf16 v[224:227], v[152:155], v[128:131], v[224:227]\n"
    "v_mfma_f32_16x16x32_bf16 v[224:227], v[184:187], v[160:163], v[224:227]\n"
    "s_cmpk_lt_u32 s40, 256\n"
    "v_mfma_f32_16x16x32_bf16 v[228:231], v[184:187], v[164:167], v[228:231]\n"
    "v_mfma_f32_16x16x32_bf16 v[228:231], v[152:155], v[132:135], v[228:231]\n"
    "s_cselect_b32 s41, s41, 2\n"
    "v_mfma_f32_16x16x32_bf16 v[232:235], v[152:155], v[136:139], v[232:235]\n"
    "v_mfma_f32_16x16x32_bf16 v[232:235], v[184:187], v[168:171], v[232:235]\n"
    "v_mfma_f32_16x16x32_bf16 v[236:239], v[184:187], v[172:175], v[236:239]\n"
    "v_mov_b32 v24, s41\n"
    "v_mfma_f32_16x16x32_bf16 v[236:239], v[152:155], v[140:143], v[236:239]\n"
    "v_mfma_f32_16x16x32_bf16 v[252:255], v[156:159], v[140:143], v[252:255]\n"
    "s_mul_i32 s41, s41, 52\n"
    "v_mfma_f32_16x16x32_bf16 v[252:255], v[188:191], v[172:175], v[252:255]\n"
    "v_mfma_f32_16x16x32_bf16 v[248:251], v[188:191], v[168:171], v[248:251]\n"
    "s_add_u32 s38, s4, s41\n"
    "v_mfma_f32_16x16x32_bf16 v[248:251], v[156:159], v[136:139], v[248:251]\n"
    "v_mfma_f32_16x16x32_bf16 v[244:247], v[156:159], v[132:135], v[244:247]\n"
    "s_addc_u32 s39, s5, 0\n"
    "v_mfma_f32_16x16x32_bf16 v[244:247], v[188:191], v[164:167], v[244:247]\n"
    "v_mfma_f32_16x16x32_bf16 v[240:243], v[188:191], v[160:163], v[240:243]\n"
    "v_mfma_f32_16x16x32_bf16 v[240:243], v[156:159], v[128:131], v[240:243]\n"
    "s_waitcnt vmcnt(8)\n"
    "s_barrier\n"
    "s_add_u32 m0, s35, 98304\n"
    "s_nop 0\n"
    "global_load_lds_dwordx4 v46, s[30:31] offset:0\n"
    "ds_read_b128 v[128:131], v56 offset:0\n"
    "ds_read_b128 v[132:135], v56 offset:2048\n"
    "ds_read_b128 v[136:139], v56 offset:4096\n"
    "global_load_lds_dwordx4 v47, s[30:31] offset:1024\n"
    "s_add_u32 s30, s30, 212992\n"
    "s_addc_u32 s31, s31, 0\n"
    "ds_read_b128 v[140:143], v56 offset:6144\n"
    "ds_read_b64_tr_b16 v[144:145], v76 offset:0\n"
    "ds_read_b64_tr_b16 v[146:147], v76 offset:1024\n"
    "s_add_u32 m0, s34, 98304\n"
    "s_nop 0\n"
    "global_load_lds_dwordx4 v52, s[36:37] offset:0\n"
    "ds_read_b64_tr_b16 v[148:149], v77 offset:0\n"
    "ds_read_b64_tr_b16 v[150:151], v77 offset:1024\n"
    "ds_read_b64_tr_b16 v[152:153], v78 offset:0\n"
    "global_load_lds_dwordx4 v53, s[36:37] offset:1024\n"
    "ds_read_b64_tr_b16 v[154:155], v78 offset:1024\n"
    "ds_read_b64_tr_b16 v[156:157], v79 offset:0\n"
    "ds_read_b64_tr_b16 v[158:159], v79 offset:1024\n"
    "global_load_lds_dwordx4 v54, s[36:37] offset:2048\n"
    "ds_read_b128 v[160:163], v57 offset:0\n"
    "ds_read_b128 v[164:167], v57 offset:2048\n"
    "ds_read_b128 v[168:171], v57 offset:4096\n"
    "global_load_lds_dwordx4 v55, s[36:37] offset:3072\n"
    "ds_read_b128 v[172:175], v57 offset:6144\n"
    "ds_read_b64_tr_b16 v[176:177], v76 offset:8192\n"
    "ds_read_b64_tr_b16 v[178:179], v76 offset:9216\n"
    "global_load_dwordx3 v[36:38], v17, s[38:39] offset:-104\n"
    "ds_read_b64_tr_b16 v[180:181], v77 offset:8192\n"
    "ds_read_b64_tr_b16 v[182:183], v77 offset:9216\n"
    "ds_read_b64_tr_b16 v[184:185], v78 offset:8192\n"
    "ds_read_b64_tr_b16 v[186:187], v78 offset:9216\n"
    "ds_read_b64_tr_b16 v[188:189], v79 offset:8192\n"
    "ds_read_b64_tr_b16 v[190:191], v79 offset:9216\n"
    "s_waitcnt vmcnt(12) lgkmcnt(0)\n"
    "s_barrier\n"
    "v_mfma_f32_16x16x32_bf16 v[192:195], v[144:147], v[128:131], v[192:195]\n"
    "v_lshlrev_b32 v112, 7, v112\n"
    "v_mfma_f32_16x16x32_bf16 v[192:195], v[176:179], v[160:163], v[192:195]\n"
    "v_add_u32_dpp v52, v111, v48 quad_perm:[0,0,0,0] row_mask:0xf bank_mask:0xf\n"
    "v_mfma_f32_16x16x32_bf16 v[196:199], v[176:179], v[164:167], v[196:199]\n"
    "v_add_u32_dpp v53, v111, v49 quad_perm:[1,1,1,1] row_mask:0xf bank_mask:0xf\n"
    "v_mfma_f32_16x16x32_bf16 v[196:199], v[144:147], v[132:135], v[196:199]\n"
    "v_add_u32_dpp v54, v111, v50 quad_perm:[2,2,2,2] row_mask:0xf bank_mask:0xf\n"
    "v_mfma_f32_16x16x32_bf16 v[200:203], v[144:147], v[136:139], v[200:203]\n"
    "v_add_u32_dpp v55, v111, v51 quad_perm:[3,3,3,3] row_mask:0xf bank_mask:0xf\n"
    "v_mfma_f32_16x16x32_bf16 v[200:203], v[176:179], v[168:171], v[200:203]\n"
    "v_lshlrev_b32 v60, 16, v84\n"
    "v_mfma_f32_16x16x32_bf16 v[204:207], v[176:179], v[172:175], v[204:207]\n"
    "v_and_b32 v61, 0xffff0000, v84\n"
    "v_mfma_f32_16x16x32_bf16 v[204:207], v[144:147], v[140:143], v[204:207]\n"
    "v_mfma_f32_16x16x32_bf16 v[220:223], v[148:151], v[140:143], v[220:223]\n"
    "v_lshlrev_b32 v62, 16, v85\n"
    "v_mfma_f32_16x16x32_bf16 v[220:223], v[180:183], v[172:175], v[220:223]\n"
    "v_and_b32 v63, 0xffff0000, v85\n"
    "v_mfma_f32_16x16x32_bf16 v[216:219], v[180:183], v[168:171], v[216:219]\n"
    "v_lshlrev_b32 v64, 16, v86\n"
    "v_mfma_f32_16x16x32_bf16 v[216:219], v[148:151], v[136:139], v[216:219]\n"
    "v_and_b32 v65, 0xffff0000, v86\n"
    "v_mfma_f32_16x16x32_bf16 v[212:215], v[148:151], v[132:135], v[212:215]\n"
    "v_lshlrev_b32 v66, 16, v87\n"
    "v_mfma_f32_16x16x32_bf16 v[212:215], v[180:183], v[164:167], v[212:215]\n"
    "v_and_b32 v67, 0xffff0000, v87\n"
    "v_mfma_f32_16x16x32_bf16 v[208:211], v[180:183], v[160:163], v[208:211]\n"
    "v_lshlrev_b32 v68, 16, v88\n"
    "v_mfma_f32_16x16x32_bf16 v[208:211], v[148:151], v[128:131], v[208:211]\n"
    "v_mfma_f32_16x16x32_bf16 v[224:227], v[152:155], v[128:131], v[224:227]\n"
    "v_and_b32 v69, 0xffff0000, v88\n"
    "v_mfma_f32_16x16x32_bf16 v[224:227], v[184:187], v[160:163], v[224:227]\n"
    "v_lshlrev_b32 v70, 16, v89\n"
    "v_mfma_f32_16x16x32_bf16 v[228:231], v[184:187], v[164:167], v[228:231]\n"
    "v_and_b32 v71, 0xffff0000, v89\n"
    "v_mfma_f32_16x16x32_bf16 v[228:231], v[152:155], v[132:135], v[228:231]\n"
    "v_lshlrev_b32 v72, 16, v90\n"
    "v_mfma_f32_16x16x32_bf16 v[232:235], v[152:155], v[136:139], v[232:235]\n"
    "v_and_b32 v73, 0xffff0000, v90\n"
    "v_mfma_f32_16x16x32_bf16 v[232:235], v[184:187], v[168:171], v[232:235]\n"
    "v_lshlrev_b32 v74, 16, v91\n"
    "v_mfma_f32_16x16x32_bf16 v[236:239], v[184:187], v[172:175], v[236:239]\n"
    "v_and_b32 v75, 0xffff0000, v91\n"
    "v_mfma_f32_16x16x32_bf16 v[236:239], v[152:155], v[140:143], v[236:239]\n"
    "v_mfma_f32_16x16x32_bf16 v[252:255], v[156:159], v[140:143], v[252:255]\n"
    "s_lshl3_add_u32 s40, s2, 4096\n"
    "v_mfma_f32_16x16x32_bf16 v[252:255], v[188:191], v[172:175], v[252:255]\n"
    "s_lshl_b32 s41, s2, 2\n"
    "v_mfma_f32_16x16x32_bf16 v[248:251], v[188:191], v[168:171], v[248:251]\n"
    "v_lshlrev_b32 v11, 3, v1\n"
    "v_mfma_f32_16x16x32_bf16 v[248:251], v[156:159], v[136:139], v[248:251]\n"
    "v_add_u32 v11, 40960, v11\n"
    "v_mfma_f32_16x16x32_bf16 v[244:247], v[156:159], v[132:135], v[244:247]\n"
    "v_writelane_b32 v11, s40, 8\n"
    "v_mfma_f32_16x16x32_bf16 v[244:247], v[188:191], v[164:167], v[244:247]\n"
    "v_writelane_b32 v11, s41, 9\n"
    "v_mfma_f32_16x16x32_bf16 v[240:243], v[188:191], v[160:163], v[240:243]\n"
    "v_mfma_f32_16x16x32_bf16 v[240:243], v[156:159], v[128:131], v[240:243]\n"
    "s_waitcnt vmcnt(8)\n"
    "s_barrier\n"
    "s_add_u32 m0, s35, 0\n"
    "s_nop 0\n"
    "global_load_lds_dwordx4 v46, s[30:31] offset:0\n"
    "ds_read_b128 v[128:131], v56 offset:49152\n"
    "ds_read_b128 v[132:135], v56 offset:51200\n"
    "ds_read_b128 v[136:139], v56 offset:53248\n"
    "global_load_lds_dwordx4 v47, s[30:31] offset:1024\n"
    "s_bfe_u32 s66, s28, 0x80018\n"
    "s_mul_i32 s66, s66, 53248\n"
    "s_add_u32 s30, s6, s66\n"
    "s_addc_u32 s31, s7, 0\n"
    "ds_read_b128 v[140:143], v56 offset:55296\n"
    "ds_read_b64_tr_b16 v[144:145], v76 offset:49152\n"
    "ds_read_b64_tr_b16 v[146:147], v76 offset:50176\n"
    "s_add_u32 m0, s34, 0\n"
    "s_nop 0\n"
    "global_load_lds_dwordx4 v52, s[36:37] offset:0\n"
    "ds_read_b64_tr_b16 v[148:149], v77 offset:49152\n"
    "ds_read_b64_tr_b16 v[150:151], v77 offset:50176\n"
    "ds_read_b64_tr_b16 v[152:153], v78 offset:49152\n"
    "global_load_lds_dwordx4 v53, s[36:37] offset:1024\n"
    "ds_read_b64_tr_b16 v[154:155], v78 offset:50176\n"
    "ds_read_b64_tr_b16 v[156:157], v79 offset:49152\n"
    "ds_read_b64_tr_b16 v[158:159], v79 offset:50176\n"
    "global_load_lds_dwordx4 v54, s[36:37] offset:2048\n"
    "ds_read_b128 v[160:163], v57 offset:49152\n"
    "ds_read_b128 v[164:167], v57 offset:51200\n"
    "ds_read_b128 v[168:171], v57 offset:53248\n"
    "global_load_lds_dwordx4 v55, s[36:37] offset:3072\n"
    "ds_read_b128 v[172:175], v57 offset:55296\n"
    "ds_read_b64_tr_b16 v[176:177], v76 offset:57344\n"
    "ds_read_b64_tr_b16 v[178:179], v76 offset:58368\n"
    "global_load_dword v41, v18, s[38:39] offset:-8\n"
    "ds_read_b64_tr_b16 v[180:181], v77 offset:57344\n"
    "ds_read_b64_tr_b16 v[182:183], v77 offset:58368\n"
    "ds_read_b64_tr_b16 v[184:185], v78 offset:57344\n"
    "ds_read_b64_tr_b16 v[186:187], v78 offset:58368\n"
    "ds_read_b64_tr_b16 v[188:189], v79 offset:57344\n"
    "ds_read_b64_tr_b16 v[190:191], v79 offset:58368\n"
    "s_waitcnt vmcnt(12) lgkmcnt(0)\n"
    "s_barrier\n"
    "v_mfma_f32_16x16x32_bf16 v[192:195], v[144:147], v[128:131], v[192:195]\n"
    "v_mfma_f32_16x16x32_bf16 v[192:195], v[176:179], v[160:163], v[192:195]\n"
    "v_lshlrev_b32 v113, 7, v113\n"
    "v_mfma_f32_16x16x32_bf16 v[196:199], v[176:179], v[164:167], v[196:199]\n"
    "v_mfma_f32_16x16x32_bf16 v[196:199], v[144:147], v[132:135], v[196:199]\n"
    "v_add_u32_dpp v52, v112, v48 quad_perm:[0,0,0,0] row_mask:0xf bank_mask:0xf\n"
    "v_mfma_f32_16x16x32_bf16 v[200:203], v[144:147], v[136:139], v[200:203]\n"
    "v_mfma_f32_16x16x32_bf16 v[200:203], v[176:179], v[168:171], v[200:203]\n"
    "v_add_u32_dpp v53, v112, v49 quad_perm:[1,1,1,1] row_mask:0xf bank_mask:0xf\n"
    "v_mfma_f32_16x16x32_bf16 v[204:207], v[176:179], v[172:175], v[204:207]\n"
    "v_mfma_f32_16x16x32_bf16 v[204:207], v[144:147], v[140:143], v[204:207]\n"
    "v_add_u32_dpp v54, v112, v50 quad_perm:[2,2,2,2] row_mask:0xf bank_mask:0xf\n"
    "v_mfma_f32_16x16x32_bf16 v[220:223], v[148:151], v[140:143], v[220:223]\n"
    "v_mfma_f32_16x16x32_bf16 v[220:223], v[180:183], v[172:175], v[220:223]\n"
    "v_add_u32_dpp v55, v112, v51 quad_perm:[3,3,3,3] row_mask:0xf bank_mask:0xf\n"
    "v_mfma_f32_16x16x32_bf16 v[216:219], v[180:183], v[168:171], v[216:219]\n"
    "v_mfma_f32_16x16x32_bf16 v[216:219], v[148:151], v[136:139], v[216:219]\n"
    "v_mfma_f32_16x16x32_bf16 v[212:215], v[148:151], v[132:135], v[212:215]\n"
    "v_mfma_f32_16x16x32_bf16 v[212:215], v[180:183], v[164:167], v[212:215]\n"
    "v_mfma_f32_16x16x32_bf16 v[208:211], v[180:183], v[160:163], v[208:211]\n"
    "v_add_f32 v192, v192, v60\n"
    "v_mfma_f32_16x16x32_bf16 v[208:211], v[148:151], v[128:131], v[208:211]\n"
    "v_add_f32 v193, v193, v61\n"
    "v_mfma_f32_16x16x32_bf16 v[224:227], v[152:155], v[128:131], v[224:227]\n"
    "v_add_f32 v194, v194, v62\n"
    "v_mfma_f32_16x16x32_bf16 v[224:227], v[184:187], v[160:163], v[224:227]\n"
    "v_add_f32 v195, v195, v63\n"
    "v_mfma_f32_16x16x32_bf16 v[228:231], v[184:187], v[164:167], v[228:231]\n"
    "v_add_f32 v196, v196, v60\n"
    "v_mfma_f32_16x16x32_bf16 v[228:231], v[152:155], v[132:135], v[228:231]\n"
    "v_add_f32 v197, v197, v61\n"
    "v_mfma_f32_16x16x32_bf16 v[232:235], v[152:155], v[136:139], v[232:235]\n"
    "v_add_f32 v198, v198, v62\n"
    "v_mfma_f32_16x16x32_bf16 v[232:235], v[184:187], v[168:171], v[232:235]\n"
    "v_add_f32 v199, v199, v63\n"
    "v_mfma_f32_16x16x32_bf16 v[236:239], v[184:187], v[172:175], v[236:239]\n"
    "v_add_f32 v200, v200, v60\n"
    "v_mfma_f32_16x16x32_bf16 v[236:239], v[152:155], v[140:143], v[236:239]\n"
    "v_add_f32 v201, v201, v61\n"
    "v_mfma_f32_16x16x32_bf16 v[252:255], v[156:159], v[140:143], v[252:255]\n"
    "v_add_f32 v202, v202, v62\n"
    "v_mfma_f32_16x16x32_bf16 v[252:255], v[188:191], v[172:175], v[252:255]\n"
    "v_add_f32 v203, v203, v63\n"
    "v_mfma_f32_16x16x32_bf16 v[248:251], v[188:191], v[168:171], v[248:251]\n"
    "v_add_f32 v204, v204, v60\n"
    "v_mfma_f32_16x16x32_bf16 v[248:251], v[156:159], v[136:139], v[248:251]\n"
    "v_add_f32 v205, v205, v61\n"
    "v_mfma_f32_16x16x32_bf16 v[244:247], v[156:159], v[132:135], v[244:247]\n"
    "v_add_f32 v206, v206, v62\n"
    "v_mfma_f32_16x16x32_bf16 v[244:247], v[188:191], v[164:167], v[244:247]\n"
    "v_add_f32 v207, v207, v63\n"
    "v_mfma_f32_16x16x32_bf16 v[240:243], v[188:191], v[160:163], v[240:243]\n"
    "v_mfma_f32_16x16x32_bf16 v[240:243], v[156:159], v[128:131], v[240:243]\n"
    "s_waitcnt vmcnt(8)\n"
    "s_barrier\n"
    "s_cmp_eq_u32 s20, 3\n"
    "s_cbranch_scc1 .Lxd_on1\n"
    ".Lxd_on1r:\n"
    "s_add_u32 m0, s35, 49152\n"
    "s_nop 0\n"
    "global_load_lds_dwordx4 v46, s[30:31] offset:0\n"
    "ds_read_b128 v[128:131], v58 offset:0\n"
    "ds_read_b128 v[132:135], v58 offset:2048\n"
    "ds_read_b128 v[136:139], v58 offset:4096\n"
    "ds_read_b128 v[140:143], v58 offset:6144\n"
    "global_load_lds_dwordx4 v47, s[30:31] offset:1024\n"
    "s_add_u32 s30, s30, 212992\n"
    "s_addc_u32 s31, s31, 0\n"
    "ds_read_b64_tr_b16 v[144:145], v80 offset:0\n"
    "ds_read_b64_tr_b16 v[146:147], v80 offset:1024\n"
    "ds_read_b64_tr_b16 v[148:149], v81 offset:0\n"
    "ds_read_b64_tr_b16 v[150:151], v81 offset:1024\n"
    "s_add_u32 m0, s34, 49152\n"
    "s_nop 0\n"
    "global_load_lds_dwordx4 v52, s[36:37] offset:0\n"
    "ds_read_b64_tr_b16 v[152:153], v82 offset:0\n"
    "ds_read_b64_tr_b16 v[154:155], v82 offset:1024\n"
    "ds_read_b64_tr_b16 v[156:157], v83 offset:0\n"
    "ds_read_b64_tr_b16 v[158:159], v83 offset:1024\n"
    "global_load_lds_dwordx4 v53, s[36:37] offset:1024\n"
    "ds_read_b128 v[160:163], v59 offset:0\n"
    "ds_read_b128 v[164:167], v59 offset:2048\n"
    "ds_read_b128 v[168:171], v59 offset:4096\n"
    "ds_read_b128 v[172:175], v59 offset:6144\n"
    "global_load_lds_dwordx4 v54, s[36:37] offset:2048\n"
    "ds_read_b64_tr_b16 v[176:177], v80 offset:8192\n"
    "ds_read_b64_tr_b16 v[178:179], v80 offset:9216\n"
    "ds_read_b64_tr_b16 v[180:181], v81 offset:8192\n"
    "ds_read_b64_tr_b16 v[182:183], v81 offset:9216\n"
    "global_load_lds_dwordx4 v55, s[36:37] offset:3072\n"
    "ds_read_b64_tr_b16 v[184:185], v82 offset:8192\n"
    "ds_read_b64_tr_b16 v[186:187], v82 offset:9216\n"
    "ds_read_b64_tr_b16 v[188:189], v83 offset:8192\n"
    "ds_read_b64_tr_b16 v[190:191], v83 offset:9216\n"
    "s_waitcnt vmcnt(11) lgkmcnt(0)\n"
    "s_barrier\n"
    "v_mfma_f32_16x16x32_bf16 v[192:195], v[144:147], v[128:131], v[192:195]\n"
    "v_mfma_f32_16x16x32_bf16 v[192:195], v[176:179], v[160:163], v[192:195]\n"
    "v_lshlrev_b32 v114, 7, v114\n"
    "v_mfma_f32_16x16x32_bf16 v[196:199], v[176:179], v[164:167], v[196:199]\n"
    "v_mfma_f32_16x16x32_bf16 v[196:199], v[144:147], v[132:135], v[196:199]\n"
    "v_add_u32_dpp v52, v113, v48 quad_perm:[0,0,0,0] row_mask:0xf bank_mask:0xf\n"
    "v_mfma_f32_16x16x32_bf16 v[200:203], v[144:147], v[136:139], v[200:203]\n"
    "v_mfma_f32_16x16x32_bf16 v[200:203], v[176:179], v[168:171], v[200:203]\n"
    "v_add_u32_dpp v53, v113, v49 quad_perm:[1,1,1,1] row_mask:0xf bank_mask:0xf\n"
    "v_mfma_f32_16x16x32_bf16 v[204:207], v[176:179], v[172:175], v[204:207]\n"
    "v_mfma_f32_16x16x32_bf16 v[204:207], v[144:147], v[140:143], v[204:207]\n"
    "v_add_u32_dpp v54, v113, v50 quad_perm:[2,2,2,2] row_mask:0xf bank_mask:0xf\n"
    "v_mfma_f32_16x16x32_bf16 v[220:223], v[148:151], v[140:143], v[220:223]\n"
    "v_mfma_f32_16x16x32_bf16 v[220:223], v[180:183], v[172:175], v[220:223]\n"
    "v_add_u32_dpp v55, v113, v51 quad_perm:[3,3,3,3] row_mask:0xf bank_mask:0xf\n"
    "v_mfma_f32_16x16x32_bf16 v[216:219], v[180:183], v[168:171], v[216:219]\n"
    "v_mfma_f32_16x16x32_bf16 v[216:219], v[148:151], v[136:139], v[216:219]\n"
    "v_mfma_f32_16x16x32_bf16 v[212:215], v[148:151], v[132:135], v[212:215]\n"
    "v_mfma_f32_16x16x32_bf16 v[212:215], v[180:183], v[164:167], v[212:215]\n"
    "v_mfma_f32_16x16x32_bf16 v[208:211], v[180:183], v[160:163], v[208:211]\n"
    "v_mfma_f32_16x16x32_bf16 v[208:211], v[148:151], v[128:131], v[208:211]\n"
    "v_mfma_f32_16x16x32_bf16 v[224:227], v[152:155], v[128:131], v[224:227]\n"
    "v_mfma_f32_16x16x32_bf16 v[224:227], v[184:187], v[160:163], v[224:227]\n"
    "v_mfma_f32_16x16x32_bf16 v[228:231], v[184:187], v[164:167], v[228:231]\n"
    "v_add_f32 v222, v222, v66\n"
    "v_add_f32 v223, v223, v67\n"
    "v_mfma_f32_16x16x32_bf16 v[228:231], v[152:155], v[132:135], v[228:231]\n"
    "v_add_f32 v221, v221, v65\n"
    "v_mfma_f32_16x16x32_bf16 v[232:235], v[152:155], v[136:139], v[232:235]\n"
    "v_add_f32 v220, v220, v64\n"
    "v_mfma_f32_16x16x32_bf16 v[232:235], v[184:187], v[168:171], v[232:235]\n"
    "v_add_f32 v219, v219, v67\n"
    "v_mfma_f32_16x16x32_bf16 v[236:239], v[184:187], v[172:175], v[236:239]\n"
    "v_add_f32 v217, v217, v65\n"
    "v_add_f32 v218, v218, v66\n"
    "v_mfma_f32_16x16x32_bf16 v[236:239], v[152:155], v[140:143], v[236:239]\n"
    "v_add_f32 v216, v216, v64\n"
    "v_mfma_f32_16x16x32_bf16 v[252:255], v[156:159], v[140:143], v[252:255]\n"
    "v_add_f32 v215, v215, v67\n"
    "v_mfma_f32_16x16x32_bf16 v[252:255], v[188:191], v[172:175], v[252:255]\n"
    "v_add_f32 v214, v214, v66\n"
    "v_mfma_f32_16x16x32_bf16 v[248:251], v[188:191], v[168:171], v[248:251]\n"
    "v_add_f32 v212, v212, v64\n"
    "v_add_f32 v213, v213, v65\n"
    "v_mfma_f32_16x16x32_bf16 v[248:251], v[156:159], v[136:139], v[248:251]\n"
    "v_add_f32 v211, v211, v67\n"
    "v_mfma_f32_16x16x32_bf16 v[244:247], v[156:159], v[132:135], v[244:247]\n"
    "v_add_f32 v210, v210, v66\n"
    "v_mfma_f32_16x16x32_bf16 v[244:247], v[188:191], v[164:167], v[244:247]\n"
    "v_add_f32 v209, v209, v65\n"
    "v_mfma_f32_16x16x32_bf16 v[240:243], v[188:191], v[160:163], v[240:243]\n"
    "v_add_f32 v208, v208, v64\n"
    "v_mfma_f32_16x16x32_bf16 v[240:243], v[156:159], v[128:131], v[240:243]\n"
    "s_waitcnt vmcnt(7)\n"
    "s_barrier\n"
    "s_add_u32 m0, s35, 98304\n"
    "s_nop 0\n"
    "global_load_lds_dwordx4 v46, s[30:31] offset:0\n"
    "ds_read_b128 v[128:131], v56 offset:0\n"
    "ds_read_b128 v[132:135], v56 offset:2048\n"
    "ds_read_b128 v[136:139], v56 offset:4096\n"
    "ds_read_b128 v[140:143], v56 offset:6144\n"
    "global_load_lds_dwordx4 v47, s[30:31] offset:1024\n"
    "s_add_u32 s30, s30, 212992\n"
    "s_addc_u32 s31, s31, 0\n"
    "ds_read_b64_tr_b16 v[144:145], v76 offset:0\n"
    "ds_read_b64_tr_b16 v[146:147], v76 offset:1024\n"
    "ds_read_b64_tr_b16 v[148:149], v77 offset:0\n"
    "ds_read_b64_tr_b16 v[150:151], v77 offset:1024\n"
    "s_add_u32 m0, s34, 98304\n"
    "s_nop 0\n"
    "global_load_lds_dwordx4 v52, s[36:37] offset:0\n"
    "ds_read_b64_tr_b16 v[152:153], v78 offset:0\n"
    "ds_read_b64_tr_b16 v[154:155], v78 offset:1024\n"
    "ds_read_b64_tr_b16 v[156:157], v79 offset:0\n"
    "ds_read_b64_tr_b16 v[158:159], v79 offset:1024\n"
    "global_load_lds_dwordx4 v53, s[36:37] offset:1024\n"
    "ds_read_b128 v[160:163], v57 offset:0\n"
    "ds_read_b128 v[164:167], v57 offset:2048\n"
    "ds_read_b128 v[168:171], v57 offset:4096\n"
    "ds_read_b128 v[172:175], v57 offset:6144\n"
    "global_load_lds_dwordx4 v54, s[36:37] offset:2048\n"
    "ds_read_b64_tr_b16 v[176:177], v76 offset:8192\n"
    "ds_read_b64_tr_b16 v[178:179], v76 offset:9216\n"
    "ds_read_b64_tr_b16 v[180:181], v77 offset:8192\n"
    "ds_read_b64_tr_b16 v[182:183], v77 offset:9216\n"
    "global_load_lds_dwordx4 v55, s[36:37] offset:3072\n"
    "ds_read_b64_tr_b16 v[184:185], v78 offset:8192\n"
    "ds_read_b64_tr_b16 v[186:187], v78 offset:9216\n"
    "ds_read_b64_tr_b16 v[188:189], v79 offset:8192\n"
    "ds_read_b64_tr_b16 v[190:191], v79 offset:9216\n"
    "s_waitcnt vmcnt(10) lgkmcnt(0)\n"
    "s_barrier\n"
    "v_mfma_f32_16x16x32_bf16 v[192:195], v[144:147], v[128:131], v[192:195]\n"
    "v_lshlrev_b32 v115, 7, v115\n"
    "v_mfma_f32_16x16x32_bf16 v[192:195], v[176:179], v[160:163], v[192:195]\n"
    "v_add_u32_dpp v52, v114, v48 quad_perm:[0,0,0,0] row_mask:0xf bank_mask:0xf\n"
    "v_mfma_f32_16x16x32_bf16 v[196:199], v[176:179], v[164:167], v[196:199]\n"
    "v_add_u32_dpp v53, v114, v49 quad_perm:[1,1,1,1] row_mask:0xf bank_mask:0xf\n"
    "v_mfma_f32_16x16x32_bf16 v[196:199], v[144:147], v[132:135], v[196:199]\n"
    "v_add_u32_dpp v54, v114, v50 quad_perm:[2,2,2,2] row_mask:0xf bank_mask:0xf\n"
    "v_mfma_f32_16x16x32_bf16 v[200:203], v[144:147], v[136:139], v[200:203]\n"
    "v_add_u32_dpp v55, v114, v51 quad_perm:[3,3,3,3] row_mask:0xf bank_mask:0xf\n"
    "v_mfma_f32_16x16x32_bf16 v[200:203], v[176:179], v[168:171], v[200:203]\n"
    "v_lshl_or_b32 v36, v36, 7, v19\n"
    "v_mfma_f32_16x16x32_bf16 v[204:207], v[176:179], v[172:175], v[204:207]\n"
    "v_mfma_f32_16x16x32_bf16 v[204:207], v[144:147], v[140:143], v[204:207]\n"
    "v_lshl_or_b32 v37, v37, 7, v19\n"
    "v_mfma_f32_16x16x32_bf16 v[220:223], v[148:151], v[140:143], v[220:223]\n"
    "v_lshl_or_b32 v38, v38, 7, v19\n"
    "v_mfma_f32_16x16x32_bf16 v[220:223], v[180:183], v[172:175], v[220:223]\n"
    "v_lshl_or_b32 v41, v41, 7, v20\n"
    "v_mfma_f32_16x16x32_bf16 v[216:219], v[180:183], v[168:171], v[216:219]\n"
    "s_mov_b32 s64, 0xbdd2d3e8\n"
    "v_mfma_f32_16x16x32_bf16 v[216:219], v[148:151], v[136:139], v[216:219]\n"
    "s_mov_b32 s65, 0xc0135761\n"
    "v_mfma_f32_16x16x32_bf16 v[212:215], v[148:151], v[132:135], v[212:215]\n"
    "v_mfma_f32_16x16x32_bf16 v[212:215], v[180:183], v[164:167], v[212:215]\n"
    "s_and_b32 s41, s20, 1\n"
    "v_mfma_f32_16x16x32_bf16 v[208:211], v[180:183], v[160:163], v[208:211]\n"
    "s_lshl1_add_u32 s40, s21, s41\n"
    "v_mfma_f32_16x16x32_bf16 v[208:211], v[148:151], v[128:131], v[208:211]\n"
    "s_lshl_b32 s41, s40, 8\n"
    "v_mfma_f32_16x16x32_bf16 v[224:227], v[152:155], v[128:131], v[224:227]\n"
    "s_add_u32 s3, s41, 149504\n"
    "v_mfma_f32_16x16x32_bf16 v[224:227], v[184:187], v[160:163], v[224:227]\n"
    "s_lshl_b32 s40, s40, 6\n"
    "v_mfma_f32_16x16x32_bf16 v[228:231], v[184:187], v[164:167], v[228:231]\n"
    "v_mfma_f32_16x16x32_bf16 v[228:231], v[152:155], v[132:135], v[228:231]\n"
    "s_add_u32 s40, s40, s24\n"
    "v_mfma_f32_16x16x32_bf16 v[232:235], v[152:155], v[136:139], v[232:235]\n"
    "s_lshl_b32 s41, s23, 12\n"
    "v_mfma_f32_16x16x32_bf16 v[232:235], v[184:187], v[168:171], v[232:235]\n"
    "s_add_u32 s40, s40, s41\n"
    "v_mfma_f32_16x16x32_bf16 v[236:239], v[184:187], v[172:175], v[236:239]\n"
    "s_lshl3_add_u32 s40, s40, 65536\n"
    "v_mfma_f32_16x16x32_bf16 v[236:239], v[152:155], v[140:143], v[236:239]\n"
    "s_add_u32 s48, s40, s16\n"
    "v_mfma_f32_16x16x32_bf16 v[252:255], v[156:159], v[140:143], v[252:255]\n"
    "v_mfma_f32_16x16x32_bf16 v[252:255], v[188:191], v[172:175], v[252:255]\n"
    "s_addc_u32 s49, s17, 0\n"
    "v_mfma_f32_16x16x32_bf16 v[248:251], v[188:191], v[168:171], v[248:251]\n"
    "s_cmp_lt_u32 s22, 2\n"
    "v_mfma_f32_16x16x32_bf16 v[248:251], v[156:159], v[136:139], v[248:251]\n"
    "s_cselect_b32 s24, 16, 0\n"
    "v_mfma_f32_16x16x32_bf16 v[244:247], v[156:159], v[132:135], v[244:247]\n"
    "s_sub_u32 s23, 3, s22\n"
    "v_mfma_f32_16x16x32_bf16 v[244:247], v[188:191], v[164:167], v[244:247]\n"
    "s_and_b32 s23, s23, 3\n"
    "v_mfma_f32_16x16x32_bf16 v[240:243], v[188:191], v[160:163], v[240:243]\n"
    "v_mfma_f32_16x16x32_bf16 v[240:243], v[156:159], v[128:131], v[240:243]\n"
    "s_waitcnt vmcnt(6)\n"
    "s_barrier\n"
    "s_add_u32 m0, s35, 0\n"
    "s_nop 0\n"
    "global_load_lds_dwordx4 v46, s[30:31] offset:0\n"
    "ds_read_b128 v[128:131], v56 offset:49152\n"
    "ds_read_b128 v[132:135], v56 offset:51200\n"
    "ds_read_b128 v[136:139], v56 offset:53248\n"
    "global_load_lds_dwordx4 v47, s[30:31] offset:1024\n"
    "s_add_u32 s30, s30, 212992\n"
    "s_addc_u32 s31, s31, 0\n"
    "ds_read_b128 v[140:143], v56 offset:55296\n"
    "ds_read_b64_tr_b16 v[144:145], v76 offset:49152\n"
    "ds_read_b64_tr_b16 v[146:147], v76 offset:50176\n"
    "s_add_u32 m0, s34, 0\n"
    "s_nop 0\n"
    "global_load_lds_dwordx4 v52, s[36:37] offset:0\n"
    "ds_read_b64_tr_b16 v[148:149], v77 offset:49152\n"
    "ds_read_b64_tr_b16 v[150:151], v77 offset:50176\n"
    "ds_read_b64_tr_b16 v[152:153], v78 offset:49152\n"
    "global_load_lds_dwordx4 v53, s[36:37] offset:1024\n"
    "ds_read_b64_tr_b16 v[154:155], v78 offset:50176\n"
    "ds_read_b64_tr_b16 v[156:157], v79 offset:49152\n"
    "ds_read_b64_tr_b16 v[158:159], v79 offset:50176\n"
    "global_load_lds_dwordx4 v54, s[36:37] offset:2048\n"
    "ds_read_b128 v[160:163], v57 offset:49152\n"
    "ds_read_b128 v[164:167], v57 offset:51200\n"
    "ds_read_b128 v[168:171], v57 offset:53248\n"
    "global_load_lds_dwordx4 v55, s[36:37] offset:3072\n"
    "ds_read_b128 v[172:175], v57 offset:55296\n"
    "ds_read_b64_tr_b16 v[176:177], v76 offset:57344\n"
    "ds_read_b64_tr_b16 v[178:179], v76 offset:58368\n"
    "global_load_dwordx4 v[28:31], v36, s[8:9]\n"
    "ds_read_b64_tr_b16 v[180:181], v77 offset:57344\n"
    "ds_read_b64_tr_b16 v[182:183], v77 offset:58368\n"
    "ds_read_b64_tr_b16 v[184:185], v78 offset:57344\n"
    "ds_read_b64_tr_b16 v[186:187], v78 offset:58368\n"
    "ds_read_b64_tr_b16 v[188:189], v79 offset:57344\n"
    "ds_read_b64_tr_b16 v[190:191], v79 offset:58368\n"
    "s_waitcnt vmcnt(11) lgkmcnt(0)\n"
    "s_barrier\n"
    "v_mfma_f32_16x16x32_bf16 v[192:195], v[144:147], v[128:131], v[192:195]\n"
    "v_mfma_f32_16x16x32_bf16 v[192:195], v[176:179], v[160:163], v[192:195]\n"
    "v_add_f32 v224, v224, v68\n"
    "v_add_f32 v225, v225, v69\n"
    "v_mfma_f32_16x16x32_bf16 v[196:199], v[176:179], v[164:167], v[196:199]\n"
    "v_add_f32 v226, v226, v70\n"
    "v_mfma_f32_16x16x32_bf16 v[196:199], v[144:147], v[132:135], v[196:199]\n"
    "v_add_f32 v227, v227, v71\n"
    "v_mfma_f32_16x16x32_bf16 v[200:203], v[144:147], v[136:139], v[200:203]\n"
    "v_add_f32 v228, v228, v68\n"
    "v_mfma_f32_16x16x32_bf16 v[200:203], v[176:179], v[168:171], v[200:203]\n"
    "v_add_f32 v229, v229, v69\n"
    "v_add_f32 v230, v230, v70\n"
    "v_mfma_f32_16x16x32_bf16 v[204:207], v[176:179], v[172:175], v[204:207]\n"
    "v_add_f32 v231, v231, v71\n"
    "v_mfma_f32_16x16x32_bf16 v[204:207], v[144:147], v[140:143], v[204:207]\n"
    "v_add_f32 v232, v232, v68\n"
    "v_mfma_f32_16x16x32_bf16 v[220:223], v[148:151], v[140:143], v[220:223]\n"
    "v_add_f32 v233, v233, v69\n"
    "v_mfma_f32_16x16x32_bf16 v[220:223], v[180:183], v[172:175], v[220:223]\n"
    "v_add_f32 v234, v234, v70\n"
    "v_add_f32 v235, v235, v71\n"
    "v_mfma_f32_16x16x32_bf16 v[216:219], v[180:183], v[168:171], v[216:219]\n"
    "v_add_f32 v236, v236, v68\n"
    "v_mfma_f32_16x16x32_bf16 v[216:219], v[148:151], v[136:139], v[216:219]\n"
    "v_add_f32 v237, v237, v69\n"
    "v_mfma_f32_16x16x32_bf16 v[212:215], v[148:151], v[132:135], v[212:215]\n"
    "v_add_f32 v238, v238, v70\n"
    "v_mfma_f32_16x16x32_bf16 v[212:215], v[180:183], v[164:167], v[212:215]\n"
    "v_add_f32 v239, v239, v71\n"
    "v_mfma_f32_16x16x32_bf16 v[208:211], v[180:183], v[160:163], v[208:211]\n"
    "v_mfma_f32_16x16x32_bf16 v[208:211], v[148:151], v[128:131], v[208:211]\n"
    "v_mfma_f32_16x16x32_bf16 v[224:227], v[152:155], v[128:131], v[224:227]\n"
    "v_mfma_f32_16x16x32_bf16 v[224:227], v[184:187], v[160:163], v[224:227]\n"
    "v_mfma_f32_16x16x32_bf16 v[228:231], v[184:187], v[164:167], v[228:231]\n"
    "v_lshlrev_b32 v116, 7, v116\n"
    "v_mfma_f32_16x16x32_bf16 v[228:231], v[152:155], v[132:135], v[228:231]\n"
    "v_add_u32_dpp v52, v115, v48 quad_perm:[0,0,0,0] row_mask:0xf bank_mask:0xf\n"
    "v_mfma_f32_16x16x32_bf16 v[232:235], v[152:155], v[136:139], v[232:235]\n"
    "v_add_u32_dpp v53, v115, v49 quad_perm:[1,1,1,1] row_mask:0xf bank_mask:0xf\n"
    "v_mfma_f32_16x16x32_bf16 v[232:235], v[184:187], v[168:171], v[232:235]\n"
    "v_add_u32_dpp v54, v115, v50 quad_perm:[2,2,2,2] row_mask:0xf bank_mask:0xf\n"
    "v_mfma_f32_16x16x32_bf16 v[236:239], v[184:187], v[172:175], v[236:239]\n"
    "v_add_u32_dpp v55, v115, v51 quad_perm:[3,3,3,3] row_mask:0xf bank_mask:0xf\n"
    "v_mfma_f32_16x16x32_bf16 v[236:239], v[152:155], v[140:143], v[236:239]\n"
    "s_getpc_b64 s[10:11]\n"
    ".Lxd_en2:\n"
    "v_mfma_f32_16x16x32_bf16 v[252:255], v[156:159], v[140:143], v[252:255]\n"
    "s_mov_b32 s40, .Lxd_ph1-.Lxd_en2\n"
    "v_mfma_f32_16x16x32_bf16 v[252:255], v[188:191], v[172:175], v[252:255]\n"
    "s_cmp_eq_u32 s22, 1\n"
    "s_cselect_b32 s40, .Lxd_ph0-.Lxd_en2, s40\n"
    "v_mfma_f32_16x16x32_bf16 v[248:251], v[188:191], v[168:171], v[248:251]\n"
    "s_cmp_eq_u32 s22, 2\n"
    "s_cselect_b32 s40, .Lxd_pl3-.Lxd_en2, s40\n"
    "v_mfma_f32_16x16x32_bf16 v[248:251], v[156:159], v[136:139], v[248:251]\n"
    "s_cmp_eq_u32 s22, 3\n"
    "s_cselect_b32 s40, .Lxd_ph2-.Lxd_en2, s40\n"
    "v_mfma_f32_16x16x32_bf16 v[244:247], v[156:159], v[132:135], v[244:247]\n"
    "s_ashr_i32 s41, s40, 31\n"
    "v_mfma_f32_16x16x32_bf16 v[244:247], v[188:191], v[164:167], v[244:247]\n"
    "s_add_u32 s10, s10, s40\n"
    "s_addc_u32 s11, s11, s41\n"
    "v_mfma_f32_16x16x32_bf16 v[240:243], v[188:191], v[160:163], v[240:243]\n"
    "v_mfma_f32_16x16x32_bf16 v[240:243], v[156:159], v[128:131], v[240:243]\n"
    "s_waitcnt vmcnt(7)\n"
    "s_barrier\n"
    "s_add_u32 m0, s35, 49152\n"
    "s_nop 0\n"
    "global_load_lds_dwordx4 v46, s[30:31] offset:0\n"
    "ds_read_b128 v[128:131], v58 offset:0\n"
    "ds_read_b128 v[132:135], v58 offset:2048\n"
    "ds_read_b128 v[136:139], v58 offset:4096\n"
    "global_load_lds_dwordx4 v47, s[30:31] offset:1024\n"
    "s_bfe_u32 s66, s29, 0x80000\n"
    "s_mul_i32 s66, s66, 53248\n"
    "s_add_u32 s30, s6, s66\n"
    "s_addc_u32 s31, s7, 0\n"
    "ds_read_b128 v[140:143], v58 offset:6144\n"
    "ds_read_b64_tr_b16 v[144:145], v80 offset:0\n"
    "ds_read_b64_tr_b16 v[146:147], v80 offset:1024\n"
    "s_add_u32 m0, s34, 49152\n"
    "s_nop 0\n"
    "global_load_lds_dwordx4 v52, s[36:37] offset:0\n"
    "ds_read_b64_tr_b16 v[148:149], v81 offset:0\n"
    "ds_read_b64_tr_b16 v[150:151], v81 offset:1024\n"
    "ds_read_b64_tr_b16 v[152:153], v82 offset:0\n"
    "global_load_lds_dwordx4 v53, s[36:37] offset:1024\n"
    "ds_read_b64_tr_b16 v[154:155], v82 offset:1024\n"
    "ds_read_b64_tr_b16 v[156:157], v83 offset:0\n"
    "ds_read_b64_tr_b16 v[158:159], v83 offset:1024\n"
    "global_load_lds_dwordx4 v54, s[36:37] offset:2048\n"
    "ds_read_b128 v[160:163], v59 offset:0\n"
    "ds_read_b128 v[164:167], v59 offset:2048\n"
    "ds_read_b128 v[168:171], v59 offset:4096\n"
    "global_load_lds_dwordx4 v55, s[36:37] offset:3072\n"
    "ds_read_b128 v[172:175], v59 offset:6144\n"
    "ds_read_b64_tr_b16 v[176:177], v80 offset:8192\n"
    "ds_read_b64_tr_b16 v[178:179], v80 offset:9216\n"
    "global_load_dwordx4 v[32:35], v37, s[8:9]\n"
    "ds_read_b64_tr_b16 v[180:181], v81 offset:8192\n"
    "ds_read_b64_tr_b16 v[182:183], v81 offset:9216\n"
    "ds_read_b64_tr_b16 v[184:185], v82 offset:8192\n"
    "ds_read_b64_tr_b16 v[186:187], v82 offset:9216\n"
    "ds_read_b64_tr_b16 v[188:189], v83 offset:8192\n"
    "ds_read_b64_tr_b16 v[190:191], v83 offset:9216\n"
    "s_waitcnt vmcnt(12) lgkmcnt(0)\n"
    "s_barrier\n"
    "v_mfma_f32_16x16x32_bf16 v[192:195], v[144:147], v[128:131], v[192:195]\n"
    "v_mfma_f32_16x16x32_bf16 v[192:195], v[176:179], v[160:163], v[192:195]\n"
    "v_mfma_f32_16x16x32_bf16 v[196:199], v[176:179], v[164:167], v[196:199]\n"
    "v_mfma_f32_16x16x32_bf16 v[196:199], v[144:147], v[132:135], v[196:199]\n"
    "v_mfma_f32_16x16x32_bf16 v[200:203], v[144:147], v[136:139], v[200:203]\n"
    "v_add_f32 v240, v240, v72\n"
    "v_mfma_f32_16x16x32_bf16 v[200:203], v[176:179], v[168:171], v[200:203]\n"
    "v_add_f32 v241, v241, v73\n"
    "v_mfma_f32_16x16x32_bf16 v[204:207], v[176:179], v[172:175], v[204:207]\n"
    "v_add_f32 v242, v242, v74\n"
    "v_mfma_f32_16x16x32_bf16 v[204:207], v[144:147], v[140:143], v[204:207]\n"
    "v_add_f32 v243, v243, v75\n"
    "v_mfma_f32_16x16x32_bf16 v[220:223], v[148:151], v[140:143], v[220:223]\n"
    "v_add_f32 v244, v244, v72\n"
    "v_mfma_f32_16x16x32_bf16 v[220:223], v[180:183], v[172:175], v[220:223]\n"
    "v_add_f32 v245, v245, v73\n"
    "v_mfma_f32_16x16x32_bf16 v[216:219], v[180:183], v[168:171], v[216:219]\n"
    "v_add_f32 v246, v246, v74\n"
    "v_mfma_f32_16x16x32_bf16 v[216:219], v[148:151], v[136:139], v[216:219]\n"
    "v_add_f32 v247, v247, v75\n"
    "v_mfma_f32_16x16x32_bf16 v[212:215], v[148:151], v[132:135], v[212:215]\n"
    "v_add_f32 v248, v248, v72\n"
    "v_mfma_f32_16x16x32_bf16 v[212:215], v[180:183], v[164:167], v[212:215]\n"
    "v_add_f32 v249, v249, v73\n"
    "v_mfma_f32_16x16x32_bf16 v[208:211], v[180:183], v[160:163], v[208:211]\n"
    "v_add_f32 v250, v250, v74\n"
    "v_mfma_f32_16x16x32_bf16 v[208:211], v[148:151], v[128:131], v[208:211]\n"
    "v_add_f32 v251, v251, v75\n"
    "v_mfma_f32_16x16x32_bf16 v[224:227], v[152:155], v[128:131], v[224:227]\n"
    "v_add_f32 v252, v252, v72\n"
    "v_mfma_f32_16x16x32_bf16 v[224:227], v[184:187], v[160:163], v[224:227]\n"
    "v_add_f32 v253, v253, v73\n"
    "v_mfma_f32_16x16x32_bf16 v[228:231], v[184:187], v[164:167], v[228:231]\n"
    "v_lshlrev_b32 v117, 7, v117\n"
    "v_add_f32 v254, v254, v74\n"
    "v_mfma_f32_16x16x32_bf16 v[228:231], v[152:155], v[132:135], v[228:231]\n"
    "v_add_f32 v255, v255, v75\n"
    "v_mfma_f32_16x16x32_bf16 v[232:235], v[152:155], v[136:139], v[232:235]\n"
    "v_add_u32_dpp v52, v116, v48 quad_perm:[0,0,0,0] row_mask:0xf bank_mask:0xf\n"
    "v_mfma_f32_16x16x32_bf16 v[232:235], v[184:187], v[168:171], v[232:235]\n"
    "v_mfma_f32_16x16x32_bf16 v[236:239], v[184:187], v[172:175], v[236:239]\n"
    "v_mfma_f32_16x16x32_bf16 v[236:239], v[152:155], v[140:143], v[236:239]\n"
    "v_add_u32_dpp v53, v116, v49 quad_perm:[1,1,1,1] row_mask:0xf bank_mask:0xf\n"
    "v_mfma_f32_16x16x32_bf16 v[252:255], v[156:159], v[140:143], v[252:255]\n"
    "v_mfma_f32_16x16x32_bf16 v[252:255], v[188:191], v[172:175], v[252:255]\n"
    "v_add_u32_dpp v54, v116, v50 quad_perm:[2,2,2,2] row_mask:0xf bank_mask:0xf\n"
    "v_mfma_f32_16x16x32_bf16 v[248:251], v[188:191], v[168:171], v[248:251]\n"
    "v_mfma_f32_16x16x32_bf16 v[248:251], v[156:159], v[136:139], v[248:251]\n"
    "v_mfma_f32_16x16x32_bf16 v[244:247], v[156:159], v[132:135], v[244:247]\n"
    "v_add_u32_dpp v55, v116, v51 quad_perm:[3,3,3,3] row_mask:0xf bank_mask:0xf\n"
    "v_mfma_f32_16x16x32_bf16 v[244:247], v[188:191], v[164:167], v[244:247]\n"
    "v_mfma_f32_16x16x32_bf16 v[240:243], v[188:191], v[160:163], v[240:243]\n"
    "v_mfma_f32_16x16x32_bf16 v[240:243], v[156:159], v[128:131], v[240:243]\n"
    "s_waitcnt vmcnt(8)\n"
    "s_barrier\n"
    "s_add_u32 m0, s35, 98304\n"
    "s_nop 0\n"
    "global_load_lds_dwordx4 v46, s[30:31] offset:0\n"
    "ds_read_b128 v[128:131], v56 offset:0\n"
    "ds_read_b128 v[132:135], v56 offset:2048\n"
    "ds_read_b128 v[136:139], v56 offset:4096\n"
    "global_load_lds_dwordx4 v47, s[30:31] offset:1024\n"
    "s_add_u32 s30, s30, 212992\n"
    "s_addc_u32 s31, s31, 0\n"
    "ds_read_b128 v[140:143], v56 offset:6144\n"
    "ds_read_b64_tr_b16 v[144:145], v76 offset:0\n"
    "ds_read_b64_tr_b16 v[146:147], v76 offset:1024\n"
    "s_add_u32 m0, s34, 98304\n"
    "s_nop 0\n"
    "global_load_lds_dwordx4 v52, s[36:37] offset:0\n"
    "ds_read_b64_tr_b16 v[148:149], v77 offset:0\n"
    "ds_read_b64_tr_b16 v[150:151], v77 offset:1024\n"
    "ds_read_b64_tr_b16 v[152:153], v78 offset:0\n"
    "global_load_lds_dwordx4 v53, s[36:37] offset:1024\n"
    "ds_read_b64_tr_b16 v[154:155], v78 offset:1024\n"
    "ds_read_b64_tr_b16 v[156:157], v79 offset:0\n"
    "ds_read_b64_tr_b16 v[158:159], v79 offset:1024\n"
    "global_load_lds_dwordx4 v54, s[36:37] offset:2048\n"
    "ds_read_b128 v[160:163], v57 offset:0\n"
    "ds_read_b128 v[164:167], v57 offset:2048\n"
    "ds_read_b128 v[168:171], v57 offset:4096\n"
    "global_load_lds_dwordx4 v55, s[36:37] offset:3072\n"
    "ds_read_b128 v[172:175], v57 offset:6144\n"
    "ds_read_b64_tr_b16 v[176:177], v76 offset:8192\n"
    "ds_read_b64_tr_b16 v[178:179], v76 offset:9216\n"
    "global_load_dword v40, v41, s[8:9]\n"
    "ds_read_b64_tr_b16 v[180:181], v77 offset:8192\n"
    "ds_read_b64_tr_b16 v[182:183], v77 offset:9216\n"
    "ds_read_b64_tr_b16 v[184:185], v78 offset:8192\n"
    "ds_read_b64_tr_b16 v[186:187], v78 offset:9216\n"
    "ds_read_b64_tr_b16 v[188:189], v79 offset:8192\n"
    "ds_read_b64_tr_b16 v[190:191], v79 offset:9216\n"
    "s_waitcnt vmcnt(12) lgkmcnt(0)\n"
    "s_barrier\n"
    "v_mfma_f32_16x16x32_bf16 v[192:195], v[144:147], v[128:131], v[192:195]\n"
    "v_lshlrev_b32 v118, 7, v118\n"
    "v_mfma_f32_16x16x32_bf16 v[192:195], v[176:179], v[160:163], v[192:195]\n"
    "v_add_u32_dpp v52, v117, v48 quad_perm:[0,0,0,0] row_mask:0xf bank_mask:0xf\n"
    "v_mfma_f32_16x16x32_bf16 v[196:199], v[176:179], v[164:167], v[196:199]\n"
    "v_add_u32_dpp v53, v117, v49 quad_perm:[1,1,1,1] row_mask:0xf bank_mask:0xf\n"
    "v_mfma_f32_16x16x32_bf16 v[196:199], v[144:147], v[132:135], v[196:199]\n"
    "v_add_u32_dpp v54, v117, v50 quad_perm:[2,2,2,2] row_mask:0xf bank_mask:0xf\n"
    "v_mfma_f32_16x16x32_bf16 v[200:203], v[144:147], v[136:139], v[200:203]\n"
    "v_add_u32_dpp v55, v117, v51 quad_perm:[3,3,3,3] row_mask:0xf bank_mask:0xf\n"
    "v_mfma_f32_16x16x32_bf16 v[200:203], v[176:179], v[168:171], v[200:203]\n"
    "v_readlane_b32 s25, v26, 9\n"
    "v_mfma_f32_16x16x32_bf16 v[204:207], v[176:179], v[172:175], v[204:207]\n"
    "v_readlane_b32 s40, v26, 8\n"
    "v_mfma_f32_16x16x32_bf16 v[204:207], v[144:147], v[140:143], v[204:207]\n"
    "v_readlane_b32 s41, v27, 8\n"
    "v_mfma_f32_16x16x32_bf16 v[220:223], v[148:151], v[140:143], v[220:223]\n"
    "s_mul_i32 s42, s25, 0x9e3779b1\n"
    "v_mfma_f32_16x16x32_bf16 v[220:223], v[180:183], v[172:175], v[220:223]\n"
    "s_lshr_b32 s42, s42, 24\n"
    "v_mfma_f32_16x16x32_bf16 v[216:219], v[180:183], v[168:171], v[216:219]\n"
    "s_xor_b32 s42, s42, s40\n"
    "v_mfma_f32_16x16x32_bf16 v[216:219], v[148:151], v[136:139], v[216:219]\n"
    "s_and_b32 s42, s42, 0xff\n"
    "v_mfma_f32_16x16x32_bf16 v[212:215], v[148:151], v[132:135], v[212:215]\n"
    "s_xor_b32 s41, s41, s25\n"
    "v_mfma_f32_16x16x32_bf16 v[212:215], v[180:183], v[164:167], v[212:215]\n"
    "s_xor_b32 s41, s41, 0xa5c30f96\n"
    "v_mfma_f32_16x16x32_bf16 v[208:211], v[180:183], v[160:163], v[208:211]\n"
    "s_or_b32 s42, s41, s42\n"
    "v_mfma_f32_16x16x32_bf16 v[208:211], v[148:151], v[128:131], v[208:211]\n"
    "v_mfma_f32_16x16x32_bf16 v[224:227], v[152:155], v[128:131], v[224:227]\n"
    "s_lshr_b32 s41, s40, 8\n"
    "v_mfma_f32_16x16x32_bf16 v[224:227], v[184:187], v[160:163], v[224:227]\n"
    "s_mul_i32 s40, s41, 0x9e3779b1\n"
    "v_mfma_f32_16x16x32_bf16 v[228:231], v[184:187], v[164:167], v[228:231]\n"
    "s_xor_b32 s41, s41, 0xa5c30f96\n"
    "v_mfma_f32_16x16x32_bf16 v[228:231], v[152:155], v[132:135], v[228:231]\n"
    "s_lshr_b32 s40, s40, 24\n"
    "v_mfma_f32_16x16x32_bf16 v[232:235], v[152:155], v[136:139], v[232:235]\n"
    "v_xor_b32 v13, s40, v26\n"
    "v_mfma_f32_16x16x32_bf16 v[232:235], v[184:187], v[168:171], v[232:235]\n"
    "v_xor_b32 v22, s41, v27\n"
    "v_mfma_f32_16x16x32_bf16 v[236:239], v[184:187], v[172:175], v[236:239]\n"
    "v_lshl_or_b32 v13, v13, 24, v22\n"
    "v_mfma_f32_16x16x32_bf16 v[236:239], v[152:155], v[140:143], v[236:239]\n"
    "v_cmp_ne_u32 s[66:67], 0, v13\n"
    "v_mfma_f32_16x16x32_bf16 v[252:255], v[156:159], v[140:143], v[252:255]\n"
    "s_and_b32 s66, s66, 0xff\n"
    "v_mfma_f32_16x16x32_bf16 v[252:255], v[188:191], v[172:175], v[252:255]\n"
    "s_or_b32 s42, s42, s66\n"
    "v_mfma_f32_16x16x32_bf16 v[248:251], v[188:191], v[168:171], v[248:251]\n"
    "s_cmp_eq_u32 s20, 3\n"
    "v_mfma_f32_16x16x32_bf16 v[248:251], v[156:159], v[136:139], v[248:251]\n"
    "s_cselect_b32 s42, s42, 0\n"
    "v_mfma_f32_16x16x32_bf16 v[244:247], v[156:159], v[132:135], v[244:247]\n"
    "s_cmp_lg_u32 s42, 0\n"
    "v_mfma_f32_16x16x32_bf16 v[244:247], v[188:191], v[164:167], v[244:247]\n"
    "s_cbranch_scc1 .Lxd_pv3\n"
    ".Lxd_pv3r:\n"
    "v_mfma_f32_16x16x32_bf16 v[240:243], v[188:191], v[160:163], v[240:243]\n"
    "v_mfma_f32_16x16x32_bf16 v[240:243], v[156:159], v[128:131], v[240:243]\n"
    "s_waitcnt vmcnt(8)\n"
    "s_barrier\n"
    "s_add_u32 m0, s35, 0\n"
    "s_nop 0\n"
    "global_load_lds_dwordx4 v46, s[30:31] offset:0\n"
    "ds_read_b128 v[128:131], v56 offset:49152\n"
    "ds_read_b128 v[132:135], v56 offset:51200\n"
    "ds_read_b128 v[136:139], v56 offset:53248\n"
    "global_load_lds_dwordx4 v47, s[30:31] offset:1024\n"
    "s_add_u32 s30, s30, 212992\n"
    "s_addc_u32 s31, s31, 0\n"
    "ds_read_b128 v[140:143], v56 offset:55296\n"
    "ds_read_b64_tr_b16 v[144:145], v76 offset:49152\n"
    "ds_read_b64_tr_b16 v[146:147], v76 offset:50176\n"
    "s_add_u32 m0, s34, 0\n"
    "s_nop 0\n"
    "global_load_lds_dwordx4 v52, s[36:37] offset:0\n"
    "ds_read_b64_tr_b16 v[148:149], v77 offset:49152\n"
    "ds_read_b64_tr_b16 v[150:151], v77 offset:50176\n"
    "ds_read_b64_tr_b16 v[152:153], v78 offset:49152\n"
    "global_load_lds_dwordx4 v53, s[36:37] offset:1024\n"
    "ds_read_b64_tr_b16 v[154:155], v78 offset:50176\n"
    "ds_read_b64_tr_b16 v[156:157], v79 offset:49152\n"
    "ds_read_b64_tr_b16 v[158:159], v79 offset:50176\n"
    "global_load_lds_dwordx4 v54, s[36:37] offset:2048\n"
    "ds_read_b128 v[160:163], v57 offset:49152\n"
    "ds_read_b128 v[164:167], v57 offset:51200\n"
    "ds_read_b128 v[168:171], v57 offset:53248\n"
    "global_load_lds_dwordx4 v55, s[36:37] offset:3072\n"
    "ds_read_b128 v[172:175], v57 offset:55296\n"
    "ds_read_b64_tr_b16 v[176:177], v76 offset:57344\n"
    "ds_read_b64_tr_b16 v[178:179], v76 offset:58368\n"
    "global_load_dwordx4 v[36:39], v38, s[8:9]\n"
    "ds_read_b64_tr_b16 v[180:181], v77 offset:57344\n"
    "ds_read_b64_tr_b16 v[182:183], v77 offset:58368\n"
    "ds_read_b64_tr_b16 v[184:185], v78 offset:57344\n"
    "ds_read_b64_tr_b16 v[186:187], v78 offset:58368\n"
    "ds_read_b64_tr_b16 v[188:189], v79 offset:57344\n"
    "ds_read_b64_tr_b16 v[190:191], v79 offset:58368\n"
    "s_waitcnt vmcnt(12) lgkmcnt(0)\n"
    "s_barrier\n"
    "v_mfma_f32_16x16x32_bf16 v[192:195], v[144:147], v[128:131], v[192:195]\n"
    "v_lshlrev_b32 v119, 7, v119\n"
    "v_mfma_f32_16x16x32_bf16 v[192:195], v[176:179], v[160:163], v[192:195]\n"
    "v_add_u32_dpp v52, v118, v48 quad_perm:[0,0,0,0] row_mask:0xf bank_mask:0xf\n"
    "v_mfma_f32_16x16x32_bf16 v[196:199], v[176:179], v[164:167], v[196:199]\n"
    "v_add_u32_dpp v53, v118, v49 quad_perm:[1,1,1,1] row_mask:0xf bank_mask:0xf\n"
    "v_mfma_f32_16x16x32_bf16 v[196:199], v[144:147], v[132:135], v[196:199]\n"
    "v_mfma_f32_16x16x32_bf16 v[200:203], v[144:147], v[136:139], v[200:203]\n"
    "v_add_u32_dpp v54, v118, v50 quad_perm:[2,2,2,2] row_mask:0xf bank_mask:0xf\n"
    "v_mfma_f32_16x16x32_bf16 v[200:203], v[176:179], v[168:171], v[200:203]\n"
    "v_add_u32_dpp v55, v118, v51 quad_perm:[3,3,3,3] row_mask:0xf bank_mask:0xf\n"
    "v_mfma_f32_16x16x32_bf16 v[204:207], v[176:179], v[172:175], v[204:207]\n"
    "v_mfma_f32_16x16x32_bf16 v[204:207], v[144:147], v[140:143], v[204:207]\n"
    "v_lshlrev_b32 v60, 16, v92\n"
    "v_mfma_f32_16x16x32_bf16 v[220:223], v[148:151], v[140:143], v[220:223]\n"
    "v_and_b32 v61, 0xffff0000, v92\n"
    "v_mfma_f32_16x16x32_bf16 v[220:223], v[180:183], v[172:175], v[220:223]\n"
    "v_mfma_f32_16x16x32_bf16 v[216:219], v[180:183], v[168:171], v[216:219]\n"
    "v_lshlrev_b32 v62, 16, v93\n"
    "v_mfma_f32_16x16x32_bf16 v[216:219], v[148:151], v[136:139], v[216:219]\n"
    "v_and_b32 v63, 0xffff0000, v93\n"
    "v_mfma_f32_16x16x32_bf16 v[212:215], v[148:151], v[132:135], v[212:215]\n"
    "v_mfma_f32_16x16x32_bf16 v[212:215], v[180:183], v[164:167], v[212:215]\n"
    "v_lshlrev_b32 v64, 16, v94\n"
    "v_mfma_f32_16x16x32_bf16 v[208:211], v[180:183], v[160:163], v[208:211]\n"
    "v_and_b32 v65, 0xffff0000, v94\n"
    "v_mfma_f32_16x16x32_bf16 v[208:211], v[148:151], v[128:131], v[208:211]\n"
    "v_mfma_f32_16x16x32_bf16 v[224:227], v[152:155], v[128:131], v[224:227]\n"
    "v_lshlrev_b32 v66, 16, v95\n"
    "v_mfma_f32_16x16x32_bf16 v[224:227], v[184:187], v[160:163], v[224:227]\n"
    "v_and_b32 v67, 0xffff0000, v95\n"
    "v_mfma_f32_16x16x32_bf16 v[228:231], v[184:187], v[164:167], v[228:231]\n"
    "v_mfma_f32_16x16x32_bf16 v[228:231], v[152:155], v[132:135], v[228:231]\n"
    "v_lshlrev_b32 v68, 16, v122\n"
    "v_mfma_f32_16x16x32_bf16 v[232:235], v[152:155], v[136:139], v[232:235]\n"
    "v_and_b32 v69, 0xffff0000, v122\n"
    "v_mfma_f32_16x16x32_bf16 v[232:235], v[184:187], v[168:171], v[232:235]\n"
    "v_mfma_f32_16x16x32_bf16 v[236:239], v[184:187], v[172:175], v[236:239]\n"
    "v_lshlrev_b32 v70, 16, v123\n"
    "v_mfma_f32_16x16x32_bf16 v[236:239], v[152:155], v[140:143], v[236:239]\n"
    "v_and_b32 v71, 0xffff0000, v123\n"
    "v_mfma_f32_16x16x32_bf16 v[252:255], v[156:159], v[140:143], v[252:255]\n"
    "v_mfma_f32_16x16x32_bf16 v[252:255], v[188:191], v[172:175], v[252:255]\n"
    "v_lshlrev_b32 v72, 16, v124\n"
    "v_mfma_f32_16x16x32_bf16 v[248:251], v[188:191], v[168:171], v[248:251]\n"
    "v_and_b32 v73, 0xffff0000, v124\n"
    "v_mfma_f32_16x16x32_bf16 v[248:251], v[156:159], v[136:139], v[248:251]\n"
    "v_mfma_f32_16x16x32_bf16 v[244:247], v[156:159], v[132:135], v[244:247]\n"
    "v_lshlrev_b32 v74, 16, v125\n"
    "v_mfma_f32_16x16x32_bf16 v[244:247], v[188:191], v[164:167], v[244:247]\n"
    "v_and_b32 v75, 0xffff0000, v125\n"
    "v_mfma_f32_16x16x32_bf16 v[240:243], v[188:191], v[160:163], v[240:243]\n"
    "v_mfma_f32_16x16x32_bf16 v[240:243], v[156:159], v[128:131], v[240:243]\n"
    "s_waitcnt vmcnt(8)\n"
    "s_barrier\n"
    "s_add_u32 m0, s35, 49152\n"
    "s_nop 0\n"
    "global_load_lds_dwordx4 v46, s[30:31] offset:0\n"
    "ds_read_b128 v[128:131], v58 offset:0\n"
    "ds_read_b128 v[132:135], v58 offset:2048\n"
    "ds_read_b128 v[136:139], v58 offset:4096\n"
    "ds_read_b128 v[140:143], v58 offset:6144\n"
    "global_load_lds_dwordx4 v47, s[30:31] offset:1024\n"
    "s_add_u32 s30, s30, 212992\n"
    "s_addc_u32 s31, s31, 0\n"
    "ds_read_b64_tr_b16 v[144:145], v80 offset:0\n"
    "ds_read_b64_tr_b16 v[146:147], v80 offset:1024\n"
    "ds_read_b64_tr_b16 v[148:149], v81 offset:0\n"
    "ds_read_b64_tr_b16 v[150:151], v81 offset:1024\n"
    "s_add_u32 m0, s34, 49152\n"
    "s_nop 0\n"
    "global_load_lds_dwordx4 v52, s[36:37] offset:0\n"
    "ds_read_b64_tr_b16 v[152:153], v82 offset:0\n"
    "ds_read_b64_tr_b16 v[154:155], v82 offset:1024\n"
    "ds_read_b64_tr_b16 v[156:157], v83 offset:0\n"
    "ds_read_b64_tr_b16 v[158:159], v83 offset:1024\n"
    "global_load_lds_dwordx4 v53, s[36:37] offset:1024\n"
    "ds_read_b128 v[160:163], v59 offset:0\n"
    "ds_read_b128 v[164:167], v59 offset:2048\n"
    "ds_read_b128 v[168:171], v59 offset:4096\n"
    "ds_read_b128 v[172:175], v59 offset:6144\n"
    "global_load_lds_dwordx4 v54, s[36:37] offset:2048\n"
    "ds_read_b64_tr_b16 v[176:177], v80 offset:8192\n"
    "ds_read_b64_tr_b16 v[178:179], v80 offset:9216\n"
    "ds_read_b64_tr_b16 v[180:181], v81 offset:8192\n"
    "ds_read_b64_tr_b16 v[182:183], v81 offset:9216\n"
    "global_load_lds_dwordx4 v55, s[36:37] offset:3072\n"
    "ds_read_b64_tr_b16 v[184:185], v82 offset:8192\n"
    "ds_read_b64_tr_b16 v[186:187], v82 offset:9216\n"
    "ds_read_b64_tr_b16 v[188:189], v83 offset:8192\n"
    "ds_read_b64_tr_b16 v[190:191], v83 offset:9216\n"
    "s_waitcnt vmcnt(11) lgkmcnt(0)\n"
    "s_barrier\n"
    "v_mfma_f32_16x16x32_bf16 v[192:195], v[144:147], v[128:131], v[192:195]\n"
    "v_lshlrev_b32 v120, 7, v120\n"
    "v_mfma_f32_16x16x32_bf16 v[192:195], v[176:179], v[160:163], v[192:195]\n"
    "v_add_u32_dpp v52, v119, v48 quad_perm:[0,0,0,0] row_mask:0xf bank_mask:0xf\n"
    "v_mfma_f32_16x16x32_bf16 v[196:199], v[176:179], v[164:167], v[196:199]\n"
    "v_add_u32_dpp v53, v119, v49 quad_perm:[1,1,1,1] row_mask:0xf bank_mask:0xf\n"
    "v_mfma_f32_16x16x32_bf16 v[196:199], v[144:147], v[132:135], v[196:199]\n"
    "v_add_u32_dpp v54, v119, v50 quad_perm:[2,2,2,2] row_mask:0xf bank_mask:0xf\n"
    "v_mfma_f32_16x16x32_bf16 v[200:203], v[144:147], v[136:139], v[200:203]\n"
    "v_add_u32_dpp v55, v119, v51 quad_perm:[3,3,3,3] row_mask:0xf bank_mask:0xf\n"
    "v_mfma_f32_16x16x32_bf16 v[200:203], v[176:179], v[168:171], v[200:203]\n"
    "v_lshrrev_b32 v22, 8, v26\n"
    "v_mfma_f32_16x16x32_bf16 v[204:207], v[176:179], v[172:175], v[204:207]\n"
    "v_mfma_f32_16x16x32_bf16 v[204:207], v[144:147], v[140:143], v[204:207]\n"
    "v_mov_b32 v26, s40\n"
    "v_mfma_f32_16x16x32_bf16 v[220:223], v[148:151], v[140:143], v[220:223]\n"
    "v_mov_b32 v27, s41\n"
    "v_mfma_f32_16x16x32_bf16 v[220:223], v[180:183], v[172:175], v[220:223]\n"
    "v_add_u32_dpp v22, v22, v22 row_shl:4 row_mask:0xf bank_mask:0xf\n"
    "v_mfma_f32_16x16x32_bf16 v[216:219], v[180:183], v[168:171], v[216:219]\n"
    "v_add_u32_e64 v43, s25, 1\n"
    "v_mfma_f32_16x16x32_bf16 v[216:219], v[148:151], v[136:139], v[216:219]\n"
    "v_lshlrev_b32 v8, 2, v1\n"
    "v_mfma_f32_16x16x32_bf16 v[212:215], v[148:151], v[132:135], v[212:215]\n"
    "v_mfma_f32_16x16x32_bf16 v[212:215], v[180:183], v[164:167], v[212:215]\n"
    "v_max_u32_dpp v13, v22, v22 quad_perm:[1,0,3,2] row_mask:0xf bank_mask:0xf\n"
    "v_mfma_f32_16x16x32_bf16 v[208:211], v[180:183], v[160:163], v[208:211]\n"
    "v_add_u32 v8, 154112, v8\n"
    "v_mfma_f32_16x16x32_bf16 v[208:211], v[148:151], v[128:131], v[208:211]\n"
    "s_nop 0\n"
    "v_mfma_f32_16x16x32_bf16 v[224:227], v[152:155], v[128:131], v[224:227]\n"
    "v_max_u32_dpp v13, v13, v13 quad_perm:[2,3,0,1] row_mask:0xf bank_mask:0xf\n"
    "v_mfma_f32_16x16x32_bf16 v[224:227], v[184:187], v[160:163], v[224:227]\n"
    "v_sub_u32 v22, v22, v13\n"
    "v_mfma_f32_16x16x32_bf16 v[228:231], v[184:187], v[164:167], v[228:231]\n"
    "v_mfma_f32_16x16x32_bf16 v[228:231], v[152:155], v[132:135], v[228:231]\n"
    "v_subrev_u32 v13, 818, v13\n"
    "v_mfma_f32_16x16x32_bf16 v[232:235], v[152:155], v[136:139], v[232:235]\n"
    "v_ldexp_f32 v22, 1.0, v22\n"
    "v_mfma_f32_16x16x32_bf16 v[232:235], v[184:187], v[168:171], v[232:235]\n"
    "v_rcp_f32 v60, v60\n"
    "v_mfma_f32_16x16x32_bf16 v[236:239], v[184:187], v[172:175], v[236:239]\n"
    "v_rcp_f32 v61, v61\n"
    "v_mfma_f32_16x16x32_bf16 v[236:239], v[152:155], v[140:143], v[236:239]\n"
    "v_rcp_f32 v62, v62\n"
    "v_mfma_f32_16x16x32_bf16 v[252:255], v[156:159], v[140:143], v[252:255]\n"
    "v_mfma_f32_16x16x32_bf16 v[252:255], v[188:191], v[172:175], v[252:255]\n"
    "v_rcp_f32 v63, v63\n"
    "v_mfma_f32_16x16x32_bf16 v[248:251], v[188:191], v[168:171], v[248:251]\n"
    "v_rcp_f32 v64, v64\n"
    "v_mfma_f32_16x16x32_bf16 v[248:251], v[156:159], v[136:139], v[248:251]\n"
    "v_rcp_f32 v65, v65\n"
    "v_mfma_f32_16x16x32_bf16 v[244:247], v[156:159], v[132:135], v[244:247]\n"
    "v_rcp_f32 v66, v66\n"
    "v_mfma_f32_16x16x32_bf16 v[244:247], v[188:191], v[164:167], v[244:247]\n"
    "v_rcp_f32 v67, v67\n"
    "v_mfma_f32_16x16x32_bf16 v[240:243], v[188:191], v[160:163], v[240:243]\n"
    "v_mfma_f32_16x16x32_bf16 v[240:243], v[156:159], v[128:131], v[240:243]\n"
    "s_waitcnt vmcnt(7)\n"
    "s_barrier\n"
    "s_add_u32 m0, s35, 98304\n"
    "s_nop 0\n"
    "global_load_lds_dwordx4 v46, s[30:31] offset:0\n"
    "ds_read_b128 v[128:131], v56 offset:0\n"
    "ds_read_b128 v[132:135], v56 offset:2048\n"
    "ds_read_b128 v[136:139], v56 offset:4096\n"
    "ds_read_b128 v[140:143], v56 offset:6144\n"
    "global_load_lds_dwordx4 v47, s[30:31] offset:1024\n"
    "s_bfe_u32 s66, s29, 0x80008\n"
    "s_mul_i32 s66, s66, 53248\n"
    "s_add_u32 s30, s6, s66\n"
    "s_addc_u32 s31, s7, 0\n"
    "ds_read_b64_tr_b16 v[144:145], v76 offset:0\n"
    "ds_read_b64_tr_b16 v[146:147], v76 offset:1024\n"
    "ds_read_b64_tr_b16 v[148:149], v77 offset:0\n"
    "ds_read_b64_tr_b16 v[150:151], v77 offset:1024\n"
    "s_add_u32 m0, s34, 98304\n"
    "s_nop 0\n"
    "global_load_lds_dwordx4 v52, s[36:37] offset:0\n"
    "ds_read_b64_tr_b16 v[152:153], v78 offset:0\n"
    "ds_read_b64_tr_b16 v[154:155], v78 offset:1024\n"
    "ds_read_b64_tr_b16 v[156:157], v79 offset:0\n"
    "ds_read_b64_tr_b16 v[158:159], v79 offset:1024\n"
    "global_load_lds_dwordx4 v53, s[36:37] offset:1024\n"
    "ds_read_b128 v[160:163], v57 offset:0\n"
    "ds_read_b128 v[164:167], v57 offset:2048\n"
    "ds_read_b128 v[168:171], v57 offset:4096\n"
    "ds_read_b128 v[172:175], v57 offset:6144\n"
    "global_load_lds_dwordx4 v54, s[36:37] offset:2048\n"
    "ds_read_b64_tr_b16 v[176:177], v76 offset:8192\n"
    "ds_read_b64_tr_b16 v[178:179], v76 offset:9216\n"
    "ds_read_b64_tr_b16 v[180:181], v77 offset:8192\n"
    "ds_read_b64_tr_b16 v[182:183], v77 offset:9216\n"
    "global_load_lds_dwordx4 v55, s[36:37] offset:3072\n"
    "ds_read_b64_tr_b16 v[184:185], v78 offset:8192\n"
    "ds_read_b64_tr_b16 v[186:187], v78 offset:9216\n"
    "ds_read_b64_tr_b16 v[188:189], v79 offset:8192\n"
    "ds_read_b64_tr_b16 v[190:191], v79 offset:9216\n"
    "s_waitcnt vmcnt(10) lgkmcnt(0)\n"
    "s_barrier\n"
    "v_mfma_f32_16x16x32_bf16 v[192:195], v[144:147], v[128:131], v[192:195]\n"
    "v_lshlrev_b32 v121, 7, v121\n"
    "v_mfma_f32_16x16x32_bf16 v[192:195], v[176:179], v[160:163], v[192:195]\n"
    "v_add_u32_dpp v52, v120, v48 quad_perm:[0,0,0,0] row_mask:0xf bank_mask:0xf\n"
    "v_mfma_f32_16x16x32_bf16 v[196:199], v[176:179], v[164:167], v[196:199]\n"
    "v_add_u32_dpp v53, v120, v49 quad_perm:[1,1,1,1] row_mask:0xf bank_mask:0xf\n"
    "v_mfma_f32_16x16x32_bf16 v[196:199], v[144:147], v[132:135], v[196:199]\n"
    "v_add_u32_dpp v54, v120, v50 quad_perm:[2,2,2,2] row_mask:0xf bank_mask:0xf\n"
    "v_mfma_f32_16x16x32_bf16 v[200:203], v[144:147], v[136:139], v[200:203]\n"
    "v_add_u32_dpp v55, v120, v51 quad_perm:[3,3,3,3] row_mask:0xf bank_mask:0xf\n"
    "v_mfma_f32_16x16x32_bf16 v[200:203], v[176:179], v[168:171], v[200:203]\n"
    "s_movk_i32 s43, 15744\n"
    "v_pk_maximum3_f16 v21, v28, v28, s43 op_sel_hi:[1,1,0] neg_lo:[0,1,0] neg_hi:[0,1,0]\n"
    "v_mfma_f32_16x16x32_bf16 v[204:207], v[176:179], v[172:175], v[204:207]\n"
    "v_pk_maximum3_f16 v21, v29, v29, v21 neg_lo:[0,1,0] neg_hi:[0,1,0]\n"
    "v_mfma_f32_16x16x32_bf16 v[204:207], v[144:147], v[140:143], v[204:207]\n"
    "v_mfma_f32_16x16x32_bf16 v[220:223], v[148:151], v[140:143], v[220:223]\n"
    "v_pk_maximum3_f16 v21, v30, v30, v21 neg_lo:[0,1,0] neg_hi:[0,1,0]\n"
    "v_mfma_f32_16x16x32_bf16 v[220:223], v[180:183], v[172:175], v[220:223]\n"
    "v_pk_maximum3_f16 v21, v31, v31, v21 neg_lo:[0,1,0] neg_hi:[0,1,0]\n"
    "v_mfma_f32_16x16x32_bf16 v[216:219], v[180:183], v[168:171], v[216:219]\n"
    "v_pk_maximum3_f16 v21, v32, v32, v21 neg_lo:[0,1,0] neg_hi:[0,1,0]\n"
    "v_mfma_f32_16x16x32_bf16 v[216:219], v[148:151], v[136:139], v[216:219]\n"
    "v_pk_maximum3_f16 v21, v33, v33, v21 neg_lo:[0,1,0] neg_hi:[0,1,0]\n"
    "v_mfma_f32_16x16x32_bf16 v[212:215], v[148:151], v[132:135], v[212:215]\n"
    "v_pk_maximum3_f16 v21, v34, v34, v21 neg_lo:[0,1,0] neg_hi:[0,1,0]\n"
    "v_mfma_f32_16x16x32_bf16 v[212:215], v[180:183], v[164:167], v[212:215]\n"
    "v_pk_maximum3_f16 v21, v35, v35, v21 neg_lo:[0,1,0] neg_hi:[0,1,0]\n"
    "v_mfma_f32_16x16x32_bf16 v[208:211], v[180:183], v[160:163], v[208:211]\n"
    "v_pk_maximum3_f16 v21, v36, v36, v21 neg_lo:[0,1,0] neg_hi:[0,1,0]\n"
    "v_mfma_f32_16x16x32_bf16 v[208:211], v[148:151], v[128:131], v[208:211]\n"
    "v_mfma_f32_16x16x32_bf16 v[224:227], v[152:155], v[128:131], v[224:227]\n"
    "v_pk_maximum3_f16 v21, v37, v37, v21 neg_lo:[0,1,0] neg_hi:[0,1,0]\n"
    "v_mfma_f32_16x16x32_bf16 v[224:227], v[184:187], v[160:163], v[224:227]\n"
    "v_pk_maximum3_f16 v21, v38, v38, v21 neg_lo:[0,1,0] neg_hi:[0,1,0]\n"
    "v_mfma_f32_16x16x32_bf16 v[228:231], v[184:187], v[164:167], v[228:231]\n"
    "v_pk_maximum3_f16 v21, v39, v39, v21 neg_lo:[0,1,0] neg_hi:[0,1,0]\n"
    "v_mfma_f32_16x16x32_bf16 v[228:231], v[152:155], v[132:135], v[228:231]\n"
    "v_pk_maximum3_f16 v21, v40, v40, v21 neg_lo:[0,1,0] neg_hi:[0,1,0]\n"
    "v_mfma_f32_16x16x32_bf16 v[232:235], v[152:155], v[136:139], v[232:235]\n"
    "v_pk_max_u16 v21, v21, v21 op_sel:[0,1] op_sel_hi:[1,0]\n"
    "v_mfma_f32_16x16x32_bf16 v[232:235], v[184:187], v[168:171], v[232:235]\n"
    "v_rcp_f32 v68, v68\n"
    "v_mfma_f32_16x16x32_bf16 v[236:239], v[184:187], v[172:175], v[236:239]\n"
    "v_rcp_f32 v69, v69\n"
    "v_mfma_f32_16x16x32_bf16 v[236:239], v[152:155], v[140:143], v[236:239]\n"
    "v_mfma_f32_16x16x32_bf16 v[252:255], v[156:159], v[140:143], v[252:255]\n"
    "v_rcp_f32 v70, v70\n"
    "v_mfma_f32_16x16x32_bf16 v[252:255], v[188:191], v[172:175], v[252:255]\n"
    "v_rcp_f32 v71, v71\n"
    "v_mfma_f32_16x16x32_bf16 v[248:251], v[188:191], v[168:171], v[248:251]\n"
    "v_rcp_f32 v72, v72\n"
    "v_mfma_f32_16x16x32_bf16 v[248:251], v[156:159], v[136:139], v[248:251]\n"
    "v_rcp_f32 v73, v73\n"
    "v_mfma_f32_16x16x32_bf16 v[244:247], v[156:159], v[132:135], v[244:247]\n"
    "v_rcp_f32 v74, v74\n"
    "v_mfma_f32_16x16x32_bf16 v[244:247], v[188:191], v[164:167], v[244:247]\n"
    "v_rcp_f32 v75, v75\n"
    "v_mfma_f32_16x16x32_bf16 v[240:243], v[188:191], v[160:163], v[240:243]\n"
    "v_mfma_f32_16x16x32_bf16 v[240:243], v[156:159], v[128:131], v[240:243]\n"
    "s_waitcnt vmcnt(6)\n"
    "s_barrier\n"
    "s_add_u32 m0, s35, 0\n"
    "s_nop 0\n"
    "global_load_lds_dwordx4 v46, s[30:31] offset:0\n"
    "ds_read_b128 v[128:131], v56 offset:49152\n"
    "ds_read_b128 v[132:135], v56 offset:51200\n"
    "ds_read_b128 v[136:139], v56 offset:53248\n"
    "ds_read_b128 v[140:143], v56 offset:55296\n"
    "global_load_lds_dwordx4 v47, s[30:31] offset:1024\n"
    "s_add_u32 s30, s30, 212992\n"
    "s_addc_u32 s31, s31, 0\n"
    "ds_read_b64_tr_b16 v[144:145], v76 offset:49152\n"
    "ds_read_b64_tr_b16 v[146:147], v76 offset:50176\n"
    "ds_read_b64_tr_b16 v[148:149], v77 offset:49152\n"
    "ds_read_b64_tr_b16 v[150:151], v77 offset:50176\n"
    "s_add_u32 m0, s34, 0\n"
    "s_nop 0\n"
    "global_load_lds_dwordx4 v52, s[36:37] offset:0\n"
    "ds_read_b64_tr_b16 v[152:153], v78 offset:49152\n"
    "ds_read_b64_tr_b16 v[154:155], v78 offset:50176\n"
    "ds_read_b64_tr_b16 v[156:157], v79 offset:49152\n"
    "ds_read_b64_tr_b16 v[158:159], v79 offset:50176\n"
    "global_load_lds_dwordx4 v53, s[36:37] offset:1024\n"
    "ds_read_b128 v[160:163], v57 offset:49152\n"
    "ds_read_b128 v[164:167], v57 offset:51200\n"
    "ds_read_b128 v[168:171], v57 offset:53248\n"
    "ds_read_b128 v[172:175], v57 offset:55296\n"
    "global_load_lds_dwordx4 v54, s[36:37] offset:2048\n"
    "ds_read_b64_tr_b16 v[176:177], v76 offset:57344\n"
    "ds_read_b64_tr_b16 v[178:179], v76 offset:58368\n"
    "ds_read_b64_tr_b16 v[180:181], v77 offset:57344\n"
    "ds_read_b64_tr_b16 v[182:183], v77 offset:58368\n"
    "global_load_lds_dwordx4 v55, s[36:37] offset:3072\n"
    "ds_read_b64_tr_b16 v[184:185], v78 offset:57344\n"
    "ds_read_b64_tr_b16 v[186:187], v78 offset:58368\n"
    "ds_read_b64_tr_b16 v[188:189], v79 offset:57344\n"
    "ds_read_b64_tr_b16 v[190:191], v79 offset:58368\n"
    "s_waitcnt vmcnt(10) lgkmcnt(0)\n"
    "s_barrier\n"
    "v_mfma_f32_16x16x32_bf16 v[192:195], v[144:147], v[128:131], v[192:195]\n"
    "v_add_u32_dpp v52, v121, v48 quad_perm:[0,0,0,0] row_mask:0xf bank_mask:0xf\n"
    "s_mov_b32 s42, 0x71800000\n"
    "v_mfma_f32_16x16x32_bf16 v[192:195], v[176:179], v[160:163], v[192:195]\n"
    "v_add_u32_dpp v53, v121, v49 quad_perm:[1,1,1,1] row_mask:0xf bank_mask:0xf\n"
    "v_mfma_f32_16x16x32_bf16 v[196:199], v[176:179], v[164:167], v[196:199]\n"
    "v_add_u32_dpp v54, v121, v50 quad_perm:[2,2,2,2] row_mask:0xf bank_mask:0xf\n"
    "v_mfma_f32_16x16x32_bf16 v[196:199], v[144:147], v[132:135], v[196:199]\n"
    "v_add_u32_dpp v55, v121, v51 quad_perm:[3,3,3,3] row_mask:0xf bank_mask:0xf\n"
    "v_mfma_f32_16x16x32_bf16 v[200:203], v[144:147], v[136:139], v[200:203]\n"
    "v_bfe_u32 v41, v1, 4, 1\n"
    "v_mfma_f32_16x16x32_bf16 v[200:203], v[176:179], v[168:171], v[200:203]\n"
    "v_lshlrev_b32 v41, 2, v41\n"
    "v_mfma_f32_16x16x32_bf16 v[204:207], v[176:179], v[172:175], v[204:207]\n"
    "v_add_u32 v41, 154112, v41\n"
    "v_mfma_f32_16x16x32_bf16 v[204:207], v[144:147], v[140:143], v[204:207]\n"
    "v_med3_f32 v60, v60, s42, -s42\n"
    "v_mfma_f32_16x16x32_bf16 v[220:223], v[148:151], v[140:143], v[220:223]\n"
    "v_max_u32_dpp v21, v21, v21 quad_perm:[1,0,3,2] row_mask:0xf bank_mask:0xf\n"
    "v_mfma_f32_16x16x32_bf16 v[220:223], v[180:183], v[172:175], v[220:223]\n"
    "v_med3_f32 v61, v61, s42, -s42\n"
    "v_mfma_f32_16x16x32_bf16 v[216:219], v[180:183], v[168:171], v[216:219]\n"
    "v_max_u32_dpp v21, v21, v21 quad_perm:[2,3,0,1] row_mask:0xf bank_mask:0xf\n"
    "v_mfma_f32_16x16x32_bf16 v[216:219], v[148:151], v[136:139], v[216:219]\n"
    "v_med3_f32 v62, v62, s42, -s42\n"
    "v_mfma_f32_16x16x32_bf16 v[212:215], v[148:151], v[132:135], v[212:215]\n"
    "v_max_u32_dpp v21, v21, v21 row_half_mirror row_mask:0xf bank_mask:0xf\n"
    "v_mfma_f32_16x16x32_bf16 v[212:215], v[180:183], v[164:167], v[212:215]\n"
    "v_med3_f32 v63, v63, s42, -s42\n"
    "v_mfma_f32_16x16x32_bf16 v[208:211], v[180:183], v[160:163], v[208:211]\n"
    "v_max_u32_dpp v21, v21, v21 row_mirror row_mask:0xf bank_mask:0xf\n"
    "v_mfma_f32_16x16x32_bf16 v[208:211], v[148:151], v[128:131], v[208:211]\n"
    "v_med3_f32 v64, v64, s42, -s42\n"
    "v_mfma_f32_16x16x32_bf16 v[224:227], v[152:155], v[128:131], v[224:227]\n"
    "v_max_u32_dpp v21, v21, v21 row_bcast:15 row_mask:0xa bank_mask:0xf\n"
    "v_mfma_f32_16x16x32_bf16 v[224:227], v[184:187], v[160:163], v[224:227]\n"
    "v_med3_f32 v65, v65, s42, -s42\n"
    "v_mfma_f32_16x16x32_bf16 v[228:231], v[184:187], v[164:167], v[228:231]\n"
    "v_max_u32_dpp v21, v21, v21 row_bcast:31 row_mask:0xc bank_mask:0xf\n"
    "v_mfma_f32_16x16x32_bf16 v[228:231], v[152:155], v[132:135], v[228:231]\n"
    "v_med3_f32 v66, v66, s42, -s42\n"
    "v_mfma_f32_16x16x32_bf16 v[232:235], v[152:155], v[136:139], v[232:235]\n"
    "v_readlane_b32 s47, v21, 63\n"
    "v_mfma_f32_16x16x32_bf16 v[232:235], v[184:187], v[168:171], v[232:235]\n"
    "v_med3_f32 v67, v67, s42, -s42\n"
    "v_mfma_f32_16x16x32_bf16 v[236:239], v[184:187], v[172:175], v[236:239]\n"
    "s_and_b32 s47, s47, 0x7f80\n"
    "v_mfma_f32_16x16x32_bf16 v[236:239], v[152:155], v[140:143], v[236:239]\n"
    "s_sub_u32 s50, s47, 2816\n"
    "v_mfma_f32_16x16x32_bf16 v[252:255], v[156:159], v[140:143], v[252:255]\n"
    "s_mul_i32 s51, s50, -8\n"
    "v_med3_f32 v68, v68, s42, -s42\n"
    "v_mfma_f32_16x16x32_bf16 v[252:255], v[188:191], v[172:175], v[252:255]\n"
    "s_lshr_b32 s47, s47, 3\n"
    "v_med3_f32 v69, v69, s42, -s42\n"
    "v_mfma_f32_16x16x32_bf16 v[248:251], v[188:191], v[168:171], v[248:251]\n"
    "v_med3_f32 v70, v70, s42, -s42\n"
    "v_mfma_f32_16x16x32_bf16 v[248:251], v[156:159], v[136:139], v[248:251]\n"
    "v_med3_f32 v71, v71, s42, -s42\n"
    "v_mfma_f32_16x16x32_bf16 v[244:247], v[156:159], v[132:135], v[244:247]\n"
    "v_med3_f32 v72, v72, s42, -s42\n"
    "v_mfma_f32_16x16x32_bf16 v[244:247], v[188:191], v[164:167], v[244:247]\n"
    "v_med3_f32 v73, v73, s42, -s42\n"
    "v_mfma_f32_16x16x32_bf16 v[240:243], v[188:191], v[160:163], v[240:243]\n"
    "v_med3_f32 v74, v74, s42, -s42\n"
    "v_mfma_f32_16x16x32_bf16 v[240:243], v[156:159], v[128:131], v[240:243]\n"
    "v_med3_f32 v75, v75, s42, -s42\n"
    "s_waitcnt vmcnt(6)\n"
    "s_barrier\n"
    "s_add_u32 m0, s35, 49152\n"
    "s_nop 0\n"
    "global_load_lds_dwordx4 v46, s[30:31] offset:0\n"
    "ds_read_b128 v[128:131], v58 offset:0\n"
    "ds_read_b128 v[132:135], v58 offset:2048\n"
    "ds_read_b128 v[136:139], v58 offset:4096\n"
    "ds_read_b128 v[140:143], v58 offset:6144\n"
    "global_load_lds_dwordx4 v47, s[30:31] offset:1024\n"
    "ds_read_b64_tr_b16 v[144:145], v80 offset:0\n"
    "ds_read_b64_tr_b16 v[146:147], v80 offset:1024\n"
    "ds_read_b64_tr_b16 v[148:149], v81 offset:0\n"
    "ds_read_b64_tr_b16 v[150:151], v81 offset:1024\n"
    "s_add_u32 m0, s34, 49152\n"
    "s_nop 0\n"
    "global_load_lds_dwordx4 v52, s[36:37] offset:0\n"
    "ds_read_b64_tr_b16 v[152:153], v82 offset:0\n"
    "ds_read_b64_tr_b16 v[154:155], v82 offset:1024\n"
    "ds_read_b64_tr_b16 v[156:157], v83 offset:0\n"
    "ds_read_b64_tr_b16 v[158:159], v83 offset:1024\n"
    "global_load_lds_dwordx4 v53, s[36:37] offset:1024\n"
    "ds_read_b128 v[160:163], v59 offset:0\n"
    "ds_read_b128 v[164:167], v59 offset:2048\n"
    "ds_read_b128 v[168:171], v59 offset:4096\n"
    "ds_read_b128 v[172:175], v59 offset:6144\n"
    "global_load_lds_dwordx4 v54, s[36:37] offset:2048\n"
    "ds_read_b64_tr_b16 v[176:177], v80 offset:8192\n"
    "ds_read_b64_tr_b16 v[178:179], v80 offset:9216\n"
    "ds_read_b64_tr_b16 v[180:181], v81 offset:8192\n"
    "ds_read_b64_tr_b16 v[182:183], v81 offset:9216\n"
    "global_load_lds_dwordx4 v55, s[36:37] offset:3072\n"
    "ds_read_b64_tr_b16 v[184:185], v82 offset:8192\n"
    "ds_read_b64_tr_b16 v[186:187], v82 offset:9216\n"
    "ds_read_b64_tr_b16 v[188:189], v83 offset:8192\n"
    "ds_read_b64_tr_b16 v[190:191], v83 offset:9216\n"
    "s_waitcnt vmcnt(10) lgkmcnt(0)\n"
    "s_barrier\n"
    "v_mfma_f32_16x16x32_bf16 v[192:195], v[144:147], v[128:131], v[192:195]\n"
    "v_pk_maximum3_f16 v21, v28, v28, s50 op_sel_hi:[1,1,0] neg_lo:[0,1,0] neg_hi:[0,1,0]\n"
    "v_mfma_f32_16x16x32_bf16 v[192:195], v[176:179], v[160:163], v[192:195]\n"
    "v_pk_mad_u16 v21, v21, 8, s51 op_sel_hi:[1,0,0]\n"
    "v_mfma_f32_16x16x32_bf16 v[196:199], v[176:179], v[164:167], v[196:199]\n"
    "v_bfi_b32 v28, s57, v21, v28\n"
    "v_mfma_f32_16x16x32_bf16 v[196:199], v[144:147], v[132:135], v[196:199]\n"
    "v_mfma_f32_16x16x32_bf16 v[200:203], v[144:147], v[136:139], v[200:203]\n"
    "v_pk_maximum3_f16 v12, v29, v29, s50 op_sel_hi:[1,1,0] neg_lo:[0,1,0] neg_hi:[0,1,0]\n"
    "v_mfma_f32_16x16x32_bf16 v[200:203], v[176:179], v[168:171], v[200:203]\n"
    "v_pk_mad_u16 v12, v12, 8, s51 op_sel_hi:[1,0,0]\n"
    "v_mfma_f32_16x16x32_bf16 v[204:207], v[176:179], v[172:175], v[204:207]\n"
    "v_bfi_b32 v29, s57, v12, v29\n"
    "v_mfma_f32_16x16x32_bf16 v[204:207], v[144:147], v[140:143], v[204:207]\n"
    "v_mfma_f32_16x16x32_bf16 v[220:223], v[148:151], v[140:143], v[220:223]\n"
    "v_pk_maximum3_f16 v21, v30, v30, s50 op_sel_hi:[1,1,0] neg_lo:[0,1,0] neg_hi:[0,1,0]\n"
    "v_mfma_f32_16x16x32_bf16 v[220:223], v[180:183], v[172:175], v[220:223]\n"
    "v_pk_mad_u16 v21, v21, 8, s51 op_sel_hi:[1,0,0]\n"
    "v_mfma_f32_16x16x32_bf16 v[216:219], v[180:183], v[168:171], v[216:219]\n"
    "v_bfi_b32 v30, s57, v21, v30\n"
    "v_mfma_f32_16x16x32_bf16 v[216:219], v[148:151], v[136:139], v[216:219]\n"
    "v_mfma_f32_16x16x32_bf16 v[212:215], v[148:151], v[132:135], v[212:215]\n"
    "v_pk_maximum3_f16 v12, v31, v31, s50 op_sel_hi:[1,1,0] neg_lo:[0,1,0] neg_hi:[0,1,0]\n"
    "v_mfma_f32_16x16x32_bf16 v[212:215], v[180:183], v[164:167], v[212:215]\n"
    "v_pk_mad_u16 v12, v12, 8, s51 op_sel_hi:[1,0,0]\n"
    "v_mfma_f32_16x16x32_bf16 v[208:211], v[180:183], v[160:163], v[208:211]\n"
    "v_bfi_b32 v31, s57, v12, v31\n"
    "v_mfma_f32_16x16x32_bf16 v[208:211], v[148:151], v[128:131], v[208:211]\n"
    "v_mfma_f32_16x16x32_bf16 v[224:227], v[152:155], v[128:131], v[224:227]\n"
    "v_pk_maximum3_f16 v21, v32, v32, s50 op_sel_hi:[1,1,0] neg_lo:[0,1,0] neg_hi:[0,1,0]\n"
    "v_mfma_f32_16x16x32_bf16 v[224:227], v[184:187], v[160:163], v[224:227]\n"
    "v_pk_mad_u16 v21, v21, 8, s51 op_sel_hi:[1,0,0]\n"
    "v_mfma_f32_16x16x32_bf16 v[228:231], v[184:187], v[164:167], v[228:231]\n"
    "v_bfi_b32 v32, s57, v21, v32\n"
    "v_mfma_f32_16x16x32_bf16 v[228:231], v[152:155], v[132:135], v[228:231]\n"
    "v_mfma_f32_16x16x32_bf16 v[232:235], v[152:155], v[136:139], v[232:235]\n"
    "v_pk_maximum3_f16 v12, v33, v33, s50 op_sel_hi:[1,1,0] neg_lo:[0,1,0] neg_hi:[0,1,0]\n"
    "v_mfma_f32_16x16x32_bf16 v[232:235], v[184:187], v[168:171], v[232:235]\n"
    "v_pk_mad_u16 v12, v12, 8, s51 op_sel_hi:[1,0,0]\n"
    "v_mfma_f32_16x16x32_bf16 v[236:239], v[184:187], v[172:175], v[236:239]\n"
    "v_bfi_b32 v33, s57, v12, v33\n"
    "v_mfma_f32_16x16x32_bf16 v[236:239], v[152:155], v[140:143], v[236:239]\n"
    "v_mfma_f32_16x16x32_bf16 v[252:255], v[156:159], v[140:143], v[252:255]\n"
    "v_pk_maximum3_f16 v21, v34, v34, s50 op_sel_hi:[1,1,0] neg_lo:[0,1,0] neg_hi:[0,1,0]\n"
    "v_mfma_f32_16x16x32_bf16 v[252:255], v[188:191], v[172:175], v[252:255]\n"
    "v_pk_mad_u16 v21, v21, 8, s51 op_sel_hi:[1,0,0]\n"
    "v_mfma_f32_16x16x32_bf16 v[248:251], v[188:191], v[168:171], v[248:251]\n"
    "v_bfi_b32 v34, s57, v21, v34\n"
    "v_mfma_f32_16x16x32_bf16 v[248:251], v[156:159], v[136:139], v[248:251]\n"
    "v_mfma_f32_16x16x32_bf16 v[244:247], v[156:159], v[132:135], v[244:247]\n"
    "v_lshlrev_b32 v52, 4, v1\n"
    "v_mfma_f32_16x16x32_bf16 v[244:247], v[188:191], v[164:167], v[244:247]\n"
    "v_add_u32 v53, 4096, v52\n"
    "v_mfma_f32_16x16x32_bf16 v[240:243], v[188:191], v[160:163], v[240:243]\n"
    "v_mfma_f32_16x16x32_bf16 v[240:243], v[156:159], v[128:131], v[240:243]\n"
    "s_waitcnt vmcnt(6)\n"
    "s_barrier\n"
    "s_mul_i32 s42, s20, 5376\n"
    "s_add_u32 s40, s42, 1048576\n"
    "s_add_u32 s38, s16, s40\n"
    "s_addc_u32 s39, s17, 0\n"
    "s_add_u32 m0, s42, 98304\n"
    "s_nop 0\n"
    "global_load_lds_dwordx4 v52, s[38:39] offset:0 sc1\n"
    "ds_read_b128 v[128:131], v56 offset:0\n"
    "ds_read_b128 v[132:135], v56 offset:2048\n"
    "ds_read_b128 v[136:139], v56 offset:4096\n"
    "ds_read_b128 v[140:143], v56 offset:6144\n"
    "global_load_lds_dwordx4 v52, s[38:39] offset:1024 sc1\n"
    "ds_read_b64_tr_b16 v[144:145], v76 offset:0\n"
    "ds_read_b64_tr_b16 v[146:147], v76 offset:1024\n"
    "ds_read_b64_tr_b16 v[148:149], v77 offset:0\n"
    "ds_read_b64_tr_b16 v[150:151], v77 offset:1024\n"
    "global_load_lds_dwordx4 v52, s[38:39] offset:2048 sc1\n"
    "ds_read_b64_tr_b16 v[152:153], v78 offset:0\n"
    "ds_read_b64_tr_b16 v[154:155], v78 offset:1024\n"
    "ds_read_b64_tr_b16 v[156:157], v79 offset:0\n"
    "ds_read_b64_tr_b16 v[158:159], v79 offset:1024\n"
    "global_load_lds_dwordx4 v52, s[38:39] offset:3072 sc1\n"
    "ds_read_b128 v[160:163], v57 offset:0\n"
    "ds_read_b128 v[164:167], v57 offset:2048\n"
    "ds_read_b128 v[168:171], v57 offset:4096\n"
    "ds_read_b128 v[172:175], v57 offset:6144\n"
    "s_add_u32 m0, s42, 102400\n"
    "s_nop 0\n"
    "global_load_lds_dwordx4 v53, s[38:39] offset:0 sc1\n"
    "ds_read_b64_tr_b16 v[176:177], v76 offset:8192\n"
    "ds_read_b64_tr_b16 v[178:179], v76 offset:9216\n"
    "ds_read_b64_tr_b16 v[180:181], v77 offset:8192\n"
    "ds_read_b64_tr_b16 v[182:183], v77 offset:9216\n"
    "global_load_lds_dwordx4 v53, s[38:39] offset:256 sc1\n"
    "ds_read_b64_tr_b16 v[184:185], v78 offset:8192\n"
    "ds_read_b64_tr_b16 v[186:187], v78 offset:9216\n"
    "ds_read_b64_tr_b16 v[188:189], v79 offset:8192\n"
    "ds_read_b64_tr_b16 v[190:191], v79 offset:9216\n"
    "s_waitcnt vmcnt(10) lgkmcnt(0)\n"
    "s_barrier\n"
    "v_mfma_f32_16x16x32_bf16 v[192:195], v[144:147], v[128:131], v[192:195]\n"
    "v_pk_maximum3_f16 v12, v35, v35, s50 op_sel_hi:[1,1,0] neg_lo:[0,1,0] neg_hi:[0,1,0]\n"
    "v_mfma_f32_16x16x32_bf16 v[192:195], v[176:179], v[160:163], v[192:195]\n"
    "v_pk_mad_u16 v12, v12, 8, s51 op_sel_hi:[1,0,0]\n"
    "v_mfma_f32_16x16x32_bf16 v[196:199], v[176:179], v[164:167], v[196:199]\n"
    "v_mfma_f32_16x16x32_bf16 v[196:199], v[144:147], v[132:135], v[196:199]\n"
    "v_bfi_b32 v35, s57, v12, v35\n"
    "v_mfma_f32_16x16x32_bf16 v[200:203], v[144:147], v[136:139], v[200:203]\n"
    "v_pk_maximum3_f16 v21, v36, v36, s50 op_sel_hi:[1,1,0] neg_lo:[0,1,0] neg_hi:[0,1,0]\n"
    "v_mfma_f32_16x16x32_bf16 v[200:203], v[176:179], v[168:171], v[200:203]\n"
    "v_mfma_f32_16x16x32_bf16 v[204:207], v[176:179], v[172:175], v[204:207]\n"
    "v_pk_mad_u16 v21, v21, 8, s51 op_sel_hi:[1,0,0]\n"
    "v_mfma_f32_16x16x32_bf16 v[204:207], v[144:147], v[140:143], v[204:207]\n"
    "v_bfi_b32 v36, s57, v21, v36\n"
    "v_mfma_f32_16x16x32_bf16 v[220:223], v[148:151], v[140:143], v[220:223]\n"
    "v_mfma_f32_16x16x32_bf16 v[220:223], v[180:183], v[172:175], v[220:223]\n"
    "v_pk_maximum3_f16 v12, v37, v37, s50 op_sel_hi:[1,1,0] neg_lo:[0,1,0] neg_hi:[0,1,0]\n"
    "v_mfma_f32_16x16x32_bf16 v[216:219], v[180:183], v[168:171], v[216:219]\n"
    "v_pk_mad_u16 v12, v12, 8, s51 op_sel_hi:[1,0,0]\n"
    "v_mfma_f32_16x16x32_bf16 v[216:219], v[148:151], v[136:139], v[216:219]\n"
    "v_mfma_f32_16x16x32_bf16 v[212:215], v[148:151], v[132:135], v[212:215]\n"
    "v_bfi_b32 v37, s57, v12, v37\n"
    "v_mfma_f32_16x16x32_bf16 v[212:215], v[180:183], v[164:167], v[212:215]\n"
    "v_pk_maximum3_f16 v21, v38, v38, s50 op_sel_hi:[1,1,0] neg_lo:[0,1,0] neg_hi:[0,1,0]\n"
    "v_mfma_f32_16x16x32_bf16 v[208:211], v[180:183], v[160:163], v[208:211]\n"
    "v_mfma_f32_16x16x32_bf16 v[208:211], v[148:151], v[128:131], v[208:211]\n"
    "v_pk_mad_u16 v21, v21, 8, s51 op_sel_hi:[1,0,0]\n"
    "v_mfma_f32_16x16x32_bf16 v[224:227], v[152:155], v[128:131], v[224:227]\n"
    "v_mfma_f32_16x16x32_bf16 v[224:227], v[184:187], v[160:163], v[224:227]\n"
    "v_bfi_b32 v38, s57, v21, v38\n"
    "v_mfma_f32_16x16x32_bf16 v[228:231], v[184:187], v[164:167], v[228:231]\n"
    "v_pk_maximum3_f16 v12, v39, v39, s50 op_sel_hi:[1,1,0] neg_lo:[0,1,0] neg_hi:[0,1,0]\n"
    "v_mfma_f32_16x16x32_bf16 v[228:231], v[152:155], v[132:135], v[228:231]\n"
    "v_mfma_f32_16x16x32_bf16 v[232:235], v[152:155], v[136:139], v[232:235]\n"
    "v_pk_mad_u16 v12, v12, 8, s51 op_sel_hi:[1,0,0]\n"
    "v_mfma_f32_16x16x32_bf16 v[232:235], v[184:187], v[168:171], v[232:235]\n"
    "v_bfi_b32 v39, s57, v12, v39\n"
    "v_mfma_f32_16x16x32_bf16 v[236:239], v[184:187], v[172:175], v[236:239]\n"
    "v_mfma_f32_16x16x32_bf16 v[236:239], v[152:155], v[140:143], v[236:239]\n"
    "v_pk_maximum3_f16 v21, v40, v40, s50 op_sel_hi:[1,1,0] neg_lo:[0,1,0] neg_hi:[0,1,0]\n"
    "v_mfma_f32_16x16x32_bf16 v[252:255], v[156:159], v[140:143], v[252:255]\n"
    "v_pk_mad_u16 v21, v21, 8, s51 op_sel_hi:[1,0,0]\n"
    "v_mfma_f32_16x16x32_bf16 v[252:255], v[188:191], v[172:175], v[252:255]\n"
    "v_mfma_f32_16x16x32_bf16 v[248:251], v[188:191], v[168:171], v[248:251]\n"
    "v_bfi_b32 v40, s57, v21, v40\n"
    "v_mfma_f32_16x16x32_bf16 v[248:251], v[156:159], v[136:139], v[248:251]\n"
    "s_sub_u32 s66, 4112, s47\n"
    "s_lshl_b32 s66, s66, 19\n"
    "v_cvt_pkrtz_f16_f32 v12, s66, s66\n"
    "v_mfma_f32_16x16x32_bf16 v[244:247], v[156:159], v[132:135], v[244:247]\n"
    "v_mfma_f32_16x16x32_bf16 v[244:247], v[188:191], v[164:167], v[244:247]\n"
    "v_lshl_or_b32 v21, v12, 16, s47\n"
    "v_mfma_f32_16x16x32_bf16 v[240:243], v[188:191], v[160:163], v[240:243]\n"
    "v_mfma_f32_16x16x32_bf16 v[240:243], v[156:159], v[128:131], v[240:243]\n"
    "s_waitcnt vmcnt(6)\n"
    "s_barrier\n"
    "ds_read_b128 v[128:131], v56 offset:49152\n"
    "ds_read_b128 v[132:135], v56 offset:51200\n"
    "ds_read_b128 v[136:139], v56 offset:53248\n"
    "ds_read_b128 v[140:143], v56 offset:55296\n"
    "ds_read_b64_tr_b16 v[144:145], v76 offset:49152\n"
    "ds_read_b64_tr_b16 v[146:147], v76 offset:50176\n"
    "ds_read_b64_tr_b16 v[148:149], v77 offset:49152\n"
    "ds_read_b64_tr_b16 v[150:151], v77 offset:50176\n"
    "ds_read_b64_tr_b16 v[152:153], v78 offset:49152\n"
    "ds_read_b64_tr_b16 v[154:155], v78 offset:50176\n"
    "ds_read_b64_tr_b16 v[156:157], v79 offset:49152\n"
    "ds_read_b64_tr_b16 v[158:159], v79 offset:50176\n"
    "ds_read_b128 v[160:163], v57 offset:49152\n"
    "ds_read_b128 v[164:167], v57 offset:51200\n"
    "ds_read_b128 v[168:171], v57 offset:53248\n"
    "ds_read_b128 v[172:175], v57 offset:55296\n"
    "ds_read_b64_tr_b16 v[176:177], v76 offset:57344\n"
    "ds_read_b64_tr_b16 v[178:179], v76 offset:58368\n"
    "ds_read_b64_tr_b16 v[180:181], v77 offset:57344\n"
    "ds_read_b64_tr_b16 v[182:183], v77 offset:58368\n"
    "ds_read_b64_tr_b16 v[184:185], v78 offset:57344\n"
    "ds_read_b64_tr_b16 v[186:187], v78 offset:58368\n"
    "ds_read_b64_tr_b16 v[188:189], v79 offset:57344\n"
    "ds_read_b64_tr_b16 v[190:191], v79 offset:58368\n"
    "s_cmp_lg_u32 s20, 3\n"
    "s_cbranch_scc1 .Lxd_on4\n"
    "s_mov_b64 exec, 1\n"
    "ds_write_b64 v8, v[26:27]\n"
    "s_mov_b64 exec, 15\n"
    "ds_write2_b32 v8, v22, v13 offset0:4 offset1:8\n"
    "s_mov_b64 exec, -1\n"
    ".Lxd_on4:\n"
    "s_waitcnt vmcnt(0) lgkmcnt(0)\n"
    "s_cmp_lg_u32 s20, 3\n"
    "s_cbranch_scc1 .Lxd_on5\n"
    "s_mov_b64 exec, 0x200\n"
    "global_store_dword v11, v43, s[16:17] sc1\n"
    "s_mov_b64 exec, -1\n"
    ".Lxd_on5:\n"
    "s_barrier\n"
    "s_setprio 3\n"
    "v_mfma_f32_16x16x32_bf16 v[192:195], v[144:147], v[128:131], v[192:195]\n"
    "v_add_u32 v42, 98304, v52\n"
    "v_mfma_f32_16x16x32_bf16 v[192:195], v[176:179], v[160:163], v[192:195]\n"
    "v_lshl_add_u32 v11, v1, 2, s3\n"
    "v_mfma_f32_16x16x32_bf16 v[196:199], v[176:179], v[164:167], v[196:199]\n"
    "v_mov_b32 v13, s58\n"
    "v_mfma_f32_16x16x32_bf16 v[196:199], v[144:147], v[132:135], v[196:199]\n"
    "s_mov_b32 s36, 0\n"
    "v_mfma_f32_16x16x32_bf16 v[200:203], v[144:147], v[136:139], v[200:203]\n"
    "v_mov_b32 v84, s65\n"
    "v_mfma_f32_16x16x32_bf16 v[200:203], v[176:179], v[168:171], v[200:203]\n"
    "v_lshl_add_u32 v7, s23, 12, v3\n"
    "v_mfma_f32_16x16x32_bf16 v[204:207], v[176:179], v[172:175], v[204:207]\n"
    "v_lshl_add_u32 v8, s23, 12, v4\n"
    "v_mfma_f32_16x16x32_bf16 v[204:207], v[144:147], v[140:143], v[204:207]\n"
    "v_lshl_add_u32 v9, s23, 12, v5\n"
    "v_mfma_f32_16x16x32_bf16 v[220:223], v[148:151], v[140:143], v[220:223]\n"
    "v_lshl_add_u32 v10, s23, 12, v6\n"
    "v_mfma_f32_16x16x32_bf16 v[220:223], v[180:183], v[172:175], v[220:223]\n"
    "v_mfma_f32_16x16x32_bf16 v[216:219], v[180:183], v[168:171], v[216:219]\n"
    "v_mul_f32 v76, v192, v192\n"
    "v_mul_f32 v77, v193, v193\n"
    "v_mfma_f32_16x16x32_bf16 v[216:219], v[148:151], v[136:139], v[216:219]\n"
    "v_mul_f32 v78, v196, v196\n"
    "v_mul_f32 v79, v197, v197\n"
    "v_mfma_f32_16x16x32_bf16 v[212:215], v[148:151], v[132:135], v[212:215]\n"
    "v_mul_f32 v80, v200, v200\n"
    "v_mul_f32 v81, v201, v201\n"
    "v_mfma_f32_16x16x32_bf16 v[212:215], v[180:183], v[164:167], v[212:215]\n"
    "v_mul_f32 v82, v204, v204\n"
    "v_mul_f32 v83, v205, v205\n"
    "v_mfma_f32_16x16x32_bf16 v[208:211], v[180:183], v[160:163], v[208:211]\n"
    "v_fma_f32 v76, v76, s64, v84\n"
    "v_fma_f32 v77, v77, s64, v84\n"
    "v_mfma_f32_16x16x32_bf16 v[208:211], v[148:151], v[128:131], v[208:211]\n"
    "v_fma_f32 v78, v78, s64, v84\n"
    "v_fma_f32 v79, v79, s64, v84\n"
    "v_mfma_f32_16x16x32_bf16 v[224:227], v[152:155], v[128:131], v[224:227]\n"
    "v_fma_f32 v80, v80, s64, v84\n"
    "v_fma_f32 v81, v81, s64, v84\n"
    "v_mfma_f32_16x16x32_bf16 v[224:227], v[184:187], v[160:163], v[224:227]\n"
    "v_fma_f32 v82, v82, s64, v84\n"
    "v_fma_f32 v83, v83, s64, v84\n"
    "v_mfma_f32_16x16x32_bf16 v[228:231], v[184:187], v[164:167], v[228:231]\n"
    "v_mul_f32 v76, v76, v192\n"
    "v_mul_f32 v77, v77, v193\n"
    "v_mfma_f32_16x16x32_bf16 v[228:231], v[152:155], v[132:135], v[228:231]\n"
    "v_mul_f32 v78, v78, v196\n"
    "v_mul_f32 v79, v79, v197\n"
    "v_mfma_f32_16x16x32_bf16 v[232:235], v[152:155], v[136:139], v[232:235]\n"
    "v_mul_f32 v80, v80, v200\n"
    "v_mul_f32 v81, v81, v201\n"
    "v_mfma_f32_16x16x32_bf16 v[232:235], v[184:187], v[168:171], v[232:235]\n"
    "v_mul_f32 v82, v82, v204\n"
    "v_mul_f32 v83, v83, v205\n"
    "v_mfma_f32_16x16x32_bf16 v[236:239], v[184:187], v[172:175], v[236:239]\n"
    "v_exp_f32 v76, v76\n"
    "v_mfma_f32_16x16x32_bf16 v[236:239], v[152:155], v[140:143], v[236:239]\n"
    "v_exp_f32 v77, v77\n"
    "v_mfma_f32_16x16x32_bf16 v[252:255], v[156:159], v[140:143], v[252:255]\n"
    "v_exp_f32 v78, v78\n"
    "v_mfma_f32_16x16x32_bf16 v[252:255], v[188:191], v[172:175], v[252:255]\n"
    "v_exp_f32 v79, v79\n"
    "v_mfma_f32_16x16x32_bf16 v[248:251], v[188:191], v[168:171], v[248:251]\n"
    "v_exp_f32 v80, v80\n"
    "v_mfma_f32_16x16x32_bf16 v[248:251], v[156:159], v[136:139], v[248:251]\n"
    "v_exp_f32 v81, v81\n"
    "v_mfma_f32_16x16x32_bf16 v[244:247], v[156:159], v[132:135], v[244:247]\n"
    "v_exp_f32 v82, v82\n"
    "v_mfma_f32_16x16x32_bf16 v[244:247], v[188:191], v[164:167], v[244:247]\n"
    "v_exp_f32 v83, v83\n"
    "v_mfma_f32_16x16x32_bf16 v[240:243], v[188:191], v[160:163], v[240:243]\n"
    "v_mfma_f32_16x16x32_bf16 v[240:243], v[156:159], v[128:131], v[240:243]\n"
    "s_cmp_lg_u32 s21, 0\n"
    "s_cbranch_scc1 .Lxd_g1\n"
    "s_barrier\n"
    ".Lxd_g1:\n"
    "s_setprio 2\n"
    "s_cmp_eq_u32 s22, 3\n"
    "s_cbranch_scc1 .Lxd_np\n"
    "ds_write_b32 v25, v24 offset:20\n"
    "ds_write_b128 v15, v[28:31] offset:0\n"
    "ds_write_b128 v15, v[32:35] offset:144\n"
    "ds_write_b128 v15, v[36:39] offset:288\n"
    "ds_write2_b32 v16, v40, v12 offset1:72\n"
    "ds_or_b32 v25, v21 offset:532\n"
    ".Lxd_np:\n"
    "ds_read2_b32 v[26:27], v41 offset1:1\n"
    "ds_read_b32 v0, v41 offset:16\n"
    "ds_read_b32 v23, v41 offset:24\n"
    "s_load_dword s26, s[68:69], 0\n"
    "v_add_u32 v25, s24, v25\n"
    "ds_read_b128 v[92:95], v42 offset:0\n"
    "ds_read_b128 v[96:99], v42 offset:1024\n"
    "ds_read_b128 v[100:103], v42 offset:2048\n"
    "ds_read_b128 v[104:107], v42 offset:3072\n"
    "ds_read_b128 v[108:111], v42 offset:4096\n"
    "ds_read_b128 v[112:115], v42 offset:5120\n"
    "ds_read_b128 v[116:119], v42 offset:6144\n"
    "ds_read_b128 v[120:123], v42 offset:7168\n"
    "ds_read_b128 v[124:127], v42 offset:8192\n"
    "v_pk_fma_f32 v[76:77], v[76:77], v[60:61], v[60:61]\n"
    "v_pk_fma_f32 v[78:79], v[78:79], v[60:61], v[60:61]\n"
    "v_pk_fma_f32 v[80:81], v[80:81], v[60:61], v[60:61]\n"
    "v_pk_fma_f32 v[82:83], v[82:83], v[60:61], v[60:61]\n"
    "v_rcp_f32 v76, v76\n"
    "v_rcp_f32 v77, v77\n"
    "v_rcp_f32 v78, v78\n"
    "v_rcp_f32 v79, v79\n"
    "v_rcp_f32 v80, v80\n"
    "v_rcp_f32 v81, v81\n"
    "v_rcp_f32 v82, v82\n"
    "v_rcp_f32 v83, v83\n"
    "v_pk_mul_f32 v[84:85], v[192:193], v[76:77]\n"
    "v_pk_mul_f32 v[86:87], v[196:197], v[78:79]\n"
    "v_pk_mul_f32 v[88:89], v[200:201], v[80:81]\n"
    "v_pk_mul_f32 v[90:91], v[204:205], v[82:83]\n"
    "s_cmp_eq_u32 s22, 3\n"
    "s_cbranch_scc1 .Lxd_on6\n"
    ".Lxd_on6r:\n"
    "s_setprio 3\n"
    "v_pk_mul_f32 v[76:77], v[194:195], v[194:195]\n"
    "v_pk_mul_f32 v[78:79], v[198:199], v[198:199]\n"
    "v_pk_mul_f32 v[80:81], v[202:203], v[202:203]\n"
    "v_pk_mul_f32 v[82:83], v[206:207], v[206:207]\n"
    "v_pk_fma_f32 v[76:77], v[76:77], s[64:65], s[64:65] op_sel:[0,0,1] op_sel_hi:[1,0,1]\n"
    "v_pk_fma_f32 v[78:79], v[78:79], s[64:65], s[64:65] op_sel:[0,0,1] op_sel_hi:[1,0,1]\n"
    "v_pk_fma_f32 v[80:81], v[80:81], s[64:65], s[64:65] op_sel:[0,0,1] op_sel_hi:[1,0,1]\n"
    "v_pk_fma_f32 v[82:83], v[82:83], s[64:65], s[64:65] op_sel:[0,0,1] op_sel_hi:[1,0,1]\n"
    "v_pk_mul_f32 v[76:77], v[76:77], v[194:195]\n"
    "v_pk_mul_f32 v[78:79], v[78:79], v[198:199]\n"
    "v_pk_mul_f32 v[80:81], v[80:81], v[202:203]\n"
    "v_pk_mul_f32 v[82:83], v[82:83], v[206:207]\n"
    "v_exp_f32 v76, v76\n"
    "v_exp_f32 v77, v77\n"
    "v_exp_f32 v78, v78\n"
    "v_exp_f32 v79, v79\n"
    "v_exp_f32 v80, v80\n"
    "v_exp_f32 v81, v81\n"
    "v_exp_f32 v82, v82\n"
    "v_exp_f32 v83, v83\n"
    "v_pk_fma_f32 v[76:77], v[76:77], v[62:63], v[62:63]\n"
    "v_pk_fma_f32 v[78:79], v[78:79], v[62:63], v[62:63]\n"
    "v_pk_fma_f32 v[80:81], v[80:81], v[62:63], v[62:63]\n"
    "v_pk_fma_f32 v[82:83], v[82:83], v[62:63], v[62:63]\n"
    "v_rcp_f32 v76, v76\n"
    "v_rcp_f32 v77, v77\n"
    "v_rcp_f32 v78, v78\n"
    "v_rcp_f32 v79, v79\n"
    "v_rcp_f32 v80, v80\n"
    "v_rcp_f32 v81, v81\n"
    "v_rcp_f32 v82, v82\n"
    "v_rcp_f32 v83, v83\n"
    "v_pk_fma_f32 v[84:85], v[194:195], v[76:77], v[84:85]\n"
    "v_pk_fma_f32 v[86:87], v[198:199], v[78:79], v[86:87]\n"
    "v_pk_fma_f32 v[88:89], v[202:203], v[80:81], v[88:89]\n"
    "v_pk_fma_f32 v[90:91], v[206:207], v[82:83], v[90:91]\n"
    "ds_read_b32 v22, v13 offset:512\n"
    "v_pk_mul_f32 v[76:77], v[208:209], v[208:209]\n"
    "v_pk_mul_f32 v[78:79], v[212:213], v[212:213]\n"
    "v_pk_mul_f32 v[80:81], v[216:217], v[216:217]\n"
    "v_pk_mul_f32 v[82:83], v[220:221], v[220:221]\n"
    "v_pk_fma_f32 v[76:77], v[76:77], s[64:65], s[64:65] op_sel:[0,0,1] op_sel_hi:[1,0,1]\n"
    "v_pk_fma_f32 v[78:79], v[78:79], s[64:65], s[64:65] op_sel:[0,0,1] op_sel_hi:[1,0,1]\n"
    "v_pk_fma_f32 v[80:81], v[80:81], s[64:65], s[64:65] op_sel:[0,0,1] op_sel_hi:[1,0,1]\n"
    "v_pk_fma_f32 v[82:83], v[82:83], s[64:65], s[64:65] op_sel:[0,0,1] op_sel_hi:[1,0,1]\n"
    "v_pk_mul_f32 v[76:77], v[76:77], v[208:209]\n"
    "v_pk_mul_f32 v[78:79], v[78:79], v[212:213]\n"
    "v_pk_mul_f32 v[80:81], v[80:81], v[216:217]\n"
    "v_pk_mul_f32 v[82:83], v[82:83], v[220:221]\n"
    "v_exp_f32 v76, v76\n"
    "v_exp_f32 v77, v77\n"
    "ds_read_b128 v[128:131], v42 offset:9216\n"
    "v_exp_f32 v78, v78\n"
    "v_exp_f32 v79, v79\n"
    "v_exp_f32 v80, v80\n"
    "ds_read_b128 v[132:135], v42 offset:10240\n"
    "v_exp_f32 v81, v81\n"
    "v_exp_f32 v82, v82\n"
    "v_exp_f32 v83, v83\n"
    "ds_read_b128 v[136:139], v42 offset:11264\n"
    "v_pk_fma_f32 v[76:77], v[76:77], v[64:65], v[64:65]\n"
    "v_pk_fma_f32 v[78:79], v[78:79], v[64:65], v[64:65]\n"
    "v_pk_fma_f32 v[80:81], v[80:81], v[64:65], v[64:65]\n"
    "v_pk_fma_f32 v[82:83], v[82:83], v[64:65], v[64:65]\n"
    "v_rcp_f32 v76, v76\n"
    "v_rcp_f32 v77, v77\n"
    "ds_read_b128 v[140:143], v42 offset:12288\n"
    "v_rcp_f32 v78, v78\n"
    "v_rcp_f32 v79, v79\n"
    "v_rcp_f32 v80, v80\n"
    "ds_read_b128 v[144:147], v42 offset:13312\n"
    "v_rcp_f32 v81, v81\n"
    "v_rcp_f32 v82, v82\n"
    "v_rcp_f32 v83, v83\n"
    "ds_read_b128 v[148:151], v42 offset:14336\n"
    "v_pk_fma_f32 v[84:85], v[208:209], v[76:77], v[84:85]\n"
    "v_pk_fma_f32 v[86:87], v[212:213], v[78:79], v[86:87]\n"
    "v_pk_fma_f32 v[88:89], v[216:217], v[80:81], v[88:89]\n"
    "v_pk_fma_f32 v[90:91], v[220:221], v[82:83], v[90:91]\n"
    "s_waitcnt lgkmcnt(6)\n"
    "v_readfirstlane_b32 s45, v22\n"
    "s_cmp_lt_i32 s45, 16\n"
    "s_cbranch_scc1 .Lxd_ck7\n"
    ".Lxd_ck7r:\n"
    "ds_read_b64_tr_b16 v[44:45], v9 offset:0\n"
    "ds_read_b64_tr_b16 v[46:47], v10 offset:0\n"
    "ds_read_b64_tr_b16 v[48:49], v7 offset:0\n"
    "ds_read_b64_tr_b16 v[50:51], v8 offset:576\n"
    "ds_read_b64_tr_b16 v[52:53], v8 offset:1152\n"
    "ds_read_b64_tr_b16 v[54:55], v8 offset:1728\n"
    "ds_read_b64_tr_b16 v[56:57], v8 offset:2304\n"
    "ds_read_b64_tr_b16 v[58:59], v8 offset:2880\n"
    "v_readfirstlane_b32 s28, v26\n"
    "v_readfirstlane_b32 s29, v27\n"
    "s_setprio 2\n"
    "v_pk_mul_f32 v[76:77], v[210:211], v[210:211]\n"
    "v_pk_mul_f32 v[78:79], v[214:215], v[214:215]\n"
    "v_pk_mul_f32 v[80:81], v[218:219], v[218:219]\n"
    "v_pk_mul_f32 v[82:83], v[222:223], v[222:223]\n"
    "v_pk_fma_f32 v[76:77], v[76:77], s[64:65], s[64:65] op_sel:[0,0,1] op_sel_hi:[1,0,1]\n"
    "v_pk_fma_f32 v[78:79], v[78:79], s[64:65], s[64:65] op_sel:[0,0,1] op_sel_hi:[1,0,1]\n"
    "v_pk_fma_f32 v[80:81], v[80:81], s[64:65], s[64:65] op_sel:[0,0,1] op_sel_hi:[1,0,1]\n"
    "v_pk_fma_f32 v[82:83], v[82:83], s[64:65], s[64:65] op_sel:[0,0,1] op_sel_hi:[1,0,1]\n"
    "v_pk_mul_f32 v[76:77], v[76:77], v[210:211]\n"
    "v_pk_mul_f32 v[78:79], v[78:79], v[214:215]\n"
    "v_pk_mul_f32 v[80:81], v[80:81], v[218:219]\n"
    "v_pk_mul_f32 v[82:83], v[82:83], v[222:223]\n"
    "v_exp_f32 v76, v76\n"
    "v_exp_f32 v77, v77\n"
    "ds_read_b128 v[152:155], v42 offset:15360\n"
    "v_exp_f32 v78, v78\n"
    "v_exp_f32 v79, v79\n"
    "v_exp_f32 v80, v80\n"
    "ds_read_b128 v[156:159], v42 offset:16384\n"
    "v_exp_f32 v81, v81\n"
    "v_exp_f32 v82, v82\n"
    "v_exp_f32 v83, v83\n"
    "ds_read_b128 v[160:163], v42 offset:17408\n"
    "v_pk_fma_f32 v[76:77], v[76:77], v[66:67], v[66:67]\n"
    "v_pk_fma_f32 v[78:79], v[78:79], v[66:67], v[66:67]\n"
    "v_pk_fma_f32 v[80:81], v[80:81], v[66:67], v[66:67]\n"
    "v_pk_fma_f32 v[82:83], v[82:83], v[66:67], v[66:67]\n"
    "v_rcp_f32 v76, v76\n"
    "v_rcp_f32 v77, v77\n"
    "ds_read_b128 v[164:167], v42 offset:18432\n"
    "v_rcp_f32 v78, v78\n"
    "v_rcp_f32 v79, v79\n"
    "v_rcp_f32 v80, v80\n"
    "ds_read_b128 v[168:171], v42 offset:19456\n"
    "v_rcp_f32 v81, v81\n"
    "v_rcp_f32 v82, v82\n"
    "v_rcp_f32 v83, v83\n"
    "ds_read_b128 v[172:175], v42 offset:20480\n"
    "v_pk_fma_f32 v[84:85], v[210:211], v[76:77], v[84:85]\n"
    "v_pk_fma_f32 v[86:87], v[214:215], v[78:79], v[86:87]\n"
    "v_pk_fma_f32 v[88:89], v[218:219], v[80:81], v[88:89]\n"
    "v_pk_fma_f32 v[90:91], v[222:223], v[82:83], v[90:91]\n"
    "s_cmp_eq_u32 s22, 3\n"
    "s_cbranch_scc1 .Lxd_on9\n"
    ".Lxd_on9r:\n"
    "s_cmp_eq_u32 s22, 0\n"
    "s_cbranch_scc1 .Lxd_on10\n"
    ".Lxd_on10r:\n"
    "v_pk_mul_f32 v[76:77], v[224:225], v[224:225]\n"
    "v_pk_mul_f32 v[78:79], v[228:229], v[228:229]\n"
    "v_pk_mul_f32 v[80:81], v[232:233], v[232:233]\n"
    "v_pk_mul_f32 v[82:83], v[236:237], v[236:237]\n"
    "v_pk_fma_f32 v[76:77], v[76:77], s[64:65], s[64:65] op_sel:[0,0,1] op_sel_hi:[1,0,1]\n"
    "v_pk_fma_f32 v[78:79], v[78:79], s[64:65], s[64:65] op_sel:[0,0,1] op_sel_hi:[1,0,1]\n"
    "v_pk_fma_f32 v[80:81], v[80:81], s[64:65], s[64:65] op_sel:[0,0,1] op_sel_hi:[1,0,1]\n"
    "v_pk_fma_f32 v[82:83], v[82:83], s[64:65], s[64:65] op_sel:[0,0,1] op_sel_hi:[1,0,1]\n"
    "v_pk_mul_f32 v[76:77], v[76:77], v[224:225]\n"
    "v_pk_mul_f32 v[78:79], v[78:79], v[228:229]\n"
    "v_pk_mul_f32 v[80:81], v[80:81], v[232:233]\n"
    "v_pk_mul_f32 v[82:83], v[82:83], v[236:237]\n"
    "v_exp_f32 v76, v76\n"
    "v_exp_f32 v77, v77\n"
    "ds_read_b128 v[176:179], v42 offset:21504\n"
    "v_exp_f32 v78, v78\n"
    "v_exp_f32 v79, v79\n"
    "v_exp_f32 v80, v80\n"
    "ds_read_b128 v[180:183], v42 offset:22528\n"
    "v_exp_f32 v81, v81\n"
    "v_exp_f32 v82, v82\n"
    "v_exp_f32 v83, v83\n"
    "ds_read_b128 v[184:187], v42 offset:23552\n"
    "v_pk_fma_f32 v[76:77], v[76:77], v[68:69], v[68:69]\n"
    "v_pk_fma_f32 v[78:79], v[78:79], v[68:69], v[68:69]\n"
    "v_pk_fma_f32 v[80:81], v[80:81], v[68:69], v[68:69]\n"
    "v_pk_fma_f32 v[82:83], v[82:83], v[68:69], v[68:69]\n"
    "v_rcp_f32 v76, v76\n"
    "v_rcp_f32 v77, v77\n"
    "ds_read_b128 v[188:191], v42 offset:24576\n"
    "v_rcp_f32 v78, v78\n"
    "v_rcp_f32 v79, v79\n"
    "v_rcp_f32 v80, v80\n"
    "ds_read_b128 v[192:195], v42 offset:25600\n"
    "v_rcp_f32 v81, v81\n"
    "v_rcp_f32 v82, v82\n"
    "v_rcp_f32 v83, v83\n"
    "ds_read_b128 v[196:199], v42 offset:26624\n"
    "v_pk_fma_f32 v[84:85], v[224:225], v[76:77], v[84:85]\n"
    "v_pk_fma_f32 v[86:87], v[228:229], v[78:79], v[86:87]\n"
    "v_pk_fma_f32 v[88:89], v[232:233], v[80:81], v[88:89]\n"
    "v_pk_fma_f32 v[90:91], v[236:237], v[82:83], v[90:91]\n"
    "s_setprio 1\n"
    "v_pk_mul_f32 v[76:77], v[226:227], v[226:227]\n"
    "v_pk_mul_f32 v[78:79], v[230:231], v[230:231]\n"
    "v_pk_mul_f32 v[80:81], v[234:235], v[234:235]\n"
    "v_pk_mul_f32 v[82:83], v[238:239], v[238:239]\n"
    "v_pk_fma_f32 v[76:77], v[76:77], s[64:65], s[64:65] op_sel:[0,0,1] op_sel_hi:[1,0,1]\n"
    "v_pk_fma_f32 v[78:79], v[78:79], s[64:65], s[64:65] op_sel:[0,0,1] op_sel_hi:[1,0,1]\n"
    "v_pk_fma_f32 v[80:81], v[80:81], s[64:65], s[64:65] op_sel:[0,0,1] op_sel_hi:[1,0,1]\n"
    "v_pk_fma_f32 v[82:83], v[82:83], s[64:65], s[64:65] op_sel:[0,0,1] op_sel_hi:[1,0,1]\n"
    "v_pk_mul_f32 v[76:77], v[76:77], v[226:227]\n"
    "v_pk_mul_f32 v[78:79], v[78:79], v[230:231]\n"
    "v_pk_mul_f32 v[80:81], v[80:81], v[234:235]\n"
    "v_pk_mul_f32 v[82:83], v[82:83], v[238:239]\n"
    "v_exp_f32 v76, v76\n"
    "v_exp_f32 v77, v77\n"
    "ds_read_b128 v[200:203], v42 offset:27648\n"
    "v_exp_f32 v78, v78\n"
    "v_exp_f32 v79, v79\n"
    "v_exp_f32 v80, v80\n"
    "ds_read_b128 v[204:207], v42 offset:28672\n"
    "v_exp_f32 v81, v81\n"
    "v_exp_f32 v82, v82\n"
    "v_exp_f32 v83, v83\n"
    "ds_read_b128 v[208:211], v42 offset:29696\n"
    "v_pk_fma_f32 v[76:77], v[76:77], v[70:71], v[70:71]\n"
    "v_pk_fma_f32 v[78:79], v[78:79], v[70:71], v[70:71]\n"
    "v_pk_fma_f32 v[80:81], v[80:81], v[70:71], v[70:71]\n"
    "v_pk_fma_f32 v[82:83], v[82:83], v[70:71], v[70:71]\n"
    "v_rcp_f32 v76, v76\n"
    "v_rcp_f32 v77, v77\n"
    "ds_read_b128 v[212:215], v42 offset:30720\n"
    "v_rcp_f32 v78, v78\n"
    "v_rcp_f32 v79, v79\n"
    "v_rcp_f32 v80, v80\n"
    "ds_read_b128 v[216:219], v42 offset:31744\n"
    "v_rcp_f32 v81, v81\n"
    "v_rcp_f32 v82, v82\n"
    "v_rcp_f32 v83, v83\n"
    "ds_read_b128 v[220:223], v42 offset:32768\n"
    "v_pk_fma_f32 v[84:85], v[226:227], v[76:77], v[84:85]\n"
    "v_pk_fma_f32 v[86:87], v[230:231], v[78:79], v[86:87]\n"
    "v_pk_fma_f32 v[88:89], v[234:235], v[80:81], v[88:89]\n"
    "v_pk_fma_f32 v[90:91], v[238:239], v[82:83], v[90:91]\n"
    "v_pk_mul_f32 v[76:77], v[240:241], v[240:241]\n"
    "v_pk_mul_f32 v[78:79], v[244:245], v[244:245]\n"
    "v_pk_mul_f32 v[80:81], v[248:249], v[248:249]\n"
    "v_pk_mul_f32 v[82:83], v[252:253], v[252:253]\n"
    "v_pk_fma_f32 v[76:77], v[76:77], s[64:65], s[64:65] op_sel:[0,0,1] op_sel_hi:[1,0,1]\n"
    "v_pk_fma_f32 v[78:79], v[78:79], s[64:65], s[64:65] op_sel:[0,0,1] op_sel_hi:[1,0,1]\n"
    "v_pk_fma_f32 v[80:81], v[80:81], s[64:65], s[64:65] op_sel:[0,0,1] op_sel_hi:[1,0,1]\n"
    "v_pk_fma_f32 v[82:83], v[82:83], s[64:65], s[64:65] op_sel:[0,0,1] op_sel_hi:[1,0,1]\n"
    "v_pk_mul_f32 v[76:77], v[76:77], v[240:241]\n"
    "v_pk_mul_f32 v[78:79], v[78:79], v[244:245]\n"
    "v_pk_mul_f32 v[80:81], v[80:81], v[248:249]\n"
    "v_pk_mul_f32 v[82:83], v[82:83], v[252:253]\n"
    "v_exp_f32 v76, v76\n"
    "v_exp_f32 v77, v77\n"
    "v_exp_f32 v78, v78\n"
    "v_exp_f32 v79, v79\n"
    "v_exp_f32 v80, v80\n"
    "v_exp_f32 v81, v81\n"
    "v_exp_f32 v82, v82\n"
    "v_exp_f32 v83, v83\n"
    "v_pk_fma_f32 v[76:77], v[76:77], v[72:73], v[72:73]\n"
    "v_pk_fma_f32 v[78:79], v[78:79], v[72:73], v[72:73]\n"
    "v_pk_fma_f32 v[80:81], v[80:81], v[72:73], v[72:73]\n"
    "v_pk_fma_f32 v[82:83], v[82:83], v[72:73], v[72:73]\n"
    "v_rcp_f32 v76, v76\n"
    "v_rcp_f32 v77, v77\n"
    "v_rcp_f32 v78, v78\n"
    "v_rcp_f32 v79, v79\n"
    "v_rcp_f32 v80, v80\n"
    "v_rcp_f32 v81, v81\n"
    "v_rcp_f32 v82, v82\n"
    "v_rcp_f32 v83, v83\n"
    "v_pk_fma_f32 v[84:85], v[240:241], v[76:77], v[84:85]\n"
    "v_pk_fma_f32 v[86:87], v[244:245], v[78:79], v[86:87]\n"
    "v_pk_fma_f32 v[88:89], v[248:249], v[80:81], v[88:89]\n"
    "v_pk_fma_f32 v[90:91], v[252:253], v[82:83], v[90:91]\n"
    "s_cmp_eq_u32 s22, 3\n"
    "s_cbranch_scc1 .Lxd_on11\n"
    ".Lxd_on11r:\n"
    "s_cmp_eq_u32 s22, 0\n"
    "s_cbranch_scc1 .Lxd_on13\n"
    ".Lxd_on13r:\n"
    "s_cmp_eq_u32 s22, 1\n"
    "s_cbranch_scc1 .Lxd_on14\n"
    ".Lxd_on14r:\n"
    "s_setprio 0\n"
    "v_pk_mul_f32 v[76:77], v[242:243], v[242:243]\n"
    "v_pk_mul_f32 v[78:79], v[246:247], v[246:247]\n"
    "v_pk_mul_f32 v[80:81], v[250:251], v[250:251]\n"
    "v_pk_mul_f32 v[82:83], v[254:255], v[254:255]\n"
    "v_pk_fma_f32 v[76:77], v[76:77], s[64:65], s[64:65] op_sel:[0,0,1] op_sel_hi:[1,0,1]\n"
    "v_pk_fma_f32 v[78:79], v[78:79], s[64:65], s[64:65] op_sel:[0,0,1] op_sel_hi:[1,0,1]\n"
    "v_pk_fma_f32 v[80:81], v[80:81], s[64:65], s[64:65] op_sel:[0,0,1] op_sel_hi:[1,0,1]\n"
    "v_pk_fma_f32 v[82:83], v[82:83], s[64:65], s[64:65] op_sel:[0,0,1] op_sel_hi:[1,0,1]\n"
    "v_pk_mul_f32 v[76:77], v[76:77], v[242:243]\n"
    "v_pk_mul_f32 v[78:79], v[78:79], v[246:247]\n"
    "v_pk_mul_f32 v[80:81], v[80:81], v[250:251]\n"
    "v_pk_mul_f32 v[82:83], v[82:83], v[254:255]\n"
    "v_exp_f32 v76, v76\n"
    "v_exp_f32 v77, v77\n"
    "v_exp_f32 v78, v78\n"
    "v_exp_f32 v79, v79\n"
    "v_exp_f32 v80, v80\n"
    "v_exp_f32 v81, v81\n"
    "v_exp_f32 v82, v82\n"
    "v_exp_f32 v83, v83\n"
    "v_pk_fma_f32 v[76:77], v[76:77], v[74:75], v[74:75]\n"
    "v_pk_fma_f32 v[78:79], v[78:79], v[74:75], v[74:75]\n"
    "v_pk_fma_f32 v[80:81], v[80:81], v[74:75], v[74:75]\n"
    "v_pk_fma_f32 v[82:83], v[82:83], v[74:75], v[74:75]\n"
    "v_rcp_f32 v76, v76\n"
    "v_rcp_f32 v77, v77\n"
    "v_rcp_f32 v78, v78\n"
    "v_rcp_f32 v79, v79\n"
    "v_rcp_f32 v80, v80\n"
    "v_rcp_f32 v81, v81\n"
    "v_rcp_f32 v82, v82\n"
    "v_rcp_f32 v83, v83\n"
    "v_pk_fma_f32 v[84:85], v[242:243], v[76:77], v[84:85]\n"
    "v_pk_fma_f32 v[86:87], v[246:247], v[78:79], v[86:87]\n"
    "v_pk_fma_f32 v[88:89], v[250:251], v[80:81], v[88:89]\n"
    "v_pk_fma_f32 v[90:91], v[254:255], v[82:83], v[90:91]\n"
    "s_cmp_eq_u32 s21, 0\n"
    "s_cbranch_scc1 .Lxd_gb0\n"
    "s_barrier\n"
    "s_setprio 3\n"
    ".Lxd_gb0:\n"
    "v_add_f32 v84, v84, v85\n"
    "v_add_f32 v86, v86, v87\n"
    "v_add_f32 v88, v88, v89\n"
    "v_add_f32 v90, v90, v91\n"
    "s_waitcnt lgkmcnt(15)\n"
    "v_pk_mul_f16 v60, v44, s45 op_sel:[0,1]\n"
    "v_pk_mul_f16 v61, v45, s45 op_sel:[0,1]\n"
    "v_permlane32_swap_b32 v84, v88\n"
    "v_permlane32_swap_b32 v86, v90\n"
    "v_add_f32 v84, v84, v88\n"
    "v_add_f32 v86, v86, v90\n"
    "v_pk_mul_f16 v62, v46, s45 op_sel:[0,1]\n"
    "v_pk_mul_f16 v63, v47, s45 op_sel:[0,1]\n"
    "v_permlane16_swap_b32 v84, v86\n"
    "v_add_f32 v84, v84, v86\n"
    "v_and_b32 v84, -2, v84\n"
    "ds_wrxchg_rtn_b32 v85, v11, v84\n"
    "ds_read_b128 v[224:227], v42 offset:33792\n"
    "ds_read_b128 v[228:231], v42 offset:34816\n"
    "ds_read_b128 v[232:235], v42 offset:35840\n"
    "ds_read_b128 v[236:239], v42 offset:36864\n"
    "ds_read_b128 v[240:243], v42 offset:37888\n"
    "ds_read_b128 v[244:247], v42 offset:38912\n"
    "ds_read_b128 v[248:251], v42 offset:39936\n"
    "ds_read_b128 v[252:255], v42 offset:40960\n"
    "v_pk_mul_f16 v64, v44, v46\n"
    "v_pk_mul_f16 v65, v45, v47\n"
    "v_pk_mul_f16 v66, v48, v48 op_sel_hi:[0,1]\n"
    "v_pk_mul_f16 v67, v48, v49 op_sel_hi:[0,1]\n"
    "v_lshlrev_b32 v86, 3, v1\n"
    "v_mov_b64 v[88:89], 0\n"
    "v_mov_b64 v[90:91], 0\n"
    "s_waitcnt lgkmcnt(8)\n"
    "v_cmp_ne_u32 vcc, -1, v85\n"
    "v_add_f32 v84, v84, v85\n"
    "v_add_u32 v84, 0x80, v84\n"
    "v_and_b32 v84, 0xffffff00, v84\n"
    "v_or_b32 v26, s28, v84\n"
    "v_mov_b32 v27, s29\n"
    "s_cbranch_vccz .Lxd_nost\n"
    "s_mov_b64 exec, vcc\n"
    "global_store_dwordx2 v86, v[26:27], s[48:49] sc1\n"
    "s_mov_b64 exec, -1\n"
    ".Lxd_nost:\n"
    "s_cmp_lg_u32 s21, 0\n"
    "s_cbranch_scc1 .Lxd_gb1\n"
    "s_barrier\n"
    ".Lxd_gb1:\n"
    ".Lxd_enter:\n"
    "s_cmp_eq_u32 s21, 0\n"
    "s_cselect_b64 vcc, 0, -1\n"
    "s_cbranch_scc1 .Lxd_pr\n"
    "s_setprio 1\n"
    ".Lxd_pr:\n"
    "s_cmp_lg_u32 s36, 0\n"
    "s_cbranch_scc1 .Lxd_el\n"
    "s_setpc_b64 s[10:11]\n"
    ".Lxd_el:\n"
    "s_cmp_eq_u32 s22, 0\n"
    "s_cbranch_scc1 .Lxd_pl1\n"
    "s_branch .Lxd_pl2\n"
    ".Lxd_ph0:\n"
    "v_smfmac_f32_16x16x64_f16 v[88:91], v[112:115], v[60:67], v2\n"
    "s_cbranch_vccnz .Lxd_pr15\n"
    "s_setprio 2\n"
    ".Lxd_pr15:\n"
    "v_pk_mul_f16 v68, v58, v58 op_sel_hi:[0,1]\n"
    "v_pk_mul_f16 v69, v58, v49 op_sel_hi:[0,1]\n"
    "v_mfma_f32_16x16x32_f16 v[84:87], v[108:111], v[64:67], 0\n"
    "v_fmac_f32 v11, v76, v80\n"
    "v_fmac_f32 v11, v77, v81\n"
    "v_mfma_f32_16x16x32_f16 v[84:87], v[96:99], v[60:63], v[84:87]\n"
    "v_fmac_f32 v11, v78, v82\n"
    "v_fma_f32 v10, v79, v83, v11\n"
    "v_mfma_f32_16x16x32_f16 v[80:83], v[92:95], v[60:63], 0\n"
    "s_waitcnt lgkmcnt(0)\n"
    "v_pk_mul_f16 v70, v48, v58 op_sel_hi:[0,1]\n"
    "v_pk_mul_f16 v71, v48, v59 op_sel_hi:[0,1]\n"
    "v_mfma_f32_16x16x32_f16 v[80:83], v[104:107], v[64:67], v[80:83]\n"
    "v_pk_mul_f16 v72, v50, v58 op_sel_hi:[0,1]\n"
    "v_pk_mul_f16 v73, v50, v59 op_sel_hi:[0,1]\n"
    "v_mfma_f32_16x16x32_f16 v[76:79], v[100:103], v[64:67], 0\n"
    "s_waitcnt vmcnt(0)\n"
    "v_pk_mul_f16 v74, v52, v58 op_sel_hi:[0,1]\n"
    "v_pk_mul_f16 v75, v52, v59 op_sel_hi:[0,1]\n"
    "v_mfma_f32_16x16x32_f16 v[76:79], v[116:119], v[68:71], v[76:79]\n"
    "v_pk_mul_f16 v60, v54, v58 op_sel_hi:[0,1]\n"
    "v_pk_mul_f16 v61, v54, v59 op_sel_hi:[0,1]\n"
    "v_mfma_f32_16x16x32_f16 v[76:79], v[128:131], v[72:75], v[76:79]\n"
    "v_pk_mul_f16 v62, v56, v58 op_sel_hi:[0,1]\n"
    "v_readfirstlane_b32 s40, v24\n"
    "v_mfma_f32_16x16x32_f16 v[80:83], v[132:135], v[72:75], v[80:83]\n"
    "s_or_b32 s40, s40, s44\n"
    "v_pk_mul_f16 v63, v56, v59 op_sel_hi:[0,1]\n"
    "v_permlane32_swap_b32 v7, v8\n"
    "v_mfma_f32_16x16x32_f16 v[80:83], v[120:123], v[68:71], v[80:83]\n"
    "s_lshl1_add_u32 s41, s40, s54\n"
    "v_pk_mul_f16 v64, v56, v56 op_sel_hi:[0,1]\n"
    "v_permlane32_swap_b32 v9, v10\n"
    "v_mfma_f32_16x16x32_f16 v[84:87], v[124:127], v[68:71], v[84:87]\n"
    "s_cmpk_lt_u32 s40, 256\n"
    "v_pk_mul_f16 v65, v56, v49 op_sel_hi:[0,1]\n"
    "v_mul_f32 v7, v0, v7\n"
    "v_mfma_f32_16x16x32_f16 v[84:87], v[136:139], v[72:75], v[84:87]\n"
    "s_cselect_b32 s41, s41, 2\n"
    "v_pk_mul_f16 v66, v48, v56 op_sel_hi:[0,1]\n"
    "v_mov_b32 v12, s41\n"
    "v_smfmac_f32_16x16x64_f16 v[88:91], v[140:143], v[68:75], v2\n"
    "s_cbranch_scc0 .Lxd_tf17\n"
    ".Lxd_tf17r:\n"
    "v_pk_mul_f16 v67, v48, v57 op_sel_hi:[0,1]\n"
    "v_mul_f32 v9, v0, v9\n"
    "v_mfma_f32_16x16x32_f16 v[76:79], v[144:147], v[60:63], v[76:79]\n"
    "s_mul_i32 s41, s41, 52\n"
    "v_pk_mul_f16 v68, v50, v56 op_sel_hi:[0,1]\n"
    "v_pk_mul_f16 v69, v50, v57 op_sel_hi:[0,1]\n"
    "v_mfma_f32_16x16x32_f16 v[76:79], v[156:159], v[64:67], v[76:79]\n"
    "s_add_u32 s38, s4, s41\n"
    "v_pk_mul_f16 v70, v52, v56 op_sel_hi:[0,1]\n"
    "v_fmac_f32 v7, v23, v8\n"
    "v_mfma_f32_16x16x32_f16 v[80:83], v[160:163], v[64:67], v[80:83]\n"
    "s_addc_u32 s39, s5, 0\n"
    "v_pk_mul_f16 v71, v52, v57 op_sel_hi:[0,1]\n"
    "v_fmac_f32 v9, v23, v10\n"
    "v_mfma_f32_16x16x32_f16 v[80:83], v[148:151], v[60:63], v[80:83]\n"
    "ds_read_b32 v22, v25 offset:516\n"
    "v_pk_mul_f16 v72, v54, v56 op_sel_hi:[0,1]\n"
    "v_permlane16_swap_b32 v7, v9\n"
    "v_mfma_f32_16x16x32_f16 v[84:87], v[152:155], v[60:63], v[84:87]\n"
    "global_load_dwordx3 v[36:38], v17, s[38:39] offset:-104\n"
    "v_pk_mul_f16 v73, v54, v57 op_sel_hi:[0,1]\n"
    "v_add_f32 v7, v7, v9\n"
    "v_mfma_f32_16x16x32_f16 v[84:87], v[164:167], v[64:67], v[84:87]\n"
    "global_load_dword v41, v18, s[38:39] offset:-8\n"
    "v_pk_mul_f16 v74, v54, v54 op_sel_hi:[0,1]\n"
    "v_add_f32_dpp v7, v7, v7 quad_perm:[1,0,3,2] row_mask:0xf bank_mask:0xf\n"
    "v_smfmac_f32_16x16x64_f16 v[88:91], v[168:171], v[60:67], v2\n"
    "ds_write_b32 v25, v12 offset:20\n"
    "v_pk_mul_f16 v75, v54, v49 op_sel_hi:[0,1]\n"
    "v_add_f32_dpp v7, v7, v7 quad_perm:[2,3,0,1] row_mask:0xf bank_mask:0xf\n"
    "v_mfma_f32_16x16x32_f16 v[76:79], v[172:175], v[68:71], v[76:79]\n"
    "v_pk_mul_f16 v60, v48, v54 op_sel_hi:[0,1]\n"
    "v_pk_mul_f16 v61, v48, v55 op_sel_hi:[0,1]\n"
    "v_mfma_f32_16x16x32_f16 v[76:79], v[184:187], v[72:75], v[76:79]\n"
    "s_waitcnt lgkmcnt(1)\n"
    "v_pk_mul_f16 v62, v50, v54 op_sel_hi:[0,1]\n"
    "v_readfirstlane_b32 s45, v22\n"
    "v_mfma_f32_16x16x32_f16 v[80:83], v[188:191], v[72:75], v[80:83]\n"
    "s_cmp_lt_i32 s45, 16\n"
    "v_pk_mul_f16 v63, v50, v55 op_sel_hi:[0,1]\n"
    "v_add_f32_dpp v7, v7, v7 row_half_mirror row_mask:0xf bank_mask:0xf\n"
    "v_mfma_f32_16x16x32_f16 v[80:83], v[176:179], v[68:71], v[80:83]\n"
    "s_cbranch_scc1 .Lxd_ck18\n"
    ".Lxd_ck18r:\n"
    "v_pk_mul_f16 v64, v52, v54 op_sel_hi:[0,1]\n"
    "v_add_f32_dpp v7, v7, v7 row_mirror row_mask:0xf bank_mask:0xf\n"
    "v_mfma_f32_16x16x32_f16 v[84:87], v[180:183], v[68:71], v[84:87]\n"
    "ds_read_b64_tr_b16 v[44:45], v5 offset:12288\n"
    "v_pk_mul_f16 v65, v52, v55 op_sel_hi:[0,1]\n"
    "v_mfma_f32_16x16x32_f16 v[84:87], v[192:195], v[72:75], v[84:87]\n"
    "ds_read_b64_tr_b16 v[46:47], v6 offset:12288\n"
    "v_pk_mul_f16 v66, v52, v52 op_sel_hi:[0,1]\n"
    "v_smfmac_f32_16x16x64_f16 v[88:91], v[196:199], v[68:75], v2\n"
    "ds_write_b32 v14, v7\n"
    "v_pk_mul_f16 v67, v52, v49 op_sel_hi:[0,1]\n"
    "v_add_u32 v14, 64, v14\n"
    "v_mfma_f32_16x16x32_f16 v[76:79], v[200:203], v[60:63], v[76:79]\n"
    "v_pk_mul_f16 v68, v48, v52 op_sel_hi:[0,1]\n"
    "v_pk_mul_f16 v69, v48, v53 op_sel_hi:[0,1]\n"
    "v_mfma_f32_16x16x32_f16 v[76:79], v[212:215], v[64:67], v[76:79]\n"
    "v_pk_mul_f16 v70, v50, v52 op_sel_hi:[0,1]\n"
    "v_mfma_f32_16x16x32_f16 v[80:83], v[216:219], v[64:67], v[80:83]\n"
    "s_cbranch_vccnz .Lxd_pr16\n"
    "s_setprio 0\n"
    ".Lxd_pr16:\n"
    "v_pk_mul_f16 v71, v50, v53 op_sel_hi:[0,1]\n"
    "v_mfma_f32_16x16x32_f16 v[80:83], v[204:207], v[60:63], v[80:83]\n"
    "v_pk_mul_f16 v72, v50, v50 op_sel_hi:[0,1]\n"
    "v_mfma_f32_16x16x32_f16 v[84:87], v[208:211], v[60:63], v[84:87]\n"
    "v_pk_mul_f16 v73, v50, v49 op_sel_hi:[0,1]\n"
    "v_pk_mul_f16 v74, v48, v50 op_sel_hi:[0,1]\n"
    "v_mfma_f32_16x16x32_f16 v[84:87], v[220:223], v[64:67], v[84:87]\n"
    "v_pk_mul_f16 v75, v48, v51 op_sel_hi:[0,1]\n"
    "v_smfmac_f32_16x16x64_f16 v[88:91], v[224:227], v[60:67], v2\n"
    "ds_read_b64_tr_b16 v[48:49], v3 offset:12288\n"
    "ds_read_b64_tr_b16 v[58:59], v4 offset:15168\n"
    "v_smfmac_f32_16x16x64_f16 v[88:91], v[252:255], v[68:75], v2\n"
    "s_waitcnt lgkmcnt(3)\n"
    "v_pk_mul_f16 v60, v44, s45 op_sel:[0,1]\n"
    "v_pk_mul_f16 v61, v45, s45 op_sel:[0,1]\n"
    "v_mfma_f32_16x16x32_f16 v[84:87], v[236:239], v[68:71], v[84:87]\n"
    "ds_read_b64_tr_b16 v[50:51], v4 offset:12864\n"
    "v_pk_mul_f16 v62, v46, s45 op_sel:[0,1]\n"
    "v_pk_mul_f16 v63, v47, s45 op_sel:[0,1]\n"
    "v_mfma_f32_16x16x32_f16 v[84:87], v[248:251], v[72:75], v[84:87]\n"
    "ds_read_b64_tr_b16 v[52:53], v4 offset:13440\n"
    "v_pk_mul_f16 v64, v44, v46\n"
    "v_pk_mul_f16 v65, v45, v47\n"
    "v_mfma_f32_16x16x32_f16 v[76:79], v[240:243], v[72:75], v[76:79]\n"
    "ds_read_b64_tr_b16 v[54:55], v4 offset:14016\n"
    "v_add_f32 v11, v88, v89\n"
    "v_add_f32 v13, v90, v91\n"
    "v_mfma_f32_16x16x32_f16 v[76:79], v[228:231], v[68:71], v[76:79]\n"
    "ds_read_b64_tr_b16 v[56:57], v4 offset:14592\n"
    "v_mov_b64 v[88:89], 0\n"
    "v_mov_b64 v[90:91], 0\n"
    "v_mfma_f32_16x16x32_f16 v[80:83], v[232:235], v[68:71], v[80:83]\n"
    "s_waitcnt lgkmcnt(4)\n"
    "v_pk_mul_f16 v66, v48, v48 op_sel_hi:[0,1]\n"
    "v_pk_mul_f16 v67, v48, v49 op_sel_hi:[0,1]\n"
    "v_mfma_f32_16x16x32_f16 v[80:83], v[244:247], v[72:75], v[80:83]\n"
    "s_cmp_lg_u32 s36, 0\n"
    "v_mul_f32 v11, v11, v13\n"
    "v_fmac_f32 v11, v84, v85\n"
    "v_fmac_f32 v11, v86, v87\n"
    "s_cbranch_scc1 .Lxd_pl1\n"
    ".Lxd_ph1:\n"
    "v_smfmac_f32_16x16x64_f16 v[88:91], v[112:115], v[60:67], v2\n"
    "s_cbranch_vccnz .Lxd_pr19\n"
    "s_setprio 2\n"
    ".Lxd_pr19:\n"
    "v_pk_mul_f16 v68, v58, v58 op_sel_hi:[0,1]\n"
    "v_pk_mul_f16 v69, v58, v49 op_sel_hi:[0,1]\n"
    "v_mfma_f32_16x16x32_f16 v[84:87], v[108:111], v[64:67], 0\n"
    "v_fmac_f32 v11, v76, v80\n"
    "v_fmac_f32 v11, v77, v81\n"
    "v_mfma_f32_16x16x32_f16 v[84:87], v[96:99], v[60:63], v[84:87]\n"
    "v_fmac_f32 v11, v78, v82\n"
    "v_fma_f32 v7, v79, v83, v11\n"
    "v_mfma_f32_16x16x32_f16 v[80:83], v[92:95], v[60:63], 0\n"
    "s_waitcnt lgkmcnt(0)\n"
    "v_pk_mul_f16 v70, v48, v58 op_sel_hi:[0,1]\n"
    "v_pk_mul_f16 v71, v48, v59 op_sel_hi:[0,1]\n"
    "v_mfma_f32_16x16x32_f16 v[80:83], v[104:107], v[64:67], v[80:83]\n"
    "v_pk_mul_f16 v72, v50, v58 op_sel_hi:[0,1]\n"
    "v_pk_mul_f16 v73, v50, v59 op_sel_hi:[0,1]\n"
    "v_mfma_f32_16x16x32_f16 v[76:79], v[100:103], v[64:67], 0\n"
    "v_pk_mul_f16 v74, v52, v58 op_sel_hi:[0,1]\n"
    "v_pk_mul_f16 v75, v52, v59 op_sel_hi:[0,1]\n"
    "v_mfma_f32_16x16x32_f16 v[76:79], v[116:119], v[68:71], v[76:79]\n"
    "s_waitcnt vmcnt(0)\n"
    "v_pk_mul_f16 v60, v54, v58 op_sel_hi:[0,1]\n"
    "v_pk_mul_f16 v61, v54, v59 op_sel_hi:[0,1]\n"
    "v_mfma_f32_16x16x32_f16 v[76:79], v[128:131], v[72:75], v[76:79]\n"
    "v_pk_mul_f16 v62, v56, v58 op_sel_hi:[0,1]\n"
    "v_lshl_or_b32 v36, v36, 7, v19\n"
    "v_mfma_f32_16x16x32_f16 v[80:83], v[132:135], v[72:75], v[80:83]\n"
    "global_load_dwordx4 v[28:31], v36, s[8:9]\n"
    "v_pk_mul_f16 v63, v56, v59 op_sel_hi:[0,1]\n"
    "v_lshl_or_b32 v37, v37, 7, v19\n"
    "v_mfma_f32_16x16x32_f16 v[80:83], v[120:123], v[68:71], v[80:83]\n"
    "global_load_dwordx4 v[32:35], v37, s[8:9]\n"
    "v_pk_mul_f16 v64, v56, v56 op_sel_hi:[0,1]\n"
    "v_lshl_or_b32 v41, v41, 7, v20\n"
    "v_mfma_f32_16x16x32_f16 v[84:87], v[124:127], v[68:71], v[84:87]\n"
    "global_load_dword v40, v41, s[8:9]\n"
    "v_pk_mul_f16 v65, v56, v49 op_sel_hi:[0,1]\n"
    "v_lshl_or_b32 v38, v38, 7, v19\n"
    "v_mfma_f32_16x16x32_f16 v[84:87], v[136:139], v[72:75], v[84:87]\n"
    "global_load_dwordx4 v[36:39], v38, s[8:9]\n"
    "v_pk_mul_f16 v66, v48, v56 op_sel_hi:[0,1]\n"
    "v_xor_b32 v15, 0x4000, v15\n"
    "v_smfmac_f32_16x16x64_f16 v[88:91], v[140:143], v[68:75], v2\n"
    "v_pk_mul_f16 v67, v48, v57 op_sel_hi:[0,1]\n"
    "v_xor_b32 v16, 0x4000, v16\n"
    "v_mfma_f32_16x16x32_f16 v[76:79], v[144:147], v[60:63], v[76:79]\n"
    "v_pk_mul_f16 v68, v50, v56 op_sel_hi:[0,1]\n"
    "v_pk_mul_f16 v69, v50, v57 op_sel_hi:[0,1]\n"
    "v_mfma_f32_16x16x32_f16 v[76:79], v[156:159], v[64:67], v[76:79]\n"
    "v_pk_mul_f16 v70, v52, v56 op_sel_hi:[0,1]\n"
    "v_xor_b32 v3, 0x4000, v3\n"
    "v_mfma_f32_16x16x32_f16 v[80:83], v[160:163], v[64:67], v[80:83]\n"
    "v_pk_mul_f16 v71, v52, v57 op_sel_hi:[0,1]\n"
    "v_xor_b32 v4, 0x4000, v4\n"
    "v_mfma_f32_16x16x32_f16 v[80:83], v[148:151], v[60:63], v[80:83]\n"
    "ds_read_b32 v22, v25 offset:520\n"
    "v_pk_mul_f16 v72, v54, v56 op_sel_hi:[0,1]\n"
    "v_xor_b32 v5, 0x4000, v5\n"
    "v_mfma_f32_16x16x32_f16 v[84:87], v[152:155], v[60:63], v[84:87]\n"
    "v_pk_mul_f16 v73, v54, v57 op_sel_hi:[0,1]\n"
    "v_xor_b32 v6, 0x4000, v6\n"
    "v_mfma_f32_16x16x32_f16 v[84:87], v[164:167], v[64:67], v[84:87]\n"
    "v_pk_mul_f16 v74, v54, v54 op_sel_hi:[0,1]\n"
    "v_smfmac_f32_16x16x64_f16 v[88:91], v[168:171], v[60:67], v2\n"
    "v_pk_mul_f16 v75, v54, v49 op_sel_hi:[0,1]\n"
    "v_mfma_f32_16x16x32_f16 v[76:79], v[172:175], v[68:71], v[76:79]\n"
    "v_pk_mul_f16 v60, v48, v54 op_sel_hi:[0,1]\n"
    "v_pk_mul_f16 v61, v48, v55 op_sel_hi:[0,1]\n"
    "v_mfma_f32_16x16x32_f16 v[76:79], v[184:187], v[72:75], v[76:79]\n"
    "s_waitcnt lgkmcnt(0)\n"
    "v_pk_mul_f16 v62, v50, v54 op_sel_hi:[0,1]\n"
    "v_readfirstlane_b32 s45, v22\n"
    "v_mfma_f32_16x16x32_f16 v[80:83], v[188:191], v[72:75], v[80:83]\n"
    "s_cmp_lt_i32 s45, 16\n"
    "v_pk_mul_f16 v63, v50, v55 op_sel_hi:[0,1]\n"
    "v_mfma_f32_16x16x32_f16 v[80:83], v[176:179], v[68:71], v[80:83]\n"
    "s_cbranch_scc1 .Lxd_ck21\n"
    ".Lxd_ck21r:\n"
    "v_pk_mul_f16 v64, v52, v54 op_sel_hi:[0,1]\n"
    "v_mfma_f32_16x16x32_f16 v[84:87], v[180:183], v[68:71], v[84:87]\n"
    "ds_read_b64_tr_b16 v[44:45], v5 offset:0\n"
    "v_pk_mul_f16 v65, v52, v55 op_sel_hi:[0,1]\n"
    "v_mfma_f32_16x16x32_f16 v[84:87], v[192:195], v[72:75], v[84:87]\n"
    "ds_read_b64_tr_b16 v[46:47], v6 offset:0\n"
    "v_pk_mul_f16 v66, v52, v52 op_sel_hi:[0,1]\n"
    "v_smfmac_f32_16x16x64_f16 v[88:91], v[196:199], v[68:75], v2\n"
    "v_pk_mul_f16 v67, v52, v49 op_sel_hi:[0,1]\n"
    "v_mfma_f32_16x16x32_f16 v[76:79], v[200:203], v[60:63], v[76:79]\n"
    "v_pk_mul_f16 v68, v48, v52 op_sel_hi:[0,1]\n"
    "v_pk_mul_f16 v69, v48, v53 op_sel_hi:[0,1]\n"
    "v_mfma_f32_16x16x32_f16 v[76:79], v[212:215], v[64:67], v[76:79]\n"
    "v_pk_mul_f16 v70, v50, v52 op_sel_hi:[0,1]\n"
    "v_mfma_f32_16x16x32_f16 v[80:83], v[216:219], v[64:67], v[80:83]\n"
    "s_cbranch_vccnz .Lxd_pr20\n"
    "s_setprio 0\n"
    ".Lxd_pr20:\n"
    "v_pk_mul_f16 v71, v50, v53 op_sel_hi:[0,1]\n"
    "v_mfma_f32_16x16x32_f16 v[80:83], v[204:207], v[60:63], v[80:83]\n"
    "v_pk_mul_f16 v72, v50, v50 op_sel_hi:[0,1]\n"
    "v_mfma_f32_16x16x32_f16 v[84:87], v[208:211], v[60:63], v[84:87]\n"
    "v_pk_mul_f16 v73, v50, v49 op_sel_hi:[0,1]\n"
    "v_pk_mul_f16 v74, v48, v50 op_sel_hi:[0,1]\n"
    "v_mfma_f32_16x16x32_f16 v[84:87], v[220:223], v[64:67], v[84:87]\n"
    "v_pk_mul_f16 v75, v48, v51 op_sel_hi:[0,1]\n"
    "v_smfmac_f32_16x16x64_f16 v[88:91], v[224:227], v[60:67], v2\n"
    "ds_read_b64_tr_b16 v[48:49], v3 offset:0\n"
    "ds_read_b64_tr_b16 v[58:59], v4 offset:2880\n"
    "v_smfmac_f32_16x16x64_f16 v[88:91], v[252:255], v[68:75], v2\n"
    "s_waitcnt lgkmcnt(2)\n"
    "v_pk_mul_f16 v60, v44, s45 op_sel:[0,1]\n"
    "v_pk_mul_f16 v61, v45, s45 op_sel:[0,1]\n"
    "v_mfma_f32_16x16x32_f16 v[84:87], v[236:239], v[68:71], v[84:87]\n"
    "ds_read_b64_tr_b16 v[50:51], v4 offset:576\n"
    "v_pk_mul_f16 v62, v46, s45 op_sel:[0,1]\n"
    "v_pk_mul_f16 v63, v47, s45 op_sel:[0,1]\n"
    "v_mfma_f32_16x16x32_f16 v[84:87], v[248:251], v[72:75], v[84:87]\n"
    "ds_read_b64_tr_b16 v[52:53], v4 offset:1152\n"
    "v_pk_mul_f16 v64, v44, v46\n"
    "v_pk_mul_f16 v65, v45, v47\n"
    "v_mfma_f32_16x16x32_f16 v[76:79], v[240:243], v[72:75], v[76:79]\n"
    "ds_read_b64_tr_b16 v[54:55], v4 offset:1728\n"
    "v_add_f32 v11, v88, v89\n"
    "v_add_f32 v13, v90, v91\n"
    "v_mfma_f32_16x16x32_f16 v[76:79], v[228:231], v[68:71], v[76:79]\n"
    "ds_read_b64_tr_b16 v[56:57], v4 offset:2304\n"
    "v_mov_b64 v[88:89], 0\n"
    "v_mov_b64 v[90:91], 0\n"
    "v_mfma_f32_16x16x32_f16 v[80:83], v[232:235], v[68:71], v[80:83]\n"
    "s_waitcnt lgkmcnt(4)\n"
    "v_pk_mul_f16 v66, v48, v48 op_sel_hi:[0,1]\n"
    "v_pk_mul_f16 v67, v48, v49 op_sel_hi:[0,1]\n"
    "v_mfma_f32_16x16x32_f16 v[80:83], v[244:247], v[72:75], v[80:83]\n"
    "v_mul_f32 v11, v11, v13\n"
    "v_fmac_f32 v11, v84, v85\n"
    "v_fmac_f32 v11, v86, v87\n"
    ".Lxd_ph2:\n"
    "v_smfmac_f32_16x16x64_f16 v[88:91], v[112:115], v[60:67], v2\n"
    "s_cbranch_vccnz .Lxd_pr22\n"
    "s_setprio 2\n"
    ".Lxd_pr22:\n"
    "v_pk_mul_f16 v68, v58, v58 op_sel_hi:[0,1]\n"
    "v_pk_mul_f16 v69, v58, v49 op_sel_hi:[0,1]\n"
    "v_mfma_f32_16x16x32_f16 v[84:87], v[108:111], v[64:67], 0\n"
    "v_fmac_f32 v11, v76, v80\n"
    "v_fmac_f32 v11, v77, v81\n"
    "v_mfma_f32_16x16x32_f16 v[84:87], v[96:99], v[60:63], v[84:87]\n"
    "v_fmac_f32 v11, v78, v82\n"
    "v_fma_f32 v8, v79, v83, v11\n"
    "v_mfma_f32_16x16x32_f16 v[80:83], v[92:95], v[60:63], 0\n"
    "s_waitcnt lgkmcnt(0)\n"
    "v_pk_mul_f16 v70, v48, v58 op_sel_hi:[0,1]\n"
    "v_pk_mul_f16 v71, v48, v59 op_sel_hi:[0,1]\n"
    "v_mfma_f32_16x16x32_f16 v[80:83], v[104:107], v[64:67], v[80:83]\n"
    "s_movk_i32 s43, 15744\n"
    "v_pk_mul_f16 v72, v50, v58 op_sel_hi:[0,1]\n"
    "v_pk_mul_f16 v73, v50, v59 op_sel_hi:[0,1]\n"
    "v_mfma_f32_16x16x32_f16 v[76:79], v[100:103], v[64:67], 0\n"
    "s_waitcnt vmcnt(0)\n"
    "v_pk_mul_f16 v74, v52, v58 op_sel_hi:[0,1]\n"
    "v_pk_mul_f16 v75, v52, v59 op_sel_hi:[0,1]\n"
    "v_mfma_f32_16x16x32_f16 v[76:79], v[116:119], v[68:71], v[76:79]\n"
    "v_pk_mul_f16 v60, v54, v58 op_sel_hi:[0,1]\n"
    "v_pk_mul_f16 v61, v54, v59 op_sel_hi:[0,1]\n"
    "v_mfma_f32_16x16x32_f16 v[76:79], v[128:131], v[72:75], v[76:79]\n"
    "v_pk_mul_f16 v62, v56, v58 op_sel_hi:[0,1]\n"
    "v_pk_maximum3_f16 v21, v28, v28, s43 op_sel_hi:[1,1,0] neg_lo:[0,1,0] neg_hi:[0,1,0]\n"
    "v_pk_maximum3_f16 v21, v29, v29, v21 neg_lo:[0,1,0] neg_hi:[0,1,0]\n"
    "v_mfma_f32_16x16x32_f16 v[80:83], v[132:135], v[72:75], v[80:83]\n"
    "v_pk_mul_f16 v63, v56, v59 op_sel_hi:[0,1]\n"
    "v_pk_maximum3_f16 v21, v30, v30, v21 neg_lo:[0,1,0] neg_hi:[0,1,0]\n"
    "v_mfma_f32_16x16x32_f16 v[80:83], v[120:123], v[68:71], v[80:83]\n"
    "v_pk_mul_f16 v64, v56, v56 op_sel_hi:[0,1]\n"
    "v_pk_maximum3_f16 v21, v31, v31, v21 neg_lo:[0,1,0] neg_hi:[0,1,0]\n"
    "v_mfma_f32_16x16x32_f16 v[84:87], v[124:127], v[68:71], v[84:87]\n"
    "v_pk_mul_f16 v65, v56, v49 op_sel_hi:[0,1]\n"
    "v_pk_maximum3_f16 v21, v32, v32, v21 neg_lo:[0,1,0] neg_hi:[0,1,0]\n"
    "v_mfma_f32_16x16x32_f16 v[84:87], v[136:139], v[72:75], v[84:87]\n"
    "s_cmp_lg_u32 s46, 0\n"
    "v_pk_mul_f16 v66, v48, v56 op_sel_hi:[0,1]\n"
    "v_pk_maximum3_f16 v21, v33, v33, v21 neg_lo:[0,1,0] neg_hi:[0,1,0]\n"
    "v_smfmac_f32_16x16x64_f16 v[88:91], v[140:143], v[68:75], v2\n"
    "s_cbranch_scc1 .Lxd_ht24\n"
    ".Lxd_ht24r:\n"
    "v_pk_mul_f16 v67, v48, v57 op_sel_hi:[0,1]\n"
    "v_pk_maximum3_f16 v21, v34, v34, v21 neg_lo:[0,1,0] neg_hi:[0,1,0]\n"
    "v_mfma_f32_16x16x32_f16 v[76:79], v[144:147], v[60:63], v[76:79]\n"
    "v_pk_mul_f16 v68, v50, v56 op_sel_hi:[0,1]\n"
    "v_pk_mul_f16 v69, v50, v57 op_sel_hi:[0,1]\n"
    "v_pk_maximum3_f16 v21, v35, v35, v21 neg_lo:[0,1,0] neg_hi:[0,1,0]\n"
    "v_mfma_f32_16x16x32_f16 v[76:79], v[156:159], v[64:67], v[76:79]\n"
    "v_pk_mul_f16 v70, v52, v56 op_sel_hi:[0,1]\n"
    "v_pk_maximum3_f16 v21, v36, v36, v21 neg_lo:[0,1,0] neg_hi:[0,1,0]\n"
    "v_mfma_f32_16x16x32_f16 v[80:83], v[160:163], v[64:67], v[80:83]\n"
    "v_pk_mul_f16 v71, v52, v57 op_sel_hi:[0,1]\n"
    "v_pk_maximum3_f16 v21, v37, v37, v21 neg_lo:[0,1,0] neg_hi:[0,1,0]\n"
    "v_mfma_f32_16x16x32_f16 v[80:83], v[148:151], v[60:63], v[80:83]\n"
    "ds_read_b32 v22, v25 offset:524\n"
    "v_pk_mul_f16 v72, v54, v56 op_sel_hi:[0,1]\n"
    "v_pk_maximum3_f16 v21, v38, v38, v21 neg_lo:[0,1,0] neg_hi:[0,1,0]\n"
    "v_mfma_f32_16x16x32_f16 v[84:87], v[152:155], v[60:63], v[84:87]\n"
    "v_pk_mul_f16 v73, v54, v57 op_sel_hi:[0,1]\n"
    "v_pk_maximum3_f16 v21, v39, v39, v21 neg_lo:[0,1,0] neg_hi:[0,1,0]\n"
    "v_mfma_f32_16x16x32_f16 v[84:87], v[164:167], v[64:67], v[84:87]\n"
    "v_pk_mul_f16 v74, v54, v54 op_sel_hi:[0,1]\n"
    "v_pk_maximum3_f16 v21, v40, v40, v21 neg_lo:[0,1,0] neg_hi:[0,1,0]\n"
    "v_smfmac_f32_16x16x64_f16 v[88:91], v[168:171], v[60:67], v2\n"
    "v_pk_mul_f16 v75, v54, v49 op_sel_hi:[0,1]\n"
    "v_pk_max_u16 v21, v21, v21 op_sel:[0,1] op_sel_hi:[1,0]\n"
    "v_mfma_f32_16x16x32_f16 v[76:79], v[172:175], v[68:71], v[76:79]\n"
    "v_pk_mul_f16 v60, v48, v54 op_sel_hi:[0,1]\n"
    "v_pk_mul_f16 v61, v48, v55 op_sel_hi:[0,1]\n"
    "v_mfma_f32_16x16x32_f16 v[76:79], v[184:187], v[72:75], v[76:79]\n"
    "s_waitcnt lgkmcnt(0)\n"
    "v_pk_mul_f16 v62, v50, v54 op_sel_hi:[0,1]\n"
    "v_readfirstlane_b32 s45, v22\n"
    "v_mfma_f32_16x16x32_f16 v[80:83], v[188:191], v[72:75], v[80:83]\n"
    "s_cmp_lt_i32 s45, 16\n"
    "v_pk_mul_f16 v63, v50, v55 op_sel_hi:[0,1]\n"
    "v_max_u32_dpp v21, v21, v21 quad_perm:[1,0,3,2] row_mask:0xf bank_mask:0xf\n"
    "v_mfma_f32_16x16x32_f16 v[80:83], v[176:179], v[68:71], v[80:83]\n"
    "s_cbranch_scc1 .Lxd_ck25\n"
    ".Lxd_ck25r:\n"
    "v_pk_mul_f16 v64, v52, v54 op_sel_hi:[0,1]\n"
    "v_max_u32_dpp v21, v21, v21 quad_perm:[2,3,0,1] row_mask:0xf bank_mask:0xf\n"
    "v_mfma_f32_16x16x32_f16 v[84:87], v[180:183], v[68:71], v[84:87]\n"
    "ds_read_b64_tr_b16 v[44:45], v5 offset:4096\n"
    "v_pk_mul_f16 v65, v52, v55 op_sel_hi:[0,1]\n"
    "v_max_u32_dpp v21, v21, v21 row_half_mirror row_mask:0xf bank_mask:0xf\n"
    "v_mfma_f32_16x16x32_f16 v[84:87], v[192:195], v[72:75], v[84:87]\n"
    "ds_read_b64_tr_b16 v[46:47], v6 offset:4096\n"
    "v_pk_mul_f16 v66, v52, v52 op_sel_hi:[0,1]\n"
    "v_max_u32_dpp v21, v21, v21 row_mirror row_mask:0xf bank_mask:0xf\n"
    "v_smfmac_f32_16x16x64_f16 v[88:91], v[196:199], v[68:75], v2\n"
    "v_pk_mul_f16 v67, v52, v49 op_sel_hi:[0,1]\n"
    "v_max_u32_dpp v21, v21, v21 row_bcast:15 row_mask:0xa bank_mask:0xf\n"
    "v_mfma_f32_16x16x32_f16 v[76:79], v[200:203], v[60:63], v[76:79]\n"
    "v_pk_mul_f16 v68, v48, v52 op_sel_hi:[0,1]\n"
    "v_pk_mul_f16 v69, v48, v53 op_sel_hi:[0,1]\n"
    "v_mfma_f32_16x16x32_f16 v[76:79], v[212:215], v[64:67], v[76:79]\n"
    "v_pk_mul_f16 v70, v50, v52 op_sel_hi:[0,1]\n"
    "v_max_u32_dpp v21, v21, v21 row_bcast:31 row_mask:0xc bank_mask:0xf\n"
    "v_mfma_f32_16x16x32_f16 v[80:83], v[216:219], v[64:67], v[80:83]\n"
    "s_cbranch_vccnz .Lxd_pr23\n"
    "s_setprio 0\n"
    ".Lxd_pr23:\n"
    "v_pk_mul_f16 v71, v50, v53 op_sel_hi:[0,1]\n"
    "v_readlane_b32 s47, v21, 63\n"
    "v_mfma_f32_16x16x32_f16 v[80:83], v[204:207], v[60:63], v[80:83]\n"
    "s_and_b32 s47, s47, 0x7f80\n"
    "v_pk_mul_f16 v72, v50, v50 op_sel_hi:[0,1]\n"
    "v_mfma_f32_16x16x32_f16 v[84:87], v[208:211], v[60:63], v[84:87]\n"
    "s_sub_u32 s50, s47, 2816\n"
    "v_pk_mul_f16 v73, v50, v49 op_sel_hi:[0,1]\n"
    "v_pk_mul_f16 v74, v48, v50 op_sel_hi:[0,1]\n"
    "v_pk_maximum3_f16 v21, v28, v28, s50 op_sel_hi:[1,1,0] neg_lo:[0,1,0] neg_hi:[0,1,0]\n"
    "v_mfma_f32_16x16x32_f16 v[84:87], v[220:223], v[64:67], v[84:87]\n"
    "s_mul_i32 s51, s50, -8\n"
    "v_pk_mul_f16 v75, v48, v51 op_sel_hi:[0,1]\n"
    "v_pk_mad_u16 v21, v21, 8, s51 op_sel_hi:[1,0,0]\n"
    "v_smfmac_f32_16x16x64_f16 v[88:91], v[224:227], v[60:67], v2\n"
    "ds_read_b64_tr_b16 v[48:49], v3 offset:4096\n"
    "ds_read_b64_tr_b16 v[58:59], v4 offset:6976\n"
    "v_bfi_b32 v28, s57, v21, v28\n"
    "v_smfmac_f32_16x16x64_f16 v[88:91], v[252:255], v[68:75], v2\n"
    "s_waitcnt lgkmcnt(2)\n"
    "v_pk_mul_f16 v60, v44, s45 op_sel:[0,1]\n"
    "v_pk_mul_f16 v61, v45, s45 op_sel:[0,1]\n"
    "v_mfma_f32_16x16x32_f16 v[84:87], v[236:239], v[68:71], v[84:87]\n"
    "ds_read_b64_tr_b16 v[50:51], v4 offset:4672\n"
    "v_pk_mul_f16 v62, v46, s45 op_sel:[0,1]\n"
    "v_pk_mul_f16 v63, v47, s45 op_sel:[0,1]\n"
    "v_mfma_f32_16x16x32_f16 v[84:87], v[248:251], v[72:75], v[84:87]\n"
    "ds_read_b64_tr_b16 v[52:53], v4 offset:5248\n"
    "v_pk_mul_f16 v64, v44, v46\n"
    "v_pk_mul_f16 v65, v45, v47\n"
    "v_mfma_f32_16x16x32_f16 v[76:79], v[240:243], v[72:75], v[76:79]\n"
    "ds_read_b64_tr_b16 v[54:55], v4 offset:5824\n"
    "v_add_f32 v11, v88, v89\n"
    "v_add_f32 v13, v90, v91\n"
    "v_mfma_f32_16x16x32_f16 v[76:79], v[228:231], v[68:71], v[76:79]\n"
    "ds_read_b64_tr_b16 v[56:57], v4 offset:6400\n"
    "v_mov_b64 v[88:89], 0\n"
    "v_mov_b64 v[90:91], 0\n"
    "v_mfma_f32_16x16x32_f16 v[80:83], v[232:235], v[68:71], v[80:83]\n"
    "s_waitcnt lgkmcnt(4)\n"
    "v_pk_mul_f16 v66, v48, v48 op_sel_hi:[0,1]\n"
    "v_pk_mul_f16 v67, v48, v49 op_sel_hi:[0,1]\n"
    "v_mfma_f32_16x16x32_f16 v[80:83], v[244:247], v[72:75], v[80:83]\n"
    "s_lshr_b32 s47, s47, 3\n"
    "v_mul_f32 v11, v11, v13\n"
    "v_fmac_f32 v11, v84, v85\n"
    "v_fmac_f32 v11, v86, v87\n"
    ".Lxd_ph3:\n"
    "v_smfmac_f32_16x16x64_f16 v[88:91], v[112:115], v[60:67], v2\n"
    "s_cbranch_vccnz .Lxd_pr26\n"
    "s_setprio 2\n"
    ".Lxd_pr26:\n"
    "v_pk_mul_f16 v68, v58, v58 op_sel_hi:[0,1]\n"
    "v_pk_mul_f16 v69, v58, v49 op_sel_hi:[0,1]\n"
    "v_mfma_f32_16x16x32_f16 v[84:87], v[108:111], v[64:67], 0\n"
    "s_cmp_lt_u32 s55, 32\n"
    "v_fmac_f32 v11, v76, v80\n"
    "v_fmac_f32 v11, v77, v81\n"
    "v_pk_maximum3_f16 v12, v29, v29, s50 op_sel_hi:[1,1,0] neg_lo:[0,1,0] neg_hi:[0,1,0]\n"
    "v_mfma_f32_16x16x32_f16 v[84:87], v[96:99], v[60:63], v[84:87]\n"
    "s_cselect_b32 s44, 0, -1\n"
    "v_fmac_f32 v11, v78, v82\n"
    "v_fma_f32 v9, v79, v83, v11\n"
    "v_mfma_f32_16x16x32_f16 v[80:83], v[92:95], v[60:63], 0\n"
    "s_waitcnt lgkmcnt(0)\n"
    "v_pk_mul_f16 v70, v48, v58 op_sel_hi:[0,1]\n"
    "v_pk_mul_f16 v71, v48, v59 op_sel_hi:[0,1]\n"
    "v_pk_mad_u16 v12, v12, 8, s51 op_sel_hi:[1,0,0]\n"
    "v_mfma_f32_16x16x32_f16 v[80:83], v[104:107], v[64:67], v[80:83]\n"
    "s_add_u32 s55, s55, 4\n"
    "v_pk_mul_f16 v72, v50, v58 op_sel_hi:[0,1]\n"
    "v_pk_mul_f16 v73, v50, v59 op_sel_hi:[0,1]\n"
    "v_mfma_f32_16x16x32_f16 v[76:79], v[100:103], v[64:67], 0\n"
    "v_pk_mul_f16 v74, v52, v58 op_sel_hi:[0,1]\n"
    "v_pk_mul_f16 v75, v52, v59 op_sel_hi:[0,1]\n"
    "v_bfi_b32 v29, s57, v12, v29\n"
    "v_mfma_f32_16x16x32_f16 v[76:79], v[116:119], v[68:71], v[76:79]\n"
    "v_pk_mul_f16 v60, v54, v58 op_sel_hi:[0,1]\n"
    "v_pk_mul_f16 v61, v54, v59 op_sel_hi:[0,1]\n"
    "v_pk_maximum3_f16 v21, v30, v30, s50 op_sel_hi:[1,1,0] neg_lo:[0,1,0] neg_hi:[0,1,0]\n"
    "v_mfma_f32_16x16x32_f16 v[76:79], v[128:131], v[72:75], v[76:79]\n"
    "v_pk_mul_f16 v62, v56, v58 op_sel_hi:[0,1]\n"
    "v_add_u32_e64 v24, s44, 1\n"
    "v_mfma_f32_16x16x32_f16 v[80:83], v[132:135], v[72:75], v[80:83]\n"
    "s_sub_u32 s66, 4112, s47\n"
    "v_pk_mul_f16 v63, v56, v59 op_sel_hi:[0,1]\n"
    "v_pk_mad_u16 v21, v21, 8, s51 op_sel_hi:[1,0,0]\n"
    "v_bfi_b32 v30, s57, v21, v30\n"
    "v_mfma_f32_16x16x32_f16 v[80:83], v[120:123], v[68:71], v[80:83]\n"
    "s_lshl_b32 s66, s66, 19\n"
    "v_pk_mul_f16 v64, v56, v56 op_sel_hi:[0,1]\n"
    "v_pk_maximum3_f16 v12, v31, v31, s50 op_sel_hi:[1,1,0] neg_lo:[0,1,0] neg_hi:[0,1,0]\n"
    "v_pk_mad_u16 v12, v12, 8, s51 op_sel_hi:[1,0,0]\n"
    "v_mfma_f32_16x16x32_f16 v[84:87], v[124:127], v[68:71], v[84:87]\n"
    "v_pk_mul_f16 v65, v56, v49 op_sel_hi:[0,1]\n"
    "v_bfi_b32 v31, s57, v12, v31\n"
    "v_mfma_f32_16x16x32_f16 v[84:87], v[136:139], v[72:75], v[84:87]\n"
    "ds_write_b128 v15, v[28:31] offset:0\n"
    "v_pk_mul_f16 v66, v48, v56 op_sel_hi:[0,1]\n"
    "v_pk_maximum3_f16 v21, v32, v32, s50 op_sel_hi:[1,1,0] neg_lo:[0,1,0] neg_hi:[0,1,0]\n"
    "v_pk_mad_u16 v21, v21, 8, s51 op_sel_hi:[1,0,0]\n"
    "v_smfmac_f32_16x16x64_f16 v[88:91], v[140:143], v[68:75], v2\n"
    "v_pk_mul_f16 v67, v48, v57 op_sel_hi:[0,1]\n"
    "v_bfi_b32 v32, s57, v21, v32\n"
    "v_mfma_f32_16x16x32_f16 v[76:79], v[144:147], v[60:63], v[76:79]\n"
    "v_pk_mul_f16 v68, v50, v56 op_sel_hi:[0,1]\n"
    "v_pk_mul_f16 v69, v50, v57 op_sel_hi:[0,1]\n"
    "v_pk_maximum3_f16 v12, v33, v33, s50 op_sel_hi:[1,1,0] neg_lo:[0,1,0] neg_hi:[0,1,0]\n"
    "v_mfma_f32_16x16x32_f16 v[76:79], v[156:159], v[64:67], v[76:79]\n"
    "v_pk_mul_f16 v70, v52, v56 op_sel_hi:[0,1]\n"
    "v_pk_mad_u16 v12, v12, 8, s51 op_sel_hi:[1,0,0]\n"
    "v_bfi_b32 v33, s57, v12, v33\n"
    "v_mfma_f32_16x16x32_f16 v[80:83], v[160:163], v[64:67], v[80:83]\n"
    "v_pk_mul_f16 v71, v52, v57 op_sel_hi:[0,1]\n"
    "v_pk_maximum3_f16 v21, v34, v34, s50 op_sel_hi:[1,1,0] neg_lo:[0,1,0] neg_hi:[0,1,0]\n"
    "v_mfma_f32_16x16x32_f16 v[80:83], v[148:151], v[60:63], v[80:83]\n"
    "ds_read_b32 v22, v25 offset:528\n"
    "v_pk_mul_f16 v72, v54, v56 op_sel_hi:[0,1]\n"
    "v_pk_mad_u16 v21, v21, 8, s51 op_sel_hi:[1,0,0]\n"
    "v_bfi_b32 v34, s57, v21, v34\n"
    "v_mfma_f32_16x16x32_f16 v[84:87], v[152:155], v[60:63], v[84:87]\n"
    "v_pk_mul_f16 v73, v54, v57 op_sel_hi:[0,1]\n"
    "v_pk_maximum3_f16 v12, v35, v35, s50 op_sel_hi:[1,1,0] neg_lo:[0,1,0] neg_hi:[0,1,0]\n"
    "v_pk_mad_u16 v12, v12, 8, s51 op_sel_hi:[1,0,0]\n"
    "v_mfma_f32_16x16x32_f16 v[84:87], v[164:167], v[64:67], v[84:87]\n"
    "v_pk_mul_f16 v74, v54, v54 op_sel_hi:[0,1]\n"
    "v_bfi_b32 v35, s57, v12, v35\n"
    "v_smfmac_f32_16x16x64_f16 v[88:91], v[168:171], v[60:67], v2\n"
    "ds_write_b128 v15, v[32:35] offset:144\n"
    "v_pk_mul_f16 v75, v54, v49 op_sel_hi:[0,1]\n"
    "v_pk_maximum3_f16 v21, v36, v36, s50 op_sel_hi:[1,1,0] neg_lo:[0,1,0] neg_hi:[0,1,0]\n"
    "v_pk_mad_u16 v21, v21, 8, s51 op_sel_hi:[1,0,0]\n"
    "v_mfma_f32_16x16x32_f16 v[76:79], v[172:175], v[68:71], v[76:79]\n"
    "v_pk_mul_f16 v60, v48, v54 op_sel_hi:[0,1]\n"
    "v_pk_mul_f16 v61, v48, v55 op_sel_hi:[0,1]\n"
    "v_mfma_f32_16x16x32_f16 v[76:79], v[184:187], v[72:75], v[76:79]\n"
    "s_waitcnt lgkmcnt(1)\n"
    "v_pk_mul_f16 v62, v50, v54 op_sel_hi:[0,1]\n"
    "v_readfirstlane_b32 s45, v22\n"
    "v_bfi_b32 v36, s57, v21, v36\n"
    "v_mfma_f32_16x16x32_f16 v[80:83], v[188:191], v[72:75], v[80:83]\n"
    "s_cmp_lt_i32 s45, 16\n"
    "v_pk_mul_f16 v63, v50, v55 op_sel_hi:[0,1]\n"
    "v_pk_maximum3_f16 v12, v37, v37, s50 op_sel_hi:[1,1,0] neg_lo:[0,1,0] neg_hi:[0,1,0]\n"
    "v_pk_mad_u16 v12, v12, 8, s51 op_sel_hi:[1,0,0]\n"
    "v_mfma_f32_16x16x32_f16 v[80:83], v[176:179], v[68:71], v[80:83]\n"
    "s_cbranch_scc1 .Lxd_ck29\n"
    ".Lxd_ck29r:\n"
    "v_pk_mul_f16 v64, v52, v54 op_sel_hi:[0,1]\n"
    "v_bfi_b32 v37, s57, v12, v37\n"
    "v_mfma_f32_16x16x32_f16 v[84:87], v[180:183], v[68:71], v[84:87]\n"
    "ds_read_b64_tr_b16 v[44:45], v5 offset:8192\n"
    "v_pk_mul_f16 v65, v52, v55 op_sel_hi:[0,1]\n"
    "v_pk_maximum3_f16 v21, v38, v38, s50 op_sel_hi:[1,1,0] neg_lo:[0,1,0] neg_hi:[0,1,0]\n"
    "v_pk_mad_u16 v21, v21, 8, s51 op_sel_hi:[1,0,0]\n"
    "v_mfma_f32_16x16x32_f16 v[84:87], v[192:195], v[72:75], v[84:87]\n"
    "ds_read_b64_tr_b16 v[46:47], v6 offset:8192\n"
    "v_pk_mul_f16 v66, v52, v52 op_sel_hi:[0,1]\n"
    "v_bfi_b32 v38, s57, v21, v38\n"
    "v_pk_maximum3_f16 v12, v39, v39, s50 op_sel_hi:[1,1,0] neg_lo:[0,1,0] neg_hi:[0,1,0]\n"
    "v_smfmac_f32_16x16x64_f16 v[88:91], v[196:199], v[68:75], v2\n"
    "v_pk_mul_f16 v67, v52, v49 op_sel_hi:[0,1]\n"
    "v_pk_mad_u16 v12, v12, 8, s51 op_sel_hi:[1,0,0]\n"
    "v_mfma_f32_16x16x32_f16 v[76:79], v[200:203], v[60:63], v[76:79]\n"
    "s_and_b32 s40, s45, 15\n"
    "v_pk_mul_f16 v68, v48, v52 op_sel_hi:[0,1]\n"
    "v_pk_mul_f16 v69, v48, v53 op_sel_hi:[0,1]\n"
    "v_bfi_b32 v39, s57, v12, v39\n"
    "v_mfma_f32_16x16x32_f16 v[76:79], v[212:215], v[64:67], v[76:79]\n"
    "s_cbranch_scc1 .Lxd_dr28\n"
    "s_mov_b64 exec, 1\n"
    "global_atomic_add v24, v17, v24, s[52:53] sc0 sc1\n"
    "s_mov_b64 exec, -1\n"
    ".Lxd_dr28r:\n"
    "v_pk_mul_f16 v70, v50, v52 op_sel_hi:[0,1]\n"
    "v_pk_maximum3_f16 v21, v40, v40, s50 op_sel_hi:[1,1,0] neg_lo:[0,1,0] neg_hi:[0,1,0]\n"
    "v_mfma_f32_16x16x32_f16 v[80:83], v[216:219], v[64:67], v[80:83]\n"
    "s_cbranch_vccnz .Lxd_pr27\n"
    "s_setprio 0\n"
    ".Lxd_pr27:\n"
    "v_pk_mul_f16 v71, v50, v53 op_sel_hi:[0,1]\n"
    "v_pk_mad_u16 v21, v21, 8, s51 op_sel_hi:[1,0,0]\n"
    "v_bfi_b32 v40, s57, v21, v40\n"
    "v_mfma_f32_16x16x32_f16 v[80:83], v[204:207], v[60:63], v[80:83]\n"
    "ds_write_b128 v15, v[36:39] offset:288\n"
    "v_pk_mul_f16 v72, v50, v50 op_sel_hi:[0,1]\n"
    "v_cvt_pkrtz_f16_f32 v12, s66, s66\n"
    "v_lshl_or_b32 v21, v12, 16, s47\n"
    "v_mfma_f32_16x16x32_f16 v[84:87], v[208:211], v[60:63], v[84:87]\n"
    "ds_write2_b32 v16, v40, v12 offset1:72\n"
    "v_pk_mul_f16 v73, v50, v49 op_sel_hi:[0,1]\n"
    "v_pk_mul_f16 v74, v48, v50 op_sel_hi:[0,1]\n"
    "v_mfma_f32_16x16x32_f16 v[84:87], v[220:223], v[64:67], v[84:87]\n"
    "ds_or_b32 v25, v21 offset:532\n"
    "v_pk_mul_f16 v75, v48, v51 op_sel_hi:[0,1]\n"
    "v_smfmac_f32_16x16x64_f16 v[88:91], v[224:227], v[60:67], v2\n"
    "ds_read_b64_tr_b16 v[48:49], v3 offset:8192\n"
    "ds_read_b64_tr_b16 v[58:59], v4 offset:11072\n"
    "v_add_u32 v25, 16, v25\n"
    "v_smfmac_f32_16x16x64_f16 v[88:91], v[252:255], v[68:75], v2\n"
    "s_waitcnt lgkmcnt(5)\n"
    "v_pk_mul_f16 v60, v44, s45 op_sel:[0,1]\n"
    "v_pk_mul_f16 v61, v45, s45 op_sel:[0,1]\n"
    "v_mfma_f32_16x16x32_f16 v[84:87], v[236:239], v[68:71], v[84:87]\n"
    "ds_read_b64_tr_b16 v[50:51], v4 offset:8768\n"
    "v_pk_mul_f16 v62, v46, s45 op_sel:[0,1]\n"
    "v_pk_mul_f16 v63, v47, s45 op_sel:[0,1]\n"
    "v_mfma_f32_16x16x32_f16 v[84:87], v[248:251], v[72:75], v[84:87]\n"
    "ds_read_b64_tr_b16 v[52:53], v4 offset:9344\n"
    "v_pk_mul_f16 v64, v44, v46\n"
    "v_pk_mul_f16 v65, v45, v47\n"
    "v_mfma_f32_16x16x32_f16 v[76:79], v[240:243], v[72:75], v[76:79]\n"
    "ds_read_b64_tr_b16 v[54:55], v4 offset:9920\n"
    "v_add_f32 v11, v88, v89\n"
    "v_add_f32 v13, v90, v91\n"
    "v_mfma_f32_16x16x32_f16 v[76:79], v[228:231], v[68:71], v[76:79]\n"
    "ds_read_b64_tr_b16 v[56:57], v4 offset:10496\n"
    "v_mov_b64 v[88:89], 0\n"
    "v_mov_b64 v[90:91], 0\n"
    "v_mfma_f32_16x16x32_f16 v[80:83], v[232:235], v[68:71], v[80:83]\n"
    "s_waitcnt lgkmcnt(4)\n"
    "v_pk_mul_f16 v66, v48, v48 op_sel_hi:[0,1]\n"
    "v_pk_mul_f16 v67, v48, v49 op_sel_hi:[0,1]\n"
    "v_mfma_f32_16x16x32_f16 v[80:83], v[244:247], v[72:75], v[80:83]\n"
    "s_cmp_eq_u32 s36, 0\n"
    "v_mul_f32 v11, v11, v13\n"
    "v_fmac_f32 v11, v84, v85\n"
    "v_fmac_f32 v11, v86, v87\n"
    "s_cbranch_scc1 .Lxd_ph0\n"
    ".Lxd_pl0:\n"
    "v_smfmac_f32_16x16x64_f16 v[88:91], v[112:115], v[60:67], v2\n"
    "s_cbranch_vccnz .Lxd_pr30\n"
    "s_setprio 2\n"
    ".Lxd_pr30:\n"
    "v_pk_mul_f16 v68, v58, v58 op_sel_hi:[0,1]\n"
    "v_pk_mul_f16 v69, v58, v49 op_sel_hi:[0,1]\n"
    "v_mfma_f32_16x16x32_f16 v[84:87], v[108:111], v[64:67], 0\n"
    "v_fmac_f32 v11, v76, v80\n"
    "v_fmac_f32 v11, v77, v81\n"
    "v_mfma_f32_16x16x32_f16 v[84:87], v[96:99], v[60:63], v[84:87]\n"
    "v_fmac_f32 v11, v78, v82\n"
    "v_fma_f32 v10, v79, v83, v11\n"
    "v_mfma_f32_16x16x32_f16 v[80:83], v[92:95], v[60:63], 0\n"
    "s_waitcnt lgkmcnt(0)\n"
    "v_pk_mul_f16 v70, v48, v58 op_sel_hi:[0,1]\n"
    "v_pk_mul_f16 v71, v48, v59 op_sel_hi:[0,1]\n"
    "v_mfma_f32_16x16x32_f16 v[80:83], v[104:107], v[64:67], v[80:83]\n"
    "v_pk_mul_f16 v72, v50, v58 op_sel_hi:[0,1]\n"
    "v_pk_mul_f16 v73, v50, v59 op_sel_hi:[0,1]\n"
    "v_mfma_f32_16x16x32_f16 v[76:79], v[100:103], v[64:67], 0\n"
    "v_pk_mul_f16 v74, v52, v58 op_sel_hi:[0,1]\n"
    "v_pk_mul_f16 v75, v52, v59 op_sel_hi:[0,1]\n"
    "v_mfma_f32_16x16x32_f16 v[76:79], v[116:119], v[68:71], v[76:79]\n"
    "v_pk_mul_f16 v60, v54, v58 op_sel_hi:[0,1]\n"
    "v_pk_mul_f16 v61, v54, v59 op_sel_hi:[0,1]\n"
    "v_mfma_f32_16x16x32_f16 v[76:79], v[128:131], v[72:75], v[76:79]\n"
    "v_pk_mul_f16 v62, v56, v58 op_sel_hi:[0,1]\n"
    "v_permlane32_swap_b32 v7, v8\n"
    "v_mfma_f32_16x16x32_f16 v[80:83], v[132:135], v[72:75], v[80:83]\n"
    "v_pk_mul_f16 v63, v56, v59 op_sel_hi:[0,1]\n"
    "v_permlane32_swap_b32 v9, v10\n"
    "v_mfma_f32_16x16x32_f16 v[80:83], v[120:123], v[68:71], v[80:83]\n"
    "v_pk_mul_f16 v64, v56, v56 op_sel_hi:[0,1]\n"
    "v_mul_f32 v7, v0, v7\n"
    "v_mfma_f32_16x16x32_f16 v[84:87], v[124:127], v[68:71], v[84:87]\n"
    "v_pk_mul_f16 v65, v56, v49 op_sel_hi:[0,1]\n"
    "v_mul_f32 v9, v0, v9\n"
    "v_mfma_f32_16x16x32_f16 v[84:87], v[136:139], v[72:75], v[84:87]\n"
    "v_pk_mul_f16 v66, v48, v56 op_sel_hi:[0,1]\n"
    "v_fmac_f32 v7, v23, v8\n"
    "v_smfmac_f32_16x16x64_f16 v[88:91], v[140:143], v[68:75], v2\n"
    "v_pk_mul_f16 v67, v48, v57 op_sel_hi:[0,1]\n"
    "v_fmac_f32 v9, v23, v10\n"
    "v_mfma_f32_16x16x32_f16 v[76:79], v[144:147], v[60:63], v[76:79]\n"
    "v_pk_mul_f16 v68, v50, v56 op_sel_hi:[0,1]\n"
    "v_pk_mul_f16 v69, v50, v57 op_sel_hi:[0,1]\n"
    "v_mfma_f32_16x16x32_f16 v[76:79], v[156:159], v[64:67], v[76:79]\n"
    "v_pk_mul_f16 v70, v52, v56 op_sel_hi:[0,1]\n"
    "v_permlane16_swap_b32 v7, v9\n"
    "v_mfma_f32_16x16x32_f16 v[80:83], v[160:163], v[64:67], v[80:83]\n"
    "v_pk_mul_f16 v71, v52, v57 op_sel_hi:[0,1]\n"
    "v_add_f32 v7, v7, v9\n"
    "v_mfma_f32_16x16x32_f16 v[80:83], v[148:151], v[60:63], v[80:83]\n"
    "ds_read_b32 v22, v25 offset:516\n"
    "v_pk_mul_f16 v72, v54, v56 op_sel_hi:[0,1]\n"
    "v_add_f32_dpp v7, v7, v7 quad_perm:[1,0,3,2] row_mask:0xf bank_mask:0xf\n"
    "v_mfma_f32_16x16x32_f16 v[84:87], v[152:155], v[60:63], v[84:87]\n"
    "v_pk_mul_f16 v73, v54, v57 op_sel_hi:[0,1]\n"
    "v_add_f32_dpp v7, v7, v7 quad_perm:[2,3,0,1] row_mask:0xf bank_mask:0xf\n"
    "v_mfma_f32_16x16x32_f16 v[84:87], v[164:167], v[64:67], v[84:87]\n"
    "v_pk_mul_f16 v74, v54, v54 op_sel_hi:[0,1]\n"
    "v_add_f32_dpp v7, v7, v7 row_half_mirror row_mask:0xf bank_mask:0xf\n"
    "v_smfmac_f32_16x16x64_f16 v[88:91], v[168:171], v[60:67], v2\n"
    "v_pk_mul_f16 v75, v54, v49 op_sel_hi:[0,1]\n"
    "v_add_f32_dpp v7, v7, v7 row_mirror row_mask:0xf bank_mask:0xf\n"
    "v_mfma_f32_16x16x32_f16 v[76:79], v[172:175], v[68:71], v[76:79]\n"
    "ds_write_b32 v14, v7\n"
    "v_pk_mul_f16 v60, v48, v54 op_sel_hi:[0,1]\n"
    "v_pk_mul_f16 v61, v48, v55 op_sel_hi:[0,1]\n"
    "v_mfma_f32_16x16x32_f16 v[76:79], v[184:187], v[72:75], v[76:79]\n"
    "s_waitcnt lgkmcnt(1)\n"
    "v_pk_mul_f16 v62, v50, v54 op_sel_hi:[0,1]\n"
    "v_readfirstlane_b32 s45, v22\n"
    "v_mfma_f32_16x16x32_f16 v[80:83], v[188:191], v[72:75], v[80:83]\n"
    "s_cmp_lt_i32 s45, 16\n"
    "v_pk_mul_f16 v63, v50, v55 op_sel_hi:[0,1]\n"
    "v_add_u32 v14, 64, v14\n"
    "v_mfma_f32_16x16x32_f16 v[80:83], v[176:179], v[68:71], v[80:83]\n"
    "s_cbranch_scc1 .Lxd_ck32\n"
    ".Lxd_ck32r:\n"
    "v_pk_mul_f16 v64, v52, v54 op_sel_hi:[0,1]\n"
    "v_mfma_f32_16x16x32_f16 v[84:87], v[180:183], v[68:71], v[84:87]\n"
    "ds_read_b64_tr_b16 v[44:45], v5 offset:12288\n"
    "v_pk_mul_f16 v65, v52, v55 op_sel_hi:[0,1]\n"
    "v_mfma_f32_16x16x32_f16 v[84:87], v[192:195], v[72:75], v[84:87]\n"
    "ds_read_b64_tr_b16 v[46:47], v6 offset:12288\n"
    "v_pk_mul_f16 v66, v52, v52 op_sel_hi:[0,1]\n"
    "v_smfmac_f32_16x16x64_f16 v[88:91], v[196:199], v[68:75], v2\n"
    "v_pk_mul_f16 v67, v52, v49 op_sel_hi:[0,1]\n"
    "v_mfma_f32_16x16x32_f16 v[76:79], v[200:203], v[60:63], v[76:79]\n"
    "v_pk_mul_f16 v68, v48, v52 op_sel_hi:[0,1]\n"
    "v_pk_mul_f16 v69, v48, v53 op_sel_hi:[0,1]\n"
    "v_mfma_f32_16x16x32_f16 v[76:79], v[212:215], v[64:67], v[76:79]\n"
    "v_pk_mul_f16 v70, v50, v52 op_sel_hi:[0,1]\n"
    "v_mfma_f32_16x16x32_f16 v[80:83], v[216:219], v[64:67], v[80:83]\n"
    "s_cbranch_vccnz .Lxd_pr31\n"
    "s_setprio 0\n"
    ".Lxd_pr31:\n"
    "v_pk_mul_f16 v71, v50, v53 op_sel_hi:[0,1]\n"
    "v_mfma_f32_16x16x32_f16 v[80:83], v[204:207], v[60:63], v[80:83]\n"
    "v_pk_mul_f16 v72, v50, v50 op_sel_hi:[0,1]\n"
    "v_mfma_f32_16x16x32_f16 v[84:87], v[208:211], v[60:63], v[84:87]\n"
    "v_pk_mul_f16 v73, v50, v49 op_sel_hi:[0,1]\n"
    "v_pk_mul_f16 v74, v48, v50 op_sel_hi:[0,1]\n"
    "v_mfma_f32_16x16x32_f16 v[84:87], v[220:223], v[64:67], v[84:87]\n"
    "v_pk_mul_f16 v75, v48, v51 op_sel_hi:[0,1]\n"
    "v_smfmac_f32_16x16x64_f16 v[88:91], v[224:227], v[60:67], v2\n"
    "ds_read_b64_tr_b16 v[48:49], v3 offset:12288\n"
    "ds_read_b64_tr_b16 v[58:59], v4 offset:15168\n"
    "v_smfmac_f32_16x16x64_f16 v[88:91], v[252:255], v[68:75], v2\n"
    "s_waitcnt lgkmcnt(2)\n"
    "v_pk_mul_f16 v60, v44, s45 op_sel:[0,1]\n"
    "v_pk_mul_f16 v61, v45, s45 op_sel:[0,1]\n"
    "v_mfma_f32_16x16x32_f16 v[84:87], v[236:239], v[68:71], v[84:87]\n"
    "ds_read_b64_tr_b16 v[50:51], v4 offset:12864\n"
    "v_pk_mul_f16 v62, v46, s45 op_sel:[0,1]\n"
    "v_pk_mul_f16 v63, v47, s45 op_sel:[0,1]\n"
    "v_mfma_f32_16x16x32_f16 v[84:87], v[248:251], v[72:75], v[84:87]\n"
    "ds_read_b64_tr_b16 v[52:53], v4 offset:13440\n"
    "v_pk_mul_f16 v64, v44, v46\n"
    "v_pk_mul_f16 v65, v45, v47\n"
    "v_mfma_f32_16x16x32_f16 v[76:79], v[240:243], v[72:75], v[76:79]\n"
    "ds_read_b64_tr_b16 v[54:55], v4 offset:14016\n"
    "v_add_f32 v11, v88, v89\n"
    "v_add_f32 v13, v90, v91\n"
    "v_mfma_f32_16x16x32_f16 v[76:79], v[228:231], v[68:71], v[76:79]\n"
    "ds_read_b64_tr_b16 v[56:57], v4 offset:14592\n"
    "v_mov_b64 v[88:89], 0\n"
    "v_mov_b64 v[90:91], 0\n"
    "v_mfma_f32_16x16x32_f16 v[80:83], v[232:235], v[68:71], v[80:83]\n"
    "s_waitcnt lgkmcnt(4)\n"
    "v_pk_mul_f16 v66, v48, v48 op_sel_hi:[0,1]\n"
    "v_pk_mul_f16 v67, v48, v49 op_sel_hi:[0,1]\n"
    "v_mfma_f32_16x16x32_f16 v[80:83], v[244:247], v[72:75], v[80:83]\n"
    "v_mul_f32 v11, v11, v13\n"
    "v_fmac_f32 v11, v84, v85\n"
    "v_fmac_f32 v11, v86, v87\n"
    ".Lxd_pl1:\n"
    "v_smfmac_f32_16x16x64_f16 v[88:91], v[112:115], v[60:67], v2\n"
    "s_cbranch_vccnz .Lxd_pr33\n"
    "s_setprio 2\n"
    ".Lxd_pr33:\n"
    "v_pk_mul_f16 v68, v58, v58 op_sel_hi:[0,1]\n"
    "v_pk_mul_f16 v69, v58, v49 op_sel_hi:[0,1]\n"
    "v_mfma_f32_16x16x32_f16 v[84:87], v[108:111], v[64:67], 0\n"
    "v_fmac_f32 v11, v76, v80\n"
    "v_fmac_f32 v11, v77, v81\n"
    "v_mfma_f32_16x16x32_f16 v[84:87], v[96:99], v[60:63], v[84:87]\n"
    "v_fmac_f32 v11, v78, v82\n"
    "v_fma_f32 v7, v79, v83, v11\n"
    "v_mfma_f32_16x16x32_f16 v[80:83], v[92:95], v[60:63], 0\n"
    "s_waitcnt lgkmcnt(0)\n"
    "v_pk_mul_f16 v70, v48, v58 op_sel_hi:[0,1]\n"
    "v_pk_mul_f16 v71, v48, v59 op_sel_hi:[0,1]\n"
    "v_mfma_f32_16x16x32_f16 v[80:83], v[104:107], v[64:67], v[80:83]\n"
    "v_pk_mul_f16 v72, v50, v58 op_sel_hi:[0,1]\n"
    "v_pk_mul_f16 v73, v50, v59 op_sel_hi:[0,1]\n"
    "v_mfma_f32_16x16x32_f16 v[76:79], v[100:103], v[64:67], 0\n"
    "v_pk_mul_f16 v74, v52, v58 op_sel_hi:[0,1]\n"
    "v_pk_mul_f16 v75, v52, v59 op_sel_hi:[0,1]\n"
    "v_mfma_f32_16x16x32_f16 v[76:79], v[116:119], v[68:71], v[76:79]\n"
    "v_pk_mul_f16 v60, v54, v58 op_sel_hi:[0,1]\n"
    "v_pk_mul_f16 v61, v54, v59 op_sel_hi:[0,1]\n"
    "v_mfma_f32_16x16x32_f16 v[76:79], v[128:131], v[72:75], v[76:79]\n"
    "v_pk_mul_f16 v62, v56, v58 op_sel_hi:[0,1]\n"
    "v_xor_b32 v3, 0x4000, v3\n"
    "v_mfma_f32_16x16x32_f16 v[80:83], v[132:135], v[72:75], v[80:83]\n"
    "v_pk_mul_f16 v63, v56, v59 op_sel_hi:[0,1]\n"
    "v_xor_b32 v4, 0x4000, v4\n"
    "v_mfma_f32_16x16x32_f16 v[80:83], v[120:123], v[68:71], v[80:83]\n"
    "v_pk_mul_f16 v64, v56, v56 op_sel_hi:[0,1]\n"
    "v_xor_b32 v5, 0x4000, v5\n"
    "v_mfma_f32_16x16x32_f16 v[84:87], v[124:127], v[68:71], v[84:87]\n"
    "v_pk_mul_f16 v65, v56, v49 op_sel_hi:[0,1]\n"
    "v_xor_b32 v6, 0x4000, v6\n"
    "v_mfma_f32_16x16x32_f16 v[84:87], v[136:139], v[72:75], v[84:87]\n"
    "v_pk_mul_f16 v66, v48, v56 op_sel_hi:[0,1]\n"
    "v_smfmac_f32_16x16x64_f16 v[88:91], v[140:143], v[68:75], v2\n"
    "v_pk_mul_f16 v67, v48, v57 op_sel_hi:[0,1]\n"
    "v_mfma_f32_16x16x32_f16 v[76:79], v[144:147], v[60:63], v[76:79]\n"
    "v_pk_mul_f16 v68, v50, v56 op_sel_hi:[0,1]\n"
    "v_pk_mul_f16 v69, v50, v57 op_sel_hi:[0,1]\n"
    "v_mfma_f32_16x16x32_f16 v[76:79], v[156:159], v[64:67], v[76:79]\n"
    "v_pk_mul_f16 v70, v52, v56 op_sel_hi:[0,1]\n"
    "v_mfma_f32_16x16x32_f16 v[80:83], v[160:163], v[64:67], v[80:83]\n"
    "v_pk_mul_f16 v71, v52, v57 op_sel_hi:[0,1]\n"
    "v_mfma_f32_16x16x32_f16 v[80:83], v[148:151], v[60:63], v[80:83]\n"
    "ds_read_b32 v22, v25 offset:520\n"
    "v_pk_mul_f16 v72, v54, v56 op_sel_hi:[0,1]\n"
    "v_mfma_f32_16x16x32_f16 v[84:87], v[152:155], v[60:63], v[84:87]\n"
    "v_pk_mul_f16 v73, v54, v57 op_sel_hi:[0,1]\n"
    "v_mfma_f32_16x16x32_f16 v[84:87], v[164:167], v[64:67], v[84:87]\n"
    "v_pk_mul_f16 v74, v54, v54 op_sel_hi:[0,1]\n"
    "v_smfmac_f32_16x16x64_f16 v[88:91], v[168:171], v[60:67], v2\n"
    "v_pk_mul_f16 v75, v54, v49 op_sel_hi:[0,1]\n"
    "v_mfma_f32_16x16x32_f16 v[76:79], v[172:175], v[68:71], v[76:79]\n"
    "v_pk_mul_f16 v60, v48, v54 op_sel_hi:[0,1]\n"
    "v_pk_mul_f16 v61, v48, v55 op_sel_hi:[0,1]\n"
    "v_mfma_f32_16x16x32_f16 v[76:79], v[184:187], v[72:75], v[76:79]\n"
    "s_waitcnt lgkmcnt(0)\n"
    "v_pk_mul_f16 v62, v50, v54 op_sel_hi:[0,1]\n"
    "v_readfirstlane_b32 s45, v22\n"
    "v_mfma_f32_16x16x32_f16 v[80:83], v[188:191], v[72:75], v[80:83]\n"
    "s_cmp_lt_i32 s45, 16\n"
    "v_pk_mul_f16 v63, v50, v55 op_sel_hi:[0,1]\n"
    "v_mfma_f32_16x16x32_f16 v[80:83], v[176:179], v[68:71], v[80:83]\n"
    "s_cbranch_scc1 .Lxd_ck35\n"
    ".Lxd_ck35r:\n"
    "v_pk_mul_f16 v64, v52, v54 op_sel_hi:[0,1]\n"
    "v_mfma_f32_16x16x32_f16 v[84:87], v[180:183], v[68:71], v[84:87]\n"
    "ds_read_b64_tr_b16 v[44:45], v5 offset:0\n"
    "v_pk_mul_f16 v65, v52, v55 op_sel_hi:[0,1]\n"
    "v_mfma_f32_16x16x32_f16 v[84:87], v[192:195], v[72:75], v[84:87]\n"
    "ds_read_b64_tr_b16 v[46:47], v6 offset:0\n"
    "v_pk_mul_f16 v66, v52, v52 op_sel_hi:[0,1]\n"
    "v_smfmac_f32_16x16x64_f16 v[88:91], v[196:199], v[68:75], v2\n"
    "v_pk_mul_f16 v67, v52, v49 op_sel_hi:[0,1]\n"
    "v_mfma_f32_16x16x32_f16 v[76:79], v[200:203], v[60:63], v[76:79]\n"
    "v_pk_mul_f16 v68, v48, v52 op_sel_hi:[0,1]\n"
    "v_pk_mul_f16 v69, v48, v53 op_sel_hi:[0,1]\n"
    "v_mfma_f32_16x16x32_f16 v[76:79], v[212:215], v[64:67], v[76:79]\n"
    "v_pk_mul_f16 v70, v50, v52 op_sel_hi:[0,1]\n"
    "v_mfma_f32_16x16x32_f16 v[80:83], v[216:219], v[64:67], v[80:83]\n"
    "s_cbranch_vccnz .Lxd_pr34\n"
    "s_setprio 0\n"
    ".Lxd_pr34:\n"
    "v_pk_mul_f16 v71, v50, v53 op_sel_hi:[0,1]\n"
    "v_mfma_f32_16x16x32_f16 v[80:83], v[204:207], v[60:63], v[80:83]\n"
    "v_pk_mul_f16 v72, v50, v50 op_sel_hi:[0,1]\n"
    "v_mfma_f32_16x16x32_f16 v[84:87], v[208:211], v[60:63], v[84:87]\n"
    "v_pk_mul_f16 v73, v50, v49 op_sel_hi:[0,1]\n"
    "v_pk_mul_f16 v74, v48, v50 op_sel_hi:[0,1]\n"
    "v_mfma_f32_16x16x32_f16 v[84:87], v[220:223], v[64:67], v[84:87]\n"
    "v_pk_mul_f16 v75, v48, v51 op_sel_hi:[0,1]\n"
    "v_smfmac_f32_16x16x64_f16 v[88:91], v[224:227], v[60:67], v2\n"
    "ds_read_b64_tr_b16 v[48:49], v3 offset:0\n"
    "ds_read_b64_tr_b16 v[58:59], v4 offset:2880\n"
    "v_smfmac_f32_16x16x64_f16 v[88:91], v[252:255], v[68:75], v2\n"
    "s_waitcnt lgkmcnt(2)\n"
    "v_pk_mul_f16 v60, v44, s45 op_sel:[0,1]\n"
    "v_pk_mul_f16 v61, v45, s45 op_sel:[0,1]\n"
    "v_mfma_f32_16x16x32_f16 v[84:87], v[236:239], v[68:71], v[84:87]\n"
    "ds_read_b64_tr_b16 v[50:51], v4 offset:576\n"
    "v_pk_mul_f16 v62, v46, s45 op_sel:[0,1]\n"
    "v_pk_mul_f16 v63, v47, s45 op_sel:[0,1]\n"
    "v_mfma_f32_16x16x32_f16 v[84:87], v[248:251], v[72:75], v[84:87]\n"
    "ds_read_b64_tr_b16 v[52:53], v4 offset:1152\n"
    "v_pk_mul_f16 v64, v44, v46\n"
    "v_pk_mul_f16 v65, v45, v47\n"
    "v_mfma_f32_16x16x32_f16 v[76:79], v[240:243], v[72:75], v[76:79]\n"
    "ds_read_b64_tr_b16 v[54:55], v4 offset:1728\n"
    "v_add_f32 v11, v88, v89\n"
    "v_add_f32 v13, v90, v91\n"
    "v_mfma_f32_16x16x32_f16 v[76:79], v[228:231], v[68:71], v[76:79]\n"
    "ds_read_b64_tr_b16 v[56:57], v4 offset:2304\n"
    "v_mov_b64 v[88:89], 0\n"
    "v_mov_b64 v[90:91], 0\n"
    "v_mfma_f32_16x16x32_f16 v[80:83], v[232:235], v[68:71], v[80:83]\n"
    "s_waitcnt lgkmcnt(4)\n"
    "v_pk_mul_f16 v66, v48, v48 op_sel_hi:[0,1]\n"
    "v_pk_mul_f16 v67, v48, v49 op_sel_hi:[0,1]\n"
    "v_mfma_f32_16x16x32_f16 v[80:83], v[244:247], v[72:75], v[80:83]\n"
    "v_mul_f32 v11, v11, v13\n"
    "v_fmac_f32 v11, v84, v85\n"
    "v_fmac_f32 v11, v86, v87\n"
    ".Lxd_pl2:\n"
    "v_smfmac_f32_16x16x64_f16 v[88:91], v[112:115], v[60:67], v2\n"
    "s_cbranch_vccnz .Lxd_pr36\n"
    "s_setprio 2\n"
    ".Lxd_pr36:\n"
    "v_pk_mul_f16 v68, v58, v58 op_sel_hi:[0,1]\n"
    "v_pk_mul_f16 v69, v58, v49 op_sel_hi:[0,1]\n"
    "v_mfma_f32_16x16x32_f16 v[84:87], v[108:111], v[64:67], 0\n"
    "v_fmac_f32 v11, v76, v80\n"
    "v_fmac_f32 v11, v77, v81\n"
    "v_mfma_f32_16x16x32_f16 v[84:87], v[96:99], v[60:63], v[84:87]\n"
    "v_fmac_f32 v11, v78, v82\n"
    "v_fma_f32 v8, v79, v83, v11\n"
    "v_mfma_f32_16x16x32_f16 v[80:83], v[92:95], v[60:63], 0\n"
    "s_waitcnt lgkmcnt(0)\n"
    "v_pk_mul_f16 v70, v48, v58 op_sel_hi:[0,1]\n"
    "v_pk_mul_f16 v71, v48, v59 op_sel_hi:[0,1]\n"
    "v_mfma_f32_16x16x32_f16 v[80:83], v[104:107], v[64:67], v[80:83]\n"
    "v_pk_mul_f16 v72, v50, v58 op_sel_hi:[0,1]\n"
    "v_pk_mul_f16 v73, v50, v59 op_sel_hi:[0,1]\n"
    "v_mfma_f32_16x16x32_f16 v[76:79], v[100:103], v[64:67], 0\n"
    "v_pk_mul_f16 v74, v52, v58 op_sel_hi:[0,1]\n"
    "v_pk_mul_f16 v75, v52, v59 op_sel_hi:[0,1]\n"
    "v_mfma_f32_16x16x32_f16 v[76:79], v[116:119], v[68:71], v[76:79]\n"
    "v_pk_mul_f16 v60, v54, v58 op_sel_hi:[0,1]\n"
    "v_pk_mul_f16 v61, v54, v59 op_sel_hi:[0,1]\n"
    "v_mfma_f32_16x16x32_f16 v[76:79], v[128:131], v[72:75], v[76:79]\n"
    "v_pk_mul_f16 v62, v56, v58 op_sel_hi:[0,1]\n"
    "v_mfma_f32_16x16x32_f16 v[80:83], v[132:135], v[72:75], v[80:83]\n"
    "v_pk_mul_f16 v63, v56, v59 op_sel_hi:[0,1]\n"
    "v_mfma_f32_16x16x32_f16 v[80:83], v[120:123], v[68:71], v[80:83]\n"
    "v_pk_mul_f16 v64, v56, v56 op_sel_hi:[0,1]\n"
    "v_mfma_f32_16x16x32_f16 v[84:87], v[124:127], v[68:71], v[84:87]\n"
    "v_pk_mul_f16 v65, v56, v49 op_sel_hi:[0,1]\n"
    "v_mfma_f32_16x16x32_f16 v[84:87], v[136:139], v[72:75], v[84:87]\n"
    "s_cmp_lg_u32 s46, 0\n"
    "v_pk_mul_f16 v66, v48, v56 op_sel_hi:[0,1]\n"
    "v_smfmac_f32_16x16x64_f16 v[88:91], v[140:143], v[68:75], v2\n"
    "s_cbranch_scc1 .Lxd_ht38\n"
    ".Lxd_ht38r:\n"
    "v_pk_mul_f16 v67, v48, v57 op_sel_hi:[0,1]\n"
    "v_mfma_f32_16x16x32_f16 v[76:79], v[144:147], v[60:63], v[76:79]\n"
    "v_pk_mul_f16 v68, v50, v56 op_sel_hi:[0,1]\n"
    "v_pk_mul_f16 v69, v50, v57 op_sel_hi:[0,1]\n"
    "v_mfma_f32_16x16x32_f16 v[76:79], v[156:159], v[64:67], v[76:79]\n"
    "v_pk_mul_f16 v70, v52, v56 op_sel_hi:[0,1]\n"
    "v_mfma_f32_16x16x32_f16 v[80:83], v[160:163], v[64:67], v[80:83]\n"
    "v_pk_mul_f16 v71, v52, v57 op_sel_hi:[0,1]\n"
    "v_mfma_f32_16x16x32_f16 v[80:83], v[148:151], v[60:63], v[80:83]\n"
    "ds_read_b32 v22, v25 offset:524\n"
    "v_pk_mul_f16 v72, v54, v56 op_sel_hi:[0,1]\n"
    "v_mfma_f32_16x16x32_f16 v[84:87], v[152:155], v[60:63], v[84:87]\n"
    "v_pk_mul_f16 v73, v54, v57 op_sel_hi:[0,1]\n"
    "v_mfma_f32_16x16x32_f16 v[84:87], v[164:167], v[64:67], v[84:87]\n"
    "v_pk_mul_f16 v74, v54, v54 op_sel_hi:[0,1]\n"
    "v_smfmac_f32_16x16x64_f16 v[88:91], v[168:171], v[60:67], v2\n"
    "v_pk_mul_f16 v75, v54, v49 op_sel_hi:[0,1]\n"
    "v_mfma_f32_16x16x32_f16 v[76:79], v[172:175], v[68:71], v[76:79]\n"
    "v_pk_mul_f16 v60, v48, v54 op_sel_hi:[0,1]\n"
    "v_pk_mul_f16 v61, v48, v55 op_sel_hi:[0,1]\n"
    "v_mfma_f32_16x16x32_f16 v[76:79], v[184:187], v[72:75], v[76:79]\n"
    "s_waitcnt lgkmcnt(0)\n"
    "v_pk_mul_f16 v62, v50, v54 op_sel_hi:[0,1]\n"
    "v_readfirstlane_b32 s45, v22\n"
    "v_mfma_f32_16x16x32_f16 v[80:83], v[188:191], v[72:75], v[80:83]\n"
    "s_cmp_lt_i32 s45, 16\n"
    "v_pk_mul_f16 v63, v50, v55 op_sel_hi:[0,1]\n"
    "v_mfma_f32_16x16x32_f16 v[80:83], v[176:179], v[68:71], v[80:83]\n"
    "s_cbranch_scc1 .Lxd_ck39\n"
    ".Lxd_ck39r:\n"
    "v_pk_mul_f16 v64, v52, v54 op_sel_hi:[0,1]\n"
    "v_mfma_f32_16x16x32_f16 v[84:87], v[180:183], v[68:71], v[84:87]\n"
    "ds_read_b64_tr_b16 v[44:45], v5 offset:4096\n"
    "v_pk_mul_f16 v65, v52, v55 op_sel_hi:[0,1]\n"
    "v_mfma_f32_16x16x32_f16 v[84:87], v[192:195], v[72:75], v[84:87]\n"
    "ds_read_b64_tr_b16 v[46:47], v6 offset:4096\n"
    "v_pk_mul_f16 v66, v52, v52 op_sel_hi:[0,1]\n"
    "v_smfmac_f32_16x16x64_f16 v[88:91], v[196:199], v[68:75], v2\n"
    "v_pk_mul_f16 v67, v52, v49 op_sel_hi:[0,1]\n"
    "v_mfma_f32_16x16x32_f16 v[76:79], v[200:203], v[60:63], v[76:79]\n"
    "v_pk_mul_f16 v68, v48, v52 op_sel_hi:[0,1]\n"
    "v_pk_mul_f16 v69, v48, v53 op_sel_hi:[0,1]\n"
    "v_mfma_f32_16x16x32_f16 v[76:79], v[212:215], v[64:67], v[76:79]\n"
    "v_pk_mul_f16 v70, v50, v52 op_sel_hi:[0,1]\n"
    "v_mfma_f32_16x16x32_f16 v[80:83], v[216:219], v[64:67], v[80:83]\n"
    "s_cbranch_vccnz .Lxd_pr37\n"
    "s_setprio 0\n"
    ".Lxd_pr37:\n"
    "v_pk_mul_f16 v71, v50, v53 op_sel_hi:[0,1]\n"
    "v_mfma_f32_16x16x32_f16 v[80:83], v[204:207], v[60:63], v[80:83]\n"
    "v_pk_mul_f16 v72, v50, v50 op_sel_hi:[0,1]\n"
    "v_mfma_f32_16x16x32_f16 v[84:87], v[208:211], v[60:63], v[84:87]\n"
    "v_pk_mul_f16 v73, v50, v49 op_sel_hi:[0,1]\n"
    "v_pk_mul_f16 v74, v48, v50 op_sel_hi:[0,1]\n"
    "v_mfma_f32_16x16x32_f16 v[84:87], v[220:223], v[64:67], v[84:87]\n"
    "v_pk_mul_f16 v75, v48, v51 op_sel_hi:[0,1]\n"
    "v_smfmac_f32_16x16x64_f16 v[88:91], v[224:227], v[60:67], v2\n"
    "ds_read_b64_tr_b16 v[48:49], v3 offset:4096\n"
    "ds_read_b64_tr_b16 v[58:59], v4 offset:6976\n"
    "v_smfmac_f32_16x16x64_f16 v[88:91], v[252:255], v[68:75], v2\n"
    "s_waitcnt lgkmcnt(2)\n"
    "v_pk_mul_f16 v60, v44, s45 op_sel:[0,1]\n"
    "v_pk_mul_f16 v61, v45, s45 op_sel:[0,1]\n"
    "v_mfma_f32_16x16x32_f16 v[84:87], v[236:239], v[68:71], v[84:87]\n"
    "ds_read_b64_tr_b16 v[50:51], v4 offset:4672\n"
    "v_pk_mul_f16 v62, v46, s45 op_sel:[0,1]\n"
    "v_pk_mul_f16 v63, v47, s45 op_sel:[0,1]\n"
    "v_mfma_f32_16x16x32_f16 v[84:87], v[248:251], v[72:75], v[84:87]\n"
    "ds_read_b64_tr_b16 v[52:53], v4 offset:5248\n"
    "v_pk_mul_f16 v64, v44, v46\n"
    "v_pk_mul_f16 v65, v45, v47\n"
    "v_mfma_f32_16x16x32_f16 v[76:79], v[240:243], v[72:75], v[76:79]\n"
    "ds_read_b64_tr_b16 v[54:55], v4 offset:5824\n"
    "v_add_f32 v11, v88, v89\n"
    "v_add_f32 v13, v90, v91\n"
    "v_mfma_f32_16x16x32_f16 v[76:79], v[228:231], v[68:71], v[76:79]\n"
    "ds_read_b64_tr_b16 v[56:57], v4 offset:6400\n"
    "v_mov_b64 v[88:89], 0\n"
    "v_mov_b64 v[90:91], 0\n"
    "v_mfma_f32_16x16x32_f16 v[80:83], v[232:235], v[68:71], v[80:83]\n"
    "s_waitcnt lgkmcnt(4)\n"
    "v_pk_mul_f16 v66, v48, v48 op_sel_hi:[0,1]\n"
    "v_pk_mul_f16 v67, v48, v49 op_sel_hi:[0,1]\n"
    "v_mfma_f32_16x16x32_f16 v[80:83], v[244:247], v[72:75], v[80:83]\n"
    "v_mul_f32 v11, v11, v13\n"
    "v_fmac_f32 v11, v84, v85\n"
    "v_fmac_f32 v11, v86, v87\n"
    ".Lxd_pl3:\n"
    "v_smfmac_f32_16x16x64_f16 v[88:91], v[112:115], v[60:67], v2\n"
    "s_cbranch_vccnz .Lxd_pr40\n"
    "s_setprio 2\n"
    ".Lxd_pr40:\n"
    "v_pk_mul_f16 v68, v58, v58 op_sel_hi:[0,1]\n"
    "v_pk_mul_f16 v69, v58, v49 op_sel_hi:[0,1]\n"
    "v_mfma_f32_16x16x32_f16 v[84:87], v[108:111], v[64:67], 0\n"
    "v_fmac_f32 v11, v76, v80\n"
    "v_fmac_f32 v11, v77, v81\n"
    "v_mfma_f32_16x16x32_f16 v[84:87], v[96:99], v[60:63], v[84:87]\n"
    "v_fmac_f32 v11, v78, v82\n"
    "v_fma_f32 v9, v79, v83, v11\n"
    "v_mfma_f32_16x16x32_f16 v[80:83], v[92:95], v[60:63], 0\n"
    "s_waitcnt lgkmcnt(0)\n"
    "v_pk_mul_f16 v70, v48, v58 op_sel_hi:[0,1]\n"
    "v_pk_mul_f16 v71, v48, v59 op_sel_hi:[0,1]\n"
    "v_mfma_f32_16x16x32_f16 v[80:83], v[104:107], v[64:67], v[80:83]\n"
    "v_pk_mul_f16 v72, v50, v58 op_sel_hi:[0,1]\n"
    "v_pk_mul_f16 v73, v50, v59 op_sel_hi:[0,1]\n"
    "v_mfma_f32_16x16x32_f16 v[76:79], v[100:103], v[64:67], 0\n"
    "v_pk_mul_f16 v74, v52, v58 op_sel_hi:[0,1]\n"
    "v_pk_mul_f16 v75, v52, v59 op_sel_hi:[0,1]\n"
    "v_mfma_f32_16x16x32_f16 v[76:79], v[116:119], v[68:71], v[76:79]\n"
    "v_pk_mul_f16 v60, v54, v58 op_sel_hi:[0,1]\n"
    "v_pk_mul_f16 v61, v54, v59 op_sel_hi:[0,1]\n"
    "v_mfma_f32_16x16x32_f16 v[76:79], v[128:131], v[72:75], v[76:79]\n"
    "v_pk_mul_f16 v62, v56, v58 op_sel_hi:[0,1]\n"
    "v_mfma_f32_16x16x32_f16 v[80:83], v[132:135], v[72:75], v[80:83]\n"
    "v_pk_mul_f16 v63, v56, v59 op_sel_hi:[0,1]\n"
    "v_mfma_f32_16x16x32_f16 v[80:83], v[120:123], v[68:71], v[80:83]\n"
    "v_pk_mul_f16 v64, v56, v56 op_sel_hi:[0,1]\n"
    "v_mfma_f32_16x16x32_f16 v[84:87], v[124:127], v[68:71], v[84:87]\n"
    "v_pk_mul_f16 v65, v56, v49 op_sel_hi:[0,1]\n"
    "v_mfma_f32_16x16x32_f16 v[84:87], v[136:139], v[72:75], v[84:87]\n"
    "v_pk_mul_f16 v66, v48, v56 op_sel_hi:[0,1]\n"
    "v_smfmac_f32_16x16x64_f16 v[88:91], v[140:143], v[68:75], v2\n"
    "v_pk_mul_f16 v67, v48, v57 op_sel_hi:[0,1]\n"
    "v_mfma_f32_16x16x32_f16 v[76:79], v[144:147], v[60:63], v[76:79]\n"
    "v_pk_mul_f16 v68, v50, v56 op_sel_hi:[0,1]\n"
    "v_pk_mul_f16 v69, v50, v57 op_sel_hi:[0,1]\n"
    "v_mfma_f32_16x16x32_f16 v[76:79], v[156:159], v[64:67], v[76:79]\n"
    "s_cmp_eq_u32 s36, 0\n"
    "s_cbranch_scc1 .Lxd_on43\n"
    ".Lxd_on43r:\n"
    "v_pk_mul_f16 v70, v52, v56 op_sel_hi:[0,1]\n"
    "v_mfma_f32_16x16x32_f16 v[80:83], v[160:163], v[64:67], v[80:83]\n"
    "v_pk_mul_f16 v71, v52, v57 op_sel_hi:[0,1]\n"
    "v_mfma_f32_16x16x32_f16 v[80:83], v[148:151], v[60:63], v[80:83]\n"
    "ds_read_b32 v22, v25 offset:528\n"
    "v_pk_mul_f16 v72, v54, v56 op_sel_hi:[0,1]\n"
    "v_mfma_f32_16x16x32_f16 v[84:87], v[152:155], v[60:63], v[84:87]\n"
    "v_pk_mul_f16 v73, v54, v57 op_sel_hi:[0,1]\n"
    "v_mfma_f32_16x16x32_f16 v[84:87], v[164:167], v[64:67], v[84:87]\n"
    "v_pk_mul_f16 v74, v54, v54 op_sel_hi:[0,1]\n"
    "v_smfmac_f32_16x16x64_f16 v[88:91], v[168:171], v[60:67], v2\n"
    "v_pk_mul_f16 v75, v54, v49 op_sel_hi:[0,1]\n"
    "v_mfma_f32_16x16x32_f16 v[76:79], v[172:175], v[68:71], v[76:79]\n"
    "v_pk_mul_f16 v60, v48, v54 op_sel_hi:[0,1]\n"
    "v_pk_mul_f16 v61, v48, v55 op_sel_hi:[0,1]\n"
    "v_mfma_f32_16x16x32_f16 v[76:79], v[184:187], v[72:75], v[76:79]\n"
    "s_waitcnt lgkmcnt(0)\n"
    "v_pk_mul_f16 v62, v50, v54 op_sel_hi:[0,1]\n"
    "v_readfirstlane_b32 s45, v22\n"
    "v_mfma_f32_16x16x32_f16 v[80:83], v[188:191], v[72:75], v[80:83]\n"
    "s_cmp_lt_i32 s45, 16\n"
    "v_pk_mul_f16 v63, v50, v55 op_sel_hi:[0,1]\n"
    "v_mfma_f32_16x16x32_f16 v[80:83], v[176:179], v[68:71], v[80:83]\n"
    "s_cbranch_scc1 .Lxd_ck44\n"
    ".Lxd_ck44r:\n"
    "v_pk_mul_f16 v64, v52, v54 op_sel_hi:[0,1]\n"
    "v_mfma_f32_16x16x32_f16 v[84:87], v[180:183], v[68:71], v[84:87]\n"
    "ds_read_b64_tr_b16 v[44:45], v5 offset:8192\n"
    "v_pk_mul_f16 v65, v52, v55 op_sel_hi:[0,1]\n"
    "v_mfma_f32_16x16x32_f16 v[84:87], v[192:195], v[72:75], v[84:87]\n"
    "ds_read_b64_tr_b16 v[46:47], v6 offset:8192\n"
    "v_pk_mul_f16 v66, v52, v52 op_sel_hi:[0,1]\n"
    "v_smfmac_f32_16x16x64_f16 v[88:91], v[196:199], v[68:75], v2\n"
    "v_pk_mul_f16 v67, v52, v49 op_sel_hi:[0,1]\n"
    "v_mfma_f32_16x16x32_f16 v[76:79], v[200:203], v[60:63], v[76:79]\n"
    "v_pk_mul_f16 v68, v48, v52 op_sel_hi:[0,1]\n"
    "v_pk_mul_f16 v69, v48, v53 op_sel_hi:[0,1]\n"
    "v_mfma_f32_16x16x32_f16 v[76:79], v[212:215], v[64:67], v[76:79]\n"
    "v_pk_mul_f16 v70, v50, v52 op_sel_hi:[0,1]\n"
    "v_mfma_f32_16x16x32_f16 v[80:83], v[216:219], v[64:67], v[80:83]\n"
    "s_cbranch_vccnz .Lxd_pr41\n"
    "s_setprio 0\n"
    ".Lxd_pr41:\n"
    "v_pk_mul_f16 v71, v50, v53 op_sel_hi:[0,1]\n"
    "v_mfma_f32_16x16x32_f16 v[80:83], v[204:207], v[60:63], v[80:83]\n"
    "v_pk_mul_f16 v72, v50, v50 op_sel_hi:[0,1]\n"
    "v_mfma_f32_16x16x32_f16 v[84:87], v[208:211], v[60:63], v[84:87]\n"
    "v_pk_mul_f16 v73, v50, v49 op_sel_hi:[0,1]\n"
    "v_pk_mul_f16 v74, v48, v50 op_sel_hi:[0,1]\n"
    "v_mfma_f32_16x16x32_f16 v[84:87], v[220:223], v[64:67], v[84:87]\n"
    "v_pk_mul_f16 v75, v48, v51 op_sel_hi:[0,1]\n"
    "v_smfmac_f32_16x16x64_f16 v[88:91], v[224:227], v[60:67], v2\n"
    "ds_read_b64_tr_b16 v[48:49], v3 offset:8192\n"
    "ds_read_b64_tr_b16 v[58:59], v4 offset:11072\n"
    "v_add_u32 v25, 16, v25\n"
    "v_smfmac_f32_16x16x64_f16 v[88:91], v[252:255], v[68:75], v2\n"
    "s_waitcnt lgkmcnt(2)\n"
    "v_pk_mul_f16 v60, v44, s45 op_sel:[0,1]\n"
    "v_pk_mul_f16 v61, v45, s45 op_sel:[0,1]\n"
    "v_mfma_f32_16x16x32_f16 v[84:87], v[236:239], v[68:71], v[84:87]\n"
    "ds_read_b64_tr_b16 v[50:51], v4 offset:8768\n"
    "v_pk_mul_f16 v62, v46, s45 op_sel:[0,1]\n"
    "v_pk_mul_f16 v63, v47, s45 op_sel:[0,1]\n"
    "v_mfma_f32_16x16x32_f16 v[84:87], v[248:251], v[72:75], v[84:87]\n"
    "ds_read_b64_tr_b16 v[52:53], v4 offset:9344\n"
    "v_pk_mul_f16 v64, v44, v46\n"
    "v_pk_mul_f16 v65, v45, v47\n"
    "v_mfma_f32_16x16x32_f16 v[76:79], v[240:243], v[72:75], v[76:79]\n"
    "ds_read_b64_tr_b16 v[54:55], v4 offset:9920\n"
    "v_add_f32 v11, v88, v89\n"
    "v_add_f32 v13, v90, v91\n"
    "v_mfma_f32_16x16x32_f16 v[76:79], v[228:231], v[68:71], v[76:79]\n"
    "ds_read_b64_tr_b16 v[56:57], v4 offset:10496\n"
    "v_mov_b64 v[88:89], 0\n"
    "v_mov_b64 v[90:91], 0\n"
    "v_mfma_f32_16x16x32_f16 v[80:83], v[232:235], v[68:71], v[80:83]\n"
    "s_waitcnt lgkmcnt(4)\n"
    "v_pk_mul_f16 v66, v48, v48 op_sel_hi:[0,1]\n"
    "v_pk_mul_f16 v67, v48, v49 op_sel_hi:[0,1]\n"
    "v_mfma_f32_16x16x32_f16 v[80:83], v[244:247], v[72:75], v[80:83]\n"
    "s_cmp_eq_u32 s36, 0\n"
    "v_mul_f32 v11, v11, v13\n"
    "v_fmac_f32 v11, v84, v85\n"
    "v_fmac_f32 v11, v86, v87\n"
    "s_cbranch_scc1 .Lxd_ph0\n"
    "s_branch .Lxd_pl0\n"
    ".Lxd_last:\n"
    "s_cbranch_vccnz .Lxd_pr45\n"
    "s_setprio 0\n"
    ".Lxd_pr45:\n"
    "v_pk_mul_f16 v64, v52, v54 op_sel_hi:[0,1]\n"
    "v_mfma_f32_16x16x32_f16 v[84:87], v[180:183], v[68:71], v[84:87]\n"
    "v_pk_mul_f16 v65, v52, v55 op_sel_hi:[0,1]\n"
    "v_mfma_f32_16x16x32_f16 v[84:87], v[192:195], v[72:75], v[84:87]\n"
    "v_pk_mul_f16 v66, v52, v52 op_sel_hi:[0,1]\n"
    "v_smfmac_f32_16x16x64_f16 v[88:91], v[196:199], v[68:75], v2\n"
    "v_pk_mul_f16 v67, v52, v49 op_sel_hi:[0,1]\n"
    "v_mfma_f32_16x16x32_f16 v[76:79], v[200:203], v[60:63], v[76:79]\n"
    "v_pk_mul_f16 v68, v48, v52 op_sel_hi:[0,1]\n"
    "v_pk_mul_f16 v69, v48, v53 op_sel_hi:[0,1]\n"
    "v_mfma_f32_16x16x32_f16 v[76:79], v[212:215], v[64:67], v[76:79]\n"
    "v_pk_mul_f16 v70, v50, v52 op_sel_hi:[0,1]\n"
    "v_mfma_f32_16x16x32_f16 v[80:83], v[216:219], v[64:67], v[80:83]\n"
    "v_pk_mul_f16 v71, v50, v53 op_sel_hi:[0,1]\n"
    "v_mfma_f32_16x16x32_f16 v[80:83], v[204:207], v[60:63], v[80:83]\n"
    "v_pk_mul_f16 v72, v50, v50 op_sel_hi:[0,1]\n"
    "v_mfma_f32_16x16x32_f16 v[84:87], v[208:211], v[60:63], v[84:87]\n"
    "v_pk_mul_f16 v73, v50, v49 op_sel_hi:[0,1]\n"
    "v_pk_mul_f16 v74, v48, v50 op_sel_hi:[0,1]\n"
    "v_mfma_f32_16x16x32_f16 v[84:87], v[220:223], v[64:67], v[84:87]\n"
    "v_pk_mul_f16 v75, v48, v51 op_sel_hi:[0,1]\n"
    "v_smfmac_f32_16x16x64_f16 v[88:91], v[224:227], v[60:67], v2\n"
    "s_nop 0\n"
    "v_smfmac_f32_16x16x64_f16 v[88:91], v[252:255], v[68:75], v2\n"
    "v_mfma_f32_16x16x32_f16 v[84:87], v[236:239], v[68:71], v[84:87]\n"
    "v_mfma_f32_16x16x32_f16 v[84:87], v[248:251], v[72:75], v[84:87]\n"
    "v_mfma_f32_16x16x32_f16 v[76:79], v[240:243], v[72:75], v[76:79]\n"
    "v_mfma_f32_16x16x32_f16 v[76:79], v[228:231], v[68:71], v[76:79]\n"
    "v_mfma_f32_16x16x32_f16 v[80:83], v[232:235], v[68:71], v[80:83]\n"
    "v_mfma_f32_16x16x32_f16 v[80:83], v[244:247], v[72:75], v[80:83]\n"
    "s_nop 1\n"
    "v_add_f32 v11, v88, v89\n"
    "v_add_f32 v13, v90, v91\n"
    "v_mul_f32 v11, v11, v13\n"
    "v_fmac_f32 v11, v84, v85\n"
    "v_fmac_f32 v11, v86, v87\n"
    "s_nop 0\n"
    "v_fmac_f32 v11, v76, v80\n"
    "v_fmac_f32 v11, v77, v81\n"
    "v_fmac_f32 v11, v78, v82\n"
    "s_setpc_b64 s[60:61]\n"
    ".Lxd_fin:\n"
    "v_lshl_add_u32 v68, v18, 6, v20\n"
    "v_mov_b32 v67, 154144\n"
    "v_permlane32_swap_b32 v7, v8\n"
    "v_permlane32_swap_b32 v9, v10\n"
    "v_mul_f32 v7, v0, v7\n"
    "v_mul_f32 v9, v0, v9\n"
    "v_fmac_f32 v7, v23, v8\n"
    "v_fmac_f32 v9, v23, v10\n"
    "ds_read_b32 v67, v67\n"
    "v_lshlrev_b32 v69, 2, v68\n"
    "v_permlane16_swap_b32 v7, v9\n"
    "v_add_f32 v7, v7, v9\n"
    "v_add_u32 v68, 150528, v68\n"
    "v_add_u32 v69, 152128, v69\n"
    "v_add_f32_dpp v7, v7, v7 quad_perm:[1,0,3,2] row_mask:0xf bank_mask:0xf\n"
    "s_lshl_b32 s26, s26, 16\n"
    "s_nop 0\n"
    "v_add_f32_dpp v7, v7, v7 quad_perm:[2,3,0,1] row_mask:0xf bank_mask:0xf\n"
    "s_nop 1\n"
    "v_add_f32_dpp v7, v7, v7 row_half_mirror row_mask:0xf bank_mask:0xf\n"
    "s_nop 1\n"
    "v_add_f32_dpp v7, v7, v7 row_mirror row_mask:0xf bank_mask:0xf\n"
    "ds_write_b32 v14, v7\n"
    "s_cmp_lg_u32 s56, 2\n"
    "s_cbranch_scc1 .Lxd_f2\n"
    ".Lxd_f3:\n"
    "s_waitcnt vmcnt(0) lgkmcnt(0)\n"
    "s_barrier\n"
    "s_cmp_lg_u32 s20, 0\n"
    "s_cbranch_scc1 .Lxd_done\n"
    "ds_read_b128 v[60:63], v69\n"
    "ds_read_b32 v64, v68\n"
    "ds_read_u16 v65, v68 offset:512\n"
    "ds_read_b32 v66, v68 offset:1024\n"
    "s_waitcnt lgkmcnt(3)\n"
    "v_pk_add_f32 v[60:61], v[60:61], v[62:63]\n"
    "s_waitcnt lgkmcnt(0)\n"
    "v_cmp_lt_i32 vcc, 0, v64\n"
    "v_lshrrev_b32 v65, 4, v65\n"
    "v_add_f32 v60, v60, v61\n"
    "v_lshl_add_u32 v65, v65, 2, v67\n"
    "v_add_f32 v66, s26, v66\n"
    "v_ldexp_f32 v60, v60, v65\n"
    "s_mov_b64 exec, vcc\n"
    "v_add_f32 v60, v60, v66\n"
    "v_cvt_pk_bf16_f32 v60, v60, v60\n"
    "global_store_short v64, v60, s[70:71] offset:-2\n"
    "s_mov_b64 exec, -1\n"
    ".Lxd_done:\n"
    "s_endpgm\n"
    ".Lxd_cin:\n"
    "s_lshr_b32 s20, s43, 6\n"
    "v_and_b32 v1, 63, v0\n"
    "v_lshlrev_b32 v42, 4, v1\n"
    "s_and_b32 s40, s2, 3\n"
    "s_lshl2_add_u32 s23, s40, s20\n"
    "s_lshl1_add_u32 s41, s40, s20\n"
    "s_add_u32 s41, s41, 12\n"
    "s_cmp_ge_u32 s20, 4\n"
    "s_cselect_b32 s23, s41, s23\n"
    "s_add_u32 s41, s40, 24\n"
    "s_cmp_ge_u32 s20, 6\n"
    "s_cselect_b32 s23, s41, s23\n"
    "s_cselect_b32 s24, 1, 0\n"
    "s_min_u32 s34, s23, 25\n"
    "s_lshr_b32 s35, s2, 2\n"
    "v_and_b32 v190, 31, v0\n"
    "v_min_u32 v190, 25, v190\n"
    "v_mul_u32_u24 v190, 52, v190\n"
    "v_bfe_u32 v191, v0, 5, 1\n"
    "v_lshl_add_u32 v190, v191, 4, v190\n"
    "s_mul_i32 s40, s34, 1352\n"
    "s_xor_b32 s66, s20, 7\n"
    "s_or_b32 s66, s66, s2\n"
    "s_waitcnt lgkmcnt(0)\n"
    "s_load_dwordx4 s[68:71], s[0:1], 64\n"
    "s_add_u32 s38, s12, s40\n"
    "s_addc_u32 s39, s13, 0\n"
    "s_cmp_lg_u32 s66, 0\n"
    "s_cbranch_scc1 .Lxd_on46\n"
    "v_and_b32 v188, 31, v0\n"
    "v_lshlrev_b32 v188, 7, v188\n"
    "v_add_u32 v188, 32768, v188\n"
    "v_mov_b32 v176, 114\n"
    "global_store_dword v188, v176, s[16:17] sc1\n"
    "global_load_dwordx4 v[180:183], v42, s[16:17] sc1\n"
    ".Lxd_on46:\n"
    "global_load_dwordx4 v[80:83], v190, s[38:39] offset:0\n"
    "global_load_dwordx4 v[84:87], v190, s[38:39] offset:32\n"
    "s_getpc_b64 s[60:61]\n"
    "s_add_u32 s60, s60, xd_tab@rel32@lo+4\n"
    "s_addc_u32 s61, s61, xd_tab@rel32@hi+12\n"
    "v_lshl_add_u32 v188, s24, 10, v42\n"
    "s_lshl1_add_u32 s40, s24, s35\n"
    "v_lshl_add_u32 v189, s40, 11, v42\n"
    "s_load_dword s25, s[16:17], 0\n"
    "global_load_dwordx4 v[44:47], v188, s[60:61]\n"
    "global_load_dwordx4 v[48:51], v189, s[60:61] offset:2048\n"
    "global_load_dwordx4 v[52:55], v189, s[60:61] offset:3072\n"
    "s_setprio 0\n"
    "v_mov_b64 v[188:189], 0\n"
    "v_lshlrev_b32 v190, 3, v0\n"
    "v_lshrrev_b32 v186, 5, v1\n"
    "ds_write_b64 v190, v[188:189] offset:36864\n"
    "v_bfe_u32 v190, v0, 1, 4\n"
    "v_lshlrev_b32 v72, 2, v186\n"
    "v_sub_u32 v190, v190, v72\n"
    "v_and_b32 v72, 16, v42\n"
    "s_movk_i32 s40, 0x3f80\n"
    "v_lshlrev_b32_e64 v191, v72, s40\n"
    "v_cmp_eq_u32 s[60:61], 0, v190\n"
    "v_cmp_eq_u32 s[62:63], 1, v190\n"
    "v_cmp_eq_u32 s[64:65], 2, v190\n"
    "v_cmp_eq_u32 s[50:51], 3, v190\n"
    "v_cndmask_b32 v72, 0, v191, s[60:61]\n"
    "v_cndmask_b32 v73, 0, v191, s[62:63]\n"
    "v_cndmask_b32 v74, 0, v191, s[64:65]\n"
    "v_cndmask_b32 v75, 0, v191, s[50:51]\n"
    "v_cmp_eq_u32 s[60:61], 8, v190\n"
    "v_cmp_eq_u32 s[62:63], 9, v190\n"
    "v_cmp_eq_u32 s[64:65], 10, v190\n"
    "v_cmp_eq_u32 s[50:51], 11, v190\n"
    "v_cndmask_b32 v76, 0, v191, s[60:61]\n"
    "v_cndmask_b32 v77, 0, v191, s[62:63]\n"
    "v_cndmask_b32 v78, 0, v191, s[64:65]\n"
    "v_cndmask_b32 v79, 0, v191, s[50:51]\n"
    "s_lshl_b32 s30, s20, 12\n"
    "s_add_u32 s30, s30, 65536\n"
    "v_and_b32 v188, 31, v0\n"
    "v_mul_u32_u24 v192, 528, v186\n"
    "v_lshl_add_u32 v192, v188, 2, v192\n"
    "v_add_u32 v192, s30, v192\n"
    "s_lshl_b32 s40, s35, 6\n"
    "s_add_u32 s40, s40, s20\n"
    "s_lshl1_add_u32 s41, s20, s35\n"
    "s_add_u32 s41, s41, 120\n"
    "s_cmp_ge_u32 s20, 4\n"
    "s_cselect_b32 s40, s41, s40\n"
    "s_lshl1_add_u32 s41, s35, 384\n"
    "s_cmp_ge_u32 s20, 6\n"
    "s_cselect_b32 s40, s41, s40\n"
    "s_cselect_b32 s42, 1, 192\n"
    "s_and_b32 s41, s2, 3\n"
    "s_lshl2_add_u32 s40, s41, s40\n"
    "v_lshrrev_b32 v188, 3, v1\n"
    "v_mul_u32_u24 v188, 448, v188\n"
    "v_bfe_u32 v189, v0, 2, 1\n"
    "v_mad_u32_u24 v188, v189, s42, v188\n"
    "v_and_b32 v189, 48, v42\n"
    "v_add3_u32 v188, v188, v189, s40\n"
    "v_lshlrev_b32 v193, 4, v188\n"
    "v_mov_b32 v177, 0\n"
    "s_waitcnt vmcnt(0) lgkmcnt(0)\n"
    "s_cmp_lg_u32 s66, 0\n"
    "s_cbranch_scc1 .Lxd_on47\n"
    "s_mov_b32 s40, 0x9e3779b1\n"
    "v_mul_lo_u32 v144, v180, s40\n"
    "v_xor_b32 v145, 0xa5c30f96, v180\n"
    "v_alignbit_b32 v144, s25, v144, 24\n"
    "v_mul_lo_u32 v146, v181, s40\n"
    "v_xor_b32 v147, 0xa5c30f96, v181\n"
    "v_alignbit_b32 v146, s25, v146, 24\n"
    "v_mul_lo_u32 v148, v182, s40\n"
    "v_xor_b32 v149, 0xa5c30f96, v182\n"
    "v_alignbit_b32 v148, s25, v148, 24\n"
    "v_mul_lo_u32 v150, v183, s40\n"
    "v_xor_b32 v151, 0xa5c30f96, v183\n"
    "v_alignbit_b32 v150, s25, v150, 24\n"
    "v_lshlrev_b32 v180, 1, v42\n"
    "s_add_u32 s40, s16, 4096\n"
    "s_addc_u32 s41, s17, 0\n"
    "global_store_dwordx4 v180, v[144:147], s[40:41] sc1\n"
    "global_store_dwordx4 v180, v[148:151], s[40:41] offset:16 sc1\n"
    ".Lxd_on47:\n"
    "s_barrier\n"
    "s_branch .Lxd_bD\n"
    ".Lxd_s2:\n"
    "s_cmp_lt_u32 s2, 4\n"
    "s_cbranch_scc1 .Lxd_cin\n"
    ".Lxd_bR:\n"
    "s_lshr_b32 s20, s43, 6\n"
    "v_and_b32 v1, 63, v0\n"
    "v_lshlrev_b32 v42, 4, v1\n"
    "s_mul_i32 s40, s20, 4480\n"
    "s_min_u32 s40, s40, 30672\n"
    "s_mov_b32 m0, s40\n"
    "s_add_u32 s40, s40, 70304\n"
    "s_waitcnt lgkmcnt(0)\n"
    "s_load_dwordx4 s[68:71], s[0:1], 64\n"
    "s_add_u32 s38, s12, s40\n"
    "s_addc_u32 s39, s13, 0\n"
    "global_load_lds_dwordx4 v42, s[38:39] offset:0\n"
    "global_load_lds_dwordx4 v42, s[38:39] offset:1024\n"
    "global_load_lds_dwordx4 v42, s[38:39] offset:2048\n"
    "global_load_lds_dwordx4 v42, s[38:39] offset:3072\n"
    "global_load_lds_dwordx4 v42, s[38:39] offset:3456\n"
    "s_and_b32 s40, s2, 3\n"
    "s_lshl2_add_u32 s23, s40, s20\n"
    "s_lshl1_add_u32 s41, s40, s20\n"
    "s_add_u32 s41, s41, 12\n"
    "s_cmp_ge_u32 s20, 4\n"
    "s_cselect_b32 s23, s41, s23\n"
    "s_add_u32 s41, s40, 24\n"
    "s_cmp_ge_u32 s20, 6\n"
    "s_cselect_b32 s23, s41, s23\n"
    "s_cselect_b32 s24, 1, 0\n"
    "s_min_u32 s34, s23, 25\n"
    "s_lshr_b32 s35, s2, 2\n"
    "v_lshrrev_b32 v190, 2, v1\n"
    "v_and_b32 v188, 48, v42\n"
    "s_movk_i32 s42, 1352\n"
    "v_min_u32 v191, 9, v190\n"
    "v_mad_u32_u24 v190, v190, s42, v188\n"
    "v_mad_u32_u24 v191, v191, s42, v188\n"
    "v_add_u32 v191, 20608, v191\n"
    "s_mul_i32 s41, s34, 52\n"
    "s_add_u32 s41, s41, 35152\n"
    "s_cmp_lg_u32 s20, 7\n"
    "s_cbranch_scc1 .Lxd_on48\n"
    "v_min_u32 v188, 38, v1\n"
    "s_mov_b32 m0, 40960\n"
    "v_lshlrev_b32 v188, 2, v188\n"
    "global_load_lds_dword v188, s[14:15]\n"
    ".Lxd_on48:\n"
    "s_lshl_b32 s28, s20, 11\n"
    "s_add_u32 s28, s28, 49152\n"
    "s_mov_b32 m0, s28\n"
    "s_add_u32 s38, s12, s41\n"
    "s_addc_u32 s39, s13, 0\n"
    "global_load_lds_dwordx4 v190, s[38:39] offset:0\n"
    "global_load_lds_dwordx4 v191, s[38:39] offset:1024\n"
    "s_getpc_b64 s[60:61]\n"
    "s_add_u32 s60, s60, xd_tab@rel32@lo+4\n"
    "s_addc_u32 s61, s61, xd_tab@rel32@hi+12\n"
    "v_lshl_add_u32 v188, s24, 10, v42\n"
    "s_lshl1_add_u32 s40, s24, s35\n"
    "v_lshl_add_u32 v189, s40, 11, v42\n"
    "s_load_dword s25, s[16:17], 0\n"
    "global_load_dwordx4 v[44:47], v188, s[60:61]\n"
    "global_load_dwordx4 v[48:51], v189, s[60:61] offset:2048\n"
    "global_load_dwordx4 v[52:55], v189, s[60:61] offset:3072\n"
    "s_setprio 0\n"
    "v_mov_b64 v[188:189], 0\n"
    "v_lshlrev_b32 v190, 3, v0\n"
    "v_lshrrev_b32 v186, 5, v1\n"
    "ds_write_b64 v190, v[188:189] offset:36864\n"
    "v_lshrrev_b32 v184, 4, v1\n"
    "v_and_b32 v185, 15, v0\n"
    "v_lshrrev_b32 v190, 2, v185\n"
    "v_lshl_add_u32 v64, v184, 3, v190\n"
    "v_add_u32 v65, 4, v64\n"
    "s_lshr_b32 s21, s20, 2\n"
    "s_and_b32 s43, s20, 3\n"
    "s_mul_i32 s40, s21, 13\n"
    "s_lshl2_add_u32 s40, s43, s40\n"
    "v_and_b32 v191, 3, v0\n"
    "v_add_lshl_u32 v188, v191, s40, 3\n"
    "v_min_u32 v64, 25, v64\n"
    "v_mad_u32_u24 v64, v64, s42, v188\n"
    "v_min_u32 v65, 25, v65\n"
    "v_mad_u32_u24 v65, v65, s42, v188\n"
    "v_lshl_add_u32 v188, s43, 4, v185\n"
    "v_cmp_lt_u32 vcc, 25, v188\n"
    "v_cmp_lt_u32 s[60:61], 51, v188\n"
    "v_lshl_add_u32 v66, s21, 6, v188\n"
    "v_cndmask_b32_e64 v189, 0, 6, vcc\n"
    "v_add_lshl_u32 v66, v66, v189, 1\n"
    "v_mov_b32 v189, 0x40000\n"
    "v_cmp_gt_u32 vcc, 26, v1\n"
    "v_cndmask_b32 v66, v66, v189, s[60:61]\n"
    "v_lshlrev_b32 v67, 6, v1\n"
    "v_cndmask_b32 v67, v189, v67, vcc\n"
    "v_lshlrev_b32 v68, 4, v184\n"
    "v_add_u32 v68, 40960, v68\n"
    "v_lshlrev_b32 v69, 1, v1\n"
    "v_lshl_add_u32 v190, v186, 3, v190\n"
    "v_and_b32 v188, 1, v184\n"
    "v_lshl_add_u32 v188, v188, 2, v191\n"
    "v_cmp_eq_u32 vcc, 7, v188\n"
    "v_lshlrev_b32 v188, 3, v188\n"
    "v_lshl_add_u32 v70, v190, 6, v188\n"
    "v_add_u32 v71, s28, v70\n"
    "v_mov_b32 v188, 38912\n"
    "v_cndmask_b32 v71, v71, v188, vcc\n"
    "v_cmp_gt_u32 vcc, 3, v184\n"
    "s_waitcnt vmcnt(5) lgkmcnt(0)\n"
    "s_barrier\n"
    "ds_read2_b64 v[176:179], v68 offset0:13 offset1:14\n"
    "ds_read_u16 v68, v69 offset:41012\n"
    "ds_read_u16 v69, v69 offset:40960\n"
    "ds_read_b64_tr_b16 v[112:113], v64 offset:0\n"
    "ds_read_b64_tr_b16 v[114:115], v65 offset:0\n"
    "ds_read_b64_tr_b16 v[116:117], v64 offset:208\n"
    "ds_read_b64_tr_b16 v[118:119], v65 offset:208\n"
    "ds_read_b64_tr_b16 v[120:121], v64 offset:416\n"
    "ds_read_b64_tr_b16 v[122:123], v65 offset:416\n"
    "ds_read_b64_tr_b16 v[124:125], v64 offset:624\n"
    "ds_read_b64_tr_b16 v[126:127], v65 offset:624\n"
    "ds_read_b64_tr_b16 v[128:129], v64 offset:832\n"
    "ds_read_b64_tr_b16 v[130:131], v65 offset:832\n"
    "ds_read_b64_tr_b16 v[132:133], v64 offset:1040\n"
    "ds_read_b64_tr_b16 v[134:135], v65 offset:1040\n"
    "ds_read_b64_tr_b16 v[136:137], v64 offset:1248\n"
    "ds_read_b64_tr_b16 v[138:139], v65 offset:1248\n"
    "s_waitcnt lgkmcnt(14)\n"
    "v_cndmask_b32 v177, 0, v177, vcc\n"
    "v_cndmask_b32 v178, 0, v178, vcc\n"
    "v_cndmask_b32 v179, 0, v179, vcc\n"
    "ds_write_b16 v67, v68 offset:36920\n"
    "s_waitcnt lgkmcnt(13)\n"
    "v_mfma_f32_16x16x32_bf16 v[144:147], v[176:179], v[112:115], 0\n"
    "s_waitcnt lgkmcnt(11)\n"
    "v_mfma_f32_16x16x32_bf16 v[148:151], v[176:179], v[116:119], 0\n"
    "s_waitcnt lgkmcnt(9)\n"
    "v_mfma_f32_16x16x32_bf16 v[152:155], v[176:179], v[120:123], 0\n"
    "s_waitcnt lgkmcnt(7)\n"
    "v_mfma_f32_16x16x32_bf16 v[156:159], v[176:179], v[124:127], 0\n"
    "s_waitcnt lgkmcnt(5)\n"
    "v_mfma_f32_16x16x32_bf16 v[160:163], v[176:179], v[128:131], 0\n"
    "s_waitcnt lgkmcnt(3)\n"
    "v_mfma_f32_16x16x32_bf16 v[164:167], v[176:179], v[132:135], 0\n"
    "s_waitcnt lgkmcnt(1)\n"
    "v_mfma_f32_16x16x32_bf16 v[168:171], v[176:179], v[136:139], 0\n"
    "s_waitcnt vmcnt(3)\n"
    "ds_read_b64_tr_b16 v[80:81], v71 offset:0\n"
    "ds_read_b64_tr_b16 v[82:83], v71 offset:256\n"
    "ds_read_b64_tr_b16 v[84:85], v71 offset:1024\n"
    "ds_read_b64_tr_b16 v[86:87], v71 offset:1280\n"
    "v_cvt_pk_bf16_f32 v144, v144, v144\n"
    "ds_write_b16 v66, v144 offset:36864\n"
    "v_cvt_pk_bf16_f32 v148, v148, v148\n"
    "ds_write_b16 v66, v148 offset:37120\n"
    "v_cvt_pk_bf16_f32 v152, v152, v152\n"
    "ds_write_b16 v66, v152 offset:37376\n"
    "v_cvt_pk_bf16_f32 v156, v156, v156\n"
    "ds_write_b16 v66, v156 offset:37632\n"
    "v_cvt_pk_bf16_f32 v160, v160, v160\n"
    "ds_write_b16 v66, v160 offset:37888\n"
    "v_cvt_pk_bf16_f32 v164, v164, v164\n"
    "ds_write_b16 v66, v164 offset:38144\n"
    "s_cmp_lg_u32 s21, 0\n"
    "s_cbranch_scc1 .Lxd_on49\n"
    "v_cvt_pk_bf16_f32 v168, v168, v168\n"
    "ds_write_b16 v66, v168 offset:38400\n"
    ".Lxd_on49:\n"
    "v_lshlrev_b32 v177, 16, v69\n"
    "s_waitcnt lgkmcnt(0)\n"
    "s_barrier\n"
    "ds_read_b64_tr_b16 v[72:73], v70 offset:36864\n"
    "ds_read_b64_tr_b16 v[74:75], v70 offset:37120\n"
    "ds_read_b64_tr_b16 v[76:77], v70 offset:37888\n"
    "ds_read_b64_tr_b16 v[78:79], v70 offset:38144\n"
    "s_lshl_b32 s30, s20, 12\n"
    "s_add_u32 s30, s30, 65536\n"
    "v_and_b32 v188, 31, v0\n"
    "v_mul_u32_u24 v192, 528, v186\n"
    "v_lshl_add_u32 v192, v188, 2, v192\n"
    "v_add_u32 v192, s30, v192\n"
    "s_lshl_b32 s40, s35, 6\n"
    "s_add_u32 s40, s40, s20\n"
    "s_lshl1_add_u32 s41, s20, s35\n"
    "s_add_u32 s41, s41, 120\n"
    "s_cmp_ge_u32 s20, 4\n"
    "s_cselect_b32 s40, s41, s40\n"
    "s_lshl1_add_u32 s41, s35, 384\n"
    "s_cmp_ge_u32 s20, 6\n"
    "s_cselect_b32 s40, s41, s40\n"
    "s_cselect_b32 s42, 1, 192\n"
    "s_and_b32 s41, s2, 3\n"
    "s_lshl2_add_u32 s40, s41, s40\n"
    "v_lshrrev_b32 v188, 3, v1\n"
    "v_mul_u32_u24 v188, 448, v188\n"
    "v_bfe_u32 v189, v0, 2, 1\n"
    "v_mad_u32_u24 v188, v189, s42, v188\n"
    "v_and_b32 v189, 48, v42\n"
    "v_add3_u32 v188, v188, v189, s40\n"
    "v_lshlrev_b32 v193, 4, v188\n"
    "s_waitcnt vmcnt(0) lgkmcnt(0)\n"
    ".Lxd_bD:\n"
    "v_mfma_f32_32x32x16_bf16 v[96:111], v[72:75], v[80:83], 0\n"
    "v_mfma_f32_32x32x16_bf16 v[96:111], v[80:83], v[72:75], v[96:111]\n"
    "v_mfma_f32_32x32x16_bf16 v[96:111], v[76:79], v[84:87], v[96:111]\n"
    "v_mfma_f32_32x32x16_bf16 v[96:111], v[84:87], v[76:79], v[96:111]\n"
    "v_readlane_b32 s43, v177, s34\n"
    "v_mad_u32_u16 v64, v44, 4, s30 op_sel:[0,0,0,0]\n"
    "v_mad_u32_u16 v65, v44, 4, s30 op_sel:[1,0,0,0]\n"
    "v_mad_u32_u16 v66, v45, 4, s30 op_sel:[0,0,0,0]\n"
    "v_mad_u32_u16 v67, v45, 4, s30 op_sel:[1,0,0,0]\n"
    "v_mad_u32_u16 v68, v46, 4, s30 op_sel:[0,0,0,0]\n"
    "v_mad_u32_u16 v69, v46, 4, s30 op_sel:[1,0,0,0]\n"
    "v_mad_u32_u16 v70, v47, 4, s30 op_sel:[0,0,0,0]\n"
    "v_mad_u32_u16 v71, v47, 4, s30 op_sel:[1,0,0,0]\n"
    "v_mov_b32 v184, 40944\n"
    "s_and_b32 s25, s25, 0xffffff\n"
    "s_mul_i32 s40, s25, 0x9e3779b1\n"
    "s_xor_b32 s41, s25, 0xa5c30f96\n"
    "s_lshr_b32 s40, s40, 24\n"
    "v_writelane_b32 v108, s43, 60\n"
    "ds_write2_b32 v192, v96, v97 offset0:0 offset1:33\n"
    "ds_write2_b32 v192, v98, v99 offset0:66 offset1:99\n"
    "v_add_u32 v192, 1056, v192\n"
    "ds_write2_b32 v192, v100, v101 offset0:0 offset1:33\n"
    "ds_write2_b32 v192, v102, v103 offset0:66 offset1:99\n"
    "v_add_u32 v192, 1056, v192\n"
    "ds_write2_b32 v192, v104, v105 offset0:0 offset1:33\n"
    "ds_write2_b32 v192, v106, v107 offset0:66 offset1:99\n"
    "v_add_u32 v192, 1056, v192\n"
    "ds_write2_b32 v192, v108, v109 offset0:0 offset1:33\n"
    "ds_read_b32 v56, v64\n"
    "ds_read_b32 v57, v65\n"
    "ds_read_b32 v58, v66\n"
    "ds_read_b32 v59, v67\n"
    "ds_read_b32 v60, v68\n"
    "ds_read_b32 v61, v69\n"
    "ds_read_b32 v62, v70\n"
    "ds_read_b32 v63, v71\n"
    "s_waitcnt lgkmcnt(6)\n"
    "v_pk_mul_f32 v[56:57], v[56:57], v[48:49]\n"
    "v_max3_f32 v188, |v56|, |v57|, 0\n"
    "s_waitcnt lgkmcnt(4)\n"
    "v_pk_mul_f32 v[58:59], v[58:59], v[50:51]\n"
    "v_max3_f32 v188, |v58|, |v59|, v188\n"
    "s_waitcnt lgkmcnt(2)\n"
    "v_pk_mul_f32 v[60:61], v[60:61], v[52:53]\n"
    "v_max3_f32 v188, |v60|, |v61|, v188\n"
    "s_waitcnt lgkmcnt(0)\n"
    "v_pk_mul_f32 v[62:63], v[62:63], v[54:55]\n"
    "v_max3_f32 v188, |v62|, |v63|, v188\n"
    "s_nop 1\n"
    "v_max_u32_dpp v188, v188, v188 quad_perm:[1,0,3,2] row_mask:0xf bank_mask:0xf\n"
    "s_nop 1\n"
    "v_max_u32_dpp v188, v188, v188 quad_perm:[2,3,0,1] row_mask:0xf bank_mask:0xf\n"
    "s_nop 1\n"
    "v_max_u32_dpp v188, v188, v188 row_half_mirror row_mask:0xf bank_mask:0xf\n"
    "s_nop 1\n"
    "v_max_u32_dpp v188, v188, v188 row_mirror row_mask:0xf bank_mask:0xf\n"
    "s_mov_b32 exec_lo, 0x10001\n"
    "s_mov_b32 exec_hi, 1\n"
    "ds_max_u32 v184, v188\n"
    "s_mov_b64 exec, -1\n"
    "s_add_u32 s38, s16, 1047552\n"
    "s_addc_u32 s39, s17, 0\n"
    "s_cmp_lt_u32 s23, 26\n"
    "s_cselect_b32 s43, 0xfff8fff8, 0\n"
    "s_waitcnt lgkmcnt(0)\n"
    "s_barrier\n"
    "ds_read_b32 v188, v184\n"
    "v_cmp_le_u32 vcc, 0x400, v193\n"
    "s_cmp_eq_u32 s20, 7\n"
    "s_cselect_b32 s42, 0, 48\n"
    "s_bfm_b64 s[60:61], s42, 0\n"
    "s_and_b64 s[60:61], s[60:61], vcc\n"
    "s_waitcnt lgkmcnt(0)\n"
    "v_lshrrev_b32 v188, 23, v188\n"
    "v_max_u32 v188, 40, v188\n"
    "v_min_u32 v188, 250, v188\n"
    "v_sub_u32 v190, 268, v188\n"
    "v_lshlrev_b32 v190, 23, v190\n"
    "v_pk_mul_f32 v[56:57], v[56:57], v[190:191] op_sel_hi:[1,0]\n"
    "v_cvt_pkrtz_f16_f32 v56, v56, v57\n"
    "v_add_u32 v56, 0x40004, v56\n"
    "v_and_b32 v56, s43, v56\n"
    "v_pk_mul_f32 v[58:59], v[58:59], v[190:191] op_sel_hi:[1,0]\n"
    "v_cvt_pkrtz_f16_f32 v57, v58, v59\n"
    "v_add_u32 v57, 0x40004, v57\n"
    "v_and_b32 v57, s43, v57\n"
    "v_pk_mul_f32 v[60:61], v[60:61], v[190:191] op_sel_hi:[1,0]\n"
    "v_cvt_pkrtz_f16_f32 v58, v60, v61\n"
    "v_add_u32 v58, 0x40004, v58\n"
    "v_and_b32 v58, s43, v58\n"
    "v_pk_mul_f32 v[62:63], v[62:63], v[190:191] op_sel_hi:[1,0]\n"
    "v_cvt_pkrtz_f16_f32 v59, v62, v63\n"
    "v_add_u32 v59, 0x40004, v59\n"
    "v_and_b32 v59, s43, v59\n"
    "v_lshl_or_b32 v188, v188, 8, s40\n"
    "v_mov_b32 v189, s41\n"
    "s_lshl3_add_u32 s40, s2, 40960\n"
    "v_mov_b32 v190, s40\n"
    "s_cmp_eq_u32 s20, 7\n"
    "s_cbranch_scc1 .Lxd_b3\n"
    "s_mov_b64 exec, s[60:61]\n"
    "global_store_dwordx4 v193, v[56:59], s[38:39] sc1\n"
    "s_mov_b64 exec, -1\n"
    ".Lxd_b3:\n"
    "s_waitcnt vmcnt(0)\n"
    "s_barrier\n"
    "s_cmp_lg_u32 s20, 0\n"
    "s_cbranch_scc1 .Lxd_on50\n"
    "s_mov_b64 exec, 1\n"
    "global_store_dwordx2 v190, v[188:189], s[16:17] sc1\n"
    "s_mov_b64 exec, -1\n"
    ".Lxd_on50:\n"
    "s_branch .Lxd_h1\n"
    ".Lxd_help:\n"
    "s_lshr_b32 s20, s43, 6\n"
    "v_and_b32 v1, 63, v0\n"
    "v_lshlrev_b32 v42, 4, v1\n"
    "s_lshr_b32 s41, s2, 3\n"
    "s_sub_u32 s41, s41, 1\n"
    "s_cmp_lg_u32 s20, 7\n"
    "s_cbranch_scc1 .Lxd_on52\n"
    "s_mul_i32 s40, s41, 60\n"
    "v_min_u32 v7, 59, v1\n"
    "v_add_lshl_u32 v7, v7, s40, 7\n"
    "s_getpc_b64 s[60:61]\n"
    ".Lxd_pc51:\n"
    "s_sub_u32 s60, s60, .Lxd_pc51-.Lxd_top\n"
    "s_subb_u32 s61, s61, 0\n"
    "v_min_u32 v7, .Lxd_end-.Lxd_top-4, v7\n"
    "global_load_dword v7, v7, s[60:61]\n"
    ".Lxd_on52:\n"
    "v_mul_u32_u24 v68, 52, v1\n"
    "v_lshrrev_b32 v68, 8, v68\n"
    "v_mad_i32_i24 v69, v68, -5, v1\n"
    "s_and_b32 s40, s2, 0xf8\n"
    "s_add_u32 s40, s40, s20\n"
    "s_mul_i32 s40, s40, 13\n"
    "s_sub_u32 s40, s40, 104\n"
    "v_add_u32 v68, s40, v68\n"
    "v_min_u32 v68, 0x1ff, v68\n"
    "v_lshrrev_b32 v70, 8, v68\n"
    "s_movk_i32 s40, 1792\n"
    "v_mad_u32_u24 v68, v70, s40, v68\n"
    "s_and_b32 s40, s2, 7\n"
    "v_lshl_add_u32 v68, s40, 8, v68\n"
    "v_mul_u32_u24 v68, 104, v68\n"
    "s_lshl_b32 s40, s2, 2\n"
    "s_lshr_b32 s40, 0xa8864220, s40\n"
    "s_and_b32 s40, s40, 14\n"
    "s_lshl_b32 s40, s40, 1\n"
    "v_add3_u32 v70, v69, s40, 2\n"
    "v_subrev_u32 v71, 24, v70\n"
    "v_min_u32 v70, v70, v71\n"
    "v_lshl_add_u32 v70, v70, 2, v68\n"
    "s_waitcnt lgkmcnt(0)\n"
    "s_load_dwordx4 s[68:71], s[0:1], 64\n"
    "global_load_dword v60, v70, s[4:5]\n"
    "v_add3_u32 v70, v69, s40, 7\n"
    "v_subrev_u32 v71, 24, v70\n"
    "v_min_u32 v70, v70, v71\n"
    "v_lshl_add_u32 v70, v70, 2, v68\n"
    "global_load_dword v61, v70, s[4:5]\n"
    "v_add3_u32 v70, v69, s40, 12\n"
    "v_subrev_u32 v71, 24, v70\n"
    "v_min_u32 v70, v70, v71\n"
    "v_lshl_add_u32 v70, v70, 2, v68\n"
    "global_load_dword v62, v70, s[4:5]\n"
    "v_add3_u32 v70, v69, s40, 17\n"
    "v_subrev_u32 v71, 24, v70\n"
    "v_min_u32 v70, v70, v71\n"
    "v_lshl_add_u32 v70, v70, 2, v68\n"
    "global_load_dword v63, v70, s[4:5]\n"
    "v_add3_u32 v70, v69, s40, 22\n"
    "v_subrev_u32 v71, 24, v70\n"
    "v_min_u32 v70, v70, v71\n"
    "v_add_u32 v71, 22, v69\n"
    "v_cmp_lt_u32 vcc, 23, v71\n"
    "v_min_u32 v71, 25, v71\n"
    "s_nop 0\n"
    "v_cndmask_b32 v70, v70, v71, vcc\n"
    "v_lshl_add_u32 v70, v70, 2, v68\n"
    "global_load_dword v64, v70, s[4:5]\n"
    "s_cmp_lg_u32 s20, 6\n"
    "s_cbranch_scc1 .Lxd_on53\n"
    "v_min_u32 v12, 25, v1\n"
    "s_mov_b32 m0, 159744\n"
    "v_lshlrev_b32 v12, 7, v12\n"
    "global_load_lds_dword v12, s[10:11]\n"
    "global_load_lds_dword v12, s[18:19]\n"
    ".Lxd_on53:\n"
    ".Lxd_h1:\n"
    "s_lshr_b32 s3, s2, 4\n"
    "s_mul_i32 s40, s3, 3\n"
    "s_add_u32 s47, s2, s40\n"
    "s_and_b32 s47, s47, 15\n"
    "s_lshr_b32 s21, s20, 2\n"
    "s_lshl1_add_u32 s22, s21, s20\n"
    "s_and_b32 s22, s22, 3\n"
    "s_lshl_b32 s58, s21, 8\n"
    "s_add_u32 s58, s58, 150528\n"
    "s_mov_b32 s40, s47\n"
    "s_mul_i32 s54, s40, 512\n"
    "s_add_u32 s54, s54, 2\n"
    "s_lshl_b32 s40, s40, 7\n"
    "s_add_u32 s40, s40, 32768\n"
    "s_add_u32 s52, s16, s40\n"
    "s_addc_u32 s53, s17, 0\n"
    "s_mov_b32 s57, 0x7fff7fff\n"
    "s_mov_b32 s56, 0\n"
    "s_mov_b32 s27, 0\n"
    "s_mov_b32 s55, 9\n"
    "s_mov_b32 s44, 0\n"
    "s_mov_b32 s46, 0\n"
    "v_lshrrev_b32 v17, 3, v1\n"
    "v_mul_u32_u24 v17, 12, v17\n"
    "v_and_b32 v19, 7, v1\n"
    "v_lshlrev_b32 v19, 4, v19\n"
    "v_lshrrev_b32 v18, 5, v1\n"
    "v_lshlrev_b32 v18, 2, v18\n"
    "v_and_b32 v20, 31, v1\n"
    "v_lshlrev_b32 v20, 2, v20\n"
    "s_lshl2_add_u32 s40, s22, s58\n"
    "s_sub_u32 s40, s40, 20\n"
    "v_lshl_add_u32 v25, v1, 18, s40\n"
    "s_lshl1_add_u32 s23, s3, s21\n"
    "s_mul_i32 s23, s23, 6\n"
    "s_add_u32 s23, s23, s22\n"
    "s_add_u32 s23, s23, 78\n"
    "v_mov_b32 v24, s23\n"
    "v_lshlrev_b32 v13, 2, v0\n"
    "v_readfirstlane_b32 s40, v24\n"
    "s_or_b32 s40, s40, s44\n"
    "s_lshl1_add_u32 s41, s40, s54\n"
    "s_cmpk_lt_u32 s40, 256\n"
    "s_cselect_b32 s41, s41, 2\n"
    "v_mov_b32 v12, s41\n"
    "s_mul_i32 s41, s41, 52\n"
    "s_add_u32 s38, s4, s41\n"
    "s_addc_u32 s39, s5, 0\n"
    "global_load_dwordx3 v[36:38], v17, s[38:39] offset:-104\n"
    "global_load_dword v41, v18, s[38:39] offset:-8\n"
    "s_setprio 0\n"
    "v_mov_b32 v21, 0\n"
    "v_add_u32 v13, 149504, v13\n"
    "ds_write_b32 v13, v21\n"
    "s_cmp_lt_u32 s2, 8\n"
    "s_cbranch_scc1 .Lxd_hp54\n"
    "s_lshl_b32 s40, s2, 2\n"
    "s_lshr_b32 s40, 0xa8864220, s40\n"
    "s_and_b32 s40, s40, 14\n"
    "s_mul_i32 s40, s40, 3328\n"
    "s_and_b32 s41, s2, 0xf8\n"
    "s_add_u32 s41, s41, s20\n"
    "s_mul_i32 s41, s41, 42\n"
    "s_add_u32 s40, s40, s41\n"
    "s_add_u32 s40, s40, 4656\n"
    "v_min_u32 v68, 41, v1\n"
    "v_add_lshl_u32 v68, v68, s40, 7\n"
    "s_mov_b32 m0, 159744\n"
    "s_nop 0\n"
    "global_load_lds_dword v68, s[6:7]\n"
    "s_waitcnt vmcnt(3)\n"
    "s_mov_b32 m0, 159744\n"
    "v_lshlrev_b32 v60, 7, v60\n"
    "global_load_lds_dword v60, s[8:9]\n"
    "s_waitcnt vmcnt(2)\n"
    "s_branch .Lxd_h3\n"
    ".Lxd_hp54:\n"
    "s_waitcnt vmcnt(0)\n"
    ".Lxd_h3:\n"
    "s_setprio 0\n"
    "v_lshl_or_b32 v36, v36, 7, v19\n"
    "v_lshl_or_b32 v37, v37, 7, v19\n"
    "v_lshl_or_b32 v38, v38, 7, v19\n"
    "v_lshl_or_b32 v41, v41, 7, v20\n"
    "global_load_dwordx4 v[28:31], v36, s[8:9]\n"
    "global_load_dwordx4 v[32:35], v37, s[8:9]\n"
    "global_load_dword v40, v41, s[8:9]\n"
    "global_load_dwordx4 v[36:39], v38, s[8:9]\n"
    "s_waitcnt lgkmcnt(0)\n"
    "s_barrier\n"
    "ds_write_b32 v25, v12 offset:20\n"
    "s_load_dword s26, s[68:69], 0\n"
    "s_lshl_b32 s43, s21, 16\n"
    "v_lshrrev_b32 v12, 4, v1\n"
    "v_bfe_u32 v13, v1, 2, 2\n"
    "v_and_b32 v21, 3, v1\n"
    "s_mul_i32 s40, s22, 0x2020\n"
    "s_and_b32 s40, s40, 0x4060\n"
    "s_add_u32 s40, s40, s43\n"
    "v_lshl_add_u32 v21, v21, 3, s40\n"
    "s_movk_i32 s41, 144\n"
    "v_add_u32 v22, v12, v13\n"
    "v_and_b32 v22, 3, v22\n"
    "v_mad_u32_u24 v4, v22, s41, v21\n"
    "v_cmp_gt_u32 vcc, 2, v13\n"
    "v_add_u32 v24, 22, v13\n"
    "v_lshlrev_b32 v13, 3, v13\n"
    "v_cndmask_b32 v22, v24, v22, vcc\n"
    "v_mad_u32_u24 v3, v22, s41, v21\n"
    "v_cmp_eq_u32 vcc, 3, v12\n"
    "v_xor_b32 v22, 0, v12\n"
    "v_and_b32 v22, 1, v22\n"
    "v_lshlrev_b32 v22, 1, v22\n"
    "v_lshl_add_u32 v22, v12, 3, v22\n"
    "s_mov_b32 s40, 0x05040100\n"
    "v_bfe_u32 v24, s40, v13, 8\n"
    "v_add_u32 v22, v22, v24\n"
    "s_mov_b32 s40, 0x1a191818\n"
    "v_bfe_u32 v24, s40, v13, 8\n"
    "v_cndmask_b32 v22, v22, v24, vcc\n"
    "v_mad_u32_u24 v5, v22, s41, v21\n"
    "v_xor_b32 v22, 1, v12\n"
    "s_cmp_lt_u32 s2, 8\n"
    "s_cbranch_scc1 .Lxd_hp55\n"
    "s_mov_b32 m0, 159744\n"
    "v_lshlrev_b32 v61, 7, v61\n"
    "global_load_lds_dword v61, s[8:9]\n"
    "s_mov_b32 m0, 159744\n"
    "v_lshlrev_b32 v62, 7, v62\n"
    "global_load_lds_dword v62, s[8:9]\n"
    ".Lxd_hp55:\n"
    "v_and_b32 v22, 1, v22\n"
    "v_lshlrev_b32 v22, 1, v22\n"
    "v_lshl_add_u32 v22, v12, 3, v22\n"
    "s_mov_b32 s40, 0x05040100\n"
    "v_bfe_u32 v24, s40, v13, 8\n"
    "v_add_u32 v22, v22, v24\n"
    "s_mov_b32 s40, 0x1a191918\n"
    "v_bfe_u32 v24, s40, v13, 8\n"
    "v_cndmask_b32 v22, v22, v24, vcc\n"
    "v_mad_u32_u24 v6, v22, s41, v21\n"
    "v_bfrev_b32 v14, v12\n"
    "v_lshrrev_b32 v14, 26, v14\n"
    "s_add_u32 s40, s22, 3\n"
    "s_and_b32 s40, s40, 3\n"
    "s_lshl2_add_u32 s40, s40, s22\n"
    "s_add_u32 s40, s40, s58\n"
    "s_lshl_b32 s40, s40, 2\n"
    "s_add_u32 s40, s40, -450048\n"
    "v_add_u32 v14, s40, v14\n"
    "v_and_b32 v2, 1, v1\n"
    "v_mul_u32_u24 v2, 0xaaaa, v2\n"
    "v_add_u32 v2, 0x4444, v2\n"
    "v_mad_u32_u24 v15, v17, 36, v19\n"
    "v_mad_u32_u24 v16, v18, 36, v20\n"
    "s_add_u32 s41, s22, 2\n"
    "v_lshrrev_b32 v12, 5, v19\n"
    "v_sub_u32 v12, 1, v12\n"
    "v_and_b32 v12, 3, v12\n"
    "v_add_lshl_u32 v12, v12, s41, 12\n"
    "v_and_b32 v12, 0x7000, v12\n"
    "v_add3_u32 v15, v12, v15, s43\n"
    "v_lshrrev_b32 v12, 5, v20\n"
    "v_sub_u32 v12, 1, v12\n"
    "v_and_b32 v12, 3, v12\n"
    "v_add_lshl_u32 v12, v12, s41, 12\n"
    "v_and_b32 v12, 0x7000, v12\n"
    "v_add3_u32 v16, v12, v16, s43\n"
    "v_add_u32 v16, 3456, v16\n"
    "s_lshl3_add_u32 s40, s2, 4096\n"
    "s_lshl_b32 s41, s2, 2\n"
    "v_lshlrev_b32 v11, 3, v1\n"
    "v_add_u32 v11, 40960, v11\n"
    "v_writelane_b32 v11, s40, 8\n"
    "v_writelane_b32 v11, s41, 9\n"
    "v_add_u32 v43, 4096, v42\n"
    "s_cmp_lt_u32 s2, 8\n"
    "s_cbranch_scc1 .Lxd_hp56\n"
    "s_waitcnt vmcnt(2)\n"
    "s_branch .Lxd_h2\n"
    ".Lxd_hp56:\n"
    "s_waitcnt vmcnt(0)\n"
    ".Lxd_h2:\n"
    "s_cmp_lg_u32 s20, 7\n"
    "s_cbranch_scc1 .Lxd_on57\n"
    "s_mov_b64 exec, 0x3ff\n"
    "global_load_dwordx2 v[26:27], v11, s[16:17] sc1\n"
    "s_mov_b64 exec, -1\n"
    ".Lxd_on57:\n"
    "s_movk_i32 s43, 15744\n"
    "v_pk_maximum3_f16 v21, v28, v28, s43 op_sel_hi:[1,1,0] neg_lo:[0,1,0] neg_hi:[0,1,0]\n"
    "v_pk_maximum3_f16 v21, v29, v29, v21 neg_lo:[0,1,0] neg_hi:[0,1,0]\n"
    "v_pk_maximum3_f16 v21, v30, v30, v21 neg_lo:[0,1,0] neg_hi:[0,1,0]\n"
    "v_pk_maximum3_f16 v21, v31, v31, v21 neg_lo:[0,1,0] neg_hi:[0,1,0]\n"
    "v_pk_maximum3_f16 v21, v32, v32, v21 neg_lo:[0,1,0] neg_hi:[0,1,0]\n"
    "v_pk_maximum3_f16 v21, v33, v33, v21 neg_lo:[0,1,0] neg_hi:[0,1,0]\n"
    "v_pk_maximum3_f16 v21, v34, v34, v21 neg_lo:[0,1,0] neg_hi:[0,1,0]\n"
    "v_pk_maximum3_f16 v21, v35, v35, v21 neg_lo:[0,1,0] neg_hi:[0,1,0]\n"
    "v_pk_maximum3_f16 v21, v36, v36, v21 neg_lo:[0,1,0] neg_hi:[0,1,0]\n"
    "v_pk_maximum3_f16 v21, v37, v37, v21 neg_lo:[0,1,0] neg_hi:[0,1,0]\n"
    "v_pk_maximum3_f16 v21, v38, v38, v21 neg_lo:[0,1,0] neg_hi:[0,1,0]\n"
    "v_pk_maximum3_f16 v21, v39, v39, v21 neg_lo:[0,1,0] neg_hi:[0,1,0]\n"
    "v_pk_maximum3_f16 v21, v40, v40, v21 neg_lo:[0,1,0] neg_hi:[0,1,0]\n"
    "v_pk_max_u16 v21, v21, v21 op_sel:[0,1] op_sel_hi:[1,0]\n"
    "s_nop 1\n"
    "v_max_u32_dpp v21, v21, v21 quad_perm:[1,0,3,2] row_mask:0xf bank_mask:0xf\n"
    "s_nop 1\n"
    "v_max_u32_dpp v21, v21, v21 quad_perm:[2,3,0,1] row_mask:0xf bank_mask:0xf\n"
    "s_nop 1\n"
    "v_max_u32_dpp v21, v21, v21 row_half_mirror row_mask:0xf bank_mask:0xf\n"
    "s_nop 1\n"
    "v_max_u32_dpp v21, v21, v21 row_mirror row_mask:0xf bank_mask:0xf\n"
    "s_nop 1\n"
    "v_max_u32_dpp v21, v21, v21 row_bcast:15 row_mask:0xa bank_mask:0xf\n"
    "s_nop 1\n"
    "v_max_u32_dpp v21, v21, v21 row_bcast:31 row_mask:0xc bank_mask:0xf\n"
    "s_nop 1\n"
    "v_readlane_b32 s47, v21, 63\n"
    "s_and_b32 s47, s47, 0x7f80\n"
    "s_sub_u32 s50, s47, 2816\n"
    "s_mul_i32 s51, s50, -8\n"
    "s_lshr_b32 s47, s47, 3\n"
    "v_pk_maximum3_f16 v21, v28, v28, s50 op_sel_hi:[1,1,0] neg_lo:[0,1,0] neg_hi:[0,1,0]\n"
    "v_pk_mad_u16 v21, v21, 8, s51 op_sel_hi:[1,0,0]\n"
    "v_bfi_b32 v28, s57, v21, v28\n"
    "v_pk_maximum3_f16 v12, v29, v29, s50 op_sel_hi:[1,1,0] neg_lo:[0,1,0] neg_hi:[0,1,0]\n"
    "v_pk_mad_u16 v12, v12, 8, s51 op_sel_hi:[1,0,0]\n"
    "v_bfi_b32 v29, s57, v12, v29\n"
    "v_pk_maximum3_f16 v21, v30, v30, s50 op_sel_hi:[1,1,0] neg_lo:[0,1,0] neg_hi:[0,1,0]\n"
    "v_pk_mad_u16 v21, v21, 8, s51 op_sel_hi:[1,0,0]\n"
    "v_bfi_b32 v30, s57, v21, v30\n"
    "v_pk_maximum3_f16 v12, v31, v31, s50 op_sel_hi:[1,1,0] neg_lo:[0,1,0] neg_hi:[0,1,0]\n"
    "v_pk_mad_u16 v12, v12, 8, s51 op_sel_hi:[1,0,0]\n"
    "v_bfi_b32 v31, s57, v12, v31\n"
    "v_pk_maximum3_f16 v21, v32, v32, s50 op_sel_hi:[1,1,0] neg_lo:[0,1,0] neg_hi:[0,1,0]\n"
    "v_pk_mad_u16 v21, v21, 8, s51 op_sel_hi:[1,0,0]\n"
    "v_bfi_b32 v32, s57, v21, v32\n"
    "v_pk_maximum3_f16 v12, v33, v33, s50 op_sel_hi:[1,1,0] neg_lo:[0,1,0] neg_hi:[0,1,0]\n"
    "v_pk_mad_u16 v12, v12, 8, s51 op_sel_hi:[1,0,0]\n"
    "v_bfi_b32 v33, s57, v12, v33\n"
    "v_pk_maximum3_f16 v21, v34, v34, s50 op_sel_hi:[1,1,0] neg_lo:[0,1,0] neg_hi:[0,1,0]\n"
    "v_pk_mad_u16 v21, v21, 8, s51 op_sel_hi:[1,0,0]\n"
    "v_bfi_b32 v34, s57, v21, v34\n"
    "v_pk_maximum3_f16 v12, v35, v35, s50 op_sel_hi:[1,1,0] neg_lo:[0,1,0] neg_hi:[0,1,0]\n"
    "v_pk_mad_u16 v12, v12, 8, s51 op_sel_hi:[1,0,0]\n"
    "v_bfi_b32 v35, s57, v12, v35\n"
    "v_pk_maximum3_f16 v21, v36, v36, s50 op_sel_hi:[1,1,0] neg_lo:[0,1,0] neg_hi:[0,1,0]\n"
    "v_pk_mad_u16 v21, v21, 8, s51 op_sel_hi:[1,0,0]\n"
    "v_bfi_b32 v36, s57, v21, v36\n"
    "v_pk_maximum3_f16 v12, v37, v37, s50 op_sel_hi:[1,1,0] neg_lo:[0,1,0] neg_hi:[0,1,0]\n"
    "v_pk_mad_u16 v12, v12, 8, s51 op_sel_hi:[1,0,0]\n"
    "v_bfi_b32 v37, s57, v12, v37\n"
    "v_pk_maximum3_f16 v21, v38, v38, s50 op_sel_hi:[1,1,0] neg_lo:[0,1,0] neg_hi:[0,1,0]\n"
    "v_pk_mad_u16 v21, v21, 8, s51 op_sel_hi:[1,0,0]\n"
    "v_bfi_b32 v38, s57, v21, v38\n"
    "v_pk_maximum3_f16 v12, v39, v39, s50 op_sel_hi:[1,1,0] neg_lo:[0,1,0] neg_hi:[0,1,0]\n"
    "v_pk_mad_u16 v12, v12, 8, s51 op_sel_hi:[1,0,0]\n"
    "v_bfi_b32 v39, s57, v12, v39\n"
    "v_pk_maximum3_f16 v21, v40, v40, s50 op_sel_hi:[1,1,0] neg_lo:[0,1,0] neg_hi:[0,1,0]\n"
    "v_pk_mad_u16 v21, v21, 8, s51 op_sel_hi:[1,0,0]\n"
    "v_bfi_b32 v40, s57, v21, v40\n"
    "s_sub_u32 s66, 4112, s47\n"
    "s_lshl_b32 s66, s66, 19\n"
    "v_cvt_pkrtz_f16_f32 v12, s66, s66\n"
    "v_lshl_or_b32 v21, v12, 16, s47\n"
    "ds_write_b128 v15, v[28:31] offset:0\n"
    "ds_write_b128 v15, v[32:35] offset:144\n"
    "ds_write_b128 v15, v[36:39] offset:288\n"
    "ds_write2_b32 v16, v40, v12 offset1:72\n"
    "ds_or_b32 v25, v21 offset:532\n"
    "s_cmp_lt_u32 s22, 2\n"
    "s_cselect_b32 s40, 16, 0\n"
    "v_add_u32 v25, s40, v25\n"
    "s_add_u32 s23, s23, 4\n"
    "v_mov_b32 v24, s23\n"
    "s_mov_b32 s36, 0\n"
    "v_mov_b32 v69, v43\n"
    "v_mov_b32 v68, v42\n"
    "s_cmp_lg_u32 s22, 0\n"
    "s_cbranch_scc1 .Lxd_on58\n"
    "v_readfirstlane_b32 s40, v24\n"
    "s_or_b32 s40, s40, s44\n"
    "s_lshl1_add_u32 s41, s40, s54\n"
    "s_cmpk_lt_u32 s40, 256\n"
    "s_cselect_b32 s41, s41, 2\n"
    "v_mov_b32 v12, s41\n"
    "s_mul_i32 s41, s41, 52\n"
    "s_add_u32 s38, s4, s41\n"
    "s_addc_u32 s39, s5, 0\n"
    "ds_write_b32 v25, v12 offset:20\n"
    "global_load_dwordx3 v[36:38], v17, s[38:39] offset:-104\n"
    "global_load_dword v41, v18, s[38:39] offset:-8\n"
    ".Lxd_on58:\n"
    "s_cmp_lg_u32 s20, 7\n"
    "s_cbranch_scc1 .Lxd_on60\n"
    "s_waitcnt vmcnt(0)\n"
    "v_readlane_b32 s25, v26, 9\n"
    "v_readlane_b32 s40, v26, 8\n"
    "v_readlane_b32 s41, v27, 8\n"
    "s_mul_i32 s42, s25, 0x9e3779b1\n"
    "s_lshr_b32 s42, s42, 24\n"
    "s_xor_b32 s42, s42, s40\n"
    "s_and_b32 s42, s42, 0xff\n"
    "s_xor_b32 s41, s41, s25\n"
    "s_xor_b32 s41, s41, 0xa5c30f96\n"
    "s_or_b32 s42, s41, s42\n"
    "s_lshr_b32 s41, s40, 8\n"
    "s_mul_i32 s40, s41, 0x9e3779b1\n"
    "s_xor_b32 s41, s41, 0xa5c30f96\n"
    "s_lshr_b32 s40, s40, 24\n"
    "v_xor_b32 v13, s40, v26\n"
    "v_xor_b32 v22, s41, v27\n"
    "v_lshl_or_b32 v13, v13, 24, v22\n"
    "v_cmp_ne_u32 s[66:67], 0, v13\n"
    "s_and_b32 s66, s66, 0xff\n"
    "s_or_b32 s42, s42, s66\n"
    "s_cmp_eq_u32 s20, 7\n"
    "s_cselect_b32 s42, s42, 0\n"
    "s_cmp_lg_u32 s42, 0\n"
    "s_cbranch_scc1 .Lxd_pv59\n"
    ".Lxd_pv59r:\n"
    "v_lshrrev_b32 v22, 8, v26\n"
    "v_mov_b32 v26, s40\n"
    "v_mov_b32 v27, s41\n"
    "v_add_u32_dpp v22, v22, v22 row_shl:4 row_mask:0xf bank_mask:0xf\n"
    "v_add_u32_e64 v43, s25, 1\n"
    "v_lshlrev_b32 v8, 2, v1\n"
    "v_max_u32_dpp v13, v22, v22 quad_perm:[1,0,3,2] row_mask:0xf bank_mask:0xf\n"
    "v_add_u32 v8, 154112, v8\n"
    "s_nop 0\n"
    "v_max_u32_dpp v13, v13, v13 quad_perm:[2,3,0,1] row_mask:0xf bank_mask:0xf\n"
    "v_sub_u32 v22, v22, v13\n"
    "v_subrev_u32 v13, 818, v13\n"
    "v_ldexp_f32 v22, 1.0, v22\n"
    "s_mov_b64 exec, 1\n"
    "ds_write_b64 v8, v[26:27]\n"
    "s_mov_b64 exec, 15\n"
    "ds_write2_b32 v8, v22, v13 offset0:4 offset1:8\n"
    "s_mov_b64 exec, 0x200\n"
    "global_store_dword v11, v43, s[16:17] sc1\n"
    "s_mov_b64 exec, -1\n"
    ".Lxd_on60:\n"
    "s_waitcnt lgkmcnt(0)\n"
    "s_barrier\n"
    "s_mul_i32 s42, s20, 5376\n"
    "s_add_u32 s40, s42, 1048576\n"
    "s_add_u32 s38, s16, s40\n"
    "s_addc_u32 s39, s17, 0\n"
    "s_add_u32 m0, s42, 98304\n"
    "s_nop 0\n"
    "global_load_lds_dwordx4 v68, s[38:39] offset:0 sc1\n"
    "global_load_lds_dwordx4 v68, s[38:39] offset:1024 sc1\n"
    "global_load_lds_dwordx4 v68, s[38:39] offset:2048 sc1\n"
    "global_load_lds_dwordx4 v68, s[38:39] offset:3072 sc1\n"
    "s_add_u32 m0, s42, 102400\n"
    "s_nop 0\n"
    "global_load_lds_dwordx4 v69, s[38:39] offset:0 sc1\n"
    "global_load_lds_dwordx4 v69, s[38:39] offset:256 sc1\n"
    "s_getpc_b64 s[10:11]\n"
    ".Lxd_en61:\n"
    "s_mov_b32 s40, .Lxd_ph1-.Lxd_en61\n"
    "s_cmp_eq_u32 s22, 1\n"
    "s_cselect_b32 s40, .Lxd_ph0-.Lxd_en61, s40\n"
    "s_cmp_eq_u32 s22, 2\n"
    "s_cselect_b32 s40, .Lxd_pl3-.Lxd_en61, s40\n"
    "s_cmp_eq_u32 s22, 3\n"
    "s_cselect_b32 s40, .Lxd_pl2-.Lxd_en61, s40\n"
    "s_ashr_i32 s41, s40, 31\n"
    "s_add_u32 s10, s10, s40\n"
    "s_addc_u32 s11, s11, s41\n"
    "v_mov_b32 v13, 154112\n"
    "ds_read_b64 v[26:27], v13\n"
    "v_add_u32 v42, 98304, v42\n"
    "v_mov_b32 v13, s58\n"
    "s_sub_u32 s40, 3, s22\n"
    "s_and_b32 s40, s40, 3\n"
    "v_lshl_add_u32 v7, s40, 12, v3\n"
    "v_lshl_add_u32 v8, s40, 12, v4\n"
    "v_lshl_add_u32 v9, s40, 12, v5\n"
    "v_lshl_add_u32 v10, s40, 12, v6\n"
    "v_bfe_u32 v21, v1, 4, 1\n"
    "v_lshlrev_b32 v21, 2, v21\n"
    "v_add_u32 v21, 154112, v21\n"
    "ds_read_b32 v0, v21 offset:16\n"
    "ds_read_b32 v23, v21 offset:24\n"
    "ds_read_b32 v22, v13 offset:512\n"
    "ds_read_b64_tr_b16 v[44:45], v9 offset:0\n"
    "ds_read_b64_tr_b16 v[46:47], v10 offset:0\n"
    "ds_read_b64_tr_b16 v[48:49], v7 offset:0\n"
    "ds_read_b64_tr_b16 v[50:51], v8 offset:576\n"
    "ds_read_b64_tr_b16 v[52:53], v8 offset:1152\n"
    "ds_read_b64_tr_b16 v[54:55], v8 offset:1728\n"
    "ds_read_b64_tr_b16 v[56:57], v8 offset:2304\n"
    "ds_read_b64_tr_b16 v[58:59], v8 offset:2880\n"
    "v_mov_b64 v[88:89], 0\n"
    "v_mov_b64 v[90:91], 0\n"
    "s_waitcnt vmcnt(0)\n"
    "s_barrier\n"
    "ds_read_b128 v[92:95], v42 offset:0\n"
    "ds_read_b128 v[96:99], v42 offset:1024\n"
    "ds_read_b128 v[100:103], v42 offset:2048\n"
    "ds_read_b128 v[104:107], v42 offset:3072\n"
    "ds_read_b128 v[108:111], v42 offset:4096\n"
    "ds_read_b128 v[112:115], v42 offset:5120\n"
    "ds_read_b128 v[116:119], v42 offset:6144\n"
    "ds_read_b128 v[120:123], v42 offset:7168\n"
    "ds_read_b128 v[124:127], v42 offset:8192\n"
    "ds_read_b128 v[128:131], v42 offset:9216\n"
    "ds_read_b128 v[132:135], v42 offset:10240\n"
    "ds_read_b128 v[136:139], v42 offset:11264\n"
    "ds_read_b128 v[140:143], v42 offset:12288\n"
    "ds_read_b128 v[144:147], v42 offset:13312\n"
    "ds_read_b128 v[148:151], v42 offset:14336\n"
    "ds_read_b128 v[152:155], v42 offset:15360\n"
    "ds_read_b128 v[156:159], v42 offset:16384\n"
    "ds_read_b128 v[160:163], v42 offset:17408\n"
    "ds_read_b128 v[164:167], v42 offset:18432\n"
    "ds_read_b128 v[168:171], v42 offset:19456\n"
    "ds_read_b128 v[172:175], v42 offset:20480\n"
    "ds_read_b128 v[176:179], v42 offset:21504\n"
    "ds_read_b128 v[180:183], v42 offset:22528\n"
    "ds_read_b128 v[184:187], v42 offset:23552\n"
    "ds_read_b128 v[188:191], v42 offset:24576\n"
    "ds_read_b128 v[192:195], v42 offset:25600\n"
    "ds_read_b128 v[196:199], v42 offset:26624\n"
    "ds_read_b128 v[200:203], v42 offset:27648\n"
    "ds_read_b128 v[204:207], v42 offset:28672\n"
    "ds_read_b128 v[208:211], v42 offset:29696\n"
    "ds_read_b128 v[212:215], v42 offset:30720\n"
    "ds_read_b128 v[216:219], v42 offset:31744\n"
    "ds_read_b128 v[220:223], v42 offset:32768\n"
    "ds_read_b128 v[224:227], v42 offset:33792\n"
    "ds_read_b128 v[228:231], v42 offset:34816\n"
    "ds_read_b128 v[232:235], v42 offset:35840\n"
    "ds_read_b128 v[236:239], v42 offset:36864\n"
    "ds_read_b128 v[240:243], v42 offset:37888\n"
    "ds_read_b128 v[244:247], v42 offset:38912\n"
    "ds_read_b128 v[248:251], v42 offset:39936\n"
    "ds_read_b128 v[252:255], v42 offset:40960\n"
    "s_waitcnt lgkmcnt(15)\n"
    "v_readfirstlane_b32 s45, v22\n"
    "v_readfirstlane_b32 s28, v26\n"
    "v_readfirstlane_b32 s29, v27\n"
    "s_cmp_lt_u32 s2, 8\n"
    "s_cbranch_scc1 .Lxd_hp63\n"
    "s_mov_b32 m0, 159744\n"
    "s_mov_b32 s46, 2\n"
    "v_lshlrev_b32 v26, 7, v63\n"
    "v_lshlrev_b32 v27, 7, v64\n"
    ".Lxd_hp63:\n"
    "v_pk_mul_f16 v64, v44, v46\n"
    "v_pk_mul_f16 v65, v45, v47\n"
    "v_pk_mul_f16 v66, v48, v48 op_sel_hi:[0,1]\n"
    "v_pk_mul_f16 v67, v48, v49 op_sel_hi:[0,1]\n"
    "v_pk_mul_f16 v60, v44, s45 op_sel:[0,1]\n"
    "v_pk_mul_f16 v61, v45, s45 op_sel:[0,1]\n"
    "v_pk_mul_f16 v62, v46, s45 op_sel:[0,1]\n"
    "v_pk_mul_f16 v63, v47, s45 op_sel:[0,1]\n"
    "s_waitcnt lgkmcnt(0)\n"
    "s_branch .Lxd_enter\n"
    ".Lxd_slow:\n"
    "s_cmp_eq_u32 s45, -1\n"
    "s_cbranch_scc1 .Lxd_sl7\n"
    "s_mov_b32 s59, 0x100000\n"
    "s_and_b32 s40, s45, 0xff0\n"
    "s_cbranch_scc0 .Lxd_sl1\n"
    ".Lxd_sl2:\n"
    "s_cmp_ge_i32 s45, -16\n"
    "s_cbranch_scc1 .Lxd_sl3\n"
    "s_bitset0_b32 s45, 31\n"
    ".Lxd_sl6:\n"
    "s_cmp_lg_u32 s56, 0\n"
    "s_cbranch_scc1 .Lxd_sl9\n"
    "v_readfirstlane_b32 s40, v13\n"
    "s_lshl4_add_u32 s41, s22, s58\n"
    "s_mov_b32 s56, 2\n"
    "s_cmp_lt_u32 s40, s41\n"
    "s_cbranch_scc0 .Lxd_gran\n"
    ".Lxd_sl9:\n"
    "s_setpc_b64 s[60:61]\n"
    ".Lxd_sl1:\n"
    "s_sleep 1\n"
    "ds_read_b32 v22, v13 offset:512\n"
    "s_waitcnt lgkmcnt(0)\n"
    "v_readfirstlane_b32 s45, v22\n"
    "s_sub_u32 s59, s59, 1\n"
    "s_cbranch_scc1 .Lxd_sl5\n"
    "s_and_b32 s40, s45, 0xff0\n"
    "s_cbranch_scc0 .Lxd_sl1\n"
    "s_cmp_gt_i32 s45, 0\n"
    "s_cbranch_scc0 .Lxd_sl2\n"
    "s_setpc_b64 s[60:61]\n"
    ".Lxd_sl3:\n"
    "v_and_b32 v13, 0x3ffff, v13\n"
    "v_lshl_add_u32 v13, v1, 2, v13\n"
    "s_mov_b64 exec, 7\n"
    ".Lxd_sl4:\n"
    "ds_read_b32 v22, v13 offset:4\n"
    "s_waitcnt lgkmcnt(0)\n"
    "v_cmp_eq_u32 s[40:41], 0, v22\n"
    "v_cmp_ne_u32 s[42:43], -1, v22\n"
    "s_sub_u32 s59, s59, 1\n"
    "s_cbranch_scc1 .Lxd_sl5\n"
    "s_cmp_lg_u32 s40, 0\n"
    "s_cbranch_scc1 .Lxd_sl4\n"
    "s_cmp_eq_u32 s42, 0\n"
    "s_cbranch_scc1 .Lxd_sl5\n"
    "s_mov_b64 exec, -1\n"
    "s_mov_b32 s45, 1\n"
    "s_mov_b32 s56, 0\n"
    "s_setpc_b64 s[60:61]\n"
    ".Lxd_sl5:\n"
    "s_mov_b64 exec, -1\n"
    "s_mov_b32 s45, -1\n"
    ".Lxd_sl7:\n"
    "s_cmp_lg_u32 s56, 1\n"
    "s_cbranch_scc1 .Lxd_sl6\n"
    "s_branch .Lxd_dsum\n"
    ".Lxd_gran:\n"
    "s_mov_b32 s46, 0\n"
    "s_lshl4_add_u32 s40, s22, s58\n"
    "v_lshrrev_b32 v13, 4, v1\n"
    "v_lshl_add_u32 v13, v13, 2, s40\n"
    "s_waitcnt vmcnt(0)\n"
    "ds_read2_b32 v[26:27], v13 offset1:16\n"
    "v_and_b32 v13, 15, v1\n"
    "v_lshlrev_b32 v13, 15, v13\n"
    "s_mov_b32 s42, 0x1fff1fff\n"
    "s_mov_b32 s43, s42\n"
    "s_add_u32 s40, s16, 65528\n"
    "s_addc_u32 s41, s17, 0\n"
    "v_mov_b32 v42, 0\n"
    "s_mov_b32 s56, 2\n"
    "s_waitcnt lgkmcnt(0)\n"
    "v_cmp_lt_i32 s[30:31], 0, v26\n"
    "v_cmp_lt_i32 s[64:65], 0, v27\n"
    "v_lshl_add_u32 v43, v26, 2, v13\n"
    "v_lshl_add_u32 v27, v27, 2, v13\n"
    "v_mov_b32 v26, 0\n"
    "s_and_b64 s[30:31], s[30:31], s[42:43]\n"
    "s_cbranch_scc0 .Lxd_gr30\n"
    "s_mov_b64 exec, s[30:31]\n"
    "global_load_dwordx2 v[42:43], v43, s[40:41] sc1\n"
    "s_mov_b64 exec, -1\n"
    "s_mov_b32 s56, 1\n"
    ".Lxd_gr30:\n"
    "s_and_b64 s[64:65], s[64:65], s[42:43]\n"
    "s_cbranch_scc0 .Lxd_gr64\n"
    "s_mov_b64 exec, s[64:65]\n"
    "global_load_dwordx2 v[26:27], v27, s[40:41] sc1\n"
    "s_mov_b64 exec, -1\n"
    "s_mov_b32 s56, 1\n"
    ".Lxd_gr64:\n"
    "s_setpc_b64 s[60:61]\n"
    ".Lxd_dsum:\n"
    "s_lshl4_add_u32 s42, s22, s58\n"
    "s_waitcnt vmcnt(0)\n"
    "s_cmp_eq_u64 s[30:31], 0\n"
    "s_cbranch_scc1 .Lxd_ds30\n"
    "v_xor_b32 v13, s28, v42\n"
    "v_xor_b32 v22, s29, v43\n"
    "v_lshl_or_b32 v13, v13, 24, v22\n"
    "v_and_b32 v22, 0xffffff00, v42\n"
    "v_cmp_ne_u32 s[40:41], 0, v13\n"
    "v_lshrrev_b32 v13, 4, v1\n"
    "v_add_f32_dpp v22, v22, v22 quad_perm:[1,0,3,2] row_mask:0xf bank_mask:0xf\n"
    "s_and_b64 s[40:41], s[40:41], s[30:31]\n"
    "s_cbranch_scc1 .Lxd_ds9\n"
    "v_add_f32_dpp v22, v22, v22 quad_perm:[2,3,0,1] row_mask:0xf bank_mask:0xf\n"
    "v_lshl_add_u32 v13, v13, 2, s42\n"
    "s_nop 0\n"
    "v_add_f32_dpp v22, v22, v22 row_half_mirror row_mask:0xf bank_mask:0xf\n"
    "s_nop 1\n"
    "v_add_f32_dpp v22, v22, v22 row_mirror row_mask:0xf bank_mask:0xf\n"
    "ds_write_b32 v13, v22 offset:1024\n"
    ".Lxd_ds30:\n"
    "s_cmp_eq_u64 s[64:65], 0\n"
    "s_cbranch_scc1 .Lxd_ds64\n"
    "v_xor_b32 v13, s28, v26\n"
    "v_xor_b32 v22, s29, v27\n"
    "v_lshl_or_b32 v13, v13, 24, v22\n"
    "v_and_b32 v22, 0xffffff00, v26\n"
    "v_cmp_ne_u32 s[40:41], 0, v13\n"
    "v_lshrrev_b32 v13, 4, v1\n"
    "v_add_f32_dpp v22, v22, v22 quad_perm:[1,0,3,2] row_mask:0xf bank_mask:0xf\n"
    "s_and_b64 s[40:41], s[40:41], s[64:65]\n"
    "s_cbranch_scc1 .Lxd_ds9\n"
    "v_add_f32_dpp v22, v22, v22 quad_perm:[2,3,0,1] row_mask:0xf bank_mask:0xf\n"
    "v_lshl_add_u32 v13, v13, 2, s42\n"
    "s_nop 0\n"
    "v_add_f32_dpp v22, v22, v22 row_half_mirror row_mask:0xf bank_mask:0xf\n"
    "s_nop 1\n"
    "v_add_f32_dpp v22, v22, v22 row_mirror row_mask:0xf bank_mask:0xf\n"
    "ds_write_b32 v13, v22 offset:1088\n"
    ".Lxd_ds64:\n"
    "s_mov_b32 s56, 2\n"
    ".Lxd_ds9:\n"
    "s_setpc_b64 s[60:61]\n"
    ".Lxd_mark:\n"
    "v_and_b32 v13, 0x3ffff, v13\n"
    "v_lshl_add_u32 v13, v1, 2, v13\n"
    "s_add_u32 s40, s58, 248\n"
    "v_mov_b32 v24, -1\n"
    "v_cmp_gt_u32 s[42:43], s40, v13\n"
    "s_mov_b32 s62, 0x88888888\n"
    "s_mov_b32 s63, s62\n"
    "s_and_b64 exec, s[42:43], s[62:63]\n"
    "ds_write_b32 v13, v24 offset:8\n"
    "s_mov_b64 exec, s[42:43]\n"
    "v_cndmask_b32 v24, 0, -16, s[62:63]\n"
    "s_brev_b32 s40, 1\n"
    "v_writelane_b32 v24, s40, 2\n"
    "s_lshl_b32 s40, 1, s22\n"
    "v_or_b32 v24, s40, v24\n"
    "s_mov_b32 s36, 1\n"
    "ds_or_b32 v13, v24 offset:520\n"
    "s_mov_b64 exec, -1\n"
    "s_setpc_b64 s[60:61]\n"
    ".Lxd_tf17:\n"
    "v_mov_b32 v12, -1\n"
    "v_mov_b32 v13, v25\n"
    "s_call_b64 s[60:61], .Lxd_mark\n"
    "s_branch .Lxd_tf17r\n"
    ".Lxd_ck18:\n"
    "v_add_u32 v13, 4, v25\n"
    "s_call_b64 s[60:61], .Lxd_slow\n"
    "s_cmp_eq_u32 s45, -1\n"
    "s_cbranch_scc0 .Lxd_ck18r\n"
    "s_call_b64 s[60:61], .Lxd_last\n"
    "v_fma_f32 v7, v79, v83, v11\n"
    "s_branch .Lxd_fin\n"
    ".Lxd_ck21:\n"
    "v_add_u32 v13, 8, v25\n"
    "s_call_b64 s[60:61], .Lxd_slow\n"
    "s_cmp_eq_u32 s45, -1\n"
    "s_cbranch_scc0 .Lxd_ck21r\n"
    "s_call_b64 s[60:61], .Lxd_last\n"
    "v_fma_f32 v8, v79, v83, v11\n"
    "s_branch .Lxd_fin\n"
    ".Lxd_ht24:\n"
    "global_load_lds_dword v26, s[8:9]\n"
    "v_mov_b32 v26, v27\n"
    "v_mov_b32 v27, v42\n"
    "s_sub_u32 s46, s46, 1\n"
    "s_branch .Lxd_ht24r\n"
    ".Lxd_ck25:\n"
    "v_add_u32 v13, 12, v25\n"
    "s_call_b64 s[60:61], .Lxd_slow\n"
    "s_cmp_eq_u32 s45, -1\n"
    "s_cbranch_scc0 .Lxd_ck25r\n"
    "s_call_b64 s[60:61], .Lxd_last\n"
    "v_fma_f32 v9, v79, v83, v11\n"
    "s_branch .Lxd_fin\n"
    ".Lxd_dr28:\n"
    "v_add_u32 v13, 16, v25\n"
    "s_call_b64 s[60:61], .Lxd_mark\n"
    "s_branch .Lxd_dr28r\n"
    ".Lxd_ck29:\n"
    "v_add_u32 v13, 16, v25\n"
    "s_call_b64 s[60:61], .Lxd_slow\n"
    "s_cmp_eq_u32 s45, -1\n"
    "s_cbranch_scc0 .Lxd_ck29r\n"
    "s_call_b64 s[60:61], .Lxd_last\n"
    "v_fma_f32 v10, v79, v83, v11\n"
    "s_branch .Lxd_fin\n"
    ".Lxd_ck32:\n"
    "v_add_u32 v13, 4, v25\n"
    "s_call_b64 s[60:61], .Lxd_slow\n"
    "s_cmp_eq_u32 s45, -1\n"
    "s_cbranch_scc0 .Lxd_ck32r\n"
    "s_call_b64 s[60:61], .Lxd_last\n"
    "v_fma_f32 v7, v79, v83, v11\n"
    "s_branch .Lxd_fin\n"
    ".Lxd_ck35:\n"
    "v_add_u32 v13, 8, v25\n"
    "s_call_b64 s[60:61], .Lxd_slow\n"
    "s_cmp_eq_u32 s45, -1\n"
    "s_cbranch_scc0 .Lxd_ck35r\n"
    "s_call_b64 s[60:61], .Lxd_last\n"
    "v_fma_f32 v8, v79, v83, v11\n"
    "s_branch .Lxd_fin\n"
    ".Lxd_ht38:\n"
    "global_load_lds_dword v26, s[8:9]\n"
    "v_mov_b32 v26, v27\n"
    "v_mov_b32 v27, v42\n"
    "s_sub_u32 s46, s46, 1\n"
    "s_branch .Lxd_ht38r\n"
    ".Lxd_ck39:\n"
    "v_add_u32 v13, 12, v25\n"
    "s_call_b64 s[60:61], .Lxd_slow\n"
    "s_cmp_eq_u32 s45, -1\n"
    "s_cbranch_scc0 .Lxd_ck39r\n"
    "s_call_b64 s[60:61], .Lxd_last\n"
    "v_fma_f32 v9, v79, v83, v11\n"
    "s_branch .Lxd_fin\n"
    ".Lxd_dr42:\n"
    "v_add_u32 v13, 16, v25\n"
    "s_call_b64 s[60:61], .Lxd_mark\n"
    "s_branch .Lxd_dr42r\n"
    ".Lxd_on43:\n"
    "v_mov_b32 v24, 1\n"
    "s_and_b32 s40, s45, 15\n"
    "s_cbranch_scc1 .Lxd_dr42\n"
    "s_mov_b64 exec, 1\n"
    "global_atomic_add v24, v17, v24, s[52:53] sc0 sc1\n"
    "s_mov_b64 exec, -1\n"
    ".Lxd_dr42r:\n"
    "s_branch .Lxd_on43r\n"
    ".Lxd_ck44:\n"
    "v_add_u32 v13, 16, v25\n"
    "s_call_b64 s[60:61], .Lxd_slow\n"
    "s_cmp_eq_u32 s45, -1\n"
    "s_cbranch_scc0 .Lxd_ck44r\n"
    "s_call_b64 s[60:61], .Lxd_last\n"
    "v_fma_f32 v10, v79, v83, v11\n"
    "s_branch .Lxd_fin\n"
    ".Lxd_f2:\n"
    "s_mov_b32 s59, 0x20000\n"
    ".Lxd_f4:\n"
    "s_call_b64 s[60:61], .Lxd_dsum\n"
    "s_cmp_eq_u32 s56, 2\n"
    "s_cbranch_scc1 .Lxd_f3\n"
    "s_sleep 2\n"
    "s_call_b64 s[60:61], .Lxd_gran\n"
    "s_sub_u32 s59, s59, 1\n"
    "s_cbranch_scc0 .Lxd_f4\n"
    "s_waitcnt vmcnt(0)\n"
    "s_branch .Lxd_f3\n"
    ".Lxd_on1:\n"
    "s_mov_b64 exec, 0x3ff\n"
    "global_load_dwordx2 v[26:27], v11, s[16:17] sc1\n"
    "s_mov_b64 exec, -1\n"
    "s_branch .Lxd_on1r\n"
    ".Lxd_pv3:\n"
    "s_mov_b32 s59, 0x20000\n"
    ".Lxd_pv3l:\n"
    "s_sleep 2\n"
    "s_mov_b64 exec, 0x3ff\n"
    "global_load_dwordx2 v[26:27], v11, s[16:17] sc1\n"
    "s_mov_b64 exec, -1\n"
    "s_waitcnt vmcnt(0)\n"
    "v_readlane_b32 s25, v26, 9\n"
    "v_readlane_b32 s40, v26, 8\n"
    "v_readlane_b32 s41, v27, 8\n"
    "s_mul_i32 s42, s25, 0x9e3779b1\n"
    "s_lshr_b32 s42, s42, 24\n"
    "s_xor_b32 s42, s42, s40\n"
    "s_and_b32 s42, s42, 0xff\n"
    "s_xor_b32 s41, s41, s25\n"
    "s_xor_b32 s41, s41, 0xa5c30f96\n"
    "s_or_b32 s42, s41, s42\n"
    "s_lshr_b32 s41, s40, 8\n"
    "s_mul_i32 s40, s41, 0x9e3779b1\n"
    "s_xor_b32 s41, s41, 0xa5c30f96\n"
    "s_lshr_b32 s40, s40, 24\n"
    "v_xor_b32 v13, s40, v26\n"
    "v_xor_b32 v22, s41, v27\n"
    "v_lshl_or_b32 v13, v13, 24, v22\n"
    "v_cmp_ne_u32 s[66:67], 0, v13\n"
    "s_and_b32 s66, s66, 0xff\n"
    "s_or_b32 s42, s42, s66\n"
    "s_cmp_eq_u32 s42, 0\n"
    "s_cbranch_scc1 .Lxd_pv3r\n"
    "s_sub_u32 s59, s59, 1\n"
    "s_cbranch_scc0 .Lxd_pv3l\n"
    "s_branch .Lxd_pv3r\n"
    ".Lxd_on6:\n"
    "v_mov_b32 v24, 1\n"
    "s_mov_b64 exec, 1\n"
    "global_atomic_add v24, v17, v24, s[52:53] sc0 sc1\n"
    "s_mov_b64 exec, -1\n"
    "s_branch .Lxd_on6r\n"
    ".Lxd_ck7:\n"
    "v_mov_b32 v13, s58\n"
    "s_call_b64 s[60:61], .Lxd_slow\n"
    "s_branch .Lxd_ck7r\n"
    ".Lxd_tf8:\n"
    "v_mov_b32 v12, -1\n"
    "v_mov_b32 v13, v25\n"
    "s_call_b64 s[60:61], .Lxd_mark\n"
    "s_branch .Lxd_tf8r\n"
    ".Lxd_on9:\n"
    "s_waitcnt vmcnt(0)\n"
    "v_readfirstlane_b32 s40, v24\n"
    "s_or_b32 s40, s40, s44\n"
    "s_lshl1_add_u32 s41, s40, s54\n"
    "s_cmpk_lt_u32 s40, 256\n"
    "s_cselect_b32 s41, s41, 2\n"
    "v_mov_b32 v12, s41\n"
    "s_cbranch_scc0 .Lxd_tf8\n"
    ".Lxd_tf8r:\n"
    "s_mul_i32 s41, s41, 52\n"
    "s_add_u32 s38, s4, s41\n"
    "s_addc_u32 s39, s5, 0\n"
    "ds_write_b32 v25, v12 offset:20\n"
    "global_load_dwordx3 v[36:38], v17, s[38:39] offset:-104\n"
    "global_load_dword v41, v18, s[38:39] offset:-8\n"
    "v_mov_b32 v13, s58\n"
    "s_branch .Lxd_on9r\n"
    ".Lxd_on10:\n"
    "v_mov_b32 v24, 1\n"
    "s_mov_b64 exec, 1\n"
    "global_atomic_add v24, v17, v24, s[52:53] sc0 sc1\n"
    "s_mov_b64 exec, -1\n"
    "s_branch .Lxd_on10r\n"
    ".Lxd_on11:\n"
    "s_waitcnt vmcnt(0)\n"
    "v_lshl_or_b32 v36, v36, 7, v19\n"
    "v_lshl_or_b32 v37, v37, 7, v19\n"
    "v_lshl_or_b32 v38, v38, 7, v19\n"
    "v_lshl_or_b32 v41, v41, 7, v20\n"
    "global_load_dwordx4 v[28:31], v36, s[8:9]\n"
    "global_load_dwordx4 v[32:35], v37, s[8:9]\n"
    "global_load_dword v40, v41, s[8:9]\n"
    "global_load_dwordx4 v[36:39], v38, s[8:9]\n"
    "s_branch .Lxd_on11r\n"
    ".Lxd_tf12:\n"
    "v_mov_b32 v12, -1\n"
    "v_mov_b32 v13, v25\n"
    "s_call_b64 s[60:61], .Lxd_mark\n"
    "s_branch .Lxd_tf12r\n"
    ".Lxd_on13:\n"
    "s_waitcnt vmcnt(0)\n"
    "v_readfirstlane_b32 s40, v24\n"
    "s_or_b32 s40, s40, s44\n"
    "s_lshl1_add_u32 s41, s40, s54\n"
    "s_cmpk_lt_u32 s40, 256\n"
    "s_cselect_b32 s41, s41, 2\n"
    "v_mov_b32 v12, s41\n"
    "s_cbranch_scc0 .Lxd_tf12\n"
    ".Lxd_tf12r:\n"
    "s_mul_i32 s41, s41, 52\n"
    "s_add_u32 s38, s4, s41\n"
    "s_addc_u32 s39, s5, 0\n"
    "ds_write_b32 v25, v12 offset:20\n"
    "global_load_dwordx3 v[36:38], v17, s[38:39] offset:-104\n"
    "global_load_dword v41, v18, s[38:39] offset:-8\n"
    "s_branch .Lxd_on13r\n"
    ".Lxd_on14:\n"
    "v_mov_b32 v24, 1\n"
    "s_mov_b64 exec, 1\n"
    "global_atomic_add v24, v17, v24, s[52:53] sc0 sc1\n"
    "s_mov_b64 exec, -1\n"
    "s_branch .Lxd_on14r\n"
    ".Lxd_pv59:\n"
    "s_mov_b32 s59, 0x20000\n"
    ".Lxd_pv59l:\n"
    "s_sleep 2\n"
    "s_mov_b64 exec, 0x3ff\n"
    "global_load_dwordx2 v[26:27], v11, s[16:17] sc1\n"
    "s_mov_b64 exec, -1\n"
    "s_waitcnt vmcnt(0)\n"
    "v_readlane_b32 s25, v26, 9\n"
    "v_readlane_b32 s40, v26, 8\n"
    "v_readlane_b32 s41, v27, 8\n"
    "s_mul_i32 s42, s25, 0x9e3779b1\n"
    "s_lshr_b32 s42, s42, 24\n"
    "s_xor_b32 s42, s42, s40\n"
    "s_and_b32 s42, s42, 0xff\n"
    "s_xor_b32 s41, s41, s25\n"
    "s_xor_b32 s41, s41, 0xa5c30f96\n"
    "s_or_b32 s42, s41, s42\n"
    "s_lshr_b32 s41, s40, 8\n"
    "s_mul_i32 s40, s41, 0x9e3779b1\n"
    "s_xor_b32 s41, s41, 0xa5c30f96\n"
    "s_lshr_b32 s40, s40, 24\n"
    "v_xor_b32 v13, s40, v26\n"
    "v_xor_b32 v22, s41, v27\n"
    "v_lshl_or_b32 v13, v13, 24, v22\n"
    "v_cmp_ne_u32 s[66:67], 0, v13\n"
    "s_and_b32 s66, s66, 0xff\n"
    "s_or_b32 s42, s42, s66\n"
    "s_cmp_eq_u32 s42, 0\n"
    "s_cbranch_scc1 .Lxd_pv59r\n"
    "s_sub_u32 s59, s59, 1\n"
    "s_cbranch_scc0 .Lxd_pv59l\n"
    "s_branch .Lxd_pv59r\n"
    ".Lxd_end:\n"
    : "+{v0}"(tid) : "{s[0:1]}"(__builtin_amdgcn_kernarg_segment_ptr()), "{s2}"(blockIdx.x)
    : "memory", "vcc", "scc", "v1", "v2", "v3", "v4", "v5", "v6", "v7", "v8", "v9", "v10", "v11", "v12", "v13", "v14", "v15", "v16", "v17", "v18", "v19", "v20", "v21", "v22", "v23", "v24", "v25", "v26", "v27", "v28", "v29", "v30", "v31", "v32", "v33", "v34", "v35", "v36", "v37", "v38", "v39", "v40", "v41", "v42", "v43", "v44", "v45", "v46", "v47", "v48", "v49", "v50", "v51", "v52", "v53", "v54", "v55", "v56", "v57", "v58", "v59", "v60", "v61", "v62", "v63", "v64", "v65", "v66", "v67", "v68", "v69", "v70", "v71", "v72", "v73", "v74", "v75", "v76", "v77", "v78", "v79", "v80", "v81", "v82", "v83", "v84", "v85", "v86", "v87", "v88", "v89", "v90", "v91", "v92", "v93", "v94", "v95", "v96", "v97", "v98", "v99", "v100", "v101", "v102", "v103", "v104", "v105", "v106", "v107", "v108", "v109", "v110", "v111", "v112", "v113", "v114", "v115", "v116", "v117", "v118", "v119", "v120", "v121", "v122", "v123", "v124", "v125", "v126", "v127", "v128", "v129", "v130", "v131", "v132", "v133", "v134", "v135", "v136", "v137", "v138", "v139", "v140", "v141", "v142", "v143", "v144", "v145", "v146", "v147", "v148", "v149", "v150", "v151", "v152", "v153", "v154", "v155", "v156", "v157", "v158", "v159", "v160", "v161", "v162", "v163", "v164", "v165", "v166", "v167", "v168", "v169", "v170", "v171", "v172", "v173", "v174", "v175", "v176", "v177", "v178", "v179", "v180", "v181", "v182", "v183", "v184", "v185", "v186", "v187", "v188", "v189", "v190", "v191", "v192", "v193", "v194", "v195", "v196", "v197", "v198", "v199", "v200", "v201", "v202", "v203", "v204", "v205", "v206", "v207", "v208", "v209", "v210", "v211", "v212", "v213", "v214", "v215", "v216", "v217", "v218", "v219", "v220", "v221", "v222", "v223", "v224", "v225", "v226", "v227", "v228", "v229", "v230", "v231", "v232", "v233", "v234", "v235", "v236", "v237", "v238", "v239", "v240", "v241", "v242", "v243", "v244", "v245", "v246", "v247", "v248", "v249", "v250", "v251", "v252", "v253", "v254", "v255", "s3", "s4", "s5", "s6", "s7", "s8", "s9", "s10", "s11", "s12", "s13", "s14", "s15", "s16", "s17", "s18", "s19", "s20", "s21", "s22", "s23", "s24", "s25", "s26", "s27", "s28", "s29", "s30", "s31", "s34", "s35", "s36", "s37", "s38", "s39", "s40", "s41", "s42", "s43", "s44", "s45", "s46", "s47", "s48", "s49", "s50", "s51", "s52", "s53", "s54", "s55", "s56", "s57", "s58", "s59", "s60", "s61", "s62", "s63", "s64", "s65", "s66", "s67", "s68", "s69", "s70", "s71");
  asm volatile("" :: "v"((__attribute__((address_space(3))) char*)shm));
}

extern "C" void kernel_launch(void* const* d_in, const int* in_sizes, int n_in, void* d_out, int out_size,
                              void* d_ws, size_t ws_size, hipStream_t stream) {
  xd_kernel<<<256, 512, 0, stream>>>((const int*)d_in[0], (const u16*)d_in[4], (const u16*)d_in[1], (const u16*)d_in[5], (const u16*)d_in[2], (const u16*)d_in[3], (char*)d_ws,
                                     (const u16*)d_in[6], (const u16*)d_in[7], (u16*)d_out);
}
```

```cpp
#include <hip/hip_runtime.h>
typedef unsigned short u16;

__device__ const __attribute__((aligned(16), used, visibility("hidden"))) u16 xd_tab[5120] asm("xd_tab") = {924, 925, 928, 929, 926, 927, 930, 931, 934, 935, 938, 939, 932, 933, 936, 937, 940, 941, 944, 945, 942, 943, 946, 947, 948, 0, 949, 0, 0, 0, 0, 0, 2, 36, 138, 172, 0, 1, 24, 25, 338, 372, 474, 508, 34, 35, 57, 58, 546, 580, 682, 716, 68, 69, 90, 91, 816, 817, 850, 952, 102, 99, 123, 124, 680, 681, 684, 685, 20, 21, 22, 23, 714, 715, 717, 718, 54, 55, 56, 53, 748, 749, 750, 751, 88, 89, 86, 87, 782, 779, 783, 784, 122, 119, 120, 121, 152, 153, 154, 155, 284, 285, 286, 287, 186, 187, 188, 185, 318, 319, 320, 317, 220, 221, 218, 219, 352, 353, 350, 351, 254, 251, 252, 253, 386, 383, 384, 385, 416, 417, 418, 419, 548, 549, 550, 551, 450, 451, 452, 449, 582, 583, 584, 581, 484, 485, 482, 483, 616, 617, 614, 615, 518, 515, 516, 517, 650, 647, 648, 649, 544, 545, 552, 553, 16, 17, 18, 19, 578, 579, 585, 586, 50, 51, 52, 49, 612, 613, 618, 619, 84, 85, 82, 83, 646, 643, 651, 652, 118, 115, 116, 117, 148, 149, 150, 151, 280, 281, 282, 283, 182, 183, 184, 181, 314, 315, 316, 313, 216, 217, 214, 215, 348, 349, 346, 347, 250, 247, 248, 249, 382, 379, 380, 381, 412, 413, 414, 415, 408, 409, 420, 421, 446, 447, 448, 445, 442, 443, 453, 454, 480, 481, 478, 479, 476, 477, 486, 487, 514, 511, 512, 513, 510, 507, 519, 520, 12, 13, 14, 15, 144, 145, 146, 147, 46, 47, 48, 45, 178, 179, 180, 177, 80, 81, 78, 79, 212, 213, 210, 211, 114, 111, 112, 113, 246, 243, 244, 245, 276, 277, 278, 279, 272, 273, 288, 289, 310, 311, 312, 309, 306, 307, 321, 322, 344, 345, 342, 343, 340, 341, 354, 355, 378, 375, 376, 377, 374, 371, 387, 388, 8, 9, 10, 11, 140, 141, 142, 143, 42, 43, 44, 41, 174, 175, 176, 173, 76, 77, 74, 75, 208, 209, 206, 207, 110, 107, 108, 109, 242, 239, 240, 241, 136, 137, 156, 157, 4, 5, 6, 7, 170, 171, 189, 190, 38, 39, 40, 37, 204, 205, 222, 223, 72, 73, 70, 71, 238, 235, 255, 256, 106, 103, 104, 105, 0, 0, 0, 0, 0, 0, 0, 0, 0, 0, 0, 0, 0, 0, 0, 0, 0, 0, 0, 0, 0, 0, 0, 0, 0, 0, 0, 0, 0, 0, 0, 0, 0, 0, 0, 0, 0, 0, 0, 0, 0, 0, 0, 0, 0, 0, 0, 0, 0, 0, 0, 0, 0, 0, 0, 0, 0, 0, 0, 0, 0, 0, 0, 0, 0, 0, 0, 0, 0, 0, 0, 0, 0, 0, 0, 0, 0, 0, 0, 0, 0, 0, 0, 0, 0, 0, 0, 0, 0, 0, 0, 0, 0, 0, 0, 0, 0, 0, 0, 0, 0, 0, 0, 0, 0, 0, 0, 0, 0, 0, 0, 0, 0, 0, 0, 0, 0, 0, 0, 0, 0, 0, 0, 0, 0, 0, 0, 0, 924, 925, 926, 927, 934, 935, 932, 933, 940, 941, 942, 943, 948, 0, 0, 0, 2, 36, 0, 1, 338, 372, 34, 35, 546, 580, 68, 69, 816, 817, 102, 99, 928, 929, 930, 931, 938, 939, 936, 937, 944, 945, 946, 947, 949, 0, 0, 0, 138, 172, 24, 25, 474, 508, 57, 58, 682, 716, 90, 91, 850, 952, 123, 124, 680, 681, 20, 21, 714, 715, 54, 55, 748, 749, 88, 89, 782, 779, 122, 119, 152, 153, 284, 285, 186, 187, 318, 319, 220, 221, 352, 353, 254, 251, 386, 383, 684, 685, 22, 23, 717, 718, 56, 53, 750, 751, 86, 87, 783, 784, 120, 121, 154, 155, 286, 287, 188, 185, 320, 317, 218, 219, 350, 351, 252, 253, 384, 385, 416, 417, 548, 549, 450, 451, 582, 583, 484, 485, 616, 617, 518, 515, 650, 647, 544, 545, 16, 17, 578, 579, 50, 51, 612, 613, 84, 85, 646, 643, 118, 115, 418, 419, 550, 551, 452, 449, 584, 581, 482, 483, 614, 615, 516, 517, 648, 649, 552, 553, 18, 19, 585, 586, 52, 49, 618, 619, 82, 83, 651, 652, 116, 117, 148, 149, 280, 281, 182, 183, 314, 315, 216, 217, 348, 349, 250, 247, 382, 379, 412, 413, 408, 409, 446, 447, 442, 443, 480, 481, 476, 477, 514, 511, 510, 507, 150, 151, 282, 283, 184, 181, 316, 313, 214, 215, 346, 347, 248, 249, 380, 381, 414, 415, 420, 421, 448, 445, 453, 454, 478, 479, 486, 487, 512, 513, 519, 520, 12, 13, 144, 145, 46, 47, 178, 179, 80, 81, 212, 213, 114, 111, 246, 243, 276, 277, 272, 273, 310, 311, 306, 307, 344, 345, 340, 341, 378, 375, 374, 371, 14, 15, 146, 147, 48, 45, 180, 177, 78, 79, 210, 211, 112, 113, 244, 245, 278, 279, 288, 289, 312, 309, 321, 322, 342, 343, 354, 355, 376, 377, 387, 388, 8, 9, 140, 141, 42, 43, 174, 175, 76, 77, 208, 209, 110, 107, 242, 239, 136, 137, 4, 5, 170, 171, 38, 39, 204, 205, 72, 73, 238, 235, 106, 103, 10, 11, 142, 143, 44, 41, 176, 173, 74, 75, 206, 207, 108, 109, 240, 241, 156, 157, 6, 7, 189, 190, 40, 37, 222, 223, 70, 71, 255, 256, 104, 105, 0, 0, 0, 0, 0, 0, 0, 0, 0, 0, 0, 0, 0, 0, 0, 0, 0, 0, 0, 0, 0, 0, 0, 0, 0, 0, 0, 0, 0, 0, 0, 0, 0, 0, 0, 0, 0, 0, 0, 0, 0, 0, 0, 0, 0, 0, 0, 0, 0, 0, 0, 0, 0, 0, 0, 0, 0, 0, 0, 0, 0, 0, 0, 0, 0, 0, 0, 0, 0, 0, 0, 0, 0, 0, 0, 0, 0, 0, 0, 0, 0, 0, 0, 0, 0, 0, 0, 0, 0, 0, 0, 0, 0, 0, 0, 0, 0, 0, 0, 0, 0, 0, 0, 0, 0, 0, 0, 0, 0, 0, 0, 0, 0, 0, 0, 0, 0, 0, 0, 0, 0, 0, 0, 0, 0, 0, 0, 0, 0, 0, 0, 0, 0, 0, 0, 0, 0, 0, 0, 0, 0, 0, 0, 0, 0, 0, 0, 0, 0, 0, 0, 0, 0, 0, 0, 0, 0, 0, 0, 0, 0, 16256, 0, 16256, 0, 16256, 0, 16256, 0, 16256, 0, 16256, 0, 16256, 0, 16256, 0, 16256, 0, 16256, 0, 16256, 0, 16256, 0, 16128, 0, 16256, 0, 16128, 0, 0, 0, 16128, 0, 16256, 0, 16256, 0, 16256, 0, 16128, 0, 16256, 0, 16256, 0, 16256, 0, 16128, 0, 16256, 0, 16256, 0, 16256, 0, 16128, 0, 16256, 0, 16256, 0, 16256, 0, 16256, 0, 16256, 0, 16256, 0, 16256, 0, 16256, 0, 16256, 0, 16256, 0, 16256, 0, 16256, 0, 16256, 0, 16256, 0, 16256, 0, 16256, 0, 16256, 0, 16256, 0, 16256, 0, 16256, 0, 16256, 0, 16256, 0, 16256, 0, 16256, 0, 16256, 0, 16256, 0, 16256, 0, 16256, 0, 16256, 0, 16256, 0, 16256, 0, 16256, 0, 16256, 0, 16256, 0, 16256, 0, 16128, 0, 16256, 0, 16256, 0, 16256, 0, 16128, 0, 16256, 0, 16256, 0, 16256, 0, 16128, 0, 16256, 0, 16256, 0, 16256, 0, 16128, 0, 16256, 0, 16256, 0, 16256, 0, 16256, 0, 16256, 0, 16256, 0, 16256, 0, 16256, 0, 16256, 0, 16256, 0, 16256, 0, 16256, 0, 16256, 0, 16256, 0, 16256, 0, 16256, 0, 16256, 0, 16256, 0, 16256, 0, 16256, 0, 16256, 0, 16256, 0, 16256, 0, 16256, 0, 16256, 0, 16256, 0, 16256, 0, 16256, 0, 16256, 0, 16256, 0, 16256, 0, 16256, 0, 16256, 0, 16256, 0, 16256, 0, 16256, 0, 16256, 0, 16256, 0, 16256, 0, 16256, 0, 16256, 0, 16256, 0, 16256, 0, 16256, 0, 16256, 0, 16256, 0, 16256, 0, 16256, 0, 16256, 0, 16256, 0, 16256, 0, 16256, 0, 16256, 0, 16256, 0, 16256, 0, 16256, 0, 16256, 0, 16256, 0, 16256, 0, 16256, 0, 16256, 0, 16256, 0, 16256, 0, 16256, 0, 16256, 0, 16256, 0, 16256, 0, 16256, 0, 16256, 0, 16256, 0, 16256, 0, 16256, 0, 16256, 0, 16256, 0, 16256, 0, 16256, 0, 16256, 0, 16256, 0, 16256, 0, 16256, 0, 16256, 0, 16256, 0, 16256, 0, 16128, 0, 16256, 0, 16256, 0, 16256, 0, 16128, 0, 16256, 0, 16256, 0, 16256, 0, 16128, 0, 16256, 0, 16256, 0, 16256, 0, 16128, 0, 16256, 0, 16256, 0, 16256, 0, 0, 0, 0, 0, 0, 0, 0, 0, 0, 0, 0, 0, 0, 0, 0, 0, 0, 0, 0, 0, 0, 0, 0, 0, 0, 0, 0, 0, 0, 0, 0, 0, 0, 0, 0, 0, 0, 0, 0, 0, 0, 0, 0, 0, 0, 0, 0, 0, 0, 0, 0, 0, 0, 0, 0, 0, 0, 0, 0, 0, 0, 0, 0, 0, 0, 0, 0, 0, 0, 0, 0, 0, 0, 0, 0, 0, 0, 0, 0, 0, 0, 0, 0, 0, 0, 0, 0, 0, 0, 0, 0, 0, 0, 0, 0, 0, 0, 0, 0, 0, 0, 0, 0, 0, 0, 0, 0, 0, 0, 0, 0, 0, 0, 0, 0, 0, 0, 0, 0, 0, 0, 0, 0, 0, 0, 0, 0, 0, 0, 0, 0, 0, 0, 0, 0, 0, 0, 0, 0, 0, 0, 0, 0, 0, 0, 0, 0, 0, 0, 0, 0, 0, 0, 0, 0, 0, 0, 0, 0, 0, 16128, 0, 16256, 0, 16256, 0, 16256, 0, 16128, 0, 16256, 0, 16256, 0, 16256, 0, 16128, 0, 16256, 0, 16256, 0, 16256, 0, 16128, 0, 16256, 0, 16256, 0, 16256, 0, 16256, 0, 16256, 0, 16256, 0, 16256, 0, 16256, 0, 16256, 0, 16256, 0, 16256, 0, 16256, 0, 16256, 0, 16256, 0, 16256, 0, 16256, 0, 16256, 0, 16256, 0, 16256, 0, 16256, 0, 16256, 0, 16256, 0, 16256, 0, 16256, 0, 16256, 0, 16256, 0, 16256, 0, 16256, 0, 16256, 0, 16256, 0, 16256, 0, 16256, 0, 16256, 0, 16256, 0, 16256, 0, 16256, 0, 16256, 0, 16256, 0, 16256, 0, 16256, 0, 16256, 0, 16256, 0, 16256, 0, 16256, 0, 16256, 0, 16256, 0, 16256, 0, 16256, 0, 16256, 0, 16256, 0, 16256, 0, 16256, 0, 16256, 0, 16256, 0, 16256, 0, 16256, 0, 16256, 0, 16256, 0, 16256, 0, 16256, 0, 16256, 0, 16256, 0, 16256, 0, 16256, 0, 16256, 0, 16256, 0, 16256, 0, 16256, 0, 16256, 0, 16256, 0, 16256, 0, 16256, 0, 16256, 0, 16256, 0, 16256, 0, 16256, 0, 16256, 0, 16256, 0, 16256, 0, 16256, 0, 16256, 0, 16256, 0, 16256, 0, 16128, 0, 16256, 0, 16256, 0, 16256, 0, 16128, 0, 16256, 0, 16256, 0, 16256, 0, 16128, 0, 16256, 0, 16256, 0, 16256, 0, 16128, 0, 16256, 0, 16256, 0, 16256, 0, 16256, 0, 16256, 0, 16256, 0, 16256, 0, 16256, 0, 16256, 0, 16256, 0, 16256, 0, 16256, 0, 16256, 0, 16256, 0, 16256, 0, 16256, 0, 16256, 0, 16256, 0, 16256, 0, 16128, 0, 16256, 0, 16256, 0, 16256, 0, 16128, 0, 16256, 0, 16256, 0, 16256, 0, 16128, 0, 16256, 0, 16256, 0, 16256, 0, 16128, 0, 16256, 0, 16256, 0, 16256, 0, 16256, 0, 16256, 0, 16256, 0, 16256, 0, 16256, 0, 16256, 0, 16256, 0, 16256, 0, 16256, 0, 16256, 0, 16256, 0, 16256, 0, 16256, 0, 16256, 0, 16256, 0, 16256, 0, 16256, 0, 16256, 0, 16256, 0, 16256, 0, 16256, 0, 16256, 0, 16256, 0, 16256, 0, 16256, 0, 16256, 0, 16256, 0, 16256, 0, 16256, 0, 16256, 0, 16256, 0, 16256, 0, 0, 0, 0, 0, 0, 0, 0, 0, 0, 0, 0, 0, 0, 0, 0, 0, 0, 0, 0, 0, 0, 0, 0, 0, 0, 0, 0, 0, 0, 0, 0, 0, 0, 0, 0, 0, 0, 0, 0, 0, 0, 0, 0, 0, 0, 0, 0, 0, 0, 0, 0, 0, 0, 0, 0, 0, 0, 0, 0, 0, 0, 0, 0, 0, 0, 0, 0, 0, 0, 0, 0, 0, 0, 0, 0, 0, 0, 0, 0, 0, 0, 0, 0, 0, 0, 0, 0, 0, 0, 0, 0, 0, 0, 0, 0, 0, 0, 0, 0, 0, 0, 0, 0, 0, 0, 0, 0, 0, 0, 0, 0, 0, 0, 0, 0, 0, 0, 0, 0, 0, 0, 0, 0, 0, 0, 0, 0, 0, 16768, 0, 16768, 0, 16768, 0, 16768, 0, 16768, 0, 16768, 0, 16768, 0, 16768, 0, 16768, 0, 16768, 0, 16768, 0, 16768, 0, 16768, 0, 0, 0, 16768, 0, 0, 0, 16256, 0, 16256, 0, 16256, 0, 16256, 0, 16256, 0, 16256, 0, 16256, 0, 16256, 0, 16256, 0, 16256, 0, 16256, 0, 16256, 0, 16128, 0, 16256, 0, 16128, 0, 17280, 0, 16128, 0, 16256, 0, 16256, 0, 16256, 0, 16128, 0, 16256, 0, 16256, 0, 16256, 0, 16128, 0, 16256, 0, 16256, 0, 16256, 0, 16128, 0, 16256, 0, 16256, 0, 16256, 0, 16256, 0, 16256, 0, 16256, 0, 16256, 0, 16256, 0, 16256, 0, 16256, 0, 16256, 0, 16256, 0, 16256, 0, 16256, 0, 16256, 0, 16256, 0, 16256, 0, 16256, 0, 16256, 0, 16256, 0, 16256, 0, 16256, 0, 16256, 0, 16256, 0, 16256, 0, 16256, 0, 16256, 0, 16256, 0, 16256, 0, 16256, 0, 16256, 0, 16256, 0, 16256, 0, 16256, 0, 16256, 0, 16128, 0, 16256, 0, 16256, 0, 16256, 0, 16128, 0, 16256, 0, 16256, 0, 16256, 0, 16128, 0, 16256, 0, 16256, 0, 16256, 0, 16128, 0, 16256, 0, 16256, 0, 16256, 0, 16256, 0, 16256, 0, 16256, 0, 16256, 0, 16256, 0, 16256, 0, 16256, 0, 16256, 0, 16256, 0, 16256, 0, 16256, 0, 16256, 0, 16256, 0, 16256, 0, 16256, 0, 16256, 0, 16256, 0, 16256, 0, 16256, 0, 16256, 0, 16256, 0, 16256, 0, 16256, 0, 16256, 0, 16256, 0, 16256, 0, 16256, 0, 16256, 0, 16256, 0, 16256, 0, 16256, 0, 16256, 0, 16256, 0, 16256, 0, 16256, 0, 16256, 0, 16256, 0, 16256, 0, 16256, 0, 16256, 0, 16256, 0, 16256, 0, 16256, 0, 16256, 0, 16256, 0, 16256, 0, 16256, 0, 16256, 0, 16256, 0, 16256, 0, 16256, 0, 16256, 0, 16256, 0, 16256, 0, 16256, 0, 16256, 0, 16256, 0, 16256, 0, 16256, 0, 16256, 0, 16256, 0, 16256, 0, 16256, 0, 16256, 0, 16256, 0, 16256, 0, 16256, 0, 16256, 0, 16256, 0, 16256, 0, 16256, 0, 16256, 0, 16256, 0, 16256, 0, 16256, 0, 16256, 0, 16256, 0, 16256, 0, 16256, 0, 16256, 0, 16128, 0, 16256, 0, 16256, 0, 16256, 0, 16128, 0, 16256, 0, 16256, 0, 16256, 0, 16128, 0, 16256, 0, 16256, 0, 16256, 0, 16128, 0, 16256, 0, 16256, 0, 16256, 0, 0, 0, 0, 0, 0, 0, 0, 0, 0, 0, 0, 0, 0, 0, 0, 0, 0, 0, 0, 0, 0, 0, 0, 0, 0, 0, 0, 0, 0, 0, 0, 0, 0, 0, 0, 0, 0, 0, 0, 0, 0, 0, 0, 0, 0, 0, 0, 0, 0, 0, 0, 0, 0, 0, 0, 0, 0, 0, 0, 0, 0, 0, 0, 0, 0, 0, 0, 0, 0, 0, 0, 0, 0, 0, 0, 0, 0, 0, 0, 0, 0, 0, 0, 0, 0, 0, 0, 0, 0, 0, 0, 0, 0, 0, 0, 0, 0, 0, 0, 0, 0, 0, 0, 0, 0, 0, 0, 0, 0, 0, 0, 0, 0, 0, 0, 0, 0, 0, 0, 0, 0, 0, 0, 0, 0, 0, 0, 0, 16768, 0, 16768, 0, 16768, 0, 16768, 0, 16768, 0, 16768, 0, 16768, 0, 16768, 0, 16768, 0, 16768, 0, 16768, 0, 16768, 0, 0, 0, 0, 0, 0, 0, 0, 0, 16128, 0, 16256, 0, 16256, 0, 16256, 0, 16128, 0, 16256, 0, 16256, 0, 16256, 0, 16128, 0, 16256, 0, 16256, 0, 16256, 0, 16128, 0, 16256, 0, 16256, 0, 16256, 0, 16256, 0, 16256, 0, 16256, 0, 16256, 0, 16256, 0, 16256, 0, 16256, 0, 16256, 0, 16256, 0, 16256, 0, 16256, 0, 16256, 0, 16256, 0, 16256, 0, 16256, 0, 16256, 0, 16256, 0, 16256, 0, 16256, 0, 16256, 0, 16256, 0, 16256, 0, 16256, 0, 16256, 0, 16256, 0, 16256, 0, 16256, 0, 16256, 0, 16256, 0, 16256, 0, 16256, 0, 16256, 0, 16256, 0, 16256, 0, 16256, 0, 16256, 0, 16256, 0, 16256, 0, 16256, 0, 16256, 0, 16256, 0, 16256, 0, 16256, 0, 16256, 0, 16256, 0, 16256, 0, 16256, 0, 16256, 0, 16256, 0, 16256, 0, 16256, 0, 16256, 0, 16256, 0, 16256, 0, 16256, 0, 16256, 0, 16256, 0, 16256, 0, 16256, 0, 16256, 0, 16256, 0, 16256, 0, 16256, 0, 16256, 0, 16256, 0, 16256, 0, 16256, 0, 16256, 0, 16256, 0, 16256, 0, 16256, 0, 16256, 0, 16256, 0, 16256, 0, 16256, 0, 16256, 0, 16256, 0, 16256, 0, 16256, 0, 16256, 0, 16128, 0, 16256, 0, 16256, 0, 16256, 0, 16128, 0, 16256, 0, 16256, 0, 16256, 0, 16128, 0, 16256, 0, 16256, 0, 16256, 0, 16128, 0, 16256, 0, 16256, 0, 16256, 0, 16256, 0, 16256, 0, 16256, 0, 16256, 0, 16256, 0, 16256, 0, 16256, 0, 16256, 0, 16256, 0, 16256, 0, 16256, 0, 16256, 0, 16256, 0, 16256, 0, 16256, 0, 16256, 0, 16128, 0, 16256, 0, 16256, 0, 16256, 0, 16128, 0, 16256, 0, 16256, 0, 16256, 0, 16128, 0, 16256, 0, 16256, 0, 16256, 0, 16128, 0, 16256, 0, 16256, 0, 16256, 0, 16256, 0, 16256, 0, 16256, 0, 16256, 0, 16256, 0, 16256, 0, 16256, 0, 16256, 0, 16256, 0, 16256, 0, 16256, 0, 16256, 0, 16256, 0, 16256, 0, 16256, 0, 16256, 0, 16256, 0, 16256, 0, 16256, 0, 16256, 0, 16256, 0, 16256, 0, 16256, 0, 16256, 0, 16256, 0, 16256, 0, 16256, 0, 16256, 0, 16256, 0, 16256, 0, 16256, 0, 16256, 0, 0, 0, 0, 0, 0, 0, 0, 0, 0, 0, 0, 0, 0, 0, 0, 0, 0, 0, 0, 0, 0, 0, 0, 0, 0, 0, 0, 0, 0, 0, 0, 0, 0, 0, 0, 0, 0, 0, 0, 0, 0, 0, 0, 0, 0, 0, 0, 0, 0, 0, 0, 0, 0, 0, 0, 0, 0, 0, 0, 0, 0, 0, 0, 0, 0, 0, 0, 0, 0, 0, 0, 0, 0, 0, 0, 0, 0, 0, 0, 0, 0, 0, 0, 0, 0, 0, 0, 0, 0, 0, 0, 0, 0, 0, 0, 0, 0, 0, 0, 0, 0, 0, 0, 0, 0, 0, 0, 0, 0, 0, 0, 0, 0, 0, 0, 0, 0, 0, 0, 0, 0, 0, 0, 0, 0, 0, 0, 0, 0, 0, 0, 0, 0, 0, 0, 0, 0, 0, 0, 0, 0, 0, 0, 0, 16256, 0, 16256, 0, 16128, 0, 16256, 0, 16256, 0, 16256, 0, 16128, 0, 16256, 0, 0, 0, 0, 0, 0, 0, 0, 0, 0, 0, 0, 0, 0, 0, 0, 0, 16256, 0, 16256, 0, 16256, 0, 16256, 0, 16256, 0, 16256, 0, 16256, 0, 16256, 0, 16128, 0, 16256, 0, 16256, 0, 16256, 0, 16128, 0, 16256, 0, 16256, 0, 16256, 0, 16256, 0, 16256, 0, 16256, 0, 16256, 0, 16256, 0, 16256, 0, 16256, 0, 16256, 0, 16256, 0, 16256, 0, 16256, 0, 16256, 0, 16256, 0, 16256, 0, 16256, 0, 16256, 0, 16256, 0, 16256, 0, 16256, 0, 16256, 0, 16256, 0, 16256, 0, 16256, 0, 16256, 0, 16256, 0, 16256, 0, 16256, 0, 16256, 0, 16256, 0, 16256, 0, 16256, 0, 16256, 0, 16128, 0, 16256, 0, 16256, 0, 16256, 0, 16128, 0, 16256, 0, 16256, 0, 16256, 0, 16256, 0, 16256, 0, 16256, 0, 16256, 0, 16256, 0, 16256, 0, 16256, 0, 16256, 0, 16256, 0, 16256, 0, 16256, 0, 16256, 0, 16256, 0, 16256, 0, 16256, 0, 16256, 0, 16256, 0, 16256, 0, 16256, 0, 16256, 0, 16256, 0, 16256, 0, 16256, 0, 16256, 0, 16256, 0, 16256, 0, 16128, 0, 16256, 0, 16256, 0, 16256, 0, 16128, 0, 16256, 0, 16256, 0, 16256, 0, 16256, 0, 16256, 0, 16256, 0, 16256, 0, 16256, 0, 16256, 0, 16256, 0, 16256, 0, 16256, 0, 16256, 0, 16256, 0, 16256, 0, 16256, 0, 16256, 0, 16256, 0, 16256, 0, 16256, 0, 16256, 0, 16256, 0, 16256, 0, 16256, 0, 16256, 0, 16256, 0, 16256, 0, 16128, 0, 16256, 0, 16256, 0, 16256, 0, 16128, 0, 16256, 0, 16256, 0, 16256, 0, 16256, 0, 16256, 0, 16256, 0, 16256, 0, 16256, 0, 16256, 0, 16256, 0, 16256, 0, 16256, 0, 16256, 0, 16256, 0, 16256, 0, 16256, 0, 16256, 0, 16256, 0, 16256, 0, 16256, 0, 16256, 0, 16256, 0, 16256, 0, 16256, 0, 16256, 0, 16128, 0, 16256, 0, 16256, 0, 16256, 0, 16128, 0, 16256, 0, 16256, 0, 16256, 0, 16256, 0, 16256, 0, 16256, 0, 16256, 0, 16256, 0, 16256, 0, 16256, 0, 16256, 0, 16256, 0, 16256, 0, 16256, 0, 16256, 0, 16256, 0, 16256, 0, 16256, 0, 16256, 0, 0, 0, 0, 0, 0, 0, 0, 0, 0, 0, 0, 0, 0, 0, 0, 0, 0, 0, 0, 0, 0, 0, 0, 0, 0, 0, 0, 0, 0, 0, 0, 0, 0, 0, 0, 0, 0, 0, 0, 0, 0, 0, 0, 0, 0, 0, 0, 0, 0, 0, 0, 0, 0, 0, 0, 0, 0, 0, 0, 0, 0, 0, 0, 0, 0, 0, 0, 0, 0, 0, 0, 0, 0, 0, 0, 0, 0, 0, 0, 0, 0, 0, 0, 0, 0, 0, 0, 0, 0, 0, 0, 0, 0, 0, 0, 0, 0, 0, 0, 0, 0, 0, 0, 0, 0, 0, 0, 0, 0, 0, 0, 0, 0, 0, 0, 0, 0, 0, 0, 0, 0, 0, 0, 0, 0, 0, 0, 0, 0, 0, 0, 0, 0, 0, 0, 0, 0, 0, 0, 0, 0, 0, 0, 0, 16256, 0, 16256, 0, 16128, 0, 16256, 0, 16128, 0, 16256, 0, 16128, 0, 16256, 0, 0, 0, 0, 0, 0, 0, 0, 0, 0, 0, 0, 0, 0, 0, 0, 0, 16256, 0, 16256, 0, 16256, 0, 16256, 0, 16128, 0, 0, 0, 16256, 0, 16256, 0, 16128, 0, 16256, 0, 16256, 0, 16256, 0, 16128, 0, 16256, 0, 16256, 0, 16256, 0, 16256, 0, 16256, 0, 16256, 0, 16256, 0, 16256, 0, 16256, 0, 16256, 0, 16256, 0, 16256, 0, 16256, 0, 16256, 0, 16256, 0, 16256, 0, 16256, 0, 16256, 0, 16256, 0, 16256, 0, 16256, 0, 16256, 0, 16256, 0, 16256, 0, 16256, 0, 16256, 0, 16256, 0, 16256, 0, 16256, 0, 16256, 0, 16256, 0, 16256, 0, 16256, 0, 16256, 0, 16256, 0, 16128, 0, 16256, 0, 16256, 0, 16256, 0, 16128, 0, 16256, 0, 16256, 0, 16256, 0, 16256, 0, 16256, 0, 16256, 0, 16256, 0, 16256, 0, 16256, 0, 16256, 0, 16256, 0, 16256, 0, 16256, 0, 16256, 0, 16256, 0, 16256, 0, 16256, 0, 16256, 0, 16256, 0, 16256, 0, 16256, 0, 16256, 0, 16256, 0, 16256, 0, 16256, 0, 16256, 0, 16256, 0, 16256, 0, 16256, 0, 16128, 0, 16256, 0, 16256, 0, 16256, 0, 16128, 0, 16256, 0, 16256, 0, 16256, 0, 16256, 0, 16256, 0, 16256, 0, 16256, 0, 16256, 0, 16256, 0, 16256, 0, 16256, 0, 16256, 0, 16256, 0, 16256, 0, 16256, 0, 16256, 0, 16256, 0, 16256, 0, 16256, 0, 16256, 0, 16256, 0, 16256, 0, 16256, 0, 16256, 0, 16256, 0, 16256, 0, 16256, 0, 16128, 0, 16256, 0, 16256, 0, 16256, 0, 16128, 0, 16256, 0, 16256, 0, 16256, 0, 16256, 0, 16256, 0, 16256, 0, 16256, 0, 16256, 0, 16256, 0, 16256, 0, 16256, 0, 16256, 0, 16256, 0, 16256, 0, 16256, 0, 16256, 0, 16256, 0, 16256, 0, 16256, 0, 16256, 0, 16256, 0, 16256, 0, 16256, 0, 16256, 0, 16256, 0, 16128, 0, 16256, 0, 16256, 0, 16256, 0, 16128, 0, 16256, 0, 16256, 0, 16256, 0, 16256, 0, 16256, 0, 16256, 0, 16256, 0, 16256, 0, 16256, 0, 16256, 0, 16256, 0, 16256, 0, 16256, 0, 16256, 0, 16256, 0, 16256, 0, 16256, 0, 16256, 0, 16256, 0, 0, 0, 0, 0, 0, 0, 0, 0, 0, 0, 0, 0, 0, 0, 0, 0, 0, 0, 0, 0, 0, 0, 0, 0, 0, 0, 0, 0, 0, 0, 0, 0, 0, 0, 0, 0, 0, 0, 0, 0, 0, 0, 0, 0, 0, 0, 0, 0, 0, 0, 0, 0, 0, 0, 0, 0, 0, 0, 0, 0, 0, 0, 0, 0, 0, 0, 0, 0, 0, 0, 0, 0, 0, 0, 0, 0, 0, 0, 0, 0, 0, 0, 0, 0, 0, 0, 0, 0, 0, 0, 0, 0, 0, 0, 0, 0, 0, 0, 0, 0, 0, 0, 0, 0, 0, 0, 0, 0, 0, 0, 0, 0, 0, 0, 0, 0, 0, 0, 0, 0, 0, 0, 0, 0, 0, 0, 0, 0, 16768, 0, 16768, 0, 16768, 0, 16768, 0, 16768, 0, 16768, 0, 16768, 0, 16768, 0, 16256, 0, 16256, 0, 16128, 0, 16256, 0, 16256, 0, 16256, 0, 16128, 0, 16256, 0, 16768, 0, 16768, 0, 16768, 0, 16768, 0, 16768, 0, 16768, 0, 16768, 0, 16768, 0, 16256, 0, 16256, 0, 16256, 0, 16256, 0, 16256, 0, 16256, 0, 16256, 0, 16256, 0, 16128, 0, 16256, 0, 16256, 0, 16256, 0, 16128, 0, 16256, 0, 16256, 0, 16256, 0, 16256, 0, 16256, 0, 16256, 0, 16256, 0, 16256, 0, 16256, 0, 16256, 0, 16256, 0, 16256, 0, 16256, 0, 16256, 0, 16256, 0, 16256, 0, 16256, 0, 16256, 0, 16256, 0, 16256, 0, 16256, 0, 16256, 0, 16256, 0, 16256, 0, 16256, 0, 16256, 0, 16256, 0, 16256, 0, 16256, 0, 16256, 0, 16256, 0, 16256, 0, 16256, 0, 16256, 0, 16256, 0, 16128, 0, 16256, 0, 16256, 0, 16256, 0, 16128, 0, 16256, 0, 16256, 0, 16256, 0, 16256, 0, 16256, 0, 16256, 0, 16256, 0, 16256, 0, 16256, 0, 16256, 0, 16256, 0, 16256, 0, 16256, 0, 16256, 0, 16256, 0, 16256, 0, 16256, 0, 16256, 0, 16256, 0, 16256, 0, 16256, 0, 16256, 0, 16256, 0, 16256, 0, 16256, 0, 16256, 0, 16256, 0, 16256, 0, 16256, 0, 16128, 0, 16256, 0, 16256, 0, 16256, 0, 16128, 0, 16256, 0, 16256, 0, 16256, 0, 16256, 0, 16256, 0, 16256, 0, 16256, 0, 16256, 0, 16256, 0, 16256, 0, 16256, 0, 16256, 0, 16256, 0, 16256, 0, 16256, 0, 16256, 0, 16256, 0, 16256, 0, 16256, 0, 16256, 0, 16256, 0, 16256, 0, 16256, 0, 16256, 0, 16256, 0, 16256, 0, 16256, 0, 16128, 0, 16256, 0, 16256, 0, 16256, 0, 16128, 0, 16256, 0, 16256, 0, 16256, 0, 16256, 0, 16256, 0, 16256, 0, 16256, 0, 16256, 0, 16256, 0, 16256, 0, 16256, 0, 16256, 0, 16256, 0, 16256, 0, 16256, 0, 16256, 0, 16256, 0, 16256, 0, 16256, 0, 16256, 0, 16256, 0, 16256, 0, 16256, 0, 16256, 0, 16256, 0, 16128, 0, 16256, 0, 16256, 0, 16256, 0, 16128, 0, 16256, 0, 16256, 0, 16256, 0, 16256, 0, 16256, 0, 16256, 0, 16256, 0, 16256, 0, 16256, 0, 16256, 0, 16256, 0, 16256, 0, 16256, 0, 16256, 0, 16256, 0, 16256, 0, 16256, 0, 16256, 0, 16256, 0, 0, 0, 0, 0, 0, 0, 0, 0, 0, 0, 0, 0, 0, 0, 0, 0, 0, 0, 0, 0, 0, 0, 0, 0, 0, 0, 0, 0, 0, 0, 0, 0, 0, 0, 0, 0, 0, 0, 0, 0, 0, 0, 0, 0, 0, 0, 0, 0, 0, 0, 0, 0, 0, 0, 0, 0, 0, 0, 0, 0, 0, 0, 0, 0, 0, 0, 0, 0, 0, 0, 0, 0, 0, 0, 0, 0, 0, 0, 0, 0, 0, 0, 0, 0, 0, 0, 0, 0, 0, 0, 0, 0, 0, 0, 0, 0, 0, 0, 0, 0, 0, 0, 0, 0, 0, 0, 0, 0, 0, 0, 0, 0, 0, 0, 0, 0, 0, 0, 0, 0, 0, 0, 0, 0, 0, 0, 0, 0, 16768, 0, 16768, 0, 16768, 0, 16768, 0, 16768, 0, 0, 0, 0, 0, 0, 0, 16256, 0, 16256, 0, 16128, 0, 16256, 0, 16128, 0, 16256, 0, 16128, 0, 16256, 0, 16768, 0, 16768, 0, 16768, 0, 16768, 0, 16768, 0, 0, 0, 0, 0, 0, 0, 16256, 0, 16256, 0, 16256, 0, 16256, 0, 16128, 0, 17280, 0, 16256, 0, 16256, 0, 16128, 0, 16256, 0, 16256, 0, 16256, 0, 16128, 0, 16256, 0, 16256, 0, 16256, 0, 16256, 0, 16256, 0, 16256, 0, 16256, 0, 16256, 0, 16256, 0, 16256, 0, 16256, 0, 16256, 0, 16256, 0, 16256, 0, 16256, 0, 16256, 0, 16256, 0, 16256, 0, 16256, 0, 16256, 0, 16256, 0, 16256, 0, 16256, 0, 16256, 0, 16256, 0, 16256, 0, 16256, 0, 16256, 0, 16256, 0, 16256, 0, 16256, 0, 16256, 0, 16256, 0, 16256, 0, 16256, 0, 16128, 0, 16256, 0, 16256, 0, 16256, 0, 16128, 0, 16256, 0, 16256, 0, 16256, 0, 16256, 0, 16256, 0, 16256, 0, 16256, 0, 16256, 0, 16256, 0, 16256, 0, 16256, 0, 16256, 0, 16256, 0, 16256, 0, 16256, 0, 16256, 0, 16256, 0, 16256, 0, 16256, 0, 16256, 0, 16256, 0, 16256, 0, 16256, 0, 16256, 0, 16256, 0, 16256, 0, 16256, 0, 16256, 0, 16256, 0, 16128, 0, 16256, 0, 16256, 0, 16256, 0, 16128, 0, 16256, 0, 16256, 0, 16256, 0, 16256, 0, 16256, 0, 16256, 0, 16256, 0, 16256, 0, 16256, 0, 16256, 0, 16256, 0, 16256, 0, 16256, 0, 16256, 0, 16256, 0, 16256, 0, 16256, 0, 16256, 0, 16256, 0, 16256, 0, 16256, 0, 16256, 0, 16256, 0, 16256, 0, 16256, 0, 16256, 0, 16256, 0, 16128, 0, 16256, 0, 16256, 0, 16256, 0, 16128, 0, 16256, 0, 16256, 0, 16256, 0, 16256, 0, 16256, 0, 16256, 0, 16256, 0, 16256, 0, 16256, 0, 16256, 0, 16256, 0, 16256, 0, 16256, 0, 16256, 0, 16256, 0, 16256, 0, 16256, 0, 16256, 0, 16256, 0, 16256, 0, 16256, 0, 16256, 0, 16256, 0, 16256, 0, 16256, 0, 16128, 0, 16256, 0, 16256, 0, 16256, 0, 16128, 0, 16256, 0, 16256, 0, 16256, 0, 16256, 0, 16256, 0, 16256, 0, 16256, 0, 16256, 0, 16256, 0, 16256, 0, 16256, 0, 16256, 0, 16256, 0, 16256, 0, 16256, 0, 16256, 0, 16256, 0, 16256, 0, 16256, 0, 0, 0, 0, 0, 0, 0, 0, 0, 0, 0, 0, 0, 0, 0, 0, 0, 0, 0, 0, 0, 0, 0, 0, 0, 0, 0, 0, 0, 0, 0, 0, 0, 0, 0, 0, 0, 0, 0, 0, 0, 0, 0, 0, 0, 0, 0, 0, 0, 0, 0, 0, 0, 0, 0, 0, 0, 0, 0, 0, 0, 0, 0, 0, 0, 0, 0, 0, 0, 0, 0, 0, 0, 0, 0, 0, 0, 0, 0, 0, 0, 0, 0, 0, 0, 0, 0, 0, 0, 0, 0, 0, 0, 0, 0, 0, 0, 0, 0, 0, 0, 0, 0, 0, 0, 0, 0, 0, 0, 0, 0, 0, 0, 0, 0, 0, 0, 0, 0, 0, 0, 0, 0, 0, 0, 0, 0, 0};

__global__ __launch_bounds__(512) void xd_kernel(const int* idx, const u16* w1, const u16* table, const u16* b1, const u16* ws_cin, const u16* wout, char* ws, const u16* w2, const u16* b_out, u16* out) {
  __shared__ __attribute__((aligned(1024))) char shm[163840];
  unsigned tid = threadIdx.x;
  asm volatile(
    "s_load_dwordx16 s[4:19], s[0:1], 0\n"
    ".Lxd_top:\n"
    "v_readfirstlane_b32 s43, v0\n"
    "s_setprio 3\n"
    "s_cmp_lt_u32 s2, 8\n"
    "s_cbranch_scc1 .Lxd_s2\n"
    "s_cmp_lt_u32 s2, 48\n"
    "s_cbranch_scc1 .Lxd_help\n"
    "s_and_b32 s24, s2, 15\n"
    "s_mul_i32 s40, s43, 36\n"
    "s_mul_i32 s41, s24, 26624\n"
    "s_add_u32 s38, s40, s41\n"
    "v_lshlrev_b32 v86, 4, v0\n"
    "s_lshl_b32 s34, s43, 6\n"
    "s_add_u32 m0, s34, 98304\n"
    "s_waitcnt lgkmcnt(0)\n"
    "s_add_u32 s38, s4, s38\n"
    "s_addc_u32 s39, s5, 0\n"
    "global_load_lds_dwordx4 v86, s[38:39] offset:0\n"
    "global_load_lds_dwordx4 v86, s[38:39] offset:1024\n"
    "global_load_lds_dwordx4 v86, s[38:39] offset:2048\n"
    "global_load_lds_dwordx4 v86, s[38:39] offset:2304\n"
    "s_sub_u32 s3, s2, 48\n"
    "s_and_b32 s28, s2, 7\n"
    "s_lshr_b32 s23, s3, 4\n"
    "s_mul_i32 s42, s28, 3\n"
    "v_bfe_u32 v85, v0, 4, 2\n"
    "v_lshlrev_b32 v87, 1, v0\n"
    "v_and_b32 v87, 0xe0, v87\n"
    "v_and_b32 v88, 0xf0, v86\n"
    "v_xor_b32 v87, v87, v88\n"
    "s_movk_i32 s41, 3328\n"
    "v_mad_u32_u24 v46, v85, s41, v87\n"
    "v_add_u32 v47, 12288, v46\n"
    "s_mul_i32 s41, s43, 416\n"
    "s_lshl_b32 s40, s23, 8\n"
    "s_add_u32 s41, s41, s40\n"
    "s_mul_i32 s30, s42, 212992\n"
    "s_lshl_b32 s35, s43, 5\n"
    "s_bitset1_b32 s35, 15\n"
    "s_add_u32 s6, s6, s41\n"
    "s_addc_u32 s7, s7, 0\n"
    "s_add_u32 s30, s6, s30\n"
    "s_addc_u32 s31, s7, 0\n"
    "s_add_u32 m0, s35, 0\n"
    "s_nop 0\n"
    "global_load_lds_dwordx4 v46, s[30:31] offset:0\n"
    "global_load_lds_dwordx4 v47, s[30:31] offset:1024\n"
    "s_add_u32 s30, s30, 212992\n"
    "s_addc_u32 s31, s31, 0\n"
    "s_setprio 2\n"
    "v_and_b32 v84, 3, v0\n"
    "v_bfe_u32 v44, v0, 3, 3\n"
    "v_lshl_add_u32 v44, v84, 3, v44\n"
    "s_add_u32 s40, s34, 98304\n"
    "v_mul_u32_u24 v44, 104, v44\n"
    "v_add_u32 v44, s40, v44\n"
    "v_lshl_add_u32 v45, s42, 2, v44\n"
    "v_xor_b32 v51, v0, v86\n"
    "v_and_b32 v86, 0x70, v86\n"
    "v_bfi_b32 v86, 48, v51, v86\n"
    "v_xor_b32 v51, 64, v86\n"
    "v_add_u32 v48, 3072, v86\n"
    "v_add_u32 v49, 2048, v51\n"
    "v_add_u32 v50, 1024, v86\n"
    "s_sub_u32 s36, s8, 3072\n"
    "s_subb_u32 s37, s9, 0\n"
    "s_add_u32 m0, s35, 49152\n"
    "s_nop 0\n"
    "global_load_lds_dwordx4 v46, s[30:31] offset:0\n"
    "global_load_lds_dwordx4 v47, s[30:31] offset:1024\n"
    "s_add_u32 s30, s30, 212992\n"
    "s_addc_u32 s31, s31, 0\n"
    "s_add_u32 m0, s35, 98304\n"
    "s_nop 0\n"
    "global_load_lds_dwordx4 v46, s[30:31] offset:0\n"
    "global_load_lds_dwordx4 v47, s[30:31] offset:1024\n"
    "s_add_u32 s66, s28, 1\n"
    "s_and_b32 s66, s66, 7\n"
    "s_mul_i32 s66, s66, 638976\n"
    "s_add_u32 s30, s6, s66\n"
    "s_addc_u32 s31, s7, 0\n"
    "s_waitcnt vmcnt(6)\n"
    "ds_read2_b32 v[96:97], v45 offset0:0 offset1:1\n"
    "ds_read_b32 v98, v45 offset:8\n"
    "s_lshr_b32 s20, s43, 6\n"
    "s_lshr_b32 s21, s43, 8\n"
    "v_and_b32 v1, 63, v0\n"
    "s_waitcnt lgkmcnt(0)\n"
    "v_lshlrev_b32 v96, 7, v96\n"
    "v_lshlrev_b32 v97, 7, v97\n"
    "v_lshlrev_b32 v98, 7, v98\n"
    "v_add_u32_dpp v52, v96, v48 quad_perm:[0,0,0,0] row_mask:0xf bank_mask:0xf\n"
    "v_add_u32_dpp v53, v96, v49 quad_perm:[1,1,1,1] row_mask:0xf bank_mask:0xf\n"
    "v_add_u32_dpp v54, v96, v50 quad_perm:[2,2,2,2] row_mask:0xf bank_mask:0xf\n"
    "v_add_u32_dpp v55, v96, v51 quad_perm:[3,3,3,3] row_mask:0xf bank_mask:0xf\n"
    "s_add_u32 m0, s34, 0\n"
    "s_nop 0\n"
    "global_load_lds_dwordx4 v52, s[36:37] offset:0\n"
    "global_load_lds_dwordx4 v53, s[36:37] offset:1024\n"
    "global_load_lds_dwordx4 v54, s[36:37] offset:2048\n"
    "global_load_lds_dwordx4 v55, s[36:37] offset:3072\n"
    "s_setprio 1\n"
    "v_add_u32_dpp v52, v97, v48 quad_perm:[0,0,0,0] row_mask:0xf bank_mask:0xf\n"
    "v_add_u32_dpp v53, v97, v49 quad_perm:[1,1,1,1] row_mask:0xf bank_mask:0xf\n"
    "v_add_u32_dpp v54, v97, v50 quad_perm:[2,2,2,2] row_mask:0xf bank_mask:0xf\n"
    "v_add_u32_dpp v55, v97, v51 quad_perm:[3,3,3,3] row_mask:0xf bank_mask:0xf\n"
    "s_load_dwordx4 s[68:71], s[0:1], 64\n"
    "s_add_u32 s40, s28, 1\n"
    "s_and_b32 s40, s40, 7\n"
    "v_mad_u32_u24 v45, s40, 12, v44\n"
    "ds_read2_b32 v[100:101], v45 offset0:1 offset1:2\n"
    "ds_read_b32 v99, v45 offset:0\n"
    "s_add_u32 s40, s28, 2\n"
    "s_and_b32 s40, s40, 7\n"
    "v_mad_u32_u24 v45, s40, 12, v44\n"
    "ds_read2_b32 v[102:103], v45 offset0:0 offset1:1\n"
    "ds_read_b32 v104, v45 offset:8\n"
    "s_add_u32 s40, s28, 3\n"
    "s_and_b32 s40, s40, 7\n"
    "v_mad_u32_u24 v45, s40, 12, v44\n"
    "ds_read2_b32 v[106:107], v45 offset0:1 offset1:2\n"
    "ds_read_b32 v105, v45 offset:0\n"
    "s_add_u32 s40, s28, 4\n"
    "s_and_b32 s40, s40, 7\n"
    "v_mad_u32_u24 v45, s40, 12, v44\n"
    "ds_read2_b32 v[108:109], v45 offset0:0 offset1:1\n"
    "ds_read_b32 v110, v45 offset:8\n"
    "s_add_u32 s40, s28, 5\n"
    "s_and_b32 s40, s40, 7\n"
    "v_mad_u32_u24 v45, s40, 12, v44\n"
    "ds_read2_b32 v[112:113], v45 offset0:1 offset1:2\n"
    "ds_read_b32 v111, v45 offset:0\n"
    "s_add_u32 s40, s28, 6\n"
    "s_and_b32 s40, s40, 7\n"
    "v_mad_u32_u24 v45, s40, 12, v44\n"
    "ds_read2_b32 v[114:115], v45 offset0:0 offset1:1\n"
    "ds_read_b32 v116, v45 offset:8\n"
    "s_add_u32 s40, s28, 7\n"
    "s_and_b32 s40, s40, 7\n"
    "v_mad_u32_u24 v45, s40, 12, v44\n"
    "ds_read2_b32 v[118:119], v45 offset0:1 offset1:2\n"
    "ds_read_b32 v117, v45 offset:0\n"
    "ds_read_b64 v[120:121], v44 offset:96\n"
    "v_lshrrev_b32 v86, 5, v88\n"
    "v_xor_b32 v86, v86, v85\n"
    "v_lshlrev_b32 v86, 4, v86\n"
    "v_lshl_add_u32 v86, v88, 3, v86\n"
    "s_and_b32 s41, s20, 1\n"
    "s_lshl1_add_u32 s40, s21, s41\n"
    "v_lshl_add_u32 v56, s40, 13, v86\n"
    "v_xor_b32 v57, 64, v56\n"
    "v_lshrrev_b32 v86, 6, v88\n"
    "v_lshl_add_u32 v88, v85, 3, v86\n"
    "v_bfe_u32 v86, v0, 2, 3\n"
    "s_and_b32 s40, s20, 2\n"
    "s_lshl1_add_u32 s40, s40, 1024\n"
    "v_xor_b32 v86, s40, v86\n"
    "v_lshlrev_b32 v86, 5, v86\n"
    "v_lshl_add_u32 v86, v88, 8, v86\n"
    "v_lshl_add_u32 v76, v84, 3, v86\n"
    "v_xor_b32 v77, 32, v76\n"
    "v_xor_b32 v78, 64, v76\n"
    "v_xor_b32 v79, 96, v76\n"
    "v_lshlrev_b32 v92, 2, v0\n"
    "v_add_u32_e64 v93, s21, -1\n"
    "v_add_u32 v92, 149504, v92\n"
    "ds_write_b32 v92, v93\n"
    "s_waitcnt lgkmcnt(0)\n"
    "v_lshlrev_b32 v99, 7, v99\n"
    "s_setprio 0\n"
    "s_cmp_eq_u32 s21, 0\n"
    "s_cbranch_scc1 .Lxd_g0\n"
    "s_waitcnt vmcnt(8)\n"
    "s_barrier\n"
    ".Lxd_g0:\n"
    "s_add_u32 m0, s34, 49152\n"
    "s_nop 0\n"
    "global_load_lds_dwordx4 v52, s[36:37] offset:0\n"
    "global_load_lds_dwordx4 v53, s[36:37] offset:1024\n"
    "global_load_lds_dwordx4 v54, s[36:37] offset:2048\n"
    "global_load_lds_dwordx4 v55, s[36:37] offset:3072\n"
    "v_add_u32_dpp v52, v98, v48 quad_perm:[0,0,0,0] row_mask:0xf bank_mask:0xf\n"
    "v_add_u32_dpp v53, v98, v49 quad_perm:[1,1,1,1] row_mask:0xf bank_mask:0xf\n"
    "v_add_u32_dpp v54, v98, v50 quad_perm:[2,2,2,2] row_mask:0xf bank_mask:0xf\n"
    "v_add_u32_dpp v55, v98, v51 quad_perm:[3,3,3,3] row_mask:0xf bank_mask:0xf\n"
    "s_waitcnt vmcnt(4) lgkmcnt(0)\n"
    "s_barrier\n"
    "ds_read_b128 v[128:131], v56 offset:0\n"
    "ds_read_b128 v[132:135], v56 offset:2048\n"
    "ds_read_b128 v[136:139], v56 offset:4096\n"
    "ds_read_b128 v[140:143], v56 offset:6144\n"
    "ds_read_b64_tr_b16 v[144:145], v76 offset:0\n"
    "ds_read_b64_tr_b16 v[146:147], v76 offset:1024\n"
    "ds_read_b64_tr_b16 v[148:149], v77 offset:0\n"
    "ds_read_b64_tr_b16 v[150:151], v77 offset:1024\n"
    "ds_read_b64_tr_b16 v[152:153], v78 offset:0\n"
    "ds_read_b64_tr_b16 v[154:155], v78 offset:1024\n"
    "ds_read_b64_tr_b16 v[156:157], v79 offset:0\n"
    "ds_read_b64_tr_b16 v[158:159], v79 offset:1024\n"
    "ds_read_b128 v[160:163], v57 offset:0\n"
    "ds_read_b128 v[164:167], v57 offset:2048\n"
    "ds_read_b128 v[168:171], v57 offset:4096\n"
    "ds_read_b128 v[172:175], v57 offset:6144\n"
    "ds_read_b64_tr_b16 v[176:177], v76 offset:8192\n"
    "ds_read_b64_tr_b16 v[178:179], v76 offset:9216\n"
    "ds_read_b64_tr_b16 v[180:181], v77 offset:8192\n"
    "ds_read_b64_tr_b16 v[182:183], v77 offset:9216\n"
    "ds_read_b64_tr_b16 v[184:185], v78 offset:8192\n"
    "ds_read_b64_tr_b16 v[186:187], v78 offset:9216\n"
    "ds_read_b64_tr_b16 v[188:189], v79 offset:8192\n"
    "ds_read_b64_tr_b16 v[190:191], v79 offset:9216\n"
    "s_add_u32 m0, s34, 98304\n"
    "s_nop 0\n"
    "global_load_lds_dwordx4 v52, s[36:37] offset:0\n"
    "global_load_lds_dwordx4 v53, s[36:37] offset:1024\n"
    "global_load_lds_dwordx4 v54, s[36:37] offset:2048\n"
    "global_load_lds_dwordx4 v55, s[36:37] offset:3072\n"
    "s_waitcnt lgkmcnt(0)\n"
    "s_barrier\n"
    "v_mfma_f32_16x16x32_bf16 v[192:195], v[144:147], v[128:131], 0\n"
    "v_lshlrev_b32 v100, 7, v100\n"
    "v_mfma_f32_16x16x32_bf16 v[192:195], v[176:179], v[160:163], v[192:195]\n"
    "v_add_u32_dpp v52, v99, v48 quad_perm:[0,0,0,0] row_mask:0xf bank_mask:0xf\n"
    "v_mfma_f32_16x16x32_bf16 v[196:199], v[176:179], v[164:167], 0\n"
    "v_mfma_f32_16x16x32_bf16 v[196:199], v[144:147], v[132:135], v[196:199]\n"
    "v_add_u32_dpp v53, v99, v49 quad_perm:[1,1,1,1] row_mask:0xf bank_mask:0xf\n"
    "v_mfma_f32_16x16x32_bf16 v[200:203], v[144:147], v[136:139], 0\n"
    "v_mfma_f32_16x16x32_bf16 v[200:203], v[176:179], v[168:171], v[200:203]\n"
    "v_add_u32_dpp v54, v99, v50 quad_perm:[2,2,2,2] row_mask:0xf bank_mask:0xf\n"
    "v_mfma_f32_16x16x32_bf16 v[204:207], v[176:179], v[172:175], 0\n"
    "v_mfma_f32_16x16x32_bf16 v[204:207], v[144:147], v[140:143], v[204:207]\n"
    "v_add_u32_dpp v55, v99, v51 quad_perm:[3,3,3,3] row_mask:0xf bank_mask:0xf\n"
    "v_mfma_f32_16x16x32_bf16 v[220:223], v[148:151], v[140:143], 0\n"
    "v_mfma_f32_16x16x32_bf16 v[220:223], v[180:183], v[172:175], v[220:223]\n"
    "s_bfe_u32 s40, s34, 0x1000d\n"
    "v_mfma_f32_16x16x32_bf16 v[216:219], v[180:183], v[168:171], 0\n"
    "v_mfma_f32_16x16x32_bf16 v[216:219], v[148:151], v[136:139], v[216:219]\n"
    "s_lshl1_add_u32 s40, s23, s40\n"
    "v_mfma_f32_16x16x32_bf16 v[212:215], v[148:151], v[132:135], 0\n"
    "v_mfma_f32_16x16x32_bf16 v[212:215], v[180:183], v[164:167], v[212:215]\n"
    "s_lshl_b32 s40, s40, 7\n"
    "v_mfma_f32_16x16x32_bf16 v[208:211], v[180:183], v[160:163], 0\n"
    "v_mfma_f32_16x16x32_bf16 v[208:211], v[148:151], v[128:131], v[208:211]\n"
    "v_lshl_add_u32 v42, v85, 3, s40\n"
    "v_mfma_f32_16x16x32_bf16 v[224:227], v[152:155], v[128:131], 0\n"
    "v_mfma_f32_16x16x32_bf16 v[224:227], v[184:187], v[160:163], v[224:227]\n"
    "s_lshl_b32 s24, s24, 8\n"
    "v_mfma_f32_16x16x32_bf16 v[228:231], v[184:187], v[164:167], 0\n"
    "v_mfma_f32_16x16x32_bf16 v[228:231], v[152:155], v[132:135], v[228:231]\n"
    "v_add_u32 v58, 98304, v56\n"
    "v_mfma_f32_16x16x32_bf16 v[232:235], v[152:155], v[136:139], 0\n"
    "v_mfma_f32_16x16x32_bf16 v[232:235], v[184:187], v[168:171], v[232:235]\n"
    "v_add_u32 v59, 98304, v57\n"
    "v_mfma_f32_16x16x32_bf16 v[236:239], v[184:187], v[172:175], 0\n"
    "v_mfma_f32_16x16x32_bf16 v[236:239], v[152:155], v[140:143], v[236:239]\n"
    "v_add_u32 v80, 98304, v76\n"
    "v_mfma_f32_16x16x32_bf16 v[252:255], v[156:159], v[140:143], 0\n"
    "v_mfma_f32_16x16x32_bf16 v[252:255], v[188:191], v[172:175], v[252:255]\n"
    "v_add_u32 v81, 98304, v77\n"
    "v_mfma_f32_16x16x32_bf16 v[248:251], v[188:191], v[168:171], 0\n"
    "v_mfma_f32_16x16x32_bf16 v[248:251], v[156:159], v[136:139], v[248:251]\n"
    "v_add_u32 v82, 98304, v78\n"
    "v_mfma_f32_16x16x32_bf16 v[244:247], v[156:159], v[132:135], 0\n"
    "v_mfma_f32_16x16x32_bf16 v[244:247], v[188:191], v[164:167], v[244:247]\n"
    "v_add_u32 v83, 98304, v79\n"
    "v_mfma_f32_16x16x32_bf16 v[240:243], v[188:191], v[160:163], 0\n"
    "v_mfma_f32_16x16x32_bf16 v[240:243], v[156:159], v[128:131], v[240:243]\n"
    "s_waitcnt vmcnt(4)\n"
    "s_barrier\n"
    "s_add_u32 m0, s35, 0\n"
    "s_nop 0\n"
    "global_load_lds_dwordx4 v46, s[30:31] offset:0\n"
    "ds_read_b128 v[128:131], v56 offset:49152\n"
    "ds_read_b128 v[132:135], v56 offset:51200\n"
    "ds_read_b128 v[136:139], v56 offset:53248\n"
    "ds_read_b128 v[140:143], v56 offset:55296\n"
    "global_load_lds_dwordx4 v47, s[30:31] offset:1024\n"
    "s_add_u32 s30, s30, 212992\n"
    "s_addc_u32 s31, s31, 0\n"
    "ds_read_b64_tr_b16 v[144:145], v76 offset:49152\n"
    "ds_read_b64_tr_b16 v[146:147], v76 offset:50176\n"
    "ds_read_b64_tr_b16 v[148:149], v77 offset:49152\n"
    "ds_read_b64_tr_b16 v[150:151], v77 offset:50176\n"
    "s_add_u32 m0, s34, 0\n"
    "s_nop 0\n"
    "global_load_lds_dwordx4 v52, s[36:37] offset:0\n"
    "ds_read_b64_tr_b16 v[152:153], v78 offset:49152\n"
    "ds_read_b64_tr_b16 v[154:155], v78 offset:50176\n"
    "ds_read_b64_tr_b16 v[156:157], v79 offset:49152\n"
    "ds_read_b64_tr_b16 v[158:159], v79 offset:50176\n"
    "global_load_lds_dwordx4 v53, s[36:37] offset:1024\n"
    "ds_read_b128 v[160:163], v57 offset:49152\n"
    "ds_read_b128 v[164:167], v57 offset:51200\n"
    "ds_read_b128 v[168:171], v57 offset:53248\n"
    "ds_read_b128 v[172:175], v57 offset:55296\n"
    "global_load_lds_dwordx4 v54, s[36:37] offset:2048\n"
    "ds_read_b64_tr_b16 v[176:177], v76 offset:57344\n"
    "ds_read_b64_tr_b16 v[178:179], v76 offset:58368\n"
    "ds_read_b64_tr_b16 v[180:181], v77 offset:57344\n"
    "ds_read_b64_tr_b16 v[182:183], v77 offset:58368\n"
    "global_load_lds_dwordx4 v55, s[36:37] offset:3072\n"
    "ds_read_b64_tr_b16 v[184:185], v78 offset:57344\n"
    "ds_read_b64_tr_b16 v[186:187], v78 offset:58368\n"
    "ds_read_b64_tr_b16 v[188:189], v79 offset:57344\n"
    "ds_read_b64_tr_b16 v[190:191], v79 offset:58368\n"
    "s_waitcnt lgkmcnt(0)\n"
    "s_barrier\n"
    "v_mfma_f32_16x16x32_bf16 v[192:195], v[144:147], v[128:131], v[192:195]\n"
    "v_lshlrev_b32 v101, 7, v101\n"
    "v_mfma_f32_16x16x32_bf16 v[192:195], v[176:179], v[160:163], v[192:195]\n"
    "v_mfma_f32_16x16x32_bf16 v[196:199], v[176:179], v[164:167], v[196:199]\n"
    "v_add_u32_dpp v52, v100, v48 quad_perm:[0,0,0,0] row_mask:0xf bank_mask:0xf\n"
    "v_mfma_f32_16x16x32_bf16 v[196:199], v[144:147], v[132:135], v[196:199]\n"
    "v_mfma_f32_16x16x32_bf16 v[200:203], v[144:147], v[136:139], v[200:203]\n"
    "v_add_u32_dpp v53, v100, v49 quad_perm:[1,1,1,1] row_mask:0xf bank_mask:0xf\n"
    "v_mfma_f32_16x16x32_bf16 v[200:203], v[176:179], v[168:171], v[200:203]\n"
    "v_mfma_f32_16x16x32_bf16 v[204:207], v[176:179], v[172:175], v[204:207]\n"
    "v_add_u32_dpp v54, v100, v50 quad_perm:[2,2,2,2] row_mask:0xf bank_mask:0xf\n"
    "v_mfma_f32_16x16x32_bf16 v[204:207], v[144:147], v[140:143], v[204:207]\n"
    "v_mfma_f32_16x16x32_bf16 v[220:223], v[148:151], v[140:143], v[220:223]\n"
    "v_add_u32_dpp v55, v100, v51 quad_perm:[3,3,3,3] row_mask:0xf bank_mask:0xf\n"
    "v_mfma_f32_16x16x32_bf16 v[220:223], v[180:183], v[172:175], v[220:223]\n"
    "v_mfma_f32_16x16x32_bf16 v[216:219], v[180:183], v[168:171], v[216:219]\n"
    "s_mul_i32 s47, s3, 5042\n"
    "v_mfma_f32_16x16x32_bf16 v[216:219], v[148:151], v[136:139], v[216:219]\n"
    "v_mfma_f32_16x16x32_bf16 v[212:215], v[148:151], v[132:135], v[212:215]\n"
    "s_lshr_b32 s47, s47, 16\n"
    "v_mfma_f32_16x16x32_bf16 v[212:215], v[180:183], v[164:167], v[212:215]\n"
    "v_mfma_f32_16x16x32_bf16 v[208:211], v[180:183], v[160:163], v[208:211]\n"
    "s_lshr_b32 s21, s20, 2\n"
    "v_mfma_f32_16x16x32_bf16 v[208:211], v[148:151], v[128:131], v[208:211]\n"
    "v_mfma_f32_16x16x32_bf16 v[224:227], v[152:155], v[128:131], v[224:227]\n"
    "s_lshl1_add_u32 s22, s21, s20\n"
    "v_mfma_f32_16x16x32_bf16 v[224:227], v[184:187], v[160:163], v[224:227]\n"
    "v_mfma_f32_16x16x32_bf16 v[228:231], v[184:187], v[164:167], v[228:231]\n"
    "s_and_b32 s22, s22, 3\n"
    "v_mfma_f32_16x16x32_bf16 v[228:231], v[152:155], v[132:135], v[228:231]\n"
    "v_mfma_f32_16x16x32_bf16 v[232:235], v[152:155], v[136:139], v[232:235]\n"
    "s_lshl_b32 s58, s21, 8\n"
    "v_mfma_f32_16x16x32_bf16 v[232:235], v[184:187], v[168:171], v[232:235]\n"
    "v_mfma_f32_16x16x32_bf16 v[236:239], v[184:187], v[172:175], v[236:239]\n"
    "s_add_u32 s58, s58, 150528\n"
    "v_mfma_f32_16x16x32_bf16 v[236:239], v[152:155], v[140:143], v[236:239]\n"
    "v_mfma_f32_16x16x32_bf16 v[252:255], v[156:159], v[140:143], v[252:255]\n"
    "s_mov_b32 s40, s47\n"
    "v_mfma_f32_16x16x32_bf16 v[252:255], v[188:191], v[172:175], v[252:255]\n"
    "v_mfma_f32_16x16x32_bf16 v[248:251], v[188:191], v[168:171], v[248:251]\n"
    "s_mul_i32 s54, s40, 512\n"
    "v_mfma_f32_16x16x32_bf16 v[248:251], v[156:159], v[136:139], v[248:251]\n"
    "v_mfma_f32_16x16x32_bf16 v[244:247], v[156:159], v[132:135], v[244:247]\n"
    "s_add_u32 s54, s54, 2\n"
    "v_mfma_f32_16x16x32_bf16 v[244:247], v[188:191], v[164:167], v[244:247]\n"
    "v_mfma_f32_16x16x32_bf16 v[240:243], v[188:191], v[160:163], v[240:243]\n"
    "v_mfma_f32_16x16x32_bf16 v[240:243], v[156:159], v[128:131], v[240:243]\n"
    "s_waitcnt vmcnt(6)\n"
    "s_barrier\n"
    "s_add_u32 m0, s35, 49152\n"
    "s_nop 0\n"
    "global_load_lds_dwordx4 v46, s[30:31] offset:0\n"
    "ds_read_b128 v[128:131], v58 offset:0\n"
    "ds_read_b128 v[132:135], v58 offset:2048\n"
    "ds_read_b128 v[136:139], v58 offset:4096\n"
    "ds_read_b128 v[140:143], v58 offset:6144\n"
    "global_load_lds_dwordx4 v47, s[30:31] offset:1024\n"
    "s_add_u32 s30, s30, 212992\n"
    "s_addc_u32 s31, s31, 0\n"
    "ds_read_b64_tr_b16 v[144:145], v80 offset:0\n"
    "ds_read_b64_tr_b16 v[146:147], v80 offset:1024\n"
    "ds_read_b64_tr_b16 v[148:149], v81 offset:0\n"
    "ds_read_b64_tr_b16 v[150:151], v81 offset:1024\n"
    "s_add_u32 m0, s34, 49152\n"
    "s_nop 0\n"
    "global_load_lds_dwordx4 v52, s[36:37] offset:0\n"
    "ds_read_b64_tr_b16 v[152:153], v82 offset:0\n"
    "ds_read_b64_tr_b16 v[154:155], v82 offset:1024\n"
    "ds_read_b64_tr_b16 v[156:157], v83 offset:0\n"
    "ds_read_b64_tr_b16 v[158:159], v83 offset:1024\n"
    "global_load_lds_dwordx4 v53, s[36:37] offset:1024\n"
    "ds_read_b128 v[160:163], v59 offset:0\n"
    "ds_read_b128 v[164:167], v59 offset:2048\n"
    "ds_read_b128 v[168:171], v59 offset:4096\n"
    "ds_read_b128 v[172:175], v59 offset:6144\n"
    "global_load_lds_dwordx4 v54, s[36:37] offset:2048\n"
    "ds_read_b64_tr_b16 v[176:177], v80 offset:8192\n"
    "ds_read_b64_tr_b16 v[178:179], v80 offset:9216\n"
    "ds_read_b64_tr_b16 v[180:181], v81 offset:8192\n"
    "ds_read_b64_tr_b16 v[182:183], v81 offset:9216\n"
    "global_load_lds_dwordx4 v55, s[36:37] offset:3072\n"
    "ds_read_b64_tr_b16 v[184:185], v82 offset:8192\n"
    "ds_read_b64_tr_b16 v[186:187], v82 offset:9216\n"
    "ds_read_b64_tr_b16 v[188:189], v83 offset:8192\n"
    "ds_read_b64_tr_b16 v[190:191], v83 offset:9216\n"
    "s_waitcnt vmcnt(10) lgkmcnt(0)\n"
    "s_barrier\n"
    "v_mfma_f32_16x16x32_bf16 v[192:195], v[144:147], v[128:131], v[192:195]\n"
    "v_lshlrev_b32 v102, 7, v102\n"
    "v_mfma_f32_16x16x32_bf16 v[192:195], v[176:179], v[160:163], v[192:195]\n"
    "v_mfma_f32_16x16x32_bf16 v[196:199], v[176:179], v[164:167], v[196:199]\n"
    "v_add_u32_dpp v52, v101, v48 quad_perm:[0,0,0,0] row_mask:0xf bank_mask:0xf\n"
    "v_mfma_f32_16x16x32_bf16 v[196:199], v[144:147], v[132:135], v[196:199]\n"
    "v_mfma_f32_16x16x32_bf16 v[200:203], v[144:147], v[136:139], v[200:203]\n"
    "v_add_u32_dpp v53, v101, v49 quad_perm:[1,1,1,1] row_mask:0xf bank_mask:0xf\n"
    "v_mfma_f32_16x16x32_bf16 v[200:203], v[176:179], v[168:171], v[200:203]\n"
    "v_mfma_f32_16x16x32_bf16 v[204:207], v[176:179], v[172:175], v[204:207]\n"
    "v_add_u32_dpp v54, v101, v50 quad_perm:[2,2,2,2] row_mask:0xf bank_mask:0xf\n"
    "v_mfma_f32_16x16x32_bf16 v[204:207], v[144:147], v[140:143], v[204:207]\n"
    "v_mfma_f32_16x16x32_bf16 v[220:223], v[148:151], v[140:143], v[220:223]\n"
    "v_add_u32_dpp v55, v101, v51 quad_perm:[3,3,3,3] row_mask:0xf bank_mask:0xf\n"
    "v_mfma_f32_16x16x32_bf16 v[220:223], v[180:183], v[172:175], v[220:223]\n"
    "v_mfma_f32_16x16x32_bf16 v[216:219], v[180:183], v[168:171], v[216:219]\n"
    "s_lshl_b32 s40, s40, 7\n"
    "v_mfma_f32_16x16x32_bf16 v[216:219], v[148:151], v[136:139], v[216:219]\n"
    "v_mfma_f32_16x16x32_bf16 v[212:215], v[148:151], v[132:135], v[212:215]\n"
    "s_add_u32 s40, s40, 32768\n"
    "v_mfma_f32_16x16x32_bf16 v[212:215], v[180:183], v[164:167], v[212:215]\n"
    "v_mfma_f32_16x16x32_bf16 v[208:211], v[180:183], v[160:163], v[208:211]\n"
    "s_add_u32 s52, s16, s40\n"
    "s_addc_u32 s53, s17, 0\n"
    "v_mfma_f32_16x16x32_bf16 v[208:211], v[148:151], v[128:131], v[208:211]\n"
    "v_mfma_f32_16x16x32_bf16 v[224:227], v[152:155], v[128:131], v[224:227]\n"
    "s_mov_b32 s57, 0x7fff7fff\n"
    "v_mfma_f32_16x16x32_bf16 v[224:227], v[184:187], v[160:163], v[224:227]\n"
    "v_mfma_f32_16x16x32_bf16 v[228:231], v[184:187], v[164:167], v[228:231]\n"
    "s_mov_b32 s56, 0\n"
    "v_mfma_f32_16x16x32_bf16 v[228:231], v[152:155], v[132:135], v[228:231]\n"
    "v_mfma_f32_16x16x32_bf16 v[232:235], v[152:155], v[136:139], v[232:235]\n"
    "s_mov_b32 s27, 0\n"
    "v_mfma_f32_16x16x32_bf16 v[232:235], v[184:187], v[168:171], v[232:235]\n"
    "v_mfma_f32_16x16x32_bf16 v[236:239], v[184:187], v[172:175], v[236:239]\n"
    "s_mov_b32 s55, 9\n"
    "v_mfma_f32_16x16x32_bf16 v[236:239], v[152:155], v[140:143], v[236:239]\n"
    "v_mfma_f32_16x16x32_bf16 v[252:255], v[156:159], v[140:143], v[252:255]\n"
    "s_mov_b32 s44, 0\n"
    "v_mfma_f32_16x16x32_bf16 v[252:255], v[188:191], v[172:175], v[252:255]\n"
    "v_mfma_f32_16x16x32_bf16 v[248:251], v[188:191], v[168:171], v[248:251]\n"
    "s_mov_b32 s46, 0\n"
    "v_mfma_f32_16x16x32_bf16 v[248:251], v[156:159], v[136:139], v[248:251]\n"
    "v_mfma_f32_16x16x32_bf16 v[244:247], v[156:159], v[132:135], v[244:247]\n"
    "v_lshrrev_b32 v17, 3, v1\n"
    "v_mfma_f32_16x16x32_bf16 v[244:247], v[188:191], v[164:167], v[244:247]\n"
    "v_mfma_f32_16x16x32_bf16 v[240:243], v[188:191], v[160:163], v[240:243]\n"
    "v_mfma_f32_16x16x32_bf16 v[240:243], v[156:159], v[128:131], v[240:243]\n"
    "s_waitcnt vmcnt(6)\n"
    "s_barrier\n"
    "s_add_u32 m0, s35, 98304\n"
    "s_nop 0\n"
    "global_load_lds_dwordx4 v46, s[30:31] offset:0\n"
    "ds_read_b128 v[128:131], v56 offset:0\n"
    "ds_read_b128 v[132:135], v56 offset:2048\n"
    "ds_read_b128 v[136:139], v56 offset:4096\n"
    "ds_read_b128 v[140:143], v56 offset:6144\n"
    "global_load_lds_dwordx4 v47, s[30:31] offset:1024\n"
    "s_add_u32 s66, s28, 2\n"
    "s_and_b32 s66, s66, 7\n"
    "s_mul_i32 s66, s66, 638976\n"
    "s_add_u32 s30, s6, s66\n"
    "s_addc_u32 s31, s7, 0\n"
    "ds_read_b64_tr_b16 v[144:145], v76 offset:0\n"
    "ds_read_b64_tr_b16 v[146:147], v76 offset:1024\n"
    "ds_read_b64_tr_b16 v[148:149], v77 offset:0\n"
    "ds_read_b64_tr_b16 v[150:151], v77 offset:1024\n"
    "s_add_u32 m0, s34, 98304\n"
    "s_nop 0\n"
    "global_load_lds_dwordx4 v52, s[36:37] offset:0\n"
    "ds_read_b64_tr_b16 v[152:153], v78 offset:0\n"
    "ds_read_b64_tr_b16 v[154:155], v78 offset:1024\n"
    "ds_read_b64_tr_b16 v[156:157], v79 offset:0\n"
    "ds_read_b64_tr_b16 v[158:159], v79 offset:1024\n"
    "global_load_lds_dwordx4 v53, s[36:37] offset:1024\n"
    "ds_read_b128 v[160:163], v57 offset:0\n"
    "ds_read_b128 v[164:167], v57 offset:2048\n"
    "ds_read_b128 v[168:171], v57 offset:4096\n"
    "ds_read_b128 v[172:175], v57 offset:6144\n"
    "global_load_lds_dwordx4 v54, s[36:37] offset:2048\n"
    "ds_read_b64_tr_b16 v[176:177], v76 offset:8192\n"
    "ds_read_b64_tr_b16 v[178:179], v76 offset:9216\n"
    "ds_read_b64_tr_b16 v[180:181], v77 offset:8192\n"
    "ds_read_b64_tr_b16 v[182:183], v77 offset:9216\n"
    "global_load_lds_dwordx4 v55, s[36:37] offset:3072\n"
    "ds_read_b64_tr_b16 v[184:185], v78 offset:8192\n"
    "ds_read_b64_tr_b16 v[186:187], v78 offset:9216\n"
    "ds_read_b64_tr_b16 v[188:189], v79 offset:8192\n"
    "ds_read_b64_tr_b16 v[190:191], v79 offset:9216\n"
    "s_waitcnt vmcnt(10) lgkmcnt(0)\n"
    "s_barrier\n"
    "v_mfma_f32_16x16x32_bf16 v[192:195], v[144:147], v[128:131], v[192:195]\n"
    "v_lshlrev_b32 v103, 7, v103\n"
    "v_mfma_f32_16x16x32_bf16 v[192:195], v[176:179], v[160:163], v[192:195]\n"
    "v_add_u32_dpp v52, v102, v48 quad_perm:[0,0,0,0] row_mask:0xf bank_mask:0xf\n"
    "v_mfma_f32_16x16x32_bf16 v[196:199], v[176:179], v[164:167], v[196:199]\n"
    "v_mfma_f32_16x16x32_bf16 v[196:199], v[144:147], v[132:135], v[196:199]\n"
    "v_add_u32_dpp v53, v102, v49 quad_perm:[1,1,1,1] row_mask:0xf bank_mask:0xf\n"
    "v_mfma_f32_16x16x32_bf16 v[200:203], v[144:147], v[136:139], v[200:203]\n"
    "v_mfma_f32_16x16x32_bf16 v[200:203], v[176:179], v[168:171], v[200:203]\n"
    "v_add_u32_dpp v54, v102, v50 quad_perm:[2,2,2,2] row_mask:0xf bank_mask:0xf\n"
    "v_mfma_f32_16x16x32_bf16 v[204:207], v[176:179], v[172:175], v[204:207]\n"
    "v_mfma_f32_16x16x32_bf16 v[204:207], v[144:147], v[140:143], v[204:207]\n"
    "v_add_u32_dpp v55, v102, v51 quad_perm:[3,3,3,3] row_mask:0xf bank_mask:0xf\n"
    "v_mfma_f32_16x16x32_bf16 v[220:223], v[148:151], v[140:143], v[220:223]\n"
    "v_mfma_f32_16x16x32_bf16 v[220:223], v[180:183], v[172:175], v[220:223]\n"
    "v_mul_u32_u24 v17, 12, v17\n"
    "v_mfma_f32_16x16x32_bf16 v[216:219], v[180:183], v[168:171], v[216:219]\n"
    "v_mfma_f32_16x16x32_bf16 v[216:219], v[148:151], v[136:139], v[216:219]\n"
    "v_and_b32 v19, 7, v1\n"
    "v_mfma_f32_16x16x32_bf16 v[212:215], v[148:151], v[132:135], v[212:215]\n"
    "v_mfma_f32_16x16x32_bf16 v[212:215], v[180:183], v[164:167], v[212:215]\n"
    "v_lshlrev_b32 v19, 4, v19\n"
    "v_mfma_f32_16x16x32_bf16 v[208:211], v[180:183], v[160:163], v[208:211]\n"
    "v_mfma_f32_16x16x32_bf16 v[208:211], v[148:151], v[128:131], v[208:211]\n"
    "v_lshrrev_b32 v18, 5, v1\n"
    "v_mfma_f32_16x16x32_bf16 v[224:227], v[152:155], v[128:131], v[224:227]\n"
    "v_mfma_f32_16x16x32_bf16 v[224:227], v[184:187], v[160:163], v[224:227]\n"
    "v_lshlrev_b32 v18, 2, v18\n"
    "v_mfma_f32_16x16x32_bf16 v[228:231], v[184:187], v[164:167], v[228:231]\n"
    "v_mfma_f32_16x16x32_bf16 v[228:231], v[152:155], v[132:135], v[228:231]\n"
    "v_and_b32 v20, 31, v1\n"
    "v_mfma_f32_16x16x32_bf16 v[232:235], v[152:155], v[136:139], v[232:235]\n"
    "v_mfma_f32_16x16x32_bf16 v[232:235], v[184:187], v[168:171], v[232:235]\n"
    "v_lshlrev_b32 v20, 2, v20\n"
    "v_mfma_f32_16x16x32_bf16 v[236:239], v[184:187], v[172:175], v[236:239]\n"
    "v_mfma_f32_16x16x32_bf16 v[236:239], v[152:155], v[140:143], v[236:239]\n"
    "s_lshl2_add_u32 s40, s22, s58\n"
    "v_mfma_f32_16x16x32_bf16 v[252:255], v[156:159], v[140:143], v[252:255]\n"
    "v_mfma_f32_16x16x32_bf16 v[252:255], v[188:191], v[172:175], v[252:255]\n"
    "s_sub_u32 s40, s40, 20\n"
    "v_mfma_f32_16x16x32_bf16 v[248:251], v[188:191], v[168:171], v[248:251]\n"
    "v_mfma_f32_16x16x32_bf16 v[248:251], v[156:159], v[136:139], v[248:251]\n"
    "v_lshl_add_u32 v25, v1, 18, s40\n"
    "v_mfma_f32_16x16x32_bf16 v[244:247], v[156:159], v[132:135], v[244:247]\n"
    "v_mfma_f32_16x16x32_bf16 v[244:247], v[188:191], v[164:167], v[244:247]\n"
    "s_lshl_b32 s43, s21, 16\n"
    "v_mfma_f32_16x16x32_bf16 v[240:243], v[188:191], v[160:163], v[240:243]\n"
    "v_mfma_f32_16x16x32_bf16 v[240:243], v[156:159], v[128:131], v[240:243]\n"
    "s_waitcnt vmcnt(6)\n"
    "s_barrier\n"
    "s_add_u32 m0, s35, 0\n"
    "s_nop 0\n"
    "global_load_lds_dwordx4 v46, s[30:31] offset:0\n"
    "ds_read_b128 v[128:131], v56 offset:49152\n"
    "ds_read_b128 v[132:135], v56 offset:51200\n"
    "ds_read_b128 v[136:139], v56 offset:53248\n"
    "global_load_lds_dwordx4 v47, s[30:31] offset:1024\n"
    "s_add_u32 s30, s30, 212992\n"
    "s_addc_u32 s31, s31, 0\n"
    "ds_read_b128 v[140:143], v56 offset:55296\n"
    "ds_read_b64_tr_b16 v[144:145], v76 offset:49152\n"
    "ds_read_b64_tr_b16 v[146:147], v76 offset:50176\n"
    "s_add_u32 m0, s34, 0\n"
    "s_nop 0\n"
    "global_load_lds_dwordx4 v52, s[36:37] offset:0\n"
    "ds_read_b64_tr_b16 v[148:149], v77 offset:49152\n"
    "ds_read_b64_tr_b16 v[150:151], v77 offset:50176\n"
    "ds_read_b64_tr_b16 v[152:153], v78 offset:49152\n"
    "global_load_lds_dwordx4 v53, s[36:37] offset:1024\n"
    "ds_read_b64_tr_b16 v[154:155], v78 offset:50176\n"
    "ds_read_b64_tr_b16 v[156:157], v79 offset:49152\n"
    "ds_read_b64_tr_b16 v[158:159], v79 offset:50176\n"
    "global_load_lds_dwordx4 v54, s[36:37] offset:2048\n"
    "ds_read_b128 v[160:163], v57 offset:49152\n"
    "ds_read_b128 v[164:167], v57 offset:51200\n"
    "ds_read_b128 v[168:171], v57 offset:53248\n"
    "global_load_lds_dwordx4 v55, s[36:37] offset:3072\n"
    "ds_read_b128 v[172:175], v57 offset:55296\n"
    "ds_read_b64_tr_b16 v[176:177], v76 offset:57344\n"
    "ds_read_b64_tr_b16 v[178:179], v76 offset:58368\n"
    "global_load_dwordx2 v[84:85], v42, s[10:11] offset:0\n"
    "ds_read_b64_tr_b16 v[180:181], v77 offset:57344\n"
    "ds_read_b64_tr_b16 v[182:183], v77 offset:58368\n"
    "ds_read_b64_tr_b16 v[184:185], v78 offset:57344\n"
    "ds_read_b64_tr_b16 v[186:187], v78 offset:58368\n"
    "ds_read_b64_tr_b16 v[188:189], v79 offset:57344\n"
    "ds_read_b64_tr_b16 v[190:191], v79 offset:58368\n"
    "s_waitcnt vmcnt(11) lgkmcnt(0)\n"
    "s_barrier\n"
    "v_mfma_f32_16x16x32_bf16 v[192:195], v[144:147], v[128:131], v[192:195]\n"
    "v_lshlrev_b32 v104, 7, v104\n"
    "v_mfma_f32_16x16x32_bf16 v[192:195], v[176:179], v[160:163], v[192:195]\n"
    "v_mfma_f32_16x16x32_bf16 v[196:199], v[176:179], v[164:167], v[196:199]\n"
    "v_add_u32_dpp v52, v103, v48 quad_perm:[0,0,0,0] row_mask:0xf bank_mask:0xf\n"
    "v_mfma_f32_16x16x32_bf16 v[196:199], v[144:147], v[132:135], v[196:199]\n"
    "v_mfma_f32_16x16x32_bf16 v[200:203], v[144:147], v[136:139], v[200:203]\n"
    "v_add_u32_dpp v53, v103, v49 quad_perm:[1,1,1,1] row_mask:0xf bank_mask:0xf\n"
    "v_mfma_f32_16x16x32_bf16 v[200:203], v[176:179], v[168:171], v[200:203]\n"
    "v_mfma_f32_16x16x32_bf16 v[204:207], v[176:179], v[172:175], v[204:207]\n"
    "v_add_u32_dpp v54, v103, v50 quad_perm:[2,2,2,2] row_mask:0xf bank_mask:0xf\n"
    "v_mfma_f32_16x16x32_bf16 v[204:207], v[144:147], v[140:143], v[204:207]\n"
    "v_mfma_f32_16x16x32_bf16 v[220:223], v[148:151], v[140:143], v[220:223]\n"
    "v_add_u32_dpp v55, v103, v51 quad_perm:[3,3,3,3] row_mask:0xf bank_mask:0xf\n"
    "v_mfma_f32_16x16x32_bf16 v[220:223], v[180:183], v[172:175], v[220:223]\n"
    "v_mfma_f32_16x16x32_bf16 v[216:219], v[180:183], v[168:171], v[216:219]\n"
    "v_lshrrev_b32 v12, 4, v1\n"
    "v_mfma_f32_16x16x32_bf16 v[216:219], v[148:151], v[136:139], v[216:219]\n"
    "v_mfma_f32_16x16x32_bf16 v[212:215], v[148:151], v[132:135], v[212:215]\n"
    "v_bfe_u32 v13, v1, 2, 2\n"
    "v_mfma_f32_16x16x32_bf16 v[212:215], v[180:183], v[164:167], v[212:215]\n"
    "v_mfma_f32_16x16x32_bf16 v[208:211], v[180:183], v[160:163], v[208:211]\n"
    "v_and_b32 v21, 3, v1\n"
    "v_mfma_f32_16x16x32_bf16 v[208:211], v[148:151], v[128:131], v[208:211]\n"
    "v_mfma_f32_16x16x32_bf16 v[224:227], v[152:155], v[128:131], v[224:227]\n"
    "s_mul_i32 s40, s22, 0x2020\n"
    "v_mfma_f32_16x16x32_bf16 v[224:227], v[184:187], v[160:163], v[224:227]\n"
    "v_mfma_f32_16x16x32_bf16 v[228:231], v[184:187], v[164:167], v[228:231]\n"
    "s_and_b32 s40, s40, 0x4060\n"
    "v_mfma_f32_16x16x32_bf16 v[228:231], v[152:155], v[132:135], v[228:231]\n"
    "v_mfma_f32_16x16x32_bf16 v[232:235], v[152:155], v[136:139], v[232:235]\n"
    "s_add_u32 s40, s40, s43\n"
    "v_mfma_f32_16x16x32_bf16 v[232:235], v[184:187], v[168:171], v[232:235]\n"
    "v_mfma_f32_16x16x32_bf16 v[236:239], v[184:187], v[172:175], v[236:239]\n"
    "v_lshl_add_u32 v21, v21, 3, s40\n"
    "v_mfma_f32_16x16x32_bf16 v[236:239], v[152:155], v[140:143], v[236:239]\n"
    "v_mfma_f32_16x16x32_bf16 v[252:255], v[156:159], v[140:143], v[252:255]\n"
    "s_movk_i32 s41, 144\n"
    "v_mfma_f32_16x16x32_bf16 v[252:255], v[188:191], v[172:175], v[252:255]\n"
    "v_mfma_f32_16x16x32_bf16 v[248:251], v[188:191], v[168:171], v[248:251]\n"
    "v_add_u32 v22, v12, v13\n"
    "v_mfma_f32_16x16x32_bf16 v[248:251], v[156:159], v[136:139], v[248:251]\n"
    "v_mfma_f32_16x16x32_bf16 v[244:247], v[156:159], v[132:135], v[244:247]\n"
    "v_and_b32 v22, 3, v22\n"
    "v_mfma_f32_16x16x32_bf16 v[244:247], v[188:191], v[164:167], v[244:247]\n"
    "v_mfma_f32_16x16x32_bf16 v[240:243], v[188:191], v[160:163], v[240:243]\n"
    "v_mfma_f32_16x16x32_bf16 v[240:243], v[156:159], v[128:131], v[240:243]\n"
    "s_waitcnt vmcnt(7)\n"
    "s_barrier\n"
    "s_add_u32 m0, s35, 49152\n"
    "s_nop 0\n"
    "global_load_lds_dwordx4 v46, s[30:31] offset:0\n"
    "ds_read_b128 v[128:131], v58 offset:0\n"
    "ds_read_b128 v[132:135], v58 offset:2048\n"
    "ds_read_b128 v[136:139], v58 offset:4096\n"
    "global_load_lds_dwordx4 v47, s[30:31] offset:1024\n"
    "s_add_u32 s30, s30, 212992\n"
    "s_addc_u32 s31, s31, 0\n"
    "ds_read_b128 v[140:143], v58 offset:6144\n"
    "ds_read_b64_tr_b16 v[144:145], v80 offset:0\n"
    "ds_read_b64_tr_b16 v[146:147], v80 offset:1024\n"
    "s_add_u32 m0, s34, 49152\n"
    "s_nop 0\n"
    "global_load_lds_dwordx4 v52, s[36:37] offset:0\n"
    "ds_read_b64_tr_b16 v[148:149], v81 offset:0\n"
    "ds_read_b64_tr_b16 v[150:151], v81 offset:1024\n"
    "ds_read_b64_tr_b16 v[152:153], v82 offset:0\n"
    "global_load_lds_dwordx4 v53, s[36:37] offset:1024\n"
    "ds_read_b64_tr_b16 v[154:155], v82 offset:1024\n"
    "ds_read_b64_tr_b16 v[156:157], v83 offset:0\n"
    "ds_read_b64_tr_b16 v[158:159], v83 offset:1024\n"
    "global_load_lds_dwordx4 v54, s[36:37] offset:2048\n"
    "ds_read_b128 v[160:163], v59 offset:0\n"
    "ds_read_b128 v[164:167], v59 offset:2048\n"
    "ds_read_b128 v[168:171], v59 offset:4096\n"
    "global_load_lds_dwordx4 v55, s[36:37] offset:3072\n"
    "ds_read_b128 v[172:175], v59 offset:6144\n"
    "ds_read_b64_tr_b16 v[176:177], v80 offset:8192\n"
    "ds_read_b64_tr_b16 v[178:179], v80 offset:9216\n"
    "global_load_dwordx2 v[86:87], v42, s[10:11] offset:32\n"
    "ds_read_b64_tr_b16 v[180:181], v81 offset:8192\n"
    "ds_read_b64_tr_b16 v[182:183], v81 offset:9216\n"
    "ds_read_b64_tr_b16 v[184:185], v82 offset:8192\n"
    "ds_read_b64_tr_b16 v[186:187], v82 offset:9216\n"
    "ds_read_b64_tr_b16 v[188:189], v83 offset:8192\n"
    "ds_read_b64_tr_b16 v[190:191], v83 offset:9216\n"
    "s_waitcnt vmcnt(12) lgkmcnt(0)\n"
    "s_barrier\n"
    "v_mfma_f32_16x16x32_bf16 v[192:195], v[144:147], v[128:131], v[192:195]\n"
    "v_lshlrev_b32 v105, 7, v105\n"
    "v_mfma_f32_16x16x32_bf16 v[192:195], v[176:179], v[160:163], v[192:195]\n"
    "v_add_u32_dpp v52, v104, v48 quad_perm:[0,0,0,0] row_mask:0xf bank_mask:0xf\n"
    "v_mfma_f32_16x16x32_bf16 v[196:199], v[176:179], v[164:167], v[196:199]\n"
    "v_mfma_f32_16x16x32_bf16 v[196:199], v[144:147], v[132:135], v[196:199]\n"
    "v_add_u32_dpp v53, v104, v49 quad_perm:[1,1,1,1] row_mask:0xf bank_mask:0xf\n"
    "v_mfma_f32_16x16x32_bf16 v[200:203], v[144:147], v[136:139], v[200:203]\n"
    "v_mfma_f32_16x16x32_bf16 v[200:203], v[176:179], v[168:171], v[200:203]\n"
    "v_add_u32_dpp v54, v104, v50 quad_perm:[2,2,2,2] row_mask:0xf bank_mask:0xf\n"
    "v_mfma_f32_16x16x32_bf16 v[204:207], v[176:179], v[172:175], v[204:207]\n"
    "v_mfma_f32_16x16x32_bf16 v[204:207], v[144:147], v[140:143], v[204:207]\n"
    "v_add_u32_dpp v55, v104, v51 quad_perm:[3,3,3,3] row_mask:0xf bank_mask:0xf\n"
    "v_mfma_f32_16x16x32_bf16 v[220:223], v[148:151], v[140:143], v[220:223]\n"
    "v_mfma_f32_16x16x32_bf16 v[220:223], v[180:183], v[172:175], v[220:223]\n"
    "v_mad_u32_u24 v4, v22, s41, v21\n"
    "v_mfma_f32_16x16x32_bf16 v[216:219], v[180:183], v[168:171], v[216:219]\n"
    "v_mfma_f32_16x16x32_bf16 v[216:219], v[148:151], v[136:139], v[216:219]\n"
    "v_cmp_gt_u32 vcc, 2, v13\n"
    "v_mfma_f32_16x16x32_bf16 v[212:215], v[148:151], v[132:135], v[212:215]\n"
    "v_mfma_f32_16x16x32_bf16 v[212:215], v[180:183], v[164:167], v[212:215]\n"
    "v_add_u32 v24, 22, v13\n"
    "v_mfma_f32_16x16x32_bf16 v[208:211], v[180:183], v[160:163], v[208:211]\n"
    "v_mfma_f32_16x16x32_bf16 v[208:211], v[148:151], v[128:131], v[208:211]\n"
    "v_lshlrev_b32 v13, 3, v13\n"
    "v_mfma_f32_16x16x32_bf16 v[224:227], v[152:155], v[128:131], v[224:227]\n"
    "v_mfma_f32_16x16x32_bf16 v[224:227], v[184:187], v[160:163], v[224:227]\n"
    "v_cndmask_b32 v22, v24, v22, vcc\n"
    "v_mfma_f32_16x16x32_bf16 v[228:231], v[184:187], v[164:167], v[228:231]\n"
    "v_mfma_f32_16x16x32_bf16 v[228:231], v[152:155], v[132:135], v[228:231]\n"
    "v_mad_u32_u24 v3, v22, s41, v21\n"
    "v_mfma_f32_16x16x32_bf16 v[232:235], v[152:155], v[136:139], v[232:235]\n"
    "v_mfma_f32_16x16x32_bf16 v[232:235], v[184:187], v[168:171], v[232:235]\n"
    "v_cmp_eq_u32 vcc, 3, v12\n"
    "v_mfma_f32_16x16x32_bf16 v[236:239], v[184:187], v[172:175], v[236:239]\n"
    "v_mfma_f32_16x16x32_bf16 v[236:239], v[152:155], v[140:143], v[236:239]\n"
    "v_xor_b32 v22, 0, v12\n"
    "v_mfma_f32_16x16x32_bf16 v[252:255], v[156:159], v[140:143], v[252:255]\n"
    "v_mfma_f32_16x16x32_bf16 v[252:255], v[188:191], v[172:175], v[252:255]\n"
    "v_and_b32 v22, 1, v22\n"
    "v_mfma_f32_16x16x32_bf16 v[248:251], v[188:191], v[168:171], v[248:251]\n"
    "v_mfma_f32_16x16x32_bf16 v[248:251], v[156:159], v[136:139], v[248:251]\n"
    "v_lshlrev_b32 v22, 1, v22\n"
    "v_mfma_f32_16x16x32_bf16 v[244:247], v[156:159], v[132:135], v[244:247]\n"
    "v_mfma_f32_16x16x32_bf16 v[244:247], v[188:191], v[164:167], v[244:247]\n"
    "v_lshl_add_u32 v22, v12, 3, v22\n"
    "v_mfma_f32_16x16x32_bf16 v[240:243], v[188:191], v[160:163], v[240:243]\n"
    "v_mfma_f32_16x16x32_bf16 v[240:243], v[156:159], v[128:131], v[240:243]\n"
    "s_waitcnt vmcnt(8)\n"
    "s_barrier\n"
    "s_add_u32 m0, s35, 98304\n"
    "s_nop 0\n"
    "global_load_lds_dwordx4 v46, s[30:31] offset:0\n"
    "ds_read_b128 v[128:131], v56 offset:0\n"
    "ds_read_b128 v[132:135], v56 offset:2048\n"
    "ds_read_b128 v[136:139], v56 offset:4096\n"
    "global_load_lds_dwordx4 v47, s[30:31] offset:1024\n"
    "s_add_u32 s66, s28, 3\n"
    "s_and_b32 s66, s66, 7\n"
    "s_mul_i32 s66, s66, 638976\n"
    "s_add_u32 s30, s6, s66\n"
    "s_addc_u32 s31, s7, 0\n"
    "ds_read_b128 v[140:143], v56 offset:6144\n"
    "ds_read_b64_tr_b16 v[144:145], v76 offset:0\n"
    "ds_read_b64_tr_b16 v[146:147], v76 offset:1024\n"
    "s_add_u32 m0, s34, 98304\n"
    "s_nop 0\n"
    "global_load_lds_dwordx4 v52, s[36:37] offset:0\n"
    "ds_read_b64_tr_b16 v[148:149], v77 offset:0\n"
    "ds_read_b64_tr_b16 v[150:151], v77 offset:1024\n"
    "ds_read_b64_tr_b16 v[152:153], v78 offset:0\n"
    "global_load_lds_dwordx4 v53, s[36:37] offset:1024\n"
    "ds_read_b64_tr_b16 v[154:155], v78 offset:1024\n"
    "ds_read_b64_tr_b16 v[156:157], v79 offset:0\n"
    "ds_read_b64_tr_b16 v[158:159], v79 offset:1024\n"
    "global_load_lds_dwordx4 v54, s[36:37] offset:2048\n"
    "ds_read_b128 v[160:163], v57 offset:0\n"
    "ds_read_b128 v[164:167], v57 offset:2048\n"
    "ds_read_b128 v[168:171], v57 offset:4096\n"
    "global_load_lds_dwordx4 v55, s[36:37] offset:3072\n"
    "ds_read_b128 v[172:175], v57 offset:6144\n"
    "ds_read_b64_tr_b16 v[176:177], v76 offset:8192\n"
    "ds_read_b64_tr_b16 v[178:179], v76 offset:9216\n"
    "global_load_dwordx2 v[88:89], v42, s[10:11] offset:64\n"
    "ds_read_b64_tr_b16 v[180:181], v77 offset:8192\n"
    "ds_read_b64_tr_b16 v[182:183], v77 offset:9216\n"
    "ds_read_b64_tr_b16 v[184:185], v78 offset:8192\n"
    "ds_read_b64_tr_b16 v[186:187], v78 offset:9216\n"
    "ds_read_b64_tr_b16 v[188:189], v79 offset:8192\n"
    "ds_read_b64_tr_b16 v[190:191], v79 offset:9216\n"
    "s_waitcnt vmcnt(12) lgkmcnt(0)\n"
    "s_barrier\n"
    "v_mfma_f32_16x16x32_bf16 v[192:195], v[144:147], v[128:131], v[192:195]\n"
    "v_lshlrev_b32 v106, 7, v106\n"
    "v_mfma_f32_16x16x32_bf16 v[192:195], v[176:179], v[160:163], v[192:195]\n"
    "v_mfma_f32_16x16x32_bf16 v[196:199], v[176:179], v[164:167], v[196:199]\n"
    "v_add_u32_dpp v52, v105, v48 quad_perm:[0,0,0,0] row_mask:0xf bank_mask:0xf\n"
    "v_mfma_f32_16x16x32_bf16 v[196:199], v[144:147], v[132:135], v[196:199]\n"
    "v_mfma_f32_16x16x32_bf16 v[200:203], v[144:147], v[136:139], v[200:203]\n"
    "v_add_u32_dpp v53, v105, v49 quad_perm:[1,1,1,1] row_mask:0xf bank_mask:0xf\n"
    "v_mfma_f32_16x16x32_bf16 v[200:203], v[176:179], v[168:171], v[200:203]\n"
    "v_mfma_f32_16x16x32_bf16 v[204:207], v[176:179], v[172:175], v[204:207]\n"
    "v_add_u32_dpp v54, v105, v50 quad_perm:[2,2,2,2] row_mask:0xf bank_mask:0xf\n"
    "v_mfma_f32_16x16x32_bf16 v[204:207], v[144:147], v[140:143], v[204:207]\n"
    "v_mfma_f32_16x16x32_bf16 v[220:223], v[148:151], v[140:143], v[220:223]\n"
    "v_add_u32_dpp v55, v105, v51 quad_perm:[3,3,3,3] row_mask:0xf bank_mask:0xf\n"
    "v_mfma_f32_16x16x32_bf16 v[220:223], v[180:183], v[172:175], v[220:223]\n"
    "v_mfma_f32_16x16x32_bf16 v[216:219], v[180:183], v[168:171], v[216:219]\n"
    "s_mov_b32 s40, 0x05040100\n"
    "v_mfma_f32_16x16x32_bf16 v[216:219], v[148:151], v[136:139], v[216:219]\n"
    "v_mfma_f32_16x16x32_bf16 v[212:215], v[148:151], v[132:135], v[212:215]\n"
    "v_bfe_u32 v24, s40, v13, 8\n"
    "v_mfma_f32_16x16x32_bf16 v[212:215], v[180:183], v[164:167], v[212:215]\n"
    "v_mfma_f32_16x16x32_bf16 v[208:211], v[180:183], v[160:163], v[208:211]\n"
    "v_add_u32 v22, v22, v24\n"
    "v_mfma_f32_16x16x32_bf16 v[208:211], v[148:151], v[128:131], v[208:211]\n"
    "v_mfma_f32_16x16x32_bf16 v[224:227], v[152:155], v[128:131], v[224:227]\n"
    "s_mov_b32 s40, 0x1a191818\n"
    "v_mfma_f32_16x16x32_bf16 v[224:227], v[184:187], v[160:163], v[224:227]\n"
    "v_mfma_f32_16x16x32_bf16 v[228:231], v[184:187], v[164:167], v[228:231]\n"
    "v_bfe_u32 v24, s40, v13, 8\n"
    "v_mfma_f32_16x16x32_bf16 v[228:231], v[152:155], v[132:135], v[228:231]\n"
    "v_mfma_f32_16x16x32_bf16 v[232:235], v[152:155], v[136:139], v[232:235]\n"
    "v_cndmask_b32 v22, v22, v24, vcc\n"
    "v_mfma_f32_16x16x32_bf16 v[232:235], v[184:187], v[168:171], v[232:235]\n"
    "v_mfma_f32_16x16x32_bf16 v[236:239], v[184:187], v[172:175], v[236:239]\n"
    "v_mad_u32_u24 v5, v22, s41, v21\n"
    "v_mfma_f32_16x16x32_bf16 v[236:239], v[152:155], v[140:143], v[236:239]\n"
    "v_mfma_f32_16x16x32_bf16 v[252:255], v[156:159], v[140:143], v[252:255]\n"
    "v_xor_b32 v22, 1, v12\n"
    "v_mfma_f32_16x16x32_bf16 v[252:255], v[188:191], v[172:175], v[252:255]\n"
    "v_mfma_f32_16x16x32_bf16 v[248:251], v[188:191], v[168:171], v[248:251]\n"
    "v_and_b32 v22, 1, v22\n"
    "v_mfma_f32_16x16x32_bf16 v[248:251], v[156:159], v[136:139], v[248:251]\n"
    "v_mfma_f32_16x16x32_bf16 v[244:247], v[156:159], v[132:135], v[244:247]\n"
    "v_lshlrev_b32 v22, 1, v22\n"
    "v_mfma_f32_16x16x32_bf16 v[244:247], v[188:191], v[164:167], v[244:247]\n"
    "v_mfma_f32_16x16x32_bf16 v[240:243], v[188:191], v[160:163], v[240:243]\n"
    "v_mfma_f32_16x16x32_bf16 v[240:243], v[156:159], v[128:131], v[240:243]\n"
    "s_waitcnt vmcnt(8)\n"
    "s_barrier\n"
    "s_add_u32 m0, s35, 0\n"
    "s_nop 0\n"
    "global_load_lds_dwordx4 v46, s[30:31] offset:0\n"
    "ds_read_b128 v[128:131], v56 offset:49152\n"
    "ds_read_b128 v[132:135], v56 offset:51200\n"
    "ds_read_b128 v[136:139], v56 offset:53248\n"
    "global_load_lds_dwordx4 v47, s[30:31] offset:1024\n"
    "s_add_u32 s30, s30, 212992\n"
    "s_addc_u32 s31, s31, 0\n"
    "ds_read_b128 v[140:143], v56 offset:55296\n"
    "ds_read_b64_tr_b16 v[144:145], v76 offset:49152\n"
    "ds_read_b64_tr_b16 v[146:147], v76 offset:50176\n"
    "s_add_u32 m0, s34, 0\n"
    "s_nop 0\n"
    "global_load_lds_dwordx4 v52, s[36:37] offset:0\n"
    "ds_read_b64_tr_b16 v[148:149], v77 offset:49152\n"
    "ds_read_b64_tr_b16 v[150:151], v77 offset:50176\n"
    "ds_read_b64_tr_b16 v[152:153], v78 offset:49152\n"
    "global_load_lds_dwordx4 v53, s[36:37] offset:1024\n"
    "ds_read_b64_tr_b16 v[154:155], v78 offset:50176\n"
    "ds_read_b64_tr_b16 v[156:157], v79 offset:49152\n"
    "ds_read_b64_tr_b16 v[158:159], v79 offset:50176\n"
    "global_load_lds_dwordx4 v54, s[36:37] offset:2048\n"
    "ds_read_b128 v[160:163], v57 offset:49152\n"
    "ds_read_b128 v[164:167], v57 offset:51200\n"
    "ds_read_b128 v[168:171], v57 offset:53248\n"
    "global_load_lds_dwordx4 v55, s[36:37] offset:3072\n"
    "ds_read_b128 v[172:175], v57 offset:55296\n"
    "ds_read_b64_tr_b16 v[176:177], v76 offset:57344\n"
    "ds_read_b64_tr_b16 v[178:179], v76 offset:58368\n"
    "global_load_dwordx2 v[90:91], v42, s[10:11] offset:96\n"
    "ds_read_b64_tr_b16 v[180:181], v77 offset:57344\n"
    "ds_read_b64_tr_b16 v[182:183], v77 offset:58368\n"
    "ds_read_b64_tr_b16 v[184:185], v78 offset:57344\n"
    "ds_read_b64_tr_b16 v[186:187], v78 offset:58368\n"
    "ds_read_b64_tr_b16 v[188:189], v79 offset:57344\n"
    "ds_read_b64_tr_b16 v[190:191], v79 offset:58368\n"
    "s_waitcnt vmcnt(12) lgkmcnt(0)\n"
    "s_barrier\n"
    "v_mfma_f32_16x16x32_bf16 v[192:195], v[144:147], v[128:131], v[192:195]\n"
    "v_lshlrev_b32 v107, 7, v107\n"
    "v_mfma_f32_16x16x32_bf16 v[192:195], v[176:179], v[160:163], v[192:195]\n"
    "v_mfma_f32_16x16x32_bf16 v[196:199], v[176:179], v[164:167], v[196:199]\n"
    "v_add_u32_dpp v52, v106, v48 quad_perm:[0,0,0,0] row_mask:0xf bank_mask:0xf\n"
    "v_mfma_f32_16x16x32_bf16 v[196:199], v[144:147], v[132:135], v[196:199]\n"
    "v_mfma_f32_16x16x32_bf16 v[200:203], v[144:147], v[136:139], v[200:203]\n"
    "v_add_u32_dpp v53, v106, v49 quad_perm:[1,1,1,1] row_mask:0xf bank_mask:0xf\n"
    "v_mfma_f32_16x16x32_bf16 v[200:203], v[176:179], v[168:171], v[200:203]\n"
    "v_mfma_f32_16x16x32_bf16 v[204:207], v[176:179], v[172:175], v[204:207]\n"
    "v_add_u32_dpp v54, v106, v50 quad_perm:[2,2,2,2] row_mask:0xf bank_mask:0xf\n"
    "v_mfma_f32_16x16x32_bf16 v[204:207], v[144:147], v[140:143], v[204:207]\n"
    "v_mfma_f32_16x16x32_bf16 v[220:223], v[148:151], v[140:143], v[220:223]\n"
    "v_add_u32_dpp v55, v106, v51 quad_perm:[3,3,3,3] row_mask:0xf bank_mask:0xf\n"
    "v_mfma_f32_16x16x32_bf16 v[220:223], v[180:183], v[172:175], v[220:223]\n"
    "v_mfma_f32_16x16x32_bf16 v[216:219], v[180:183], v[168:171], v[216:219]\n"
    "v_lshl_add_u32 v22, v12, 3, v22\n"
    "v_mfma_f32_16x16x32_bf16 v[216:219], v[148:151], v[136:139], v[216:219]\n"
    "v_mfma_f32_16x16x32_bf16 v[212:215], v[148:151], v[132:135], v[212:215]\n"
    "s_mov_b32 s40, 0x05040100\n"
    "v_mfma_f32_16x16x32_bf16 v[212:215], v[180:183], v[164:167], v[212:215]\n"
    "v_mfma_f32_16x16x32_bf16 v[208:211], v[180:183], v[160:163], v[208:211]\n"
    "v_bfe_u32 v24, s40, v13, 8\n"
    "v_mfma_f32_16x16x32_bf16 v[208:211], v[148:151], v[128:131], v[208:211]\n"
    "v_mfma_f32_16x16x32_bf16 v[224:227], v[152:155], v[128:131], v[224:227]\n"
    "v_add_u32 v22, v22, v24\n"
    "v_mfma_f32_16x16x32_bf16 v[224:227], v[184:187], v[160:163], v[224:227]\n"
    "v_mfma_f32_16x16x32_bf16 v[228:231], v[184:187], v[164:167], v[228:231]\n"
    "s_mov_b32 s40, 0x1a191918\n"
    "v_mfma_f32_16x16x32_bf16 v[228:231], v[152:155], v[132:135], v[228:231]\n"
    "v_mfma_f32_16x16x32_bf16 v[232:235], v[152:155], v[136:139], v[232:235]\n"
    "v_bfe_u32 v24, s40, v13, 8\n"
    "v_mfma_f32_16x16x32_bf16 v[232:235], v[184:187], v[168:171], v[232:235]\n"
    "v_mfma_f32_16x16x32_bf16 v[236:239], v[184:187], v[172:175], v[236:239]\n"
    "v_cndmask_b32 v22, v22, v24, vcc\n"
    "v_mfma_f32_16x16x32_bf16 v[236:239], v[152:155], v[140:143], v[236:239]\n"
    "v_mfma_f32_16x16x32_bf16 v[252:255], v[156:159], v[140:143], v[252:255]\n"
    "v_mad_u32_u24 v6, v22, s41, v21\n"
    "v_mfma_f32_16x16x32_bf16 v[252:255], v[188:191], v[172:175], v[252:255]\n"
    "v_mfma_f32_16x16x32_bf16 v[248:251], v[188:191], v[168:171], v[248:251]\n"
    "v_bfrev_b32 v14, v12\n"
    "v_mfma_f32_16x16x32_bf16 v[248:251], v[156:159], v[136:139], v[248:251]\n"
    "v_mfma_f32_16x16x32_bf16 v[244:247], v[156:159], v[132:135], v[244:247]\n"
    "v_lshrrev_b32 v14, 26, v14\n"
    "v_mfma_f32_16x16x32_bf16 v[244:247], v[188:191], v[164:167], v[244:247]\n"
    "v_mfma_f32_16x16x32_bf16 v[240:243], v[188:191], v[160:163], v[240:243]\n"
    "v_mfma_f32_16x16x32_bf16 v[240:243], v[156:159], v[128:131], v[240:243]\n"
    "s_waitcnt vmcnt(8)\n"
    "s_barrier\n"
    "s_add_u32 m0, s35, 49152\n"
    "s_nop 0\n"
    "global_load_lds_dwordx4 v46, s[30:31] offset:0\n"
    "ds_read_b128 v[128:131], v58 offset:0\n"
    "ds_read_b128 v[132:135], v58 offset:2048\n"
    "ds_read_b128 v[136:139], v58 offset:4096\n"
    "global_load_lds_dwordx4 v47, s[30:31] offset:1024\n"
    "s_add_u32 s30, s30, 212992\n"
    "s_addc_u32 s31, s31, 0\n"
    "ds_read_b128 v[140:143], v58 offset:6144\n"
    "ds_read_b64_tr_b16 v[144:145], v80 offset:0\n"
    "ds_read_b64_tr_b16 v[146:147], v80 offset:1024\n"
    "s_add_u32 m0, s34, 49152\n"
    "s_nop 0\n"
    "global_load_lds_dwordx4 v52, s[36:37] offset:0\n"
    "ds_read_b64_tr_b16 v[148:149], v81 offset:0\n"
    "ds_read_b64_tr_b16 v[150:151], v81 offset:1024\n"
    "ds_read_b64_tr_b16 v[152:153], v82 offset:0\n"
    "global_load_lds_dwordx4 v53, s[36:37] offset:1024\n"
    "ds_read_b64_tr_b16 v[154:155], v82 offset:1024\n"
    "ds_read_b64_tr_b16 v[156:157], v83 offset:0\n"
    "ds_read_b64_tr_b16 v[158:159], v83 offset:1024\n"
    "global_load_lds_dwordx4 v54, s[36:37] offset:2048\n"
    "ds_read_b128 v[160:163], v59 offset:0\n"
    "ds_read_b128 v[164:167], v59 offset:2048\n"
    "ds_read_b128 v[168:171], v59 offset:4096\n"
    "global_load_lds_dwordx4 v55, s[36:37] offset:3072\n"
    "ds_read_b128 v[172:175], v59 offset:6144\n"
    "ds_read_b64_tr_b16 v[176:177], v80 offset:8192\n"
    "ds_read_b64_tr_b16 v[178:179], v80 offset:9216\n"
    "global_load_dwordx2 v[92:93], v42, s[18:19] offset:0\n"
    "ds_read_b64_tr_b16 v[180:181], v81 offset:8192\n"
    "ds_read_b64_tr_b16 v[182:183], v81 offset:9216\n"
    "ds_read_b64_tr_b16 v[184:185], v82 offset:8192\n"
    "ds_read_b64_tr_b16 v[186:187], v82 offset:9216\n"
    "ds_read_b64_tr_b16 v[188:189], v83 offset:8192\n"
    "ds_read_b64_tr_b16 v[190:191], v83 offset:9216\n"
    "s_waitcnt vmcnt(12) lgkmcnt(0)\n"
    "s_barrier\n"
    "v_mfma_f32_16x16x32_bf16 v[192:195], v[144:147], v[128:131], v[192:195]\n"
    "v_lshlrev_b32 v108, 7, v108\n"
    "v_mfma_f32_16x16x32_bf16 v[192:195], v[176:179], v[160:163], v[192:195]\n"
    "v_add_u32_dpp v52, v107, v48 quad_perm:[0,0,0,0] row_mask:0xf bank_mask:0xf\n"
    "v_mfma_f32_16x16x32_bf16 v[196:199], v[176:179], v[164:167], v[196:199]\n"
    "v_mfma_f32_16x16x32_bf16 v[196:199], v[144:147], v[132:135], v[196:199]\n"
    "v_add_u32_dpp v53, v107, v49 quad_perm:[1,1,1,1] row_mask:0xf bank_mask:0xf\n"
    "v_mfma_f32_16x16x32_bf16 v[200:203], v[144:147], v[136:139], v[200:203]\n"
    "v_mfma_f32_16x16x32_bf16 v[200:203], v[176:179], v[168:171], v[200:203]\n"
    "v_add_u32_dpp v54, v107, v50 quad_perm:[2,2,2,2] row_mask:0xf bank_mask:0xf\n"
    "v_mfma_f32_16x16x32_bf16 v[204:207], v[176:179], v[172:175], v[204:207]\n"
    "v_mfma_f32_16x16x32_bf16 v[204:207], v[144:147], v[140:143], v[204:207]\n"
    "v_add_u32_dpp v55, v107, v51 quad_perm:[3,3,3,3] row_mask:0xf bank_mask:0xf\n"
    "v_mfma_f32_16x16x32_bf16 v[220:223], v[148:151], v[140:143], v[220:223]\n"
    "v_mfma_f32_16x16x32_bf16 v[220:223], v[180:183], v[172:175], v[220:223]\n"
    "s_add_u32 s40, s22, 3\n"
    "v_mfma_f32_16x16x32_bf16 v[216:219], v[180:183], v[168:171], v[216:219]\n"
    "v_mfma_f32_16x16x32_bf16 v[216:219], v[148:151], v[136:139], v[216:219]\n"
    "s_and_b32 s40, s40, 3\n"
    "v_mfma_f32_16x16x32_bf16 v[212:215], v[148:151], v[132:135], v[212:215]\n"
    "v_mfma_f32_16x16x32_bf16 v[212:215], v[180:183], v[164:167], v[212:215]\n"
    "s_lshl2_add_u32 s40, s40, s22\n"
    "v_mfma_f32_16x16x32_bf16 v[208:211], v[180:183], v[160:163], v[208:211]\n"
    "v_mfma_f32_16x16x32_bf16 v[208:211], v[148:151], v[128:131], v[208:211]\n"
    "s_add_u32 s40, s40, s58\n"
    "v_mfma_f32_16x16x32_bf16 v[224:227], v[152:155], v[128:131], v[224:227]\n"
    "v_mfma_f32_16x16x32_bf16 v[224:227], v[184:187], v[160:163], v[224:227]\n"
    "s_lshl_b32 s40, s40, 2\n"
    "v_mfma_f32_16x16x32_bf16 v[228:231], v[184:187], v[164:167], v[228:231]\n"
    "v_mfma_f32_16x16x32_bf16 v[228:231], v[152:155], v[132:135], v[228:231]\n"
    "s_add_u32 s40, s40, -450048\n"
    "v_mfma_f32_16x16x32_bf16 v[232:235], v[152:155], v[136:139], v[232:235]\n"
    "v_mfma_f32_16x16x32_bf16 v[232:235], v[184:187], v[168:171], v[232:235]\n"
    "v_add_u32 v14, s40, v14\n"
    "v_mfma_f32_16x16x32_bf16 v[236:239], v[184:187], v[172:175], v[236:239]\n"
    "v_mfma_f32_16x16x32_bf16 v[236:239], v[152:155], v[140:143], v[236:239]\n"
    "v_and_b32 v2, 1, v1\n"
    "v_mfma_f32_16x16x32_bf16 v[252:255], v[156:159], v[140:143], v[252:255]\n"
    "v_mfma_f32_16x16x32_bf16 v[252:255], v[188:191], v[172:175], v[252:255]\n"
    "v_mul_u32_u24 v2, 0xaaaa, v2\n"
    "v_mfma_f32_16x16x32_bf16 v[248:251], v[188:191], v[168:171], v[248:251]\n"
    "v_mfma_f32_16x16x32_bf16 v[248:251], v[156:159], v[136:139], v[248:251]\n"
    "v_add_u32 v2, 0x4444, v2\n"
    "v_mfma_f32_16x16x32_bf16 v[244:247], v[156:159], v[132:135], v[244:247]\n"
    "v_mfma_f32_16x16x32_bf16 v[244:247], v[188:191], v[164:167], v[244:247]\n"
    "v_mad_u32_u24 v15, v17, 36, v19\n"
    "v_mfma_f32_16x16x32_bf16 v[240:243], v[188:191], v[160:163], v[240:243]\n"
    "v_mfma_f32_16x16x32_bf16 v[240:243], v[156:159], v[128:131], v[240:243]\n"
    "s_waitcnt vmcnt(8)\n"
    "s_barrier\n"
    "s_add_u32 m0, s35, 98304\n"
    "s_nop 0\n"
    "global_load_lds_dwordx4 v46, s[30:31] offset:0\n"
    "ds_read_b128 v[128:131], v56 offset:0\n"
    "ds_read_b128 v[132:135], v56 offset:2048\n"
    "ds_read_b128 v[136:139], v56 offset:4096\n"
    "global_load_lds_dwordx4 v47, s[30:31] offset:1024\n"
    "s_add_u32 s66, s28, 4\n"
    "s_and_b32 s66, s66, 7\n"
    "s_mul_i32 s66, s66, 638976\n"
    "s_add_u32 s30, s6, s66\n"
    "s_addc_u32 s31, s7, 0\n"
    "ds_read_b128 v[140:143], v56 offset:6144\n"
    "ds_read_b64_tr_b16 v[144:145], v76 offset:0\n"
    "ds_read_b64_tr_b16 v[146:147], v76 offset:1024\n"
    "s_add_u32 m0, s34, 98304\n"
    "s_nop 0\n"
    "global_load_lds_dwordx4 v52, s[36:37] offset:0\n"
    "ds_read_b64_tr_b16 v[148:149], v77 offset:0\n"
    "ds_read_b64_tr_b16 v[150:151], v77 offset:1024\n"
    "ds_read_b64_tr_b16 v[152:153], v78 offset:0\n"
    "global_load_lds_dwordx4 v53, s[36:37] offset:1024\n"
    "ds_read_b64_tr_b16 v[154:155], v78 offset:1024\n"
    "ds_read_b64_tr_b16 v[156:157], v79 offset:0\n"
    "ds_read_b64_tr_b16 v[158:159], v79 offset:1024\n"
    "global_load_lds_dwordx4 v54, s[36:37] offset:2048\n"
    "ds_read_b128 v[160:163], v57 offset:0\n"
    "ds_read_b128 v[164:167], v57 offset:2048\n"
    "ds_read_b128 v[168:171], v57 offset:4096\n"
    "global_load_lds_dwordx4 v55, s[36:37] offset:3072\n"
    "ds_read_b128 v[172:175], v57 offset:6144\n"
    "ds_read_b64_tr_b16 v[176:177], v76 offset:8192\n"
    "ds_read_b64_tr_b16 v[178:179], v76 offset:9216\n"
    "global_load_dwordx2 v[94:95], v42, s[18:19] offset:32\n"
    "ds_read_b64_tr_b16 v[180:181], v77 offset:8192\n"
    "ds_read_b64_tr_b16 v[182:183], v77 offset:9216\n"
    "ds_read_b64_tr_b16 v[184:185], v78 offset:8192\n"
    "ds_read_b64_tr_b16 v[186:187], v78 offset:9216\n"
    "ds_read_b64_tr_b16 v[188:189], v79 offset:8192\n"
    "ds_read_b64_tr_b16 v[190:191], v79 offset:9216\n"
    "s_waitcnt vmcnt(12) lgkmcnt(0)\n"
    "s_barrier\n"
    "v_mfma_f32_16x16x32_bf16 v[192:195], v[144:147], v[128:131], v[192:195]\n"
    "v_lshlrev_b32 v109, 7, v109\n"
    "v_mfma_f32_16x16x32_bf16 v[192:195], v[176:179], v[160:163], v[192:195]\n"
    "v_mfma_f32_16x16x32_bf16 v[196:199], v[176:179], v[164:167], v[196:199]\n"
    "v_add_u32_dpp v52, v108, v48 quad_perm:[0,0,0,0] row_mask:0xf bank_mask:0xf\n"
    "v_mfma_f32_16x16x32_bf16 v[196:199], v[144:147], v[132:135], v[196:199]\n"
    "v_mfma_f32_16x16x32_bf16 v[200:203], v[144:147], v[136:139], v[200:203]\n"
    "v_add_u32_dpp v53, v108, v49 quad_perm:[1,1,1,1] row_mask:0xf bank_mask:0xf\n"
    "v_mfma_f32_16x16x32_bf16 v[200:203], v[176:179], v[168:171], v[200:203]\n"
    "v_mfma_f32_16x16x32_bf16 v[204:207], v[176:179], v[172:175], v[204:207]\n"
    "v_add_u32_dpp v54, v108, v50 quad_perm:[2,2,2,2] row_mask:0xf bank_mask:0xf\n"
    "v_mfma_f32_16x16x32_bf16 v[204:207], v[144:147], v[140:143], v[204:207]\n"
    "v_mfma_f32_16x16x32_bf16 v[220:223], v[148:151], v[140:143], v[220:223]\n"
    "v_add_u32_dpp v55, v108, v51 quad_perm:[3,3,3,3] row_mask:0xf bank_mask:0xf\n"
    "v_mfma_f32_16x16x32_bf16 v[220:223], v[180:183], v[172:175], v[220:223]\n"
    "v_mfma_f32_16x16x32_bf16 v[216:219], v[180:183], v[168:171], v[216:219]\n"
    "v_mad_u32_u24 v16, v18, 36, v20\n"
    "v_mfma_f32_16x16x32_bf16 v[216:219], v[148:151], v[136:139], v[216:219]\n"
    "v_mfma_f32_16x16x32_bf16 v[212:215], v[148:151], v[132:135], v[212:215]\n"
    "s_add_u32 s41, s22, 2\n"
    "v_mfma_f32_16x16x32_bf16 v[212:215], v[180:183], v[164:167], v[212:215]\n"
    "v_mfma_f32_16x16x32_bf16 v[208:211], v[180:183], v[160:163], v[208:211]\n"
    "v_lshrrev_b32 v12, 5, v19\n"
    "v_mfma_f32_16x16x32_bf16 v[208:211], v[148:151], v[128:131], v[208:211]\n"
    "v_mfma_f32_16x16x32_bf16 v[224:227], v[152:155], v[128:131], v[224:227]\n"
    "v_sub_u32 v12, 1, v12\n"
    "v_mfma_f32_16x16x32_bf16 v[224:227], v[184:187], v[160:163], v[224:227]\n"
    "v_mfma_f32_16x16x32_bf16 v[228:231], v[184:187], v[164:167], v[228:231]\n"
    "v_and_b32 v12, 3, v12\n"
    "v_mfma_f32_16x16x32_bf16 v[228:231], v[152:155], v[132:135], v[228:231]\n"
    "v_mfma_f32_16x16x32_bf16 v[232:235], v[152:155], v[136:139], v[232:235]\n"
    "v_add_lshl_u32 v12, v12, s41, 12\n"
    "v_mfma_f32_16x16x32_bf16 v[232:235], v[184:187], v[168:171], v[232:235]\n"
    "v_mfma_f32_16x16x32_bf16 v[236:239], v[184:187], v[172:175], v[236:239]\n"
    "v_and_b32 v12, 0x7000, v12\n"
    "v_mfma_f32_16x16x32_bf16 v[236:239], v[152:155], v[140:143], v[236:239]\n"
    "v_mfma_f32_16x16x32_bf16 v[252:255], v[156:159], v[140:143], v[252:255]\n"
    "v_add3_u32 v15, v12, v15, s43\n"
    "v_mfma_f32_16x16x32_bf16 v[252:255], v[188:191], v[172:175], v[252:255]\n"
    "v_mfma_f32_16x16x32_bf16 v[248:251], v[188:191], v[168:171], v[248:251]\n"
    "v_lshrrev_b32 v12, 5, v20\n"
    "v_mfma_f32_16x16x32_bf16 v[248:251], v[156:159], v[136:139], v[248:251]\n"
    "v_mfma_f32_16x16x32_bf16 v[244:247], v[156:159], v[132:135], v[244:247]\n"
    "v_sub_u32 v12, 1, v12\n"
    "v_mfma_f32_16x16x32_bf16 v[244:247], v[188:191], v[164:167], v[244:247]\n"
    "v_mfma_f32_16x16x32_bf16 v[240:243], v[188:191], v[160:163], v[240:243]\n"
    "v_mfma_f32_16x16x32_bf16 v[240:243], v[156:159], v[128:131], v[240:243]\n"
    "s_waitcnt vmcnt(8)\n"
    "s_barrier\n"
    "s_add_u32 m0, s35, 0\n"
    "s_nop 0\n"
    "global_load_lds_dwordx4 v46, s[30:31] offset:0\n"
    "ds_read_b128 v[128:131], v56 offset:49152\n"
    "ds_read_b128 v[132:135], v56 offset:51200\n"
    "ds_read_b128 v[136:139], v56 offset:53248\n"
    "global_load_lds_dwordx4 v47, s[30:31] offset:1024\n"
    "s_add_u32 s30, s30, 212992\n"
    "s_addc_u32 s31, s31, 0\n"
    "ds_read_b128 v[140:143], v56 offset:55296\n"
    "ds_read_b64_tr_b16 v[144:145], v76 offset:49152\n"
    "ds_read_b64_tr_b16 v[146:147], v76 offset:50176\n"
    "s_add_u32 m0, s34, 0\n"
    "s_nop 0\n"
    "global_load_lds_dwordx4 v52, s[36:37] offset:0\n"
    "ds_read_b64_tr_b16 v[148:149], v77 offset:49152\n"
    "ds_read_b64_tr_b16 v[150:151], v77 offset:50176\n"
    "ds_read_b64_tr_b16 v[152:153], v78 offset:49152\n"
    "global_load_lds_dwordx4 v53, s[36:37] offset:1024\n"
    "ds_read_b64_tr_b16 v[154:155], v78 offset:50176\n"
    "ds_read_b64_tr_b16 v[156:157], v79 offset:49152\n"
    "ds_read_b64_tr_b16 v[158:159], v79 offset:50176\n"
    "global_load_lds_dwordx4 v54, s[36:37] offset:2048\n"
    "ds_read_b128 v[160:163], v57 offset:49152\n"
    "ds_read_b128 v[164:167], v57 offset:51200\n"
    "ds_read_b128 v[168:171], v57 offset:53248\n"
    "global_load_lds_dwordx4 v55, s[36:37] offset:3072\n"
    "ds_read_b128 v[172:175], v57 offset:55296\n"
    "ds_read_b64_tr_b16 v[176:177], v76 offset:57344\n"
    "ds_read_b64_tr_b16 v[178:179], v76 offset:58368\n"
    "global_load_dwordx2 v[122:123], v42, s[18:19] offset:64\n"
    "ds_read_b64_tr_b16 v[180:181], v77 offset:57344\n"
    "ds_read_b64_tr_b16 v[182:183], v77 offset:58368\n"
    "ds_read_b64_tr_b16 v[184:185], v78 offset:57344\n"
    "ds_read_b64_tr_b16 v[186:187], v78 offset:58368\n"
    "ds_read_b64_tr_b16 v[188:189], v79 offset:57344\n"
    "ds_read_b64_tr_b16 v[190:191], v79 offset:58368\n"
    "s_waitcnt vmcnt(12) lgkmcnt(0)\n"
    "s_barrier\n"
    "v_mfma_f32_16x16x32_bf16 v[192:195], v[144:147], v[128:131], v[192:195]\n"
    "v_lshlrev_b32 v110, 7, v110\n"
    "v_mfma_f32_16x16x32_bf16 v[192:195], v[176:179], v[160:163], v[192:195]\n"
    "v_add_u32_dpp v52, v109, v48 quad_perm:[0,0,0,0] row_mask:0xf bank_mask:0xf\n"
    "v_mfma_f32_16x16x32_bf16 v[196:199], v[176:179], v[164:167], v[196:199]\n"
    "v_mfma_f32_16x16x32_bf16 v[196:199], v[144:147], v[132:135], v[196:199]\n"
    "v_add_u32_dpp v53, v109, v49 quad_perm:[1,1,1,1] row_mask:0xf bank_mask:0xf\n"
    "v_mfma_f32_16x16x32_bf16 v[200:203], v[144:147], v[136:139], v[200:203]\n"
    "v_mfma_f32_16x16x32_bf16 v[200:203], v[176:179], v[168:171], v[200:203]\n"
    "v_add_u32_dpp v54, v109, v50 quad_perm:[2,2,2,2] row_mask:0xf bank_mask:0xf\n"
    "v_mfma_f32_16x16x32_bf16 v[204:207], v[176:179], v[172:175], v[204:207]\n"
    "v_mfma_f32_16x16x32_bf16 v[204:207], v[144:147], v[140:143], v[204:207]\n"
    "v_add_u32_dpp v55, v109, v51 quad_perm:[3,3,3,3] row_mask:0xf bank_mask:0xf\n"
    "v_mfma_f32_16x16x32_bf16 v[220:223], v[148:151], v[140:143], v[220:223]\n"
    "v_mfma_f32_16x16x32_bf16 v[220:223], v[180:183], v[172:175], v[220:223]\n"
    "v_and_b32 v12, 3, v12\n"
    "v_mfma_f32_16x16x32_bf16 v[216:219], v[180:183], v[168:171], v[216:219]\n"
    "v_mfma_f32_16x16x32_bf16 v[216:219], v[148:151], v[136:139], v[216:219]\n"
    "v_add_lshl_u32 v12, v12, s41, 12\n"
    "v_mfma_f32_16x16x32_bf16 v[212:215], v[148:151], v[132:135], v[212:215]\n"
    "v_mfma_f32_16x16x32_bf16 v[212:215], v[180:183], v[164:167], v[212:215]\n"
    "v_and_b32 v12, 0x7000, v12\n"
    "v_mfma_f32_16x16x32_bf16 v[208:211], v[180:183], v[160:163], v[208:211]\n"
    "v_mfma_f32_16x16x32_bf16 v[208:211], v[148:151], v[128:131], v[208:211]\n"
    "v_add3_u32 v16, v12, v16, s43\n"
    "v_mfma_f32_16x16x32_bf16 v[224:227], v[152:155], v[128:131], v[224:227]\n"
    "v_mfma_f32_16x16x32_bf16 v[224:227], v[184:187], v[160:163], v[224:227]\n"
    "v_add_u32 v16, 3456, v16\n"
    "v_mfma_f32_16x16x32_bf16 v[228:231], v[184:187], v[164:167], v[228:231]\n"
    "v_mfma_f32_16x16x32_bf16 v[228:231], v[152:155], v[132:135], v[228:231]\n"
    "s_mul_i32 s41, s47, 13\n"
    "v_mfma_f32_16x16x32_bf16 v[232:235], v[152:155], v[136:139], v[232:235]\n"
    "v_mfma_f32_16x16x32_bf16 v[232:235], v[184:187], v[168:171], v[232:235]\n"
    "s_sub_u32 s41, s3, s41\n"
    "v_mfma_f32_16x16x32_bf16 v[236:239], v[184:187], v[172:175], v[236:239]\n"
    "v_mfma_f32_16x16x32_bf16 v[236:239], v[152:155], v[140:143], v[236:239]\n"
    "s_lshl1_add_u32 s41, s41, s21\n"
    "v_mfma_f32_16x16x32_bf16 v[252:255], v[156:159], v[140:143], v[252:255]\n"
    "v_mfma_f32_16x16x32_bf16 v[252:255], v[188:191], v[172:175], v[252:255]\n"
    "s_mul_i32 s41, s41, 3\n"
    "v_mfma_f32_16x16x32_bf16 v[248:251], v[188:191], v[168:171], v[248:251]\n"
    "v_mfma_f32_16x16x32_bf16 v[248:251], v[156:159], v[136:139], v[248:251]\n"
    "s_add_u32 s41, s41, s22\n"
    "v_mfma_f32_16x16x32_bf16 v[244:247], v[156:159], v[132:135], v[244:247]\n"
    "v_mfma_f32_16x16x32_bf16 v[244:247], v[188:191], v[164:167], v[244:247]\n"
    "v_mov_b32 v24, s41\n"
    "v_mfma_f32_16x16x32_bf16 v[240:243], v[188:191], v[160:163], v[240:243]\n"
    "v_mfma_f32_16x16x32_bf16 v[240:243], v[156:159], v[128:131], v[240:243]\n"
    "s_waitcnt vmcnt(8)\n"
    "s_barrier\n"
    "s_add_u32 m0, s35, 49152\n"
    "s_nop 0\n"
    "global_load_lds_dwordx4 v46, s[30:31] offset:0\n"
    "ds_read_b128 v[128:131], v58 offset:0\n"
    "ds_read_b128 v[132:135], v58 offset:2048\n"
    "ds_read_b128 v[136:139], v58 offset:4096\n"
    "global_load_lds_dwordx4 v47, s[30:31] offset:1024\n"
    "s_add_u32 s30, s30, 212992\n"
    "s_addc_u32 s31, s31, 0\n"
    "ds_read_b128 v[140:143], v58 offset:6144\n"
    "ds_read_b64_tr_b16 v[144:145], v80 offset:0\n"
    "ds_read_b64_tr_b16 v[146:147], v80 offset:1024\n"
    "s_add_u32 m0, s34, 49152\n"
    "s_nop 0\n"
    "global_load_lds_dwordx4 v52, s[36:37] offset:0\n"
    "ds_read_b64_tr_b16 v[148:149], v81 offset:0\n"
    "ds_read_b64_tr_b16 v[150:151], v81 offset:1024\n"
    "ds_read_b64_tr_b16 v[152:153], v82 offset:0\n"
    "global_load_lds_dwordx4 v53, s[36:37] offset:1024\n"
    "ds_read_b64_tr_b16 v[154:155], v82 offset:1024\n"
    "ds_read_b64_tr_b16 v[156:157], v83 offset:0\n"
    "ds_read_b64_tr_b16 v[158:159], v83 offset:1024\n"
    "global_load_lds_dwordx4 v54, s[36:37] offset:2048\n"
    "ds_read_b128 v[160:163], v59 offset:0\n"
    "ds_read_b128 v[164:167], v59 offset:2048\n"
    "ds_read_b128 v[168:171], v59 offset:4096\n"
    "global_load_lds_dwordx4 v55, s[36:37] offset:3072\n"
    "ds_read_b128 v[172:175], v59 offset:6144\n"
    "ds_read_b64_tr_b16 v[176:177], v80 offset:8192\n"
    "ds_read_b64_tr_b16 v[178:179], v80 offset:9216\n"
    "global_load_dwordx2 v[124:125], v42, s[18:19] offset:96\n"
    "ds_read_b64_tr_b16 v[180:181], v81 offset:8192\n"
    "ds_read_b64_tr_b16 v[182:183], v81 offset:9216\n"
    "ds_read_b64_tr_b16 v[184:185], v82 offset:8192\n"
    "ds_read_b64_tr_b16 v[186:187], v82 offset:9216\n"
    "ds_read_b64_tr_b16 v[188:189], v83 offset:8192\n"
    "ds_read_b64_tr_b16 v[190:191], v83 offset:9216\n"
    "s_waitcnt vmcnt(12) lgkmcnt(0)\n"
    "s_barrier\n"
    "v_mfma_f32_16x16x32_bf16 v[192:195], v[144:147], v[128:131], v[192:195]\n"
    "v_lshlrev_b32 v111, 7, v111\n"
    "v_mfma_f32_16x16x32_bf16 v[192:195], v[176:179], v[160:163], v[192:195]\n"
    "v_mfma_f32_16x16x32_bf16 v[196:199], v[176:179], v[164:167], v[196:199]\n"
    "v_add_u32_dpp v52, v110, v48 quad_perm:[0,0,0,0] row_mask:0xf bank_mask:0xf\n"
    "v_mfma_f32_16x16x32_bf16 v[196:199], v[144:147], v[132:135], v[196:199]\n"
    "v_mfma_f32_16x16x32_bf16 v[200:203], v[144:147], v[136:139], v[200:203]\n"
    "v_add_u32_dpp v53, v110, v49 quad_perm:[1,1,1,1] row_mask:0xf bank_mask:0xf\n"
    "v_mfma_f32_16x16x32_bf16 v[200:203], v[176:179], v[168:171], v[200:203]\n"
    "v_mfma_f32_16x16x32_bf16 v[204:207], v[176:179], v[172:175], v[204:207]\n"
    "v_add_u32_dpp v54, v110, v50 quad_perm:[2,2,2,2] row_mask:0xf bank_mask:0xf\n"
    "v_mfma_f32_16x16x32_bf16 v[204:207], v[144:147], v[140:143], v[204:207]\n"
    "v_mfma_f32_16x16x32_bf16 v[220:223], v[148:151], v[140:143], v[220:223]\n"
    "v_add_u32_dpp v55, v110, v51 quad_perm:[3,3,3,3] row_mask:0xf bank_mask:0xf\n"
    "v_mfma_f32_16x16x32_bf16 v[220:223], v[180:183], v[172:175], v[220:223]\n"
    "v_mfma_f32_16x16x32_bf16 v[216:219], v[180:183], v[168:171], v[216:219]\n"
    "v_mfma_f32_16x16x32_bf16 v[216:219], v[148:151], v[136:139], v[216:219]\n"
    "v_readfirstlane_b32 s40, v24\n"
    "v_mfma_f32_16x16x32_bf16 v[212:215], v[148:151], v[132:135], v[212:215]\n"
    "v_mfma_f32_16x16x32_bf16 v[212:215], v[180:183], v[164:167], v[212:215]\n"
    "s_or_b32 s40, s40, s44\n"
    "v_mfma_f32_16x16x32_bf16 v[208:211], v[180:183], v[160:163], v[208:211]\n"
    "v_mfma_f32_16x16x32_bf16 v[208:211], v[148:151], v[128:131], v[208:211]\n"
    "s_lshl1_add_u32 s41, s40, s54\n"
    "v_mfma_f32_16x16x32_bf16 v[224:227], v[152:155], v[128:131], v[224:227]\n"
    "v_mfma_f32_16x16x32_bf16 v[224:227], v[184:187], v[160:163], v[224:227]\n"
    "s_cmpk_lt_u32 s40, 256\n"
    "v_mfma_f32_16x16x32_bf16 v[228:231], v[184:187], v[164:167], v[228:231]\n"
    "v_mfma_f32_16x16x32_bf16 v[228:231], v[152:155], v[132:135], v[228:231]\n"
    "s_cselect_b32 s41, s41, 2\n"
    "v_mfma_f32_16x16x32_bf16 v[232:235], v[152:155], v[136:139], v[232:235]\n"
    "v_mfma_f32_16x16x32_bf16 v[232:235], v[184:187], v[168:171], v[232:235]\n"
    "v_mfma_f32_16x16x32_bf16 v[236:239], v[184:187], v[172:175], v[236:239]\n"
    "v_mov_b32 v24, s41\n"
    "v_mfma_f32_16x16x32_bf16 v[236:239], v[152:155], v[140:143], v[236:239]\n"
    "v_mfma_f32_16x16x32_bf16 v[252:255], v[156:159], v[140:143], v[252:255]\n"
    "s_mul_i32 s41, s41, 52\n"
    "v_mfma_f32_16x16x32_bf16 v[252:255], v[188:191], v[172:175], v[252:255]\n"
    "v_mfma_f32_16x16x32_bf16 v[248:251], v[188:191], v[168:171], v[248:251]\n"
    "s_add_u32 s38, s4, s41\n"
    "v_mfma_f32_16x16x32_bf16 v[248:251], v[156:159], v[136:139], v[248:251]\n"
    "v_mfma_f32_16x16x32_bf16 v[244:247], v[156:159], v[132:135], v[244:247]\n"
    "s_addc_u32 s39, s5, 0\n"
    "v_mfma_f32_16x16x32_bf16 v[244:247], v[188:191], v[164:167], v[244:247]\n"
    "v_mfma_f32_16x16x32_bf16 v[240:243], v[188:191], v[160:163], v[240:243]\n"
    "v_mfma_f32_16x16x32_bf16 v[240:243], v[156:159], v[128:131], v[240:243]\n"
    "s_waitcnt vmcnt(8)\n"
    "s_barrier\n"
    "s_add_u32 m0, s35, 98304\n"
    "s_nop 0\n"
    "global_load_lds_dwordx4 v46, s[30:31] offset:0\n"
    "ds_read_b128 v[128:131], v56 offset:0\n"
    "ds_read_b128 v[132:135], v56 offset:2048\n"
    "ds_read_b128 v[136:139], v56 offset:4096\n"
    "global_load_lds_dwordx4 v47, s[30:31] offset:1024\n"
    "s_add_u32 s66, s28, 5\n"
    "s_and_b32 s66, s66, 7\n"
    "s_mul_i32 s66, s66, 638976\n"
    "s_add_u32 s30, s6, s66\n"
    "s_addc_u32 s31, s7, 0\n"
    "ds_read_b128 v[140:143], v56 offset:6144\n"
    "ds_read_b64_tr_b16 v[144:145], v76 offset:0\n"
    "ds_read_b64_tr_b16 v[146:147], v76 offset:1024\n"
    "s_add_u32 m0, s34, 98304\n"
    "s_nop 0\n"
    "global_load_lds_dwordx4 v52, s[36:37] offset:0\n"
    "ds_read_b64_tr_b16 v[148:149], v77 offset:0\n"
    "ds_read_b64_tr_b16 v[150:151], v77 offset:1024\n"
    "ds_read_b64_tr_b16 v[152:153], v78 offset:0\n"
    "global_load_lds_dwordx4 v53, s[36:37] offset:1024\n"
    "ds_read_b64_tr_b16 v[154:155], v78 offset:1024\n"
    "ds_read_b64_tr_b16 v[156:157], v79 offset:0\n"
    "ds_read_b64_tr_b16 v[158:159], v79 offset:1024\n"
    "global_load_lds_dwordx4 v54, s[36:37] offset:2048\n"
    "ds_read_b128 v[160:163], v57 offset:0\n"
    "ds_read_b128 v[164:167], v57 offset:2048\n"
    "ds_read_b128 v[168:171], v57 offset:4096\n"
    "global_load_lds_dwordx4 v55, s[36:37] offset:3072\n"
    "ds_read_b128 v[172:175], v57 offset:6144\n"
    "ds_read_b64_tr_b16 v[176:177], v76 offset:8192\n"
    "ds_read_b64_tr_b16 v[178:179], v76 offset:9216\n"
    "global_load_dwordx3 v[36:38], v17, s[38:39] offset:-104\n"
    "ds_read_b64_tr_b16 v[180:181], v77 offset:8192\n"
    "ds_read_b64_tr_b16 v[182:183], v77 offset:9216\n"
    "ds_read_b64_tr_b16 v[184:185], v78 offset:8192\n"
    "ds_read_b64_tr_b16 v[186:187], v78 offset:9216\n"
    "ds_read_b64_tr_b16 v[188:189], v79 offset:8192\n"
    "ds_read_b64_tr_b16 v[190:191], v79 offset:9216\n"
    "s_waitcnt vmcnt(12) lgkmcnt(0)\n"
    "s_barrier\n"
    "v_mfma_f32_16x16x32_bf16 v[192:195], v[144:147], v[128:131], v[192:195]\n"
    "v_lshlrev_b32 v112, 7, v112\n"
    "v_mfma_f32_16x16x32_bf16 v[192:195], v[176:179], v[160:163], v[192:195]\n"
    "v_add_u32_dpp v52, v111, v48 quad_perm:[0,0,0,0] row_mask:0xf bank_mask:0xf\n"
    "v_mfma_f32_16x16x32_bf16 v[196:199], v[176:179], v[164:167], v[196:199]\n"
    "v_add_u32_dpp v53, v111, v49 quad_perm:[1,1,1,1] row_mask:0xf bank_mask:0xf\n"
    "v_mfma_f32_16x16x32_bf16 v[196:199], v[144:147], v[132:135], v[196:199]\n"
    "v_add_u32_dpp v54, v111, v50 quad_perm:[2,2,2,2] row_mask:0xf bank_mask:0xf\n"
    "v_mfma_f32_16x16x32_bf16 v[200:203], v[144:147], v[136:139], v[200:203]\n"
    "v_add_u32_dpp v55, v111, v51 quad_perm:[3,3,3,3] row_mask:0xf bank_mask:0xf\n"
    "v_mfma_f32_16x16x32_bf16 v[200:203], v[176:179], v[168:171], v[200:203]\n"
    "v_lshlrev_b32 v60, 16, v84\n"
    "v_mfma_f32_16x16x32_bf16 v[204:207], v[176:179], v[172:175], v[204:207]\n"
    "v_and_b32 v61, 0xffff0000, v84\n"
    "v_mfma_f32_16x16x32_bf16 v[204:207], v[144:147], v[140:143], v[204:207]\n"
    "v_mfma_f32_16x16x32_bf16 v[220:223], v[148:151], v[140:143], v[220:223]\n"
    "v_lshlrev_b32 v62, 16, v85\n"
    "v_mfma_f32_16x16x32_bf16 v[220:223], v[180:183], v[172:175], v[220:223]\n"
    "v_and_b32 v63, 0xffff0000, v85\n"
    "v_mfma_f32_16x16x32_bf16 v[216:219], v[180:183], v[168:171], v[216:219]\n"
    "v_lshlrev_b32 v64, 16, v86\n"
    "v_mfma_f32_16x16x32_bf16 v[216:219], v[148:151], v[136:139], v[216:219]\n"
    "v_and_b32 v65, 0xffff0000, v86\n"
    "v_mfma_f32_16x16x32_bf16 v[212:215], v[148:151], v[132:135], v[212:215]\n"
    "v_lshlrev_b32 v66, 16, v87\n"
    "v_mfma_f32_16x16x32_bf16 v[212:215], v[180:183], v[164:167], v[212:215]\n"
    "v_and_b32 v67, 0xffff0000, v87\n"
    "v_mfma_f32_16x16x32_bf16 v[208:211], v[180:183], v[160:163], v[208:211]\n"
    "v_lshlrev_b32 v68, 16, v88\n"
    "v_mfma_f32_16x16x32_bf16 v[208:211], v[148:151], v[128:131], v[208:211]\n"
    "v_mfma_f32_16x16x32_bf16 v[224:227], v[152:155], v[128:131], v[224:227]\n"
    "v_and_b32 v69, 0xffff0000, v88\n"
    "v_mfma_f32_16x16x32_bf16 v[224:227], v[184:187], v[160:163], v[224:227]\n"
    "v_lshlrev_b32 v70, 16, v89\n"
    "v_mfma_f32_16x16x32_bf16 v[228:231], v[184:187], v[164:167], v[228:231]\n"
    "v_and_b32 v71, 0xffff0000, v89\n"
    "v_mfma_f32_16x16x32_bf16 v[228:231], v[152:155], v[132:135], v[228:231]\n"
    "v_lshlrev_b32 v72, 16, v90\n"
    "v_mfma_f32_16x16x32_bf16 v[232:235], v[152:155], v[136:139], v[232:235]\n"
    "v_and_b32 v73, 0xffff0000, v90\n"
    "v_mfma_f32_16x16x32_bf16 v[232:235], v[184:187], v[168:171], v[232:235]\n"
    "v_lshlrev_b32 v74, 16, v91\n"
    "v_mfma_f32_16x16x32_bf16 v[236:239], v[184:187], v[172:175], v[236:239]\n"
    "v_and_b32 v75, 0xffff0000, v91\n"
    "v_mfma_f32_16x16x32_bf16 v[236:239], v[152:155], v[140:143], v[236:239]\n"
    "v_mfma_f32_16x16x32_bf16 v[252:255], v[156:159], v[140:143], v[252:255]\n"
    "s_lshl3_add_u32 s40, s2, 4096\n"
    "v_mfma_f32_16x16x32_bf16 v[252:255], v[188:191], v[172:175], v[252:255]\n"
    "s_lshl_b32 s41, s2, 2\n"
    "v_mfma_f32_16x16x32_bf16 v[248:251], v[188:191], v[168:171], v[248:251]\n"
    "v_lshlrev_b32 v11, 3, v1\n"
    "v_mfma_f32_16x16x32_bf16 v[248:251], v[156:159], v[136:139], v[248:251]\n"
    "v_add_u32 v11, 40960, v11\n"
    "v_mfma_f32_16x16x32_bf16 v[244:247], v[156:159], v[132:135], v[244:247]\n"
    "v_writelane_b32 v11, s40, 8\n"
    "v_mfma_f32_16x16x32_bf16 v[244:247], v[188:191], v[164:167], v[244:247]\n"
    "v_writelane_b32 v11, s41, 9\n"
    "v_mfma_f32_16x16x32_bf16 v[240:243], v[188:191], v[160:163], v[240:243]\n"
    "v_mfma_f32_16x16x32_bf16 v[240:243], v[156:159], v[128:131], v[240:243]\n"
    "s_waitcnt vmcnt(8)\n"
    "s_barrier\n"
    "s_add_u32 m0, s35, 0\n"
    "s_nop 0\n"
    "global_load_lds_dwordx4 v46, s[30:31] offset:0\n"
    "ds_read_b128 v[128:131], v56 offset:49152\n"
    "ds_read_b128 v[132:135], v56 offset:51200\n"
    "ds_read_b128 v[136:139], v56 offset:53248\n"
    "global_load_lds_dwordx4 v47, s[30:31] offset:1024\n"
    "s_add_u32 s30, s30, 212992\n"
    "s_addc_u32 s31, s31, 0\n"
    "ds_read_b128 v[140:143], v56 offset:55296\n"
    "ds_read_b64_tr_b16 v[144:145], v76 offset:49152\n"
    "ds_read_b64_tr_b16 v[146:147], v76 offset:50176\n"
    "s_add_u32 m0, s34, 0\n"
    "s_nop 0\n"
    "global_load_lds_dwordx4 v52, s[36:37] offset:0\n"
    "ds_read_b64_tr_b16 v[148:149], v77 offset:49152\n"
    "ds_read_b64_tr_b16 v[150:151], v77 offset:50176\n"
    "ds_read_b64_tr_b16 v[152:153], v78 offset:49152\n"
    "global_load_lds_dwordx4 v53, s[36:37] offset:1024\n"
    "ds_read_b64_tr_b16 v[154:155], v78 offset:50176\n"
    "ds_read_b64_tr_b16 v[156:157], v79 offset:49152\n"
    "ds_read_b64_tr_b16 v[158:159], v79 offset:50176\n"
    "global_load_lds_dwordx4 v54, s[36:37] offset:2048\n"
    "ds_read_b128 v[160:163], v57 offset:49152\n"
    "ds_read_b128 v[164:167], v57 offset:51200\n"
    "ds_read_b128 v[168:171], v57 offset:53248\n"
    "global_load_lds_dwordx4 v55, s[36:37] offset:3072\n"
    "ds_read_b128 v[172:175], v57 offset:55296\n"
    "ds_read_b64_tr_b16 v[176:177], v76 offset:57344\n"
    "ds_read_b64_tr_b16 v[178:179], v76 offset:58368\n"
    "global_load_dword v41, v18, s[38:39] offset:-8\n"
    "ds_read_b64_tr_b16 v[180:181], v77 offset:57344\n"
    "ds_read_b64_tr_b16 v[182:183], v77 offset:58368\n"
    "ds_read_b64_tr_b16 v[184:185], v78 offset:57344\n"
    "ds_read_b64_tr_b16 v[186:187], v78 offset:58368\n"
    "ds_read_b64_tr_b16 v[188:189], v79 offset:57344\n"
    "ds_read_b64_tr_b16 v[190:191], v79 offset:58368\n"
    "s_waitcnt vmcnt(12) lgkmcnt(0)\n"
    "s_barrier\n"
    "v_mfma_f32_16x16x32_bf16 v[192:195], v[144:147], v[128:131], v[192:195]\n"
    "v_mfma_f32_16x16x32_bf16 v[192:195], v[176:179], v[160:163], v[192:195]\n"
    "v_lshlrev_b32 v113, 7, v113\n"
    "v_mfma_f32_16x16x32_bf16 v[196:199], v[176:179], v[164:167], v[196:199]\n"
    "v_mfma_f32_16x16x32_bf16 v[196:199], v[144:147], v[132:135], v[196:199]\n"
    "v_add_u32_dpp v52, v112, v48 quad_perm:[0,0,0,0] row_mask:0xf bank_mask:0xf\n"
    "v_mfma_f32_16x16x32_bf16 v[200:203], v[144:147], v[136:139], v[200:203]\n"
    "v_mfma_f32_16x16x32_bf16 v[200:203], v[176:179], v[168:171], v[200:203]\n"
    "v_add_u32_dpp v53, v112, v49 quad_perm:[1,1,1,1] row_mask:0xf bank_mask:0xf\n"
    "v_mfma_f32_16x16x32_bf16 v[204:207], v[176:179], v[172:175], v[204:207]\n"
    "v_mfma_f32_16x16x32_bf16 v[204:207], v[144:147], v[140:143], v[204:207]\n"
    "v_add_u32_dpp v54, v112, v50 quad_perm:[2,2,2,2] row_mask:0xf bank_mask:0xf\n"
    "v_mfma_f32_16x16x32_bf16 v[220:223], v[148:151], v[140:143], v[220:223]\n"
    "v_mfma_f32_16x16x32_bf16 v[220:223], v[180:183], v[172:175], v[220:223]\n"
    "v_add_u32_dpp v55, v112, v51 quad_perm:[3,3,3,3] row_mask:0xf bank_mask:0xf\n"
    "v_mfma_f32_16x16x32_bf16 v[216:219], v[180:183], v[168:171], v[216:219]\n"
    "v_mfma_f32_16x16x32_bf16 v[216:219], v[148:151], v[136:139], v[216:219]\n"
    "v_mfma_f32_16x16x32_bf16 v[212:215], v[148:151], v[132:135], v[212:215]\n"
    "v_mfma_f32_16x16x32_bf16 v[212:215], v[180:183], v[164:167], v[212:215]\n"
    "v_mfma_f32_16x16x32_bf16 v[208:211], v[180:183], v[160:163], v[208:211]\n"
    "v_add_f32 v192, v192, v60\n"
    "v_mfma_f32_16x16x32_bf16 v[208:211], v[148:151], v[128:131], v[208:211]\n"
    "v_add_f32 v193, v193, v61\n"
    "v_mfma_f32_16x16x32_bf16 v[224:227], v[152:155], v[128:131], v[224:227]\n"
    "v_add_f32 v194, v194, v62\n"
    "v_mfma_f32_16x16x32_bf16 v[224:227], v[184:187], v[160:163], v[224:227]\n"
    "v_add_f32 v195, v195, v63\n"
    "v_mfma_f32_16x16x32_bf16 v[228:231], v[184:187], v[164:167], v[228:231]\n"
    "v_add_f32 v196, v196, v60\n"
    "v_mfma_f32_16x16x32_bf16 v[228:231], v[152:155], v[132:135], v[228:231]\n"
    "v_add_f32 v197, v197, v61\n"
    "v_mfma_f32_16x16x32_bf16 v[232:235], v[152:155], v[136:139], v[232:235]\n"
    "v_add_f32 v198, v198, v62\n"
    "v_mfma_f32_16x16x32_bf16 v[232:235], v[184:187], v[168:171], v[232:235]\n"
    "v_add_f32 v199, v199, v63\n"
    "v_mfma_f32_16x16x32_bf16 v[236:239], v[184:187], v[172:175], v[236:239]\n"
    "v_add_f32 v200, v200, v60\n"
    "v_mfma_f32_16x16x32_bf16 v[236:239], v[152:155], v[140:143], v[236:239]\n"
    "v_add_f32 v201, v201, v61\n"
    "v_mfma_f32_16x16x32_bf16 v[252:255], v[156:159], v[140:143], v[252:255]\n"
    "v_add_f32 v202, v202, v62\n"
    "v_mfma_f32_16x16x32_bf16 v[252:255], v[188:191], v[172:175], v[252:255]\n"
    "v_add_f32 v203, v203, v63\n"
    "v_mfma_f32_16x16x32_bf16 v[248:251], v[188:191], v[168:171], v[248:251]\n"
    "v_add_f32 v204, v204, v60\n"
    "v_mfma_f32_16x16x32_bf16 v[248:251], v[156:159], v[136:139], v[248:251]\n"
    "v_add_f32 v205, v205, v61\n"
    "v_mfma_f32_16x16x32_bf16 v[244:247], v[156:159], v[132:135], v[244:247]\n"
    "v_add_f32 v206, v206, v62\n"
    "v_mfma_f32_16x16x32_bf16 v[244:247], v[188:191], v[164:167], v[244:247]\n"
    "v_add_f32 v207, v207, v63\n"
    "v_mfma_f32_16x16x32_bf16 v[240:243], v[188:191], v[160:163], v[240:243]\n"
    "v_mfma_f32_16x16x32_bf16 v[240:243], v[156:159], v[128:131], v[240:243]\n"
    "s_waitcnt vmcnt(8)\n"
    "s_barrier\n"
    "s_cmp_eq_u32 s20, 3\n"
    "s_cbranch_scc1 .Lxd_on1\n"
    ".Lxd_on1r:\n"
    "s_add_u32 m0, s35, 49152\n"
    "s_nop 0\n"
    "global_load_lds_dwordx4 v46, s[30:31] offset:0\n"
    "ds_read_b128 v[128:131], v58 offset:0\n"
    "ds_read_b128 v[132:135], v58 offset:2048\n"
    "ds_read_b128 v[136:139], v58 offset:4096\n"
    "ds_read_b128 v[140:143], v58 offset:6144\n"
    "global_load_lds_dwordx4 v47, s[30:31] offset:1024\n"
    "s_add_u32 s30, s30, 212992\n"
    "s_addc_u32 s31, s31, 0\n"
    "ds_read_b64_tr_b16 v[144:145], v80 offset:0\n"
    "ds_read_b64_tr_b16 v[146:147], v80 offset:1024\n"
    "ds_read_b64_tr_b16 v[148:149], v81 offset:0\n"
    "ds_read_b64_tr_b16 v[150:151], v81 offset:1024\n"
    "s_add_u32 m0, s34, 49152\n"
    "s_nop 0\n"
    "global_load_lds_dwordx4 v52, s[36:37] offset:0\n"
    "ds_read_b64_tr_b16 v[152:153], v82 offset:0\n"
    "ds_read_b64_tr_b16 v[154:155], v82 offset:1024\n"
    "ds_read_b64_tr_b16 v[156:157], v83 offset:0\n"
    "ds_read_b64_tr_b16 v[158:159], v83 offset:1024\n"
    "global_load_lds_dwordx4 v53, s[36:37] offset:1024\n"
    "ds_read_b128 v[160:163], v59 offset:0\n"
    "ds_read_b128 v[164:167], v59 offset:2048\n"
    "ds_read_b128 v[168:171], v59 offset:4096\n"
    "ds_read_b128 v[172:175], v59 offset:6144\n"
    "global_load_lds_dwordx4 v54, s[36:37] offset:2048\n"
    "ds_read_b64_tr_b16 v[176:177], v80 offset:8192\n"
    "ds_read_b64_tr_b16 v[178:179], v80 offset:9216\n"
    "ds_read_b64_tr_b16 v[180:181], v81 offset:8192\n"
    "ds_read_b64_tr_b16 v[182:183], v81 offset:9216\n"
    "global_load_lds_dwordx4 v55, s[36:37] offset:3072\n"
    "ds_read_b64_tr_b16 v[184:185], v82 offset:8192\n"
    "ds_read_b64_tr_b16 v[186:187], v82 offset:9216\n"
    "ds_read_b64_tr_b16 v[188:189], v83 offset:8192\n"
    "ds_read_b64_tr_b16 v[190:191], v83 offset:9216\n"
    "s_waitcnt vmcnt(11) lgkmcnt(0)\n"
    "s_barrier\n"
    "v_mfma_f32_16x16x32_bf16 v[192:195], v[144:147], v[128:131], v[192:195]\n"
    "v_mfma_f32_16x16x32_bf16 v[192:195], v[176:179], v[160:163], v[192:195]\n"
    "v_lshlrev_b32 v114, 7, v114\n"
    "v_mfma_f32_16x16x32_bf16 v[196:199], v[176:179], v[164:167], v[196:199]\n"
    "v_mfma_f32_16x16x32_bf16 v[196:199], v[144:147], v[132:135], v[196:199]\n"
    "v_add_u32_dpp v52, v113, v48 quad_perm:[0,0,0,0] row_mask:0xf bank_mask:0xf\n"
    "v_mfma_f32_16x16x32_bf16 v[200:203], v[144:147], v[136:139], v[200:203]\n"
    "v_mfma_f32_16x16x32_bf16 v[200:203], v[176:179], v[168:171], v[200:203]\n"
    "v_add_u32_dpp v53, v113, v49 quad_perm:[1,1,1,1] row_mask:0xf bank_mask:0xf\n"
    "v_mfma_f32_16x16x32_bf16 v[204:207], v[176:179], v[172:175], v[204:207]\n"
    "v_mfma_f32_16x16x32_bf16 v[204:207], v[144:147], v[140:143], v[204:207]\n"
    "v_add_u32_dpp v54, v113, v50 quad_perm:[2,2,2,2] row_mask:0xf bank_mask:0xf\n"
    "v_mfma_f32_16x16x32_bf16 v[220:223], v[148:151], v[140:143], v[220:223]\n"
    "v_mfma_f32_16x16x32_bf16 v[220:223], v[180:183], v[172:175], v[220:223]\n"
    "v_add_u32_dpp v55, v113, v51 quad_perm:[3,3,3,3] row_mask:0xf bank_mask:0xf\n"
    "v_mfma_f32_16x16x32_bf16 v[216:219], v[180:183], v[168:171], v[216:219]\n"
    "v_mfma_f32_16x16x32_bf16 v[216:219], v[148:151], v[136:139], v[216:219]\n"
    "v_mfma_f32_16x16x32_bf16 v[212:215], v[148:151], v[132:135], v[212:215]\n"
    "v_mfma_f32_16x16x32_bf16 v[212:215], v[180:183], v[164:167], v[212:215]\n"
    "v_mfma_f32_16x16x32_bf16 v[208:211], v[180:183], v[160:163], v[208:211]\n"
    "v_mfma_f32_16x16x32_bf16 v[208:211], v[148:151], v[128:131], v[208:211]\n"
    "v_mfma_f32_16x16x32_bf16 v[224:227], v[152:155], v[128:131], v[224:227]\n"
    "v_mfma_f32_16x16x32_bf16 v[224:227], v[184:187], v[160:163], v[224:227]\n"
    "v_mfma_f32_16x16x32_bf16 v[228:231], v[184:187], v[164:167], v[228:231]\n"
    "v_add_f32 v222, v222, v66\n"
    "v_add_f32 v223, v223, v67\n"
    "v_mfma_f32_16x16x32_bf16 v[228:231], v[152:155], v[132:135], v[228:231]\n"
    "v_add_f32 v221, v221, v65\n"
    "v_mfma_f32_16x16x32_bf16 v[232:235], v[152:155], v[136:139], v[232:235]\n"
    "v_add_f32 v220, v220, v64\n"
    "v_mfma_f32_16x16x32_bf16 v[232:235], v[184:187], v[168:171], v[232:235]\n"
    "v_add_f32 v219, v219, v67\n"
    "v_mfma_f32_16x16x32_bf16 v[236:239], v[184:187], v[172:175], v[236:239]\n"
    "v_add_f32 v217, v217, v65\n"
    "v_add_f32 v218, v218, v66\n"
    "v_mfma_f32_16x16x32_bf16 v[236:239], v[152:155], v[140:143], v[236:239]\n"
    "v_add_f32 v216, v216, v64\n"
    "v_mfma_f32_16x16x32_bf16 v[252:255], v[156:159], v[140:143], v[252:255]\n"
    "v_add_f32 v215, v215, v67\n"
    "v_mfma_f32_16x16x32_bf16 v[252:255], v[188:191], v[172:175], v[252:255]\n"
    "v_add_f32 v214, v214, v66\n"
    "v_mfma_f32_16x16x32_bf16 v[248:251], v[188:191], v[168:171], v[248:251]\n"
    "v_add_f32 v212, v212, v64\n"
    "v_add_f32 v213, v213, v65\n"
    "v_mfma_f32_16x16x32_bf16 v[248:251], v[156:159], v[136:139], v[248:251]\n"
    "v_add_f32 v211, v211, v67\n"
    "v_mfma_f32_16x16x32_bf16 v[244:247], v[156:159], v[132:135], v[244:247]\n"
    "v_add_f32 v210, v210, v66\n"
    "v_mfma_f32_16x16x32_bf16 v[244:247], v[188:191], v[164:167], v[244:247]\n"
    "v_add_f32 v209, v209, v65\n"
    "v_mfma_f32_16x16x32_bf16 v[240:243], v[188:191], v[160:163], v[240:243]\n"
    "v_add_f32 v208, v208, v64\n"
    "v_mfma_f32_16x16x32_bf16 v[240:243], v[156:159], v[128:131], v[240:243]\n"
    "s_waitcnt vmcnt(7)\n"
    "s_barrier\n"
    "s_add_u32 m0, s35, 98304\n"
    "s_nop 0\n"
    "global_load_lds_dwordx4 v46, s[30:31] offset:0\n"
    "ds_read_b128 v[128:131], v56 offset:0\n"
    "ds_read_b128 v[132:135], v56 offset:2048\n"
    "ds_read_b128 v[136:139], v56 offset:4096\n"
    "ds_read_b128 v[140:143], v56 offset:6144\n"
    "global_load_lds_dwordx4 v47, s[30:31] offset:1024\n"
    "s_add_u32 s66, s28, 6\n"
    "s_and_b32 s66, s66, 7\n"
    "s_mul_i32 s66, s66, 638976\n"
    "s_add_u32 s30, s6, s66\n"
    "s_addc_u32 s31, s7, 0\n"
    "ds_read_b64_tr_b16 v[144:145], v76 offset:0\n"
    "ds_read_b64_tr_b16 v[146:147], v76 offset:1024\n"
    "ds_read_b64_tr_b16 v[148:149], v77 offset:0\n"
    "ds_read_b64_tr_b16 v[150:151], v77 offset:1024\n"
    "s_add_u32 m0, s34, 98304\n"
    "s_nop 0\n"
    "global_load_lds_dwordx4 v52, s[36:37] offset:0\n"
    "ds_read_b64_tr_b16 v[152:153], v78 offset:0\n"
    "ds_read_b64_tr_b16 v[154:155], v78 offset:1024\n"
    "ds_read_b64_tr_b16 v[156:157], v79 offset:0\n"
    "ds_read_b64_tr_b16 v[158:159], v79 offset:1024\n"
    "global_load_lds_dwordx4 v53, s[36:37] offset:1024\n"
    "ds_read_b128 v[160:163], v57 offset:0\n"
    "ds_read_b128 v[164:167], v57 offset:2048\n"
    "ds_read_b128 v[168:171], v57 offset:4096\n"
    "ds_read_b128 v[172:175], v57 offset:6144\n"
    "global_load_lds_dwordx4 v54, s[36:37] offset:2048\n"
    "ds_read_b64_tr_b16 v[176:177], v76 offset:8192\n"
    "ds_read_b64_tr_b16 v[178:179], v76 offset:9216\n"
    "ds_read_b64_tr_b16 v[180:181], v77 offset:8192\n"
    "ds_read_b64_tr_b16 v[182:183], v77 offset:9216\n"
    "global_load_lds_dwordx4 v55, s[36:37] offset:3072\n"
    "ds_read_b64_tr_b16 v[184:185], v78 offset:8192\n"
    "ds_read_b64_tr_b16 v[186:187], v78 offset:9216\n"
    "ds_read_b64_tr_b16 v[188:189], v79 offset:8192\n"
    "ds_read_b64_tr_b16 v[190:191], v79 offset:9216\n"
    "s_waitcnt vmcnt(10) lgkmcnt(0)\n"
    "s_barrier\n"
    "v_mfma_f32_16x16x32_bf16 v[192:195], v[144:147], v[128:131], v[192:195]\n"
    "v_lshlrev_b32 v115, 7, v115\n"
    "v_mfma_f32_16x16x32_bf16 v[192:195], v[176:179], v[160:163], v[192:195]\n"
    "v_add_u32_dpp v52, v114, v48 quad_perm:[0,0,0,0] row_mask:0xf bank_mask:0xf\n"
    "v_mfma_f32_16x16x32_bf16 v[196:199], v[176:179], v[164:167], v[196:199]\n"
    "v_add_u32_dpp v53, v114, v49 quad_perm:[1,1,1,1] row_mask:0xf bank_mask:0xf\n"
    "v_mfma_f32_16x16x32_bf16 v[196:199], v[144:147], v[132:135], v[196:199]\n"
    "v_add_u32_dpp v54, v114, v50 quad_perm:[2,2,2,2] row_mask:0xf bank_mask:0xf\n"
    "v_mfma_f32_16x16x32_bf16 v[200:203], v[144:147], v[136:139], v[200:203]\n"
    "v_add_u32_dpp v55, v114, v51 quad_perm:[3,3,3,3] row_mask:0xf bank_mask:0xf\n"
    "v_mfma_f32_16x16x32_bf16 v[200:203], v[176:179], v[168:171], v[200:203]\n"
    "v_lshl_or_b32 v36, v36, 7, v19\n"
    "v_mfma_f32_16x16x32_bf16 v[204:207], v[176:179], v[172:175], v[204:207]\n"
    "v_mfma_f32_16x16x32_bf16 v[204:207], v[144:147], v[140:143], v[204:207]\n"
    "v_lshl_or_b32 v37, v37, 7, v19\n"
    "v_mfma_f32_16x16x32_bf16 v[220:223], v[148:151], v[140:143], v[220:223]\n"
    "v_lshl_or_b32 v38, v38, 7, v19\n"
    "v_mfma_f32_16x16x32_bf16 v[220:223], v[180:183], v[172:175], v[220:223]\n"
    "v_lshl_or_b32 v41, v41, 7, v20\n"
    "v_mfma_f32_16x16x32_bf16 v[216:219], v[180:183], v[168:171], v[216:219]\n"
    "s_mov_b32 s64, 0xbdd2d3e8\n"
    "v_mfma_f32_16x16x32_bf16 v[216:219], v[148:151], v[136:139], v[216:219]\n"
    "s_mov_b32 s65, 0xc0135761\n"
    "v_mfma_f32_16x16x32_bf16 v[212:215], v[148:151], v[132:135], v[212:215]\n"
    "v_mfma_f32_16x16x32_bf16 v[212:215], v[180:183], v[164:167], v[212:215]\n"
    "s_and_b32 s41, s20, 1\n"
    "v_mfma_f32_16x16x32_bf16 v[208:211], v[180:183], v[160:163], v[208:211]\n"
    "s_lshl1_add_u32 s40, s21, s41\n"
    "v_mfma_f32_16x16x32_bf16 v[208:211], v[148:151], v[128:131], v[208:211]\n"
    "s_lshl_b32 s41, s40, 8\n"
    "v_mfma_f32_16x16x32_bf16 v[224:227], v[152:155], v[128:131], v[224:227]\n"
    "s_add_u32 s3, s41, 149504\n"
    "v_mfma_f32_16x16x32_bf16 v[224:227], v[184:187], v[160:163], v[224:227]\n"
    "s_lshl_b32 s40, s40, 6\n"
    "v_mfma_f32_16x16x32_bf16 v[228:231], v[184:187], v[164:167], v[228:231]\n"
    "v_mfma_f32_16x16x32_bf16 v[228:231], v[152:155], v[132:135], v[228:231]\n"
    "s_add_u32 s40, s40, s24\n"
    "v_mfma_f32_16x16x32_bf16 v[232:235], v[152:155], v[136:139], v[232:235]\n"
    "s_lshl_b32 s41, s23, 12\n"
    "v_mfma_f32_16x16x32_bf16 v[232:235], v[184:187], v[168:171], v[232:235]\n"
    "s_add_u32 s40, s40, s41\n"
    "v_mfma_f32_16x16x32_bf16 v[236:239], v[184:187], v[172:175], v[236:239]\n"
    "s_lshl3_add_u32 s40, s40, 65536\n"
    "v_mfma_f32_16x16x32_bf16 v[236:239], v[152:155], v[140:143], v[236:239]\n"
    "s_add_u32 s48, s40, s16\n"
    "v_mfma_f32_16x16x32_bf16 v[252:255], v[156:159], v[140:143], v[252:255]\n"
    "v_mfma_f32_16x16x32_bf16 v[252:255], v[188:191], v[172:175], v[252:255]\n"
    "s_addc_u32 s49, s17, 0\n"
    "v_mfma_f32_16x16x32_bf16 v[248:251], v[188:191], v[168:171], v[248:251]\n"
    "s_cmp_lt_u32 s22, 2\n"
    "v_mfma_f32_16x16x32_bf16 v[248:251], v[156:159], v[136:139], v[248:251]\n"
    "s_cselect_b32 s24, 16, 0\n"
    "v_mfma_f32_16x16x32_bf16 v[244:247], v[156:159], v[132:135], v[244:247]\n"
    "s_sub_u32 s23, 3, s22\n"
    "v_mfma_f32_16x16x32_bf16 v[244:247], v[188:191], v[164:167], v[244:247]\n"
    "s_and_b32 s23, s23, 3\n"
    "v_mfma_f32_16x16x32_bf16 v[240:243], v[188:191], v[160:163], v[240:243]\n"
    "v_mfma_f32_16x16x32_bf16 v[240:243], v[156:159], v[128:131], v[240:243]\n"
    "s_waitcnt vmcnt(6)\n"
    "s_barrier\n"
    "s_add_u32 m0, s35, 0\n"
    "s_nop 0\n"
    "global_load_lds_dwordx4 v46, s[30:31] offset:0\n"
    "ds_read_b128 v[128:131], v56 offset:49152\n"
    "ds_read_b128 v[132:135], v56 offset:51200\n"
    "ds_read_b128 v[136:139], v56 offset:53248\n"
    "global_load_lds_dwordx4 v47, s[30:31] offset:1024\n"
    "s_add_u32 s30, s30, 212992\n"
    "s_addc_u32 s31, s31, 0\n"
    "ds_read_b128 v[140:143], v56 offset:55296\n"
    "ds_read_b64_tr_b16 v[144:145], v76 offset:49152\n"
    "ds_read_b64_tr_b16 v[146:147], v76 offset:50176\n"
    "s_add_u32 m0, s34, 0\n"
    "s_nop 0\n"
    "global_load_lds_dwordx4 v52, s[36:37] offset:0\n"
    "ds_read_b64_tr_b16 v[148:149], v77 offset:49152\n"
    "ds_read_b64_tr_b16 v[150:151], v77 offset:50176\n"
    "ds_read_b64_tr_b16 v[152:153], v78 offset:49152\n"
    "global_load_lds_dwordx4 v53, s[36:37] offset:1024\n"
    "ds_read_b64_tr_b16 v[154:155], v78 offset:50176\n"
    "ds_read_b64_tr_b16 v[156:157], v79 offset:49152\n"
    "ds_read_b64_tr_b16 v[158:159], v79 offset:50176\n"
    "global_load_lds_dwordx4 v54, s[36:37] offset:2048\n"
    "ds_read_b128 v[160:163], v57 offset:49152\n"
    "ds_read_b128 v[164:167], v57 offset:51200\n"
    "ds_read_b128 v[168:171], v57 offset:53248\n"
    "global_load_lds_dwordx4 v55, s[36:37] offset:3072\n"
    "ds_read_b128 v[172:175], v57 offset:55296\n"
    "ds_read_b64_tr_b16 v[176:177], v76 offset:57344\n"
    "ds_read_b64_tr_b16 v[178:179], v76 offset:58368\n"
    "global_load_dwordx4 v[28:31], v36, s[8:9]\n"
    "ds_read_b64_tr_b16 v[180:181], v77 offset:57344\n"
    "ds_read_b64_tr_b16 v[182:183], v77 offset:58368\n"
    "ds_read_b64_tr_b16 v[184:185], v78 offset:57344\n"
    "ds_read_b64_tr_b16 v[186:187], v78 offset:58368\n"
    "ds_read_b64_tr_b16 v[188:189], v79 offset:57344\n"
    "ds_read_b64_tr_b16 v[190:191], v79 offset:58368\n"
    "s_waitcnt vmcnt(11) lgkmcnt(0)\n"
    "s_barrier\n"
    "v_mfma_f32_16x16x32_bf16 v[192:195], v[144:147], v[128:131], v[192:195]\n"
    "v_mfma_f32_16x16x32_bf16 v[192:195], v[176:179], v[160:163], v[192:195]\n"
    "v_add_f32 v224, v224, v68\n"
    "v_add_f32 v225, v225, v69\n"
    "v_mfma_f32_16x16x32_bf16 v[196:199], v[176:179], v[164:167], v[196:199]\n"
    "v_add_f32 v226, v226, v70\n"
    "v_mfma_f32_16x16x32_bf16 v[196:199], v[144:147], v[132:135], v[196:199]\n"
    "v_add_f32 v227, v227, v71\n"
    "v_mfma_f32_16x16x32_bf16 v[200:203], v[144:147], v[136:139], v[200:203]\n"
    "v_add_f32 v228, v228, v68\n"
    "v_mfma_f32_16x16x32_bf16 v[200:203], v[176:179], v[168:171], v[200:203]\n"
    "v_add_f32 v229, v229, v69\n"
    "v_add_f32 v230, v230, v70\n"
    "v_mfma_f32_16x16x32_bf16 v[204:207], v[176:179], v[172:175], v[204:207]\n"
    "v_add_f32 v231, v231, v71\n"
    "v_mfma_f32_16x16x32_bf16 v[204:207], v[144:147], v[140:143], v[204:207]\n"
    "v_add_f32 v232, v232, v68\n"
    "v_mfma_f32_16x16x32_bf16 v[220:223], v[148:151], v[140:143], v[220:223]\n"
    "v_add_f32 v233, v233, v69\n"
    "v_mfma_f32_16x16x32_bf16 v[220:223], v[180:183], v[172:175], v[220:223]\n"
    "v_add_f32 v234, v234, v70\n"
    "v_add_f32 v235, v235, v71\n"
    "v_mfma_f32_16x16x32_bf16 v[216:219], v[180:183], v[168:171], v[216:219]\n"
    "v_add_f32 v236, v236, v68\n"
    "v_mfma_f32_16x16x32_bf16 v[216:219], v[148:151], v[136:139], v[216:219]\n"
    "v_add_f32 v237, v237, v69\n"
    "v_mfma_f32_16x16x32_bf16 v[212:215], v[148:151], v[132:135], v[212:215]\n"
    "v_add_f32 v238, v238, v70\n"
    "v_mfma_f32_16x16x32_bf16 v[212:215], v[180:183], v[164:167], v[212:215]\n"
    "v_add_f32 v239, v239, v71\n"
    "v_mfma_f32_16x16x32_bf16 v[208:211], v[180:183], v[160:163], v[208:211]\n"
    "v_mfma_f32_16x16x32_bf16 v[208:211], v[148:151], v[128:131], v[208:211]\n"
    "v_mfma_f32_16x16x32_bf16 v[224:227], v[152:155], v[128:131], v[224:227]\n"
    "v_mfma_f32_16x16x32_bf16 v[224:227], v[184:187], v[160:163], v[224:227]\n"
    "v_mfma_f32_16x16x32_bf16 v[228:231], v[184:187], v[164:167], v[228:231]\n"
    "v_lshlrev_b32 v116, 7, v116\n"
    "v_mfma_f32_16x16x32_bf16 v[228:231], v[152:155], v[132:135], v[228:231]\n"
    "v_add_u32_dpp v52, v115, v48 quad_perm:[0,0,0,0] row_mask:0xf bank_mask:0xf\n"
    "v_mfma_f32_16x16x32_bf16 v[232:235], v[152:155], v[136:139], v[232:235]\n"
    "v_add_u32_dpp v53, v115, v49 quad_perm:[1,1,1,1] row_mask:0xf bank_mask:0xf\n"
    "v_mfma_f32_16x16x32_bf16 v[232:235], v[184:187], v[168:171], v[232:235]\n"
    "v_add_u32_dpp v54, v115, v50 quad_perm:[2,2,2,2] row_mask:0xf bank_mask:0xf\n"
    "v_mfma_f32_16x16x32_bf16 v[236:239], v[184:187], v[172:175], v[236:239]\n"
    "v_add_u32_dpp v55, v115, v51 quad_perm:[3,3,3,3] row_mask:0xf bank_mask:0xf\n"
    "v_mfma_f32_16x16x32_bf16 v[236:239], v[152:155], v[140:143], v[236:239]\n"
    "s_getpc_b64 s[10:11]\n"
    ".Lxd_en2:\n"
    "v_mfma_f32_16x16x32_bf16 v[252:255], v[156:159], v[140:143], v[252:255]\n"
    "s_mov_b32 s40, .Lxd_ph1-.Lxd_en2\n"
    "v_mfma_f32_16x16x32_bf16 v[252:255], v[188:191], v[172:175], v[252:255]\n"
    "s_cmp_eq_u32 s22, 1\n"
    "s_cselect_b32 s40, .Lxd_ph0-.Lxd_en2, s40\n"
    "v_mfma_f32_16x16x32_bf16 v[248:251], v[188:191], v[168:171], v[248:251]\n"
    "s_cmp_eq_u32 s22, 2\n"
    "s_cselect_b32 s40, .Lxd_pl3-.Lxd_en2, s40\n"
    "v_mfma_f32_16x16x32_bf16 v[248:251], v[156:159], v[136:139], v[248:251]\n"
    "s_cmp_eq_u32 s22, 3\n"
    "s_cselect_b32 s40, .Lxd_ph2-.Lxd_en2, s40\n"
    "v_mfma_f32_16x16x32_bf16 v[244:247], v[156:159], v[132:135], v[244:247]\n"
    "s_ashr_i32 s41, s40, 31\n"
    "v_mfma_f32_16x16x32_bf16 v[244:247], v[188:191], v[164:167], v[244:247]\n"
    "s_add_u32 s10, s10, s40\n"
    "s_addc_u32 s11, s11, s41\n"
    "v_mfma_f32_16x16x32_bf16 v[240:243], v[188:191], v[160:163], v[240:243]\n"
    "v_mfma_f32_16x16x32_bf16 v[240:243], v[156:159], v[128:131], v[240:243]\n"
    "s_waitcnt vmcnt(7)\n"
    "s_barrier\n"
    "s_add_u32 m0, s35, 49152\n"
    "s_nop 0\n"
    "global_load_lds_dwordx4 v46, s[30:31] offset:0\n"
    "ds_read_b128 v[128:131], v58 offset:0\n"
    "ds_read_b128 v[132:135], v58 offset:2048\n"
    "ds_read_b128 v[136:139], v58 offset:4096\n"
    "global_load_lds_dwordx4 v47, s[30:31] offset:1024\n"
    "s_add_u32 s30, s30, 212992\n"
    "s_addc_u32 s31, s31, 0\n"
    "ds_read_b128 v[140:143], v58 offset:6144\n"
    "ds_read_b64_tr_b16 v[144:145], v80 offset:0\n"
    "ds_read_b64_tr_b16 v[146:147], v80 offset:1024\n"
    "s_add_u32 m0, s34, 49152\n"
    "s_nop 0\n"
    "global_load_lds_dwordx4 v52, s[36:37] offset:0\n"
    "ds_read_b64_tr_b16 v[148:149], v81 offset:0\n"
    "ds_read_b64_tr_b16 v[150:151], v81 offset:1024\n"
    "ds_read_b64_tr_b16 v[152:153], v82 offset:0\n"
    "global_load_lds_dwordx4 v53, s[36:37] offset:1024\n"
    "ds_read_b64_tr_b16 v[154:155], v82 offset:1024\n"
    "ds_read_b64_tr_b16 v[156:157], v83 offset:0\n"
    "ds_read_b64_tr_b16 v[158:159], v83 offset:1024\n"
    "global_load_lds_dwordx4 v54, s[36:37] offset:2048\n"
    "ds_read_b128 v[160:163], v59 offset:0\n"
    "ds_read_b128 v[164:167], v59 offset:2048\n"
    "ds_read_b128 v[168:171], v59 offset:4096\n"
    "global_load_lds_dwordx4 v55, s[36:37] offset:3072\n"
    "ds_read_b128 v[172:175], v59 offset:6144\n"
    "ds_read_b64_tr_b16 v[176:177], v80 offset:8192\n"
    "ds_read_b64_tr_b16 v[178:179], v80 offset:9216\n"
    "global_load_dwordx4 v[32:35], v37, s[8:9]\n"
    "ds_read_b64_tr_b16 v[180:181], v81 offset:8192\n"
    "ds_read_b64_tr_b16 v[182:183], v81 offset:9216\n"
    "ds_read_b64_tr_b16 v[184:185], v82 offset:8192\n"
    "ds_read_b64_tr_b16 v[186:187], v82 offset:9216\n"
    "ds_read_b64_tr_b16 v[188:189], v83 offset:8192\n"
    "ds_read_b64_tr_b16 v[190:191], v83 offset:9216\n"
    "s_waitcnt vmcnt(12) lgkmcnt(0)\n"
    "s_barrier\n"
    "v_mfma_f32_16x16x32_bf16 v[192:195], v[144:147], v[128:131], v[192:195]\n"
    "v_mfma_f32_16x16x32_bf16 v[192:195], v[176:179], v[160:163], v[192:195]\n"
    "v_mfma_f32_16x16x32_bf16 v[196:199], v[176:179], v[164:167], v[196:199]\n"
    "v_mfma_f32_16x16x32_bf16 v[196:199], v[144:147], v[132:135], v[196:199]\n"
    "v_mfma_f32_16x16x32_bf16 v[200:203], v[144:147], v[136:139], v[200:203]\n"
    "v_add_f32 v240, v240, v72\n"
    "v_mfma_f32_16x16x32_bf16 v[200:203], v[176:179], v[168:171], v[200:203]\n"
    "v_add_f32 v241, v241, v73\n"
    "v_mfma_f32_16x16x32_bf16 v[204:207], v[176:179], v[172:175], v[204:207]\n"
    "v_add_f32 v242, v242, v74\n"
    "v_mfma_f32_16x16x32_bf16 v[204:207], v[144:147], v[140:143], v[204:207]\n"
    "v_add_f32 v243, v243, v75\n"
    "v_mfma_f32_16x16x32_bf16 v[220:223], v[148:151], v[140:143], v[220:223]\n"
    "v_add_f32 v244, v244, v72\n"
    "v_mfma_f32_16x16x32_bf16 v[220:223], v[180:183], v[172:175], v[220:223]\n"
    "v_add_f32 v245, v245, v73\n"
    "v_mfma_f32_16x16x32_bf16 v[216:219], v[180:183], v[168:171], v[216:219]\n"
    "v_add_f32 v246, v246, v74\n"
    "v_mfma_f32_16x16x32_bf16 v[216:219], v[148:151], v[136:139], v[216:219]\n"
    "v_add_f32 v247, v247, v75\n"
    "v_mfma_f32_16x16x32_bf16 v[212:215], v[148:151], v[132:135], v[212:215]\n"
    "v_add_f32 v248, v248, v72\n"
    "v_mfma_f32_16x16x32_bf16 v[212:215], v[180:183], v[164:167], v[212:215]\n"
    "v_add_f32 v249, v249, v73\n"
    "v_mfma_f32_16x16x32_bf16 v[208:211], v[180:183], v[160:163], v[208:211]\n"
    "v_add_f32 v250, v250, v74\n"
    "v_mfma_f32_16x16x32_bf16 v[208:211], v[148:151], v[128:131], v[208:211]\n"
    "v_add_f32 v251, v251, v75\n"
    "v_mfma_f32_16x16x32_bf16 v[224:227], v[152:155], v[128:131], v[224:227]\n"
    "v_add_f32 v252, v252, v72\n"
    "v_mfma_f32_16x16x32_bf16 v[224:227], v[184:187], v[160:163], v[224:227]\n"
    "v_add_f32 v253, v253, v73\n"
    "v_mfma_f32_16x16x32_bf16 v[228:231], v[184:187], v[164:167], v[228:231]\n"
    "v_lshlrev_b32 v117, 7, v117\n"
    "v_add_f32 v254, v254, v74\n"
    "v_mfma_f32_16x16x32_bf16 v[228:231], v[152:155], v[132:135], v[228:231]\n"
    "v_add_f32 v255, v255, v75\n"
    "v_mfma_f32_16x16x32_bf16 v[232:235], v[152:155], v[136:139], v[232:235]\n"
    "v_add_u32_dpp v52, v116, v48 quad_perm:[0,0,0,0] row_mask:0xf bank_mask:0xf\n"
    "v_mfma_f32_16x16x32_bf16 v[232:235], v[184:187], v[168:171], v[232:235]\n"
    "v_mfma_f32_16x16x32_bf16 v[236:239], v[184:187], v[172:175], v[236:239]\n"
    "v_mfma_f32_16x16x32_bf16 v[236:239], v[152:155], v[140:143], v[236:239]\n"
    "v_add_u32_dpp v53, v116, v49 quad_perm:[1,1,1,1] row_mask:0xf bank_mask:0xf\n"
    "v_mfma_f32_16x16x32_bf16 v[252:255], v[156:159], v[140:143], v[252:255]\n"
    "v_mfma_f32_16x16x32_bf16 v[252:255], v[188:191], v[172:175], v[252:255]\n"
    "v_add_u32_dpp v54, v116, v50 quad_perm:[2,2,2,2] row_mask:0xf bank_mask:0xf\n"
    "v_mfma_f32_16x16x32_bf16 v[248:251], v[188:191], v[168:171], v[248:251]\n"
    "v_mfma_f32_16x16x32_bf16 v[248:251], v[156:159], v[136:139], v[248:251]\n"
    "v_mfma_f32_16x16x32_bf16 v[244:247], v[156:159], v[132:135], v[244:247]\n"
    "v_add_u32_dpp v55, v116, v51 quad_perm:[3,3,3,3] row_mask:0xf bank_mask:0xf\n"
    "v_mfma_f32_16x16x32_bf16 v[244:247], v[188:191], v[164:167], v[244:247]\n"
    "v_mfma_f32_16x16x32_bf16 v[240:243], v[188:191], v[160:163], v[240:243]\n"
    "v_mfma_f32_16x16x32_bf16 v[240:243], v[156:159], v[128:131], v[240:243]\n"
    "s_waitcnt vmcnt(8)\n"
    "s_barrier\n"
    "s_add_u32 m0, s35, 98304\n"
    "s_nop 0\n"
    "global_load_lds_dwordx4 v46, s[30:31] offset:0\n"
    "ds_read_b128 v[128:131], v56 offset:0\n"
    "ds_read_b128 v[132:135], v56 offset:2048\n"
    "ds_read_b128 v[136:139], v56 offset:4096\n"
    "global_load_lds_dwordx4 v47, s[30:31] offset:1024\n"
    "s_add_u32 s66, s28, 7\n"
    "s_and_b32 s66, s66, 7\n"
    "s_mul_i32 s66, s66, 638976\n"
    "s_add_u32 s30, s6, s66\n"
    "s_addc_u32 s31, s7, 0\n"
    "ds_read_b128 v[140:143], v56 offset:6144\n"
    "ds_read_b64_tr_b16 v[144:145], v76 offset:0\n"
    "ds_read_b64_tr_b16 v[146:147], v76 offset:1024\n"
    "s_add_u32 m0, s34, 98304\n"
    "s_nop 0\n"
    "global_load_lds_dwordx4 v52, s[36:37] offset:0\n"
    "ds_read_b64_tr_b16 v[148:149], v77 offset:0\n"
    "ds_read_b64_tr_b16 v[150:151], v77 offset:1024\n"
    "ds_read_b64_tr_b16 v[152:153], v78 offset:0\n"
    "global_load_lds_dwordx4 v53, s[36:37] offset:1024\n"
    "ds_read_b64_tr_b16 v[154:155], v78 offset:1024\n"
    "ds_read_b64_tr_b16 v[156:157], v79 offset:0\n"
    "ds_read_b64_tr_b16 v[158:159], v79 offset:1024\n"
    "global_load_lds_dwordx4 v54, s[36:37] offset:2048\n"
    "ds_read_b128 v[160:163], v57 offset:0\n"
    "ds_read_b128 v[164:167], v57 offset:2048\n"
    "ds_read_b128 v[168:171], v57 offset:4096\n"
    "global_load_lds_dwordx4 v55, s[36:37] offset:3072\n"
    "ds_read_b128 v[172:175], v57 offset:6144\n"
    "ds_read_b64_tr_b16 v[176:177], v76 offset:8192\n"
    "ds_read_b64_tr_b16 v[178:179], v76 offset:9216\n"
    "global_load_dword v40, v41, s[8:9]\n"
    "ds_read_b64_tr_b16 v[180:181], v77 offset:8192\n"
    "ds_read_b64_tr_b16 v[182:183], v77 offset:9216\n"
    "ds_read_b64_tr_b16 v[184:185], v78 offset:8192\n"
    "ds_read_b64_tr_b16 v[186:187], v78 offset:9216\n"
    "ds_read_b64_tr_b16 v[188:189], v79 offset:8192\n"
    "ds_read_b64_tr_b16 v[190:191], v79 offset:9216\n"
    "s_waitcnt vmcnt(12) lgkmcnt(0)\n"
    "s_barrier\n"
    "v_mfma_f32_16x16x32_bf16 v[192:195], v[144:147], v[128:131], v[192:195]\n"
    "v_lshlrev_b32 v118, 7, v118\n"
    "v_mfma_f32_16x16x32_bf16 v[192:195], v[176:179], v[160:163], v[192:195]\n"
    "v_add_u32_dpp v52, v117, v48 quad_perm:[0,0,0,0] row_mask:0xf bank_mask:0xf\n"
    "v_mfma_f32_16x16x32_bf16 v[196:199], v[176:179], v[164:167], v[196:199]\n"
    "v_add_u32_dpp v53, v117, v49 quad_perm:[1,1,1,1] row_mask:0xf bank_mask:0xf\n"
    "v_mfma_f32_16x16x32_bf16 v[196:199], v[144:147], v[132:135], v[196:199]\n"
    "v_add_u32_dpp v54, v117, v50 quad_perm:[2,2,2,2] row_mask:0xf bank_mask:0xf\n"
    "v_mfma_f32_16x16x32_bf16 v[200:203], v[144:147], v[136:139], v[200:203]\n"
    "v_add_u32_dpp v55, v117, v51 quad_perm:[3,3,3,3] row_mask:0xf bank_mask:0xf\n"
    "v_mfma_f32_16x16x32_bf16 v[200:203], v[176:179], v[168:171], v[200:203]\n"
    "v_readlane_b32 s25, v26, 9\n"
    "v_mfma_f32_16x16x32_bf16 v[204:207], v[176:179], v[172:175], v[204:207]\n"
    "v_readlane_b32 s40, v26, 8\n"
    "v_mfma_f32_16x16x32_bf16 v[204:207], v[144:147], v[140:143], v[204:207]\n"
    "v_readlane_b32 s41, v27, 8\n"
    "v_mfma_f32_16x16x32_bf16 v[220:223], v[148:151], v[140:143], v[220:223]\n"
    "s_mul_i32 s42, s25, 0x9e3779b1\n"
    "v_mfma_f32_16x16x32_bf16 v[220:223], v[180:183], v[172:175], v[220:223]\n"
    "s_lshr_b32 s42, s42, 24\n"
    "v_mfma_f32_16x16x32_bf16 v[216:219], v[180:183], v[168:171], v[216:219]\n"
    "s_xor_b32 s42, s42, s40\n"
    "v_mfma_f32_16x16x32_bf16 v[216:219], v[148:151], v[136:139], v[216:219]\n"
    "s_and_b32 s42, s42, 0xff\n"
    "v_mfma_f32_16x16x32_bf16 v[212:215], v[148:151], v[132:135], v[212:215]\n"
    "s_xor_b32 s41, s41, s25\n"
    "v_mfma_f32_16x16x32_bf16 v[212:215], v[180:183], v[164:167], v[212:215]\n"
    "s_xor_b32 s41, s41, 0xa5c30f96\n"
    "v_mfma_f32_16x16x32_bf16 v[208:211], v[180:183], v[160:163], v[208:211]\n"
    "s_or_b32 s42, s41, s42\n"
    "v_mfma_f32_16x16x32_bf16 v[208:211], v[148:151], v[128:131], v[208:211]\n"
    "v_mfma_f32_16x16x32_bf16 v[224:227], v[152:155], v[128:131], v[224:227]\n"
    "s_lshr_b32 s41, s40, 8\n"
    "v_mfma_f32_16x16x32_bf16 v[224:227], v[184:187], v[160:163], v[224:227]\n"
    "s_mul_i32 s40, s41, 0x9e3779b1\n"
    "v_mfma_f32_16x16x32_bf16 v[228:231], v[184:187], v[164:167], v[228:231]\n"
    "s_xor_b32 s41, s41, 0xa5c30f96\n"
    "v_mfma_f32_16x16x32_bf16 v[228:231], v[152:155], v[132:135], v[228:231]\n"
    "s_lshr_b32 s40, s40, 24\n"
    "v_mfma_f32_16x16x32_bf16 v[232:235], v[152:155], v[136:139], v[232:235]\n"
    "v_xor_b32 v13, s40, v26\n"
    "v_mfma_f32_16x16x32_bf16 v[232:235], v[184:187], v[168:171], v[232:235]\n"
    "v_xor_b32 v22, s41, v27\n"
    "v_mfma_f32_16x16x32_bf16 v[236:239], v[184:187], v[172:175], v[236:239]\n"
    "v_lshl_or_b32 v13, v13, 24, v22\n"
    "v_mfma_f32_16x16x32_bf16 v[236:239], v[152:155], v[140:143], v[236:239]\n"
    "v_cmp_ne_u32 s[66:67], 0, v13\n"
    "v_mfma_f32_16x16x32_bf16 v[252:255], v[156:159], v[140:143], v[252:255]\n"
    "s_and_b32 s66, s66, 0xff\n"
    "v_mfma_f32_16x16x32_bf16 v[252:255], v[188:191], v[172:175], v[252:255]\n"
    "s_or_b32 s42, s42, s66\n"
    "v_mfma_f32_16x16x32_bf16 v[248:251], v[188:191], v[168:171], v[248:251]\n"
    "s_cmp_eq_u32 s20, 3\n"
    "v_mfma_f32_16x16x32_bf16 v[248:251], v[156:159], v[136:139], v[248:251]\n"
    "s_cselect_b32 s42, s42, 0\n"
    "v_mfma_f32_16x16x32_bf16 v[244:247], v[156:159], v[132:135], v[244:247]\n"
    "s_cmp_lg_u32 s42, 0\n"
    "v_mfma_f32_16x16x32_bf16 v[244:247], v[188:191], v[164:167], v[244:247]\n"
    "s_cbranch_scc1 .Lxd_pv3\n"
    ".Lxd_pv3r:\n"
    "v_mfma_f32_16x16x32_bf16 v[240:243], v[188:191], v[160:163], v[240:243]\n"
    "v_mfma_f32_16x16x32_bf16 v[240:243], v[156:159], v[128:131], v[240:243]\n"
    "s_waitcnt vmcnt(8)\n"
    "s_barrier\n"
    "s_add_u32 m0, s35, 0\n"
    "s_nop 0\n"
    "global_load_lds_dwordx4 v46, s[30:31] offset:0\n"
    "ds_read_b128 v[128:131], v56 offset:49152\n"
    "ds_read_b128 v[132:135], v56 offset:51200\n"
    "ds_read_b128 v[136:139], v56 offset:53248\n"
    "global_load_lds_dwordx4 v47, s[30:31] offset:1024\n"
    "s_add_u32 s30, s30, 212992\n"
    "s_addc_u32 s31, s31, 0\n"
    "ds_read_b128 v[140:143], v56 offset:55296\n"
    "ds_read_b64_tr_b16 v[144:145], v76 offset:49152\n"
    "ds_read_b64_tr_b16 v[146:147], v76 offset:50176\n"
    "s_add_u32 m0, s34, 0\n"
    "s_nop 0\n"
    "global_load_lds_dwordx4 v52, s[36:37] offset:0\n"
    "ds_read_b64_tr_b16 v[148:149], v77 offset:49152\n"
    "ds_read_b64_tr_b16 v[150:151], v77 offset:50176\n"
    "ds_read_b64_tr_b16 v[152:153], v78 offset:49152\n"
    "global_load_lds_dwordx4 v53, s[36:37] offset:1024\n"
    "ds_read_b64_tr_b16 v[154:155], v78 offset:50176\n"
    "ds_read_b64_tr_b16 v[156:157], v79 offset:49152\n"
    "ds_read_b64_tr_b16 v[158:159], v79 offset:50176\n"
    "global_load_lds_dwordx4 v54, s[36:37] offset:2048\n"
    "ds_read_b128 v[160:163], v57 offset:49152\n"
    "ds_read_b128 v[164:167], v57 offset:51200\n"
    "ds_read_b128 v[168:171], v57 offset:53248\n"
    "global_load_lds_dwordx4 v55, s[36:37] offset:3072\n"
    "ds_read_b128 v[172:175], v57 offset:55296\n"
    "ds_read_b64_tr_b16 v[176:177], v76 offset:57344\n"
    "ds_read_b64_tr_b16 v[178:179], v76 offset:58368\n"
    "global_load_dwordx4 v[36:39], v38, s[8:9]\n"
    "ds_read_b64_tr_b16 v[180:181], v77 offset:57344\n"
    "ds_read_b64_tr_b16 v[182:183], v77 offset:58368\n"
    "ds_read_b64_tr_b16 v[184:185], v78 offset:57344\n"
    "ds_read_b64_tr_b16 v[186:187], v78 offset:58368\n"
    "ds_read_b64_tr_b16 v[188:189], v79 offset:57344\n"
    "ds_read_b64_tr_b16 v[190:191], v79 offset:58368\n"
    "s_waitcnt vmcnt(12) lgkmcnt(0)\n"
    "s_barrier\n"
    "v_mfma_f32_16x16x32_bf16 v[192:195], v[144:147], v[128:131], v[192:195]\n"
    "v_lshlrev_b32 v119, 7, v119\n"
    "v_mfma_f32_16x16x32_bf16 v[192:195], v[176:179], v[160:163], v[192:195]\n"
    "v_add_u32_dpp v52, v118, v48 quad_perm:[0,0,0,0] row_mask:0xf bank_mask:0xf\n"
    "v_mfma_f32_16x16x32_bf16 v[196:199], v[176:179], v[164:167], v[196:199]\n"
    "v_add_u32_dpp v53, v118, v49 quad_perm:[1,1,1,1] row_mask:0xf bank_mask:0xf\n"
    "v_mfma_f32_16x16x32_bf16 v[196:199], v[144:147], v[132:135], v[196:199]\n"
    "v_mfma_f32_16x16x32_bf16 v[200:203], v[144:147], v[136:139], v[200:203]\n"
    "v_add_u32_dpp v54, v118, v50 quad_perm:[2,2,2,2] row_mask:0xf bank_mask:0xf\n"
    "v_mfma_f32_16x16x32_bf16 v[200:203], v[176:179], v[168:171], v[200:203]\n"
    "v_add_u32_dpp v55, v118, v51 quad_perm:[3,3,3,3] row_mask:0xf bank_mask:0xf\n"
    "v_mfma_f32_16x16x32_bf16 v[204:207], v[176:179], v[172:175], v[204:207]\n"
    "v_mfma_f32_16x16x32_bf16 v[204:207], v[144:147], v[140:143], v[204:207]\n"
    "v_lshlrev_b32 v60, 16, v92\n"
    "v_mfma_f32_16x16x32_bf16 v[220:223], v[148:151], v[140:143], v[220:223]\n"
    "v_and_b32 v61, 0xffff0000, v92\n"
    "v_mfma_f32_16x16x32_bf16 v[220:223], v[180:183], v[172:175], v[220:223]\n"
    "v_mfma_f32_16x16x32_bf16 v[216:219], v[180:183], v[168:171], v[216:219]\n"
    "v_lshlrev_b32 v62, 16, v93\n"
    "v_mfma_f32_16x16x32_bf16 v[216:219], v[148:151], v[136:139], v[216:219]\n"
    "v_and_b32 v63, 0xffff0000, v93\n"
    "v_mfma_f32_16x16x32_bf16 v[212:215], v[148:151], v[132:135], v[212:215]\n"
    "v_mfma_f32_16x16x32_bf16 v[212:215], v[180:183], v[164:167], v[212:215]\n"
    "v_lshlrev_b32 v64, 16, v94\n"
    "v_mfma_f32_16x16x32_bf16 v[208:211], v[180:183], v[160:163], v[208:211]\n"
    "v_and_b32 v65, 0xffff0000, v94\n"
    "v_mfma_f32_16x16x32_bf16 v[208:211], v[148:151], v[128:131], v[208:211]\n"
    "v_mfma_f32_16x16x32_bf16 v[224:227], v[152:155], v[128:131], v[224:227]\n"
    "v_lshlrev_b32 v66, 16, v95\n"
    "v_mfma_f32_16x16x32_bf16 v[224:227], v[184:187], v[160:163], v[224:227]\n"
    "v_and_b32 v67, 0xffff0000, v95\n"
    "v_mfma_f32_16x16x32_bf16 v[228:231], v[184:187], v[164:167], v[228:231]\n"
    "v_mfma_f32_16x16x32_bf16 v[228:231], v[152:155], v[132:135], v[228:231]\n"
    "v_lshlrev_b32 v68, 16, v122\n"
    "v_mfma_f32_16x16x32_bf16 v[232:235], v[152:155], v[136:139], v[232:235]\n"
    "v_and_b32 v69, 0xffff0000, v122\n"
    "v_mfma_f32_16x16x32_bf16 v[232:235], v[184:187], v[168:171], v[232:235]\n"
    "v_mfma_f32_16x16x32_bf16 v[236:239], v[184:187], v[172:175], v[236:239]\n"
    "v_lshlrev_b32 v70, 16, v123\n"
    "v_mfma_f32_16x16x32_bf16 v[236:239], v[152:155], v[140:143], v[236:239]\n"
    "v_and_b32 v71, 0xffff0000, v123\n"
    "v_mfma_f32_16x16x32_bf16 v[252:255], v[156:159], v[140:143], v[252:255]\n"
    "v_mfma_f32_16x16x32_bf16 v[252:255], v[188:191], v[172:175], v[252:255]\n"
    "v_lshlrev_b32 v72, 16, v124\n"
    "v_mfma_f32_16x16x32_bf16 v[248:251], v[188:191], v[168:171], v[248:251]\n"
    "v_and_b32 v73, 0xffff0000, v124\n"
    "v_mfma_f32_16x16x32_bf16 v[248:251], v[156:159], v[136:139], v[248:251]\n"
    "v_mfma_f32_16x16x32_bf16 v[244:247], v[156:159], v[132:135], v[244:247]\n"
    "v_lshlrev_b32 v74, 16, v125\n"
    "v_mfma_f32_16x16x32_bf16 v[244:247], v[188:191], v[164:167], v[244:247]\n"
    "v_and_b32 v75, 0xffff0000, v125\n"
    "v_mfma_f32_16x16x32_bf16 v[240:243], v[188:191], v[160:163], v[240:243]\n"
    "v_mfma_f32_16x16x32_bf16 v[240:243], v[156:159], v[128:131], v[240:243]\n"
    "s_waitcnt vmcnt(8)\n"
    "s_barrier\n"
    "s_add_u32 m0, s35, 49152\n"
    "s_nop 0\n"
    "global_load_lds_dwordx4 v46, s[30:31] offset:0\n"
    "ds_read_b128 v[128:131], v58 offset:0\n"
    "ds_read_b128 v[132:135], v58 offset:2048\n"
    "ds_read_b128 v[136:139], v58 offset:4096\n"
    "ds_read_b128 v[140:143], v58 offset:6144\n"
    "global_load_lds_dwordx4 v47, s[30:31] offset:1024\n"
    "s_add_u32 s30, s30, 212992\n"
    "s_addc_u32 s31, s31, 0\n"
    "ds_read_b64_tr_b16 v[144:145], v80 offset:0\n"
    "ds_read_b64_tr_b16 v[146:147], v80 offset:1024\n"
    "ds_read_b64_tr_b16 v[148:149], v81 offset:0\n"
    "ds_read_b64_tr_b16 v[150:151], v81 offset:1024\n"
    "s_add_u32 m0, s34, 49152\n"
    "s_nop 0\n"
    "global_load_lds_dwordx4 v52, s[36:37] offset:0\n"
    "ds_read_b64_tr_b16 v[152:153], v82 offset:0\n"
    "ds_read_b64_tr_b16 v[154:155], v82 offset:1024\n"
    "ds_read_b64_tr_b16 v[156:157], v83 offset:0\n"
    "ds_read_b64_tr_b16 v[158:159], v83 offset:1024\n"
    "global_load_lds_dwordx4 v53, s[36:37] offset:1024\n"
    "ds_read_b128 v[160:163], v59 offset:0\n"
    "ds_read_b128 v[164:167], v59 offset:2048\n"
    "ds_read_b128 v[168:171], v59 offset:4096\n"
    "ds_read_b128 v[172:175], v59 offset:6144\n"
    "global_load_lds_dwordx4 v54, s[36:37] offset:2048\n"
    "ds_read_b64_tr_b16 v[176:177], v80 offset:8192\n"
    "ds_read_b64_tr_b16 v[178:179], v80 offset:9216\n"
    "ds_read_b64_tr_b16 v[180:181], v81 offset:8192\n"
    "ds_read_b64_tr_b16 v[182:183], v81 offset:9216\n"
    "global_load_lds_dwordx4 v55, s[36:37] offset:3072\n"
    "ds_read_b64_tr_b16 v[184:185], v82 offset:8192\n"
    "ds_read_b64_tr_b16 v[186:187], v82 offset:9216\n"
    "ds_read_b64_tr_b16 v[188:189], v83 offset:8192\n"
    "ds_read_b64_tr_b16 v[190:191], v83 offset:9216\n"
    "s_waitcnt vmcnt(11) lgkmcnt(0)\n"
    "s_barrier\n"
    "v_mfma_f32_16x16x32_bf16 v[192:195], v[144:147], v[128:131], v[192:195]\n"
    "v_lshlrev_b32 v120, 7, v120\n"
    "v_mfma_f32_16x16x32_bf16 v[192:195], v[176:179], v[160:163], v[192:195]\n"
    "v_add_u32_dpp v52, v119, v48 quad_perm:[0,0,0,0] row_mask:0xf bank_mask:0xf\n"
    "v_mfma_f32_16x16x32_bf16 v[196:199], v[176:179], v[164:167], v[196:199]\n"
    "v_add_u32_dpp v53, v119, v49 quad_perm:[1,1,1,1] row_mask:0xf bank_mask:0xf\n"
    "v_mfma_f32_16x16x32_bf16 v[196:199], v[144:147], v[132:135], v[196:199]\n"
    "v_add_u32_dpp v54, v119, v50 quad_perm:[2,2,2,2] row_mask:0xf bank_mask:0xf\n"
    "v_mfma_f32_16x16x32_bf16 v[200:203], v[144:147], v[136:139], v[200:203]\n"
    "v_add_u32_dpp v55, v119, v51 quad_perm:[3,3,3,3] row_mask:0xf bank_mask:0xf\n"
    "v_mfma_f32_16x16x32_bf16 v[200:203], v[176:179], v[168:171], v[200:203]\n"
    "v_lshrrev_b32 v22, 8, v26\n"
    "v_mfma_f32_16x16x32_bf16 v[204:207], v[176:179], v[172:175], v[204:207]\n"
    "v_mfma_f32_16x16x32_bf16 v[204:207], v[144:147], v[140:143], v[204:207]\n"
    "v_mov_b32 v26, s40\n"
    "v_mfma_f32_16x16x32_bf16 v[220:223], v[148:151], v[140:143], v[220:223]\n"
    "v_mov_b32 v27, s41\n"
    "v_mfma_f32_16x16x32_bf16 v[220:223], v[180:183], v[172:175], v[220:223]\n"
    "v_add_u32_dpp v22, v22, v22 row_shl:4 row_mask:0xf bank_mask:0xf\n"
    "v_mfma_f32_16x16x32_bf16 v[216:219], v[180:183], v[168:171], v[216:219]\n"
    "v_add_u32_e64 v43, s25, 1\n"
    "v_mfma_f32_16x16x32_bf16 v[216:219], v[148:151], v[136:139], v[216:219]\n"
    "v_lshlrev_b32 v8, 2, v1\n"
    "v_mfma_f32_16x16x32_bf16 v[212:215], v[148:151], v[132:135], v[212:215]\n"
    "v_mfma_f32_16x16x32_bf16 v[212:215], v[180:183], v[164:167], v[212:215]\n"
    "v_max_u32_dpp v13, v22, v22 quad_perm:[1,0,3,2] row_mask:0xf bank_mask:0xf\n"
    "v_mfma_f32_16x16x32_bf16 v[208:211], v[180:183], v[160:163], v[208:211]\n"
    "v_add_u32 v8, 154112, v8\n"
    "v_mfma_f32_16x16x32_bf16 v[208:211], v[148:151], v[128:131], v[208:211]\n"
    "s_nop 0\n"
    "v_mfma_f32_16x16x32_bf16 v[224:227], v[152:155], v[128:131], v[224:227]\n"
    "v_max_u32_dpp v13, v13, v13 quad_perm:[2,3,0,1] row_mask:0xf bank_mask:0xf\n"
    "v_mfma_f32_16x16x32_bf16 v[224:227], v[184:187], v[160:163], v[224:227]\n"
    "v_sub_u32 v22, v22, v13\n"
    "v_mfma_f32_16x16x32_bf16 v[228:231], v[184:187], v[164:167], v[228:231]\n"
    "v_mfma_f32_16x16x32_bf16 v[228:231], v[152:155], v[132:135], v[228:231]\n"
    "v_subrev_u32 v13, 818, v13\n"
    "v_mfma_f32_16x16x32_bf16 v[232:235], v[152:155], v[136:139], v[232:235]\n"
    "v_ldexp_f32 v22, 1.0, v22\n"
    "v_mfma_f32_16x16x32_bf16 v[232:235], v[184:187], v[168:171], v[232:235]\n"
    "v_rcp_f32 v60, v60\n"
    "v_mfma_f32_16x16x32_bf16 v[236:239], v[184:187], v[172:175], v[236:239]\n"
    "v_rcp_f32 v61, v61\n"
    "v_mfma_f32_16x16x32_bf16 v[236:239], v[152:155], v[140:143], v[236:239]\n"
    "v_rcp_f32 v62, v62\n"
    "v_mfma_f32_16x16x32_bf16 v[252:255], v[156:159], v[140:143], v[252:255]\n"
    "v_mfma_f32_16x16x32_bf16 v[252:255], v[188:191], v[172:175], v[252:255]\n"
    "v_rcp_f32 v63, v63\n"
    "v_mfma_f32_16x16x32_bf16 v[248:251], v[188:191], v[168:171], v[248:251]\n"
    "v_rcp_f32 v64, v64\n"
    "v_mfma_f32_16x16x32_bf16 v[248:251], v[156:159], v[136:139], v[248:251]\n"
    "v_rcp_f32 v65, v65\n"
    "v_mfma_f32_16x16x32_bf16 v[244:247], v[156:159], v[132:135], v[244:247]\n"
    "v_rcp_f32 v66, v66\n"
    "v_mfma_f32_16x16x32_bf16 v[244:247], v[188:191], v[164:167], v[244:247]\n"
    "v_rcp_f32 v67, v67\n"
    "v_mfma_f32_16x16x32_bf16 v[240:243], v[188:191], v[160:163], v[240:243]\n"
    "v_mfma_f32_16x16x32_bf16 v[240:243], v[156:159], v[128:131], v[240:243]\n"
    "s_waitcnt vmcnt(7)\n"
    "s_barrier\n"
    "s_add_u32 m0, s35, 98304\n"
    "s_nop 0\n"
    "global_load_lds_dwordx4 v46, s[30:31] offset:0\n"
    "ds_read_b128 v[128:131], v56 offset:0\n"
    "ds_read_b128 v[132:135], v56 offset:2048\n"
    "ds_read_b128 v[136:139], v56 offset:4096\n"
    "ds_read_b128 v[140:143], v56 offset:6144\n"
    "global_load_lds_dwordx4 v47, s[30:31] offset:1024\n"
    "s_add_u32 s30, s6, 5111808\n"
    "s_addc_u32 s31, s7, 0\n"
    "ds_read_b64_tr_b16 v[144:145], v76 offset:0\n"
    "ds_read_b64_tr_b16 v[146:147], v76 offset:1024\n"
    "ds_read_b64_tr_b16 v[148:149], v77 offset:0\n"
    "ds_read_b64_tr_b16 v[150:151], v77 offset:1024\n"
    "s_add_u32 m0, s34, 98304\n"
    "s_nop 0\n"
    "global_load_lds_dwordx4 v52, s[36:37] offset:0\n"
    "ds_read_b64_tr_b16 v[152:153], v78 offset:0\n"
    "ds_read_b64_tr_b16 v[154:155], v78 offset:1024\n"
    "ds_read_b64_tr_b16 v[156:157], v79 offset:0\n"
    "ds_read_b64_tr_b16 v[158:159], v79 offset:1024\n"
    "global_load_lds_dwordx4 v53, s[36:37] offset:1024\n"
    "ds_read_b128 v[160:163], v57 offset:0\n"
    "ds_read_b128 v[164:167], v57 offset:2048\n"
    "ds_read_b128 v[168:171], v57 offset:4096\n"
    "ds_read_b128 v[172:175], v57 offset:6144\n"
    "global_load_lds_dwordx4 v54, s[36:37] offset:2048\n"
    "ds_read_b64_tr_b16 v[176:177], v76 offset:8192\n"
    "ds_read_b64_tr_b16 v[178:179], v76 offset:9216\n"
    "ds_read_b64_tr_b16 v[180:181], v77 offset:8192\n"
    "ds_read_b64_tr_b16 v[182:183], v77 offset:9216\n"
    "global_load_lds_dwordx4 v55, s[36:37] offset:3072\n"
    "ds_read_b64_tr_b16 v[184:185], v78 offset:8192\n"
    "ds_read_b64_tr_b16 v[186:187], v78 offset:9216\n"
    "ds_read_b64_tr_b16 v[188:189], v79 offset:8192\n"
    "ds_read_b64_tr_b16 v[190:191], v79 offset:9216\n"
    "s_waitcnt vmcnt(10) lgkmcnt(0)\n"
    "s_barrier\n"
    "v_mfma_f32_16x16x32_bf16 v[192:195], v[144:147], v[128:131], v[192:195]\n"
    "v_lshlrev_b32 v121, 7, v121\n"
    "v_mfma_f32_16x16x32_bf16 v[192:195], v[176:179], v[160:163], v[192:195]\n"
    "v_add_u32_dpp v52, v120, v48 quad_perm:[0,0,0,0] row_mask:0xf bank_mask:0xf\n"
    "v_mfma_f32_16x16x32_bf16 v[196:199], v[176:179], v[164:167], v[196:199]\n"
    "v_add_u32_dpp v53, v120, v49 quad_perm:[1,1,1,1] row_mask:0xf bank_mask:0xf\n"
    "v_mfma_f32_16x16x32_bf16 v[196:199], v[144:147], v[132:135], v[196:199]\n"
    "v_add_u32_dpp v54, v120, v50 quad_perm:[2,2,2,2] row_mask:0xf bank_mask:0xf\n"
    "v_mfma_f32_16x16x32_bf16 v[200:203], v[144:147], v[136:139], v[200:203]\n"
    "v_add_u32_dpp v55, v120, v51 quad_perm:[3,3,3,3] row_mask:0xf bank_mask:0xf\n"
    "v_mfma_f32_16x16x32_bf16 v[200:203], v[176:179], v[168:171], v[200:203]\n"
    "s_movk_i32 s43, 15744\n"
    "v_pk_maximum3_f16 v21, v28, v28, s43 op_sel_hi:[1,1,0] neg_lo:[0,1,0] neg_hi:[0,1,0]\n"
    "v_mfma_f32_16x16x32_bf16 v[204:207], v[176:179], v[172:175], v[204:207]\n"
    "v_pk_maximum3_f16 v21, v29, v29, v21 neg_lo:[0,1,0] neg_hi:[0,1,0]\n"
    "v_mfma_f32_16x16x32_bf16 v[204:207], v[144:147], v[140:143], v[204:207]\n"
    "v_mfma_f32_16x16x32_bf16 v[220:223], v[148:151], v[140:143], v[220:223]\n"
    "v_pk_maximum3_f16 v21, v30, v30, v21 neg_lo:[0,1,0] neg_hi:[0,1,0]\n"
    "v_mfma_f32_16x16x32_bf16 v[220:223], v[180:183], v[172:175], v[220:223]\n"
    "v_pk_maximum3_f16 v21, v31, v31, v21 neg_lo:[0,1,0] neg_hi:[0,1,0]\n"
    "v_mfma_f32_16x16x32_bf16 v[216:219], v[180:183], v[168:171], v[216:219]\n"
    "v_pk_maximum3_f16 v21, v32, v32, v21 neg_lo:[0,1,0] neg_hi:[0,1,0]\n"
    "v_mfma_f32_16x16x32_bf16 v[216:219], v[148:151], v[136:139], v[216:219]\n"
    "v_pk_maximum3_f16 v21, v33, v33, v21 neg_lo:[0,1,0] neg_hi:[0,1,0]\n"
    "v_mfma_f32_16x16x32_bf16 v[212:215], v[148:151], v[132:135], v[212:215]\n"
    "v_pk_maximum3_f16 v21, v34, v34, v21 neg_lo:[0,1,0] neg_hi:[0,1,0]\n"
    "v_mfma_f32_16x16x32_bf16 v[212:215], v[180:183], v[164:167], v[212:215]\n"
    "v_pk_maximum3_f16 v21, v35, v35, v21 neg_lo:[0,1,0] neg_hi:[0,1,0]\n"
    "v_mfma_f32_16x16x32_bf16 v[208:211], v[180:183], v[160:163], v[208:211]\n"
    "v_pk_maximum3_f16 v21, v36, v36, v21 neg_lo:[0,1,0] neg_hi:[0,1,0]\n"
    "v_mfma_f32_16x16x32_bf16 v[208:211], v[148:151], v[128:131], v[208:211]\n"
    "v_mfma_f32_16x16x32_bf16 v[224:227], v[152:155], v[128:131], v[224:227]\n"
    "v_pk_maximum3_f16 v21, v37, v37, v21 neg_lo:[0,1,0] neg_hi:[0,1,0]\n"
    "v_mfma_f32_16x16x32_bf16 v[224:227], v[184:187], v[160:163], v[224:227]\n"
    "v_pk_maximum3_f16 v21, v38, v38, v21 neg_lo:[0,1,0] neg_hi:[0,1,0]\n"
    "v_mfma_f32_16x16x32_bf16 v[228:231], v[184:187], v[164:167], v[228:231]\n"
    "v_pk_maximum3_f16 v21, v39, v39, v21 neg_lo:[0,1,0] neg_hi:[0,1,0]\n"
    "v_mfma_f32_16x16x32_bf16 v[228:231], v[152:155], v[132:135], v[228:231]\n"
    "v_pk_maximum3_f16 v21, v40, v40, v21 neg_lo:[0,1,0] neg_hi:[0,1,0]\n"
    "v_mfma_f32_16x16x32_bf16 v[232:235], v[152:155], v[136:139], v[232:235]\n"
    "v_pk_max_u16 v21, v21, v21 op_sel:[0,1] op_sel_hi:[1,0]\n"
    "v_mfma_f32_16x16x32_bf16 v[232:235], v[184:187], v[168:171], v[232:235]\n"
    "v_rcp_f32 v68, v68\n"
    "v_mfma_f32_16x16x32_bf16 v[236:239], v[184:187], v[172:175], v[236:239]\n"
    "v_rcp_f32 v69, v69\n"
    "v_mfma_f32_16x16x32_bf16 v[236:239], v[152:155], v[140:143], v[236:239]\n"
    "v_mfma_f32_16x16x32_bf16 v[252:255], v[156:159], v[140:143], v[252:255]\n"
    "v_rcp_f32 v70, v70\n"
    "v_mfma_f32_16x16x32_bf16 v[252:255], v[188:191], v[172:175], v[252:255]\n"
    "v_rcp_f32 v71, v71\n"
    "v_mfma_f32_16x16x32_bf16 v[248:251], v[188:191], v[168:171], v[248:251]\n"
    "v_rcp_f32 v72, v72\n"
    "v_mfma_f32_16x16x32_bf16 v[248:251], v[156:159], v[136:139], v[248:251]\n"
    "v_rcp_f32 v73, v73\n"
    "v_mfma_f32_16x16x32_bf16 v[244:247], v[156:159], v[132:135], v[244:247]\n"
    "v_rcp_f32 v74, v74\n"
    "v_mfma_f32_16x16x32_bf16 v[244:247], v[188:191], v[164:167], v[244:247]\n"
    "v_rcp_f32 v75, v75\n"
    "v_mfma_f32_16x16x32_bf16 v[240:243], v[188:191], v[160:163], v[240:243]\n"
    "v_mfma_f32_16x16x32_bf16 v[240:243], v[156:159], v[128:131], v[240:243]\n"
    "s_waitcnt vmcnt(6)\n"
    "s_barrier\n"
    "s_add_u32 m0, s35, 0\n"
    "s_nop 0\n"
    "global_load_lds_dwordx4 v46, s[30:31] offset:0\n"
    "ds_read_b128 v[128:131], v56 offset:49152\n"
    "ds_read_b128 v[132:135], v56 offset:51200\n"
    "ds_read_b128 v[136:139], v56 offset:53248\n"
    "ds_read_b128 v[140:143], v56 offset:55296\n"
    "global_load_lds_dwordx4 v47, s[30:31] offset:1024\n"
    "s_add_u32 s30, s30, 212992\n"
    "s_addc_u32 s31, s31, 0\n"
    "ds_read_b64_tr_b16 v[144:145], v76 offset:49152\n"
    "ds_read_b64_tr_b16 v[146:147], v76 offset:50176\n"
    "ds_read_b64_tr_b16 v[148:149], v77 offset:49152\n"
    "ds_read_b64_tr_b16 v[150:151], v77 offset:50176\n"
    "s_add_u32 m0, s34, 0\n"
    "s_nop 0\n"
    "global_load_lds_dwordx4 v52, s[36:37] offset:0\n"
    "ds_read_b64_tr_b16 v[152:153], v78 offset:49152\n"
    "ds_read_b64_tr_b16 v[154:155], v78 offset:50176\n"
    "ds_read_b64_tr_b16 v[156:157], v79 offset:49152\n"
    "ds_read_b64_tr_b16 v[158:159], v79 offset:50176\n"
    "global_load_lds_dwordx4 v53, s[36:37] offset:1024\n"
    "ds_read_b128 v[160:163], v57 offset:49152\n"
    "ds_read_b128 v[164:167], v57 offset:51200\n"
    "ds_read_b128 v[168:171], v57 offset:53248\n"
    "ds_read_b128 v[172:175], v57 offset:55296\n"
    "global_load_lds_dwordx4 v54, s[36:37] offset:2048\n"
    "ds_read_b64_tr_b16 v[176:177], v76 offset:57344\n"
    "ds_read_b64_tr_b16 v[178:179], v76 offset:58368\n"
    "ds_read_b64_tr_b16 v[180:181], v77 offset:57344\n"
    "ds_read_b64_tr_b16 v[182:183], v77 offset:58368\n"
    "global_load_lds_dwordx4 v55, s[36:37] offset:3072\n"
    "ds_read_b64_tr_b16 v[184:185], v78 offset:57344\n"
    "ds_read_b64_tr_b16 v[186:187], v78 offset:58368\n"
    "ds_read_b64_tr_b16 v[188:189], v79 offset:57344\n"
    "ds_read_b64_tr_b16 v[190:191], v79 offset:58368\n"
    "s_waitcnt vmcnt(10) lgkmcnt(0)\n"
    "s_barrier\n"
    "v_mfma_f32_16x16x32_bf16 v[192:195], v[144:147], v[128:131], v[192:195]\n"
    "v_add_u32_dpp v52, v121, v48 quad_perm:[0,0,0,0] row_mask:0xf bank_mask:0xf\n"
    "s_mov_b32 s42, 0x71800000\n"
    "v_mfma_f32_16x16x32_bf16 v[192:195], v[176:179], v[160:163], v[192:195]\n"
    "v_add_u32_dpp v53, v121, v49 quad_perm:[1,1,1,1] row_mask:0xf bank_mask:0xf\n"
    "v_mfma_f32_16x16x32_bf16 v[196:199], v[176:179], v[164:167], v[196:199]\n"
    "v_add_u32_dpp v54, v121, v50 quad_perm:[2,2,2,2] row_mask:0xf bank_mask:0xf\n"
    "v_mfma_f32_16x16x32_bf16 v[196:199], v[144:147], v[132:135], v[196:199]\n"
    "v_add_u32_dpp v55, v121, v51 quad_perm:[3,3,3,3] row_mask:0xf bank_mask:0xf\n"
    "v_mfma_f32_16x16x32_bf16 v[200:203], v[144:147], v[136:139], v[200:203]\n"
    "v_bfe_u32 v41, v1, 4, 1\n"
    "v_mfma_f32_16x16x32_bf16 v[200:203], v[176:179], v[168:171], v[200:203]\n"
    "v_lshlrev_b32 v41, 2, v41\n"
    "v_mfma_f32_16x16x32_bf16 v[204:207], v[176:179], v[172:175], v[204:207]\n"
    "v_add_u32 v41, 154112, v41\n"
    "v_mfma_f32_16x16x32_bf16 v[204:207], v[144:147], v[140:143], v[204:207]\n"
    "v_med3_f32 v60, v60, s42, -s42\n"
    "v_mfma_f32_16x16x32_bf16 v[220:223], v[148:151], v[140:143], v[220:223]\n"
    "v_max_u32_dpp v21, v21, v21 quad_perm:[1,0,3,2] row_mask:0xf bank_mask:0xf\n"
    "v_mfma_f32_16x16x32_bf16 v[220:223], v[180:183], v[172:175], v[220:223]\n"
    "v_med3_f32 v61, v61, s42, -s42\n"
    "v_mfma_f32_16x16x32_bf16 v[216:219], v[180:183], v[168:171], v[216:219]\n"
    "v_max_u32_dpp v21, v21, v21 quad_perm:[2,3,0,1] row_mask:0xf bank_mask:0xf\n"
    "v_mfma_f32_16x16x32_bf16 v[216:219], v[148:151], v[136:139], v[216:219]\n"
    "v_med3_f32 v62, v62, s42, -s42\n"
    "v_mfma_f32_16x16x32_bf16 v[212:215], v[148:151], v[132:135], v[212:215]\n"
    "v_max_u32_dpp v21, v21, v21 row_half_mirror row_mask:0xf bank_mask:0xf\n"
    "v_mfma_f32_16x16x32_bf16 v[212:215], v[180:183], v[164:167], v[212:215]\n"
    "v_med3_f32 v63, v63, s42, -s42\n"
    "v_mfma_f32_16x16x32_bf16 v[208:211], v[180:183], v[160:163], v[208:211]\n"
    "v_max_u32_dpp v21, v21, v21 row_mirror row_mask:0xf bank_mask:0xf\n"
    "v_mfma_f32_16x16x32_bf16 v[208:211], v[148:151], v[128:131], v[208:211]\n"
    "v_med3_f32 v64, v64, s42, -s42\n"
    "v_mfma_f32_16x16x32_bf16 v[224:227], v[152:155], v[128:131], v[224:227]\n"
    "v_max_u32_dpp v21, v21, v21 row_bcast:15 row_mask:0xa bank_mask:0xf\n"
    "v_mfma_f32_16x16x32_bf16 v[224:227], v[184:187], v[160:163], v[224:227]\n"
    "v_med3_f32 v65, v65, s42, -s42\n"
    "v_mfma_f32_16x16x32_bf16 v[228:231], v[184:187], v[164:167], v[228:231]\n"
    "v_max_u32_dpp v21, v21, v21 row_bcast:31 row_mask:0xc bank_mask:0xf\n"
    "v_mfma_f32_16x16x32_bf16 v[228:231], v[152:155], v[132:135], v[228:231]\n"
    "v_med3_f32 v66, v66, s42, -s42\n"
    "v_mfma_f32_16x16x32_bf16 v[232:235], v[152:155], v[136:139], v[232:235]\n"
    "v_readlane_b32 s47, v21, 63\n"
    "v_mfma_f32_16x16x32_bf16 v[232:235], v[184:187], v[168:171], v[232:235]\n"
    "v_med3_f32 v67, v67, s42, -s42\n"
    "v_mfma_f32_16x16x32_bf16 v[236:239], v[184:187], v[172:175], v[236:239]\n"
    "s_and_b32 s47, s47, 0x7f80\n"
    "v_mfma_f32_16x16x32_bf16 v[236:239], v[152:155], v[140:143], v[236:239]\n"
    "s_sub_u32 s50, s47, 2816\n"
    "v_mfma_f32_16x16x32_bf16 v[252:255], v[156:159], v[140:143], v[252:255]\n"
    "s_mul_i32 s51, s50, -8\n"
    "v_med3_f32 v68, v68, s42, -s42\n"
    "v_mfma_f32_16x16x32_bf16 v[252:255], v[188:191], v[172:175], v[252:255]\n"
    "s_lshr_b32 s47, s47, 3\n"
    "v_med3_f32 v69, v69, s42, -s42\n"
    "v_mfma_f32_16x16x32_bf16 v[248:251], v[188:191], v[168:171], v[248:251]\n"
    "v_med3_f32 v70, v70, s42, -s42\n"
    "v_mfma_f32_16x16x32_bf16 v[248:251], v[156:159], v[136:139], v[248:251]\n"
    "v_med3_f32 v71, v71, s42, -s42\n"
    "v_mfma_f32_16x16x32_bf16 v[244:247], v[156:159], v[132:135], v[244:247]\n"
    "v_med3_f32 v72, v72, s42, -s42\n"
    "v_mfma_f32_16x16x32_bf16 v[244:247], v[188:191], v[164:167], v[244:247]\n"
    "v_med3_f32 v73, v73, s42, -s42\n"
    "v_mfma_f32_16x16x32_bf16 v[240:243], v[188:191], v[160:163], v[240:243]\n"
    "v_med3_f32 v74, v74, s42, -s42\n"
    "v_mfma_f32_16x16x32_bf16 v[240:243], v[156:159], v[128:131], v[240:243]\n"
    "v_med3_f32 v75, v75, s42, -s42\n"
    "s_waitcnt vmcnt(6)\n"
    "s_barrier\n"
    "s_add_u32 m0, s35, 49152\n"
    "s_nop 0\n"
    "global_load_lds_dwordx4 v46, s[30:31] offset:0\n"
    "ds_read_b128 v[128:131], v58 offset:0\n"
    "ds_read_b128 v[132:135], v58 offset:2048\n"
    "ds_read_b128 v[136:139], v58 offset:4096\n"
    "ds_read_b128 v[140:143], v58 offset:6144\n"
    "global_load_lds_dwordx4 v47, s[30:31] offset:1024\n"
    "ds_read_b64_tr_b16 v[144:145], v80 offset:0\n"
    "ds_read_b64_tr_b16 v[146:147], v80 offset:1024\n"
    "ds_read_b64_tr_b16 v[148:149], v81 offset:0\n"
    "ds_read_b64_tr_b16 v[150:151], v81 offset:1024\n"
    "s_add_u32 m0, s34, 49152\n"
    "s_nop 0\n"
    "global_load_lds_dwordx4 v52, s[36:37] offset:0\n"
    "ds_read_b64_tr_b16 v[152:153], v82 offset:0\n"
    "ds_read_b64_tr_b16 v[154:155], v82 offset:1024\n"
    "ds_read_b64_tr_b16 v[156:157], v83 offset:0\n"
    "ds_read_b64_tr_b16 v[158:159], v83 offset:1024\n"
    "global_load_lds_dwordx4 v53, s[36:37] offset:1024\n"
    "ds_read_b128 v[160:163], v59 offset:0\n"
    "ds_read_b128 v[164:167], v59 offset:2048\n"
    "ds_read_b128 v[168:171], v59 offset:4096\n"
    "ds_read_b128 v[172:175], v59 offset:6144\n"
    "global_load_lds_dwordx4 v54, s[36:37] offset:2048\n"
    "ds_read_b64_tr_b16 v[176:177], v80 offset:8192\n"
    "ds_read_b64_tr_b16 v[178:179], v80 offset:9216\n"
    "ds_read_b64_tr_b16 v[180:181], v81 offset:8192\n"
    "ds_read_b64_tr_b16 v[182:183], v81 offset:9216\n"
    "global_load_lds_dwordx4 v55, s[36:37] offset:3072\n"
    "ds_read_b64_tr_b16 v[184:185], v82 offset:8192\n"
    "ds_read_b64_tr_b16 v[186:187], v82 offset:9216\n"
    "ds_read_b64_tr_b16 v[188:189], v83 offset:8192\n"
    "ds_read_b64_tr_b16 v[190:191], v83 offset:9216\n"
    "s_waitcnt vmcnt(10) lgkmcnt(0)\n"
    "s_barrier\n"
    "v_mfma_f32_16x16x32_bf16 v[192:195], v[144:147], v[128:131], v[192:195]\n"
    "v_pk_maximum3_f16 v21, v28, v28, s50 op_sel_hi:[1,1,0] neg_lo:[0,1,0] neg_hi:[0,1,0]\n"
    "v_mfma_f32_16x16x32_bf16 v[192:195], v[176:179], v[160:163], v[192:195]\n"
    "v_pk_mad_u16 v21, v21, 8, s51 op_sel_hi:[1,0,0]\n"
    "v_mfma_f32_16x16x32_bf16 v[196:199], v[176:179], v[164:167], v[196:199]\n"
    "v_bfi_b32 v28, s57, v21, v28\n"
    "v_mfma_f32_16x16x32_bf16 v[196:199], v[144:147], v[132:135], v[196:199]\n"
    "v_mfma_f32_16x16x32_bf16 v[200:203], v[144:147], v[136:139], v[200:203]\n"
    "v_pk_maximum3_f16 v12, v29, v29, s50 op_sel_hi:[1,1,0] neg_lo:[0,1,0] neg_hi:[0,1,0]\n"
    "v_mfma_f32_16x16x32_bf16 v[200:203], v[176:179], v[168:171], v[200:203]\n"
    "v_pk_mad_u16 v12, v12, 8, s51 op_sel_hi:[1,0,0]\n"
    "v_mfma_f32_16x16x32_bf16 v[204:207], v[176:179], v[172:175], v[204:207]\n"
    "v_bfi_b32 v29, s57, v12, v29\n"
    "v_mfma_f32_16x16x32_bf16 v[204:207], v[144:147], v[140:143], v[204:207]\n"
    "v_mfma_f32_16x16x32_bf16 v[220:223], v[148:151], v[140:143], v[220:223]\n"
    "v_pk_maximum3_f16 v21, v30, v30, s50 op_sel_hi:[1,1,0] neg_lo:[0,1,0] neg_hi:[0,1,0]\n"
    "v_mfma_f32_16x16x32_bf16 v[220:223], v[180:183], v[172:175], v[220:223]\n"
    "v_pk_mad_u16 v21, v21, 8, s51 op_sel_hi:[1,0,0]\n"
    "v_mfma_f32_16x16x32_bf16 v[216:219], v[180:183], v[168:171], v[216:219]\n"
    "v_bfi_b32 v30, s57, v21, v30\n"
    "v_mfma_f32_16x16x32_bf16 v[216:219], v[148:151], v[136:139], v[216:219]\n"
    "v_mfma_f32_16x16x32_bf16 v[212:215], v[148:151], v[132:135], v[212:215]\n"
    "v_pk_maximum3_f16 v12, v31, v31, s50 op_sel_hi:[1,1,0] neg_lo:[0,1,0] neg_hi:[0,1,0]\n"
    "v_mfma_f32_16x16x32_bf16 v[212:215], v[180:183], v[164:167], v[212:215]\n"
    "v_pk_mad_u16 v12, v12, 8, s51 op_sel_hi:[1,0,0]\n"
    "v_mfma_f32_16x16x32_bf16 v[208:211], v[180:183], v[160:163], v[208:211]\n"
    "v_bfi_b32 v31, s57, v12, v31\n"
    "v_mfma_f32_16x16x32_bf16 v[208:211], v[148:151], v[128:131], v[208:211]\n"
    "v_mfma_f32_16x16x32_bf16 v[224:227], v[152:155], v[128:131], v[224:227]\n"
    "v_pk_maximum3_f16 v21, v32, v32, s50 op_sel_hi:[1,1,0] neg_lo:[0,1,0] neg_hi:[0,1,0]\n"
    "v_mfma_f32_16x16x32_bf16 v[224:227], v[184:187], v[160:163], v[224:227]\n"
    "v_pk_mad_u16 v21, v21, 8, s51 op_sel_hi:[1,0,0]\n"
    "v_mfma_f32_16x16x32_bf16 v[228:231], v[184:187], v[164:167], v[228:231]\n"
    "v_bfi_b32 v32, s57, v21, v32\n"
    "v_mfma_f32_16x16x32_bf16 v[228:231], v[152:155], v[132:135], v[228:231]\n"
    "v_mfma_f32_16x16x32_bf16 v[232:235], v[152:155], v[136:139], v[232:235]\n"
    "v_pk_maximum3_f16 v12, v33, v33, s50 op_sel_hi:[1,1,0] neg_lo:[0,1,0] neg_hi:[0,1,0]\n"
    "v_mfma_f32_16x16x32_bf16 v[232:235], v[184:187], v[168:171], v[232:235]\n"
    "v_pk_mad_u16 v12, v12, 8, s51 op_sel_hi:[1,0,0]\n"
    "v_mfma_f32_16x16x32_bf16 v[236:239], v[184:187], v[172:175], v[236:239]\n"
    "v_bfi_b32 v33, s57, v12, v33\n"
    "v_mfma_f32_16x16x32_bf16 v[236:239], v[152:155], v[140:143], v[236:239]\n"
    "v_mfma_f32_16x16x32_bf16 v[252:255], v[156:159], v[140:143], v[252:255]\n"
    "v_pk_maximum3_f16 v21, v34, v34, s50 op_sel_hi:[1,1,0] neg_lo:[0,1,0] neg_hi:[0,1,0]\n"
    "v_mfma_f32_16x16x32_bf16 v[252:255], v[188:191], v[172:175], v[252:255]\n"
    "v_pk_mad_u16 v21, v21, 8, s51 op_sel_hi:[1,0,0]\n"
    "v_mfma_f32_16x16x32_bf16 v[248:251], v[188:191], v[168:171], v[248:251]\n"
    "v_bfi_b32 v34, s57, v21, v34\n"
    "v_mfma_f32_16x16x32_bf16 v[248:251], v[156:159], v[136:139], v[248:251]\n"
    "v_mfma_f32_16x16x32_bf16 v[244:247], v[156:159], v[132:135], v[244:247]\n"
    "v_lshlrev_b32 v52, 4, v1\n"
    "v_mfma_f32_16x16x32_bf16 v[244:247], v[188:191], v[164:167], v[244:247]\n"
    "v_add_u32 v53, 4096, v52\n"
    "v_mfma_f32_16x16x32_bf16 v[240:243], v[188:191], v[160:163], v[240:243]\n"
    "v_mfma_f32_16x16x32_bf16 v[240:243], v[156:159], v[128:131], v[240:243]\n"
    "s_waitcnt vmcnt(6)\n"
    "s_barrier\n"
    "s_mul_i32 s42, s20, 5376\n"
    "s_add_u32 s40, s42, 1048576\n"
    "s_add_u32 s38, s16, s40\n"
    "s_addc_u32 s39, s17, 0\n"
    "s_add_u32 m0, s42, 98304\n"
    "s_nop 0\n"
    "global_load_lds_dwordx4 v52, s[38:39] offset:0 sc1\n"
    "ds_read_b128 v[128:131], v56 offset:0\n"
    "ds_read_b128 v[132:135], v56 offset:2048\n"
    "ds_read_b128 v[136:139], v56 offset:4096\n"
    "ds_read_b128 v[140:143], v56 offset:6144\n"
    "global_load_lds_dwordx4 v52, s[38:39] offset:1024 sc1\n"
    "ds_read_b64_tr_b16 v[144:145], v76 offset:0\n"
    "ds_read_b64_tr_b16 v[146:147], v76 offset:1024\n"
    "ds_read_b64_tr_b16 v[148:149], v77 offset:0\n"
    "ds_read_b64_tr_b16 v[150:151], v77 offset:1024\n"
    "global_load_lds_dwordx4 v52, s[38:39] offset:2048 sc1\n"
    "ds_read_b64_tr_b16 v[152:153], v78 offset:0\n"
    "ds_read_b64_tr_b16 v[154:155], v78 offset:1024\n"
    "ds_read_b64_tr_b16 v[156:157], v79 offset:0\n"
    "ds_read_b64_tr_b16 v[158:159], v79 offset:1024\n"
    "global_load_lds_dwordx4 v52, s[38:39] offset:3072 sc1\n"
    "ds_read_b128 v[160:163], v57 offset:0\n"
    "ds_read_b128 v[164:167], v57 offset:2048\n"
    "ds_read_b128 v[168:171], v57 offset:4096\n"
    "ds_read_b128 v[172:175], v57 offset:6144\n"
    "s_add_u32 m0, s42, 102400\n"
    "s_nop 0\n"
    "global_load_lds_dwordx4 v53, s[38:39] offset:0 sc1\n"
    "ds_read_b64_tr_b16 v[176:177], v76 offset:8192\n"
    "ds_read_b64_tr_b16 v[178:179], v76 offset:9216\n"
    "ds_read_b64_tr_b16 v[180:181], v77 offset:8192\n"
    "ds_read_b64_tr_b16 v[182:183], v77 offset:9216\n"
    "global_load_lds_dwordx4 v53, s[38:39] offset:256 sc1\n"
    "ds_read_b64_tr_b16 v[184:185], v78 offset:8192\n"
    "ds_read_b64_tr_b16 v[186:187], v78 offset:9216\n"
    "ds_read_b64_tr_b16 v[188:189], v79 offset:8192\n"
    "ds_read_b64_tr_b16 v[190:191], v79 offset:9216\n"
    "s_waitcnt vmcnt(10) lgkmcnt(0)\n"
    "s_barrier\n"
    "v_mfma_f32_16x16x32_bf16 v[192:195], v[144:147], v[128:131], v[192:195]\n"
    "v_pk_maximum3_f16 v12, v35, v35, s50 op_sel_hi:[1,1,0] neg_lo:[0,1,0] neg_hi:[0,1,0]\n"
    "v_mfma_f32_16x16x32_bf16 v[192:195], v[176:179], v[160:163], v[192:195]\n"
    "v_pk_mad_u16 v12, v12, 8, s51 op_sel_hi:[1,0,0]\n"
    "v_mfma_f32_16x16x32_bf16 v[196:199], v[176:179], v[164:167], v[196:199]\n"
    "v_mfma_f32_16x16x32_bf16 v[196:199], v[144:147], v[132:135], v[196:199]\n"
    "v_bfi_b32 v35, s57, v12, v35\n"
    "v_mfma_f32_16x16x32_bf16 v[200:203], v[144:147], v[136:139], v[200:203]\n"
    "v_pk_maximum3_f16 v21, v36, v36, s50 op_sel_hi:[1,1,0] neg_lo:[0,1,0] neg_hi:[0,1,0]\n"
    "v_mfma_f32_16x16x32_bf16 v[200:203], v[176:179], v[168:171], v[200:203]\n"
    "v_mfma_f32_16x16x32_bf16 v[204:207], v[176:179], v[172:175], v[204:207]\n"
    "v_pk_mad_u16 v21, v21, 8, s51 op_sel_hi:[1,0,0]\n"
    "v_mfma_f32_16x16x32_bf16 v[204:207], v[144:147], v[140:143], v[204:207]\n"
    "v_bfi_b32 v36, s57, v21, v36\n"
    "v_mfma_f32_16x16x32_bf16 v[220:223], v[148:151], v[140:143], v[220:223]\n"
    "v_mfma_f32_16x16x32_bf16 v[220:223], v[180:183], v[172:175], v[220:223]\n"
    "v_pk_maximum3_f16 v12, v37, v37, s50 op_sel_hi:[1,1,0] neg_lo:[0,1,0] neg_hi:[0,1,0]\n"
    "v_mfma_f32_16x16x32_bf16 v[216:219], v[180:183], v[168:171], v[216:219]\n"
    "v_pk_mad_u16 v12, v12, 8, s51 op_sel_hi:[1,0,0]\n"
    "v_mfma_f32_16x16x32_bf16 v[216:219], v[148:151], v[136:139], v[216:219]\n"
    "v_mfma_f32_16x16x32_bf16 v[212:215], v[148:151], v[132:135], v[212:215]\n"
    "v_bfi_b32 v37, s57, v12, v37\n"
    "v_mfma_f32_16x16x32_bf16 v[212:215], v[180:183], v[164:167], v[212:215]\n"
    "v_pk_maximum3_f16 v21, v38, v38, s50 op_sel_hi:[1,1,0] neg_lo:[0,1,0] neg_hi:[0,1,0]\n"
    "v_mfma_f32_16x16x32_bf16 v[208:211], v[180:183], v[160:163], v[208:211]\n"
    "v_mfma_f32_16x16x32_bf16 v[208:211], v[148:151], v[128:131], v[208:211]\n"
    "v_pk_mad_u16 v21, v21, 8, s51 op_sel_hi:[1,0,0]\n"
    "v_mfma_f32_16x16x32_bf16 v[224:227], v[152:155], v[128:131], v[224:227]\n"
    "v_mfma_f32_16x16x32_bf16 v[224:227], v[184:187], v[160:163], v[224:227]\n"
    "v_bfi_b32 v38, s57, v21, v38\n"
    "v_mfma_f32_16x16x32_bf16 v[228:231], v[184:187], v[164:167], v[228:231]\n"
    "v_pk_maximum3_f16 v12, v39, v39, s50 op_sel_hi:[1,1,0] neg_lo:[0,1,0] neg_hi:[0,1,0]\n"
    "v_mfma_f32_16x16x32_bf16 v[228:231], v[152:155], v[132:135], v[228:231]\n"
    "v_mfma_f32_16x16x32_bf16 v[232:235], v[152:155], v[136:139], v[232:235]\n"
    "v_pk_mad_u16 v12, v12, 8, s51 op_sel_hi:[1,0,0]\n"
    "v_mfma_f32_16x16x32_bf16 v[232:235], v[184:187], v[168:171], v[232:235]\n"
    "v_bfi_b32 v39, s57, v12, v39\n"
    "v_mfma_f32_16x16x32_bf16 v[236:239], v[184:187], v[172:175], v[236:239]\n"
    "v_mfma_f32_16x16x32_bf16 v[236:239], v[152:155], v[140:143], v[236:239]\n"
    "v_pk_maximum3_f16 v21, v40, v40, s50 op_sel_hi:[1,1,0] neg_lo:[0,1,0] neg_hi:[0,1,0]\n"
    "v_mfma_f32_16x16x32_bf16 v[252:255], v[156:159], v[140:143], v[252:255]\n"
    "v_pk_mad_u16 v21, v21, 8, s51 op_sel_hi:[1,0,0]\n"
    "v_mfma_f32_16x16x32_bf16 v[252:255], v[188:191], v[172:175], v[252:255]\n"
    "v_mfma_f32_16x16x32_bf16 v[248:251], v[188:191], v[168:171], v[248:251]\n"
    "v_bfi_b32 v40, s57, v21, v40\n"
    "v_mfma_f32_16x16x32_bf16 v[248:251], v[156:159], v[136:139], v[248:251]\n"
    "s_sub_u32 s66, 4112, s47\n"
    "s_lshl_b32 s66, s66, 19\n"
    "v_cvt_pkrtz_f16_f32 v12, s66, s66\n"
    "v_mfma_f32_16x16x32_bf16 v[244:247], v[156:159], v[132:135], v[244:247]\n"
    "v_mfma_f32_16x16x32_bf16 v[244:247], v[188:191], v[164:167], v[244:247]\n"
    "v_lshl_or_b32 v21, v12, 16, s47\n"
    "v_mfma_f32_16x16x32_bf16 v[240:243], v[188:191], v[160:163], v[240:243]\n"
    "v_mfma_f32_16x16x32_bf16 v[240:243], v[156:159], v[128:131], v[240:243]\n"
    "s_waitcnt vmcnt(6)\n"
    "s_barrier\n"
    "ds_read_b128 v[128:131], v56 offset:49152\n"
    "ds_read_b128 v[132:135], v56 offset:51200\n"
    "ds_read_b128 v[136:139], v56 offset:53248\n"
    "ds_read_b128 v[140:143], v56 offset:55296\n"
    "ds_read_b64_tr_b16 v[144:145], v76 offset:49152\n"
    "ds_read_b64_tr_b16 v[146:147], v76 offset:50176\n"
    "ds_read_b64_tr_b16 v[148:149], v77 offset:49152\n"
    "ds_read_b64_tr_b16 v[150:151], v77 offset:50176\n"
    "ds_read_b64_tr_b16 v[152:153], v78 offset:49152\n"
    "ds_read_b64_tr_b16 v[154:155], v78 offset:50176\n"
    "ds_read_b64_tr_b16 v[156:157], v79 offset:49152\n"
    "ds_read_b64_tr_b16 v[158:159], v79 offset:50176\n"
    "ds_read_b128 v[160:163], v57 offset:49152\n"
    "ds_read_b128 v[164:167], v57 offset:51200\n"
    "ds_read_b128 v[168:171], v57 offset:53248\n"
    "ds_read_b128 v[172:175], v57 offset:55296\n"
    "ds_read_b64_tr_b16 v[176:177], v76 offset:57344\n"
    "ds_read_b64_tr_b16 v[178:179], v76 offset:58368\n"
    "ds_read_b64_tr_b16 v[180:181], v77 offset:57344\n"
    "ds_read_b64_tr_b16 v[182:183], v77 offset:58368\n"
    "ds_read_b64_tr_b16 v[184:185], v78 offset:57344\n"
    "ds_read_b64_tr_b16 v[186:187], v78 offset:58368\n"
    "ds_read_b64_tr_b16 v[188:189], v79 offset:57344\n"
    "ds_read_b64_tr_b16 v[190:191], v79 offset:58368\n"
    "s_cmp_lg_u32 s20, 3\n"
    "s_cbranch_scc1 .Lxd_on4\n"
    "s_mov_b64 exec, 1\n"
    "ds_write_b64 v8, v[26:27]\n"
    "s_mov_b64 exec, 15\n"
    "ds_write2_b32 v8, v22, v13 offset0:4 offset1:8\n"
    "s_mov_b64 exec, -1\n"
    ".Lxd_on4:\n"
    "s_waitcnt vmcnt(0) lgkmcnt(0)\n"
    "s_cmp_lg_u32 s20, 3\n"
    "s_cbranch_scc1 .Lxd_on5\n"
    "s_mov_b64 exec, 0x200\n"
    "global_store_dword v11, v43, s[16:17] sc1\n"
    "s_mov_b64 exec, -1\n"
    ".Lxd_on5:\n"
    "s_barrier\n"
    "s_setprio 3\n"
    "v_mfma_f32_16x16x32_bf16 v[192:195], v[144:147], v[128:131], v[192:195]\n"
    "v_add_u32 v42, 98304, v52\n"
    "v_mfma_f32_16x16x32_bf16 v[192:195], v[176:179], v[160:163], v[192:195]\n"
    "v_lshl_add_u32 v11, v1, 2, s3\n"
    "v_mfma_f32_16x16x32_bf16 v[196:199], v[176:179], v[164:167], v[196:199]\n"
    "v_mov_b32 v13, s58\n"
    "v_mfma_f32_16x16x32_bf16 v[196:199], v[144:147], v[132:135], v[196:199]\n"
    "s_mov_b32 s36, 0\n"
    "v_mfma_f32_16x16x32_bf16 v[200:203], v[144:147], v[136:139], v[200:203]\n"
    "v_mov_b32 v84, s65\n"
    "v_mfma_f32_16x16x32_bf16 v[200:203], v[176:179], v[168:171], v[200:203]\n"
    "v_lshl_add_u32 v7, s23, 12, v3\n"
    "v_mfma_f32_16x16x32_bf16 v[204:207], v[176:179], v[172:175], v[204:207]\n"
    "v_lshl_add_u32 v8, s23, 12, v4\n"
    "v_mfma_f32_16x16x32_bf16 v[204:207], v[144:147], v[140:143], v[204:207]\n"
    "v_lshl_add_u32 v9, s23, 12, v5\n"
    "v_mfma_f32_16x16x32_bf16 v[220:223], v[148:151], v[140:143], v[220:223]\n"
    "v_lshl_add_u32 v10, s23, 12, v6\n"
    "v_mfma_f32_16x16x32_bf16 v[220:223], v[180:183], v[172:175], v[220:223]\n"
    "v_mfma_f32_16x16x32_bf16 v[216:219], v[180:183], v[168:171], v[216:219]\n"
    "v_mul_f32 v76, v192, v192\n"
    "v_mul_f32 v77, v193, v193\n"
    "v_mfma_f32_16x16x32_bf16 v[216:219], v[148:151], v[136:139], v[216:219]\n"
    "v_mul_f32 v78, v196, v196\n"
    "v_mul_f32 v79, v197, v197\n"
    "v_mfma_f32_16x16x32_bf16 v[212:215], v[148:151], v[132:135], v[212:215]\n"
    "v_mul_f32 v80, v200, v200\n"
    "v_mul_f32 v81, v201, v201\n"
    "v_mfma_f32_16x16x32_bf16 v[212:215], v[180:183], v[164:167], v[212:215]\n"
    "v_mul_f32 v82, v204, v204\n"
    "v_mul_f32 v83, v205, v205\n"
    "v_mfma_f32_16x16x32_bf16 v[208:211], v[180:183], v[160:163], v[208:211]\n"
    "v_fma_f32 v76, v76, s64, v84\n"
    "v_fma_f32 v77, v77, s64, v84\n"
    "v_mfma_f32_16x16x32_bf16 v[208:211], v[148:151], v[128:131], v[208:211]\n"
    "v_fma_f32 v78, v78, s64, v84\n"
    "v_fma_f32 v79, v79, s64, v84\n"
    "v_mfma_f32_16x16x32_bf16 v[224:227], v[152:155], v[128:131], v[224:227]\n"
    "v_fma_f32 v80, v80, s64, v84\n"
    "v_fma_f32 v81, v81, s64, v84\n"
    "v_mfma_f32_16x16x32_bf16 v[224:227], v[184:187], v[160:163], v[224:227]\n"
    "v_fma_f32 v82, v82, s64, v84\n"
    "v_fma_f32 v83, v83, s64, v84\n"
    "v_mfma_f32_16x16x32_bf16 v[228:231], v[184:187], v[164:167], v[228:231]\n"
    "v_mul_f32 v76, v76, v192\n"
    "v_mul_f32 v77, v77, v193\n"
    "v_mfma_f32_16x16x32_bf16 v[228:231], v[152:155], v[132:135], v[228:231]\n"
    "v_mul_f32 v78, v78, v196\n"
    "v_mul_f32 v79, v79, v197\n"
    "v_mfma_f32_16x16x32_bf16 v[232:235], v[152:155], v[136:139], v[232:235]\n"
    "v_mul_f32 v80, v80, v200\n"
    "v_mul_f32 v81, v81, v201\n"
    "v_mfma_f32_16x16x32_bf16 v[232:235], v[184:187], v[168:171], v[232:235]\n"
    "v_mul_f32 v82, v82, v204\n"
    "v_mul_f32 v83, v83, v205\n"
    "v_mfma_f32_16x16x32_bf16 v[236:239], v[184:187], v[172:175], v[236:239]\n"
    "v_exp_f32 v76, v76\n"
    "v_mfma_f32_16x16x32_bf16 v[236:239], v[152:155], v[140:143], v[236:239]\n"
    "v_exp_f32 v77, v77\n"
    "v_mfma_f32_16x16x32_bf16 v[252:255], v[156:159], v[140:143], v[252:255]\n"
    "v_exp_f32 v78, v78\n"
    "v_mfma_f32_16x16x32_bf16 v[252:255], v[188:191], v[172:175], v[252:255]\n"
    "v_exp_f32 v79, v79\n"
    "v_mfma_f32_16x16x32_bf16 v[248:251], v[188:191], v[168:171], v[248:251]\n"
    "v_exp_f32 v80, v80\n"
    "v_mfma_f32_16x16x32_bf16 v[248:251], v[156:159], v[136:139], v[248:251]\n"
    "v_exp_f32 v81, v81\n"
    "v_mfma_f32_16x16x32_bf16 v[244:247], v[156:159], v[132:135], v[244:247]\n"
    "v_exp_f32 v82, v82\n"
    "v_mfma_f32_16x16x32_bf16 v[244:247], v[188:191], v[164:167], v[244:247]\n"
    "v_exp_f32 v83, v83\n"
    "v_mfma_f32_16x16x32_bf16 v[240:243], v[188:191], v[160:163], v[240:243]\n"
    "v_mfma_f32_16x16x32_bf16 v[240:243], v[156:159], v[128:131], v[240:243]\n"
    "s_cmp_lg_u32 s21, 0\n"
    "s_cbranch_scc1 .Lxd_g1\n"
    "s_barrier\n"
    ".Lxd_g1:\n"
    "s_setprio 2\n"
    "s_cmp_eq_u32 s22, 3\n"
    "s_cbranch_scc1 .Lxd_np\n"
    "ds_write_b32 v25, v24 offset:20\n"
    "ds_write_b128 v15, v[28:31] offset:0\n"
    "ds_write_b128 v15, v[32:35] offset:144\n"
    "ds_write_b128 v15, v[36:39] offset:288\n"
    "ds_write2_b32 v16, v40, v12 offset1:72\n"
    "ds_or_b32 v25, v21 offset:532\n"
    ".Lxd_np:\n"
    "ds_read2_b32 v[26:27], v41 offset1:1\n"
    "ds_read_b32 v0, v41 offset:16\n"
    "ds_read_b32 v23, v41 offset:24\n"
    "s_load_dword s26, s[68:69], 0\n"
    "v_add_u32 v25, s24, v25\n"
    "ds_read_b128 v[92:95], v42 offset:0\n"
    "ds_read_b128 v[96:99], v42 offset:1024\n"
    "ds_read_b128 v[100:103], v42 offset:2048\n"
    "ds_read_b128 v[104:107], v42 offset:3072\n"
    "ds_read_b128 v[108:111], v42 offset:4096\n"
    "ds_read_b128 v[112:115], v42 offset:5120\n"
    "ds_read_b128 v[116:119], v42 offset:6144\n"
    "ds_read_b128 v[120:123], v42 offset:7168\n"
    "ds_read_b128 v[124:127], v42 offset:8192\n"
    "v_pk_fma_f32 v[76:77], v[76:77], v[60:61], v[60:61]\n"
    "v_pk_fma_f32 v[78:79], v[78:79], v[60:61], v[60:61]\n"
    "v_pk_fma_f32 v[80:81], v[80:81], v[60:61], v[60:61]\n"
    "v_pk_fma_f32 v[82:83], v[82:83], v[60:61], v[60:61]\n"
    "v_rcp_f32 v76, v76\n"
    "v_rcp_f32 v77, v77\n"
    "v_rcp_f32 v78, v78\n"
    "v_rcp_f32 v79, v79\n"
    "v_rcp_f32 v80, v80\n"
    "v_rcp_f32 v81, v81\n"
    "v_rcp_f32 v82, v82\n"
    "v_rcp_f32 v83, v83\n"
    "v_pk_mul_f32 v[84:85], v[192:193], v[76:77]\n"
    "v_pk_mul_f32 v[86:87], v[196:197], v[78:79]\n"
    "v_pk_mul_f32 v[88:89], v[200:201], v[80:81]\n"
    "v_pk_mul_f32 v[90:91], v[204:205], v[82:83]\n"
    "s_cmp_eq_u32 s22, 3\n"
    "s_cbranch_scc1 .Lxd_on6\n"
    ".Lxd_on6r:\n"
    "s_setprio 3\n"
    "v_pk_mul_f32 v[76:77], v[194:195], v[194:195]\n"
    "v_pk_mul_f32 v[78:79], v[198:199], v[198:199]\n"
    "v_pk_mul_f32 v[80:81], v[202:203], v[202:203]\n"
    "v_pk_mul_f32 v[82:83], v[206:207], v[206:207]\n"
    "v_pk_fma_f32 v[76:77], v[76:77], s[64:65], s[64:65] op_sel:[0,0,1] op_sel_hi:[1,0,1]\n"
    "v_pk_fma_f32 v[78:79], v[78:79], s[64:65], s[64:65] op_sel:[0,0,1] op_sel_hi:[1,0,1]\n"
    "v_pk_fma_f32 v[80:81], v[80:81], s[64:65], s[64:65] op_sel:[0,0,1] op_sel_hi:[1,0,1]\n"
    "v_pk_fma_f32 v[82:83], v[82:83], s[64:65], s[64:65] op_sel:[0,0,1] op_sel_hi:[1,0,1]\n"
    "v_pk_mul_f32 v[76:77], v[76:77], v[194:195]\n"
    "v_pk_mul_f32 v[78:79], v[78:79], v[198:199]\n"
    "v_pk_mul_f32 v[80:81], v[80:81], v[202:203]\n"
    "v_pk_mul_f32 v[82:83], v[82:83], v[206:207]\n"
    "v_exp_f32 v76, v76\n"
    "v_exp_f32 v77, v77\n"
    "v_exp_f32 v78, v78\n"
    "v_exp_f32 v79, v79\n"
    "v_exp_f32 v80, v80\n"
    "v_exp_f32 v81, v81\n"
    "v_exp_f32 v82, v82\n"
    "v_exp_f32 v83, v83\n"
    "v_pk_fma_f32 v[76:77], v[76:77], v[62:63], v[62:63]\n"
    "v_pk_fma_f32 v[78:79], v[78:79], v[62:63], v[62:63]\n"
    "v_pk_fma_f32 v[80:81], v[80:81], v[62:63], v[62:63]\n"
    "v_pk_fma_f32 v[82:83], v[82:83], v[62:63], v[62:63]\n"
    "v_rcp_f32 v76, v76\n"
    "v_rcp_f32 v77, v77\n"
    "v_rcp_f32 v78, v78\n"
    "v_rcp_f32 v79, v79\n"
    "v_rcp_f32 v80, v80\n"
    "v_rcp_f32 v81, v81\n"
    "v_rcp_f32 v82, v82\n"
    "v_rcp_f32 v83, v83\n"
    "v_pk_fma_f32 v[84:85], v[194:195], v[76:77], v[84:85]\n"
    "v_pk_fma_f32 v[86:87], v[198:199], v[78:79], v[86:87]\n"
    "v_pk_fma_f32 v[88:89], v[202:203], v[80:81], v[88:89]\n"
    "v_pk_fma_f32 v[90:91], v[206:207], v[82:83], v[90:91]\n"
    "ds_read_b32 v22, v13 offset:512\n"
    "v_pk_mul_f32 v[76:77], v[208:209], v[208:209]\n"
    "v_pk_mul_f32 v[78:79], v[212:213], v[212:213]\n"
    "v_pk_mul_f32 v[80:81], v[216:217], v[216:217]\n"
    "v_pk_mul_f32 v[82:83], v[220:221], v[220:221]\n"
    "v_pk_fma_f32 v[76:77], v[76:77], s[64:65], s[64:65] op_sel:[0,0,1] op_sel_hi:[1,0,1]\n"
    "v_pk_fma_f32 v[78:79], v[78:79], s[64:65], s[64:65] op_sel:[0,0,1] op_sel_hi:[1,0,1]\n"
    "v_pk_fma_f32 v[80:81], v[80:81], s[64:65], s[64:65] op_sel:[0,0,1] op_sel_hi:[1,0,1]\n"
    "v_pk_fma_f32 v[82:83], v[82:83], s[64:65], s[64:65] op_sel:[0,0,1] op_sel_hi:[1,0,1]\n"
    "v_pk_mul_f32 v[76:77], v[76:77], v[208:209]\n"
    "v_pk_mul_f32 v[78:79], v[78:79], v[212:213]\n"
    "v_pk_mul_f32 v[80:81], v[80:81], v[216:217]\n"
    "v_pk_mul_f32 v[82:83], v[82:83], v[220:221]\n"
    "v_exp_f32 v76, v76\n"
    "v_exp_f32 v77, v77\n"
    "ds_read_b128 v[128:131], v42 offset:9216\n"
    "v_exp_f32 v78, v78\n"
    "v_exp_f32 v79, v79\n"
    "v_exp_f32 v80, v80\n"
    "ds_read_b128 v[132:135], v42 offset:10240\n"
    "v_exp_f32 v81, v81\n"
    "v_exp_f32 v82, v82\n"
    "v_exp_f32 v83, v83\n"
    "ds_read_b128 v[136:139], v42 offset:11264\n"
    "v_pk_fma_f32 v[76:77], v[76:77], v[64:65], v[64:65]\n"
    "v_pk_fma_f32 v[78:79], v[78:79], v[64:65], v[64:65]\n"
    "v_pk_fma_f32 v[80:81], v[80:81], v[64:65], v[64:65]\n"
    "v_pk_fma_f32 v[82:83], v[82:83], v[64:65], v[64:65]\n"
    "v_rcp_f32 v76, v76\n"
    "v_rcp_f32 v77, v77\n"
    "ds_read_b128 v[140:143], v42 offset:12288\n"
    "v_rcp_f32 v78, v78\n"
    "v_rcp_f32 v79, v79\n"
    "v_rcp_f32 v80, v80\n"
    "ds_read_b128 v[144:147], v42 offset:13312\n"
    "v_rcp_f32 v81, v81\n"
    "v_rcp_f32 v82, v82\n"
    "v_rcp_f32 v83, v83\n"
    "ds_read_b128 v[148:151], v42 offset:14336\n"
    "v_pk_fma_f32 v[84:85], v[208:209], v[76:77], v[84:85]\n"
    "v_pk_fma_f32 v[86:87], v[212:213], v[78:79], v[86:87]\n"
    "v_pk_fma_f32 v[88:89], v[216:217], v[80:81], v[88:89]\n"
    "v_pk_fma_f32 v[90:91], v[220:221], v[82:83], v[90:91]\n"
    "s_waitcnt lgkmcnt(6)\n"
    "v_readfirstlane_b32 s45, v22\n"
    "s_cmp_lt_i32 s45, 16\n"
    "s_cbranch_scc1 .Lxd_ck7\n"
    ".Lxd_ck7r:\n"
    "ds_read_b64_tr_b16 v[44:45], v9 offset:0\n"
    "ds_read_b64_tr_b16 v[46:47], v10 offset:0\n"
    "ds_read_b64_tr_b16 v[48:49], v7 offset:0\n"
    "ds_read_b64_tr_b16 v[50:51], v8 offset:576\n"
    "ds_read_b64_tr_b16 v[52:53], v8 offset:1152\n"
    "ds_read_b64_tr_b16 v[54:55], v8 offset:1728\n"
    "ds_read_b64_tr_b16 v[56:57], v8 offset:2304\n"
    "ds_read_b64_tr_b16 v[58:59], v8 offset:2880\n"
    "v_readfirstlane_b32 s28, v26\n"
    "v_readfirstlane_b32 s29, v27\n"
    "s_setprio 2\n"
    "v_pk_mul_f32 v[76:77], v[210:211], v[210:211]\n"
    "v_pk_mul_f32 v[78:79], v[214:215], v[214:215]\n"
    "v_pk_mul_f32 v[80:81], v[218:219], v[218:219]\n"
    "v_pk_mul_f32 v[82:83], v[222:223], v[222:223]\n"
    "v_pk_fma_f32 v[76:77], v[76:77], s[64:65], s[64:65] op_sel:[0,0,1] op_sel_hi:[1,0,1]\n"
    "v_pk_fma_f32 v[78:79], v[78:79], s[64:65], s[64:65] op_sel:[0,0,1] op_sel_hi:[1,0,1]\n"
    "v_pk_fma_f32 v[80:81], v[80:81], s[64:65], s[64:65] op_sel:[0,0,1] op_sel_hi:[1,0,1]\n"
    "v_pk_fma_f32 v[82:83], v[82:83], s[64:65], s[64:65] op_sel:[0,0,1] op_sel_hi:[1,0,1]\n"
    "v_pk_mul_f32 v[76:77], v[76:77], v[210:211]\n"
    "v_pk_mul_f32 v[78:79], v[78:79], v[214:215]\n"
    "v_pk_mul_f32 v[80:81], v[80:81], v[218:219]\n"
    "v_pk_mul_f32 v[82:83], v[82:83], v[222:223]\n"
    "v_exp_f32 v76, v76\n"
    "v_exp_f32 v77, v77\n"
    "ds_read_b128 v[152:155], v42 offset:15360\n"
    "v_exp_f32 v78, v78\n"
    "v_exp_f32 v79, v79\n"
    "v_exp_f32 v80, v80\n"
    "ds_read_b128 v[156:159], v42 offset:16384\n"
    "v_exp_f32 v81, v81\n"
    "v_exp_f32 v82, v82\n"
    "v_exp_f32 v83, v83\n"
    "ds_read_b128 v[160:163], v42 offset:17408\n"
    "v_pk_fma_f32 v[76:77], v[76:77], v[66:67], v[66:67]\n"
    "v_pk_fma_f32 v[78:79], v[78:79], v[66:67], v[66:67]\n"
    "v_pk_fma_f32 v[80:81], v[80:81], v[66:67], v[66:67]\n"
    "v_pk_fma_f32 v[82:83], v[82:83], v[66:67], v[66:67]\n"
    "v_rcp_f32 v76, v76\n"
    "v_rcp_f32 v77, v77\n"
    "ds_read_b128 v[164:167], v42 offset:18432\n"
    "v_rcp_f32 v78, v78\n"
    "v_rcp_f32 v79, v79\n"
    "v_rcp_f32 v80, v80\n"
    "ds_read_b128 v[168:171], v42 offset:19456\n"
    "v_rcp_f32 v81, v81\n"
    "v_rcp_f32 v82, v82\n"
    "v_rcp_f32 v83, v83\n"
    "ds_read_b128 v[172:175], v42 offset:20480\n"
    "v_pk_fma_f32 v[84:85], v[210:211], v[76:77], v[84:85]\n"
    "v_pk_fma_f32 v[86:87], v[214:215], v[78:79], v[86:87]\n"
    "v_pk_fma_f32 v[88:89], v[218:219], v[80:81], v[88:89]\n"
    "v_pk_fma_f32 v[90:91], v[222:223], v[82:83], v[90:91]\n"
    "s_cmp_eq_u32 s22, 3\n"
    "s_cbranch_scc1 .Lxd_on9\n"
    ".Lxd_on9r:\n"
    "s_cmp_eq_u32 s22, 0\n"
    "s_cbranch_scc1 .Lxd_on10\n"
    ".Lxd_on10r:\n"
    "v_pk_mul_f32 v[76:77], v[224:225], v[224:225]\n"
    "v_pk_mul_f32 v[78:79], v[228:229], v[228:229]\n"
    "v_pk_mul_f32 v[80:81], v[232:233], v[232:233]\n"
    "v_pk_mul_f32 v[82:83], v[236:237], v[236:237]\n"
    "v_pk_fma_f32 v[76:77], v[76:77], s[64:65], s[64:65] op_sel:[0,0,1] op_sel_hi:[1,0,1]\n"
    "v_pk_fma_f32 v[78:79], v[78:79], s[64:65], s[64:65] op_sel:[0,0,1] op_sel_hi:[1,0,1]\n"
    "v_pk_fma_f32 v[80:81], v[80:81], s[64:65], s[64:65] op_sel:[0,0,1] op_sel_hi:[1,0,1]\n"
    "v_pk_fma_f32 v[82:83], v[82:83], s[64:65], s[64:65] op_sel:[0,0,1] op_sel_hi:[1,0,1]\n"
    "v_pk_mul_f32 v[76:77], v[76:77], v[224:225]\n"
    "v_pk_mul_f32 v[78:79], v[78:79], v[228:229]\n"
    "v_pk_mul_f32 v[80:81], v[80:81], v[232:233]\n"
    "v_pk_mul_f32 v[82:83], v[82:83], v[236:237]\n"
    "v_exp_f32 v76, v76\n"
    "v_exp_f32 v77, v77\n"
    "ds_read_b128 v[176:179], v42 offset:21504\n"
    "v_exp_f32 v78, v78\n"
    "v_exp_f32 v79, v79\n"
    "v_exp_f32 v80, v80\n"
    "ds_read_b128 v[180:183], v42 offset:22528\n"
    "v_exp_f32 v81, v81\n"
    "v_exp_f32 v82, v82\n"
    "v_exp_f32 v83, v83\n"
    "ds_read_b128 v[184:187], v42 offset:23552\n"
    "v_pk_fma_f32 v[76:77], v[76:77], v[68:69], v[68:69]\n"
    "v_pk_fma_f32 v[78:79], v[78:79], v[68:69], v[68:69]\n"
    "v_pk_fma_f32 v[80:81], v[80:81], v[68:69], v[68:69]\n"
    "v_pk_fma_f32 v[82:83], v[82:83], v[68:69], v[68:69]\n"
    "v_rcp_f32 v76, v76\n"
    "v_rcp_f32 v77, v77\n"
    "ds_read_b128 v[188:191], v42 offset:24576\n"
    "v_rcp_f32 v78, v78\n"
    "v_rcp_f32 v79, v79\n"
    "v_rcp_f32 v80, v80\n"
    "ds_read_b128 v[192:195], v42 offset:25600\n"
    "v_rcp_f32 v81, v81\n"
    "v_rcp_f32 v82, v82\n"
    "v_rcp_f32 v83, v83\n"
    "ds_read_b128 v[196:199], v42 offset:26624\n"
    "v_pk_fma_f32 v[84:85], v[224:225], v[76:77], v[84:85]\n"
    "v_pk_fma_f32 v[86:87], v[228:229], v[78:79], v[86:87]\n"
    "v_pk_fma_f32 v[88:89], v[232:233], v[80:81], v[88:89]\n"
    "v_pk_fma_f32 v[90:91], v[236:237], v[82:83], v[90:91]\n"
    "s_setprio 1\n"
    "v_pk_mul_f32 v[76:77], v[226:227], v[226:227]\n"
    "v_pk_mul_f32 v[78:79], v[230:231], v[230:231]\n"
    "v_pk_mul_f32 v[80:81], v[234:235], v[234:235]\n"
    "v_pk_mul_f32 v[82:83], v[238:239], v[238:239]\n"
    "v_pk_fma_f32 v[76:77], v[76:77], s[64:65], s[64:65] op_sel:[0,0,1] op_sel_hi:[1,0,1]\n"
    "v_pk_fma_f32 v[78:79], v[78:79], s[64:65], s[64:65] op_sel:[0,0,1] op_sel_hi:[1,0,1]\n"
    "v_pk_fma_f32 v[80:81], v[80:81], s[64:65], s[64:65] op_sel:[0,0,1] op_sel_hi:[1,0,1]\n"
    "v_pk_fma_f32 v[82:83], v[82:83], s[64:65], s[64:65] op_sel:[0,0,1] op_sel_hi:[1,0,1]\n"
    "v_pk_mul_f32 v[76:77], v[76:77], v[226:227]\n"
    "v_pk_mul_f32 v[78:79], v[78:79], v[230:231]\n"
    "v_pk_mul_f32 v[80:81], v[80:81], v[234:235]\n"
    "v_pk_mul_f32 v[82:83], v[82:83], v[238:239]\n"
    "v_exp_f32 v76, v76\n"
    "v_exp_f32 v77, v77\n"
    "ds_read_b128 v[200:203], v42 offset:27648\n"
    "v_exp_f32 v78, v78\n"
    "v_exp_f32 v79, v79\n"
    "v_exp_f32 v80, v80\n"
    "ds_read_b128 v[204:207], v42 offset:28672\n"
    "v_exp_f32 v81, v81\n"
    "v_exp_f32 v82, v82\n"
    "v_exp_f32 v83, v83\n"
    "ds_read_b128 v[208:211], v42 offset:29696\n"
    "v_pk_fma_f32 v[76:77], v[76:77], v[70:71], v[70:71]\n"
    "v_pk_fma_f32 v[78:79], v[78:79], v[70:71], v[70:71]\n"
    "v_pk_fma_f32 v[80:81], v[80:81], v[70:71], v[70:71]\n"
    "v_pk_fma_f32 v[82:83], v[82:83], v[70:71], v[70:71]\n"
    "v_rcp_f32 v76, v76\n"
    "v_rcp_f32 v77, v77\n"
    "ds_read_b128 v[212:215], v42 offset:30720\n"
    "v_rcp_f32 v78, v78\n"
    "v_rcp_f32 v79, v79\n"
    "v_rcp_f32 v80, v80\n"
    "ds_read_b128 v[216:219], v42 offset:31744\n"
    "v_rcp_f32 v81, v81\n"
    "v_rcp_f32 v82, v82\n"
    "v_rcp_f32 v83, v83\n"
    "ds_read_b128 v[220:223], v42 offset:32768\n"
    "v_pk_fma_f32 v[84:85], v[226:227], v[76:77], v[84:85]\n"
    "v_pk_fma_f32 v[86:87], v[230:231], v[78:79], v[86:87]\n"
    "v_pk_fma_f32 v[88:89], v[234:235], v[80:81], v[88:89]\n"
    "v_pk_fma_f32 v[90:91], v[238:239], v[82:83], v[90:91]\n"
    "v_pk_mul_f32 v[76:77], v[240:241], v[240:241]\n"
    "v_pk_mul_f32 v[78:79], v[244:245], v[244:245]\n"
    "v_pk_mul_f32 v[80:81], v[248:249], v[248:249]\n"
    "v_pk_mul_f32 v[82:83], v[252:253], v[252:253]\n"
    "v_pk_fma_f32 v[76:77], v[76:77], s[64:65], s[64:65] op_sel:[0,0,1] op_sel_hi:[1,0,1]\n"
    "v_pk_fma_f32 v[78:79], v[78:79], s[64:65], s[64:65] op_sel:[0,0,1] op_sel_hi:[1,0,1]\n"
    "v_pk_fma_f32 v[80:81], v[80:81], s[64:65], s[64:65] op_sel:[0,0,1] op_sel_hi:[1,0,1]\n"
    "v_pk_fma_f32 v[82:83], v[82:83], s[64:65], s[64:65] op_sel:[0,0,1] op_sel_hi:[1,0,1]\n"
    "v_pk_mul_f32 v[76:77], v[76:77], v[240:241]\n"
    "v_pk_mul_f32 v[78:79], v[78:79], v[244:245]\n"
    "v_pk_mul_f32 v[80:81], v[80:81], v[248:249]\n"
    "v_pk_mul_f32 v[82:83], v[82:83], v[252:253]\n"
    "v_exp_f32 v76, v76\n"
    "v_exp_f32 v77, v77\n"
    "v_exp_f32 v78, v78\n"
    "v_exp_f32 v79, v79\n"
    "v_exp_f32 v80, v80\n"
    "v_exp_f32 v81, v81\n"
    "v_exp_f32 v82, v82\n"
    "v_exp_f32 v83, v83\n"
    "v_pk_fma_f32 v[76:77], v[76:77], v[72:73], v[72:73]\n"
    "v_pk_fma_f32 v[78:79], v[78:79], v[72:73], v[72:73]\n"
    "v_pk_fma_f32 v[80:81], v[80:81], v[72:73], v[72:73]\n"
    "v_pk_fma_f32 v[82:83], v[82:83], v[72:73], v[72:73]\n"
    "v_rcp_f32 v76, v76\n"
    "v_rcp_f32 v77, v77\n"
    "v_rcp_f32 v78, v78\n"
    "v_rcp_f32 v79, v79\n"
    "v_rcp_f32 v80, v80\n"
    "v_rcp_f32 v81, v81\n"
    "v_rcp_f32 v82, v82\n"
    "v_rcp_f32 v83, v83\n"
    "v_pk_fma_f32 v[84:85], v[240:241], v[76:77], v[84:85]\n"
    "v_pk_fma_f32 v[86:87], v[244:245], v[78:79], v[86:87]\n"
    "v_pk_fma_f32 v[88:89], v[248:249], v[80:81], v[88:89]\n"
    "v_pk_fma_f32 v[90:91], v[252:253], v[82:83], v[90:91]\n"
    "s_cmp_eq_u32 s22, 3\n"
    "s_cbranch_scc1 .Lxd_on11\n"
    ".Lxd_on11r:\n"
    "s_cmp_eq_u32 s22, 0\n"
    "s_cbranch_scc1 .Lxd_on13\n"
    ".Lxd_on13r:\n"
    "s_cmp_eq_u32 s22, 1\n"
    "s_cbranch_scc1 .Lxd_on14\n"
    ".Lxd_on14r:\n"
    "s_setprio 0\n"
    "v_pk_mul_f32 v[76:77], v[242:243], v[242:243]\n"
    "v_pk_mul_f32 v[78:79], v[246:247], v[246:247]\n"
    "v_pk_mul_f32 v[80:81], v[250:251], v[250:251]\n"
    "v_pk_mul_f32 v[82:83], v[254:255], v[254:255]\n"
    "v_pk_fma_f32 v[76:77], v[76:77], s[64:65], s[64:65] op_sel:[0,0,1] op_sel_hi:[1,0,1]\n"
    "v_pk_fma_f32 v[78:79], v[78:79], s[64:65], s[64:65] op_sel:[0,0,1] op_sel_hi:[1,0,1]\n"
    "v_pk_fma_f32 v[80:81], v[80:81], s[64:65], s[64:65] op_sel:[0,0,1] op_sel_hi:[1,0,1]\n"
    "v_pk_fma_f32 v[82:83], v[82:83], s[64:65], s[64:65] op_sel:[0,0,1] op_sel_hi:[1,0,1]\n"
    "v_pk_mul_f32 v[76:77], v[76:77], v[242:243]\n"
    "v_pk_mul_f32 v[78:79], v[78:79], v[246:247]\n"
    "v_pk_mul_f32 v[80:81], v[80:81], v[250:251]\n"
    "v_pk_mul_f32 v[82:83], v[82:83], v[254:255]\n"
    "v_exp_f32 v76, v76\n"
    "v_exp_f32 v77, v77\n"
    "v_exp_f32 v78, v78\n"
    "v_exp_f32 v79, v79\n"
    "v_exp_f32 v80, v80\n"
    "v_exp_f32 v81, v81\n"
    "v_exp_f32 v82, v82\n"
    "v_exp_f32 v83, v83\n"
    "v_pk_fma_f32 v[76:77], v[76:77], v[74:75], v[74:75]\n"
    "v_pk_fma_f32 v[78:79], v[78:79], v[74:75], v[74:75]\n"
    "v_pk_fma_f32 v[80:81], v[80:81], v[74:75], v[74:75]\n"
    "v_pk_fma_f32 v[82:83], v[82:83], v[74:75], v[74:75]\n"
    "v_rcp_f32 v76, v76\n"
    "v_rcp_f32 v77, v77\n"
    "v_rcp_f32 v78, v78\n"
    "v_rcp_f32 v79, v79\n"
    "v_rcp_f32 v80, v80\n"
    "v_rcp_f32 v81, v81\n"
    "v_rcp_f32 v82, v82\n"
    "v_rcp_f32 v83, v83\n"
    "v_pk_fma_f32 v[84:85], v[242:243], v[76:77], v[84:85]\n"
    "v_pk_fma_f32 v[86:87], v[246:247], v[78:79], v[86:87]\n"
    "v_pk_fma_f32 v[88:89], v[250:251], v[80:81], v[88:89]\n"
    "v_pk_fma_f32 v[90:91], v[254:255], v[82:83], v[90:91]\n"
    "s_cmp_eq_u32 s21, 0\n"
    "s_cbranch_scc1 .Lxd_gb0\n"
    "s_barrier\n"
    "s_setprio 3\n"
    ".Lxd_gb0:\n"
    "v_add_f32 v84, v84, v85\n"
    "v_add_f32 v86, v86, v87\n"
    "v_add_f32 v88, v88, v89\n"
    "v_add_f32 v90, v90, v91\n"
    "s_waitcnt lgkmcnt(15)\n"
    "v_pk_mul_f16 v60, v44, s45 op_sel:[0,1]\n"
    "v_pk_mul_f16 v61, v45, s45 op_sel:[0,1]\n"
    "v_permlane32_swap_b32 v84, v88\n"
    "v_permlane32_swap_b32 v86, v90\n"
    "v_add_f32 v84, v84, v88\n"
    "v_add_f32 v86, v86, v90\n"
    "v_pk_mul_f16 v62, v46, s45 op_sel:[0,1]\n"
    "v_pk_mul_f16 v63, v47, s45 op_sel:[0,1]\n"
    "v_permlane16_swap_b32 v84, v86\n"
    "v_add_f32 v84, v84, v86\n"
    "v_and_b32 v84, -2, v84\n"
    "ds_wrxchg_rtn_b32 v85, v11, v84\n"
    "ds_read_b128 v[224:227], v42 offset:33792\n"
    "ds_read_b128 v[228:231], v42 offset:34816\n"
    "ds_read_b128 v[232:235], v42 offset:35840\n"
    "ds_read_b128 v[236:239], v42 offset:36864\n"
    "ds_read_b128 v[240:243], v42 offset:37888\n"
    "ds_read_b128 v[244:247], v42 offset:38912\n"
    "ds_read_b128 v[248:251], v42 offset:39936\n"
    "ds_read_b128 v[252:255], v42 offset:40960\n"
    "v_pk_mul_f16 v64, v44, v46\n"
    "v_pk_mul_f16 v65, v45, v47\n"
    "v_pk_mul_f16 v66, v48, v48 op_sel_hi:[0,1]\n"
    "v_pk_mul_f16 v67, v48, v49 op_sel_hi:[0,1]\n"
    "v_lshlrev_b32 v86, 3, v1\n"
    "v_mov_b64 v[88:89], 0\n"
    "v_mov_b64 v[90:91], 0\n"
    "s_waitcnt lgkmcnt(8)\n"
    "v_cmp_ne_u32 vcc, -1, v85\n"
    "v_add_f32 v84, v84, v85\n"
    "v_add_u32 v84, 0x80, v84\n"
    "v_and_b32 v84, 0xffffff00, v84\n"
    "v_or_b32 v26, s28, v84\n"
    "v_mov_b32 v27, s29\n"
    "s_cbranch_vccz .Lxd_nost\n"
    "s_mov_b64 exec, vcc\n"
    "global_store_dwordx2 v86, v[26:27], s[48:49] sc1\n"
    "s_mov_b64 exec, -1\n"
    ".Lxd_nost:\n"
    "s_cmp_lg_u32 s21, 0\n"
    "s_cbranch_scc1 .Lxd_gb1\n"
    "s_barrier\n"
    ".Lxd_gb1:\n"
    ".Lxd_enter:\n"
    "s_cmp_eq_u32 s21, 0\n"
    "s_cselect_b64 vcc, 0, -1\n"
    "s_cbranch_scc1 .Lxd_pr\n"
    "s_setprio 1\n"
    ".Lxd_pr:\n"
    "s_cmp_lg_u32 s36, 0\n"
    "s_cbranch_scc1 .Lxd_el\n"
    "s_setpc_b64 s[10:11]\n"
    ".Lxd_el:\n"
    "s_cmp_eq_u32 s22, 0\n"
    "s_cbranch_scc1 .Lxd_pl1\n"
    "s_branch .Lxd_pl2\n"
    ".Lxd_ph0:\n"
    "v_smfmac_f32_16x16x64_f16 v[88:91], v[112:115], v[60:67], v2\n"
    "s_cbranch_vccnz .Lxd_pr15\n"
    "s_setprio 2\n"
    ".Lxd_pr15:\n"
    "v_pk_mul_f16 v68, v58, v58 op_sel_hi:[0,1]\n"
    "v_pk_mul_f16 v69, v58, v49 op_sel_hi:[0,1]\n"
    "v_mfma_f32_16x16x32_f16 v[84:87], v[108:111], v[64:67], 0\n"
    "v_fmac_f32 v11, v76, v80\n"
    "v_fmac_f32 v11, v77, v81\n"
    "v_mfma_f32_16x16x32_f16 v[84:87], v[96:99], v[60:63], v[84:87]\n"
    "v_fmac_f32 v11, v78, v82\n"
    "v_fma_f32 v10, v79, v83, v11\n"
    "v_mfma_f32_16x16x32_f16 v[80:83], v[92:95], v[60:63], 0\n"
    "s_waitcnt lgkmcnt(0)\n"
    "v_pk_mul_f16 v70, v48, v58 op_sel_hi:[0,1]\n"
    "v_pk_mul_f16 v71, v48, v59 op_sel_hi:[0,1]\n"
    "v_mfma_f32_16x16x32_f16 v[80:83], v[104:107], v[64:67], v[80:83]\n"
    "v_pk_mul_f16 v72, v50, v58 op_sel_hi:[0,1]\n"
    "v_pk_mul_f16 v73, v50, v59 op_sel_hi:[0,1]\n"
    "v_mfma_f32_16x16x32_f16 v[76:79], v[100:103], v[64:67], 0\n"
    "s_waitcnt vmcnt(0)\n"
    "v_pk_mul_f16 v74, v52, v58 op_sel_hi:[0,1]\n"
    "v_pk_mul_f16 v75, v52, v59 op_sel_hi:[0,1]\n"
    "v_mfma_f32_16x16x32_f16 v[76:79], v[116:119], v[68:71], v[76:79]\n"
    "v_pk_mul_f16 v60, v54, v58 op_sel_hi:[0,1]\n"
    "v_pk_mul_f16 v61, v54, v59 op_sel_hi:[0,1]\n"
    "v_mfma_f32_16x16x32_f16 v[76:79], v[128:131], v[72:75], v[76:79]\n"
    "v_pk_mul_f16 v62, v56, v58 op_sel_hi:[0,1]\n"
    "v_readfirstlane_b32 s40, v24\n"
    "v_mfma_f32_16x16x32_f16 v[80:83], v[132:135], v[72:75], v[80:83]\n"
    "s_or_b32 s40, s40, s44\n"
    "v_pk_mul_f16 v63, v56, v59 op_sel_hi:[0,1]\n"
    "v_permlane32_swap_b32 v7, v8\n"
    "v_mfma_f32_16x16x32_f16 v[80:83], v[120:123], v[68:71], v[80:83]\n"
    "s_lshl1_add_u32 s41, s40, s54\n"
    "v_pk_mul_f16 v64, v56, v56 op_sel_hi:[0,1]\n"
    "v_permlane32_swap_b32 v9, v10\n"
    "v_mfma_f32_16x16x32_f16 v[84:87], v[124:127], v[68:71], v[84:87]\n"
    "s_cmpk_lt_u32 s40, 256\n"
    "v_pk_mul_f16 v65, v56, v49 op_sel_hi:[0,1]\n"
    "v_mul_f32 v7, v0, v7\n"
    "v_mfma_f32_16x16x32_f16 v[84:87], v[136:139], v[72:75], v[84:87]\n"
    "s_cselect_b32 s41, s41, 2\n"
    "v_pk_mul_f16 v66, v48, v56 op_sel_hi:[0,1]\n"
    "v_mov_b32 v12, s41\n"
    "v_smfmac_f32_16x16x64_f16 v[88:91], v[140:143], v[68:75], v2\n"
    "s_cbranch_scc0 .Lxd_tf17\n"
    ".Lxd_tf17r:\n"
    "v_pk_mul_f16 v67, v48, v57 op_sel_hi:[0,1]\n"
    "v_mul_f32 v9, v0, v9\n"
    "v_mfma_f32_16x16x32_f16 v[76:79], v[144:147], v[60:63], v[76:79]\n"
    "s_mul_i32 s41, s41, 52\n"
    "v_pk_mul_f16 v68, v50, v56 op_sel_hi:[0,1]\n"
    "v_pk_mul_f16 v69, v50, v57 op_sel_hi:[0,1]\n"
    "v_mfma_f32_16x16x32_f16 v[76:79], v[156:159], v[64:67], v[76:79]\n"
    "s_add_u32 s38, s4, s41\n"
    "v_pk_mul_f16 v70, v52, v56 op_sel_hi:[0,1]\n"
    "v_fmac_f32 v7, v23, v8\n"
    "v_mfma_f32_16x16x32_f16 v[80:83], v[160:163], v[64:67], v[80:83]\n"
    "s_addc_u32 s39, s5, 0\n"
    "v_pk_mul_f16 v71, v52, v57 op_sel_hi:[0,1]\n"
    "v_fmac_f32 v9, v23, v10\n"
    "v_mfma_f32_16x16x32_f16 v[80:83], v[148:151], v[60:63], v[80:83]\n"
    "ds_read_b32 v22, v25 offset:516\n"
    "v_pk_mul_f16 v72, v54, v56 op_sel_hi:[0,1]\n"
    "v_permlane16_swap_b32 v7, v9\n"
    "v_mfma_f32_16x16x32_f16 v[84:87], v[152:155], v[60:63], v[84:87]\n"
    "global_load_dwordx3 v[36:38], v17, s[38:39] offset:-104\n"
    "v_pk_mul_f16 v73, v54, v57 op_sel_hi:[0,1]\n"
    "v_add_f32 v7, v7, v9\n"
    "v_mfma_f32_16x16x32_f16 v[84:87], v[164:167], v[64:67], v[84:87]\n"
    "global_load_dword v41, v18, s[38:39] offset:-8\n"
    "v_pk_mul_f16 v74, v54, v54 op_sel_hi:[0,1]\n"
    "v_add_f32_dpp v7, v7, v7 quad_perm:[1,0,3,2] row_mask:0xf bank_mask:0xf\n"
    "v_smfmac_f32_16x16x64_f16 v[88:91], v[168:171], v[60:67], v2\n"
    "ds_write_b32 v25, v12 offset:20\n"
    "v_pk_mul_f16 v75, v54, v49 op_sel_hi:[0,1]\n"
    "v_add_f32_dpp v7, v7, v7 quad_perm:[2,3,0,1] row_mask:0xf bank_mask:0xf\n"
    "v_mfma_f32_16x16x32_f16 v[76:79], v[172:175], v[68:71], v[76:79]\n"
    "v_pk_mul_f16 v60, v48, v54 op_sel_hi:[0,1]\n"
    "v_pk_mul_f16 v61, v48, v55 op_sel_hi:[0,1]\n"
    "v_mfma_f32_16x16x32_f16 v[76:79], v[184:187], v[72:75], v[76:79]\n"
    "s_waitcnt lgkmcnt(1)\n"
    "v_pk_mul_f16 v62, v50, v54 op_sel_hi:[0,1]\n"
    "v_readfirstlane_b32 s45, v22\n"
    "v_mfma_f32_16x16x32_f16 v[80:83], v[188:191], v[72:75], v[80:83]\n"
    "s_cmp_lt_i32 s45, 16\n"
    "v_pk_mul_f16 v63, v50, v55 op_sel_hi:[0,1]\n"
    "v_add_f32_dpp v7, v7, v7 row_half_mirror row_mask:0xf bank_mask:0xf\n"
    "v_mfma_f32_16x16x32_f16 v[80:83], v[176:179], v[68:71], v[80:83]\n"
    "s_cbranch_scc1 .Lxd_ck18\n"
    ".Lxd_ck18r:\n"
    "v_pk_mul_f16 v64, v52, v54 op_sel_hi:[0,1]\n"
    "v_add_f32_dpp v7, v7, v7 row_mirror row_mask:0xf bank_mask:0xf\n"
    "v_mfma_f32_16x16x32_f16 v[84:87], v[180:183], v[68:71], v[84:87]\n"
    "ds_read_b64_tr_b16 v[44:45], v5 offset:12288\n"
    "v_pk_mul_f16 v65, v52, v55 op_sel_hi:[0,1]\n"
    "v_mfma_f32_16x16x32_f16 v[84:87], v[192:195], v[72:75], v[84:87]\n"
    "ds_read_b64_tr_b16 v[46:47], v6 offset:12288\n"
    "v_pk_mul_f16 v66, v52, v52 op_sel_hi:[0,1]\n"
    "v_smfmac_f32_16x16x64_f16 v[88:91], v[196:199], v[68:75], v2\n"
    "ds_write_b32 v14, v7\n"
    "v_pk_mul_f16 v67, v52, v49 op_sel_hi:[0,1]\n"
    "v_add_u32 v14, 64, v14\n"
    "v_mfma_f32_16x16x32_f16 v[76:79], v[200:203], v[60:63], v[76:79]\n"
    "v_pk_mul_f16 v68, v48, v52 op_sel_hi:[0,1]\n"
    "v_pk_mul_f16 v69, v48, v53 op_sel_hi:[0,1]\n"
    "v_mfma_f32_16x16x32_f16 v[76:79], v[212:215], v[64:67], v[76:79]\n"
    "v_pk_mul_f16 v70, v50, v52 op_sel_hi:[0,1]\n"
    "v_mfma_f32_16x16x32_f16 v[80:83], v[216:219], v[64:67], v[80:83]\n"
    "s_cbranch_vccnz .Lxd_pr16\n"
    "s_setprio 0\n"
    ".Lxd_pr16:\n"
    "v_pk_mul_f16 v71, v50, v53 op_sel_hi:[0,1]\n"
    "v_mfma_f32_16x16x32_f16 v[80:83], v[204:207], v[60:63], v[80:83]\n"
    "v_pk_mul_f16 v72, v50, v50 op_sel_hi:[0,1]\n"
    "v_mfma_f32_16x16x32_f16 v[84:87], v[208:211], v[60:63], v[84:87]\n"
    "v_pk_mul_f16 v73, v50, v49 op_sel_hi:[0,1]\n"
    "v_pk_mul_f16 v74, v48, v50 op_sel_hi:[0,1]\n"
    "v_mfma_f32_16x16x32_f16 v[84:87], v[220:223], v[64:67], v[84:87]\n"
    "v_pk_mul_f16 v75, v48, v51 op_sel_hi:[0,1]\n"
    "v_smfmac_f32_16x16x64_f16 v[88:91], v[224:227], v[60:67], v2\n"
    "ds_read_b64_tr_b16 v[48:49], v3 offset:12288\n"
    "ds_read_b64_tr_b16 v[58:59], v4 offset:15168\n"
    "v_smfmac_f32_16x16x64_f16 v[88:91], v[252:255], v[68:75], v2\n"
    "s_waitcnt lgkmcnt(3)\n"
    "v_pk_mul_f16 v60, v44, s45 op_sel:[0,1]\n"
    "v_pk_mul_f16 v61, v45, s45 op_sel:[0,1]\n"
    "v_mfma_f32_16x16x32_f16 v[84:87], v[236:239], v[68:71], v[84:87]\n"
    "ds_read_b64_tr_b16 v[50:51], v4 offset:12864\n"
    "v_pk_mul_f16 v62, v46, s45 op_sel:[0,1]\n"
    "v_pk_mul_f16 v63, v47, s45 op_sel:[0,1]\n"
    "v_mfma_f32_16x16x32_f16 v[84:87], v[248:251], v[72:75], v[84:87]\n"
    "ds_read_b64_tr_b16 v[52:53], v4 offset:13440\n"
    "v_pk_mul_f16 v64, v44, v46\n"
    "v_pk_mul_f16 v65, v45, v47\n"
    "v_mfma_f32_16x16x32_f16 v[76:79], v[240:243], v[72:75], v[76:79]\n"
    "ds_read_b64_tr_b16 v[54:55], v4 offset:14016\n"
    "v_add_f32 v11, v88, v89\n"
    "v_add_f32 v13, v90, v91\n"
    "v_mfma_f32_16x16x32_f16 v[76:79], v[228:231], v[68:71], v[76:79]\n"
    "ds_read_b64_tr_b16 v[56:57], v4 offset:14592\n"
    "v_mov_b64 v[88:89], 0\n"
    "v_mov_b64 v[90:91], 0\n"
    "v_mfma_f32_16x16x32_f16 v[80:83], v[232:235], v[68:71], v[80:83]\n"
    "s_waitcnt lgkmcnt(4)\n"
    "v_pk_mul_f16 v66, v48, v48 op_sel_hi:[0,1]\n"
    "v_pk_mul_f16 v67, v48, v49 op_sel_hi:[0,1]\n"
    "v_mfma_f32_16x16x32_f16 v[80:83], v[244:247], v[72:75], v[80:83]\n"
    "s_cmp_lg_u32 s36, 0\n"
    "v_mul_f32 v11, v11, v13\n"
    "v_fmac_f32 v11, v84, v85\n"
    "v_fmac_f32 v11, v86, v87\n"
    "s_cbranch_scc1 .Lxd_pl1\n"
    ".Lxd_ph1:\n"
    "v_smfmac_f32_16x16x64_f16 v[88:91], v[112:115], v[60:67], v2\n"
    "s_cbranch_vccnz .Lxd_pr19\n"
    "s_setprio 2\n"
    ".Lxd_pr19:\n"
    "v_pk_mul_f16 v68, v58, v58 op_sel_hi:[0,1]\n"
    "v_pk_mul_f16 v69, v58, v49 op_sel_hi:[0,1]\n"
    "v_mfma_f32_16x16x32_f16 v[84:87], v[108:111], v[64:67], 0\n"
    "v_fmac_f32 v11, v76, v80\n"
    "v_fmac_f32 v11, v77, v81\n"
    "v_mfma_f32_16x16x32_f16 v[84:87], v[96:99], v[60:63], v[84:87]\n"
    "v_fmac_f32 v11, v78, v82\n"
    "v_fma_f32 v7, v79, v83, v11\n"
    "v_mfma_f32_16x16x32_f16 v[80:83], v[92:95], v[60:63], 0\n"
    "s_waitcnt lgkmcnt(0)\n"
    "v_pk_mul_f16 v70, v48, v58 op_sel_hi:[0,1]\n"
    "v_pk_mul_f16 v71, v48, v59 op_sel_hi:[0,1]\n"
    "v_mfma_f32_16x16x32_f16 v[80:83], v[104:107], v[64:67], v[80:83]\n"
    "v_pk_mul_f16 v72, v50, v58 op_sel_hi:[0,1]\n"
    "v_pk_mul_f16 v73, v50, v59 op_sel_hi:[0,1]\n"
    "v_mfma_f32_16x16x32_f16 v[76:79], v[100:103], v[64:67], 0\n"
    "v_pk_mul_f16 v74, v52, v58 op_sel_hi:[0,1]\n"
    "v_pk_mul_f16 v75, v52, v59 op_sel_hi:[0,1]\n"
    "v_mfma_f32_16x16x32_f16 v[76:79], v[116:119], v[68:71], v[76:79]\n"
    "s_waitcnt vmcnt(0)\n"
    "v_pk_mul_f16 v60, v54, v58 op_sel_hi:[0,1]\n"
    "v_pk_mul_f16 v61, v54, v59 op_sel_hi:[0,1]\n"
    "v_mfma_f32_16x16x32_f16 v[76:79], v[128:131], v[72:75], v[76:79]\n"
    "v_pk_mul_f16 v62, v56, v58 op_sel_hi:[0,1]\n"
    "v_lshl_or_b32 v36, v36, 7, v19\n"
    "v_mfma_f32_16x16x32_f16 v[80:83], v[132:135], v[72:75], v[80:83]\n"
    "global_load_dwordx4 v[28:31], v36, s[8:9]\n"
    "v_pk_mul_f16 v63, v56, v59 op_sel_hi:[0,1]\n"
    "v_lshl_or_b32 v37, v37, 7, v19\n"
    "v_mfma_f32_16x16x32_f16 v[80:83], v[120:123], v[68:71], v[80:83]\n"
    "global_load_dwordx4 v[32:35], v37, s[8:9]\n"
    "v_pk_mul_f16 v64, v56, v56 op_sel_hi:[0,1]\n"
    "v_lshl_or_b32 v41, v41, 7, v20\n"
    "v_mfma_f32_16x16x32_f16 v[84:87], v[124:127], v[68:71], v[84:87]\n"
    "global_load_dword v40, v41, s[8:9]\n"
    "v_pk_mul_f16 v65, v56, v49 op_sel_hi:[0,1]\n"
    "v_lshl_or_b32 v38, v38, 7, v19\n"
    "v_mfma_f32_16x16x32_f16 v[84:87], v[136:139], v[72:75], v[84:87]\n"
    "s_cmp_lg_u32 s46, 0\n"
    "v_pk_mul_f16 v66, v48, v56 op_sel_hi:[0,1]\n"
    "v_xor_b32 v3, 0x4000, v3\n"
    "v_smfmac_f32_16x16x64_f16 v[88:91], v[140:143], v[68:75], v2\n"
    "s_cbranch_scc1 .Lxd_ht21\n"
    ".Lxd_ht21r:\n"
    "v_pk_mul_f16 v67, v48, v57 op_sel_hi:[0,1]\n"
    "v_xor_b32 v4, 0x4000, v4\n"
    "v_mfma_f32_16x16x32_f16 v[76:79], v[144:147], v[60:63], v[76:79]\n"
    "global_load_dwordx4 v[36:39], v38, s[8:9]\n"
    "v_pk_mul_f16 v68, v50, v56 op_sel_hi:[0,1]\n"
    "v_pk_mul_f16 v69, v50, v57 op_sel_hi:[0,1]\n"
    "v_mfma_f32_16x16x32_f16 v[76:79], v[156:159], v[64:67], v[76:79]\n"
    "v_pk_mul_f16 v70, v52, v56 op_sel_hi:[0,1]\n"
    "v_xor_b32 v15, 0x4000, v15\n"
    "v_mfma_f32_16x16x32_f16 v[80:83], v[160:163], v[64:67], v[80:83]\n"
    "v_pk_mul_f16 v71, v52, v57 op_sel_hi:[0,1]\n"
    "v_xor_b32 v16, 0x4000, v16\n"
    "v_mfma_f32_16x16x32_f16 v[80:83], v[148:151], v[60:63], v[80:83]\n"
    "ds_read_b32 v22, v25 offset:520\n"
    "v_pk_mul_f16 v72, v54, v56 op_sel_hi:[0,1]\n"
    "v_xor_b32 v5, 0x4000, v5\n"
    "v_mfma_f32_16x16x32_f16 v[84:87], v[152:155], v[60:63], v[84:87]\n"
    "v_pk_mul_f16 v73, v54, v57 op_sel_hi:[0,1]\n"
    "v_xor_b32 v6, 0x4000, v6\n"
    "v_mfma_f32_16x16x32_f16 v[84:87], v[164:167], v[64:67], v[84:87]\n"
    "v_pk_mul_f16 v74, v54, v54 op_sel_hi:[0,1]\n"
    "v_smfmac_f32_16x16x64_f16 v[88:91], v[168:171], v[60:67], v2\n"
    "v_pk_mul_f16 v75, v54, v49 op_sel_hi:[0,1]\n"
    "v_mfma_f32_16x16x32_f16 v[76:79], v[172:175], v[68:71], v[76:79]\n"
    "v_pk_mul_f16 v60, v48, v54 op_sel_hi:[0,1]\n"
    "v_pk_mul_f16 v61, v48, v55 op_sel_hi:[0,1]\n"
    "v_mfma_f32_16x16x32_f16 v[76:79], v[184:187], v[72:75], v[76:79]\n"
    "s_waitcnt lgkmcnt(0)\n"
    "v_pk_mul_f16 v62, v50, v54 op_sel_hi:[0,1]\n"
    "v_readfirstlane_b32 s45, v22\n"
    "v_mfma_f32_16x16x32_f16 v[80:83], v[188:191], v[72:75], v[80:83]\n"
    "s_cmp_lt_i32 s45, 16\n"
    "v_pk_mul_f16 v63, v50, v55 op_sel_hi:[0,1]\n"
    "v_mfma_f32_16x16x32_f16 v[80:83], v[176:179], v[68:71], v[80:83]\n"
    "s_cbranch_scc1 .Lxd_ck22\n"
    ".Lxd_ck22r:\n"
    "v_pk_mul_f16 v64, v52, v54 op_sel_hi:[0,1]\n"
    "v_mfma_f32_16x16x32_f16 v[84:87], v[180:183], v[68:71], v[84:87]\n"
    "ds_read_b64_tr_b16 v[44:45], v5 offset:0\n"
    "v_pk_mul_f16 v65, v52, v55 op_sel_hi:[0,1]\n"
    "v_mfma_f32_16x16x32_f16 v[84:87], v[192:195], v[72:75], v[84:87]\n"
    "ds_read_b64_tr_b16 v[46:47], v6 offset:0\n"
    "v_pk_mul_f16 v66, v52, v52 op_sel_hi:[0,1]\n"
    "v_smfmac_f32_16x16x64_f16 v[88:91], v[196:199], v[68:75], v2\n"
    "v_pk_mul_f16 v67, v52, v49 op_sel_hi:[0,1]\n"
    "v_mfma_f32_16x16x32_f16 v[76:79], v[200:203], v[60:63], v[76:79]\n"
    "v_pk_mul_f16 v68, v48, v52 op_sel_hi:[0,1]\n"
    "v_pk_mul_f16 v69, v48, v53 op_sel_hi:[0,1]\n"
    "v_mfma_f32_16x16x32_f16 v[76:79], v[212:215], v[64:67], v[76:79]\n"
    "v_pk_mul_f16 v70, v50, v52 op_sel_hi:[0,1]\n"
    "v_mfma_f32_16x16x32_f16 v[80:83], v[216:219], v[64:67], v[80:83]\n"
    "s_cbranch_vccnz .Lxd_pr20\n"
    "s_setprio 0\n"
    ".Lxd_pr20:\n"
    "v_pk_mul_f16 v71, v50, v53 op_sel_hi:[0,1]\n"
    "v_mfma_f32_16x16x32_f16 v[80:83], v[204:207], v[60:63], v[80:83]\n"
    "v_pk_mul_f16 v72, v50, v50 op_sel_hi:[0,1]\n"
    "v_mfma_f32_16x16x32_f16 v[84:87], v[208:211], v[60:63], v[84:87]\n"
    "v_pk_mul_f16 v73, v50, v49 op_sel_hi:[0,1]\n"
    "v_pk_mul_f16 v74, v48, v50 op_sel_hi:[0,1]\n"
    "v_mfma_f32_16x16x32_f16 v[84:87], v[220:223], v[64:67], v[84:87]\n"
    "v_pk_mul_f16 v75, v48, v51 op_sel_hi:[0,1]\n"
    "v_smfmac_f32_16x16x64_f16 v[88:91], v[224:227], v[60:67], v2\n"
    "ds_read_b64_tr_b16 v[48:49], v3 offset:0\n"
    "ds_read_b64_tr_b16 v[58:59], v4 offset:2880\n"
    "v_smfmac_f32_16x16x64_f16 v[88:91], v[252:255], v[68:75], v2\n"
    "s_waitcnt lgkmcnt(2)\n"
    "v_pk_mul_f16 v60, v44, s45 op_sel:[0,1]\n"
    "v_pk_mul_f16 v61, v45, s45 op_sel:[0,1]\n"
    "v_mfma_f32_16x16x32_f16 v[84:87], v[236:239], v[68:71], v[84:87]\n"
    "ds_read_b64_tr_b16 v[50:51], v4 offset:576\n"
    "v_pk_mul_f16 v62, v46, s45 op_sel:[0,1]\n"
    "v_pk_mul_f16 v63, v47, s45 op_sel:[0,1]\n"
    "v_mfma_f32_16x16x32_f16 v[84:87], v[248:251], v[72:75], v[84:87]\n"
    "ds_read_b64_tr_b16 v[52:53], v4 offset:1152\n"
    "v_pk_mul_f16 v64, v44, v46\n"
    "v_pk_mul_f16 v65, v45, v47\n"
    "v_mfma_f32_16x16x32_f16 v[76:79], v[240:243], v[72:75], v[76:79]\n"
    "ds_read_b64_tr_b16 v[54:55], v4 offset:1728\n"
    "v_add_f32 v11, v88, v89\n"
    "v_add_f32 v13, v90, v91\n"
    "v_mfma_f32_16x16x32_f16 v[76:79], v[228:231], v[68:71], v[76:79]\n"
    "ds_read_b64_tr_b16 v[56:57], v4 offset:2304\n"
    "v_mov_b64 v[88:89], 0\n"
    "v_mov_b64 v[90:91], 0\n"
    "v_mfma_f32_16x16x32_f16 v[80:83], v[232:235], v[68:71], v[80:83]\n"
    "s_waitcnt lgkmcnt(4)\n"
    "v_pk_mul_f16 v66, v48, v48 op_sel_hi:[0,1]\n"
    "v_pk_mul_f16 v67, v48, v49 op_sel_hi:[0,1]\n"
    "v_mfma_f32_16x16x32_f16 v[80:83], v[244:247], v[72:75], v[80:83]\n"
    "v_mul_f32 v11, v11, v13\n"
    "v_fmac_f32 v11, v84, v85\n"
    "v_fmac_f32 v11, v86, v87\n"
    ".Lxd_ph2:\n"
    "v_smfmac_f32_16x16x64_f16 v[88:91], v[112:115], v[60:67], v2\n"
    "s_cbranch_vccnz .Lxd_pr23\n"
    "s_setprio 2\n"
    ".Lxd_pr23:\n"
    "v_pk_mul_f16 v68, v58, v58 op_sel_hi:[0,1]\n"
    "v_pk_mul_f16 v69, v58, v49 op_sel_hi:[0,1]\n"
    "v_mfma_f32_16x16x32_f16 v[84:87], v[108:111], v[64:67], 0\n"
    "v_fmac_f32 v11, v76, v80\n"
    "v_fmac_f32 v11, v77, v81\n"
    "v_mfma_f32_16x16x32_f16 v[84:87], v[96:99], v[60:63], v[84:87]\n"
    "v_fmac_f32 v11, v78, v82\n"
    "v_fma_f32 v8, v79, v83, v11\n"
    "v_mfma_f32_16x16x32_f16 v[80:83], v[92:95], v[60:63], 0\n"
    "s_waitcnt lgkmcnt(0)\n"
    "v_pk_mul_f16 v70, v48, v58 op_sel_hi:[0,1]\n"
    "v_pk_mul_f16 v71, v48, v59 op_sel_hi:[0,1]\n"
    "v_mfma_f32_16x16x32_f16 v[80:83], v[104:107], v[64:67], v[80:83]\n"
    "s_movk_i32 s43, 15744\n"
    "v_pk_mul_f16 v72, v50, v58 op_sel_hi:[0,1]\n"
    "v_pk_mul_f16 v73, v50, v59 op_sel_hi:[0,1]\n"
    "v_mfma_f32_16x16x32_f16 v[76:79], v[100:103], v[64:67], 0\n"
    "s_waitcnt vmcnt(0)\n"
    "v_pk_mul_f16 v74, v52, v58 op_sel_hi:[0,1]\n"
    "v_pk_mul_f16 v75, v52, v59 op_sel_hi:[0,1]\n"
    "v_mfma_f32_16x16x32_f16 v[76:79], v[116:119], v[68:71], v[76:79]\n"
    "v_pk_mul_f16 v60, v54, v58 op_sel_hi:[0,1]\n"
    "v_pk_mul_f16 v61, v54, v59 op_sel_hi:[0,1]\n"
    "v_mfma_f32_16x16x32_f16 v[76:79], v[128:131], v[72:75], v[76:79]\n"
    "v_pk_mul_f16 v62, v56, v58 op_sel_hi:[0,1]\n"
    "v_pk_maximum3_f16 v21, v28, v28, s43 op_sel_hi:[1,1,0] neg_lo:[0,1,0] neg_hi:[0,1,0]\n"
    "v_pk_maximum3_f16 v21, v29, v29, v21 neg_lo:[0,1,0] neg_hi:[0,1,0]\n"
    "v_mfma_f32_16x16x32_f16 v[80:83], v[132:135], v[72:75], v[80:83]\n"
    "v_pk_mul_f16 v63, v56, v59 op_sel_hi:[0,1]\n"
    "v_pk_maximum3_f16 v21, v30, v30, v21 neg_lo:[0,1,0] neg_hi:[0,1,0]\n"
    "v_mfma_f32_16x16x32_f16 v[80:83], v[120:123], v[68:71], v[80:83]\n"
    "v_pk_mul_f16 v64, v56, v56 op_sel_hi:[0,1]\n"
    "v_pk_maximum3_f16 v21, v31, v31, v21 neg_lo:[0,1,0] neg_hi:[0,1,0]\n"
    "v_mfma_f32_16x16x32_f16 v[84:87], v[124:127], v[68:71], v[84:87]\n"
    "v_pk_mul_f16 v65, v56, v49 op_sel_hi:[0,1]\n"
    "v_pk_maximum3_f16 v21, v32, v32, v21 neg_lo:[0,1,0] neg_hi:[0,1,0]\n"
    "v_mfma_f32_16x16x32_f16 v[84:87], v[136:139], v[72:75], v[84:87]\n"
    "s_cmp_lg_u32 s46, 0\n"
    "v_pk_mul_f16 v66, v48, v56 op_sel_hi:[0,1]\n"
    "v_pk_maximum3_f16 v21, v33, v33, v21 neg_lo:[0,1,0] neg_hi:[0,1,0]\n"
    "v_smfmac_f32_16x16x64_f16 v[88:91], v[140:143], v[68:75], v2\n"
    "s_cbranch_scc1 .Lxd_ht25\n"
    ".Lxd_ht25r:\n"
    "v_pk_mul_f16 v67, v48, v57 op_sel_hi:[0,1]\n"
    "v_pk_maximum3_f16 v21, v34, v34, v21 neg_lo:[0,1,0] neg_hi:[0,1,0]\n"
    "v_mfma_f32_16x16x32_f16 v[76:79], v[144:147], v[60:63], v[76:79]\n"
    "v_pk_mul_f16 v68, v50, v56 op_sel_hi:[0,1]\n"
    "v_pk_mul_f16 v69, v50, v57 op_sel_hi:[0,1]\n"
    "v_pk_maximum3_f16 v21, v35, v35, v21 neg_lo:[0,1,0] neg_hi:[0,1,0]\n"
    "v_mfma_f32_16x16x32_f16 v[76:79], v[156:159], v[64:67], v[76:79]\n"
    "v_pk_mul_f16 v70, v52, v56 op_sel_hi:[0,1]\n"
    "v_pk_maximum3_f16 v21, v36, v36, v21 neg_lo:[0,1,0] neg_hi:[0,1,0]\n"
    "v_mfma_f32_16x16x32_f16 v[80:83], v[160:163], v[64:67], v[80:83]\n"
    "v_pk_mul_f16 v71, v52, v57 op_sel_hi:[0,1]\n"
    "v_pk_maximum3_f16 v21, v37, v37, v21 neg_lo:[0,1,0] neg_hi:[0,1,0]\n"
    "v_mfma_f32_16x16x32_f16 v[80:83], v[148:151], v[60:63], v[80:83]\n"
    "ds_read_b32 v22, v25 offset:524\n"
    "v_pk_mul_f16 v72, v54, v56 op_sel_hi:[0,1]\n"
    "v_pk_maximum3_f16 v21, v38, v38, v21 neg_lo:[0,1,0] neg_hi:[0,1,0]\n"
    "v_mfma_f32_16x16x32_f16 v[84:87], v[152:155], v[60:63], v[84:87]\n"
    "v_pk_mul_f16 v73, v54, v57 op_sel_hi:[0,1]\n"
    "v_pk_maximum3_f16 v21, v39, v39, v21 neg_lo:[0,1,0] neg_hi:[0,1,0]\n"
    "v_mfma_f32_16x16x32_f16 v[84:87], v[164:167], v[64:67], v[84:87]\n"
    "v_pk_mul_f16 v74, v54, v54 op_sel_hi:[0,1]\n"
    "v_pk_maximum3_f16 v21, v40, v40, v21 neg_lo:[0,1,0] neg_hi:[0,1,0]\n"
    "v_smfmac_f32_16x16x64_f16 v[88:91], v[168:171], v[60:67], v2\n"
    "v_pk_mul_f16 v75, v54, v49 op_sel_hi:[0,1]\n"
    "v_pk_max_u16 v21, v21, v21 op_sel:[0,1] op_sel_hi:[1,0]\n"
    "v_mfma_f32_16x16x32_f16 v[76:79], v[172:175], v[68:71], v[76:79]\n"
    "v_pk_mul_f16 v60, v48, v54 op_sel_hi:[0,1]\n"
    "v_pk_mul_f16 v61, v48, v55 op_sel_hi:[0,1]\n"
    "v_mfma_f32_16x16x32_f16 v[76:79], v[184:187], v[72:75], v[76:79]\n"
    "s_waitcnt lgkmcnt(0)\n"
    "v_pk_mul_f16 v62, v50, v54 op_sel_hi:[0,1]\n"
    "v_readfirstlane_b32 s45, v22\n"
    "v_mfma_f32_16x16x32_f16 v[80:83], v[188:191], v[72:75], v[80:83]\n"
    "s_cmp_lt_i32 s45, 16\n"
    "v_pk_mul_f16 v63, v50, v55 op_sel_hi:[0,1]\n"
    "v_max_u32_dpp v21, v21, v21 quad_perm:[1,0,3,2] row_mask:0xf bank_mask:0xf\n"
    "v_mfma_f32_16x16x32_f16 v[80:83], v[176:179], v[68:71], v[80:83]\n"
    "s_cbranch_scc1 .Lxd_ck26\n"
    ".Lxd_ck26r:\n"
    "v_pk_mul_f16 v64, v52, v54 op_sel_hi:[0,1]\n"
    "v_max_u32_dpp v21, v21, v21 quad_perm:[2,3,0,1] row_mask:0xf bank_mask:0xf\n"
    "v_mfma_f32_16x16x32_f16 v[84:87], v[180:183], v[68:71], v[84:87]\n"
    "ds_read_b64_tr_b16 v[44:45], v5 offset:4096\n"
    "v_pk_mul_f16 v65, v52, v55 op_sel_hi:[0,1]\n"
    "v_max_u32_dpp v21, v21, v21 row_half_mirror row_mask:0xf bank_mask:0xf\n"
    "v_mfma_f32_16x16x32_f16 v[84:87], v[192:195], v[72:75], v[84:87]\n"
    "ds_read_b64_tr_b16 v[46:47], v6 offset:4096\n"
    "v_pk_mul_f16 v66, v52, v52 op_sel_hi:[0,1]\n"
    "v_max_u32_dpp v21, v21, v21 row_mirror row_mask:0xf bank_mask:0xf\n"
    "v_smfmac_f32_16x16x64_f16 v[88:91], v[196:199], v[68:75], v2\n"
    "v_pk_mul_f16 v67, v52, v49 op_sel_hi:[0,1]\n"
    "v_max_u32_dpp v21, v21, v21 row_bcast:15 row_mask:0xa bank_mask:0xf\n"
    "v_mfma_f32_16x16x32_f16 v[76:79], v[200:203], v[60:63], v[76:79]\n"
    "v_pk_mul_f16 v68, v48, v52 op_sel_hi:[0,1]\n"
    "v_pk_mul_f16 v69, v48, v53 op_sel_hi:[0,1]\n"
    "v_mfma_f32_16x16x32_f16 v[76:79], v[212:215], v[64:67], v[76:79]\n"
    "v_pk_mul_f16 v70, v50, v52 op_sel_hi:[0,1]\n"
    "v_max_u32_dpp v21, v21, v21 row_bcast:31 row_mask:0xc bank_mask:0xf\n"
    "v_mfma_f32_16x16x32_f16 v[80:83], v[216:219], v[64:67], v[80:83]\n"
    "s_cbranch_vccnz .Lxd_pr24\n"
    "s_setprio 0\n"
    ".Lxd_pr24:\n"
    "v_pk_mul_f16 v71, v50, v53 op_sel_hi:[0,1]\n"
    "v_readlane_b32 s47, v21, 63\n"
    "v_mfma_f32_16x16x32_f16 v[80:83], v[204:207], v[60:63], v[80:83]\n"
    "s_and_b32 s47, s47, 0x7f80\n"
    "v_pk_mul_f16 v72, v50, v50 op_sel_hi:[0,1]\n"
    "v_mfma_f32_16x16x32_f16 v[84:87], v[208:211], v[60:63], v[84:87]\n"
    "s_sub_u32 s50, s47, 2816\n"
    "v_pk_mul_f16 v73, v50, v49 op_sel_hi:[0,1]\n"
    "v_pk_mul_f16 v74, v48, v50 op_sel_hi:[0,1]\n"
    "v_pk_maximum3_f16 v21, v28, v28, s50 op_sel_hi:[1,1,0] neg_lo:[0,1,0] neg_hi:[0,1,0]\n"
    "v_mfma_f32_16x16x32_f16 v[84:87], v[220:223], v[64:67], v[84:87]\n"
    "s_mul_i32 s51, s50, -8\n"
    "v_pk_mul_f16 v75, v48, v51 op_sel_hi:[0,1]\n"
    "v_pk_mad_u16 v21, v21, 8, s51 op_sel_hi:[1,0,0]\n"
    "v_smfmac_f32_16x16x64_f16 v[88:91], v[224:227], v[60:67], v2\n"
    "ds_read_b64_tr_b16 v[48:49], v3 offset:4096\n"
    "ds_read_b64_tr_b16 v[58:59], v4 offset:6976\n"
    "v_bfi_b32 v28, s57, v21, v28\n"
    "v_smfmac_f32_16x16x64_f16 v[88:91], v[252:255], v[68:75], v2\n"
    "s_waitcnt lgkmcnt(2)\n"
    "v_pk_mul_f16 v60, v44, s45 op_sel:[0,1]\n"
    "v_pk_mul_f16 v61, v45, s45 op_sel:[0,1]\n"
    "v_mfma_f32_16x16x32_f16 v[84:87], v[236:239], v[68:71], v[84:87]\n"
    "ds_read_b64_tr_b16 v[50:51], v4 offset:4672\n"
    "v_pk_mul_f16 v62, v46, s45 op_sel:[0,1]\n"
    "v_pk_mul_f16 v63, v47, s45 op_sel:[0,1]\n"
    "v_mfma_f32_16x16x32_f16 v[84:87], v[248:251], v[72:75], v[84:87]\n"
    "ds_read_b64_tr_b16 v[52:53], v4 offset:5248\n"
    "v_pk_mul_f16 v64, v44, v46\n"
    "v_pk_mul_f16 v65, v45, v47\n"
    "v_mfma_f32_16x16x32_f16 v[76:79], v[240:243], v[72:75], v[76:79]\n"
    "ds_read_b64_tr_b16 v[54:55], v4 offset:5824\n"
    "v_add_f32 v11, v88, v89\n"
    "v_add_f32 v13, v90, v91\n"
    "v_mfma_f32_16x16x32_f16 v[76:79], v[228:231], v[68:71], v[76:79]\n"
    "ds_read_b64_tr_b16 v[56:57], v4 offset:6400\n"
    "v_mov_b64 v[88:89], 0\n"
    "v_mov_b64 v[90:91], 0\n"
    "v_mfma_f32_16x16x32_f16 v[80:83], v[232:235], v[68:71], v[80:83]\n"
    "s_waitcnt lgkmcnt(4)\n"
    "v_pk_mul_f16 v66, v48, v48 op_sel_hi:[0,1]\n"
    "v_pk_mul_f16 v67, v48, v49 op_sel_hi:[0,1]\n"
    "v_mfma_f32_16x16x32_f16 v[80:83], v[244:247], v[72:75], v[80:83]\n"
    "s_lshr_b32 s47, s47, 3\n"
    "v_mul_f32 v11, v11, v13\n"
    "v_fmac_f32 v11, v84, v85\n"
    "v_fmac_f32 v11, v86, v87\n"
    ".Lxd_ph3:\n"
    "v_smfmac_f32_16x16x64_f16 v[88:91], v[112:115], v[60:67], v2\n"
    "s_cbranch_vccnz .Lxd_pr27\n"
    "s_setprio 2\n"
    ".Lxd_pr27:\n"
    "v_pk_mul_f16 v68, v58, v58 op_sel_hi:[0,1]\n"
    "v_pk_mul_f16 v69, v58, v49 op_sel_hi:[0,1]\n"
    "v_mfma_f32_16x16x32_f16 v[84:87], v[108:111], v[64:67], 0\n"
    "s_cmp_lt_u32 s55, 32\n"
    "v_fmac_f32 v11, v76, v80\n"
    "v_fmac_f32 v11, v77, v81\n"
    "v_pk_maximum3_f16 v12, v29, v29, s50 op_sel_hi:[1,1,0] neg_lo:[0,1,0] neg_hi:[0,1,0]\n"
    "v_mfma_f32_16x16x32_f16 v[84:87], v[96:99], v[60:63], v[84:87]\n"
    "s_cselect_b32 s44, 0, -1\n"
    "v_fmac_f32 v11, v78, v82\n"
    "v_fma_f32 v9, v79, v83, v11\n"
    "v_mfma_f32_16x16x32_f16 v[80:83], v[92:95], v[60:63], 0\n"
    "s_waitcnt lgkmcnt(0)\n"
    "v_pk_mul_f16 v70, v48, v58 op_sel_hi:[0,1]\n"
    "v_pk_mul_f16 v71, v48, v59 op_sel_hi:[0,1]\n"
    "v_pk_mad_u16 v12, v12, 8, s51 op_sel_hi:[1,0,0]\n"
    "v_mfma_f32_16x16x32_f16 v[80:83], v[104:107], v[64:67], v[80:83]\n"
    "s_add_u32 s55, s55, 4\n"
    "v_pk_mul_f16 v72, v50, v58 op_sel_hi:[0,1]\n"
    "v_pk_mul_f16 v73, v50, v59 op_sel_hi:[0,1]\n"
    "v_mfma_f32_16x16x32_f16 v[76:79], v[100:103], v[64:67], 0\n"
    "v_pk_mul_f16 v74, v52, v58 op_sel_hi:[0,1]\n"
    "v_pk_mul_f16 v75, v52, v59 op_sel_hi:[0,1]\n"
    "v_bfi_b32 v29, s57, v12, v29\n"
    "v_mfma_f32_16x16x32_f16 v[76:79], v[116:119], v[68:71], v[76:79]\n"
    "v_pk_mul_f16 v60, v54, v58 op_sel_hi:[0,1]\n"
    "v_pk_mul_f16 v61, v54, v59 op_sel_hi:[0,1]\n"
    "v_pk_maximum3_f16 v21, v30, v30, s50 op_sel_hi:[1,1,0] neg_lo:[0,1,0] neg_hi:[0,1,0]\n"
    "v_mfma_f32_16x16x32_f16 v[76:79], v[128:131], v[72:75], v[76:79]\n"
    "v_pk_mul_f16 v62, v56, v58 op_sel_hi:[0,1]\n"
    "v_add_u32_e64 v24, s44, 1\n"
    "v_mfma_f32_16x16x32_f16 v[80:83], v[132:135], v[72:75], v[80:83]\n"
    "s_sub_u32 s66, 4112, s47\n"
    "v_pk_mul_f16 v63, v56, v59 op_sel_hi:[0,1]\n"
    "v_pk_mad_u16 v21, v21, 8, s51 op_sel_hi:[1,0,0]\n"
    "v_bfi_b32 v30, s57, v21, v30\n"
    "v_mfma_f32_16x16x32_f16 v[80:83], v[120:123], v[68:71], v[80:83]\n"
    "s_lshl_b32 s66, s66, 19\n"
    "v_pk_mul_f16 v64, v56, v56 op_sel_hi:[0,1]\n"
    "v_pk_maximum3_f16 v12, v31, v31, s50 op_sel_hi:[1,1,0] neg_lo:[0,1,0] neg_hi:[0,1,0]\n"
    "v_pk_mad_u16 v12, v12, 8, s51 op_sel_hi:[1,0,0]\n"
    "v_mfma_f32_16x16x32_f16 v[84:87], v[124:127], v[68:71], v[84:87]\n"
    "v_pk_mul_f16 v65, v56, v49 op_sel_hi:[0,1]\n"
    "v_bfi_b32 v31, s57, v12, v31\n"
    "v_mfma_f32_16x16x32_f16 v[84:87], v[136:139], v[72:75], v[84:87]\n"
    "ds_write_b128 v15, v[28:31] offset:0\n"
    "v_pk_mul_f16 v66, v48, v56 op_sel_hi:[0,1]\n"
    "v_pk_maximum3_f16 v21, v32, v32, s50 op_sel_hi:[1,1,0] neg_lo:[0,1,0] neg_hi:[0,1,0]\n"
    "v_pk_mad_u16 v21, v21, 8, s51 op_sel_hi:[1,0,0]\n"
    "v_smfmac_f32_16x16x64_f16 v[88:91], v[140:143], v[68:75], v2\n"
    "v_pk_mul_f16 v67, v48, v57 op_sel_hi:[0,1]\n"
    "v_bfi_b32 v32, s57, v21, v32\n"
    "v_mfma_f32_16x16x32_f16 v[76:79], v[144:147], v[60:63], v[76:79]\n"
    "v_pk_mul_f16 v68, v50, v56 op_sel_hi:[0,1]\n"
    "v_pk_mul_f16 v69, v50, v57 op_sel_hi:[0,1]\n"
    "v_pk_maximum3_f16 v12, v33, v33, s50 op_sel_hi:[1,1,0] neg_lo:[0,1,0] neg_hi:[0,1,0]\n"
    "v_mfma_f32_16x16x32_f16 v[76:79], v[156:159], v[64:67], v[76:79]\n"
    "v_pk_mul_f16 v70, v52, v56 op_sel_hi:[0,1]\n"
    "v_pk_mad_u16 v12, v12, 8, s51 op_sel_hi:[1,0,0]\n"
    "v_bfi_b32 v33, s57, v12, v33\n"
    "v_mfma_f32_16x16x32_f16 v[80:83], v[160:163], v[64:67], v[80:83]\n"
    "v_pk_mul_f16 v71, v52, v57 op_sel_hi:[0,1]\n"
    "v_pk_maximum3_f16 v21, v34, v34, s50 op_sel_hi:[1,1,0] neg_lo:[0,1,0] neg_hi:[0,1,0]\n"
    "v_mfma_f32_16x16x32_f16 v[80:83], v[148:151], v[60:63], v[80:83]\n"
    "ds_read_b32 v22, v25 offset:528\n"
    "v_pk_mul_f16 v72, v54, v56 op_sel_hi:[0,1]\n"
    "v_pk_mad_u16 v21, v21, 8, s51 op_sel_hi:[1,0,0]\n"
    "v_bfi_b32 v34, s57, v21, v34\n"
    "v_mfma_f32_16x16x32_f16 v[84:87], v[152:155], v[60:63], v[84:87]\n"
    "v_pk_mul_f16 v73, v54, v57 op_sel_hi:[0,1]\n"
    "v_pk_maximum3_f16 v12, v35, v35, s50 op_sel_hi:[1,1,0] neg_lo:[0,1,0] neg_hi:[0,1,0]\n"
    "v_pk_mad_u16 v12, v12, 8, s51 op_sel_hi:[1,0,0]\n"
    "v_mfma_f32_16x16x32_f16 v[84:87], v[164:167], v[64:67], v[84:87]\n"
    "v_pk_mul_f16 v74, v54, v54 op_sel_hi:[0,1]\n"
    "v_bfi_b32 v35, s57, v12, v35\n"
    "v_smfmac_f32_16x16x64_f16 v[88:91], v[168:171], v[60:67], v2\n"
    "ds_write_b128 v15, v[32:35] offset:144\n"
    "v_pk_mul_f16 v75, v54, v49 op_sel_hi:[0,1]\n"
    "v_pk_maximum3_f16 v21, v36, v36, s50 op_sel_hi:[1,1,0] neg_lo:[0,1,0] neg_hi:[0,1,0]\n"
    "v_pk_mad_u16 v21, v21, 8, s51 op_sel_hi:[1,0,0]\n"
    "v_mfma_f32_16x16x32_f16 v[76:79], v[172:175], v[68:71], v[76:79]\n"
    "v_pk_mul_f16 v60, v48, v54 op_sel_hi:[0,1]\n"
    "v_pk_mul_f16 v61, v48, v55 op_sel_hi:[0,1]\n"
    "v_mfma_f32_16x16x32_f16 v[76:79], v[184:187], v[72:75], v[76:79]\n"
    "s_waitcnt lgkmcnt(1)\n"
    "v_pk_mul_f16 v62, v50, v54 op_sel_hi:[0,1]\n"
    "v_readfirstlane_b32 s45, v22\n"
    "v_bfi_b32 v36, s57, v21, v36\n"
    "v_mfma_f32_16x16x32_f16 v[80:83], v[188:191], v[72:75], v[80:83]\n"
    "s_cmp_lt_i32 s45, 16\n"
    "v_pk_mul_f16 v63, v50, v55 op_sel_hi:[0,1]\n"
    "v_pk_maximum3_f16 v12, v37, v37, s50 op_sel_hi:[1,1,0] neg_lo:[0,1,0] neg_hi:[0,1,0]\n"
    "v_pk_mad_u16 v12, v12, 8, s51 op_sel_hi:[1,0,0]\n"
    "v_mfma_f32_16x16x32_f16 v[80:83], v[176:179], v[68:71], v[80:83]\n"
    "s_cbranch_scc1 .Lxd_ck30\n"
    ".Lxd_ck30r:\n"
    "v_pk_mul_f16 v64, v52, v54 op_sel_hi:[0,1]\n"
    "v_bfi_b32 v37, s57, v12, v37\n"
    "v_mfma_f32_16x16x32_f16 v[84:87], v[180:183], v[68:71], v[84:87]\n"
    "ds_read_b64_tr_b16 v[44:45], v5 offset:8192\n"
    "v_pk_mul_f16 v65, v52, v55 op_sel_hi:[0,1]\n"
    "v_pk_maximum3_f16 v21, v38, v38, s50 op_sel_hi:[1,1,0] neg_lo:[0,1,0] neg_hi:[0,1,0]\n"
    "v_pk_mad_u16 v21, v21, 8, s51 op_sel_hi:[1,0,0]\n"
    "v_mfma_f32_16x16x32_f16 v[84:87], v[192:195], v[72:75], v[84:87]\n"
    "ds_read_b64_tr_b16 v[46:47], v6 offset:8192\n"
    "v_pk_mul_f16 v66, v52, v52 op_sel_hi:[0,1]\n"
    "v_bfi_b32 v38, s57, v21, v38\n"
    "v_pk_maximum3_f16 v12, v39, v39, s50 op_sel_hi:[1,1,0] neg_lo:[0,1,0] neg_hi:[0,1,0]\n"
    "v_smfmac_f32_16x16x64_f16 v[88:91], v[196:199], v[68:75], v2\n"
    "v_pk_mul_f16 v67, v52, v49 op_sel_hi:[0,1]\n"
    "v_pk_mad_u16 v12, v12, 8, s51 op_sel_hi:[1,0,0]\n"
    "v_mfma_f32_16x16x32_f16 v[76:79], v[200:203], v[60:63], v[76:79]\n"
    "s_and_b32 s40, s45, 15\n"
    "v_pk_mul_f16 v68, v48, v52 op_sel_hi:[0,1]\n"
    "v_pk_mul_f16 v69, v48, v53 op_sel_hi:[0,1]\n"
    "v_bfi_b32 v39, s57, v12, v39\n"
    "v_mfma_f32_16x16x32_f16 v[76:79], v[212:215], v[64:67], v[76:79]\n"
    "s_cbranch_scc1 .Lxd_dr29\n"
    "s_mov_b64 exec, 1\n"
    "global_atomic_add v24, v17, v24, s[52:53] sc0 sc1\n"
    "s_mov_b64 exec, -1\n"
    ".Lxd_dr29r:\n"
    "v_pk_mul_f16 v70, v50, v52 op_sel_hi:[0,1]\n"
    "v_pk_maximum3_f16 v21, v40, v40, s50 op_sel_hi:[1,1,0] neg_lo:[0,1,0] neg_hi:[0,1,0]\n"
    "v_mfma_f32_16x16x32_f16 v[80:83], v[216:219], v[64:67], v[80:83]\n"
    "s_cbranch_vccnz .Lxd_pr28\n"
    "s_setprio 0\n"
    ".Lxd_pr28:\n"
    "v_pk_mul_f16 v71, v50, v53 op_sel_hi:[0,1]\n"
    "v_pk_mad_u16 v21, v21, 8, s51 op_sel_hi:[1,0,0]\n"
    "v_bfi_b32 v40, s57, v21, v40\n"
    "v_mfma_f32_16x16x32_f16 v[80:83], v[204:207], v[60:63], v[80:83]\n"
    "ds_write_b128 v15, v[36:39] offset:288\n"
    "v_pk_mul_f16 v72, v50, v50 op_sel_hi:[0,1]\n"
    "v_cvt_pkrtz_f16_f32 v12, s66, s66\n"
    "v_lshl_or_b32 v21, v12, 16, s47\n"
    "v_mfma_f32_16x16x32_f16 v[84:87], v[208:211], v[60:63], v[84:87]\n"
    "ds_write2_b32 v16, v40, v12 offset1:72\n"
    "v_pk_mul_f16 v73, v50, v49 op_sel_hi:[0,1]\n"
    "v_pk_mul_f16 v74, v48, v50 op_sel_hi:[0,1]\n"
    "v_mfma_f32_16x16x32_f16 v[84:87], v[220:223], v[64:67], v[84:87]\n"
    "ds_or_b32 v25, v21 offset:532\n"
    "v_pk_mul_f16 v75, v48, v51 op_sel_hi:[0,1]\n"
    "v_smfmac_f32_16x16x64_f16 v[88:91], v[224:227], v[60:67], v2\n"
    "ds_read_b64_tr_b16 v[48:49], v3 offset:8192\n"
    "ds_read_b64_tr_b16 v[58:59], v4 offset:11072\n"
    "v_add_u32 v25, 16, v25\n"
    "v_smfmac_f32_16x16x64_f16 v[88:91], v[252:255], v[68:75], v2\n"
    "s_waitcnt lgkmcnt(5)\n"
    "v_pk_mul_f16 v60, v44, s45 op_sel:[0,1]\n"
    "v_pk_mul_f16 v61, v45, s45 op_sel:[0,1]\n"
    "v_mfma_f32_16x16x32_f16 v[84:87], v[236:239], v[68:71], v[84:87]\n"
    "ds_read_b64_tr_b16 v[50:51], v4 offset:8768\n"
    "v_pk_mul_f16 v62, v46, s45 op_sel:[0,1]\n"
    "v_pk_mul_f16 v63, v47, s45 op_sel:[0,1]\n"
    "v_mfma_f32_16x16x32_f16 v[84:87], v[248:251], v[72:75], v[84:87]\n"
    "ds_read_b64_tr_b16 v[52:53], v4 offset:9344\n"
    "v_pk_mul_f16 v64, v44, v46\n"
    "v_pk_mul_f16 v65, v45, v47\n"
    "v_mfma_f32_16x16x32_f16 v[76:79], v[240:243], v[72:75], v[76:79]\n"
    "ds_read_b64_tr_b16 v[54:55], v4 offset:9920\n"
    "v_add_f32 v11, v88, v89\n"
    "v_add_f32 v13, v90, v91\n"
    "v_mfma_f32_16x16x32_f16 v[76:79], v[228:231], v[68:71], v[76:79]\n"
    "ds_read_b64_tr_b16 v[56:57], v4 offset:10496\n"
    "v_mov_b64 v[88:89], 0\n"
    "v_mov_b64 v[90:91], 0\n"
    "v_mfma_f32_16x16x32_f16 v[80:83], v[232:235], v[68:71], v[80:83]\n"
    "s_waitcnt lgkmcnt(4)\n"
    "v_pk_mul_f16 v66, v48, v48 op_sel_hi:[0,1]\n"
    "v_pk_mul_f16 v67, v48, v49 op_sel_hi:[0,1]\n"
    "v_mfma_f32_16x16x32_f16 v[80:83], v[244:247], v[72:75], v[80:83]\n"
    "s_cmp_eq_u32 s36, 0\n"
    "v_mul_f32 v11, v11, v13\n"
    "v_fmac_f32 v11, v84, v85\n"
    "v_fmac_f32 v11, v86, v87\n"
    "s_cbranch_scc1 .Lxd_ph0\n"
    ".Lxd_pl0:\n"
    "v_smfmac_f32_16x16x64_f16 v[88:91], v[112:115], v[60:67], v2\n"
    "s_cbranch_vccnz .Lxd_pr31\n"
    "s_setprio 2\n"
    ".Lxd_pr31:\n"
    "v_pk_mul_f16 v68, v58, v58 op_sel_hi:[0,1]\n"
    "v_pk_mul_f16 v69, v58, v49 op_sel_hi:[0,1]\n"
    "v_mfma_f32_16x16x32_f16 v[84:87], v[108:111], v[64:67], 0\n"
    "v_fmac_f32 v11, v76, v80\n"
    "v_fmac_f32 v11, v77, v81\n"
    "v_mfma_f32_16x16x32_f16 v[84:87], v[96:99], v[60:63], v[84:87]\n"
    "v_fmac_f32 v11, v78, v82\n"
    "v_fma_f32 v10, v79, v83, v11\n"
    "v_mfma_f32_16x16x32_f16 v[80:83], v[92:95], v[60:63], 0\n"
    "s_waitcnt lgkmcnt(0)\n"
    "v_pk_mul_f16 v70, v48, v58 op_sel_hi:[0,1]\n"
    "v_pk_mul_f16 v71, v48, v59 op_sel_hi:[0,1]\n"
    "v_mfma_f32_16x16x32_f16 v[80:83], v[104:107], v[64:67], v[80:83]\n"
    "v_pk_mul_f16 v72, v50, v58 op_sel_hi:[0,1]\n"
    "v_pk_mul_f16 v73, v50, v59 op_sel_hi:[0,1]\n"
    "v_mfma_f32_16x16x32_f16 v[76:79], v[100:103], v[64:67], 0\n"
    "v_pk_mul_f16 v74, v52, v58 op_sel_hi:[0,1]\n"
    "v_pk_mul_f16 v75, v52, v59 op_sel_hi:[0,1]\n"
    "v_mfma_f32_16x16x32_f16 v[76:79], v[116:119], v[68:71], v[76:79]\n"
    "v_pk_mul_f16 v60, v54, v58 op_sel_hi:[0,1]\n"
    "v_pk_mul_f16 v61, v54, v59 op_sel_hi:[0,1]\n"
    "v_mfma_f32_16x16x32_f16 v[76:79], v[128:131], v[72:75], v[76:79]\n"
    "v_pk_mul_f16 v62, v56, v58 op_sel_hi:[0,1]\n"
    "v_permlane32_swap_b32 v7, v8\n"
    "v_mfma_f32_16x16x32_f16 v[80:83], v[132:135], v[72:75], v[80:83]\n"
    "v_pk_mul_f16 v63, v56, v59 op_sel_hi:[0,1]\n"
    "v_permlane32_swap_b32 v9, v10\n"
    "v_mfma_f32_16x16x32_f16 v[80:83], v[120:123], v[68:71], v[80:83]\n"
    "v_pk_mul_f16 v64, v56, v56 op_sel_hi:[0,1]\n"
    "v_mul_f32 v7, v0, v7\n"
    "v_mfma_f32_16x16x32_f16 v[84:87], v[124:127], v[68:71], v[84:87]\n"
    "v_pk_mul_f16 v65, v56, v49 op_sel_hi:[0,1]\n"
    "v_mul_f32 v9, v0, v9\n"
    "v_mfma_f32_16x16x32_f16 v[84:87], v[136:139], v[72:75], v[84:87]\n"
    "v_pk_mul_f16 v66, v48, v56 op_sel_hi:[0,1]\n"
    "v_fmac_f32 v7, v23, v8\n"
    "v_smfmac_f32_16x16x64_f16 v[88:91], v[140:143], v[68:75], v2\n"
    "v_pk_mul_f16 v67, v48, v57 op_sel_hi:[0,1]\n"
    "v_fmac_f32 v9, v23, v10\n"
    "v_mfma_f32_16x16x32_f16 v[76:79], v[144:147], v[60:63], v[76:79]\n"
    "v_pk_mul_f16 v68, v50, v56 op_sel_hi:[0,1]\n"
    "v_pk_mul_f16 v69, v50, v57 op_sel_hi:[0,1]\n"
    "v_mfma_f32_16x16x32_f16 v[76:79], v[156:159], v[64:67], v[76:79]\n"
    "v_pk_mul_f16 v70, v52, v56 op_sel_hi:[0,1]\n"
    "v_permlane16_swap_b32 v7, v9\n"
    "v_mfma_f32_16x16x32_f16 v[80:83], v[160:163], v[64:67], v[80:83]\n"
    "v_pk_mul_f16 v71, v52, v57 op_sel_hi:[0,1]\n"
    "v_add_f32 v7, v7, v9\n"
    "v_mfma_f32_16x16x32_f16 v[80:83], v[148:151], v[60:63], v[80:83]\n"
    "ds_read_b32 v22, v25 offset:516\n"
    "v_pk_mul_f16 v72, v54, v56 op_sel_hi:[0,1]\n"
    "v_add_f32_dpp v7, v7, v7 quad_perm:[1,0,3,2] row_mask:0xf bank_mask:0xf\n"
    "v_mfma_f32_16x16x32_f16 v[84:87], v[152:155], v[60:63], v[84:87]\n"
    "v_pk_mul_f16 v73, v54, v57 op_sel_hi:[0,1]\n"
    "v_add_f32_dpp v7, v7, v7 quad_perm:[2,3,0,1] row_mask:0xf bank_mask:0xf\n"
    "v_mfma_f32_16x16x32_f16 v[84:87], v[164:167], v[64:67], v[84:87]\n"
    "v_pk_mul_f16 v74, v54, v54 op_sel_hi:[0,1]\n"
    "v_add_f32_dpp v7, v7, v7 row_half_mirror row_mask:0xf bank_mask:0xf\n"
    "v_smfmac_f32_16x16x64_f16 v[88:91], v[168:171], v[60:67], v2\n"
    "v_pk_mul_f16 v75, v54, v49 op_sel_hi:[0,1]\n"
    "v_add_f32_dpp v7, v7, v7 row_mirror row_mask:0xf bank_mask:0xf\n"
    "v_mfma_f32_16x16x32_f16 v[76:79], v[172:175], v[68:71], v[76:79]\n"
    "ds_write_b32 v14, v7\n"
    "v_pk_mul_f16 v60, v48, v54 op_sel_hi:[0,1]\n"
    "v_pk_mul_f16 v61, v48, v55 op_sel_hi:[0,1]\n"
    "v_mfma_f32_16x16x32_f16 v[76:79], v[184:187], v[72:75], v[76:79]\n"
    "s_waitcnt lgkmcnt(1)\n"
    "v_pk_mul_f16 v62, v50, v54 op_sel_hi:[0,1]\n"
    "v_readfirstlane_b32 s45, v22\n"
    "v_mfma_f32_16x16x32_f16 v[80:83], v[188:191], v[72:75], v[80:83]\n"
    "s_cmp_lt_i32 s45, 16\n"
    "v_pk_mul_f16 v63, v50, v55 op_sel_hi:[0,1]\n"
    "v_add_u32 v14, 64, v14\n"
    "v_mfma_f32_16x16x32_f16 v[80:83], v[176:179], v[68:71], v[80:83]\n"
    "s_cbranch_scc1 .Lxd_ck33\n"
    ".Lxd_ck33r:\n"
    "v_pk_mul_f16 v64, v52, v54 op_sel_hi:[0,1]\n"
    "v_mfma_f32_16x16x32_f16 v[84:87], v[180:183], v[68:71], v[84:87]\n"
    "ds_read_b64_tr_b16 v[44:45], v5 offset:12288\n"
    "v_pk_mul_f16 v65, v52, v55 op_sel_hi:[0,1]\n"
    "v_mfma_f32_16x16x32_f16 v[84:87], v[192:195], v[72:75], v[84:87]\n"
    "ds_read_b64_tr_b16 v[46:47], v6 offset:12288\n"
    "v_pk_mul_f16 v66, v52, v52 op_sel_hi:[0,1]\n"
    "v_smfmac_f32_16x16x64_f16 v[88:91], v[196:199], v[68:75], v2\n"
    "v_pk_mul_f16 v67, v52, v49 op_sel_hi:[0,1]\n"
    "v_mfma_f32_16x16x32_f16 v[76:79], v[200:203], v[60:63], v[76:79]\n"
    "v_pk_mul_f16 v68, v48, v52 op_sel_hi:[0,1]\n"
    "v_pk_mul_f16 v69, v48, v53 op_sel_hi:[0,1]\n"
    "v_mfma_f32_16x16x32_f16 v[76:79], v[212:215], v[64:67], v[76:79]\n"
    "v_pk_mul_f16 v70, v50, v52 op_sel_hi:[0,1]\n"
    "v_mfma_f32_16x16x32_f16 v[80:83], v[216:219], v[64:67], v[80:83]\n"
    "s_cbranch_vccnz .Lxd_pr32\n"
    "s_setprio 0\n"
    ".Lxd_pr32:\n"
    "v_pk_mul_f16 v71, v50, v53 op_sel_hi:[0,1]\n"
    "v_mfma_f32_16x16x32_f16 v[80:83], v[204:207], v[60:63], v[80:83]\n"
    "v_pk_mul_f16 v72, v50, v50 op_sel_hi:[0,1]\n"
    "v_mfma_f32_16x16x32_f16 v[84:87], v[208:211], v[60:63], v[84:87]\n"
    "v_pk_mul_f16 v73, v50, v49 op_sel_hi:[0,1]\n"
    "v_pk_mul_f16 v74, v48, v50 op_sel_hi:[0,1]\n"
    "v_mfma_f32_16x16x32_f16 v[84:87], v[220:223], v[64:67], v[84:87]\n"
    "v_pk_mul_f16 v75, v48, v51 op_sel_hi:[0,1]\n"
    "v_smfmac_f32_16x16x64_f16 v[88:91], v[224:227], v[60:67], v2\n"
    "ds_read_b64_tr_b16 v[48:49], v3 offset:12288\n"
    "ds_read_b64_tr_b16 v[58:59], v4 offset:15168\n"
    "v_smfmac_f32_16x16x64_f16 v[88:91], v[252:255], v[68:75], v2\n"
    "s_waitcnt lgkmcnt(2)\n"
    "v_pk_mul_f16 v60, v44, s45 op_sel:[0,1]\n"
    "v_pk_mul_f16 v61, v45, s45 op_sel:[0,1]\n"
    "v_mfma_f32_16x16x32_f16 v[84:87], v[236:239], v[68:71], v[84:87]\n"
    "ds_read_b64_tr_b16 v[50:51], v4 offset:12864\n"
    "v_pk_mul_f16 v62, v46, s45 op_sel:[0,1]\n"
    "v_pk_mul_f16 v63, v47, s45 op_sel:[0,1]\n"
    "v_mfma_f32_16x16x32_f16 v[84:87], v[248:251], v[72:75], v[84:87]\n"
    "ds_read_b64_tr_b16 v[52:53], v4 offset:13440\n"
    "v_pk_mul_f16 v64, v44, v46\n"
    "v_pk_mul_f16 v65, v45, v47\n"
    "v_mfma_f32_16x16x32_f16 v[76:79], v[240:243], v[72:75], v[76:79]\n"
    "ds_read_b64_tr_b16 v[54:55], v4 offset:14016\n"
    "v_add_f32 v11, v88, v89\n"
    "v_add_f32 v13, v90, v91\n"
    "v_mfma_f32_16x16x32_f16 v[76:79], v[228:231], v[68:71], v[76:79]\n"
    "ds_read_b64_tr_b16 v[56:57], v4 offset:14592\n"
    "v_mov_b64 v[88:89], 0\n"
    "v_mov_b64 v[90:91], 0\n"
    "v_mfma_f32_16x16x32_f16 v[80:83], v[232:235], v[68:71], v[80:83]\n"
    "s_waitcnt lgkmcnt(4)\n"
    "v_pk_mul_f16 v66, v48, v48 op_sel_hi:[0,1]\n"
    "v_pk_mul_f16 v67, v48, v49 op_sel_hi:[0,1]\n"
    "v_mfma_f32_16x16x32_f16 v[80:83], v[244:247], v[72:75], v[80:83]\n"
    "v_mul_f32 v11, v11, v13\n"
    "v_fmac_f32 v11, v84, v85\n"
    "v_fmac_f32 v11, v86, v87\n"
    ".Lxd_pl1:\n"
    "v_smfmac_f32_16x16x64_f16 v[88:91], v[112:115], v[60:67], v2\n"
    "s_cbranch_vccnz .Lxd_pr34\n"
    "s_setprio 2\n"
    ".Lxd_pr34:\n"
    "v_pk_mul_f16 v68, v58, v58 op_sel_hi:[0,1]\n"
    "v_pk_mul_f16 v69, v58, v49 op_sel_hi:[0,1]\n"
    "v_mfma_f32_16x16x32_f16 v[84:87], v[108:111], v[64:67], 0\n"
    "v_fmac_f32 v11, v76, v80\n"
    "v_fmac_f32 v11, v77, v81\n"
    "v_mfma_f32_16x16x32_f16 v[84:87], v[96:99], v[60:63], v[84:87]\n"
    "v_fmac_f32 v11, v78, v82\n"
    "v_fma_f32 v7, v79, v83, v11\n"
    "v_mfma_f32_16x16x32_f16 v[80:83], v[92:95], v[60:63], 0\n"
    "s_waitcnt lgkmcnt(0)\n"
    "v_pk_mul_f16 v70, v48, v58 op_sel_hi:[0,1]\n"
    "v_pk_mul_f16 v71, v48, v59 op_sel_hi:[0,1]\n"
    "v_mfma_f32_16x16x32_f16 v[80:83], v[104:107], v[64:67], v[80:83]\n"
    "v_pk_mul_f16 v72, v50, v58 op_sel_hi:[0,1]\n"
    "v_pk_mul_f16 v73, v50, v59 op_sel_hi:[0,1]\n"
    "v_mfma_f32_16x16x32_f16 v[76:79], v[100:103], v[64:67], 0\n"
    "v_pk_mul_f16 v74, v52, v58 op_sel_hi:[0,1]\n"
    "v_pk_mul_f16 v75, v52, v59 op_sel_hi:[0,1]\n"
    "v_mfma_f32_16x16x32_f16 v[76:79], v[116:119], v[68:71], v[76:79]\n"
    "v_pk_mul_f16 v60, v54, v58 op_sel_hi:[0,1]\n"
    "v_pk_mul_f16 v61, v54, v59 op_sel_hi:[0,1]\n"
    "v_mfma_f32_16x16x32_f16 v[76:79], v[128:131], v[72:75], v[76:79]\n"
    "v_pk_mul_f16 v62, v56, v58 op_sel_hi:[0,1]\n"
    "v_xor_b32 v3, 0x4000, v3\n"
    "v_mfma_f32_16x16x32_f16 v[80:83], v[132:135], v[72:75], v[80:83]\n"
    "v_pk_mul_f16 v63, v56, v59 op_sel_hi:[0,1]\n"
    "v_xor_b32 v4, 0x4000, v4\n"
    "v_mfma_f32_16x16x32_f16 v[80:83], v[120:123], v[68:71], v[80:83]\n"
    "v_pk_mul_f16 v64, v56, v56 op_sel_hi:[0,1]\n"
    "v_xor_b32 v5, 0x4000, v5\n"
    "v_mfma_f32_16x16x32_f16 v[84:87], v[124:127], v[68:71], v[84:87]\n"
    "v_pk_mul_f16 v65, v56, v49 op_sel_hi:[0,1]\n"
    "v_xor_b32 v6, 0x4000, v6\n"
    "v_mfma_f32_16x16x32_f16 v[84:87], v[136:139], v[72:75], v[84:87]\n"
    "s_cmp_lg_u32 s46, 0\n"
    "v_pk_mul_f16 v66, v48, v56 op_sel_hi:[0,1]\n"
    "v_smfmac_f32_16x16x64_f16 v[88:91], v[140:143], v[68:75], v2\n"
    "s_cbranch_scc1 .Lxd_ht36\n"
    ".Lxd_ht36r:\n"
    "v_pk_mul_f16 v67, v48, v57 op_sel_hi:[0,1]\n"
    "v_mfma_f32_16x16x32_f16 v[76:79], v[144:147], v[60:63], v[76:79]\n"
    "v_pk_mul_f16 v68, v50, v56 op_sel_hi:[0,1]\n"
    "v_pk_mul_f16 v69, v50, v57 op_sel_hi:[0,1]\n"
    "v_mfma_f32_16x16x32_f16 v[76:79], v[156:159], v[64:67], v[76:79]\n"
    "v_pk_mul_f16 v70, v52, v56 op_sel_hi:[0,1]\n"
    "v_mfma_f32_16x16x32_f16 v[80:83], v[160:163], v[64:67], v[80:83]\n"
    "v_pk_mul_f16 v71, v52, v57 op_sel_hi:[0,1]\n"
    "v_mfma_f32_16x16x32_f16 v[80:83], v[148:151], v[60:63], v[80:83]\n"
    "ds_read_b32 v22, v25 offset:520\n"
    "v_pk_mul_f16 v72, v54, v56 op_sel_hi:[0,1]\n"
    "v_mfma_f32_16x16x32_f16 v[84:87], v[152:155], v[60:63], v[84:87]\n"
    "v_pk_mul_f16 v73, v54, v57 op_sel_hi:[0,1]\n"
    "v_mfma_f32_16x16x32_f16 v[84:87], v[164:167], v[64:67], v[84:87]\n"
    "v_pk_mul_f16 v74, v54, v54 op_sel_hi:[0,1]\n"
    "v_smfmac_f32_16x16x64_f16 v[88:91], v[168:171], v[60:67], v2\n"
    "v_pk_mul_f16 v75, v54, v49 op_sel_hi:[0,1]\n"
    "v_mfma_f32_16x16x32_f16 v[76:79], v[172:175], v[68:71], v[76:79]\n"
    "v_pk_mul_f16 v60, v48, v54 op_sel_hi:[0,1]\n"
    "v_pk_mul_f16 v61, v48, v55 op_sel_hi:[0,1]\n"
    "v_mfma_f32_16x16x32_f16 v[76:79], v[184:187], v[72:75], v[76:79]\n"
    "s_waitcnt lgkmcnt(0)\n"
    "v_pk_mul_f16 v62, v50, v54 op_sel_hi:[0,1]\n"
    "v_readfirstlane_b32 s45, v22\n"
    "v_mfma_f32_16x16x32_f16 v[80:83], v[188:191], v[72:75], v[80:83]\n"
    "s_cmp_lt_i32 s45, 16\n"
    "v_pk_mul_f16 v63, v50, v55 op_sel_hi:[0,1]\n"
    "v_mfma_f32_16x16x32_f16 v[80:83], v[176:179], v[68:71], v[80:83]\n"
    "s_cbranch_scc1 .Lxd_ck37\n"
    ".Lxd_ck37r:\n"
    "v_pk_mul_f16 v64, v52, v54 op_sel_hi:[0,1]\n"
    "v_mfma_f32_16x16x32_f16 v[84:87], v[180:183], v[68:71], v[84:87]\n"
    "ds_read_b64_tr_b16 v[44:45], v5 offset:0\n"
    "v_pk_mul_f16 v65, v52, v55 op_sel_hi:[0,1]\n"
    "v_mfma_f32_16x16x32_f16 v[84:87], v[192:195], v[72:75], v[84:87]\n"
    "ds_read_b64_tr_b16 v[46:47], v6 offset:0\n"
    "v_pk_mul_f16 v66, v52, v52 op_sel_hi:[0,1]\n"
    "v_smfmac_f32_16x16x64_f16 v[88:91], v[196:199], v[68:75], v2\n"
    "v_pk_mul_f16 v67, v52, v49 op_sel_hi:[0,1]\n"
    "v_mfma_f32_16x16x32_f16 v[76:79], v[200:203], v[60:63], v[76:79]\n"
    "v_pk_mul_f16 v68, v48, v52 op_sel_hi:[0,1]\n"
    "v_pk_mul_f16 v69, v48, v53 op_sel_hi:[0,1]\n"
    "v_mfma_f32_16x16x32_f16 v[76:79], v[212:215], v[64:67], v[76:79]\n"
    "v_pk_mul_f16 v70, v50, v52 op_sel_hi:[0,1]\n"
    "v_mfma_f32_16x16x32_f16 v[80:83], v[216:219], v[64:67], v[80:83]\n"
    "s_cbranch_vccnz .Lxd_pr35\n"
    "s_setprio 0\n"
    ".Lxd_pr35:\n"
    "v_pk_mul_f16 v71, v50, v53 op_sel_hi:[0,1]\n"
    "v_mfma_f32_16x16x32_f16 v[80:83], v[204:207], v[60:63], v[80:83]\n"
    "v_pk_mul_f16 v72, v50, v50 op_sel_hi:[0,1]\n"
    "v_mfma_f32_16x16x32_f16 v[84:87], v[208:211], v[60:63], v[84:87]\n"
    "v_pk_mul_f16 v73, v50, v49 op_sel_hi:[0,1]\n"
    "v_pk_mul_f16 v74, v48, v50 op_sel_hi:[0,1]\n"
    "v_mfma_f32_16x16x32_f16 v[84:87], v[220:223], v[64:67], v[84:87]\n"
    "v_pk_mul_f16 v75, v48, v51 op_sel_hi:[0,1]\n"
    "v_smfmac_f32_16x16x64_f16 v[88:91], v[224:227], v[60:67], v2\n"
    "ds_read_b64_tr_b16 v[48:49], v3 offset:0\n"
    "ds_read_b64_tr_b16 v[58:59], v4 offset:2880\n"
    "v_smfmac_f32_16x16x64_f16 v[88:91], v[252:255], v[68:75], v2\n"
    "s_waitcnt lgkmcnt(2)\n"
    "v_pk_mul_f16 v60, v44, s45 op_sel:[0,1]\n"
    "v_pk_mul_f16 v61, v45, s45 op_sel:[0,1]\n"
    "v_mfma_f32_16x16x32_f16 v[84:87], v[236:239], v[68:71], v[84:87]\n"
    "ds_read_b64_tr_b16 v[50:51], v4 offset:576\n"
    "v_pk_mul_f16 v62, v46, s45 op_sel:[0,1]\n"
    "v_pk_mul_f16 v63, v47, s45 op_sel:[0,1]\n"
    "v_mfma_f32_16x16x32_f16 v[84:87], v[248:251], v[72:75], v[84:87]\n"
    "ds_read_b64_tr_b16 v[52:53], v4 offset:1152\n"
    "v_pk_mul_f16 v64, v44, v46\n"
    "v_pk_mul_f16 v65, v45, v47\n"
    "v_mfma_f32_16x16x32_f16 v[76:79], v[240:243], v[72:75], v[76:79]\n"
    "ds_read_b64_tr_b16 v[54:55], v4 offset:1728\n"
    "v_add_f32 v11, v88, v89\n"
    "v_add_f32 v13, v90, v91\n"
    "v_mfma_f32_16x16x32_f16 v[76:79], v[228:231], v[68:71], v[76:79]\n"
    "ds_read_b64_tr_b16 v[56:57], v4 offset:2304\n"
    "v_mov_b64 v[88:89], 0\n"
    "v_mov_b64 v[90:91], 0\n"
    "v_mfma_f32_16x16x32_f16 v[80:83], v[232:235], v[68:71], v[80:83]\n"
    "s_waitcnt lgkmcnt(4)\n"
    "v_pk_mul_f16 v66, v48, v48 op_sel_hi:[0,1]\n"
    "v_pk_mul_f16 v67, v48, v49 op_sel_hi:[0,1]\n"
    "v_mfma_f32_16x16x32_f16 v[80:83], v[244:247], v[72:75], v[80:83]\n"
    "v_mul_f32 v11, v11, v13\n"
    "v_fmac_f32 v11, v84, v85\n"
    "v_fmac_f32 v11, v86, v87\n"
    ".Lxd_pl2:\n"
    "v_smfmac_f32_16x16x64_f16 v[88:91], v[112:115], v[60:67], v2\n"
    "s_cbranch_vccnz .Lxd_pr38\n"
    "s_setprio 2\n"
    ".Lxd_pr38:\n"
    "v_pk_mul_f16 v68, v58, v58 op_sel_hi:[0,1]\n"
    "v_pk_mul_f16 v69, v58, v49 op_sel_hi:[0,1]\n"
    "v_mfma_f32_16x16x32_f16 v[84:87], v[108:111], v[64:67], 0\n"
    "v_fmac_f32 v11, v76, v80\n"
    "v_fmac_f32 v11, v77, v81\n"
    "v_mfma_f32_16x16x32_f16 v[84:87], v[96:99], v[60:63], v[84:87]\n"
    "v_fmac_f32 v11, v78, v82\n"
    "v_fma_f32 v8, v79, v83, v11\n"
    "v_mfma_f32_16x16x32_f16 v[80:83], v[92:95], v[60:63], 0\n"
    "s_waitcnt lgkmcnt(0)\n"
    "v_pk_mul_f16 v70, v48, v58 op_sel_hi:[0,1]\n"
    "v_pk_mul_f16 v71, v48, v59 op_sel_hi:[0,1]\n"
    "v_mfma_f32_16x16x32_f16 v[80:83], v[104:107], v[64:67], v[80:83]\n"
    "v_pk_mul_f16 v72, v50, v58 op_sel_hi:[0,1]\n"
    "v_pk_mul_f16 v73, v50, v59 op_sel_hi:[0,1]\n"
    "v_mfma_f32_16x16x32_f16 v[76:79], v[100:103], v[64:67], 0\n"
    "v_pk_mul_f16 v74, v52, v58 op_sel_hi:[0,1]\n"
    "v_pk_mul_f16 v75, v52, v59 op_sel_hi:[0,1]\n"
    "v_mfma_f32_16x16x32_f16 v[76:79], v[116:119], v[68:71], v[76:79]\n"
    "v_pk_mul_f16 v60, v54, v58 op_sel_hi:[0,1]\n"
    "v_pk_mul_f16 v61, v54, v59 op_sel_hi:[0,1]\n"
    "v_mfma_f32_16x16x32_f16 v[76:79], v[128:131], v[72:75], v[76:79]\n"
    "v_pk_mul_f16 v62, v56, v58 op_sel_hi:[0,1]\n"
    "v_mfma_f32_16x16x32_f16 v[80:83], v[132:135], v[72:75], v[80:83]\n"
    "v_pk_mul_f16 v63, v56, v59 op_sel_hi:[0,1]\n"
    "v_mfma_f32_16x16x32_f16 v[80:83], v[120:123], v[68:71], v[80:83]\n"
    "v_pk_mul_f16 v64, v56, v56 op_sel_hi:[0,1]\n"
    "v_mfma_f32_16x16x32_f16 v[84:87], v[124:127], v[68:71], v[84:87]\n"
    "v_pk_mul_f16 v65, v56, v49 op_sel_hi:[0,1]\n"
    "v_mfma_f32_16x16x32_f16 v[84:87], v[136:139], v[72:75], v[84:87]\n"
    "s_cmp_lg_u32 s46, 0\n"
    "v_pk_mul_f16 v66, v48, v56 op_sel_hi:[0,1]\n"
    "v_smfmac_f32_16x16x64_f16 v[88:91], v[140:143], v[68:75], v2\n"
    "s_cbranch_scc1 .Lxd_ht40\n"
    ".Lxd_ht40r:\n"
    "v_pk_mul_f16 v67, v48, v57 op_sel_hi:[0,1]\n"
    "v_mfma_f32_16x16x32_f16 v[76:79], v[144:147], v[60:63], v[76:79]\n"
    "v_pk_mul_f16 v68, v50, v56 op_sel_hi:[0,1]\n"
    "v_pk_mul_f16 v69, v50, v57 op_sel_hi:[0,1]\n"
    "v_mfma_f32_16x16x32_f16 v[76:79], v[156:159], v[64:67], v[76:79]\n"
    "v_pk_mul_f16 v70, v52, v56 op_sel_hi:[0,1]\n"
    "v_mfma_f32_16x16x32_f16 v[80:83], v[160:163], v[64:67], v[80:83]\n"
    "v_pk_mul_f16 v71, v52, v57 op_sel_hi:[0,1]\n"
    "v_mfma_f32_16x16x32_f16 v[80:83], v[148:151], v[60:63], v[80:83]\n"
    "ds_read_b32 v22, v25 offset:524\n"
    "v_pk_mul_f16 v72, v54, v56 op_sel_hi:[0,1]\n"
    "v_mfma_f32_16x16x32_f16 v[84:87], v[152:155], v[60:63], v[84:87]\n"
    "v_pk_mul_f16 v73, v54, v57 op_sel_hi:[0,1]\n"
    "v_mfma_f32_16x16x32_f16 v[84:87], v[164:167], v[64:67], v[84:87]\n"
    "v_pk_mul_f16 v74, v54, v54 op_sel_hi:[0,1]\n"
    "v_smfmac_f32_16x16x64_f16 v[88:91], v[168:171], v[60:67], v2\n"
    "v_pk_mul_f16 v75, v54, v49 op_sel_hi:[0,1]\n"
    "v_mfma_f32_16x16x32_f16 v[76:79], v[172:175], v[68:71], v[76:79]\n"
    "v_pk_mul_f16 v60, v48, v54 op_sel_hi:[0,1]\n"
    "v_pk_mul_f16 v61, v48, v55 op_sel_hi:[0,1]\n"
    "v_mfma_f32_16x16x32_f16 v[76:79], v[184:187], v[72:75], v[76:79]\n"
    "s_waitcnt lgkmcnt(0)\n"
    "v_pk_mul_f16 v62, v50, v54 op_sel_hi:[0,1]\n"
    "v_readfirstlane_b32 s45, v22\n"
    "v_mfma_f32_16x16x32_f16 v[80:83], v[188:191], v[72:75], v[80:83]\n"
    "s_cmp_lt_i32 s45, 16\n"
    "v_pk_mul_f16 v63, v50, v55 op_sel_hi:[0,1]\n"
    "v_mfma_f32_16x16x32_f16 v[80:83], v[176:179], v[68:71], v[80:83]\n"
    "s_cbranch_scc1 .Lxd_ck41\n"
    ".Lxd_ck41r:\n"
    "v_pk_mul_f16 v64, v52, v54 op_sel_hi:[0,1]\n"
    "v_mfma_f32_16x16x32_f16 v[84:87], v[180:183], v[68:71], v[84:87]\n"
    "ds_read_b64_tr_b16 v[44:45], v5 offset:4096\n"
    "v_pk_mul_f16 v65, v52, v55 op_sel_hi:[0,1]\n"
    "v_mfma_f32_16x16x32_f16 v[84:87], v[192:195], v[72:75], v[84:87]\n"
    "ds_read_b64_tr_b16 v[46:47], v6 offset:4096\n"
    "v_pk_mul_f16 v66, v52, v52 op_sel_hi:[0,1]\n"
    "v_smfmac_f32_16x16x64_f16 v[88:91], v[196:199], v[68:75], v2\n"
    "v_pk_mul_f16 v67, v52, v49 op_sel_hi:[0,1]\n"
    "v_mfma_f32_16x16x32_f16 v[76:79], v[200:203], v[60:63], v[76:79]\n"
    "v_pk_mul_f16 v68, v48, v52 op_sel_hi:[0,1]\n"
    "v_pk_mul_f16 v69, v48, v53 op_sel_hi:[0,1]\n"
    "v_mfma_f32_16x16x32_f16 v[76:79], v[212:215], v[64:67], v[76:79]\n"
    "v_pk_mul_f16 v70, v50, v52 op_sel_hi:[0,1]\n"
    "v_mfma_f32_16x16x32_f16 v[80:83], v[216:219], v[64:67], v[80:83]\n"
    "s_cbranch_vccnz .Lxd_pr39\n"
    "s_setprio 0\n"
    ".Lxd_pr39:\n"
    "v_pk_mul_f16 v71, v50, v53 op_sel_hi:[0,1]\n"
    "v_mfma_f32_16x16x32_f16 v[80:83], v[204:207], v[60:63], v[80:83]\n"
    "v_pk_mul_f16 v72, v50, v50 op_sel_hi:[0,1]\n"
    "v_mfma_f32_16x16x32_f16 v[84:87], v[208:211], v[60:63], v[84:87]\n"
    "v_pk_mul_f16 v73, v50, v49 op_sel_hi:[0,1]\n"
    "v_pk_mul_f16 v74, v48, v50 op_sel_hi:[0,1]\n"
    "v_mfma_f32_16x16x32_f16 v[84:87], v[220:223], v[64:67], v[84:87]\n"
    "v_pk_mul_f16 v75, v48, v51 op_sel_hi:[0,1]\n"
    "v_smfmac_f32_16x16x64_f16 v[88:91], v[224:227], v[60:67], v2\n"
    "ds_read_b64_tr_b16 v[48:49], v3 offset:4096\n"
    "ds_read_b64_tr_b16 v[58:59], v4 offset:6976\n"
    "v_smfmac_f32_16x16x64_f16 v[88:91], v[252:255], v[68:75], v2\n"
    "s_waitcnt lgkmcnt(2)\n"
    "v_pk_mul_f16 v60, v44, s45 op_sel:[0,1]\n"
    "v_pk_mul_f16 v61, v45, s45 op_sel:[0,1]\n"
    "v_mfma_f32_16x16x32_f16 v[84:87], v[236:239], v[68:71], v[84:87]\n"
    "ds_read_b64_tr_b16 v[50:51], v4 offset:4672\n"
    "v_pk_mul_f16 v62, v46, s45 op_sel:[0,1]\n"
    "v_pk_mul_f16 v63, v47, s45 op_sel:[0,1]\n"
    "v_mfma_f32_16x16x32_f16 v[84:87], v[248:251], v[72:75], v[84:87]\n"
    "ds_read_b64_tr_b16 v[52:53], v4 offset:5248\n"
    "v_pk_mul_f16 v64, v44, v46\n"
    "v_pk_mul_f16 v65, v45, v47\n"
    "v_mfma_f32_16x16x32_f16 v[76:79], v[240:243], v[72:75], v[76:79]\n"
    "ds_read_b64_tr_b16 v[54:55], v4 offset:5824\n"
    "v_add_f32 v11, v88, v89\n"
    "v_add_f32 v13, v90, v91\n"
    "v_mfma_f32_16x16x32_f16 v[76:79], v[228:231], v[68:71], v[76:79]\n"
    "ds_read_b64_tr_b16 v[56:57], v4 offset:6400\n"
    "v_mov_b64 v[88:89], 0\n"
    "v_mov_b64 v[90:91], 0\n"
    "v_mfma_f32_16x16x32_f16 v[80:83], v[232:235], v[68:71], v[80:83]\n"
    "s_waitcnt lgkmcnt(4)\n"
    "v_pk_mul_f16 v66, v48, v48 op_sel_hi:[0,1]\n"
    "v_pk_mul_f16 v67, v48, v49 op_sel_hi:[0,1]\n"
    "v_mfma_f32_16x16x32_f16 v[80:83], v[244:247], v[72:75], v[80:83]\n"
    "v_mul_f32 v11, v11, v13\n"
    "v_fmac_f32 v11, v84, v85\n"
    "v_fmac_f32 v11, v86, v87\n"
    ".Lxd_pl3:\n"
    "v_smfmac_f32_16x16x64_f16 v[88:91], v[112:115], v[60:67], v2\n"
    "s_cbranch_vccnz .Lxd_pr42\n"
    "s_setprio 2\n"
    ".Lxd_pr42:\n"
    "v_pk_mul_f16 v68, v58, v58 op_sel_hi:[0,1]\n"
    "v_pk_mul_f16 v69, v58, v49 op_sel_hi:[0,1]\n"
    "v_mfma_f32_16x16x32_f16 v[84:87], v[108:111], v[64:67], 0\n"
    "v_fmac_f32 v11, v76, v80\n"
    "v_fmac_f32 v11, v77, v81\n"
    "v_mfma_f32_16x16x32_f16 v[84:87], v[96:99], v[60:63], v[84:87]\n"
    "v_fmac_f32 v11, v78, v82\n"
    "v_fma_f32 v9, v79, v83, v11\n"
    "v_mfma_f32_16x16x32_f16 v[80:83], v[92:95], v[60:63], 0\n"
    "s_waitcnt lgkmcnt(0)\n"
    "v_pk_mul_f16 v70, v48, v58 op_sel_hi:[0,1]\n"
    "v_pk_mul_f16 v71, v48, v59 op_sel_hi:[0,1]\n"
    "v_mfma_f32_16x16x32_f16 v[80:83], v[104:107], v[64:67], v[80:83]\n"
    "v_pk_mul_f16 v72, v50, v58 op_sel_hi:[0,1]\n"
    "v_pk_mul_f16 v73, v50, v59 op_sel_hi:[0,1]\n"
    "v_mfma_f32_16x16x32_f16 v[76:79], v[100:103], v[64:67], 0\n"
    "v_pk_mul_f16 v74, v52, v58 op_sel_hi:[0,1]\n"
    "v_pk_mul_f16 v75, v52, v59 op_sel_hi:[0,1]\n"
    "v_mfma_f32_16x16x32_f16 v[76:79], v[116:119], v[68:71], v[76:79]\n"
    "v_pk_mul_f16 v60, v54, v58 op_sel_hi:[0,1]\n"
    "v_pk_mul_f16 v61, v54, v59 op_sel_hi:[0,1]\n"
    "v_mfma_f32_16x16x32_f16 v[76:79], v[128:131], v[72:75], v[76:79]\n"
    "v_pk_mul_f16 v62, v56, v58 op_sel_hi:[0,1]\n"
    "v_mfma_f32_16x16x32_f16 v[80:83], v[132:135], v[72:75], v[80:83]\n"
    "v_pk_mul_f16 v63, v56, v59 op_sel_hi:[0,1]\n"
    "v_mfma_f32_16x16x32_f16 v[80:83], v[120:123], v[68:71], v[80:83]\n"
    "v_pk_mul_f16 v64, v56, v56 op_sel_hi:[0,1]\n"
    "v_mfma_f32_16x16x32_f16 v[84:87], v[124:127], v[68:71], v[84:87]\n"
    "v_pk_mul_f16 v65, v56, v49 op_sel_hi:[0,1]\n"
    "v_mfma_f32_16x16x32_f16 v[84:87], v[136:139], v[72:75], v[84:87]\n"
    "v_pk_mul_f16 v66, v48, v56 op_sel_hi:[0,1]\n"
    "v_smfmac_f32_16x16x64_f16 v[88:91], v[140:143], v[68:75], v2\n"
    "v_pk_mul_f16 v67, v48, v57 op_sel_hi:[0,1]\n"
    "v_mfma_f32_16x16x32_f16 v[76:79], v[144:147], v[60:63], v[76:79]\n"
    "v_pk_mul_f16 v68, v50, v56 op_sel_hi:[0,1]\n"
    "v_pk_mul_f16 v69, v50, v57 op_sel_hi:[0,1]\n"
    "v_mfma_f32_16x16x32_f16 v[76:79], v[156:159], v[64:67], v[76:79]\n"
    "s_cmp_eq_u32 s36, 0\n"
    "s_cbranch_scc1 .Lxd_on45\n"
    ".Lxd_on45r:\n"
    "v_pk_mul_f16 v70, v52, v56 op_sel_hi:[0,1]\n"
    "v_mfma_f32_16x16x32_f16 v[80:83], v[160:163], v[64:67], v[80:83]\n"
    "v_pk_mul_f16 v71, v52, v57 op_sel_hi:[0,1]\n"
    "v_mfma_f32_16x16x32_f16 v[80:83], v[148:151], v[60:63], v[80:83]\n"
    "ds_read_b32 v22, v25 offset:528\n"
    "v_pk_mul_f16 v72, v54, v56 op_sel_hi:[0,1]\n"
    "v_mfma_f32_16x16x32_f16 v[84:87], v[152:155], v[60:63], v[84:87]\n"
    "v_pk_mul_f16 v73, v54, v57 op_sel_hi:[0,1]\n"
    "v_mfma_f32_16x16x32_f16 v[84:87], v[164:167], v[64:67], v[84:87]\n"
    "v_pk_mul_f16 v74, v54, v54 op_sel_hi:[0,1]\n"
    "v_smfmac_f32_16x16x64_f16 v[88:91], v[168:171], v[60:67], v2\n"
    "v_pk_mul_f16 v75, v54, v49 op_sel_hi:[0,1]\n"
    "v_mfma_f32_16x16x32_f16 v[76:79], v[172:175], v[68:71], v[76:79]\n"
    "v_pk_mul_f16 v60, v48, v54 op_sel_hi:[0,1]\n"
    "v_pk_mul_f16 v61, v48, v55 op_sel_hi:[0,1]\n"
    "v_mfma_f32_16x16x32_f16 v[76:79], v[184:187], v[72:75], v[76:79]\n"
    "s_waitcnt lgkmcnt(0)\n"
    "v_pk_mul_f16 v62, v50, v54 op_sel_hi:[0,1]\n"
    "v_readfirstlane_b32 s45, v22\n"
    "v_mfma_f32_16x16x32_f16 v[80:83], v[188:191], v[72:75], v[80:83]\n"
    "s_cmp_lt_i32 s45, 16\n"
    "v_pk_mul_f16 v63, v50, v55 op_sel_hi:[0,1]\n"
    "v_mfma_f32_16x16x32_f16 v[80:83], v[176:179], v[68:71], v[80:83]\n"
    "s_cbranch_scc1 .Lxd_ck46\n"
    ".Lxd_ck46r:\n"
    "v_pk_mul_f16 v64, v52, v54 op_sel_hi:[0,1]\n"
    "v_mfma_f32_16x16x32_f16 v[84:87], v[180:183], v[68:71], v[84:87]\n"
    "ds_read_b64_tr_b16 v[44:45], v5 offset:8192\n"
    "v_pk_mul_f16 v65, v52, v55 op_sel_hi:[0,1]\n"
    "v_mfma_f32_16x16x32_f16 v[84:87], v[192:195], v[72:75], v[84:87]\n"
    "ds_read_b64_tr_b16 v[46:47], v6 offset:8192\n"
    "v_pk_mul_f16 v66, v52, v52 op_sel_hi:[0,1]\n"
    "v_smfmac_f32_16x16x64_f16 v[88:91], v[196:199], v[68:75], v2\n"
    "v_pk_mul_f16 v67, v52, v49 op_sel_hi:[0,1]\n"
    "v_mfma_f32_16x16x32_f16 v[76:79], v[200:203], v[60:63], v[76:79]\n"
    "v_pk_mul_f16 v68, v48, v52 op_sel_hi:[0,1]\n"
    "v_pk_mul_f16 v69, v48, v53 op_sel_hi:[0,1]\n"
    "v_mfma_f32_16x16x32_f16 v[76:79], v[212:215], v[64:67], v[76:79]\n"
    "v_pk_mul_f16 v70, v50, v52 op_sel_hi:[0,1]\n"
    "v_mfma_f32_16x16x32_f16 v[80:83], v[216:219], v[64:67], v[80:83]\n"
    "s_cbranch_vccnz .Lxd_pr43\n"
    "s_setprio 0\n"
    ".Lxd_pr43:\n"
    "v_pk_mul_f16 v71, v50, v53 op_sel_hi:[0,1]\n"
    "v_mfma_f32_16x16x32_f16 v[80:83], v[204:207], v[60:63], v[80:83]\n"
    "v_pk_mul_f16 v72, v50, v50 op_sel_hi:[0,1]\n"
    "v_mfma_f32_16x16x32_f16 v[84:87], v[208:211], v[60:63], v[84:87]\n"
    "v_pk_mul_f16 v73, v50, v49 op_sel_hi:[0,1]\n"
    "v_pk_mul_f16 v74, v48, v50 op_sel_hi:[0,1]\n"
    "v_mfma_f32_16x16x32_f16 v[84:87], v[220:223], v[64:67], v[84:87]\n"
    "v_pk_mul_f16 v75, v48, v51 op_sel_hi:[0,1]\n"
    "v_smfmac_f32_16x16x64_f16 v[88:91], v[224:227], v[60:67], v2\n"
    "ds_read_b64_tr_b16 v[48:49], v3 offset:8192\n"
    "ds_read_b64_tr_b16 v[58:59], v4 offset:11072\n"
    "v_add_u32 v25, 16, v25\n"
    "v_smfmac_f32_16x16x64_f16 v[88:91], v[252:255], v[68:75], v2\n"
    "s_waitcnt lgkmcnt(2)\n"
    "v_pk_mul_f16 v60, v44, s45 op_sel:[0,1]\n"
    "v_pk_mul_f16 v61, v45, s45 op_sel:[0,1]\n"
    "v_mfma_f32_16x16x32_f16 v[84:87], v[236:239], v[68:71], v[84:87]\n"
    "ds_read_b64_tr_b16 v[50:51], v4 offset:8768\n"
    "v_pk_mul_f16 v62, v46, s45 op_sel:[0,1]\n"
    "v_pk_mul_f16 v63, v47, s45 op_sel:[0,1]\n"
    "v_mfma_f32_16x16x32_f16 v[84:87], v[248:251], v[72:75], v[84:87]\n"
    "ds_read_b64_tr_b16 v[52:53], v4 offset:9344\n"
    "v_pk_mul_f16 v64, v44, v46\n"
    "v_pk_mul_f16 v65, v45, v47\n"
    "v_mfma_f32_16x16x32_f16 v[76:79], v[240:243], v[72:75], v[76:79]\n"
    "ds_read_b64_tr_b16 v[54:55], v4 offset:9920\n"
    "v_add_f32 v11, v88, v89\n"
    "v_add_f32 v13, v90, v91\n"
    "v_mfma_f32_16x16x32_f16 v[76:79], v[228:231], v[68:71], v[76:79]\n"
    "ds_read_b64_tr_b16 v[56:57], v4 offset:10496\n"
    "v_mov_b64 v[88:89], 0\n"
    "v_mov_b64 v[90:91], 0\n"
    "v_mfma_f32_16x16x32_f16 v[80:83], v[232:235], v[68:71], v[80:83]\n"
    "s_waitcnt lgkmcnt(4)\n"
    "v_pk_mul_f16 v66, v48, v48 op_sel_hi:[0,1]\n"
    "v_pk_mul_f16 v67, v48, v49 op_sel_hi:[0,1]\n"
    "v_mfma_f32_16x16x32_f16 v[80:83], v[244:247], v[72:75], v[80:83]\n"
    "s_cmp_eq_u32 s36, 0\n"
    "v_mul_f32 v11, v11, v13\n"
    "v_fmac_f32 v11, v84, v85\n"
    "v_fmac_f32 v11, v86, v87\n"
    "s_cbranch_scc1 .Lxd_ph0\n"
    "s_branch .Lxd_pl0\n"
    ".Lxd_last:\n"
    "s_cbranch_vccnz .Lxd_pr47\n"
    "s_setprio 0\n"
    ".Lxd_pr47:\n"
    "v_pk_mul_f16 v64, v52, v54 op_sel_hi:[0,1]\n"
    "v_mfma_f32_16x16x32_f16 v[84:87], v[180:183], v[68:71], v[84:87]\n"
    "v_pk_mul_f16 v65, v52, v55 op_sel_hi:[0,1]\n"
    "v_mfma_f32_16x16x32_f16 v[84:87], v[192:195], v[72:75], v[84:87]\n"
    "v_pk_mul_f16 v66, v52, v52 op_sel_hi:[0,1]\n"
    "v_smfmac_f32_16x16x64_f16 v[88:91], v[196:199], v[68:75], v2\n"
    "v_pk_mul_f16 v67, v52, v49 op_sel_hi:[0,1]\n"
    "v_mfma_f32_16x16x32_f16 v[76:79], v[200:203], v[60:63], v[76:79]\n"
    "v_pk_mul_f16 v68, v48, v52 op_sel_hi:[0,1]\n"
    "v_pk_mul_f16 v69, v48, v53 op_sel_hi:[0,1]\n"
    "v_mfma_f32_16x16x32_f16 v[76:79], v[212:215], v[64:67], v[76:79]\n"
    "v_pk_mul_f16 v70, v50, v52 op_sel_hi:[0,1]\n"
    "v_mfma_f32_16x16x32_f16 v[80:83], v[216:219], v[64:67], v[80:83]\n"
    "v_pk_mul_f16 v71, v50, v53 op_sel_hi:[0,1]\n"
    "v_mfma_f32_16x16x32_f16 v[80:83], v[204:207], v[60:63], v[80:83]\n"
    "v_pk_mul_f16 v72, v50, v50 op_sel_hi:[0,1]\n"
    "v_mfma_f32_16x16x32_f16 v[84:87], v[208:211], v[60:63], v[84:87]\n"
    "v_pk_mul_f16 v73, v50, v49 op_sel_hi:[0,1]\n"
    "v_pk_mul_f16 v74, v48, v50 op_sel_hi:[0,1]\n"
    "v_mfma_f32_16x16x32_f16 v[84:87], v[220:223], v[64:67], v[84:87]\n"
    "v_pk_mul_f16 v75, v48, v51 op_sel_hi:[0,1]\n"
    "v_smfmac_f32_16x16x64_f16 v[88:91], v[224:227], v[60:67], v2\n"
    "s_nop 0\n"
    "v_smfmac_f32_16x16x64_f16 v[88:91], v[252:255], v[68:75], v2\n"
    "v_mfma_f32_16x16x32_f16 v[84:87], v[236:239], v[68:71], v[84:87]\n"
    "v_mfma_f32_16x16x32_f16 v[84:87], v[248:251], v[72:75], v[84:87]\n"
    "v_mfma_f32_16x16x32_f16 v[76:79], v[240:243], v[72:75], v[76:79]\n"
    "v_mfma_f32_16x16x32_f16 v[76:79], v[228:231], v[68:71], v[76:79]\n"
    "v_mfma_f32_16x16x32_f16 v[80:83], v[232:235], v[68:71], v[80:83]\n"
    "v_mfma_f32_16x16x32_f16 v[80:83], v[244:247], v[72:75], v[80:83]\n"
    "s_nop 1\n"
    "v_add_f32 v11, v88, v89\n"
    "v_add_f32 v13, v90, v91\n"
    "v_mul_f32 v11, v11, v13\n"
    "v_fmac_f32 v11, v84, v85\n"
    "v_fmac_f32 v11, v86, v87\n"
    "s_nop 0\n"
    "v_fmac_f32 v11, v76, v80\n"
    "v_fmac_f32 v11, v77, v81\n"
    "v_fmac_f32 v11, v78, v82\n"
    "s_setpc_b64 s[60:61]\n"
    ".Lxd_fin:\n"
    "v_lshl_add_u32 v68, v18, 6, v20\n"
    "v_mov_b32 v67, 154144\n"
    "v_permlane32_swap_b32 v7, v8\n"
    "v_permlane32_swap_b32 v9, v10\n"
    "v_mul_f32 v7, v0, v7\n"
    "v_mul_f32 v9, v0, v9\n"
    "v_fmac_f32 v7, v23, v8\n"
    "v_fmac_f32 v9, v23, v10\n"
    "ds_read_b32 v67, v67\n"
    "v_lshlrev_b32 v69, 2, v68\n"
    "v_permlane16_swap_b32 v7, v9\n"
    "v_add_f32 v7, v7, v9\n"
    "v_add_u32 v68, 150528, v68\n"
    "v_add_u32 v69, 152128, v69\n"
    "v_add_f32_dpp v7, v7, v7 quad_perm:[1,0,3,2] row_mask:0xf bank_mask:0xf\n"
    "s_lshl_b32 s26, s26, 16\n"
    "s_nop 0\n"
    "v_add_f32_dpp v7, v7, v7 quad_perm:[2,3,0,1] row_mask:0xf bank_mask:0xf\n"
    "s_nop 1\n"
    "v_add_f32_dpp v7, v7, v7 row_half_mirror row_mask:0xf bank_mask:0xf\n"
    "s_nop 1\n"
    "v_add_f32_dpp v7, v7, v7 row_mirror row_mask:0xf bank_mask:0xf\n"
    "ds_write_b32 v14, v7\n"
    "s_cmp_lg_u32 s56, 2\n"
    "s_cbranch_scc1 .Lxd_f2\n"
    ".Lxd_f3:\n"
    "s_waitcnt vmcnt(0) lgkmcnt(0)\n"
    "s_barrier\n"
    "s_cmp_lg_u32 s20, 0\n"
    "s_cbranch_scc1 .Lxd_done\n"
    "ds_read_b128 v[60:63], v69\n"
    "ds_read_b32 v64, v68\n"
    "ds_read_u16 v65, v68 offset:512\n"
    "ds_read_b32 v66, v68 offset:1024\n"
    "s_waitcnt lgkmcnt(3)\n"
    "v_pk_add_f32 v[60:61], v[60:61], v[62:63]\n"
    "s_waitcnt lgkmcnt(0)\n"
    "v_cmp_lt_i32 vcc, 0, v64\n"
    "v_lshrrev_b32 v65, 4, v65\n"
    "v_add_f32 v60, v60, v61\n"
    "v_lshl_add_u32 v65, v65, 2, v67\n"
    "v_add_f32 v66, s26, v66\n"
    "v_ldexp_f32 v60, v60, v65\n"
    "s_mov_b64 exec, vcc\n"
    "v_add_f32 v60, v60, v66\n"
    "v_cvt_pk_bf16_f32 v60, v60, v60\n"
    "global_store_short v64, v60, s[70:71] offset:-2\n"
    "s_mov_b64 exec, -1\n"
    ".Lxd_done:\n"
    "s_endpgm\n"
    ".Lxd_cin:\n"
    "s_lshr_b32 s20, s43, 6\n"
    "v_and_b32 v1, 63, v0\n"
    "v_lshlrev_b32 v42, 4, v1\n"
    "s_and_b32 s40, s2, 3\n"
    "s_lshl2_add_u32 s23, s40, s20\n"
    "s_lshl1_add_u32 s41, s40, s20\n"
    "s_add_u32 s41, s41, 12\n"
    "s_cmp_ge_u32 s20, 4\n"
    "s_cselect_b32 s23, s41, s23\n"
    "s_add_u32 s41, s40, 24\n"
    "s_cmp_ge_u32 s20, 6\n"
    "s_cselect_b32 s23, s41, s23\n"
    "s_cselect_b32 s24, 1, 0\n"
    "s_min_u32 s34, s23, 25\n"
    "s_lshr_b32 s35, s2, 2\n"
    "v_and_b32 v190, 31, v0\n"
    "v_min_u32 v190, 25, v190\n"
    "v_mul_u32_u24 v190, 52, v190\n"
    "v_bfe_u32 v191, v0, 5, 1\n"
    "v_lshl_add_u32 v190, v191, 4, v190\n"
    "s_mul_i32 s40, s34, 1352\n"
    "s_xor_b32 s66, s20, 7\n"
    "s_or_b32 s66, s66, s2\n"
    "s_waitcnt lgkmcnt(0)\n"
    "s_load_dwordx4 s[68:71], s[0:1], 64\n"
    "s_add_u32 s38, s12, s40\n"
    "s_addc_u32 s39, s13, 0\n"
    "s_cmp_lg_u32 s66, 0\n"
    "s_cbranch_scc1 .Lxd_on48\n"
    "v_and_b32 v188, 31, v0\n"
    "v_lshlrev_b32 v188, 7, v188\n"
    "v_add_u32 v188, 32768, v188\n"
    "v_mov_b32 v176, 114\n"
    "global_store_dword v188, v176, s[16:17] sc1\n"
    "global_load_dwordx4 v[180:183], v42, s[16:17] sc1\n"
    ".Lxd_on48:\n"
    "global_load_dwordx4 v[80:83], v190, s[38:39] offset:0\n"
    "global_load_dwordx4 v[84:87], v190, s[38:39] offset:32\n"
    "s_getpc_b64 s[60:61]\n"
    "s_add_u32 s60, s60, xd_tab@rel32@lo+4\n"
    "s_addc_u32 s61, s61, xd_tab@rel32@hi+12\n"
    "v_lshl_add_u32 v188, s24, 10, v42\n"
    "s_lshl1_add_u32 s40, s24, s35\n"
    "v_lshl_add_u32 v189, s40, 11, v42\n"
    "s_load_dword s25, s[16:17], 0\n"
    "global_load_dwordx4 v[44:47], v188, s[60:61]\n"
    "global_load_dwordx4 v[48:51], v189, s[60:61] offset:2048\n"
    "global_load_dwordx4 v[52:55], v189, s[60:61] offset:3072\n"
    "s_setprio 0\n"
    "v_mov_b64 v[188:189], 0\n"
    "v_lshlrev_b32 v190, 3, v0\n"
    "v_lshrrev_b32 v186, 5, v1\n"
    "ds_write_b64 v190, v[188:189] offset:36864\n"
    "v_bfe_u32 v190, v0, 1, 4\n"
    "v_lshlrev_b32 v72, 2, v186\n"
    "v_sub_u32 v190, v190, v72\n"
    "v_and_b32 v72, 16, v42\n"
    "s_movk_i32 s40, 0x3f80\n"
    "v_lshlrev_b32_e64 v191, v72, s40\n"
    "v_cmp_eq_u32 s[60:61], 0, v190\n"
    "v_cmp_eq_u32 s[62:63], 1, v190\n"
    "v_cmp_eq_u32 s[64:65], 2, v190\n"
    "v_cmp_eq_u32 s[50:51], 3, v190\n"
    "v_cndmask_b32 v72, 0, v191, s[60:61]\n"
    "v_cndmask_b32 v73, 0, v191, s[62:63]\n"
    "v_cndmask_b32 v74, 0, v191, s[64:65]\n"
    "v_cndmask_b32 v75, 0, v191, s[50:51]\n"
    "v_cmp_eq_u32 s[60:61], 8, v190\n"
    "v_cmp_eq_u32 s[62:63], 9, v190\n"
    "v_cmp_eq_u32 s[64:65], 10, v190\n"
    "v_cmp_eq_u32 s[50:51], 11, v190\n"
    "v_cndmask_b32 v76, 0, v191, s[60:61]\n"
    "v_cndmask_b32 v77, 0, v191, s[62:63]\n"
    "v_cndmask_b32 v78, 0, v191, s[64:65]\n"
    "v_cndmask_b32 v79, 0, v191, s[50:51]\n"
    "s_lshl_b32 s30, s20, 12\n"
    "s_add_u32 s30, s30, 65536\n"
    "v_and_b32 v188, 31, v0\n"
    "v_mul_u32_u24 v192, 528, v186\n"
    "v_lshl_add_u32 v192, v188, 2, v192\n"
    "v_add_u32 v192, s30, v192\n"
    "s_lshl_b32 s40, s35, 6\n"
    "s_add_u32 s40, s40, s20\n"
    "s_lshl1_add_u32 s41, s20, s35\n"
    "s_add_u32 s41, s41, 120\n"
    "s_cmp_ge_u32 s20, 4\n"
    "s_cselect_b32 s40, s41, s40\n"
    "s_lshl1_add_u32 s41, s35, 384\n"
    "s_cmp_ge_u32 s20, 6\n"
    "s_cselect_b32 s40, s41, s40\n"
    "s_cselect_b32 s42, 1, 192\n"
    "s_and_b32 s41, s2, 3\n"
    "s_lshl2_add_u32 s40, s41, s40\n"
    "v_lshrrev_b32 v188, 3, v1\n"
    "v_mul_u32_u24 v188, 448, v188\n"
    "v_bfe_u32 v189, v0, 2, 1\n"
    "v_mad_u32_u24 v188, v189, s42, v188\n"
    "v_and_b32 v189, 48, v42\n"
    "v_add3_u32 v188, v188, v189, s40\n"
    "v_lshlrev_b32 v193, 4, v188\n"
    "v_mov_b32 v177, 0\n"
    "s_waitcnt vmcnt(0) lgkmcnt(0)\n"
    "s_cmp_lg_u32 s66, 0\n"
    "s_cbranch_scc1 .Lxd_on49\n"
    "s_mov_b32 s40, 0x9e3779b1\n"
    "v_mul_lo_u32 v144, v180, s40\n"
    "v_xor_b32 v145, 0xa5c30f96, v180\n"
    "v_alignbit_b32 v144, s25, v144, 24\n"
    "v_mul_lo_u32 v146, v181, s40\n"
    "v_xor_b32 v147, 0xa5c30f96, v181\n"
    "v_alignbit_b32 v146, s25, v146, 24\n"
    "v_mul_lo_u32 v148, v182, s40\n"
    "v_xor_b32 v149, 0xa5c30f96, v182\n"
    "v_alignbit_b32 v148, s25, v148, 24\n"
    "v_mul_lo_u32 v150, v183, s40\n"
    "v_xor_b32 v151, 0xa5c30f96, v183\n"
    "v_alignbit_b32 v150, s25, v150, 24\n"
    "v_lshlrev_b32 v180, 1, v42\n"
    "s_add_u32 s40, s16, 4096\n"
    "s_addc_u32 s41, s17, 0\n"
    "global_store_dwordx4 v180, v[144:147], s[40:41] sc1\n"
    "global_store_dwordx4 v180, v[148:151], s[40:41] offset:16 sc1\n"
    ".Lxd_on49:\n"
    "s_barrier\n"
    "s_branch .Lxd_bD\n"
    ".Lxd_s2:\n"
    "s_cmp_lt_u32 s2, 4\n"
    "s_cbranch_scc1 .Lxd_cin\n"
    ".Lxd_bR:\n"
    "s_lshr_b32 s20, s43, 6\n"
    "v_and_b32 v1, 63, v0\n"
    "v_lshlrev_b32 v42, 4, v1\n"
    "s_mul_i32 s40, s20, 4480\n"
    "s_min_u32 s40, s40, 30672\n"
    "s_mov_b32 m0, s40\n"
    "s_add_u32 s40, s40, 70304\n"
    "s_waitcnt lgkmcnt(0)\n"
    "s_load_dwordx4 s[68:71], s[0:1], 64\n"
    "s_add_u32 s38, s12, s40\n"
    "s_addc_u32 s39, s13, 0\n"
    "global_load_lds_dwordx4 v42, s[38:39] offset:0\n"
    "global_load_lds_dwordx4 v42, s[38:39] offset:1024\n"
    "global_load_lds_dwordx4 v42, s[38:39] offset:2048\n"
    "global_load_lds_dwordx4 v42, s[38:39] offset:3072\n"
    "global_load_lds_dwordx4 v42, s[38:39] offset:3456\n"
    "s_and_b32 s40, s2, 3\n"
    "s_lshl2_add_u32 s23, s40, s20\n"
    "s_lshl1_add_u32 s41, s40, s20\n"
    "s_add_u32 s41, s41, 12\n"
    "s_cmp_ge_u32 s20, 4\n"
    "s_cselect_b32 s23, s41, s23\n"
    "s_add_u32 s41, s40, 24\n"
    "s_cmp_ge_u32 s20, 6\n"
    "s_cselect_b32 s23, s41, s23\n"
    "s_cselect_b32 s24, 1, 0\n"
    "s_min_u32 s34, s23, 25\n"
    "s_lshr_b32 s35, s2, 2\n"
    "v_lshrrev_b32 v190, 2, v1\n"
    "v_and_b32 v188, 48, v42\n"
    "s_movk_i32 s42, 1352\n"
    "v_min_u32 v191, 9, v190\n"
    "v_mad_u32_u24 v190, v190, s42, v188\n"
    "v_mad_u32_u24 v191, v191, s42, v188\n"
    "v_add_u32 v191, 20608, v191\n"
    "s_mul_i32 s41, s34, 52\n"
    "s_add_u32 s41, s41, 35152\n"
    "s_cmp_lg_u32 s20, 7\n"
    "s_cbranch_scc1 .Lxd_on50\n"
    "v_min_u32 v188, 38, v1\n"
    "s_mov_b32 m0, 40960\n"
    "v_lshlrev_b32 v188, 2, v188\n"
    "global_load_lds_dword v188, s[14:15]\n"
    ".Lxd_on50:\n"
    "s_lshl_b32 s28, s20, 11\n"
    "s_add_u32 s28, s28, 49152\n"
    "s_mov_b32 m0, s28\n"
    "s_add_u32 s38, s12, s41\n"
    "s_addc_u32 s39, s13, 0\n"
    "global_load_lds_dwordx4 v190, s[38:39] offset:0\n"
    "global_load_lds_dwordx4 v191, s[38:39] offset:1024\n"
    "s_getpc_b64 s[60:61]\n"
    "s_add_u32 s60, s60, xd_tab@rel32@lo+4\n"
    "s_addc_u32 s61, s61, xd_tab@rel32@hi+12\n"
    "v_lshl_add_u32 v188, s24, 10, v42\n"
    "s_lshl1_add_u32 s40, s24, s35\n"
    "v_lshl_add_u32 v189, s40, 11, v42\n"
    "s_load_dword s25, s[16:17], 0\n"
    "global_load_dwordx4 v[44:47], v188, s[60:61]\n"
    "global_load_dwordx4 v[48:51], v189, s[60:61] offset:2048\n"
    "global_load_dwordx4 v[52:55], v189, s[60:61] offset:3072\n"
    "s_setprio 0\n"
    "v_mov_b64 v[188:189], 0\n"
    "v_lshlrev_b32 v190, 3, v0\n"
    "v_lshrrev_b32 v186, 5, v1\n"
    "ds_write_b64 v190, v[188:189] offset:36864\n"
    "v_lshrrev_b32 v184, 4, v1\n"
    "v_and_b32 v185, 15, v0\n"
    "v_lshrrev_b32 v190, 2, v185\n"
    "v_lshl_add_u32 v64, v184, 3, v190\n"
    "v_add_u32 v65, 4, v64\n"
    "s_lshr_b32 s21, s20, 2\n"
    "s_and_b32 s43, s20, 3\n"
    "s_mul_i32 s40, s21, 13\n"
    "s_lshl2_add_u32 s40, s43, s40\n"
    "v_and_b32 v191, 3, v0\n"
    "v_add_lshl_u32 v188, v191, s40, 3\n"
    "v_min_u32 v64, 25, v64\n"
    "v_mad_u32_u24 v64, v64, s42, v188\n"
    "v_min_u32 v65, 25, v65\n"
    "v_mad_u32_u24 v65, v65, s42, v188\n"
    "v_lshl_add_u32 v188, s43, 4, v185\n"
    "v_cmp_lt_u32 vcc, 25, v188\n"
    "v_cmp_lt_u32 s[60:61], 51, v188\n"
    "v_lshl_add_u32 v66, s21, 6, v188\n"
    "v_cndmask_b32_e64 v189, 0, 6, vcc\n"
    "v_add_lshl_u32 v66, v66, v189, 1\n"
    "v_mov_b32 v189, 0x40000\n"
    "v_cmp_gt_u32 vcc, 26, v1\n"
    "v_cndmask_b32 v66, v66, v189, s[60:61]\n"
    "v_lshlrev_b32 v67, 6, v1\n"
    "v_cndmask_b32 v67, v189, v67, vcc\n"
    "v_lshlrev_b32 v68, 4, v184\n"
    "v_add_u32 v68, 40960, v68\n"
    "v_lshlrev_b32 v69, 1, v1\n"
    "v_lshl_add_u32 v190, v186, 3, v190\n"
    "v_and_b32 v188, 1, v184\n"
    "v_lshl_add_u32 v188, v188, 2, v191\n"
    "v_cmp_eq_u32 vcc, 7, v188\n"
    "v_lshlrev_b32 v188, 3, v188\n"
    "v_lshl_add_u32 v70, v190, 6, v188\n"
    "v_add_u32 v71, s28, v70\n"
    "v_mov_b32 v188, 38912\n"
    "v_cndmask_b32 v71, v71, v188, vcc\n"
    "v_cmp_gt_u32 vcc, 3, v184\n"
    "s_waitcnt vmcnt(5) lgkmcnt(0)\n"
    "s_barrier\n"
    "ds_read2_b64 v[176:179], v68 offset0:13 offset1:14\n"
    "ds_read_u16 v68, v69 offset:41012\n"
    "ds_read_u16 v69, v69 offset:40960\n"
    "ds_read_b64_tr_b16 v[112:113], v64 offset:0\n"
    "ds_read_b64_tr_b16 v[114:115], v65 offset:0\n"
    "ds_read_b64_tr_b16 v[116:117], v64 offset:208\n"
    "ds_read_b64_tr_b16 v[118:119], v65 offset:208\n"
    "ds_read_b64_tr_b16 v[120:121], v64 offset:416\n"
    "ds_read_b64_tr_b16 v[122:123], v65 offset:416\n"
    "ds_read_b64_tr_b16 v[124:125], v64 offset:624\n"
    "ds_read_b64_tr_b16 v[126:127], v65 offset:624\n"
    "ds_read_b64_tr_b16 v[128:129], v64 offset:832\n"
    "ds_read_b64_tr_b16 v[130:131], v65 offset:832\n"
    "ds_read_b64_tr_b16 v[132:133], v64 offset:1040\n"
    "ds_read_b64_tr_b16 v[134:135], v65 offset:1040\n"
    "ds_read_b64_tr_b16 v[136:137], v64 offset:1248\n"
    "ds_read_b64_tr_b16 v[138:139], v65 offset:1248\n"
    "s_waitcnt lgkmcnt(14)\n"
    "v_cndmask_b32 v177, 0, v177, vcc\n"
    "v_cndmask_b32 v178, 0, v178, vcc\n"
    "v_cndmask_b32 v179, 0, v179, vcc\n"
    "ds_write_b16 v67, v68 offset:36920\n"
    "s_waitcnt lgkmcnt(13)\n"
    "v_mfma_f32_16x16x32_bf16 v[144:147], v[176:179], v[112:115], 0\n"
    "s_waitcnt lgkmcnt(11)\n"
    "v_mfma_f32_16x16x32_bf16 v[148:151], v[176:179], v[116:119], 0\n"
    "s_waitcnt lgkmcnt(9)\n"
    "v_mfma_f32_16x16x32_bf16 v[152:155], v[176:179], v[120:123], 0\n"
    "s_waitcnt lgkmcnt(7)\n"
    "v_mfma_f32_16x16x32_bf16 v[156:159], v[176:179], v[124:127], 0\n"
    "s_waitcnt lgkmcnt(5)\n"
    "v_mfma_f32_16x16x32_bf16 v[160:163], v[176:179], v[128:131], 0\n"
    "s_waitcnt lgkmcnt(3)\n"
    "v_mfma_f32_16x16x32_bf16 v[164:167], v[176:179], v[132:135], 0\n"
    "s_waitcnt lgkmcnt(1)\n"
    "v_mfma_f32_16x16x32_bf16 v[168:171], v[176:179], v[136:139], 0\n"
    "s_waitcnt vmcnt(3)\n"
    "ds_read_b64_tr_b16 v[80:81], v71 offset:0\n"
    "ds_read_b64_tr_b16 v[82:83], v71 offset:256\n"
    "ds_read_b64_tr_b16 v[84:85], v71 offset:1024\n"
    "ds_read_b64_tr_b16 v[86:87], v71 offset:1280\n"
    "v_cvt_pk_bf16_f32 v144, v144, v144\n"
    "ds_write_b16 v66, v144 offset:36864\n"
    "v_cvt_pk_bf16_f32 v148, v148, v148\n"
    "ds_write_b16 v66, v148 offset:37120\n"
    "v_cvt_pk_bf16_f32 v152, v152, v152\n"
    "ds_write_b16 v66, v152 offset:37376\n"
    "v_cvt_pk_bf16_f32 v156, v156, v156\n"
    "ds_write_b16 v66, v156 offset:37632\n"
    "v_cvt_pk_bf16_f32 v160, v160, v160\n"
    "ds_write_b16 v66, v160 offset:37888\n"
    "v_cvt_pk_bf16_f32 v164, v164, v164\n"
    "ds_write_b16 v66, v164 offset:38144\n"
    "s_cmp_lg_u32 s21, 0\n"
    "s_cbranch_scc1 .Lxd_on51\n"
    "v_cvt_pk_bf16_f32 v168, v168, v168\n"
    "ds_write_b16 v66, v168 offset:38400\n"
    ".Lxd_on51:\n"
    "v_lshlrev_b32 v177, 16, v69\n"
    "s_waitcnt lgkmcnt(0)\n"
    "s_barrier\n"
    "ds_read_b64_tr_b16 v[72:73], v70 offset:36864\n"
    "ds_read_b64_tr_b16 v[74:75], v70 offset:37120\n"
    "ds_read_b64_tr_b16 v[76:77], v70 offset:37888\n"
    "ds_read_b64_tr_b16 v[78:79], v70 offset:38144\n"
    "s_lshl_b32 s30, s20, 12\n"
    "s_add_u32 s30, s30, 65536\n"
    "v_and_b32 v188, 31, v0\n"
    "v_mul_u32_u24 v192, 528, v186\n"
    "v_lshl_add_u32 v192, v188, 2, v192\n"
    "v_add_u32 v192, s30, v192\n"
    "s_lshl_b32 s40, s35, 6\n"
    "s_add_u32 s40, s40, s20\n"
    "s_lshl1_add_u32 s41, s20, s35\n"
    "s_add_u32 s41, s41, 120\n"
    "s_cmp_ge_u32 s20, 4\n"
    "s_cselect_b32 s40, s41, s40\n"
    "s_lshl1_add_u32 s41, s35, 384\n"
    "s_cmp_ge_u32 s20, 6\n"
    "s_cselect_b32 s40, s41, s40\n"
    "s_cselect_b32 s42, 1, 192\n"
    "s_and_b32 s41, s2, 3\n"
    "s_lshl2_add_u32 s40, s41, s40\n"
    "v_lshrrev_b32 v188, 3, v1\n"
    "v_mul_u32_u24 v188, 448, v188\n"
    "v_bfe_u32 v189, v0, 2, 1\n"
    "v_mad_u32_u24 v188, v189, s42, v188\n"
    "v_and_b32 v189, 48, v42\n"
    "v_add3_u32 v188, v188, v189, s40\n"
    "v_lshlrev_b32 v193, 4, v188\n"
    "s_waitcnt vmcnt(0) lgkmcnt(0)\n"
    ".Lxd_bD:\n"
    "v_mfma_f32_32x32x16_bf16 v[96:111], v[72:75], v[80:83], 0\n"
    "v_mfma_f32_32x32x16_bf16 v[96:111], v[80:83], v[72:75], v[96:111]\n"
    "v_mfma_f32_32x32x16_bf16 v[96:111], v[76:79], v[84:87], v[96:111]\n"
    "v_mfma_f32_32x32x16_bf16 v[96:111], v[84:87], v[76:79], v[96:111]\n"
    "v_readlane_b32 s43, v177, s34\n"
    "v_mad_u32_u16 v64, v44, 4, s30 op_sel:[0,0,0,0]\n"
    "v_mad_u32_u16 v65, v44, 4, s30 op_sel:[1,0,0,0]\n"
    "v_mad_u32_u16 v66, v45, 4, s30 op_sel:[0,0,0,0]\n"
    "v_mad_u32_u16 v67, v45, 4, s30 op_sel:[1,0,0,0]\n"
    "v_mad_u32_u16 v68, v46, 4, s30 op_sel:[0,0,0,0]\n"
    "v_mad_u32_u16 v69, v46, 4, s30 op_sel:[1,0,0,0]\n"
    "v_mad_u32_u16 v70, v47, 4, s30 op_sel:[0,0,0,0]\n"
    "v_mad_u32_u16 v71, v47, 4, s30 op_sel:[1,0,0,0]\n"
    "v_mov_b32 v184, 40944\n"
    "s_and_b32 s25, s25, 0xffffff\n"
    "s_mul_i32 s40, s25, 0x9e3779b1\n"
    "s_xor_b32 s41, s25, 0xa5c30f96\n"
    "s_lshr_b32 s40, s40, 24\n"
    "v_writelane_b32 v108, s43, 60\n"
    "ds_write2_b32 v192, v96, v97 offset0:0 offset1:33\n"
    "ds_write2_b32 v192, v98, v99 offset0:66 offset1:99\n"
    "v_add_u32 v192, 1056, v192\n"
    "ds_write2_b32 v192, v100, v101 offset0:0 offset1:33\n"
    "ds_write2_b32 v192, v102, v103 offset0:66 offset1:99\n"
    "v_add_u32 v192, 1056, v192\n"
    "ds_write2_b32 v192, v104, v105 offset0:0 offset1:33\n"
    "ds_write2_b32 v192, v106, v107 offset0:66 offset1:99\n"
    "v_add_u32 v192, 1056, v192\n"
    "ds_write2_b32 v192, v108, v109 offset0:0 offset1:33\n"
    "ds_read_b32 v56, v64\n"
    "ds_read_b32 v57, v65\n"
    "ds_read_b32 v58, v66\n"
    "ds_read_b32 v59, v67\n"
    "ds_read_b32 v60, v68\n"
    "ds_read_b32 v61, v69\n"
    "ds_read_b32 v62, v70\n"
    "ds_read_b32 v63, v71\n"
    "s_waitcnt lgkmcnt(6)\n"
    "v_pk_mul_f32 v[56:57], v[56:57], v[48:49]\n"
    "v_max3_f32 v188, |v56|, |v57|, 0\n"
    "s_waitcnt lgkmcnt(4)\n"
    "v_pk_mul_f32 v[58:59], v[58:59], v[50:51]\n"
    "v_max3_f32 v188, |v58|, |v59|, v188\n"
    "s_waitcnt lgkmcnt(2)\n"
    "v_pk_mul_f32 v[60:61], v[60:61], v[52:53]\n"
    "v_max3_f32 v188, |v60|, |v61|, v188\n"
    "s_waitcnt lgkmcnt(0)\n"
    "v_pk_mul_f32 v[62:63], v[62:63], v[54:55]\n"
    "v_max3_f32 v188, |v62|, |v63|, v188\n"
    "s_nop 1\n"
    "v_max_u32_dpp v188, v188, v188 quad_perm:[1,0,3,2] row_mask:0xf bank_mask:0xf\n"
    "s_nop 1\n"
    "v_max_u32_dpp v188, v188, v188 quad_perm:[2,3,0,1] row_mask:0xf bank_mask:0xf\n"
    "s_nop 1\n"
    "v_max_u32_dpp v188, v188, v188 row_half_mirror row_mask:0xf bank_mask:0xf\n"
    "s_nop 1\n"
    "v_max_u32_dpp v188, v188, v188 row_mirror row_mask:0xf bank_mask:0xf\n"
    "s_mov_b32 exec_lo, 0x10001\n"
    "s_mov_b32 exec_hi, 1\n"
    "ds_max_u32 v184, v188\n"
    "s_mov_b64 exec, -1\n"
    "s_add_u32 s38, s16, 1047552\n"
    "s_addc_u32 s39, s17, 0\n"
    "s_cmp_lt_u32 s23, 26\n"
    "s_cselect_b32 s43, 0xfff8fff8, 0\n"
    "s_waitcnt lgkmcnt(0)\n"
    "s_barrier\n"
    "ds_read_b32 v188, v184\n"
    "v_cmp_le_u32 vcc, 0x400, v193\n"
    "s_cmp_eq_u32 s20, 7\n"
    "s_cselect_b32 s42, 0, 48\n"
    "s_bfm_b64 s[60:61], s42, 0\n"
    "s_and_b64 s[60:61], s[60:61], vcc\n"
    "s_waitcnt lgkmcnt(0)\n"
    "v_lshrrev_b32 v188, 23, v188\n"
    "v_max_u32 v188, 40, v188\n"
    "v_min_u32 v188, 250, v188\n"
    "v_sub_u32 v190, 268, v188\n"
    "v_lshlrev_b32 v190, 23, v190\n"
    "v_pk_mul_f32 v[56:57], v[56:57], v[190:191] op_sel_hi:[1,0]\n"
    "v_cvt_pkrtz_f16_f32 v56, v56, v57\n"
    "v_add_u32 v56, 0x40004, v56\n"
    "v_and_b32 v56, s43, v56\n"
    "v_pk_mul_f32 v[58:59], v[58:59], v[190:191] op_sel_hi:[1,0]\n"
    "v_cvt_pkrtz_f16_f32 v57, v58, v59\n"
    "v_add_u32 v57, 0x40004, v57\n"
    "v_and_b32 v57, s43, v57\n"
    "v_pk_mul_f32 v[60:61], v[60:61], v[190:191] op_sel_hi:[1,0]\n"
    "v_cvt_pkrtz_f16_f32 v58, v60, v61\n"
    "v_add_u32 v58, 0x40004, v58\n"
    "v_and_b32 v58, s43, v58\n"
    "v_pk_mul_f32 v[62:63], v[62:63], v[190:191] op_sel_hi:[1,0]\n"
    "v_cvt_pkrtz_f16_f32 v59, v62, v63\n"
    "v_add_u32 v59, 0x40004, v59\n"
    "v_and_b32 v59, s43, v59\n"
    "v_lshl_or_b32 v188, v188, 8, s40\n"
    "v_mov_b32 v189, s41\n"
    "s_lshl3_add_u32 s40, s2, 40960\n"
    "v_mov_b32 v190, s40\n"
    "s_cmp_eq_u32 s20, 7\n"
    "s_cbranch_scc1 .Lxd_b3\n"
    "s_mov_b64 exec, s[60:61]\n"
    "global_store_dwordx4 v193, v[56:59], s[38:39] sc1\n"
    "s_mov_b64 exec, -1\n"
    ".Lxd_b3:\n"
    "s_waitcnt vmcnt(0)\n"
    "s_barrier\n"
    "s_cmp_lg_u32 s20, 0\n"
    "s_cbranch_scc1 .Lxd_on52\n"
    "s_mov_b64 exec, 1\n"
    "global_store_dwordx2 v190, v[188:189], s[16:17] sc1\n"
    "s_mov_b64 exec, -1\n"
    ".Lxd_on52:\n"
    "s_branch .Lxd_h1\n"
    ".Lxd_help:\n"
    "s_lshr_b32 s20, s43, 6\n"
    "v_and_b32 v1, 63, v0\n"
    "v_lshlrev_b32 v42, 4, v1\n"
    "s_lshr_b32 s41, s2, 3\n"
    "s_sub_u32 s41, s41, 1\n"
    "s_cmp_lg_u32 s20, 7\n"
    "s_cbranch_scc1 .Lxd_on54\n"
    "s_mul_i32 s40, s41, 60\n"
    "v_min_u32 v7, 59, v1\n"
    "v_add_lshl_u32 v7, v7, s40, 7\n"
    "s_getpc_b64 s[60:61]\n"
    ".Lxd_pc53:\n"
    "s_sub_u32 s60, s60, .Lxd_pc53-.Lxd_top\n"
    "s_subb_u32 s61, s61, 0\n"
    "v_min_u32 v7, .Lxd_end-.Lxd_top-4, v7\n"
    "global_load_dword v7, v7, s[60:61]\n"
    ".Lxd_on54:\n"
    "v_mul_u32_u24 v68, 52, v1\n"
    "v_lshrrev_b32 v68, 8, v68\n"
    "v_mad_i32_i24 v69, v68, -5, v1\n"
    "s_and_b32 s40, s2, 0xf8\n"
    "s_add_u32 s40, s40, s20\n"
    "s_mul_i32 s40, s40, 13\n"
    "s_sub_u32 s40, s40, 104\n"
    "v_add_u32 v68, s40, v68\n"
    "v_min_u32 v68, 0x1ff, v68\n"
    "v_lshrrev_b32 v70, 8, v68\n"
    "s_movk_i32 s40, 1792\n"
    "v_mad_u32_u24 v68, v70, s40, v68\n"
    "s_and_b32 s40, s2, 7\n"
    "v_lshl_add_u32 v68, s40, 8, v68\n"
    "v_mul_u32_u24 v68, 104, v68\n"
    "s_mul_i32 s40, s40, 3\n"
    "v_add3_u32 v70, v69, s40, 2\n"
    "v_subrev_u32 v71, 24, v70\n"
    "v_min_u32 v70, v70, v71\n"
    "v_lshl_add_u32 v70, v70, 2, v68\n"
    "s_waitcnt lgkmcnt(0)\n"
    "s_load_dwordx4 s[68:71], s[0:1], 64\n"
    "global_load_dword v60, v70, s[4:5]\n"
    "v_add3_u32 v70, v69, s40, 7\n"
    "v_subrev_u32 v71, 24, v70\n"
    "v_min_u32 v70, v70, v71\n"
    "v_lshl_add_u32 v70, v70, 2, v68\n"
    "global_load_dword v61, v70, s[4:5]\n"
    "v_add3_u32 v70, v69, s40, 12\n"
    "v_subrev_u32 v71, 24, v70\n"
    "v_min_u32 v70, v70, v71\n"
    "v_lshl_add_u32 v70, v70, 2, v68\n"
    "global_load_dword v62, v70, s[4:5]\n"
    "v_add3_u32 v70, v69, s40, 17\n"
    "v_subrev_u32 v71, 24, v70\n"
    "v_min_u32 v70, v70, v71\n"
    "v_lshl_add_u32 v70, v70, 2, v68\n"
    "global_load_dword v63, v70, s[4:5]\n"
    "v_add3_u32 v70, v69, s40, 22\n"
    "v_subrev_u32 v71, 24, v70\n"
    "v_min_u32 v70, v70, v71\n"
    "v_add_u32 v71, 22, v69\n"
    "v_cmp_lt_u32 vcc, 23, v71\n"
    "v_min_u32 v71, 25, v71\n"
    "s_nop 0\n"
    "v_cndmask_b32 v70, v70, v71, vcc\n"
    "v_lshl_add_u32 v70, v70, 2, v68\n"
    "global_load_dword v64, v70, s[4:5]\n"
    "s_cmp_lg_u32 s20, 6\n"
    "s_cbranch_scc1 .Lxd_on55\n"
    "v_min_u32 v12, 25, v1\n"
    "s_mov_b32 m0, 159744\n"
    "v_lshlrev_b32 v12, 7, v12\n"
    "global_load_lds_dword v12, s[10:11]\n"
    "global_load_lds_dword v12, s[18:19]\n"
    ".Lxd_on55:\n"
    ".Lxd_h1:\n"
    "s_lshr_b32 s3, s2, 4\n"
    "s_mul_i32 s40, s3, 3\n"
    "s_add_u32 s47, s2, s40\n"
    "s_and_b32 s47, s47, 15\n"
    "s_lshr_b32 s21, s20, 2\n"
    "s_lshl1_add_u32 s22, s21, s20\n"
    "s_and_b32 s22, s22, 3\n"
    "s_lshl_b32 s58, s21, 8\n"
    "s_add_u32 s58, s58, 150528\n"
    "s_mov_b32 s40, s47\n"
    "s_mul_i32 s54, s40, 512\n"
    "s_add_u32 s54, s54, 2\n"
    "s_lshl_b32 s40, s40, 7\n"
    "s_add_u32 s40, s40, 32768\n"
    "s_add_u32 s52, s16, s40\n"
    "s_addc_u32 s53, s17, 0\n"
    "s_mov_b32 s57, 0x7fff7fff\n"
    "s_mov_b32 s56, 0\n"
    "s_mov_b32 s27, 0\n"
    "s_mov_b32 s55, 9\n"
    "s_mov_b32 s44, 0\n"
    "s_mov_b32 s46, 0\n"
    "v_lshrrev_b32 v17, 3, v1\n"
    "v_mul_u32_u24 v17, 12, v17\n"
    "v_and_b32 v19, 7, v1\n"
    "v_lshlrev_b32 v19, 4, v19\n"
    "v_lshrrev_b32 v18, 5, v1\n"
    "v_lshlrev_b32 v18, 2, v18\n"
    "v_and_b32 v20, 31, v1\n"
    "v_lshlrev_b32 v20, 2, v20\n"
    "s_lshl2_add_u32 s40, s22, s58\n"
    "s_sub_u32 s40, s40, 20\n"
    "v_lshl_add_u32 v25, v1, 18, s40\n"
    "s_lshl1_add_u32 s23, s3, s21\n"
    "s_mul_i32 s23, s23, 6\n"
    "s_add_u32 s23, s23, s22\n"
    "s_add_u32 s23, s23, 78\n"
    "v_mov_b32 v24, s23\n"
    "v_lshlrev_b32 v13, 2, v0\n"
    "v_readfirstlane_b32 s40, v24\n"
    "s_or_b32 s40, s40, s44\n"
    "s_lshl1_add_u32 s41, s40, s54\n"
    "s_cmpk_lt_u32 s40, 256\n"
    "s_cselect_b32 s41, s41, 2\n"
    "v_mov_b32 v12, s41\n"
    "s_mul_i32 s41, s41, 52\n"
    "s_add_u32 s38, s4, s41\n"
    "s_addc_u32 s39, s5, 0\n"
    "global_load_dwordx3 v[36:38], v17, s[38:39] offset:-104\n"
    "global_load_dword v41, v18, s[38:39] offset:-8\n"
    "s_setprio 0\n"
    "v_mov_b32 v21, 0\n"
    "v_add_u32 v13, 149504, v13\n"
    "ds_write_b32 v13, v21\n"
    "s_cmp_lt_u32 s2, 8\n"
    "s_cbranch_scc1 .Lxd_hp56\n"
    "s_waitcnt vmcnt(2)\n"
    "s_mov_b32 m0, 159744\n"
    "v_lshlrev_b32 v60, 7, v60\n"
    "global_load_lds_dword v60, s[8:9]\n"
    "s_mov_b32 m0, 159744\n"
    "v_lshlrev_b32 v61, 7, v61\n"
    "global_load_lds_dword v61, s[8:9]\n"
    "s_waitcnt vmcnt(2)\n"
    "s_branch .Lxd_h3\n"
    ".Lxd_hp56:\n"
    "s_waitcnt vmcnt(0)\n"
    ".Lxd_h3:\n"
    "v_lshl_or_b32 v36, v36, 7, v19\n"
    "v_lshl_or_b32 v37, v37, 7, v19\n"
    "v_lshl_or_b32 v38, v38, 7, v19\n"
    "v_lshl_or_b32 v41, v41, 7, v20\n"
    "global_load_dwordx4 v[28:31], v36, s[8:9]\n"
    "global_load_dwordx4 v[32:35], v37, s[8:9]\n"
    "global_load_dword v40, v41, s[8:9]\n"
    "global_load_dwordx4 v[36:39], v38, s[8:9]\n"
    "s_waitcnt lgkmcnt(0)\n"
    "s_barrier\n"
    "ds_write_b32 v25, v12 offset:20\n"
    "s_load_dword s26, s[68:69], 0\n"
    "s_lshl_b32 s43, s21, 16\n"
    "v_lshrrev_b32 v12, 4, v1\n"
    "v_bfe_u32 v13, v1, 2, 2\n"
    "v_and_b32 v21, 3, v1\n"
    "s_mul_i32 s40, s22, 0x2020\n"
    "s_and_b32 s40, s40, 0x4060\n"
    "s_add_u32 s40, s40, s43\n"
    "v_lshl_add_u32 v21, v21, 3, s40\n"
    "s_movk_i32 s41, 144\n"
    "v_add_u32 v22, v12, v13\n"
    "v_and_b32 v22, 3, v22\n"
    "v_mad_u32_u24 v4, v22, s41, v21\n"
    "v_cmp_gt_u32 vcc, 2, v13\n"
    "v_add_u32 v24, 22, v13\n"
    "v_lshlrev_b32 v13, 3, v13\n"
    "v_cndmask_b32 v22, v24, v22, vcc\n"
    "v_mad_u32_u24 v3, v22, s41, v21\n"
    "v_cmp_eq_u32 vcc, 3, v12\n"
    "v_xor_b32 v22, 0, v12\n"
    "v_and_b32 v22, 1, v22\n"
    "v_lshlrev_b32 v22, 1, v22\n"
    "v_lshl_add_u32 v22, v12, 3, v22\n"
    "s_mov_b32 s40, 0x05040100\n"
    "v_bfe_u32 v24, s40, v13, 8\n"
    "v_add_u32 v22, v22, v24\n"
    "s_mov_b32 s40, 0x1a191818\n"
    "v_bfe_u32 v24, s40, v13, 8\n"
    "v_cndmask_b32 v22, v22, v24, vcc\n"
    "v_mad_u32_u24 v5, v22, s41, v21\n"
    "v_xor_b32 v22, 1, v12\n"
    "v_and_b32 v22, 1, v22\n"
    "v_lshlrev_b32 v22, 1, v22\n"
    "v_lshl_add_u32 v22, v12, 3, v22\n"
    "s_mov_b32 s40, 0x05040100\n"
    "v_bfe_u32 v24, s40, v13, 8\n"
    "v_add_u32 v22, v22, v24\n"
    "s_mov_b32 s40, 0x1a191918\n"
    "v_bfe_u32 v24, s40, v13, 8\n"
    "v_cndmask_b32 v22, v22, v24, vcc\n"
    "v_mad_u32_u24 v6, v22, s41, v21\n"
    "v_bfrev_b32 v14, v12\n"
    "v_lshrrev_b32 v14, 26, v14\n"
    "s_add_u32 s40, s22, 3\n"
    "s_and_b32 s40, s40, 3\n"
    "s_lshl2_add_u32 s40, s40, s22\n"
    "s_add_u32 s40, s40, s58\n"
    "s_lshl_b32 s40, s40, 2\n"
    "s_add_u32 s40, s40, -450048\n"
    "v_add_u32 v14, s40, v14\n"
    "v_and_b32 v2, 1, v1\n"
    "v_mul_u32_u24 v2, 0xaaaa, v2\n"
    "v_add_u32 v2, 0x4444, v2\n"
    "v_mad_u32_u24 v15, v17, 36, v19\n"
    "v_mad_u32_u24 v16, v18, 36, v20\n"
    "s_add_u32 s41, s22, 2\n"
    "v_lshrrev_b32 v12, 5, v19\n"
    "v_sub_u32 v12, 1, v12\n"
    "v_and_b32 v12, 3, v12\n"
    "v_add_lshl_u32 v12, v12, s41, 12\n"
    "v_and_b32 v12, 0x7000, v12\n"
    "v_add3_u32 v15, v12, v15, s43\n"
    "v_lshrrev_b32 v12, 5, v20\n"
    "v_sub_u32 v12, 1, v12\n"
    "v_and_b32 v12, 3, v12\n"
    "v_add_lshl_u32 v12, v12, s41, 12\n"
    "v_and_b32 v12, 0x7000, v12\n"
    "v_add3_u32 v16, v12, v16, s43\n"
    "v_add_u32 v16, 3456, v16\n"
    "s_lshl3_add_u32 s40, s2, 4096\n"
    "s_lshl_b32 s41, s2, 2\n"
    "v_lshlrev_b32 v11, 3, v1\n"
    "v_add_u32 v11, 40960, v11\n"
    "v_writelane_b32 v11, s40, 8\n"
    "v_writelane_b32 v11, s41, 9\n"
    "s_getpc_b64 s[10:11]\n"
    ".Lxd_en57:\n"
    "s_mov_b32 s40, .Lxd_ph1-.Lxd_en57\n"
    "s_cmp_eq_u32 s22, 1\n"
    "s_cselect_b32 s40, .Lxd_ph0-.Lxd_en57, s40\n"
    "s_cmp_eq_u32 s22, 2\n"
    "s_cselect_b32 s40, .Lxd_pl3-.Lxd_en57, s40\n"
    "s_cmp_eq_u32 s22, 3\n"
    "s_cselect_b32 s40, .Lxd_pl2-.Lxd_en57, s40\n"
    "s_ashr_i32 s41, s40, 31\n"
    "s_add_u32 s10, s10, s40\n"
    "s_addc_u32 s11, s11, s41\n"
    "v_mov_b32 v68, v42\n"
    "v_add_u32 v69, 4096, v42\n"
    "v_add_u32 v42, 98304, v42\n"
    "v_bfe_u32 v70, v1, 4, 1\n"
    "v_lshlrev_b32 v70, 2, v70\n"
    "v_add_u32 v70, 154112, v70\n"
    "s_mov_b32 s36, 0\n"
    "v_mov_b64 v[88:89], 0\n"
    "v_mov_b64 v[90:91], 0\n"
    "s_waitcnt vmcnt(0)\n"
    "s_movk_i32 s43, 15744\n"
    "v_pk_maximum3_f16 v21, v28, v28, s43 op_sel_hi:[1,1,0] neg_lo:[0,1,0] neg_hi:[0,1,0]\n"
    "v_pk_maximum3_f16 v21, v29, v29, v21 neg_lo:[0,1,0] neg_hi:[0,1,0]\n"
    "v_pk_maximum3_f16 v21, v30, v30, v21 neg_lo:[0,1,0] neg_hi:[0,1,0]\n"
    "v_pk_maximum3_f16 v21, v31, v31, v21 neg_lo:[0,1,0] neg_hi:[0,1,0]\n"
    "v_pk_maximum3_f16 v21, v32, v32, v21 neg_lo:[0,1,0] neg_hi:[0,1,0]\n"
    "v_pk_maximum3_f16 v21, v33, v33, v21 neg_lo:[0,1,0] neg_hi:[0,1,0]\n"
    "v_pk_maximum3_f16 v21, v34, v34, v21 neg_lo:[0,1,0] neg_hi:[0,1,0]\n"
    "v_pk_maximum3_f16 v21, v35, v35, v21 neg_lo:[0,1,0] neg_hi:[0,1,0]\n"
    "v_pk_maximum3_f16 v21, v36, v36, v21 neg_lo:[0,1,0] neg_hi:[0,1,0]\n"
    "v_pk_maximum3_f16 v21, v37, v37, v21 neg_lo:[0,1,0] neg_hi:[0,1,0]\n"
    "v_pk_maximum3_f16 v21, v38, v38, v21 neg_lo:[0,1,0] neg_hi:[0,1,0]\n"
    "v_pk_maximum3_f16 v21, v39, v39, v21 neg_lo:[0,1,0] neg_hi:[0,1,0]\n"
    "v_pk_maximum3_f16 v21, v40, v40, v21 neg_lo:[0,1,0] neg_hi:[0,1,0]\n"
    "v_pk_max_u16 v21, v21, v21 op_sel:[0,1] op_sel_hi:[1,0]\n"
    "s_nop 1\n"
    "v_max_u32_dpp v21, v21, v21 quad_perm:[1,0,3,2] row_mask:0xf bank_mask:0xf\n"
    "s_nop 1\n"
    "v_max_u32_dpp v21, v21, v21 quad_perm:[2,3,0,1] row_mask:0xf bank_mask:0xf\n"
    "s_nop 1\n"
    "v_max_u32_dpp v21, v21, v21 row_half_mirror row_mask:0xf bank_mask:0xf\n"
    "s_nop 1\n"
    "v_max_u32_dpp v21, v21, v21 row_mirror row_mask:0xf bank_mask:0xf\n"
    "s_nop 1\n"
    "v_max_u32_dpp v21, v21, v21 row_bcast:15 row_mask:0xa bank_mask:0xf\n"
    "s_nop 1\n"
    "v_max_u32_dpp v21, v21, v21 row_bcast:31 row_mask:0xc bank_mask:0xf\n"
    "s_nop 1\n"
    "v_readlane_b32 s47, v21, 63\n"
    "s_and_b32 s47, s47, 0x7f80\n"
    "s_sub_u32 s50, s47, 2816\n"
    "s_mul_i32 s51, s50, -8\n"
    "s_lshr_b32 s47, s47, 3\n"
    "v_pk_maximum3_f16 v21, v28, v28, s50 op_sel_hi:[1,1,0] neg_lo:[0,1,0] neg_hi:[0,1,0]\n"
    "v_pk_mad_u16 v21, v21, 8, s51 op_sel_hi:[1,0,0]\n"
    "v_bfi_b32 v28, s57, v21, v28\n"
    "v_pk_maximum3_f16 v12, v29, v29, s50 op_sel_hi:[1,1,0] neg_lo:[0,1,0] neg_hi:[0,1,0]\n"
    "v_pk_mad_u16 v12, v12, 8, s51 op_sel_hi:[1,0,0]\n"
    "v_bfi_b32 v29, s57, v12, v29\n"
    "v_pk_maximum3_f16 v21, v30, v30, s50 op_sel_hi:[1,1,0] neg_lo:[0,1,0] neg_hi:[0,1,0]\n"
    "v_pk_mad_u16 v21, v21, 8, s51 op_sel_hi:[1,0,0]\n"
    "v_bfi_b32 v30, s57, v21, v30\n"
    "v_pk_maximum3_f16 v12, v31, v31, s50 op_sel_hi:[1,1,0] neg_lo:[0,1,0] neg_hi:[0,1,0]\n"
    "v_pk_mad_u16 v12, v12, 8, s51 op_sel_hi:[1,0,0]\n"
    "v_bfi_b32 v31, s57, v12, v31\n"
    "v_pk_maximum3_f16 v21, v32, v32, s50 op_sel_hi:[1,1,0] neg_lo:[0,1,0] neg_hi:[0,1,0]\n"
    "v_pk_mad_u16 v21, v21, 8, s51 op_sel_hi:[1,0,0]\n"
    "v_bfi_b32 v32, s57, v21, v32\n"
    "v_pk_maximum3_f16 v12, v33, v33, s50 op_sel_hi:[1,1,0] neg_lo:[0,1,0] neg_hi:[0,1,0]\n"
    "v_pk_mad_u16 v12, v12, 8, s51 op_sel_hi:[1,0,0]\n"
    "v_bfi_b32 v33, s57, v12, v33\n"
    "s_cmp_lg_u32 s20, 7\n"
    "s_cbranch_scc1 .Lxd_on58\n"
    "s_mov_b64 exec, 0x3ff\n"
    "global_load_dwordx2 v[26:27], v11, s[16:17] sc1\n"
    "s_mov_b64 exec, -1\n"
    ".Lxd_on58:\n"
    "v_pk_maximum3_f16 v21, v34, v34, s50 op_sel_hi:[1,1,0] neg_lo:[0,1,0] neg_hi:[0,1,0]\n"
    "v_pk_mad_u16 v21, v21, 8, s51 op_sel_hi:[1,0,0]\n"
    "v_bfi_b32 v34, s57, v21, v34\n"
    "v_pk_maximum3_f16 v12, v35, v35, s50 op_sel_hi:[1,1,0] neg_lo:[0,1,0] neg_hi:[0,1,0]\n"
    "v_pk_mad_u16 v12, v12, 8, s51 op_sel_hi:[1,0,0]\n"
    "v_bfi_b32 v35, s57, v12, v35\n"
    "v_pk_maximum3_f16 v21, v36, v36, s50 op_sel_hi:[1,1,0] neg_lo:[0,1,0] neg_hi:[0,1,0]\n"
    "v_pk_mad_u16 v21, v21, 8, s51 op_sel_hi:[1,0,0]\n"
    "v_bfi_b32 v36, s57, v21, v36\n"
    "v_pk_maximum3_f16 v12, v37, v37, s50 op_sel_hi:[1,1,0] neg_lo:[0,1,0] neg_hi:[0,1,0]\n"
    "v_pk_mad_u16 v12, v12, 8, s51 op_sel_hi:[1,0,0]\n"
    "v_bfi_b32 v37, s57, v12, v37\n"
    "v_pk_maximum3_f16 v21, v38, v38, s50 op_sel_hi:[1,1,0] neg_lo:[0,1,0] neg_hi:[0,1,0]\n"
    "v_pk_mad_u16 v21, v21, 8, s51 op_sel_hi:[1,0,0]\n"
    "v_bfi_b32 v38, s57, v21, v38\n"
    "v_pk_maximum3_f16 v12, v39, v39, s50 op_sel_hi:[1,1,0] neg_lo:[0,1,0] neg_hi:[0,1,0]\n"
    "v_pk_mad_u16 v12, v12, 8, s51 op_sel_hi:[1,0,0]\n"
    "v_bfi_b32 v39, s57, v12, v39\n"
    "v_pk_maximum3_f16 v21, v40, v40, s50 op_sel_hi:[1,1,0] neg_lo:[0,1,0] neg_hi:[0,1,0]\n"
    "v_pk_mad_u16 v21, v21, 8, s51 op_sel_hi:[1,0,0]\n"
    "v_bfi_b32 v40, s57, v21, v40\n"
    "s_sub_u32 s66, 4112, s47\n"
    "s_lshl_b32 s66, s66, 19\n"
    "v_cvt_pkrtz_f16_f32 v12, s66, s66\n"
    "v_lshl_or_b32 v21, v12, 16, s47\n"
    "ds_write_b128 v15, v[28:31] offset:0\n"
    "ds_write_b128 v15, v[32:35] offset:144\n"
    "ds_write_b128 v15, v[36:39] offset:288\n"
    "ds_write2_b32 v16, v40, v12 offset1:72\n"
    "ds_or_b32 v25, v21 offset:532\n"
    "s_cmp_lg_u32 s20, 7\n"
    "s_cbranch_scc1 .Lxd_on60\n"
    "s_waitcnt vmcnt(0)\n"
    "v_readlane_b32 s25, v26, 9\n"
    "v_readlane_b32 s40, v26, 8\n"
    "v_readlane_b32 s41, v27, 8\n"
    "s_mul_i32 s42, s25, 0x9e3779b1\n"
    "s_lshr_b32 s42, s42, 24\n"
    "s_xor_b32 s42, s42, s40\n"
    "s_and_b32 s42, s42, 0xff\n"
    "s_xor_b32 s41, s41, s25\n"
    "s_xor_b32 s41, s41, 0xa5c30f96\n"
    "s_or_b32 s42, s41, s42\n"
    "s_lshr_b32 s41, s40, 8\n"
    "s_mul_i32 s40, s41, 0x9e3779b1\n"
    "s_xor_b32 s41, s41, 0xa5c30f96\n"
    "s_lshr_b32 s40, s40, 24\n"
    "v_xor_b32 v13, s40, v26\n"
    "v_xor_b32 v22, s41, v27\n"
    "v_lshl_or_b32 v13, v13, 24, v22\n"
    "v_cmp_ne_u32 s[66:67], 0, v13\n"
    "s_and_b32 s66, s66, 0xff\n"
    "s_or_b32 s42, s42, s66\n"
    "s_cmp_eq_u32 s20, 7\n"
    "s_cselect_b32 s42, s42, 0\n"
    "s_cmp_lg_u32 s42, 0\n"
    "s_cbranch_scc1 .Lxd_pv59\n"
    ".Lxd_pv59r:\n"
    "s_mov_b64 s[64:65], s[40:41]\n"
    ".Lxd_on60:\n"
    "s_waitcnt lgkmcnt(0)\n"
    "s_barrier\n"
    "s_mul_i32 s42, s20, 5376\n"
    "s_add_u32 s40, s42, 1048576\n"
    "s_add_u32 s38, s16, s40\n"
    "s_addc_u32 s39, s17, 0\n"
    "s_add_u32 m0, s42, 98304\n"
    "s_nop 0\n"
    "global_load_lds_dwordx4 v68, s[38:39] offset:0 sc1\n"
    "global_load_lds_dwordx4 v68, s[38:39] offset:1024 sc1\n"
    "global_load_lds_dwordx4 v68, s[38:39] offset:2048 sc1\n"
    "global_load_lds_dwordx4 v68, s[38:39] offset:3072 sc1\n"
    "s_add_u32 m0, s42, 102400\n"
    "s_nop 0\n"
    "global_load_lds_dwordx4 v69, s[38:39] offset:0 sc1\n"
    "global_load_lds_dwordx4 v69, s[38:39] offset:256 sc1\n"
    "s_cmp_lg_u32 s20, 7\n"
    "s_cbranch_scc1 .Lxd_on61\n"
    "s_mov_b64 s[40:41], s[64:65]\n"
    "v_lshrrev_b32 v22, 8, v26\n"
    "v_mov_b32 v26, s40\n"
    "v_mov_b32 v27, s41\n"
    "v_add_u32_dpp v22, v22, v22 row_shl:4 row_mask:0xf bank_mask:0xf\n"
    "v_add_u32_e64 v43, s25, 1\n"
    "v_lshlrev_b32 v8, 2, v1\n"
    "v_max_u32_dpp v13, v22, v22 quad_perm:[1,0,3,2] row_mask:0xf bank_mask:0xf\n"
    "v_add_u32 v8, 154112, v8\n"
    "s_nop 0\n"
    "v_max_u32_dpp v13, v13, v13 quad_perm:[2,3,0,1] row_mask:0xf bank_mask:0xf\n"
    "v_sub_u32 v22, v22, v13\n"
    "v_subrev_u32 v13, 818, v13\n"
    "v_ldexp_f32 v22, 1.0, v22\n"
    "s_mov_b64 exec, 1\n"
    "ds_write_b64 v8, v[26:27]\n"
    "s_mov_b64 exec, 15\n"
    "ds_write2_b32 v8, v22, v13 offset0:4 offset1:8\n"
    "s_mov_b64 exec, -1\n"
    ".Lxd_on61:\n"
    "s_cmp_lt_u32 s22, 2\n"
    "s_cselect_b32 s40, 16, 0\n"
    "v_add_u32 v25, s40, v25\n"
    "s_add_u32 s23, s23, 4\n"
    "v_mov_b32 v24, s23\n"
    "v_mov_b32 v13, s58\n"
    "s_sub_u32 s40, 3, s22\n"
    "s_and_b32 s40, s40, 3\n"
    "v_lshl_add_u32 v7, s40, 12, v3\n"
    "v_lshl_add_u32 v8, s40, 12, v4\n"
    "v_lshl_add_u32 v9, s40, 12, v5\n"
    "v_lshl_add_u32 v10, s40, 12, v6\n"
    "ds_read_b32 v22, v13 offset:512\n"
    "ds_read_b64_tr_b16 v[44:45], v9 offset:0\n"
    "ds_read_b64_tr_b16 v[46:47], v10 offset:0\n"
    "ds_read_b64_tr_b16 v[48:49], v7 offset:0\n"
    "ds_read_b64_tr_b16 v[50:51], v8 offset:576\n"
    "ds_read_b64_tr_b16 v[52:53], v8 offset:1152\n"
    "ds_read_b64_tr_b16 v[54:55], v8 offset:1728\n"
    "ds_read_b64_tr_b16 v[56:57], v8 offset:2304\n"
    "ds_read_b64_tr_b16 v[58:59], v8 offset:2880\n"
    "s_cmp_lg_u32 s22, 0\n"
    "s_cbranch_scc1 .Lxd_on62\n"
    "v_readfirstlane_b32 s40, v24\n"
    "s_or_b32 s40, s40, s44\n"
    "s_lshl1_add_u32 s41, s40, s54\n"
    "s_cmpk_lt_u32 s40, 256\n"
    "s_cselect_b32 s41, s41, 2\n"
    "v_mov_b32 v12, s41\n"
    "s_mul_i32 s41, s41, 52\n"
    "s_add_u32 s38, s4, s41\n"
    "s_addc_u32 s39, s5, 0\n"
    "ds_write_b32 v25, v12 offset:20\n"
    "global_load_dwordx3 v[36:38], v17, s[38:39] offset:-104\n"
    "global_load_dword v41, v18, s[38:39] offset:-8\n"
    ".Lxd_on62:\n"
    "s_waitcnt vmcnt(0) lgkmcnt(0)\n"
    "s_barrier\n"
    "ds_read2_b32 v[26:27], v70 offset1:1\n"
    "ds_read_b32 v0, v70 offset:16\n"
    "ds_read_b32 v23, v70 offset:24\n"
    "s_cmp_lg_u32 s20, 7\n"
    "s_cbranch_scc1 .Lxd_on63\n"
    "s_mov_b64 exec, 0x200\n"
    "global_store_dword v11, v43, s[16:17] sc1\n"
    "s_mov_b64 exec, -1\n"
    ".Lxd_on63:\n"
    "ds_read_b128 v[92:95], v42 offset:0\n"
    "ds_read_b128 v[96:99], v42 offset:1024\n"
    "ds_read_b128 v[100:103], v42 offset:2048\n"
    "ds_read_b128 v[104:107], v42 offset:3072\n"
    "ds_read_b128 v[108:111], v42 offset:4096\n"
    "ds_read_b128 v[112:115], v42 offset:5120\n"
    "ds_read_b128 v[116:119], v42 offset:6144\n"
    "ds_read_b128 v[120:123], v42 offset:7168\n"
    "ds_read_b128 v[124:127], v42 offset:8192\n"
    "ds_read_b128 v[128:131], v42 offset:9216\n"
    "ds_read_b128 v[132:135], v42 offset:10240\n"
    "ds_read_b128 v[136:139], v42 offset:11264\n"
    "ds_read_b128 v[140:143], v42 offset:12288\n"
    "ds_read_b128 v[144:147], v42 offset:13312\n"
    "s_cmp_ge_u32 s2, 8\n"
    "s_cbranch_scc1 .Lxd_h4\n"
    "s_lshl3_add_u32 s40, s2, s20\n"
    "s_mul_i32 s40, s40, 52\n"
    "s_add_u32 s40, s40, 39936\n"
    "v_min_u32 v68, 51, v1\n"
    "s_mov_b32 m0, 159744\n"
    "v_add_lshl_u32 v68, v68, s40, 7\n"
    "global_load_lds_dword v68, s[6:7]\n"
    ".Lxd_h4:\n"
    "ds_read_b128 v[148:151], v42 offset:14336\n"
    "ds_read_b128 v[152:155], v42 offset:15360\n"
    "ds_read_b128 v[156:159], v42 offset:16384\n"
    "ds_read_b128 v[160:163], v42 offset:17408\n"
    "ds_read_b128 v[164:167], v42 offset:18432\n"
    "ds_read_b128 v[168:171], v42 offset:19456\n"
    "ds_read_b128 v[172:175], v42 offset:20480\n"
    "ds_read_b128 v[176:179], v42 offset:21504\n"
    "ds_read_b128 v[180:183], v42 offset:22528\n"
    "ds_read_b128 v[184:187], v42 offset:23552\n"
    "ds_read_b128 v[188:191], v42 offset:24576\n"
    "ds_read_b128 v[192:195], v42 offset:25600\n"
    "ds_read_b128 v[196:199], v42 offset:26624\n"
    "ds_read_b128 v[200:203], v42 offset:27648\n"
    "ds_read_b128 v[204:207], v42 offset:28672\n"
    "ds_read_b128 v[208:211], v42 offset:29696\n"
    "ds_read_b128 v[212:215], v42 offset:30720\n"
    "ds_read_b128 v[216:219], v42 offset:31744\n"
    "ds_read_b128 v[220:223], v42 offset:32768\n"
    "ds_read_b128 v[224:227], v42 offset:33792\n"
    "ds_read_b128 v[228:231], v42 offset:34816\n"
    "ds_read_b128 v[232:235], v42 offset:35840\n"
    "ds_read_b128 v[236:239], v42 offset:36864\n"
    "ds_read_b128 v[240:243], v42 offset:37888\n"
    "ds_read_b128 v[244:247], v42 offset:38912\n"
    "ds_read_b128 v[248:251], v42 offset:39936\n"
    "ds_read_b128 v[252:255], v42 offset:40960\n"
    "s_waitcnt lgkmcnt(15)\n"
    "v_readfirstlane_b32 s45, v22\n"
    "v_readfirstlane_b32 s28, v26\n"
    "v_readfirstlane_b32 s29, v27\n"
    "s_cmp_lt_u32 s2, 8\n"
    "s_cbranch_scc1 .Lxd_hp65\n"
    "s_mov_b32 m0, 159744\n"
    "s_mov_b32 s46, 3\n"
    "v_lshlrev_b32 v26, 7, v62\n"
    "v_lshlrev_b32 v27, 7, v63\n"
    "v_lshlrev_b32 v42, 7, v64\n"
    ".Lxd_hp65:\n"
    "v_pk_mul_f16 v64, v44, v46\n"
    "v_pk_mul_f16 v65, v45, v47\n"
    "v_pk_mul_f16 v66, v48, v48 op_sel_hi:[0,1]\n"
    "v_pk_mul_f16 v67, v48, v49 op_sel_hi:[0,1]\n"
    "v_pk_mul_f16 v60, v44, s45 op_sel:[0,1]\n"
    "v_pk_mul_f16 v61, v45, s45 op_sel:[0,1]\n"
    "v_pk_mul_f16 v62, v46, s45 op_sel:[0,1]\n"
    "v_pk_mul_f16 v63, v47, s45 op_sel:[0,1]\n"
    "s_waitcnt lgkmcnt(0)\n"
    "s_branch .Lxd_enter\n"
    ".Lxd_slow:\n"
    "s_cmp_eq_u32 s45, -1\n"
    "s_cbranch_scc1 .Lxd_sl7\n"
    "s_mov_b32 s59, 0x100000\n"
    "s_and_b32 s40, s45, 0xff0\n"
    "s_cbranch_scc0 .Lxd_sl1\n"
    ".Lxd_sl2:\n"
    "s_cmp_ge_i32 s45, -16\n"
    "s_cbranch_scc1 .Lxd_sl3\n"
    "s_bitset0_b32 s45, 31\n"
    ".Lxd_sl6:\n"
    "s_cmp_lg_u32 s56, 0\n"
    "s_cbranch_scc1 .Lxd_sl9\n"
    "v_readfirstlane_b32 s40, v13\n"
    "s_lshl4_add_u32 s41, s22, s58\n"
    "s_mov_b32 s56, 2\n"
    "s_cmp_lt_u32 s40, s41\n"
    "s_cbranch_scc0 .Lxd_gran\n"
    ".Lxd_sl9:\n"
    "s_setpc_b64 s[60:61]\n"
    ".Lxd_sl1:\n"
    "s_sleep 1\n"
    "ds_read_b32 v22, v13 offset:512\n"
    "s_waitcnt lgkmcnt(0)\n"
    "v_readfirstlane_b32 s45, v22\n"
    "s_sub_u32 s59, s59, 1\n"
    "s_cbranch_scc1 .Lxd_sl5\n"
    "s_and_b32 s40, s45, 0xff0\n"
    "s_cbranch_scc0 .Lxd_sl1\n"
    "s_cmp_gt_i32 s45, 0\n"
    "s_cbranch_scc0 .Lxd_sl2\n"
    "s_setpc_b64 s[60:61]\n"
    ".Lxd_sl3:\n"
    "v_and_b32 v13, 0x3ffff, v13\n"
    "v_lshl_add_u32 v13, v1, 2, v13\n"
    "s_mov_b64 exec, 7\n"
    ".Lxd_sl4:\n"
    "ds_read_b32 v22, v13 offset:4\n"
    "s_waitcnt lgkmcnt(0)\n"
    "v_cmp_eq_u32 s[40:41], 0, v22\n"
    "v_cmp_ne_u32 s[42:43], -1, v22\n"
    "s_sub_u32 s59, s59, 1\n"
    "s_cbranch_scc1 .Lxd_sl5\n"
    "s_cmp_lg_u32 s40, 0\n"
    "s_cbranch_scc1 .Lxd_sl4\n"
    "s_cmp_eq_u32 s42, 0\n"
    "s_cbranch_scc1 .Lxd_sl5\n"
    "s_mov_b64 exec, -1\n"
    "s_mov_b32 s45, 1\n"
    "s_mov_b32 s56, 0\n"
    "s_setpc_b64 s[60:61]\n"
    ".Lxd_sl5:\n"
    "s_mov_b64 exec, -1\n"
    "s_mov_b32 s45, -1\n"
    ".Lxd_sl7:\n"
    "s_cmp_lg_u32 s56, 1\n"
    "s_cbranch_scc1 .Lxd_sl6\n"
    "s_branch .Lxd_dsum\n"
    ".Lxd_gran:\n"
    "s_mov_b32 s46, 0\n"
    "s_lshl4_add_u32 s40, s22, s58\n"
    "v_lshrrev_b32 v13, 4, v1\n"
    "v_lshl_add_u32 v13, v13, 2, s40\n"
    "s_waitcnt vmcnt(0)\n"
    "ds_read2_b32 v[26:27], v13 offset1:16\n"
    "v_and_b32 v13, 15, v1\n"
    "v_lshlrev_b32 v13, 15, v13\n"
    "s_mov_b32 s42, 0x1fff1fff\n"
    "s_mov_b32 s43, s42\n"
    "s_add_u32 s40, s16, 65528\n"
    "s_addc_u32 s41, s17, 0\n"
    "v_mov_b32 v42, 0\n"
    "s_mov_b32 s56, 2\n"
    "s_waitcnt lgkmcnt(0)\n"
    "v_cmp_lt_i32 s[30:31], 0, v26\n"
    "v_cmp_lt_i32 s[64:65], 0, v27\n"
    "v_lshl_add_u32 v43, v26, 2, v13\n"
    "v_lshl_add_u32 v27, v27, 2, v13\n"
    "v_mov_b32 v26, 0\n"
    "s_and_b64 s[30:31], s[30:31], s[42:43]\n"
    "s_cbranch_scc0 .Lxd_gr30\n"
    "s_mov_b64 exec, s[30:31]\n"
    "global_load_dwordx2 v[42:43], v43, s[40:41] sc1\n"
    "s_mov_b64 exec, -1\n"
    "s_mov_b32 s56, 1\n"
    ".Lxd_gr30:\n"
    "s_and_b64 s[64:65], s[64:65], s[42:43]\n"
    "s_cbranch_scc0 .Lxd_gr64\n"
    "s_mov_b64 exec, s[64:65]\n"
    "global_load_dwordx2 v[26:27], v27, s[40:41] sc1\n"
    "s_mov_b64 exec, -1\n"
    "s_mov_b32 s56, 1\n"
    ".Lxd_gr64:\n"
    "s_setpc_b64 s[60:61]\n"
    ".Lxd_dsum:\n"
    "s_lshl4_add_u32 s42, s22, s58\n"
    "s_waitcnt vmcnt(0)\n"
    "s_cmp_eq_u64 s[30:31], 0\n"
    "s_cbranch_scc1 .Lxd_ds30\n"
    "v_xor_b32 v13, s28, v42\n"
    "v_xor_b32 v22, s29, v43\n"
    "v_lshl_or_b32 v13, v13, 24, v22\n"
    "v_and_b32 v22, 0xffffff00, v42\n"
    "v_cmp_ne_u32 s[40:41], 0, v13\n"
    "v_lshrrev_b32 v13, 4, v1\n"
    "v_add_f32_dpp v22, v22, v22 quad_perm:[1,0,3,2] row_mask:0xf bank_mask:0xf\n"
    "s_and_b64 s[40:41], s[40:41], s[30:31]\n"
    "s_cbranch_scc1 .Lxd_ds9\n"
    "v_add_f32_dpp v22, v22, v22 quad_perm:[2,3,0,1] row_mask:0xf bank_mask:0xf\n"
    "v_lshl_add_u32 v13, v13, 2, s42\n"
    "s_nop 0\n"
    "v_add_f32_dpp v22, v22, v22 row_half_mirror row_mask:0xf bank_mask:0xf\n"
    "s_nop 1\n"
    "v_add_f32_dpp v22, v22, v22 row_mirror row_mask:0xf bank_mask:0xf\n"
    "ds_write_b32 v13, v22 offset:1024\n"
    ".Lxd_ds30:\n"
    "s_cmp_eq_u64 s[64:65], 0\n"
    "s_cbranch_scc1 .Lxd_ds64\n"
    "v_xor_b32 v13, s28, v26\n"
    "v_xor_b32 v22, s29, v27\n"
    "v_lshl_or_b32 v13, v13, 24, v22\n"
    "v_and_b32 v22, 0xffffff00, v26\n"
    "v_cmp_ne_u32 s[40:41], 0, v13\n"
    "v_lshrrev_b32 v13, 4, v1\n"
    "v_add_f32_dpp v22, v22, v22 quad_perm:[1,0,3,2] row_mask:0xf bank_mask:0xf\n"
    "s_and_b64 s[40:41], s[40:41], s[64:65]\n"
    "s_cbranch_scc1 .Lxd_ds9\n"
    "v_add_f32_dpp v22, v22, v22 quad_perm:[2,3,0,1] row_mask:0xf bank_mask:0xf\n"
    "v_lshl_add_u32 v13, v13, 2, s42\n"
    "s_nop 0\n"
    "v_add_f32_dpp v22, v22, v22 row_half_mirror row_mask:0xf bank_mask:0xf\n"
    "s_nop 1\n"
    "v_add_f32_dpp v22, v22, v22 row_mirror row_mask:0xf bank_mask:0xf\n"
    "ds_write_b32 v13, v22 offset:1088\n"
    ".Lxd_ds64:\n"
    "s_mov_b32 s56, 2\n"
    ".Lxd_ds9:\n"
    "s_setpc_b64 s[60:61]\n"
    ".Lxd_mark:\n"
    "v_and_b32 v13, 0x3ffff, v13\n"
    "v_lshl_add_u32 v13, v1, 2, v13\n"
    "s_add_u32 s40, s58, 248\n"
    "v_mov_b32 v24, -1\n"
    "v_cmp_gt_u32 s[42:43], s40, v13\n"
    "s_mov_b32 s62, 0x88888888\n"
    "s_mov_b32 s63, s62\n"
    "s_and_b64 exec, s[42:43], s[62:63]\n"
    "ds_write_b32 v13, v24 offset:8\n"
    "s_mov_b64 exec, s[42:43]\n"
    "v_cndmask_b32 v24, 0, -16, s[62:63]\n"
    "s_brev_b32 s40, 1\n"
    "v_writelane_b32 v24, s40, 2\n"
    "s_lshl_b32 s40, 1, s22\n"
    "v_or_b32 v24, s40, v24\n"
    "s_mov_b32 s36, 1\n"
    "ds_or_b32 v13, v24 offset:520\n"
    "s_mov_b64 exec, -1\n"
    "s_setpc_b64 s[60:61]\n"
    ".Lxd_tf17:\n"
    "v_mov_b32 v12, -1\n"
    "v_mov_b32 v13, v25\n"
    "s_call_b64 s[60:61], .Lxd_mark\n"
    "s_branch .Lxd_tf17r\n"
    ".Lxd_ck18:\n"
    "v_add_u32 v13, 4, v25\n"
    "s_call_b64 s[60:61], .Lxd_slow\n"
    "s_cmp_eq_u32 s45, -1\n"
    "s_cbranch_scc0 .Lxd_ck18r\n"
    "s_call_b64 s[60:61], .Lxd_last\n"
    "v_fma_f32 v7, v79, v83, v11\n"
    "s_branch .Lxd_fin\n"
    ".Lxd_ht21:\n"
    "global_load_lds_dword v26, s[8:9]\n"
    "v_mov_b32 v26, v27\n"
    "v_mov_b32 v27, v42\n"
    "s_sub_u32 s46, s46, 1\n"
    "s_branch .Lxd_ht21r\n"
    ".Lxd_ck22:\n"
    "v_add_u32 v13, 8, v25\n"
    "s_call_b64 s[60:61], .Lxd_slow\n"
    "s_cmp_eq_u32 s45, -1\n"
    "s_cbranch_scc0 .Lxd_ck22r\n"
    "s_call_b64 s[60:61], .Lxd_last\n"
    "v_fma_f32 v8, v79, v83, v11\n"
    "s_branch .Lxd_fin\n"
    ".Lxd_ht25:\n"
    "global_load_lds_dword v26, s[8:9]\n"
    "v_mov_b32 v26, v27\n"
    "v_mov_b32 v27, v42\n"
    "s_sub_u32 s46, s46, 1\n"
    "s_branch .Lxd_ht25r\n"
    ".Lxd_ck26:\n"
    "v_add_u32 v13, 12, v25\n"
    "s_call_b64 s[60:61], .Lxd_slow\n"
    "s_cmp_eq_u32 s45, -1\n"
    "s_cbranch_scc0 .Lxd_ck26r\n"
    "s_call_b64 s[60:61], .Lxd_last\n"
    "v_fma_f32 v9, v79, v83, v11\n"
    "s_branch .Lxd_fin\n"
    ".Lxd_dr29:\n"
    "v_add_u32 v13, 16, v25\n"
    "s_call_b64 s[60:61], .Lxd_mark\n"
    "s_branch .Lxd_dr29r\n"
    ".Lxd_ck30:\n"
    "v_add_u32 v13, 16, v25\n"
    "s_call_b64 s[60:61], .Lxd_slow\n"
    "s_cmp_eq_u32 s45, -1\n"
    "s_cbranch_scc0 .Lxd_ck30r\n"
    "s_call_b64 s[60:61], .Lxd_last\n"
    "v_fma_f32 v10, v79, v83, v11\n"
    "s_branch .Lxd_fin\n"
    ".Lxd_ck33:\n"
    "v_add_u32 v13, 4, v25\n"
    "s_call_b64 s[60:61], .Lxd_slow\n"
    "s_cmp_eq_u32 s45, -1\n"
    "s_cbranch_scc0 .Lxd_ck33r\n"
    "s_call_b64 s[60:61], .Lxd_last\n"
    "v_fma_f32 v7, v79, v83, v11\n"
    "s_branch .Lxd_fin\n"
    ".Lxd_ht36:\n"
    "global_load_lds_dword v26, s[8:9]\n"
    "v_mov_b32 v26, v27\n"
    "v_mov_b32 v27, v42\n"
    "s_sub_u32 s46, s46, 1\n"
    "s_branch .Lxd_ht36r\n"
    ".Lxd_ck37:\n"
    "v_add_u32 v13, 8, v25\n"
    "s_call_b64 s[60:61], .Lxd_slow\n"
    "s_cmp_eq_u32 s45, -1\n"
    "s_cbranch_scc0 .Lxd_ck37r\n"
    "s_call_b64 s[60:61], .Lxd_last\n"
    "v_fma_f32 v8, v79, v83, v11\n"
    "s_branch .Lxd_fin\n"
    ".Lxd_ht40:\n"
    "global_load_lds_dword v26, s[8:9]\n"
    "v_mov_b32 v26, v27\n"
    "v_mov_b32 v27, v42\n"
    "s_sub_u32 s46, s46, 1\n"
    "s_branch .Lxd_ht40r\n"
    ".Lxd_ck41:\n"
    "v_add_u32 v13, 12, v25\n"
    "s_call_b64 s[60:61], .Lxd_slow\n"
    "s_cmp_eq_u32 s45, -1\n"
    "s_cbranch_scc0 .Lxd_ck41r\n"
    "s_call_b64 s[60:61], .Lxd_last\n"
    "v_fma_f32 v9, v79, v83, v11\n"
    "s_branch .Lxd_fin\n"
    ".Lxd_dr44:\n"
    "v_add_u32 v13, 16, v25\n"
    "s_call_b64 s[60:61], .Lxd_mark\n"
    "s_branch .Lxd_dr44r\n"
    ".Lxd_on45:\n"
    "v_mov_b32 v24, 1\n"
    "s_and_b32 s40, s45, 15\n"
    "s_cbranch_scc1 .Lxd_dr44\n"
    "s_mov_b64 exec, 1\n"
    "global_atomic_add v24, v17, v24, s[52:53] sc0 sc1\n"
    "s_mov_b64 exec, -1\n"
    ".Lxd_dr44r:\n"
    "s_branch .Lxd_on45r\n"
    ".Lxd_ck46:\n"
    "v_add_u32 v13, 16, v25\n"
    "s_call_b64 s[60:61], .Lxd_slow\n"
    "s_cmp_eq_u32 s45, -1\n"
    "s_cbranch_scc0 .Lxd_ck46r\n"
    "s_call_b64 s[60:61], .Lxd_last\n"
    "v_fma_f32 v10, v79, v83, v11\n"
    "s_branch .Lxd_fin\n"
    ".Lxd_f2:\n"
    "s_mov_b32 s59, 0x20000\n"
    ".Lxd_f4:\n"
    "s_call_b64 s[60:61], .Lxd_dsum\n"
    "s_cmp_eq_u32 s56, 2\n"
    "s_cbranch_scc1 .Lxd_f3\n"
    "s_sleep 2\n"
    "s_call_b64 s[60:61], .Lxd_gran\n"
    "s_sub_u32 s59, s59, 1\n"
    "s_cbranch_scc0 .Lxd_f4\n"
    "s_waitcnt vmcnt(0)\n"
    "s_branch .Lxd_f3\n"
    ".Lxd_on1:\n"
    "s_mov_b64 exec, 0x3ff\n"
    "global_load_dwordx2 v[26:27], v11, s[16:17] sc1\n"
    "s_mov_b64 exec, -1\n"
    "s_branch .Lxd_on1r\n"
    ".Lxd_pv3:\n"
    "s_mov_b32 s59, 0x20000\n"
    ".Lxd_pv3l:\n"
    "s_sleep 1\n"
    "s_mov_b64 exec, 0x3ff\n"
    "global_load_dwordx2 v[26:27], v11, s[16:17] sc1\n"
    "s_mov_b64 exec, -1\n"
    "s_waitcnt vmcnt(0)\n"
    "v_readlane_b32 s25, v26, 9\n"
    "v_readlane_b32 s40, v26, 8\n"
    "v_readlane_b32 s41, v27, 8\n"
    "s_mul_i32 s42, s25, 0x9e3779b1\n"
    "s_lshr_b32 s42, s42, 24\n"
    "s_xor_b32 s42, s42, s40\n"
    "s_and_b32 s42, s42, 0xff\n"
    "s_xor_b32 s41, s41, s25\n"
    "s_xor_b32 s41, s41, 0xa5c30f96\n"
    "s_or_b32 s42, s41, s42\n"
    "s_lshr_b32 s41, s40, 8\n"
    "s_mul_i32 s40, s41, 0x9e3779b1\n"
    "s_xor_b32 s41, s41, 0xa5c30f96\n"
    "s_lshr_b32 s40, s40, 24\n"
    "v_xor_b32 v13, s40, v26\n"
    "v_xor_b32 v22, s41, v27\n"
    "v_lshl_or_b32 v13, v13, 24, v22\n"
    "v_cmp_ne_u32 s[66:67], 0, v13\n"
    "s_and_b32 s66, s66, 0xff\n"
    "s_or_b32 s42, s42, s66\n"
    "s_cmp_eq_u32 s42, 0\n"
    "s_cbranch_scc1 .Lxd_pv3r\n"
    "s_sub_u32 s59, s59, 1\n"
    "s_cbranch_scc0 .Lxd_pv3l\n"
    "s_branch .Lxd_pv3r\n"
    ".Lxd_on6:\n"
    "v_mov_b32 v24, 1\n"
    "s_mov_b64 exec, 1\n"
    "global_atomic_add v24, v17, v24, s[52:53] sc0 sc1\n"
    "s_mov_b64 exec, -1\n"
    "s_branch .Lxd_on6r\n"
    ".Lxd_ck7:\n"
    "v_mov_b32 v13, s58\n"
    "s_call_b64 s[60:61], .Lxd_slow\n"
    "s_branch .Lxd_ck7r\n"
    ".Lxd_tf8:\n"
    "v_mov_b32 v12, -1\n"
    "v_mov_b32 v13, v25\n"
    "s_call_b64 s[60:61], .Lxd_mark\n"
    "s_branch .Lxd_tf8r\n"
    ".Lxd_on9:\n"
    "s_waitcnt vmcnt(0)\n"
    "v_readfirstlane_b32 s40, v24\n"
    "s_or_b32 s40, s40, s44\n"
    "s_lshl1_add_u32 s41, s40, s54\n"
    "s_cmpk_lt_u32 s40, 256\n"
    "s_cselect_b32 s41, s41, 2\n"
    "v_mov_b32 v12, s41\n"
    "s_cbranch_scc0 .Lxd_tf8\n"
    ".Lxd_tf8r:\n"
    "s_mul_i32 s41, s41, 52\n"
    "s_add_u32 s38, s4, s41\n"
    "s_addc_u32 s39, s5, 0\n"
    "ds_write_b32 v25, v12 offset:20\n"
    "global_load_dwordx3 v[36:38], v17, s[38:39] offset:-104\n"
    "global_load_dword v41, v18, s[38:39] offset:-8\n"
    "v_mov_b32 v13, s58\n"
    "s_branch .Lxd_on9r\n"
    ".Lxd_on10:\n"
    "v_mov_b32 v24, 1\n"
    "s_mov_b64 exec, 1\n"
    "global_atomic_add v24, v17, v24, s[52:53] sc0 sc1\n"
    "s_mov_b64 exec, -1\n"
    "s_branch .Lxd_on10r\n"
    ".Lxd_on11:\n"
    "s_waitcnt vmcnt(0)\n"
    "v_lshl_or_b32 v36, v36, 7, v19\n"
    "v_lshl_or_b32 v37, v37, 7, v19\n"
    "v_lshl_or_b32 v38, v38, 7, v19\n"
    "v_lshl_or_b32 v41, v41, 7, v20\n"
    "global_load_dwordx4 v[28:31], v36, s[8:9]\n"
    "global_load_dwordx4 v[32:35], v37, s[8:9]\n"
    "global_load_dword v40, v41, s[8:9]\n"
    "global_load_dwordx4 v[36:39], v38, s[8:9]\n"
    "s_branch .Lxd_on11r\n"
    ".Lxd_tf12:\n"
    "v_mov_b32 v12, -1\n"
    "v_mov_b32 v13, v25\n"
    "s_call_b64 s[60:61], .Lxd_mark\n"
    "s_branch .Lxd_tf12r\n"
    ".Lxd_on13:\n"
    "s_waitcnt vmcnt(0)\n"
    "v_readfirstlane_b32 s40, v24\n"
    "s_or_b32 s40, s40, s44\n"
    "s_lshl1_add_u32 s41, s40, s54\n"
    "s_cmpk_lt_u32 s40, 256\n"
    "s_cselect_b32 s41, s41, 2\n"
    "v_mov_b32 v12, s41\n"
    "s_cbranch_scc0 .Lxd_tf12\n"
    ".Lxd_tf12r:\n"
    "s_mul_i32 s41, s41, 52\n"
    "s_add_u32 s38, s4, s41\n"
    "s_addc_u32 s39, s5, 0\n"
    "ds_write_b32 v25, v12 offset:20\n"
    "global_load_dwordx3 v[36:38], v17, s[38:39] offset:-104\n"
    "global_load_dword v41, v18, s[38:39] offset:-8\n"
    "s_branch .Lxd_on13r\n"
    ".Lxd_on14:\n"
    "v_mov_b32 v24, 1\n"
    "s_mov_b64 exec, 1\n"
    "global_atomic_add v24, v17, v24, s[52:53] sc0 sc1\n"
    "s_mov_b64 exec, -1\n"
    "s_branch .Lxd_on14r\n"
    ".Lxd_pv59:\n"
    "s_mov_b32 s59, 0x20000\n"
    ".Lxd_pv59l:\n"
    "s_sleep 1\n"
    "s_mov_b64 exec, 0x3ff\n"
    "global_load_dwordx2 v[26:27], v11, s[16:17] sc1\n"
    "s_mov_b64 exec, -1\n"
    "s_waitcnt vmcnt(0)\n"
    "v_readlane_b32 s25, v26, 9\n"
    "v_readlane_b32 s40, v26, 8\n"
    "v_readlane_b32 s41, v27, 8\n"
    "s_mul_i32 s42, s25, 0x9e3779b1\n"
    "s_lshr_b32 s42, s42, 24\n"
    "s_xor_b32 s42, s42, s40\n"
    "s_and_b32 s42, s42, 0xff\n"
    "s_xor_b32 s41, s41, s25\n"
    "s_xor_b32 s41, s41, 0xa5c30f96\n"
    "s_or_b32 s42, s41, s42\n"
    "s_lshr_b32 s41, s40, 8\n"
    "s_mul_i32 s40, s41, 0x9e3779b1\n"
    "s_xor_b32 s41, s41, 0xa5c30f96\n"
    "s_lshr_b32 s40, s40, 24\n"
    "v_xor_b32 v13, s40, v26\n"
    "v_xor_b32 v22, s41, v27\n"
    "v_lshl_or_b32 v13, v13, 24, v22\n"
    "v_cmp_ne_u32 s[66:67], 0, v13\n"
    "s_and_b32 s66, s66, 0xff\n"
    "s_or_b32 s42, s42, s66\n"
    "s_cmp_eq_u32 s42, 0\n"
    "s_cbranch_scc1 .Lxd_pv59r\n"
    "s_sub_u32 s59, s59, 1\n"
    "s_cbranch_scc0 .Lxd_pv59l\n"
    "s_branch .Lxd_pv59r\n"
    ".Lxd_end:\n"
    : "+{v0}"(tid) : "{s[0:1]}"(__builtin_amdgcn_kernarg_segment_ptr()), "{s2}"(blockIdx.x)
    : "memory", "vcc", "scc", "v1", "v2", "v3", "v4", "v5", "v6", "v7", "v8", "v9", "v10", "v11", "v12", "v13", "v14", "v15", "v16", "v17", "v18", "v19", "v20", "v21", "v22", "v23", "v24", "v25", "v26", "v27", "v28", "v29", "v30", "v31", "v32", "v33", "v34", "v35", "v36", "v37", "v38", "v39", "v40", "v41", "v42", "v43", "v44", "v45", "v46", "v47", "v48", "v49", "v50", "v51", "v52", "v53", "v54", "v55", "v56", "v57", "v58", "v59", "v60", "v61", "v62", "v63", "v64", "v65", "v66", "v67", "v68", "v69", "v70", "v71", "v72", "v73", "v74", "v75", "v76", "v77", "v78", "v79", "v80", "v81", "v82", "v83", "v84", "v85", "v86", "v87", "v88", "v89", "v90", "v91", "v92", "v93", "v94", "v95", "v96", "v97", "v98", "v99", "v100", "v101", "v102", "v103", "v104", "v105", "v106", "v107", "v108", "v109", "v110", "v111", "v112", "v113", "v114", "v115", "v116", "v117", "v118", "v119", "v120", "v121", "v122", "v123", "v124", "v125", "v126", "v127", "v128", "v129", "v130", "v131", "v132", "v133", "v134", "v135", "v136", "v137", "v138", "v139", "v140", "v141", "v142", "v143", "v144", "v145", "v146", "v147", "v148", "v149", "v150", "v151", "v152", "v153", "v154", "v155", "v156", "v157", "v158", "v159", "v160", "v161", "v162", "v163", "v164", "v165", "v166", "v167", "v168", "v169", "v170", "v171", "v172", "v173", "v174", "v175", "v176", "v177", "v178", "v179", "v180", "v181", "v182", "v183", "v184", "v185", "v186", "v187", "v188", "v189", "v190", "v191", "v192", "v193", "v194", "v195", "v196", "v197", "v198", "v199", "v200", "v201", "v202", "v203", "v204", "v205", "v206", "v207", "v208", "v209", "v210", "v211", "v212", "v213", "v214", "v215", "v216", "v217", "v218", "v219", "v220", "v221", "v222", "v223", "v224", "v225", "v226", "v227", "v228", "v229", "v230", "v231", "v232", "v233", "v234", "v235", "v236", "v237", "v238", "v239", "v240", "v241", "v242", "v243", "v244", "v245", "v246", "v247", "v248", "v249", "v250", "v251", "v252", "v253", "v254", "v255", "s3", "s4", "s5", "s6", "s7", "s8", "s9", "s10", "s11", "s12", "s13", "s14", "s15", "s16", "s17", "s18", "s19", "s20", "s21", "s22", "s23", "s24", "s25", "s26", "s27", "s28", "s29", "s30", "s31", "s34", "s35", "s36", "s37", "s38", "s39", "s40", "s41", "s42", "s43", "s44", "s45", "s46", "s47", "s48", "s49", "s50", "s51", "s52", "s53", "s54", "s55", "s56", "s57", "s58", "s59", "s60", "s61", "s62", "s63", "s64", "s65", "s66", "s67", "s68", "s69", "s70", "s71");
  asm volatile("" :: "v"((__attribute__((address_space(3))) char*)shm));
}

extern "C" void kernel_launch(void* const* d_in, const int* in_sizes, int n_in, void* d_out, int out_size,
                              void* d_ws, size_t ws_size, hipStream_t stream) {
  xd_kernel<<<256, 512, 0, stream>>>((const int*)d_in[0], (const u16*)d_in[4], (const u16*)d_in[1], (const u16*)d_in[5], (const u16*)d_in[2], (const u16*)d_in[3], (char*)d_ws,
                                     (const u16*)d_in[6], (const u16*)d_in[7], (u16*)d_out);
}
```

```cpp
#include <hip/hip_runtime.h>
typedef unsigned short u16;

__device__ const __attribute__((aligned(16), used, visibility("hidden"))) u16 xd_tab[5120] asm("xd_tab") = {924, 925, 928, 929, 926, 927, 930, 931, 934, 935, 938, 939, 932, 933, 936, 937, 940, 941, 944, 945, 942, 943, 946, 947, 948, 0, 949, 0, 0, 0, 0, 0, 2, 36, 138, 172, 0, 1, 24, 25, 338, 372, 474, 508, 34, 35, 57, 58, 546, 580, 682, 716, 68, 69, 90, 91, 816, 817, 850, 952, 102, 99, 123, 124, 680, 681, 684, 685, 20, 21, 22, 23, 714, 715, 717, 718, 54, 55, 56, 53, 748, 749, 750, 751, 88, 89, 86, 87, 782, 779, 783, 784, 122, 119, 120, 121, 152, 153, 154, 155, 284, 285, 286, 287, 186, 187, 188, 185, 318, 319, 320, 317, 220, 221, 218, 219, 352, 353, 350, 351, 254, 251, 252, 253, 386, 383, 384, 385, 416, 417, 418, 419, 548, 549, 550, 551, 450, 451, 452, 449, 582, 583, 584, 581, 484, 485, 482, 483, 616, 617, 614, 615, 518, 515, 516, 517, 650, 647, 648, 649, 544, 545, 552, 553, 16, 17, 18, 19, 578, 579, 585, 586, 50, 51, 52, 49, 612, 613, 618, 619, 84, 85, 82, 83, 646, 643, 651, 652, 118, 115, 116, 117, 148, 149, 150, 151, 280, 281, 282, 283, 182, 183, 184, 181, 314, 315, 316, 313, 216, 217, 214, 215, 348, 349, 346, 347, 250, 247, 248, 249, 382, 379, 380, 381, 412, 413, 414, 415, 408, 409, 420, 421, 446, 447, 448, 445, 442, 443, 453, 454, 480, 481, 478, 479, 476, 477, 486, 487, 514, 511, 512, 513, 510, 507, 519, 520, 12, 13, 14, 15, 144, 145, 146, 147, 46, 47, 48, 45, 178, 179, 180, 177, 80, 81, 78, 79, 212, 213, 210, 211, 114, 111, 112, 113, 246, 243, 244, 245, 276, 277, 278, 279, 272, 273, 288, 289, 310, 311, 312, 309, 306, 307, 321, 322, 344, 345, 342, 343, 340, 341, 354, 355, 378, 375, 376, 377, 374, 371, 387, 388, 8, 9, 10, 11, 140, 141, 142, 143, 42, 43, 44, 41, 174, 175, 176, 173, 76, 77, 74, 75, 208, 209, 206, 207, 110, 107, 108, 109, 242, 239, 240, 241, 136, 137, 156, 157, 4, 5, 6, 7, 170, 171, 189, 190, 38, 39, 40, 37, 204, 205, 222, 223, 72, 73, 70, 71, 238, 235, 255, 256, 106, 103, 104, 105, 0, 0, 0, 0, 0, 0, 0, 0, 0, 0, 0, 0, 0, 0, 0, 0, 0, 0, 0, 0, 0, 0, 0, 0, 0, 0, 0, 0, 0, 0, 0, 0, 0, 0, 0, 0, 0, 0, 0, 0, 0, 0, 0, 0, 0, 0, 0, 0, 0, 0, 0, 0, 0, 0, 0, 0, 0, 0, 0, 0, 0, 0, 0, 0, 0, 0, 0, 0, 0, 0, 0, 0, 0, 0, 0, 0, 0, 0, 0, 0, 0, 0, 0, 0, 0, 0, 0, 0, 0, 0, 0, 0, 0, 0, 0, 0, 0, 0, 0, 0, 0, 0, 0, 0, 0, 0, 0, 0, 0, 0, 0, 0, 0, 0, 0, 0, 0, 0, 0, 0, 0, 0, 0, 0, 0, 0, 0, 0, 924, 925, 926, 927, 934, 935, 932, 933, 940, 941, 942, 943, 948, 0, 0, 0, 2, 36, 0, 1, 338, 372, 34, 35, 546, 580, 68, 69, 816, 817, 102, 99, 928, 929, 930, 931, 938, 939, 936, 937, 944, 945, 946, 947, 949, 0, 0, 0, 138, 172, 24, 25, 474, 508, 57, 58, 682, 716, 90, 91, 850, 952, 123, 124, 680, 681, 20, 21, 714, 715, 54, 55, 748, 749, 88, 89, 782, 779, 122, 119, 152, 153, 284, 285, 186, 187, 318, 319, 220, 221, 352, 353, 254, 251, 386, 383, 684, 685, 22, 23, 717, 718, 56, 53, 750, 751, 86, 87, 783, 784, 120, 121, 154, 155, 286, 287, 188, 185, 320, 317, 218, 219, 350, 351, 252, 253, 384, 385, 416, 417, 548, 549, 450, 451, 582, 583, 484, 485, 616, 617, 518, 515, 650, 647, 544, 545, 16, 17, 578, 579, 50, 51, 612, 613, 84, 85, 646, 643, 118, 115, 418, 419, 550, 551, 452, 449, 584, 581, 482, 483, 614, 615, 516, 517, 648, 649, 552, 553, 18, 19, 585, 586, 52, 49, 618, 619, 82, 83, 651, 652, 116, 117, 148, 149, 280, 281, 182, 183, 314, 315, 216, 217, 348, 349, 250, 247, 382, 379, 412, 413, 408, 409, 446, 447, 442, 443, 480, 481, 476, 477, 514, 511, 510, 507, 150, 151, 282, 283, 184, 181, 316, 313, 214, 215, 346, 347, 248, 249, 380, 381, 414, 415, 420, 421, 448, 445, 453, 454, 478, 479, 486, 487, 512, 513, 519, 520, 12, 13, 144, 145, 46, 47, 178, 179, 80, 81, 212, 213, 114, 111, 246, 243, 276, 277, 272, 273, 310, 311, 306, 307, 344, 345, 340, 341, 378, 375, 374, 371, 14, 15, 146, 147, 48, 45, 180, 177, 78, 79, 210, 211, 112, 113, 244, 245, 278, 279, 288, 289, 312, 309, 321, 322, 342, 343, 354, 355, 376, 377, 387, 388, 8, 9, 140, 141, 42, 43, 174, 175, 76, 77, 208, 209, 110, 107, 242, 239, 136, 137, 4, 5, 170, 171, 38, 39, 204, 205, 72, 73, 238, 235, 106, 103, 10, 11, 142, 143, 44, 41, 176, 173, 74, 75, 206, 207, 108, 109, 240, 241, 156, 157, 6, 7, 189, 190, 40, 37, 222, 223, 70, 71, 255, 256, 104, 105, 0, 0, 0, 0, 0, 0, 0, 0, 0, 0, 0, 0, 0, 0, 0, 0, 0, 0, 0, 0, 0, 0, 0, 0, 0, 0, 0, 0, 0, 0, 0, 0, 0, 0, 0, 0, 0, 0, 0, 0, 0, 0, 0, 0, 0, 0, 0, 0, 0, 0, 0, 0, 0, 0, 0, 0, 0, 0, 0, 0, 0, 0, 0, 0, 0, 0, 0, 0, 0, 0, 0, 0, 0, 0, 0, 0, 0, 0, 0, 0, 0, 0, 0, 0, 0, 0, 0, 0, 0, 0, 0, 0, 0, 0, 0, 0, 0, 0, 0, 0, 0, 0, 0, 0, 0, 0, 0, 0, 0, 0, 0, 0, 0, 0, 0, 0, 0, 0, 0, 0, 0, 0, 0, 0, 0, 0, 0, 0, 0, 0, 0, 0, 0, 0, 0, 0, 0, 0, 0, 0, 0, 0, 0, 0, 0, 0, 0, 0, 0, 0, 0, 0, 0, 0, 0, 0, 0, 0, 0, 0, 0, 16256, 0, 16256, 0, 16256, 0, 16256, 0, 16256, 0, 16256, 0, 16256, 0, 16256, 0, 16256, 0, 16256, 0, 16256, 0, 16256, 0, 16128, 0, 16256, 0, 16128, 0, 0, 0, 16128, 0, 16256, 0, 16256, 0, 16256, 0, 16128, 0, 16256, 0, 16256, 0, 16256, 0, 16128, 0, 16256, 0, 16256, 0, 16256, 0, 16128, 0, 16256, 0, 16256, 0, 16256, 0, 16256, 0, 16256, 0, 16256, 0, 16256, 0, 16256, 0, 16256, 0, 16256, 0, 16256, 0, 16256, 0, 16256, 0, 16256, 0, 16256, 0, 16256, 0, 16256, 0, 16256, 0, 16256, 0, 16256, 0, 16256, 0, 16256, 0, 16256, 0, 16256, 0, 16256, 0, 16256, 0, 16256, 0, 16256, 0, 16256, 0, 16256, 0, 16256, 0, 16256, 0, 16256, 0, 16256, 0, 16256, 0, 16128, 0, 16256, 0, 16256, 0, 16256, 0, 16128, 0, 16256, 0, 16256, 0, 16256, 0, 16128, 0, 16256, 0, 16256, 0, 16256, 0, 16128, 0, 16256, 0, 16256, 0, 16256, 0, 16256, 0, 16256, 0, 16256, 0, 16256, 0, 16256, 0, 16256, 0, 16256, 0, 16256, 0, 16256, 0, 16256, 0, 16256, 0, 16256, 0, 16256, 0, 16256, 0, 16256, 0, 16256, 0, 16256, 0, 16256, 0, 16256, 0, 16256, 0, 16256, 0, 16256, 0, 16256, 0, 16256, 0, 16256, 0, 16256, 0, 16256, 0, 16256, 0, 16256, 0, 16256, 0, 16256, 0, 16256, 0, 16256, 0, 16256, 0, 16256, 0, 16256, 0, 16256, 0, 16256, 0, 16256, 0, 16256, 0, 16256, 0, 16256, 0, 16256, 0, 16256, 0, 16256, 0, 16256, 0, 16256, 0, 16256, 0, 16256, 0, 16256, 0, 16256, 0, 16256, 0, 16256, 0, 16256, 0, 16256, 0, 16256, 0, 16256, 0, 16256, 0, 16256, 0, 16256, 0, 16256, 0, 16256, 0, 16256, 0, 16256, 0, 16256, 0, 16256, 0, 16256, 0, 16256, 0, 16256, 0, 16256, 0, 16256, 0, 16256, 0, 16256, 0, 16256, 0, 16256, 0, 16256, 0, 16256, 0, 16256, 0, 16256, 0, 16256, 0, 16128, 0, 16256, 0, 16256, 0, 16256, 0, 16128, 0, 16256, 0, 16256, 0, 16256, 0, 16128, 0, 16256, 0, 16256, 0, 16256, 0, 16128, 0, 16256, 0, 16256, 0, 16256, 0, 0, 0, 0, 0, 0, 0, 0, 0, 0, 0, 0, 0, 0, 0, 0, 0, 0, 0, 0, 0, 0, 0, 0, 0, 0, 0, 0, 0, 0, 0, 0, 0, 0, 0, 0, 0, 0, 0, 0, 0, 0, 0, 0, 0, 0, 0, 0, 0, 0, 0, 0, 0, 0, 0, 0, 0, 0, 0, 0, 0, 0, 0, 0, 0, 0, 0, 0, 0, 0, 0, 0, 0, 0, 0, 0, 0, 0, 0, 0, 0, 0, 0, 0, 0, 0, 0, 0, 0, 0, 0, 0, 0, 0, 0, 0, 0, 0, 0, 0, 0, 0, 0, 0, 0, 0, 0, 0, 0, 0, 0, 0, 0, 0, 0, 0, 0, 0, 0, 0, 0, 0, 0, 0, 0, 0, 0, 0, 0, 0, 0, 0, 0, 0, 0, 0, 0, 0, 0, 0, 0, 0, 0, 0, 0, 0, 0, 0, 0, 0, 0, 0, 0, 0, 0, 0, 0, 0, 0, 0, 0, 16128, 0, 16256, 0, 16256, 0, 16256, 0, 16128, 0, 16256, 0, 16256, 0, 16256, 0, 16128, 0, 16256, 0, 16256, 0, 16256, 0, 16128, 0, 16256, 0, 16256, 0, 16256, 0, 16256, 0, 16256, 0, 16256, 0, 16256, 0, 16256, 0, 16256, 0, 16256, 0, 16256, 0, 16256, 0, 16256, 0, 16256, 0, 16256, 0, 16256, 0, 16256, 0, 16256, 0, 16256, 0, 16256, 0, 16256, 0, 16256, 0, 16256, 0, 16256, 0, 16256, 0, 16256, 0, 16256, 0, 16256, 0, 16256, 0, 16256, 0, 16256, 0, 16256, 0, 16256, 0, 16256, 0, 16256, 0, 16256, 0, 16256, 0, 16256, 0, 16256, 0, 16256, 0, 16256, 0, 16256, 0, 16256, 0, 16256, 0, 16256, 0, 16256, 0, 16256, 0, 16256, 0, 16256, 0, 16256, 0, 16256, 0, 16256, 0, 16256, 0, 16256, 0, 16256, 0, 16256, 0, 16256, 0, 16256, 0, 16256, 0, 16256, 0, 16256, 0, 16256, 0, 16256, 0, 16256, 0, 16256, 0, 16256, 0, 16256, 0, 16256, 0, 16256, 0, 16256, 0, 16256, 0, 16256, 0, 16256, 0, 16256, 0, 16256, 0, 16256, 0, 16256, 0, 16256, 0, 16256, 0, 16256, 0, 16256, 0, 16256, 0, 16256, 0, 16128, 0, 16256, 0, 16256, 0, 16256, 0, 16128, 0, 16256, 0, 16256, 0, 16256, 0, 16128, 0, 16256, 0, 16256, 0, 16256, 0, 16128, 0, 16256, 0, 16256, 0, 16256, 0, 16256, 0, 16256, 0, 16256, 0, 16256, 0, 16256, 0, 16256, 0, 16256, 0, 16256, 0, 16256, 0, 16256, 0, 16256, 0, 16256, 0, 16256, 0, 16256, 0, 16256, 0, 16256, 0, 16128, 0, 16256, 0, 16256, 0, 16256, 0, 16128, 0, 16256, 0, 16256, 0, 16256, 0, 16128, 0, 16256, 0, 16256, 0, 16256, 0, 16128, 0, 16256, 0, 16256, 0, 16256, 0, 16256, 0, 16256, 0, 16256, 0, 16256, 0, 16256, 0, 16256, 0, 16256, 0, 16256, 0, 16256, 0, 16256, 0, 16256, 0, 16256, 0, 16256, 0, 16256, 0, 16256, 0, 16256, 0, 16256, 0, 16256, 0, 16256, 0, 16256, 0, 16256, 0, 16256, 0, 16256, 0, 16256, 0, 16256, 0, 16256, 0, 16256, 0, 16256, 0, 16256, 0, 16256, 0, 16256, 0, 16256, 0, 0, 0, 0, 0, 0, 0, 0, 0, 0, 0, 0, 0, 0, 0, 0, 0, 0, 0, 0, 0, 0, 0, 0, 0, 0, 0, 0, 0, 0, 0, 0, 0, 0, 0, 0, 0, 0, 0, 0, 0, 0, 0, 0, 0, 0, 0, 0, 0, 0, 0, 0, 0, 0, 0, 0, 0, 0, 0, 0, 0, 0, 0, 0, 0, 0, 0, 0, 0, 0, 0, 0, 0, 0, 0, 0, 0, 0, 0, 0, 0, 0, 0, 0, 0, 0, 0, 0, 0, 0, 0, 0, 0, 0, 0, 0, 0, 0, 0, 0, 0, 0, 0, 0, 0, 0, 0, 0, 0, 0, 0, 0, 0, 0, 0, 0, 0, 0, 0, 0, 0, 0, 0, 0, 0, 0, 0, 0, 0, 16768, 0, 16768, 0, 16768, 0, 16768, 0, 16768, 0, 16768, 0, 16768, 0, 16768, 0, 16768, 0, 16768, 0, 16768, 0, 16768, 0, 16768, 0, 0, 0, 16768, 0, 0, 0, 16256, 0, 16256, 0, 16256, 0, 16256, 0, 16256, 0, 16256, 0, 16256, 0, 16256, 0, 16256, 0, 16256, 0, 16256, 0, 16256, 0, 16128, 0, 16256, 0, 16128, 0, 17280, 0, 16128, 0, 16256, 0, 16256, 0, 16256, 0, 16128, 0, 16256, 0, 16256, 0, 16256, 0, 16128, 0, 16256, 0, 16256, 0, 16256, 0, 16128, 0, 16256, 0, 16256, 0, 16256, 0, 16256, 0, 16256, 0, 16256, 0, 16256, 0, 16256, 0, 16256, 0, 16256, 0, 16256, 0, 16256, 0, 16256, 0, 16256, 0, 16256, 0, 16256, 0, 16256, 0, 16256, 0, 16256, 0, 16256, 0, 16256, 0, 16256, 0, 16256, 0, 16256, 0, 16256, 0, 16256, 0, 16256, 0, 16256, 0, 16256, 0, 16256, 0, 16256, 0, 16256, 0, 16256, 0, 16256, 0, 16256, 0, 16128, 0, 16256, 0, 16256, 0, 16256, 0, 16128, 0, 16256, 0, 16256, 0, 16256, 0, 16128, 0, 16256, 0, 16256, 0, 16256, 0, 16128, 0, 16256, 0, 16256, 0, 16256, 0, 16256, 0, 16256, 0, 16256, 0, 16256, 0, 16256, 0, 16256, 0, 16256, 0, 16256, 0, 16256, 0, 16256, 0, 16256, 0, 16256, 0, 16256, 0, 16256, 0, 16256, 0, 16256, 0, 16256, 0, 16256, 0, 16256, 0, 16256, 0, 16256, 0, 16256, 0, 16256, 0, 16256, 0, 16256, 0, 16256, 0, 16256, 0, 16256, 0, 16256, 0, 16256, 0, 16256, 0, 16256, 0, 16256, 0, 16256, 0, 16256, 0, 16256, 0, 16256, 0, 16256, 0, 16256, 0, 16256, 0, 16256, 0, 16256, 0, 16256, 0, 16256, 0, 16256, 0, 16256, 0, 16256, 0, 16256, 0, 16256, 0, 16256, 0, 16256, 0, 16256, 0, 16256, 0, 16256, 0, 16256, 0, 16256, 0, 16256, 0, 16256, 0, 16256, 0, 16256, 0, 16256, 0, 16256, 0, 16256, 0, 16256, 0, 16256, 0, 16256, 0, 16256, 0, 16256, 0, 16256, 0, 16256, 0, 16256, 0, 16256, 0, 16256, 0, 16256, 0, 16256, 0, 16256, 0, 16256, 0, 16256, 0, 16256, 0, 16256, 0, 16128, 0, 16256, 0, 16256, 0, 16256, 0, 16128, 0, 16256, 0, 16256, 0, 16256, 0, 16128, 0, 16256, 0, 16256, 0, 16256, 0, 16128, 0, 16256, 0, 16256, 0, 16256, 0, 0, 0, 0, 0, 0, 0, 0, 0, 0, 0, 0, 0, 0, 0, 0, 0, 0, 0, 0, 0, 0, 0, 0, 0, 0, 0, 0, 0, 0, 0, 0, 0, 0, 0, 0, 0, 0, 0, 0, 0, 0, 0, 0, 0, 0, 0, 0, 0, 0, 0, 0, 0, 0, 0, 0, 0, 0, 0, 0, 0, 0, 0, 0, 0, 0, 0, 0, 0, 0, 0, 0, 0, 0, 0, 0, 0, 0, 0, 0, 0, 0, 0, 0, 0, 0, 0, 0, 0, 0, 0, 0, 0, 0, 0, 0, 0, 0, 0, 0, 0, 0, 0, 0, 0, 0, 0, 0, 0, 0, 0, 0, 0, 0, 0, 0, 0, 0, 0, 0, 0, 0, 0, 0, 0, 0, 0, 0, 0, 16768, 0, 16768, 0, 16768, 0, 16768, 0, 16768, 0, 16768, 0, 16768, 0, 16768, 0, 16768, 0, 16768, 0, 16768, 0, 16768, 0, 0, 0, 0, 0, 0, 0, 0, 0, 16128, 0, 16256, 0, 16256, 0, 16256, 0, 16128, 0, 16256, 0, 16256, 0, 16256, 0, 16128, 0, 16256, 0, 16256, 0, 16256, 0, 16128, 0, 16256, 0, 16256, 0, 16256, 0, 16256, 0, 16256, 0, 16256, 0, 16256, 0, 16256, 0, 16256, 0, 16256, 0, 16256, 0, 16256, 0, 16256, 0, 16256, 0, 16256, 0, 16256, 0, 16256, 0, 16256, 0, 16256, 0, 16256, 0, 16256, 0, 16256, 0, 16256, 0, 16256, 0, 16256, 0, 16256, 0, 16256, 0, 16256, 0, 16256, 0, 16256, 0, 16256, 0, 16256, 0, 16256, 0, 16256, 0, 16256, 0, 16256, 0, 16256, 0, 16256, 0, 16256, 0, 16256, 0, 16256, 0, 16256, 0, 16256, 0, 16256, 0, 16256, 0, 16256, 0, 16256, 0, 16256, 0, 16256, 0, 16256, 0, 16256, 0, 16256, 0, 16256, 0, 16256, 0, 16256, 0, 16256, 0, 16256, 0, 16256, 0, 16256, 0, 16256, 0, 16256, 0, 16256, 0, 16256, 0, 16256, 0, 16256, 0, 16256, 0, 16256, 0, 16256, 0, 16256, 0, 16256, 0, 16256, 0, 16256, 0, 16256, 0, 16256, 0, 16256, 0, 16256, 0, 16256, 0, 16256, 0, 16256, 0, 16256, 0, 16256, 0, 16256, 0, 16256, 0, 16128, 0, 16256, 0, 16256, 0, 16256, 0, 16128, 0, 16256, 0, 16256, 0, 16256, 0, 16128, 0, 16256, 0, 16256, 0, 16256, 0, 16128, 0, 16256, 0, 16256, 0, 16256, 0, 16256, 0, 16256, 0, 16256, 0, 16256, 0, 16256, 0, 16256, 0, 16256, 0, 16256, 0, 16256, 0, 16256, 0, 16256, 0, 16256, 0, 16256, 0, 16256, 0, 16256, 0, 16256, 0, 16128, 0, 16256, 0, 16256, 0, 16256, 0, 16128, 0, 16256, 0, 16256, 0, 16256, 0, 16128, 0, 16256, 0, 16256, 0, 16256, 0, 16128, 0, 16256, 0, 16256, 0, 16256, 0, 16256, 0, 16256, 0, 16256, 0, 16256, 0, 16256, 0, 16256, 0, 16256, 0, 16256, 0, 16256, 0, 16256, 0, 16256, 0, 16256, 0, 16256, 0, 16256, 0, 16256, 0, 16256, 0, 16256, 0, 16256, 0, 16256, 0, 16256, 0, 16256, 0, 16256, 0, 16256, 0, 16256, 0, 16256, 0, 16256, 0, 16256, 0, 16256, 0, 16256, 0, 16256, 0, 16256, 0, 16256, 0, 0, 0, 0, 0, 0, 0, 0, 0, 0, 0, 0, 0, 0, 0, 0, 0, 0, 0, 0, 0, 0, 0, 0, 0, 0, 0, 0, 0, 0, 0, 0, 0, 0, 0, 0, 0, 0, 0, 0, 0, 0, 0, 0, 0, 0, 0, 0, 0, 0, 0, 0, 0, 0, 0, 0, 0, 0, 0, 0, 0, 0, 0, 0, 0, 0, 0, 0, 0, 0, 0, 0, 0, 0, 0, 0, 0, 0, 0, 0, 0, 0, 0, 0, 0, 0, 0, 0, 0, 0, 0, 0, 0, 0, 0, 0, 0, 0, 0, 0, 0, 0, 0, 0, 0, 0, 0, 0, 0, 0, 0, 0, 0, 0, 0, 0, 0, 0, 0, 0, 0, 0, 0, 0, 0, 0, 0, 0, 0, 0, 0, 0, 0, 0, 0, 0, 0, 0, 0, 0, 0, 0, 0, 0, 0, 16256, 0, 16256, 0, 16128, 0, 16256, 0, 16256, 0, 16256, 0, 16128, 0, 16256, 0, 0, 0, 0, 0, 0, 0, 0, 0, 0, 0, 0, 0, 0, 0, 0, 0, 16256, 0, 16256, 0, 16256, 0, 16256, 0, 16256, 0, 16256, 0, 16256, 0, 16256, 0, 16128, 0, 16256, 0, 16256, 0, 16256, 0, 16128, 0, 16256, 0, 16256, 0, 16256, 0, 16256, 0, 16256, 0, 16256, 0, 16256, 0, 16256, 0, 16256, 0, 16256, 0, 16256, 0, 16256, 0, 16256, 0, 16256, 0, 16256, 0, 16256, 0, 16256, 0, 16256, 0, 16256, 0, 16256, 0, 16256, 0, 16256, 0, 16256, 0, 16256, 0, 16256, 0, 16256, 0, 16256, 0, 16256, 0, 16256, 0, 16256, 0, 16256, 0, 16256, 0, 16256, 0, 16256, 0, 16256, 0, 16128, 0, 16256, 0, 16256, 0, 16256, 0, 16128, 0, 16256, 0, 16256, 0, 16256, 0, 16256, 0, 16256, 0, 16256, 0, 16256, 0, 16256, 0, 16256, 0, 16256, 0, 16256, 0, 16256, 0, 16256, 0, 16256, 0, 16256, 0, 16256, 0, 16256, 0, 16256, 0, 16256, 0, 16256, 0, 16256, 0, 16256, 0, 16256, 0, 16256, 0, 16256, 0, 16256, 0, 16256, 0, 16256, 0, 16256, 0, 16128, 0, 16256, 0, 16256, 0, 16256, 0, 16128, 0, 16256, 0, 16256, 0, 16256, 0, 16256, 0, 16256, 0, 16256, 0, 16256, 0, 16256, 0, 16256, 0, 16256, 0, 16256, 0, 16256, 0, 16256, 0, 16256, 0, 16256, 0, 16256, 0, 16256, 0, 16256, 0, 16256, 0, 16256, 0, 16256, 0, 16256, 0, 16256, 0, 16256, 0, 16256, 0, 16256, 0, 16256, 0, 16128, 0, 16256, 0, 16256, 0, 16256, 0, 16128, 0, 16256, 0, 16256, 0, 16256, 0, 16256, 0, 16256, 0, 16256, 0, 16256, 0, 16256, 0, 16256, 0, 16256, 0, 16256, 0, 16256, 0, 16256, 0, 16256, 0, 16256, 0, 16256, 0, 16256, 0, 16256, 0, 16256, 0, 16256, 0, 16256, 0, 16256, 0, 16256, 0, 16256, 0, 16256, 0, 16128, 0, 16256, 0, 16256, 0, 16256, 0, 16128, 0, 16256, 0, 16256, 0, 16256, 0, 16256, 0, 16256, 0, 16256, 0, 16256, 0, 16256, 0, 16256, 0, 16256, 0, 16256, 0, 16256, 0, 16256, 0, 16256, 0, 16256, 0, 16256, 0, 16256, 0, 16256, 0, 16256, 0, 0, 0, 0, 0, 0, 0, 0, 0, 0, 0, 0, 0, 0, 0, 0, 0, 0, 0, 0, 0, 0, 0, 0, 0, 0, 0, 0, 0, 0, 0, 0, 0, 0, 0, 0, 0, 0, 0, 0, 0, 0, 0, 0, 0, 0, 0, 0, 0, 0, 0, 0, 0, 0, 0, 0, 0, 0, 0, 0, 0, 0, 0, 0, 0, 0, 0, 0, 0, 0, 0, 0, 0, 0, 0, 0, 0, 0, 0, 0, 0, 0, 0, 0, 0, 0, 0, 0, 0, 0, 0, 0, 0, 0, 0, 0, 0, 0, 0, 0, 0, 0, 0, 0, 0, 0, 0, 0, 0, 0, 0, 0, 0, 0, 0, 0, 0, 0, 0, 0, 0, 0, 0, 0, 0, 0, 0, 0, 0, 0, 0, 0, 0, 0, 0, 0, 0, 0, 0, 0, 0, 0, 0, 0, 0, 16256, 0, 16256, 0, 16128, 0, 16256, 0, 16128, 0, 16256, 0, 16128, 0, 16256, 0, 0, 0, 0, 0, 0, 0, 0, 0, 0, 0, 0, 0, 0, 0, 0, 0, 16256, 0, 16256, 0, 16256, 0, 16256, 0, 16128, 0, 0, 0, 16256, 0, 16256, 0, 16128, 0, 16256, 0, 16256, 0, 16256, 0, 16128, 0, 16256, 0, 16256, 0, 16256, 0, 16256, 0, 16256, 0, 16256, 0, 16256, 0, 16256, 0, 16256, 0, 16256, 0, 16256, 0, 16256, 0, 16256, 0, 16256, 0, 16256, 0, 16256, 0, 16256, 0, 16256, 0, 16256, 0, 16256, 0, 16256, 0, 16256, 0, 16256, 0, 16256, 0, 16256, 0, 16256, 0, 16256, 0, 16256, 0, 16256, 0, 16256, 0, 16256, 0, 16256, 0, 16256, 0, 16256, 0, 16256, 0, 16128, 0, 16256, 0, 16256, 0, 16256, 0, 16128, 0, 16256, 0, 16256, 0, 16256, 0, 16256, 0, 16256, 0, 16256, 0, 16256, 0, 16256, 0, 16256, 0, 16256, 0, 16256, 0, 16256, 0, 16256, 0, 16256, 0, 16256, 0, 16256, 0, 16256, 0, 16256, 0, 16256, 0, 16256, 0, 16256, 0, 16256, 0, 16256, 0, 16256, 0, 16256, 0, 16256, 0, 16256, 0, 16256, 0, 16256, 0, 16128, 0, 16256, 0, 16256, 0, 16256, 0, 16128, 0, 16256, 0, 16256, 0, 16256, 0, 16256, 0, 16256, 0, 16256, 0, 16256, 0, 16256, 0, 16256, 0, 16256, 0, 16256, 0, 16256, 0, 16256, 0, 16256, 0, 16256, 0, 16256, 0, 16256, 0, 16256, 0, 16256, 0, 16256, 0, 16256, 0, 16256, 0, 16256, 0, 16256, 0, 16256, 0, 16256, 0, 16256, 0, 16128, 0, 16256, 0, 16256, 0, 16256, 0, 16128, 0, 16256, 0, 16256, 0, 16256, 0, 16256, 0, 16256, 0, 16256, 0, 16256, 0, 16256, 0, 16256, 0, 16256, 0, 16256, 0, 16256, 0, 16256, 0, 16256, 0, 16256, 0, 16256, 0, 16256, 0, 16256, 0, 16256, 0, 16256, 0, 16256, 0, 16256, 0, 16256, 0, 16256, 0, 16256, 0, 16128, 0, 16256, 0, 16256, 0, 16256, 0, 16128, 0, 16256, 0, 16256, 0, 16256, 0, 16256, 0, 16256, 0, 16256, 0, 16256, 0, 16256, 0, 16256, 0, 16256, 0, 16256, 0, 16256, 0, 16256, 0, 16256, 0, 16256, 0, 16256, 0, 16256, 0, 16256, 0, 16256, 0, 0, 0, 0, 0, 0, 0, 0, 0, 0, 0, 0, 0, 0, 0, 0, 0, 0, 0, 0, 0, 0, 0, 0, 0, 0, 0, 0, 0, 0, 0, 0, 0, 0, 0, 0, 0, 0, 0, 0, 0, 0, 0, 0, 0, 0, 0, 0, 0, 0, 0, 0, 0, 0, 0, 0, 0, 0, 0, 0, 0, 0, 0, 0, 0, 0, 0, 0, 0, 0, 0, 0, 0, 0, 0, 0, 0, 0, 0, 0, 0, 0, 0, 0, 0, 0, 0, 0, 0, 0, 0, 0, 0, 0, 0, 0, 0, 0, 0, 0, 0, 0, 0, 0, 0, 0, 0, 0, 0, 0, 0, 0, 0, 0, 0, 0, 0, 0, 0, 0, 0, 0, 0, 0, 0, 0, 0, 0, 0, 16768, 0, 16768, 0, 16768, 0, 16768, 0, 16768, 0, 16768, 0, 16768, 0, 16768, 0, 16256, 0, 16256, 0, 16128, 0, 16256, 0, 16256, 0, 16256, 0, 16128, 0, 16256, 0, 16768, 0, 16768, 0, 16768, 0, 16768, 0, 16768, 0, 16768, 0, 16768, 0, 16768, 0, 16256, 0, 16256, 0, 16256, 0, 16256, 0, 16256, 0, 16256, 0, 16256, 0, 16256, 0, 16128, 0, 16256, 0, 16256, 0, 16256, 0, 16128, 0, 16256, 0, 16256, 0, 16256, 0, 16256, 0, 16256, 0, 16256, 0, 16256, 0, 16256, 0, 16256, 0, 16256, 0, 16256, 0, 16256, 0, 16256, 0, 16256, 0, 16256, 0, 16256, 0, 16256, 0, 16256, 0, 16256, 0, 16256, 0, 16256, 0, 16256, 0, 16256, 0, 16256, 0, 16256, 0, 16256, 0, 16256, 0, 16256, 0, 16256, 0, 16256, 0, 16256, 0, 16256, 0, 16256, 0, 16256, 0, 16256, 0, 16128, 0, 16256, 0, 16256, 0, 16256, 0, 16128, 0, 16256, 0, 16256, 0, 16256, 0, 16256, 0, 16256, 0, 16256, 0, 16256, 0, 16256, 0, 16256, 0, 16256, 0, 16256, 0, 16256, 0, 16256, 0, 16256, 0, 16256, 0, 16256, 0, 16256, 0, 16256, 0, 16256, 0, 16256, 0, 16256, 0, 16256, 0, 16256, 0, 16256, 0, 16256, 0, 16256, 0, 16256, 0, 16256, 0, 16256, 0, 16128, 0, 16256, 0, 16256, 0, 16256, 0, 16128, 0, 16256, 0, 16256, 0, 16256, 0, 16256, 0, 16256, 0, 16256, 0, 16256, 0, 16256, 0, 16256, 0, 16256, 0, 16256, 0, 16256, 0, 16256, 0, 16256, 0, 16256, 0, 16256, 0, 16256, 0, 16256, 0, 16256, 0, 16256, 0, 16256, 0, 16256, 0, 16256, 0, 16256, 0, 16256, 0, 16256, 0, 16256, 0, 16128, 0, 16256, 0, 16256, 0, 16256, 0, 16128, 0, 16256, 0, 16256, 0, 16256, 0, 16256, 0, 16256, 0, 16256, 0, 16256, 0, 16256, 0, 16256, 0, 16256, 0, 16256, 0, 16256, 0, 16256, 0, 16256, 0, 16256, 0, 16256, 0, 16256, 0, 16256, 0, 16256, 0, 16256, 0, 16256, 0, 16256, 0, 16256, 0, 16256, 0, 16256, 0, 16128, 0, 16256, 0, 16256, 0, 16256, 0, 16128, 0, 16256, 0, 16256, 0, 16256, 0, 16256, 0, 16256, 0, 16256, 0, 16256, 0, 16256, 0, 16256, 0, 16256, 0, 16256, 0, 16256, 0, 16256, 0, 16256, 0, 16256, 0, 16256, 0, 16256, 0, 16256, 0, 16256, 0, 0, 0, 0, 0, 0, 0, 0, 0, 0, 0, 0, 0, 0, 0, 0, 0, 0, 0, 0, 0, 0, 0, 0, 0, 0, 0, 0, 0, 0, 0, 0, 0, 0, 0, 0, 0, 0, 0, 0, 0, 0, 0, 0, 0, 0, 0, 0, 0, 0, 0, 0, 0, 0, 0, 0, 0, 0, 0, 0, 0, 0, 0, 0, 0, 0, 0, 0, 0, 0, 0, 0, 0, 0, 0, 0, 0, 0, 0, 0, 0, 0, 0, 0, 0, 0, 0, 0, 0, 0, 0, 0, 0, 0, 0, 0, 0, 0, 0, 0, 0, 0, 0, 0, 0, 0, 0, 0, 0, 0, 0, 0, 0, 0, 0, 0, 0, 0, 0, 0, 0, 0, 0, 0, 0, 0, 0, 0, 0, 16768, 0, 16768, 0, 16768, 0, 16768, 0, 16768, 0, 0, 0, 0, 0, 0, 0, 16256, 0, 16256, 0, 16128, 0, 16256, 0, 16128, 0, 16256, 0, 16128, 0, 16256, 0, 16768, 0, 16768, 0, 16768, 0, 16768, 0, 16768, 0, 0, 0, 0, 0, 0, 0, 16256, 0, 16256, 0, 16256, 0, 16256, 0, 16128, 0, 17280, 0, 16256, 0, 16256, 0, 16128, 0, 16256, 0, 16256, 0, 16256, 0, 16128, 0, 16256, 0, 16256, 0, 16256, 0, 16256, 0, 16256, 0, 16256, 0, 16256, 0, 16256, 0, 16256, 0, 16256, 0, 16256, 0, 16256, 0, 16256, 0, 16256, 0, 16256, 0, 16256, 0, 16256, 0, 16256, 0, 16256, 0, 16256, 0, 16256, 0, 16256, 0, 16256, 0, 16256, 0, 16256, 0, 16256, 0, 16256, 0, 16256, 0, 16256, 0, 16256, 0, 16256, 0, 16256, 0, 16256, 0, 16256, 0, 16256, 0, 16128, 0, 16256, 0, 16256, 0, 16256, 0, 16128, 0, 16256, 0, 16256, 0, 16256, 0, 16256, 0, 16256, 0, 16256, 0, 16256, 0, 16256, 0, 16256, 0, 16256, 0, 16256, 0, 16256, 0, 16256, 0, 16256, 0, 16256, 0, 16256, 0, 16256, 0, 16256, 0, 16256, 0, 16256, 0, 16256, 0, 16256, 0, 16256, 0, 16256, 0, 16256, 0, 16256, 0, 16256, 0, 16256, 0, 16256, 0, 16128, 0, 16256, 0, 16256, 0, 16256, 0, 16128, 0, 16256, 0, 16256, 0, 16256, 0, 16256, 0, 16256, 0, 16256, 0, 16256, 0, 16256, 0, 16256, 0, 16256, 0, 16256, 0, 16256, 0, 16256, 0, 16256, 0, 16256, 0, 16256, 0, 16256, 0, 16256, 0, 16256, 0, 16256, 0, 16256, 0, 16256, 0, 16256, 0, 16256, 0, 16256, 0, 16256, 0, 16256, 0, 16128, 0, 16256, 0, 16256, 0, 16256, 0, 16128, 0, 16256, 0, 16256, 0, 16256, 0, 16256, 0, 16256, 0, 16256, 0, 16256, 0, 16256, 0, 16256, 0, 16256, 0, 16256, 0, 16256, 0, 16256, 0, 16256, 0, 16256, 0, 16256, 0, 16256, 0, 16256, 0, 16256, 0, 16256, 0, 16256, 0, 16256, 0, 16256, 0, 16256, 0, 16256, 0, 16128, 0, 16256, 0, 16256, 0, 16256, 0, 16128, 0, 16256, 0, 16256, 0, 16256, 0, 16256, 0, 16256, 0, 16256, 0, 16256, 0, 16256, 0, 16256, 0, 16256, 0, 16256, 0, 16256, 0, 16256, 0, 16256, 0, 16256, 0, 16256, 0, 16256, 0, 16256, 0, 16256, 0, 0, 0, 0, 0, 0, 0, 0, 0, 0, 0, 0, 0, 0, 0, 0, 0, 0, 0, 0, 0, 0, 0, 0, 0, 0, 0, 0, 0, 0, 0, 0, 0, 0, 0, 0, 0, 0, 0, 0, 0, 0, 0, 0, 0, 0, 0, 0, 0, 0, 0, 0, 0, 0, 0, 0, 0, 0, 0, 0, 0, 0, 0, 0, 0, 0, 0, 0, 0, 0, 0, 0, 0, 0, 0, 0, 0, 0, 0, 0, 0, 0, 0, 0, 0, 0, 0, 0, 0, 0, 0, 0, 0, 0, 0, 0, 0, 0, 0, 0, 0, 0, 0, 0, 0, 0, 0, 0, 0, 0, 0, 0, 0, 0, 0, 0, 0, 0, 0, 0, 0, 0, 0, 0, 0, 0, 0, 0};

__global__ __launch_bounds__(512) void xd_kernel(const int* idx, const u16* w1, const u16* table, const u16* b1, const u16* ws_cin, const u16* wout, char* ws, const u16* w2, const u16* b_out, u16* out) {
  __shared__ __attribute__((aligned(1024))) char shm[163840];
  unsigned tid = threadIdx.x;
  asm volatile(
    "s_load_dwordx16 s[4:19], s[0:1], 0\n"
    ".Lxd_top:\n"
    "v_readfirstlane_b32 s43, v0\n"
    "s_setprio 3\n"
    "s_cmp_lt_u32 s2, 8\n"
    "s_cbranch_scc1 .Lxd_s2\n"
    "s_cmp_lt_u32 s2, 48\n"
    "s_cbranch_scc1 .Lxd_help\n"
    "s_and_b32 s24, s2, 15\n"
    "s_mul_i32 s40, s43, 36\n"
    "s_mul_i32 s41, s24, 26624\n"
    "s_add_u32 s38, s40, s41\n"
    "v_lshlrev_b32 v86, 4, v0\n"
    "s_lshl_b32 s34, s43, 6\n"
    "s_add_u32 m0, s34, 98304\n"
    "s_waitcnt lgkmcnt(0)\n"
    "s_add_u32 s38, s4, s38\n"
    "s_addc_u32 s39, s5, 0\n"
    "global_load_lds_dwordx4 v86, s[38:39] offset:0\n"
    "global_load_lds_dwordx4 v86, s[38:39] offset:1024\n"
    "global_load_lds_dwordx4 v86, s[38:39] offset:2048\n"
    "global_load_lds_dwordx4 v86, s[38:39] offset:2304\n"
    "s_sub_u32 s3, s2, 48\n"
    "s_and_b32 s28, s2, 7\n"
    "s_lshr_b32 s23, s3, 4\n"
    "s_mul_i32 s42, s28, 3\n"
    "v_bfe_u32 v85, v0, 4, 2\n"
    "v_lshlrev_b32 v87, 1, v0\n"
    "v_and_b32 v87, 0xe0, v87\n"
    "v_and_b32 v88, 0xf0, v86\n"
    "v_xor_b32 v87, v87, v88\n"
    "s_movk_i32 s41, 3328\n"
    "v_mad_u32_u24 v46, v85, s41, v87\n"
    "v_add_u32 v47, 12288, v46\n"
    "s_mul_i32 s41, s43, 416\n"
    "s_lshl_b32 s40, s23, 8\n"
    "s_add_u32 s41, s41, s40\n"
    "s_mul_i32 s30, s42, 212992\n"
    "s_lshl_b32 s35, s43, 5\n"
    "s_bitset1_b32 s35, 15\n"
    "s_add_u32 s6, s6, s41\n"
    "s_addc_u32 s7, s7, 0\n"
    "s_add_u32 s30, s6, s30\n"
    "s_addc_u32 s31, s7, 0\n"
    "s_add_u32 m0, s35, 0\n"
    "s_nop 0\n"
    "global_load_lds_dwordx4 v46, s[30:31] offset:0\n"
    "global_load_lds_dwordx4 v47, s[30:31] offset:1024\n"
    "s_add_u32 s30, s30, 212992\n"
    "s_addc_u32 s31, s31, 0\n"
    "s_setprio 2\n"
    "v_and_b32 v84, 3, v0\n"
    "v_bfe_u32 v44, v0, 3, 3\n"
    "v_lshl_add_u32 v44, v84, 3, v44\n"
    "s_add_u32 s40, s34, 98304\n"
    "v_mul_u32_u24 v44, 104, v44\n"
    "v_add_u32 v44, s40, v44\n"
    "v_lshl_add_u32 v45, s42, 2, v44\n"
    "v_xor_b32 v51, v0, v86\n"
    "v_and_b32 v86, 0x70, v86\n"
    "v_bfi_b32 v86, 48, v51, v86\n"
    "v_xor_b32 v51, 64, v86\n"
    "v_add_u32 v48, 3072, v86\n"
    "v_add_u32 v49, 2048, v51\n"
    "v_add_u32 v50, 1024, v86\n"
    "s_sub_u32 s36, s8, 3072\n"
    "s_subb_u32 s37, s9, 0\n"
    "s_add_u32 m0, s35, 49152\n"
    "s_nop 0\n"
    "global_load_lds_dwordx4 v46, s[30:31] offset:0\n"
    "global_load_lds_dwordx4 v47, s[30:31] offset:1024\n"
    "s_add_u32 s30, s30, 212992\n"
    "s_addc_u32 s31, s31, 0\n"
    "s_add_u32 m0, s35, 98304\n"
    "s_nop 0\n"
    "global_load_lds_dwordx4 v46, s[30:31] offset:0\n"
    "global_load_lds_dwordx4 v47, s[30:31] offset:1024\n"
    "s_add_u32 s66, s28, 1\n"
    "s_and_b32 s66, s66, 7\n"
    "s_mul_i32 s66, s66, 638976\n"
    "s_add_u32 s30, s6, s66\n"
    "s_addc_u32 s31, s7, 0\n"
    "s_waitcnt vmcnt(6)\n"
    "ds_read2_b32 v[96:97], v45 offset0:0 offset1:1\n"
    "ds_read_b32 v98, v45 offset:8\n"
    "s_lshr_b32 s20, s43, 6\n"
    "s_lshr_b32 s21, s43, 8\n"
    "v_and_b32 v1, 63, v0\n"
    "s_waitcnt lgkmcnt(0)\n"
    "v_lshlrev_b32 v96, 7, v96\n"
    "v_lshlrev_b32 v97, 7, v97\n"
    "v_lshlrev_b32 v98, 7, v98\n"
    "v_add_u32_dpp v52, v96, v48 quad_perm:[0,0,0,0] row_mask:0xf bank_mask:0xf\n"
    "v_add_u32_dpp v53, v96, v49 quad_perm:[1,1,1,1] row_mask:0xf bank_mask:0xf\n"
    "v_add_u32_dpp v54, v96, v50 quad_perm:[2,2,2,2] row_mask:0xf bank_mask:0xf\n"
    "v_add_u32_dpp v55, v96, v51 quad_perm:[3,3,3,3] row_mask:0xf bank_mask:0xf\n"
    "s_add_u32 m0, s34, 0\n"
    "s_nop 0\n"
    "global_load_lds_dwordx4 v52, s[36:37] offset:0\n"
    "global_load_lds_dwordx4 v53, s[36:37] offset:1024\n"
    "global_load_lds_dwordx4 v54, s[36:37] offset:2048\n"
    "global_load_lds_dwordx4 v55, s[36:37] offset:3072\n"
    "s_setprio 1\n"
    "v_add_u32_dpp v52, v97, v48 quad_perm:[0,0,0,0] row_mask:0xf bank_mask:0xf\n"
    "v_add_u32_dpp v53, v97, v49 quad_perm:[1,1,1,1] row_mask:0xf bank_mask:0xf\n"
    "v_add_u32_dpp v54, v97, v50 quad_perm:[2,2,2,2] row_mask:0xf bank_mask:0xf\n"
    "v_add_u32_dpp v55, v97, v51 quad_perm:[3,3,3,3] row_mask:0xf bank_mask:0xf\n"
    "s_load_dwordx4 s[68:71], s[0:1], 64\n"
    "s_add_u32 s40, s28, 1\n"
    "s_and_b32 s40, s40, 7\n"
    "v_mad_u32_u24 v45, s40, 12, v44\n"
    "ds_read2_b32 v[100:101], v45 offset0:1 offset1:2\n"
    "ds_read_b32 v99, v45 offset:0\n"
    "s_add_u32 s40, s28, 2\n"
    "s_and_b32 s40, s40, 7\n"
    "v_mad_u32_u24 v45, s40, 12, v44\n"
    "ds_read2_b32 v[102:103], v45 offset0:0 offset1:1\n"
    "ds_read_b32 v104, v45 offset:8\n"
    "s_add_u32 s40, s28, 3\n"
    "s_and_b32 s40, s40, 7\n"
    "v_mad_u32_u24 v45, s40, 12, v44\n"
    "ds_read2_b32 v[106:107], v45 offset0:1 offset1:2\n"
    "ds_read_b32 v105, v45 offset:0\n"
    "s_add_u32 s40, s28, 4\n"
    "s_and_b32 s40, s40, 7\n"
    "v_mad_u32_u24 v45, s40, 12, v44\n"
    "ds_read2_b32 v[108:109], v45 offset0:0 offset1:1\n"
    "ds_read_b32 v110, v45 offset:8\n"
    "s_add_u32 s40, s28, 5\n"
    "s_and_b32 s40, s40, 7\n"
    "v_mad_u32_u24 v45, s40, 12, v44\n"
    "ds_read2_b32 v[112:113], v45 offset0:1 offset1:2\n"
    "ds_read_b32 v111, v45 offset:0\n"
    "s_add_u32 s40, s28, 6\n"
    "s_and_b32 s40, s40, 7\n"
    "v_mad_u32_u24 v45, s40, 12, v44\n"
    "ds_read2_b32 v[114:115], v45 offset0:0 offset1:1\n"
    "ds_read_b32 v116, v45 offset:8\n"
    "s_add_u32 s40, s28, 7\n"
    "s_and_b32 s40, s40, 7\n"
    "v_mad_u32_u24 v45, s40, 12, v44\n"
    "ds_read2_b32 v[118:119], v45 offset0:1 offset1:2\n"
    "ds_read_b32 v117, v45 offset:0\n"
    "ds_read_b64 v[120:121], v44 offset:96\n"
    "v_lshrrev_b32 v86, 5, v88\n"
    "v_xor_b32 v86, v86, v85\n"
    "v_lshlrev_b32 v86, 4, v86\n"
    "v_lshl_add_u32 v86, v88, 3, v86\n"
    "s_and_b32 s41, s20, 1\n"
    "s_lshl1_add_u32 s40, s21, s41\n"
    "v_lshl_add_u32 v56, s40, 13, v86\n"
    "v_xor_b32 v57, 64, v56\n"
    "v_lshrrev_b32 v86, 6, v88\n"
    "v_lshl_add_u32 v88, v85, 3, v86\n"
    "v_bfe_u32 v86, v0, 2, 3\n"
    "s_and_b32 s40, s20, 2\n"
    "s_lshl1_add_u32 s40, s40, 1024\n"
    "v_xor_b32 v86, s40, v86\n"
    "v_lshlrev_b32 v86, 5, v86\n"
    "v_lshl_add_u32 v86, v88, 8, v86\n"
    "v_lshl_add_u32 v76, v84, 3, v86\n"
    "v_xor_b32 v77, 32, v76\n"
    "v_xor_b32 v78, 64, v76\n"
    "v_xor_b32 v79, 96, v76\n"
    "v_lshlrev_b32 v92, 2, v0\n"
    "v_add_u32_e64 v93, s21, -1\n"
    "v_add_u32 v92, 149504, v92\n"
    "ds_write_b32 v92, v93\n"
    "s_waitcnt lgkmcnt(0)\n"
    "v_lshlrev_b32 v99, 7, v99\n"
    "s_setprio 0\n"
    "s_cmp_eq_u32 s21, 0\n"
    "s_cbranch_scc1 .Lxd_g0\n"
    "s_waitcnt vmcnt(8)\n"
    "s_barrier\n"
    ".Lxd_g0:\n"
    "s_add_u32 m0, s34, 49152\n"
    "s_nop 0\n"
    "global_load_lds_dwordx4 v52, s[36:37] offset:0\n"
    "global_load_lds_dwordx4 v53, s[36:37] offset:1024\n"
    "global_load_lds_dwordx4 v54, s[36:37] offset:2048\n"
    "global_load_lds_dwordx4 v55, s[36:37] offset:3072\n"
    "v_add_u32_dpp v52, v98, v48 quad_perm:[0,0,0,0] row_mask:0xf bank_mask:0xf\n"
    "v_add_u32_dpp v53, v98, v49 quad_perm:[1,1,1,1] row_mask:0xf bank_mask:0xf\n"
    "v_add_u32_dpp v54, v98, v50 quad_perm:[2,2,2,2] row_mask:0xf bank_mask:0xf\n"
    "v_add_u32_dpp v55, v98, v51 quad_perm:[3,3,3,3] row_mask:0xf bank_mask:0xf\n"
    "s_waitcnt vmcnt(4) lgkmcnt(0)\n"
    "s_barrier\n"
    "ds_read_b128 v[128:131], v56 offset:0\n"
    "ds_read_b128 v[132:135], v56 offset:2048\n"
    "ds_read_b128 v[136:139], v56 offset:4096\n"
    "ds_read_b128 v[140:143], v56 offset:6144\n"
    "ds_read_b64_tr_b16 v[144:145], v76 offset:0\n"
    "ds_read_b64_tr_b16 v[146:147], v76 offset:1024\n"
    "ds_read_b64_tr_b16 v[148:149], v77 offset:0\n"
    "ds_read_b64_tr_b16 v[150:151], v77 offset:1024\n"
    "ds_read_b64_tr_b16 v[152:153], v78 offset:0\n"
    "ds_read_b64_tr_b16 v[154:155], v78 offset:1024\n"
    "ds_read_b64_tr_b16 v[156:157], v79 offset:0\n"
    "ds_read_b64_tr_b16 v[158:159], v79 offset:1024\n"
    "ds_read_b128 v[160:163], v57 offset:0\n"
    "ds_read_b128 v[164:167], v57 offset:2048\n"
    "ds_read_b128 v[168:171], v57 offset:4096\n"
    "ds_read_b128 v[172:175], v57 offset:6144\n"
    "ds_read_b64_tr_b16 v[176:177], v76 offset:8192\n"
    "ds_read_b64_tr_b16 v[178:179], v76 offset:9216\n"
    "ds_read_b64_tr_b16 v[180:181], v77 offset:8192\n"
    "ds_read_b64_tr_b16 v[182:183], v77 offset:9216\n"
    "ds_read_b64_tr_b16 v[184:185], v78 offset:8192\n"
    "ds_read_b64_tr_b16 v[186:187], v78 offset:9216\n"
    "ds_read_b64_tr_b16 v[188:189], v79 offset:8192\n"
    "ds_read_b64_tr_b16 v[190:191], v79 offset:9216\n"
    "s_add_u32 m0, s34, 98304\n"
    "s_nop 0\n"
    "global_load_lds_dwordx4 v52, s[36:37] offset:0\n"
    "global_load_lds_dwordx4 v53, s[36:37] offset:1024\n"
    "global_load_lds_dwordx4 v54, s[36:37] offset:2048\n"
    "global_load_lds_dwordx4 v55, s[36:37] offset:3072\n"
    "s_waitcnt lgkmcnt(0)\n"
    "s_barrier\n"
    "v_mfma_f32_16x16x32_bf16 v[192:195], v[144:147], v[128:131], 0\n"
    "v_lshlrev_b32 v100, 7, v100\n"
    "v_mfma_f32_16x16x32_bf16 v[192:195], v[176:179], v[160:163], v[192:195]\n"
    "v_add_u32_dpp v52, v99, v48 quad_perm:[0,0,0,0] row_mask:0xf bank_mask:0xf\n"
    "v_mfma_f32_16x16x32_bf16 v[196:199], v[176:179], v[164:167], 0\n"
    "v_mfma_f32_16x16x32_bf16 v[196:199], v[144:147], v[132:135], v[196:199]\n"
    "v_add_u32_dpp v53, v99, v49 quad_perm:[1,1,1,1] row_mask:0xf bank_mask:0xf\n"
    "v_mfma_f32_16x16x32_bf16 v[200:203], v[144:147], v[136:139], 0\n"
    "v_mfma_f32_16x16x32_bf16 v[200:203], v[176:179], v[168:171], v[200:203]\n"
    "v_add_u32_dpp v54, v99, v50 quad_perm:[2,2,2,2] row_mask:0xf bank_mask:0xf\n"
    "v_mfma_f32_16x16x32_bf16 v[204:207], v[176:179], v[172:175], 0\n"
    "v_mfma_f32_16x16x32_bf16 v[204:207], v[144:147], v[140:143], v[204:207]\n"
    "v_add_u32_dpp v55, v99, v51 quad_perm:[3,3,3,3] row_mask:0xf bank_mask:0xf\n"
    "v_mfma_f32_16x16x32_bf16 v[220:223], v[148:151], v[140:143], 0\n"
    "v_mfma_f32_16x16x32_bf16 v[220:223], v[180:183], v[172:175], v[220:223]\n"
    "s_bfe_u32 s40, s34, 0x1000d\n"
    "v_mfma_f32_16x16x32_bf16 v[216:219], v[180:183], v[168:171], 0\n"
    "v_mfma_f32_16x16x32_bf16 v[216:219], v[148:151], v[136:139], v[216:219]\n"
    "s_lshl1_add_u32 s40, s23, s40\n"
    "v_mfma_f32_16x16x32_bf16 v[212:215], v[148:151], v[132:135], 0\n"
    "v_mfma_f32_16x16x32_bf16 v[212:215], v[180:183], v[164:167], v[212:215]\n"
    "s_lshl_b32 s40, s40, 7\n"
    "v_mfma_f32_16x16x32_bf16 v[208:211], v[180:183], v[160:163], 0\n"
    "v_mfma_f32_16x16x32_bf16 v[208:211], v[148:151], v[128:131], v[208:211]\n"
    "v_lshl_add_u32 v42, v85, 3, s40\n"
    "v_mfma_f32_16x16x32_bf16 v[224:227], v[152:155], v[128:131], 0\n"
    "v_mfma_f32_16x16x32_bf16 v[224:227], v[184:187], v[160:163], v[224:227]\n"
    "s_lshl_b32 s24, s24, 8\n"
    "v_mfma_f32_16x16x32_bf16 v[228:231], v[184:187], v[164:167], 0\n"
    "v_mfma_f32_16x16x32_bf16 v[228:231], v[152:155], v[132:135], v[228:231]\n"
    "v_add_u32 v58, 98304, v56\n"
    "v_mfma_f32_16x16x32_bf16 v[232:235], v[152:155], v[136:139], 0\n"
    "v_mfma_f32_16x16x32_bf16 v[232:235], v[184:187], v[168:171], v[232:235]\n"
    "v_add_u32 v59, 98304, v57\n"
    "v_mfma_f32_16x16x32_bf16 v[236:239], v[184:187], v[172:175], 0\n"
    "v_mfma_f32_16x16x32_bf16 v[236:239], v[152:155], v[140:143], v[236:239]\n"
    "v_add_u32 v80, 98304, v76\n"
    "v_mfma_f32_16x16x32_bf16 v[252:255], v[156:159], v[140:143], 0\n"
    "v_mfma_f32_16x16x32_bf16 v[252:255], v[188:191], v[172:175], v[252:255]\n"
    "v_add_u32 v81, 98304, v77\n"
    "v_mfma_f32_16x16x32_bf16 v[248:251], v[188:191], v[168:171], 0\n"
    "v_mfma_f32_16x16x32_bf16 v[248:251], v[156:159], v[136:139], v[248:251]\n"
    "v_add_u32 v82, 98304, v78\n"
    "v_mfma_f32_16x16x32_bf16 v[244:247], v[156:159], v[132:135], 0\n"
    "v_mfma_f32_16x16x32_bf16 v[244:247], v[188:191], v[164:167], v[244:247]\n"
    "v_add_u32 v83, 98304, v79\n"
    "v_mfma_f32_16x16x32_bf16 v[240:243], v[188:191], v[160:163], 0\n"
    "v_mfma_f32_16x16x32_bf16 v[240:243], v[156:159], v[128:131], v[240:243]\n"
    "s_waitcnt vmcnt(4)\n"
    "s_barrier\n"
    "s_add_u32 m0, s35, 0\n"
    "s_nop 0\n"
    "global_load_lds_dwordx4 v46, s[30:31] offset:0\n"
    "ds_read_b128 v[128:131], v56 offset:49152\n"
    "ds_read_b128 v[132:135], v56 offset:51200\n"
    "ds_read_b128 v[136:139], v56 offset:53248\n"
    "ds_read_b128 v[140:143], v56 offset:55296\n"
    "global_load_lds_dwordx4 v47, s[30:31] offset:1024\n"
    "s_add_u32 s30, s30, 212992\n"
    "s_addc_u32 s31, s31, 0\n"
    "ds_read_b64_tr_b16 v[144:145], v76 offset:49152\n"
    "ds_read_b64_tr_b16 v[146:147], v76 offset:50176\n"
    "ds_read_b64_tr_b16 v[148:149], v77 offset:49152\n"
    "ds_read_b64_tr_b16 v[150:151], v77 offset:50176\n"
    "s_add_u32 m0, s34, 0\n"
    "s_nop 0\n"
    "global_load_lds_dwordx4 v52, s[36:37] offset:0\n"
    "ds_read_b64_tr_b16 v[152:153], v78 offset:49152\n"
    "ds_read_b64_tr_b16 v[154:155], v78 offset:50176\n"
    "ds_read_b64_tr_b16 v[156:157], v79 offset:49152\n"
    "ds_read_b64_tr_b16 v[158:159], v79 offset:50176\n"
    "global_load_lds_dwordx4 v53, s[36:37] offset:1024\n"
    "ds_read_b128 v[160:163], v57 offset:49152\n"
    "ds_read_b128 v[164:167], v57 offset:51200\n"
    "ds_read_b128 v[168:171], v57 offset:53248\n"
    "ds_read_b128 v[172:175], v57 offset:55296\n"
    "global_load_lds_dwordx4 v54, s[36:37] offset:2048\n"
    "ds_read_b64_tr_b16 v[176:177], v76 offset:57344\n"
    "ds_read_b64_tr_b16 v[178:179], v76 offset:58368\n"
    "ds_read_b64_tr_b16 v[180:181], v77 offset:57344\n"
    "ds_read_b64_tr_b16 v[182:183], v77 offset:58368\n"
    "global_load_lds_dwordx4 v55, s[36:37] offset:3072\n"
    "ds_read_b64_tr_b16 v[184:185], v78 offset:57344\n"
    "ds_read_b64_tr_b16 v[186:187], v78 offset:58368\n"
    "ds_read_b64_tr_b16 v[188:189], v79 offset:57344\n"
    "ds_read_b64_tr_b16 v[190:191], v79 offset:58368\n"
    "s_waitcnt lgkmcnt(0)\n"
    "s_barrier\n"
    "v_mfma_f32_16x16x32_bf16 v[192:195], v[144:147], v[128:131], v[192:195]\n"
    "v_lshlrev_b32 v101, 7, v101\n"
    "v_mfma_f32_16x16x32_bf16 v[192:195], v[176:179], v[160:163], v[192:195]\n"
    "v_mfma_f32_16x16x32_bf16 v[196:199], v[176:179], v[164:167], v[196:199]\n"
    "v_add_u32_dpp v52, v100, v48 quad_perm:[0,0,0,0] row_mask:0xf bank_mask:0xf\n"
    "v_mfma_f32_16x16x32_bf16 v[196:199], v[144:147], v[132:135], v[196:199]\n"
    "v_mfma_f32_16x16x32_bf16 v[200:203], v[144:147], v[136:139], v[200:203]\n"
    "v_add_u32_dpp v53, v100, v49 quad_perm:[1,1,1,1] row_mask:0xf bank_mask:0xf\n"
    "v_mfma_f32_16x16x32_bf16 v[200:203], v[176:179], v[168:171], v[200:203]\n"
    "v_mfma_f32_16x16x32_bf16 v[204:207], v[176:179], v[172:175], v[204:207]\n"
    "v_add_u32_dpp v54, v100, v50 quad_perm:[2,2,2,2] row_mask:0xf bank_mask:0xf\n"
    "v_mfma_f32_16x16x32_bf16 v[204:207], v[144:147], v[140:143], v[204:207]\n"
    "v_mfma_f32_16x16x32_bf16 v[220:223], v[148:151], v[140:143], v[220:223]\n"
    "v_add_u32_dpp v55, v100, v51 quad_perm:[3,3,3,3] row_mask:0xf bank_mask:0xf\n"
    "v_mfma_f32_16x16x32_bf16 v[220:223], v[180:183], v[172:175], v[220:223]\n"
    "v_mfma_f32_16x16x32_bf16 v[216:219], v[180:183], v[168:171], v[216:219]\n"
    "s_mul_i32 s47, s3, 5042\n"
    "v_mfma_f32_16x16x32_bf16 v[216:219], v[148:151], v[136:139], v[216:219]\n"
    "v_mfma_f32_16x16x32_bf16 v[212:215], v[148:151], v[132:135], v[212:215]\n"
    "s_lshr_b32 s47, s47, 16\n"
    "v_mfma_f32_16x16x32_bf16 v[212:215], v[180:183], v[164:167], v[212:215]\n"
    "v_mfma_f32_16x16x32_bf16 v[208:211], v[180:183], v[160:163], v[208:211]\n"
    "s_lshr_b32 s21, s20, 2\n"
    "v_mfma_f32_16x16x32_bf16 v[208:211], v[148:151], v[128:131], v[208:211]\n"
    "v_mfma_f32_16x16x32_bf16 v[224:227], v[152:155], v[128:131], v[224:227]\n"
    "s_lshl1_add_u32 s22, s21, s20\n"
    "v_mfma_f32_16x16x32_bf16 v[224:227], v[184:187], v[160:163], v[224:227]\n"
    "v_mfma_f32_16x16x32_bf16 v[228:231], v[184:187], v[164:167], v[228:231]\n"
    "s_and_b32 s22, s22, 3\n"
    "v_mfma_f32_16x16x32_bf16 v[228:231], v[152:155], v[132:135], v[228:231]\n"
    "v_mfma_f32_16x16x32_bf16 v[232:235], v[152:155], v[136:139], v[232:235]\n"
    "s_lshl_b32 s58, s21, 8\n"
    "v_mfma_f32_16x16x32_bf16 v[232:235], v[184:187], v[168:171], v[232:235]\n"
    "v_mfma_f32_16x16x32_bf16 v[236:239], v[184:187], v[172:175], v[236:239]\n"
    "s_add_u32 s58, s58, 150528\n"
    "v_mfma_f32_16x16x32_bf16 v[236:239], v[152:155], v[140:143], v[236:239]\n"
    "v_mfma_f32_16x16x32_bf16 v[252:255], v[156:159], v[140:143], v[252:255]\n"
    "s_mov_b32 s40, s47\n"
    "v_mfma_f32_16x16x32_bf16 v[252:255], v[188:191], v[172:175], v[252:255]\n"
    "v_mfma_f32_16x16x32_bf16 v[248:251], v[188:191], v[168:171], v[248:251]\n"
    "s_mul_i32 s54, s40, 512\n"
    "v_mfma_f32_16x16x32_bf16 v[248:251], v[156:159], v[136:139], v[248:251]\n"
    "v_mfma_f32_16x16x32_bf16 v[244:247], v[156:159], v[132:135], v[244:247]\n"
    "s_add_u32 s54, s54, 2\n"
    "v_mfma_f32_16x16x32_bf16 v[244:247], v[188:191], v[164:167], v[244:247]\n"
    "v_mfma_f32_16x16x32_bf16 v[240:243], v[188:191], v[160:163], v[240:243]\n"
    "v_mfma_f32_16x16x32_bf16 v[240:243], v[156:159], v[128:131], v[240:243]\n"
    "s_waitcnt vmcnt(6)\n"
    "s_barrier\n"
    "s_add_u32 m0, s35, 49152\n"
    "s_nop 0\n"
    "global_load_lds_dwordx4 v46, s[30:31] offset:0\n"
    "ds_read_b128 v[128:131], v58 offset:0\n"
    "ds_read_b128 v[132:135], v58 offset:2048\n"
    "ds_read_b128 v[136:139], v58 offset:4096\n"
    "ds_read_b128 v[140:143], v58 offset:6144\n"
    "global_load_lds_dwordx4 v47, s[30:31] offset:1024\n"
    "s_add_u32 s30, s30, 212992\n"
    "s_addc_u32 s31, s31, 0\n"
    "ds_read_b64_tr_b16 v[144:145], v80 offset:0\n"
    "ds_read_b64_tr_b16 v[146:147], v80 offset:1024\n"
    "ds_read_b64_tr_b16 v[148:149], v81 offset:0\n"
    "ds_read_b64_tr_b16 v[150:151], v81 offset:1024\n"
    "s_add_u32 m0, s34, 49152\n"
    "s_nop 0\n"
    "global_load_lds_dwordx4 v52, s[36:37] offset:0\n"
    "ds_read_b64_tr_b16 v[152:153], v82 offset:0\n"
    "ds_read_b64_tr_b16 v[154:155], v82 offset:1024\n"
    "ds_read_b64_tr_b16 v[156:157], v83 offset:0\n"
    "ds_read_b64_tr_b16 v[158:159], v83 offset:1024\n"
    "global_load_lds_dwordx4 v53, s[36:37] offset:1024\n"
    "ds_read_b128 v[160:163], v59 offset:0\n"
    "ds_read_b128 v[164:167], v59 offset:2048\n"
    "ds_read_b128 v[168:171], v59 offset:4096\n"
    "ds_read_b128 v[172:175], v59 offset:6144\n"
    "global_load_lds_dwordx4 v54, s[36:37] offset:2048\n"
    "ds_read_b64_tr_b16 v[176:177], v80 offset:8192\n"
    "ds_read_b64_tr_b16 v[178:179], v80 offset:9216\n"
    "ds_read_b64_tr_b16 v[180:181], v81 offset:8192\n"
    "ds_read_b64_tr_b16 v[182:183], v81 offset:9216\n"
    "global_load_lds_dwordx4 v55, s[36:37] offset:3072\n"
    "ds_read_b64_tr_b16 v[184:185], v82 offset:8192\n"
    "ds_read_b64_tr_b16 v[186:187], v82 offset:9216\n"
    "ds_read_b64_tr_b16 v[188:189], v83 offset:8192\n"
    "ds_read_b64_tr_b16 v[190:191], v83 offset:9216\n"
    "s_waitcnt vmcnt(10) lgkmcnt(0)\n"
    "s_barrier\n"
    "v_mfma_f32_16x16x32_bf16 v[192:195], v[144:147], v[128:131], v[192:195]\n"
    "v_lshlrev_b32 v102, 7, v102\n"
    "v_mfma_f32_16x16x32_bf16 v[192:195], v[176:179], v[160:163], v[192:195]\n"
    "v_mfma_f32_16x16x32_bf16 v[196:199], v[176:179], v[164:167], v[196:199]\n"
    "v_add_u32_dpp v52, v101, v48 quad_perm:[0,0,0,0] row_mask:0xf bank_mask:0xf\n"
    "v_mfma_f32_16x16x32_bf16 v[196:199], v[144:147], v[132:135], v[196:199]\n"
    "v_mfma_f32_16x16x32_bf16 v[200:203], v[144:147], v[136:139], v[200:203]\n"
    "v_add_u32_dpp v53, v101, v49 quad_perm:[1,1,1,1] row_mask:0xf bank_mask:0xf\n"
    "v_mfma_f32_16x16x32_bf16 v[200:203], v[176:179], v[168:171], v[200:203]\n"
    "v_mfma_f32_16x16x32_bf16 v[204:207], v[176:179], v[172:175], v[204:207]\n"
    "v_add_u32_dpp v54, v101, v50 quad_perm:[2,2,2,2] row_mask:0xf bank_mask:0xf\n"
    "v_mfma_f32_16x16x32_bf16 v[204:207], v[144:147], v[140:143], v[204:207]\n"
    "v_mfma_f32_16x16x32_bf16 v[220:223], v[148:151], v[140:143], v[220:223]\n"
    "v_add_u32_dpp v55, v101, v51 quad_perm:[3,3,3,3] row_mask:0xf bank_mask:0xf\n"
    "v_mfma_f32_16x16x32_bf16 v[220:223], v[180:183], v[172:175], v[220:223]\n"
    "v_mfma_f32_16x16x32_bf16 v[216:219], v[180:183], v[168:171], v[216:219]\n"
    "s_lshl_b32 s40, s40, 7\n"
    "v_mfma_f32_16x16x32_bf16 v[216:219], v[148:151], v[136:139], v[216:219]\n"
    "v_mfma_f32_16x16x32_bf16 v[212:215], v[148:151], v[132:135], v[212:215]\n"
    "s_add_u32 s40, s40, 32768\n"
    "v_mfma_f32_16x16x32_bf16 v[212:215], v[180:183], v[164:167], v[212:215]\n"
    "v_mfma_f32_16x16x32_bf16 v[208:211], v[180:183], v[160:163], v[208:211]\n"
    "s_add_u32 s52, s16, s40\n"
    "s_addc_u32 s53, s17, 0\n"
    "v_mfma_f32_16x16x32_bf16 v[208:211], v[148:151], v[128:131], v[208:211]\n"
    "v_mfma_f32_16x16x32_bf16 v[224:227], v[152:155], v[128:131], v[224:227]\n"
    "s_mov_b32 s57, 0x7fff7fff\n"
    "v_mfma_f32_16x16x32_bf16 v[224:227], v[184:187], v[160:163], v[224:227]\n"
    "v_mfma_f32_16x16x32_bf16 v[228:231], v[184:187], v[164:167], v[228:231]\n"
    "s_mov_b32 s56, 0\n"
    "v_mfma_f32_16x16x32_bf16 v[228:231], v[152:155], v[132:135], v[228:231]\n"
    "v_mfma_f32_16x16x32_bf16 v[232:235], v[152:155], v[136:139], v[232:235]\n"
    "s_mov_b32 s27, 0\n"
    "v_mfma_f32_16x16x32_bf16 v[232:235], v[184:187], v[168:171], v[232:235]\n"
    "v_mfma_f32_16x16x32_bf16 v[236:239], v[184:187], v[172:175], v[236:239]\n"
    "s_mov_b32 s55, 9\n"
    "v_mfma_f32_16x16x32_bf16 v[236:239], v[152:155], v[140:143], v[236:239]\n"
    "v_mfma_f32_16x16x32_bf16 v[252:255], v[156:159], v[140:143], v[252:255]\n"
    "s_mov_b32 s44, 0\n"
    "v_mfma_f32_16x16x32_bf16 v[252:255], v[188:191], v[172:175], v[252:255]\n"
    "v_mfma_f32_16x16x32_bf16 v[248:251], v[188:191], v[168:171], v[248:251]\n"
    "s_mov_b32 s46, 0\n"
    "v_mfma_f32_16x16x32_bf16 v[248:251], v[156:159], v[136:139], v[248:251]\n"
    "v_mfma_f32_16x16x32_bf16 v[244:247], v[156:159], v[132:135], v[244:247]\n"
    "v_lshrrev_b32 v17, 3, v1\n"
    "v_mfma_f32_16x16x32_bf16 v[244:247], v[188:191], v[164:167], v[244:247]\n"
    "v_mfma_f32_16x16x32_bf16 v[240:243], v[188:191], v[160:163], v[240:243]\n"
    "v_mfma_f32_16x16x32_bf16 v[240:243], v[156:159], v[128:131], v[240:243]\n"
    "s_waitcnt vmcnt(6)\n"
    "s_barrier\n"
    "s_add_u32 m0, s35, 98304\n"
    "s_nop 0\n"
    "global_load_lds_dwordx4 v46, s[30:31] offset:0\n"
    "ds_read_b128 v[128:131], v56 offset:0\n"
    "ds_read_b128 v[132:135], v56 offset:2048\n"
    "ds_read_b128 v[136:139], v56 offset:4096\n"
    "ds_read_b128 v[140:143], v56 offset:6144\n"
    "global_load_lds_dwordx4 v47, s[30:31] offset:1024\n"
    "s_add_u32 s66, s28, 2\n"
    "s_and_b32 s66, s66, 7\n"
    "s_mul_i32 s66, s66, 638976\n"
    "s_add_u32 s30, s6, s66\n"
    "s_addc_u32 s31, s7, 0\n"
    "ds_read_b64_tr_b16 v[144:145], v76 offset:0\n"
    "ds_read_b64_tr_b16 v[146:147], v76 offset:1024\n"
    "ds_read_b64_tr_b16 v[148:149], v77 offset:0\n"
    "ds_read_b64_tr_b16 v[150:151], v77 offset:1024\n"
    "s_add_u32 m0, s34, 98304\n"
    "s_nop 0\n"
    "global_load_lds_dwordx4 v52, s[36:37] offset:0\n"
    "ds_read_b64_tr_b16 v[152:153], v78 offset:0\n"
    "ds_read_b64_tr_b16 v[154:155], v78 offset:1024\n"
    "ds_read_b64_tr_b16 v[156:157], v79 offset:0\n"
    "ds_read_b64_tr_b16 v[158:159], v79 offset:1024\n"
    "global_load_lds_dwordx4 v53, s[36:37] offset:1024\n"
    "ds_read_b128 v[160:163], v57 offset:0\n"
    "ds_read_b128 v[164:167], v57 offset:2048\n"
    "ds_read_b128 v[168:171], v57 offset:4096\n"
    "ds_read_b128 v[172:175], v57 offset:6144\n"
    "global_load_lds_dwordx4 v54, s[36:37] offset:2048\n"
    "ds_read_b64_tr_b16 v[176:177], v76 offset:8192\n"
    "ds_read_b64_tr_b16 v[178:179], v76 offset:9216\n"
    "ds_read_b64_tr_b16 v[180:181], v77 offset:8192\n"
    "ds_read_b64_tr_b16 v[182:183], v77 offset:9216\n"
    "global_load_lds_dwordx4 v55, s[36:37] offset:3072\n"
    "ds_read_b64_tr_b16 v[184:185], v78 offset:8192\n"
    "ds_read_b64_tr_b16 v[186:187], v78 offset:9216\n"
    "ds_read_b64_tr_b16 v[188:189], v79 offset:8192\n"
    "ds_read_b64_tr_b16 v[190:191], v79 offset:9216\n"
    "s_waitcnt vmcnt(10) lgkmcnt(0)\n"
    "s_barrier\n"
    "v_mfma_f32_16x16x32_bf16 v[192:195], v[144:147], v[128:131], v[192:195]\n"
    "v_lshlrev_b32 v103, 7, v103\n"
    "v_mfma_f32_16x16x32_bf16 v[192:195], v[176:179], v[160:163], v[192:195]\n"
    "v_add_u32_dpp v52, v102, v48 quad_perm:[0,0,0,0] row_mask:0xf bank_mask:0xf\n"
    "v_mfma_f32_16x16x32_bf16 v[196:199], v[176:179], v[164:167], v[196:199]\n"
    "v_mfma_f32_16x16x32_bf16 v[196:199], v[144:147], v[132:135], v[196:199]\n"
    "v_add_u32_dpp v53, v102, v49 quad_perm:[1,1,1,1] row_mask:0xf bank_mask:0xf\n"
    "v_mfma_f32_16x16x32_bf16 v[200:203], v[144:147], v[136:139], v[200:203]\n"
    "v_mfma_f32_16x16x32_bf16 v[200:203], v[176:179], v[168:171], v[200:203]\n"
    "v_add_u32_dpp v54, v102, v50 quad_perm:[2,2,2,2] row_mask:0xf bank_mask:0xf\n"
    "v_mfma_f32_16x16x32_bf16 v[204:207], v[176:179], v[172:175], v[204:207]\n"
    "v_mfma_f32_16x16x32_bf16 v[204:207], v[144:147], v[140:143], v[204:207]\n"
    "v_add_u32_dpp v55, v102, v51 quad_perm:[3,3,3,3] row_mask:0xf bank_mask:0xf\n"
    "v_mfma_f32_16x16x32_bf16 v[220:223], v[148:151], v[140:143], v[220:223]\n"
    "v_mfma_f32_16x16x32_bf16 v[220:223], v[180:183], v[172:175], v[220:223]\n"
    "v_mul_u32_u24 v17, 12, v17\n"
    "v_mfma_f32_16x16x32_bf16 v[216:219], v[180:183], v[168:171], v[216:219]\n"
    "v_mfma_f32_16x16x32_bf16 v[216:219], v[148:151], v[136:139], v[216:219]\n"
    "v_and_b32 v19, 7, v1\n"
    "v_mfma_f32_16x16x32_bf16 v[212:215], v[148:151], v[132:135], v[212:215]\n"
    "v_mfma_f32_16x16x32_bf16 v[212:215], v[180:183], v[164:167], v[212:215]\n"
    "v_lshlrev_b32 v19, 4, v19\n"
    "v_mfma_f32_16x16x32_bf16 v[208:211], v[180:183], v[160:163], v[208:211]\n"
    "v_mfma_f32_16x16x32_bf16 v[208:211], v[148:151], v[128:131], v[208:211]\n"
    "v_lshrrev_b32 v18, 5, v1\n"
    "v_mfma_f32_16x16x32_bf16 v[224:227], v[152:155], v[128:131], v[224:227]\n"
    "v_mfma_f32_16x16x32_bf16 v[224:227], v[184:187], v[160:163], v[224:227]\n"
    "v_lshlrev_b32 v18, 2, v18\n"
    "v_mfma_f32_16x16x32_bf16 v[228:231], v[184:187], v[164:167], v[228:231]\n"
    "v_mfma_f32_16x16x32_bf16 v[228:231], v[152:155], v[132:135], v[228:231]\n"
    "v_and_b32 v20, 31, v1\n"
    "v_mfma_f32_16x16x32_bf16 v[232:235], v[152:155], v[136:139], v[232:235]\n"
    "v_mfma_f32_16x16x32_bf16 v[232:235], v[184:187], v[168:171], v[232:235]\n"
    "v_lshlrev_b32 v20, 2, v20\n"
    "v_mfma_f32_16x16x32_bf16 v[236:239], v[184:187], v[172:175], v[236:239]\n"
    "v_mfma_f32_16x16x32_bf16 v[236:239], v[152:155], v[140:143], v[236:239]\n"
    "s_lshl2_add_u32 s40, s22, s58\n"
    "v_mfma_f32_16x16x32_bf16 v[252:255], v[156:159], v[140:143], v[252:255]\n"
    "v_mfma_f32_16x16x32_bf16 v[252:255], v[188:191], v[172:175], v[252:255]\n"
    "s_sub_u32 s40, s40, 20\n"
    "v_mfma_f32_16x16x32_bf16 v[248:251], v[188:191], v[168:171], v[248:251]\n"
    "v_mfma_f32_16x16x32_bf16 v[248:251], v[156:159], v[136:139], v[248:251]\n"
    "v_lshl_add_u32 v25, v1, 18, s40\n"
    "v_mfma_f32_16x16x32_bf16 v[244:247], v[156:159], v[132:135], v[244:247]\n"
    "v_mfma_f32_16x16x32_bf16 v[244:247], v[188:191], v[164:167], v[244:247]\n"
    "s_lshl_b32 s43, s21, 16\n"
    "v_mfma_f32_16x16x32_bf16 v[240:243], v[188:191], v[160:163], v[240:243]\n"
    "v_mfma_f32_16x16x32_bf16 v[240:243], v[156:159], v[128:131], v[240:243]\n"
    "s_waitcnt vmcnt(6)\n"
    "s_barrier\n"
    "s_add_u32 m0, s35, 0\n"
    "s_nop 0\n"
    "global_load_lds_dwordx4 v46, s[30:31] offset:0\n"
    "ds_read_b128 v[128:131], v56 offset:49152\n"
    "ds_read_b128 v[132:135], v56 offset:51200\n"
    "ds_read_b128 v[136:139], v56 offset:53248\n"
    "global_load_lds_dwordx4 v47, s[30:31] offset:1024\n"
    "s_add_u32 s30, s30, 212992\n"
    "s_addc_u32 s31, s31, 0\n"
    "ds_read_b128 v[140:143], v56 offset:55296\n"
    "ds_read_b64_tr_b16 v[144:145], v76 offset:49152\n"
    "ds_read_b64_tr_b16 v[146:147], v76 offset:50176\n"
    "s_add_u32 m0, s34, 0\n"
    "s_nop 0\n"
    "global_load_lds_dwordx4 v52, s[36:37] offset:0\n"
    "ds_read_b64_tr_b16 v[148:149], v77 offset:49152\n"
    "ds_read_b64_tr_b16 v[150:151], v77 offset:50176\n"
    "ds_read_b64_tr_b16 v[152:153], v78 offset:49152\n"
    "global_load_lds_dwordx4 v53, s[36:37] offset:1024\n"
    "ds_read_b64_tr_b16 v[154:155], v78 offset:50176\n"
    "ds_read_b64_tr_b16 v[156:157], v79 offset:49152\n"
    "ds_read_b64_tr_b16 v[158:159], v79 offset:50176\n"
    "global_load_lds_dwordx4 v54, s[36:37] offset:2048\n"
    "ds_read_b128 v[160:163], v57 offset:49152\n"
    "ds_read_b128 v[164:167], v57 offset:51200\n"
    "ds_read_b128 v[168:171], v57 offset:53248\n"
    "global_load_lds_dwordx4 v55, s[36:37] offset:3072\n"
    "ds_read_b128 v[172:175], v57 offset:55296\n"
    "ds_read_b64_tr_b16 v[176:177], v76 offset:57344\n"
    "ds_read_b64_tr_b16 v[178:179], v76 offset:58368\n"
    "global_load_dwordx2 v[84:85], v42, s[10:11] offset:0\n"
    "ds_read_b64_tr_b16 v[180:181], v77 offset:57344\n"
    "ds_read_b64_tr_b16 v[182:183], v77 offset:58368\n"
    "ds_read_b64_tr_b16 v[184:185], v78 offset:57344\n"
    "ds_read_b64_tr_b16 v[186:187], v78 offset:58368\n"
    "ds_read_b64_tr_b16 v[188:189], v79 offset:57344\n"
    "ds_read_b64_tr_b16 v[190:191], v79 offset:58368\n"
    "s_waitcnt vmcnt(11) lgkmcnt(0)\n"
    "s_barrier\n"
    "v_mfma_f32_16x16x32_bf16 v[192:195], v[144:147], v[128:131], v[192:195]\n"
    "v_lshlrev_b32 v104, 7, v104\n"
    "v_mfma_f32_16x16x32_bf16 v[192:195], v[176:179], v[160:163], v[192:195]\n"
    "v_mfma_f32_16x16x32_bf16 v[196:199], v[176:179], v[164:167], v[196:199]\n"
    "v_add_u32_dpp v52, v103, v48 quad_perm:[0,0,0,0] row_mask:0xf bank_mask:0xf\n"
    "v_mfma_f32_16x16x32_bf16 v[196:199], v[144:147], v[132:135], v[196:199]\n"
    "v_mfma_f32_16x16x32_bf16 v[200:203], v[144:147], v[136:139], v[200:203]\n"
    "v_add_u32_dpp v53, v103, v49 quad_perm:[1,1,1,1] row_mask:0xf bank_mask:0xf\n"
    "v_mfma_f32_16x16x32_bf16 v[200:203], v[176:179], v[168:171], v[200:203]\n"
    "v_mfma_f32_16x16x32_bf16 v[204:207], v[176:179], v[172:175], v[204:207]\n"
    "v_add_u32_dpp v54, v103, v50 quad_perm:[2,2,2,2] row_mask:0xf bank_mask:0xf\n"
    "v_mfma_f32_16x16x32_bf16 v[204:207], v[144:147], v[140:143], v[204:207]\n"
    "v_mfma_f32_16x16x32_bf16 v[220:223], v[148:151], v[140:143], v[220:223]\n"
    "v_add_u32_dpp v55, v103, v51 quad_perm:[3,3,3,3] row_mask:0xf bank_mask:0xf\n"
    "v_mfma_f32_16x16x32_bf16 v[220:223], v[180:183], v[172:175], v[220:223]\n"
    "v_mfma_f32_16x16x32_bf16 v[216:219], v[180:183], v[168:171], v[216:219]\n"
    "v_lshrrev_b32 v12, 4, v1\n"
    "v_mfma_f32_16x16x32_bf16 v[216:219], v[148:151], v[136:139], v[216:219]\n"
    "v_mfma_f32_16x16x32_bf16 v[212:215], v[148:151], v[132:135], v[212:215]\n"
    "v_bfe_u32 v13, v1, 2, 2\n"
    "v_mfma_f32_16x16x32_bf16 v[212:215], v[180:183], v[164:167], v[212:215]\n"
    "v_mfma_f32_16x16x32_bf16 v[208:211], v[180:183], v[160:163], v[208:211]\n"
    "v_and_b32 v21, 3, v1\n"
    "v_mfma_f32_16x16x32_bf16 v[208:211], v[148:151], v[128:131], v[208:211]\n"
    "v_mfma_f32_16x16x32_bf16 v[224:227], v[152:155], v[128:131], v[224:227]\n"
    "s_mul_i32 s40, s22, 0x2020\n"
    "v_mfma_f32_16x16x32_bf16 v[224:227], v[184:187], v[160:163], v[224:227]\n"
    "v_mfma_f32_16x16x32_bf16 v[228:231], v[184:187], v[164:167], v[228:231]\n"
    "s_and_b32 s40, s40, 0x4060\n"
    "v_mfma_f32_16x16x32_bf16 v[228:231], v[152:155], v[132:135], v[228:231]\n"
    "v_mfma_f32_16x16x32_bf16 v[232:235], v[152:155], v[136:139], v[232:235]\n"
    "s_add_u32 s40, s40, s43\n"
    "v_mfma_f32_16x16x32_bf16 v[232:235], v[184:187], v[168:171], v[232:235]\n"
    "v_mfma_f32_16x16x32_bf16 v[236:239], v[184:187], v[172:175], v[236:239]\n"
    "v_lshl_add_u32 v21, v21, 3, s40\n"
    "v_mfma_f32_16x16x32_bf16 v[236:239], v[152:155], v[140:143], v[236:239]\n"
    "v_mfma_f32_16x16x32_bf16 v[252:255], v[156:159], v[140:143], v[252:255]\n"
    "s_movk_i32 s41, 144\n"
    "v_mfma_f32_16x16x32_bf16 v[252:255], v[188:191], v[172:175], v[252:255]\n"
    "v_mfma_f32_16x16x32_bf16 v[248:251], v[188:191], v[168:171], v[248:251]\n"
    "v_add_u32 v22, v12, v13\n"
    "v_mfma_f32_16x16x32_bf16 v[248:251], v[156:159], v[136:139], v[248:251]\n"
    "v_mfma_f32_16x16x32_bf16 v[244:247], v[156:159], v[132:135], v[244:247]\n"
    "v_and_b32 v22, 3, v22\n"
    "v_mfma_f32_16x16x32_bf16 v[244:247], v[188:191], v[164:167], v[244:247]\n"
    "v_mfma_f32_16x16x32_bf16 v[240:243], v[188:191], v[160:163], v[240:243]\n"
    "v_mfma_f32_16x16x32_bf16 v[240:243], v[156:159], v[128:131], v[240:243]\n"
    "s_waitcnt vmcnt(7)\n"
    "s_barrier\n"
    "s_add_u32 m0, s35, 49152\n"
    "s_nop 0\n"
    "global_load_lds_dwordx4 v46, s[30:31] offset:0\n"
    "ds_read_b128 v[128:131], v58 offset:0\n"
    "ds_read_b128 v[132:135], v58 offset:2048\n"
    "ds_read_b128 v[136:139], v58 offset:4096\n"
    "global_load_lds_dwordx4 v47, s[30:31] offset:1024\n"
    "s_add_u32 s30, s30, 212992\n"
    "s_addc_u32 s31, s31, 0\n"
    "ds_read_b128 v[140:143], v58 offset:6144\n"
    "ds_read_b64_tr_b16 v[144:145], v80 offset:0\n"
    "ds_read_b64_tr_b16 v[146:147], v80 offset:1024\n"
    "s_add_u32 m0, s34, 49152\n"
    "s_nop 0\n"
    "global_load_lds_dwordx4 v52, s[36:37] offset:0\n"
    "ds_read_b64_tr_b16 v[148:149], v81 offset:0\n"
    "ds_read_b64_tr_b16 v[150:151], v81 offset:1024\n"
    "ds_read_b64_tr_b16 v[152:153], v82 offset:0\n"
    "global_load_lds_dwordx4 v53, s[36:37] offset:1024\n"
    "ds_read_b64_tr_b16 v[154:155], v82 offset:1024\n"
    "ds_read_b64_tr_b16 v[156:157], v83 offset:0\n"
    "ds_read_b64_tr_b16 v[158:159], v83 offset:1024\n"
    "global_load_lds_dwordx4 v54, s[36:37] offset:2048\n"
    "ds_read_b128 v[160:163], v59 offset:0\n"
    "ds_read_b128 v[164:167], v59 offset:2048\n"
    "ds_read_b128 v[168:171], v59 offset:4096\n"
    "global_load_lds_dwordx4 v55, s[36:37] offset:3072\n"
    "ds_read_b128 v[172:175], v59 offset:6144\n"
    "ds_read_b64_tr_b16 v[176:177], v80 offset:8192\n"
    "ds_read_b64_tr_b16 v[178:179], v80 offset:9216\n"
    "global_load_dwordx2 v[86:87], v42, s[10:11] offset:32\n"
    "ds_read_b64_tr_b16 v[180:181], v81 offset:8192\n"
    "ds_read_b64_tr_b16 v[182:183], v81 offset:9216\n"
    "ds_read_b64_tr_b16 v[184:185], v82 offset:8192\n"
    "ds_read_b64_tr_b16 v[186:187], v82 offset:9216\n"
    "ds_read_b64_tr_b16 v[188:189], v83 offset:8192\n"
    "ds_read_b64_tr_b16 v[190:191], v83 offset:9216\n"
    "s_waitcnt vmcnt(12) lgkmcnt(0)\n"
    "s_barrier\n"
    "v_mfma_f32_16x16x32_bf16 v[192:195], v[144:147], v[128:131], v[192:195]\n"
    "v_lshlrev_b32 v105, 7, v105\n"
    "v_mfma_f32_16x16x32_bf16 v[192:195], v[176:179], v[160:163], v[192:195]\n"
    "v_add_u32_dpp v52, v104, v48 quad_perm:[0,0,0,0] row_mask:0xf bank_mask:0xf\n"
    "v_mfma_f32_16x16x32_bf16 v[196:199], v[176:179], v[164:167], v[196:199]\n"
    "v_mfma_f32_16x16x32_bf16 v[196:199], v[144:147], v[132:135], v[196:199]\n"
    "v_add_u32_dpp v53, v104, v49 quad_perm:[1,1,1,1] row_mask:0xf bank_mask:0xf\n"
    "v_mfma_f32_16x16x32_bf16 v[200:203], v[144:147], v[136:139], v[200:203]\n"
    "v_mfma_f32_16x16x32_bf16 v[200:203], v[176:179], v[168:171], v[200:203]\n"
    "v_add_u32_dpp v54, v104, v50 quad_perm:[2,2,2,2] row_mask:0xf bank_mask:0xf\n"
    "v_mfma_f32_16x16x32_bf16 v[204:207], v[176:179], v[172:175], v[204:207]\n"
    "v_mfma_f32_16x16x32_bf16 v[204:207], v[144:147], v[140:143], v[204:207]\n"
    "v_add_u32_dpp v55, v104, v51 quad_perm:[3,3,3,3] row_mask:0xf bank_mask:0xf\n"
    "v_mfma_f32_16x16x32_bf16 v[220:223], v[148:151], v[140:143], v[220:223]\n"
    "v_mfma_f32_16x16x32_bf16 v[220:223], v[180:183], v[172:175], v[220:223]\n"
    "v_mad_u32_u24 v4, v22, s41, v21\n"
    "v_mfma_f32_16x16x32_bf16 v[216:219], v[180:183], v[168:171], v[216:219]\n"
    "v_mfma_f32_16x16x32_bf16 v[216:219], v[148:151], v[136:139], v[216:219]\n"
    "v_cmp_gt_u32 vcc, 2, v13\n"
    "v_mfma_f32_16x16x32_bf16 v[212:215], v[148:151], v[132:135], v[212:215]\n"
    "v_mfma_f32_16x16x32_bf16 v[212:215], v[180:183], v[164:167], v[212:215]\n"
    "v_add_u32 v24, 22, v13\n"
    "v_mfma_f32_16x16x32_bf16 v[208:211], v[180:183], v[160:163], v[208:211]\n"
    "v_mfma_f32_16x16x32_bf16 v[208:211], v[148:151], v[128:131], v[208:211]\n"
    "v_lshlrev_b32 v13, 3, v13\n"
    "v_mfma_f32_16x16x32_bf16 v[224:227], v[152:155], v[128:131], v[224:227]\n"
    "v_mfma_f32_16x16x32_bf16 v[224:227], v[184:187], v[160:163], v[224:227]\n"
    "v_cndmask_b32 v22, v24, v22, vcc\n"
    "v_mfma_f32_16x16x32_bf16 v[228:231], v[184:187], v[164:167], v[228:231]\n"
    "v_mfma_f32_16x16x32_bf16 v[228:231], v[152:155], v[132:135], v[228:231]\n"
    "v_mad_u32_u24 v3, v22, s41, v21\n"
    "v_mfma_f32_16x16x32_bf16 v[232:235], v[152:155], v[136:139], v[232:235]\n"
    "v_mfma_f32_16x16x32_bf16 v[232:235], v[184:187], v[168:171], v[232:235]\n"
    "v_cmp_eq_u32 vcc, 3, v12\n"
    "v_mfma_f32_16x16x32_bf16 v[236:239], v[184:187], v[172:175], v[236:239]\n"
    "v_mfma_f32_16x16x32_bf16 v[236:239], v[152:155], v[140:143], v[236:239]\n"
    "v_xor_b32 v22, 0, v12\n"
    "v_mfma_f32_16x16x32_bf16 v[252:255], v[156:159], v[140:143], v[252:255]\n"
    "v_mfma_f32_16x16x32_bf16 v[252:255], v[188:191], v[172:175], v[252:255]\n"
    "v_and_b32 v22, 1, v22\n"
    "v_mfma_f32_16x16x32_bf16 v[248:251], v[188:191], v[168:171], v[248:251]\n"
    "v_mfma_f32_16x16x32_bf16 v[248:251], v[156:159], v[136:139], v[248:251]\n"
    "v_lshlrev_b32 v22, 1, v22\n"
    "v_mfma_f32_16x16x32_bf16 v[244:247], v[156:159], v[132:135], v[244:247]\n"
    "v_mfma_f32_16x16x32_bf16 v[244:247], v[188:191], v[164:167], v[244:247]\n"
    "v_lshl_add_u32 v22, v12, 3, v22\n"
    "v_mfma_f32_16x16x32_bf16 v[240:243], v[188:191], v[160:163], v[240:243]\n"
    "v_mfma_f32_16x16x32_bf16 v[240:243], v[156:159], v[128:131], v[240:243]\n"
    "s_waitcnt vmcnt(8)\n"
    "s_barrier\n"
    "s_add_u32 m0, s35, 98304\n"
    "s_nop 0\n"
    "global_load_lds_dwordx4 v46, s[30:31] offset:0\n"
    "ds_read_b128 v[128:131], v56 offset:0\n"
    "ds_read_b128 v[132:135], v56 offset:2048\n"
    "ds_read_b128 v[136:139], v56 offset:4096\n"
    "global_load_lds_dwordx4 v47, s[30:31] offset:1024\n"
    "s_add_u32 s66, s28, 3\n"
    "s_and_b32 s66, s66, 7\n"
    "s_mul_i32 s66, s66, 638976\n"
    "s_add_u32 s30, s6, s66\n"
    "s_addc_u32 s31, s7, 0\n"
    "ds_read_b128 v[140:143], v56 offset:6144\n"
    "ds_read_b64_tr_b16 v[144:145], v76 offset:0\n"
    "ds_read_b64_tr_b16 v[146:147], v76 offset:1024\n"
    "s_add_u32 m0, s34, 98304\n"
    "s_nop 0\n"
    "global_load_lds_dwordx4 v52, s[36:37] offset:0\n"
    "ds_read_b64_tr_b16 v[148:149], v77 offset:0\n"
    "ds_read_b64_tr_b16 v[150:151], v77 offset:1024\n"
    "ds_read_b64_tr_b16 v[152:153], v78 offset:0\n"
    "global_load_lds_dwordx4 v53, s[36:37] offset:1024\n"
    "ds_read_b64_tr_b16 v[154:155], v78 offset:1024\n"
    "ds_read_b64_tr_b16 v[156:157], v79 offset:0\n"
    "ds_read_b64_tr_b16 v[158:159], v79 offset:1024\n"
    "global_load_lds_dwordx4 v54, s[36:37] offset:2048\n"
    "ds_read_b128 v[160:163], v57 offset:0\n"
    "ds_read_b128 v[164:167], v57 offset:2048\n"
    "ds_read_b128 v[168:171], v57 offset:4096\n"
    "global_load_lds_dwordx4 v55, s[36:37] offset:3072\n"
    "ds_read_b128 v[172:175], v57 offset:6144\n"
    "ds_read_b64_tr_b16 v[176:177], v76 offset:8192\n"
    "ds_read_b64_tr_b16 v[178:179], v76 offset:9216\n"
    "global_load_dwordx2 v[88:89], v42, s[10:11] offset:64\n"
    "ds_read_b64_tr_b16 v[180:181], v77 offset:8192\n"
    "ds_read_b64_tr_b16 v[182:183], v77 offset:9216\n"
    "ds_read_b64_tr_b16 v[184:185], v78 offset:8192\n"
    "ds_read_b64_tr_b16 v[186:187], v78 offset:9216\n"
    "ds_read_b64_tr_b16 v[188:189], v79 offset:8192\n"
    "ds_read_b64_tr_b16 v[190:191], v79 offset:9216\n"
    "s_waitcnt vmcnt(12) lgkmcnt(0)\n"
    "s_barrier\n"
    "v_mfma_f32_16x16x32_bf16 v[192:195], v[144:147], v[128:131], v[192:195]\n"
    "v_lshlrev_b32 v106, 7, v106\n"
    "v_mfma_f32_16x16x32_bf16 v[192:195], v[176:179], v[160:163], v[192:195]\n"
    "v_mfma_f32_16x16x32_bf16 v[196:199], v[176:179], v[164:167], v[196:199]\n"
    "v_add_u32_dpp v52, v105, v48 quad_perm:[0,0,0,0] row_mask:0xf bank_mask:0xf\n"
    "v_mfma_f32_16x16x32_bf16 v[196:199], v[144:147], v[132:135], v[196:199]\n"
    "v_mfma_f32_16x16x32_bf16 v[200:203], v[144:147], v[136:139], v[200:203]\n"
    "v_add_u32_dpp v53, v105, v49 quad_perm:[1,1,1,1] row_mask:0xf bank_mask:0xf\n"
    "v_mfma_f32_16x16x32_bf16 v[200:203], v[176:179], v[168:171], v[200:203]\n"
    "v_mfma_f32_16x16x32_bf16 v[204:207], v[176:179], v[172:175], v[204:207]\n"
    "v_add_u32_dpp v54, v105, v50 quad_perm:[2,2,2,2] row_mask:0xf bank_mask:0xf\n"
    "v_mfma_f32_16x16x32_bf16 v[204:207], v[144:147], v[140:143], v[204:207]\n"
    "v_mfma_f32_16x16x32_bf16 v[220:223], v[148:151], v[140:143], v[220:223]\n"
    "v_add_u32_dpp v55, v105, v51 quad_perm:[3,3,3,3] row_mask:0xf bank_mask:0xf\n"
    "v_mfma_f32_16x16x32_bf16 v[220:223], v[180:183], v[172:175], v[220:223]\n"
    "v_mfma_f32_16x16x32_bf16 v[216:219], v[180:183], v[168:171], v[216:219]\n"
    "s_mov_b32 s40, 0x05040100\n"
    "v_mfma_f32_16x16x32_bf16 v[216:219], v[148:151], v[136:139], v[216:219]\n"
    "v_mfma_f32_16x16x32_bf16 v[212:215], v[148:151], v[132:135], v[212:215]\n"
    "v_bfe_u32 v24, s40, v13, 8\n"
    "v_mfma_f32_16x16x32_bf16 v[212:215], v[180:183], v[164:167], v[212:215]\n"
    "v_mfma_f32_16x16x32_bf16 v[208:211], v[180:183], v[160:163], v[208:211]\n"
    "v_add_u32 v22, v22, v24\n"
    "v_mfma_f32_16x16x32_bf16 v[208:211], v[148:151], v[128:131], v[208:211]\n"
    "v_mfma_f32_16x16x32_bf16 v[224:227], v[152:155], v[128:131], v[224:227]\n"
    "s_mov_b32 s40, 0x1a191818\n"
    "v_mfma_f32_16x16x32_bf16 v[224:227], v[184:187], v[160:163], v[224:227]\n"
    "v_mfma_f32_16x16x32_bf16 v[228:231], v[184:187], v[164:167], v[228:231]\n"
    "v_bfe_u32 v24, s40, v13, 8\n"
    "v_mfma_f32_16x16x32_bf16 v[228:231], v[152:155], v[132:135], v[228:231]\n"
    "v_mfma_f32_16x16x32_bf16 v[232:235], v[152:155], v[136:139], v[232:235]\n"
    "v_cndmask_b32 v22, v22, v24, vcc\n"
    "v_mfma_f32_16x16x32_bf16 v[232:235], v[184:187], v[168:171], v[232:235]\n"
    "v_mfma_f32_16x16x32_bf16 v[236:239], v[184:187], v[172:175], v[236:239]\n"
    "v_mad_u32_u24 v5, v22, s41, v21\n"
    "v_mfma_f32_16x16x32_bf16 v[236:239], v[152:155], v[140:143], v[236:239]\n"
    "v_mfma_f32_16x16x32_bf16 v[252:255], v[156:159], v[140:143], v[252:255]\n"
    "v_xor_b32 v22, 1, v12\n"
    "v_mfma_f32_16x16x32_bf16 v[252:255], v[188:191], v[172:175], v[252:255]\n"
    "v_mfma_f32_16x16x32_bf16 v[248:251], v[188:191], v[168:171], v[248:251]\n"
    "v_and_b32 v22, 1, v22\n"
    "v_mfma_f32_16x16x32_bf16 v[248:251], v[156:159], v[136:139], v[248:251]\n"
    "v_mfma_f32_16x16x32_bf16 v[244:247], v[156:159], v[132:135], v[244:247]\n"
    "v_lshlrev_b32 v22, 1, v22\n"
    "v_mfma_f32_16x16x32_bf16 v[244:247], v[188:191], v[164:167], v[244:247]\n"
    "v_mfma_f32_16x16x32_bf16 v[240:243], v[188:191], v[160:163], v[240:243]\n"
    "v_mfma_f32_16x16x32_bf16 v[240:243], v[156:159], v[128:131], v[240:243]\n"
    "s_waitcnt vmcnt(8)\n"
    "s_barrier\n"
    "s_add_u32 m0, s35, 0\n"
    "s_nop 0\n"
    "global_load_lds_dwordx4 v46, s[30:31] offset:0\n"
    "ds_read_b128 v[128:131], v56 offset:49152\n"
    "ds_read_b128 v[132:135], v56 offset:51200\n"
    "ds_read_b128 v[136:139], v56 offset:53248\n"
    "global_load_lds_dwordx4 v47, s[30:31] offset:1024\n"
    "s_add_u32 s30, s30, 212992\n"
    "s_addc_u32 s31, s31, 0\n"
    "ds_read_b128 v[140:143], v56 offset:55296\n"
    "ds_read_b64_tr_b16 v[144:145], v76 offset:49152\n"
    "ds_read_b64_tr_b16 v[146:147], v76 offset:50176\n"
    "s_add_u32 m0, s34, 0\n"
    "s_nop 0\n"
    "global_load_lds_dwordx4 v52, s[36:37] offset:0\n"
    "ds_read_b64_tr_b16 v[148:149], v77 offset:49152\n"
    "ds_read_b64_tr_b16 v[150:151], v77 offset:50176\n"
    "ds_read_b64_tr_b16 v[152:153], v78 offset:49152\n"
    "global_load_lds_dwordx4 v53, s[36:37] offset:1024\n"
    "ds_read_b64_tr_b16 v[154:155], v78 offset:50176\n"
    "ds_read_b64_tr_b16 v[156:157], v79 offset:49152\n"
    "ds_read_b64_tr_b16 v[158:159], v79 offset:50176\n"
    "global_load_lds_dwordx4 v54, s[36:37] offset:2048\n"
    "ds_read_b128 v[160:163], v57 offset:49152\n"
    "ds_read_b128 v[164:167], v57 offset:51200\n"
    "ds_read_b128 v[168:171], v57 offset:53248\n"
    "global_load_lds_dwordx4 v55, s[36:37] offset:3072\n"
    "ds_read_b128 v[172:175], v57 offset:55296\n"
    "ds_read_b64_tr_b16 v[176:177], v76 offset:57344\n"
    "ds_read_b64_tr_b16 v[178:179], v76 offset:58368\n"
    "global_load_dwordx2 v[90:91], v42, s[10:11] offset:96\n"
    "ds_read_b64_tr_b16 v[180:181], v77 offset:57344\n"
    "ds_read_b64_tr_b16 v[182:183], v77 offset:58368\n"
    "ds_read_b64_tr_b16 v[184:185], v78 offset:57344\n"
    "ds_read_b64_tr_b16 v[186:187], v78 offset:58368\n"
    "ds_read_b64_tr_b16 v[188:189], v79 offset:57344\n"
    "ds_read_b64_tr_b16 v[190:191], v79 offset:58368\n"
    "s_waitcnt vmcnt(12) lgkmcnt(0)\n"
    "s_barrier\n"
    "v_mfma_f32_16x16x32_bf16 v[192:195], v[144:147], v[128:131], v[192:195]\n"
    "v_lshlrev_b32 v107, 7, v107\n"
    "v_mfma_f32_16x16x32_bf16 v[192:195], v[176:179], v[160:163], v[192:195]\n"
    "v_mfma_f32_16x16x32_bf16 v[196:199], v[176:179], v[164:167], v[196:199]\n"
    "v_add_u32_dpp v52, v106, v48 quad_perm:[0,0,0,0] row_mask:0xf bank_mask:0xf\n"
    "v_mfma_f32_16x16x32_bf16 v[196:199], v[144:147], v[132:135], v[196:199]\n"
    "v_mfma_f32_16x16x32_bf16 v[200:203], v[144:147], v[136:139], v[200:203]\n"
    "v_add_u32_dpp v53, v106, v49 quad_perm:[1,1,1,1] row_mask:0xf bank_mask:0xf\n"
    "v_mfma_f32_16x16x32_bf16 v[200:203], v[176:179], v[168:171], v[200:203]\n"
    "v_mfma_f32_16x16x32_bf16 v[204:207], v[176:179], v[172:175], v[204:207]\n"
    "v_add_u32_dpp v54, v106, v50 quad_perm:[2,2,2,2] row_mask:0xf bank_mask:0xf\n"
    "v_mfma_f32_16x16x32_bf16 v[204:207], v[144:147], v[140:143], v[204:207]\n"
    "v_mfma_f32_16x16x32_bf16 v[220:223], v[148:151], v[140:143], v[220:223]\n"
    "v_add_u32_dpp v55, v106, v51 quad_perm:[3,3,3,3] row_mask:0xf bank_mask:0xf\n"
    "v_mfma_f32_16x16x32_bf16 v[220:223], v[180:183], v[172:175], v[220:223]\n"
    "v_mfma_f32_16x16x32_bf16 v[216:219], v[180:183], v[168:171], v[216:219]\n"
    "v_lshl_add_u32 v22, v12, 3, v22\n"
    "v_mfma_f32_16x16x32_bf16 v[216:219], v[148:151], v[136:139], v[216:219]\n"
    "v_mfma_f32_16x16x32_bf16 v[212:215], v[148:151], v[132:135], v[212:215]\n"
    "s_mov_b32 s40, 0x05040100\n"
    "v_mfma_f32_16x16x32_bf16 v[212:215], v[180:183], v[164:167], v[212:215]\n"
    "v_mfma_f32_16x16x32_bf16 v[208:211], v[180:183], v[160:163], v[208:211]\n"
    "v_bfe_u32 v24, s40, v13, 8\n"
    "v_mfma_f32_16x16x32_bf16 v[208:211], v[148:151], v[128:131], v[208:211]\n"
    "v_mfma_f32_16x16x32_bf16 v[224:227], v[152:155], v[128:131], v[224:227]\n"
    "v_add_u32 v22, v22, v24\n"
    "v_mfma_f32_16x16x32_bf16 v[224:227], v[184:187], v[160:163], v[224:227]\n"
    "v_mfma_f32_16x16x32_bf16 v[228:231], v[184:187], v[164:167], v[228:231]\n"
    "s_mov_b32 s40, 0x1a191918\n"
    "v_mfma_f32_16x16x32_bf16 v[228:231], v[152:155], v[132:135], v[228:231]\n"
    "v_mfma_f32_16x16x32_bf16 v[232:235], v[152:155], v[136:139], v[232:235]\n"
    "v_bfe_u32 v24, s40, v13, 8\n"
    "v_mfma_f32_16x16x32_bf16 v[232:235], v[184:187], v[168:171], v[232:235]\n"
    "v_mfma_f32_16x16x32_bf16 v[236:239], v[184:187], v[172:175], v[236:239]\n"
    "v_cndmask_b32 v22, v22, v24, vcc\n"
    "v_mfma_f32_16x16x32_bf16 v[236:239], v[152:155], v[140:143], v[236:239]\n"
    "v_mfma_f32_16x16x32_bf16 v[252:255], v[156:159], v[140:143], v[252:255]\n"
    "v_mad_u32_u24 v6, v22, s41, v21\n"
    "v_mfma_f32_16x16x32_bf16 v[252:255], v[188:191], v[172:175], v[252:255]\n"
    "v_mfma_f32_16x16x32_bf16 v[248:251], v[188:191], v[168:171], v[248:251]\n"
    "v_bfrev_b32 v14, v12\n"
    "v_mfma_f32_16x16x32_bf16 v[248:251], v[156:159], v[136:139], v[248:251]\n"
    "v_mfma_f32_16x16x32_bf16 v[244:247], v[156:159], v[132:135], v[244:247]\n"
    "v_lshrrev_b32 v14, 26, v14\n"
    "v_mfma_f32_16x16x32_bf16 v[244:247], v[188:191], v[164:167], v[244:247]\n"
    "v_mfma_f32_16x16x32_bf16 v[240:243], v[188:191], v[160:163], v[240:243]\n"
    "v_mfma_f32_16x16x32_bf16 v[240:243], v[156:159], v[128:131], v[240:243]\n"
    "s_waitcnt vmcnt(8)\n"
    "s_barrier\n"
    "s_add_u32 m0, s35, 49152\n"
    "s_nop 0\n"
    "global_load_lds_dwordx4 v46, s[30:31] offset:0\n"
    "ds_read_b128 v[128:131], v58 offset:0\n"
    "ds_read_b128 v[132:135], v58 offset:2048\n"
    "ds_read_b128 v[136:139], v58 offset:4096\n"
    "global_load_lds_dwordx4 v47, s[30:31] offset:1024\n"
    "s_add_u32 s30, s30, 212992\n"
    "s_addc_u32 s31, s31, 0\n"
    "ds_read_b128 v[140:143], v58 offset:6144\n"
    "ds_read_b64_tr_b16 v[144:145], v80 offset:0\n"
    "ds_read_b64_tr_b16 v[146:147], v80 offset:1024\n"
    "s_add_u32 m0, s34, 49152\n"
    "s_nop 0\n"
    "global_load_lds_dwordx4 v52, s[36:37] offset:0\n"
    "ds_read_b64_tr_b16 v[148:149], v81 offset:0\n"
    "ds_read_b64_tr_b16 v[150:151], v81 offset:1024\n"
    "ds_read_b64_tr_b16 v[152:153], v82 offset:0\n"
    "global_load_lds_dwordx4 v53, s[36:37] offset:1024\n"
    "ds_read_b64_tr_b16 v[154:155], v82 offset:1024\n"
    "ds_read_b64_tr_b16 v[156:157], v83 offset:0\n"
    "ds_read_b64_tr_b16 v[158:159], v83 offset:1024\n"
    "global_load_lds_dwordx4 v54, s[36:37] offset:2048\n"
    "ds_read_b128 v[160:163], v59 offset:0\n"
    "ds_read_b128 v[164:167], v59 offset:2048\n"
    "ds_read_b128 v[168:171], v59 offset:4096\n"
    "global_load_lds_dwordx4 v55, s[36:37] offset:3072\n"
    "ds_read_b128 v[172:175], v59 offset:6144\n"
    "ds_read_b64_tr_b16 v[176:177], v80 offset:8192\n"
    "ds_read_b64_tr_b16 v[178:179], v80 offset:9216\n"
    "global_load_dwordx2 v[92:93], v42, s[18:19] offset:0\n"
    "ds_read_b64_tr_b16 v[180:181], v81 offset:8192\n"
    "ds_read_b64_tr_b16 v[182:183], v81 offset:9216\n"
    "ds_read_b64_tr_b16 v[184:185], v82 offset:8192\n"
    "ds_read_b64_tr_b16 v[186:187], v82 offset:9216\n"
    "ds_read_b64_tr_b16 v[188:189], v83 offset:8192\n"
    "ds_read_b64_tr_b16 v[190:191], v83 offset:9216\n"
    "s_waitcnt vmcnt(12) lgkmcnt(0)\n"
    "s_barrier\n"
    "v_mfma_f32_16x16x32_bf16 v[192:195], v[144:147], v[128:131], v[192:195]\n"
    "v_lshlrev_b32 v108, 7, v108\n"
    "v_mfma_f32_16x16x32_bf16 v[192:195], v[176:179], v[160:163], v[192:195]\n"
    "v_add_u32_dpp v52, v107, v48 quad_perm:[0,0,0,0] row_mask:0xf bank_mask:0xf\n"
    "v_mfma_f32_16x16x32_bf16 v[196:199], v[176:179], v[164:167], v[196:199]\n"
    "v_mfma_f32_16x16x32_bf16 v[196:199], v[144:147], v[132:135], v[196:199]\n"
    "v_add_u32_dpp v53, v107, v49 quad_perm:[1,1,1,1] row_mask:0xf bank_mask:0xf\n"
    "v_mfma_f32_16x16x32_bf16 v[200:203], v[144:147], v[136:139], v[200:203]\n"
    "v_mfma_f32_16x16x32_bf16 v[200:203], v[176:179], v[168:171], v[200:203]\n"
    "v_add_u32_dpp v54, v107, v50 quad_perm:[2,2,2,2] row_mask:0xf bank_mask:0xf\n"
    "v_mfma_f32_16x16x32_bf16 v[204:207], v[176:179], v[172:175], v[204:207]\n"
    "v_mfma_f32_16x16x32_bf16 v[204:207], v[144:147], v[140:143], v[204:207]\n"
    "v_add_u32_dpp v55, v107, v51 quad_perm:[3,3,3,3] row_mask:0xf bank_mask:0xf\n"
    "v_mfma_f32_16x16x32_bf16 v[220:223], v[148:151], v[140:143], v[220:223]\n"
    "v_mfma_f32_16x16x32_bf16 v[220:223], v[180:183], v[172:175], v[220:223]\n"
    "s_add_u32 s40, s22, 3\n"
    "v_mfma_f32_16x16x32_bf16 v[216:219], v[180:183], v[168:171], v[216:219]\n"
    "v_mfma_f32_16x16x32_bf16 v[216:219], v[148:151], v[136:139], v[216:219]\n"
    "s_and_b32 s40, s40, 3\n"
    "v_mfma_f32_16x16x32_bf16 v[212:215], v[148:151], v[132:135], v[212:215]\n"
    "v_mfma_f32_16x16x32_bf16 v[212:215], v[180:183], v[164:167], v[212:215]\n"
    "s_lshl2_add_u32 s40, s40, s22\n"
    "v_mfma_f32_16x16x32_bf16 v[208:211], v[180:183], v[160:163], v[208:211]\n"
    "v_mfma_f32_16x16x32_bf16 v[208:211], v[148:151], v[128:131], v[208:211]\n"
    "s_add_u32 s40, s40, s58\n"
    "v_mfma_f32_16x16x32_bf16 v[224:227], v[152:155], v[128:131], v[224:227]\n"
    "v_mfma_f32_16x16x32_bf16 v[224:227], v[184:187], v[160:163], v[224:227]\n"
    "s_lshl_b32 s40, s40, 2\n"
    "v_mfma_f32_16x16x32_bf16 v[228:231], v[184:187], v[164:167], v[228:231]\n"
    "v_mfma_f32_16x16x32_bf16 v[228:231], v[152:155], v[132:135], v[228:231]\n"
    "s_add_u32 s40, s40, -450048\n"
    "v_mfma_f32_16x16x32_bf16 v[232:235], v[152:155], v[136:139], v[232:235]\n"
    "v_mfma_f32_16x16x32_bf16 v[232:235], v[184:187], v[168:171], v[232:235]\n"
    "v_add_u32 v14, s40, v14\n"
    "v_mfma_f32_16x16x32_bf16 v[236:239], v[184:187], v[172:175], v[236:239]\n"
    "v_mfma_f32_16x16x32_bf16 v[236:239], v[152:155], v[140:143], v[236:239]\n"
    "v_and_b32 v2, 1, v1\n"
    "v_mfma_f32_16x16x32_bf16 v[252:255], v[156:159], v[140:143], v[252:255]\n"
    "v_mfma_f32_16x16x32_bf16 v[252:255], v[188:191], v[172:175], v[252:255]\n"
    "v_mul_u32_u24 v2, 0xaaaa, v2\n"
    "v_mfma_f32_16x16x32_bf16 v[248:251], v[188:191], v[168:171], v[248:251]\n"
    "v_mfma_f32_16x16x32_bf16 v[248:251], v[156:159], v[136:139], v[248:251]\n"
    "v_add_u32 v2, 0x4444, v2\n"
    "v_mfma_f32_16x16x32_bf16 v[244:247], v[156:159], v[132:135], v[244:247]\n"
    "v_mfma_f32_16x16x32_bf16 v[244:247], v[188:191], v[164:167], v[244:247]\n"
    "v_mad_u32_u24 v15, v17, 36, v19\n"
    "v_mfma_f32_16x16x32_bf16 v[240:243], v[188:191], v[160:163], v[240:243]\n"
    "v_mfma_f32_16x16x32_bf16 v[240:243], v[156:159], v[128:131], v[240:243]\n"
    "s_waitcnt vmcnt(8)\n"
    "s_barrier\n"
    "s_add_u32 m0, s35, 98304\n"
    "s_nop 0\n"
    "global_load_lds_dwordx4 v46, s[30:31] offset:0\n"
    "ds_read_b128 v[128:131], v56 offset:0\n"
    "ds_read_b128 v[132:135], v56 offset:2048\n"
    "ds_read_b128 v[136:139], v56 offset:4096\n"
    "global_load_lds_dwordx4 v47, s[30:31] offset:1024\n"
    "s_add_u32 s66, s28, 4\n"
    "s_and_b32 s66, s66, 7\n"
    "s_mul_i32 s66, s66, 638976\n"
    "s_add_u32 s30, s6, s66\n"
    "s_addc_u32 s31, s7, 0\n"
    "ds_read_b128 v[140:143], v56 offset:6144\n"
    "ds_read_b64_tr_b16 v[144:145], v76 offset:0\n"
    "ds_read_b64_tr_b16 v[146:147], v76 offset:1024\n"
    "s_add_u32 m0, s34, 98304\n"
    "s_nop 0\n"
    "global_load_lds_dwordx4 v52, s[36:37] offset:0\n"
    "ds_read_b64_tr_b16 v[148:149], v77 offset:0\n"
    "ds_read_b64_tr_b16 v[150:151], v77 offset:1024\n"
    "ds_read_b64_tr_b16 v[152:153], v78 offset:0\n"
    "global_load_lds_dwordx4 v53, s[36:37] offset:1024\n"
    "ds_read_b64_tr_b16 v[154:155], v78 offset:1024\n"
    "ds_read_b64_tr_b16 v[156:157], v79 offset:0\n"
    "ds_read_b64_tr_b16 v[158:159], v79 offset:1024\n"
    "global_load_lds_dwordx4 v54, s[36:37] offset:2048\n"
    "ds_read_b128 v[160:163], v57 offset:0\n"
    "ds_read_b128 v[164:167], v57 offset:2048\n"
    "ds_read_b128 v[168:171], v57 offset:4096\n"
    "global_load_lds_dwordx4 v55, s[36:37] offset:3072\n"
    "ds_read_b128 v[172:175], v57 offset:6144\n"
    "ds_read_b64_tr_b16 v[176:177], v76 offset:8192\n"
    "ds_read_b64_tr_b16 v[178:179], v76 offset:9216\n"
    "global_load_dwordx2 v[94:95], v42, s[18:19] offset:32\n"
    "ds_read_b64_tr_b16 v[180:181], v77 offset:8192\n"
    "ds_read_b64_tr_b16 v[182:183], v77 offset:9216\n"
    "ds_read_b64_tr_b16 v[184:185], v78 offset:8192\n"
    "ds_read_b64_tr_b16 v[186:187], v78 offset:9216\n"
    "ds_read_b64_tr_b16 v[188:189], v79 offset:8192\n"
    "ds_read_b64_tr_b16 v[190:191], v79 offset:9216\n"
    "s_waitcnt vmcnt(12) lgkmcnt(0)\n"
    "s_barrier\n"
    "v_mfma_f32_16x16x32_bf16 v[192:195], v[144:147], v[128:131], v[192:195]\n"
    "v_lshlrev_b32 v109, 7, v109\n"
    "v_mfma_f32_16x16x32_bf16 v[192:195], v[176:179], v[160:163], v[192:195]\n"
    "v_mfma_f32_16x16x32_bf16 v[196:199], v[176:179], v[164:167], v[196:199]\n"
    "v_add_u32_dpp v52, v108, v48 quad_perm:[0,0,0,0] row_mask:0xf bank_mask:0xf\n"
    "v_mfma_f32_16x16x32_bf16 v[196:199], v[144:147], v[132:135], v[196:199]\n"
    "v_mfma_f32_16x16x32_bf16 v[200:203], v[144:147], v[136:139], v[200:203]\n"
    "v_add_u32_dpp v53, v108, v49 quad_perm:[1,1,1,1] row_mask:0xf bank_mask:0xf\n"
    "v_mfma_f32_16x16x32_bf16 v[200:203], v[176:179], v[168:171], v[200:203]\n"
    "v_mfma_f32_16x16x32_bf16 v[204:207], v[176:179], v[172:175], v[204:207]\n"
    "v_add_u32_dpp v54, v108, v50 quad_perm:[2,2,2,2] row_mask:0xf bank_mask:0xf\n"
    "v_mfma_f32_16x16x32_bf16 v[204:207], v[144:147], v[140:143], v[204:207]\n"
    "v_mfma_f32_16x16x32_bf16 v[220:223], v[148:151], v[140:143], v[220:223]\n"
    "v_add_u32_dpp v55, v108, v51 quad_perm:[3,3,3,3] row_mask:0xf bank_mask:0xf\n"
    "v_mfma_f32_16x16x32_bf16 v[220:223], v[180:183], v[172:175], v[220:223]\n"
    "v_mfma_f32_16x16x32_bf16 v[216:219], v[180:183], v[168:171], v[216:219]\n"
    "v_mad_u32_u24 v16, v18, 36, v20\n"
    "v_mfma_f32_16x16x32_bf16 v[216:219], v[148:151], v[136:139], v[216:219]\n"
    "v_mfma_f32_16x16x32_bf16 v[212:215], v[148:151], v[132:135], v[212:215]\n"
    "s_add_u32 s41, s22, 2\n"
    "v_mfma_f32_16x16x32_bf16 v[212:215], v[180:183], v[164:167], v[212:215]\n"
    "v_mfma_f32_16x16x32_bf16 v[208:211], v[180:183], v[160:163], v[208:211]\n"
    "v_lshrrev_b32 v12, 5, v19\n"
    "v_mfma_f32_16x16x32_bf16 v[208:211], v[148:151], v[128:131], v[208:211]\n"
    "v_mfma_f32_16x16x32_bf16 v[224:227], v[152:155], v[128:131], v[224:227]\n"
    "v_sub_u32 v12, 1, v12\n"
    "v_mfma_f32_16x16x32_bf16 v[224:227], v[184:187], v[160:163], v[224:227]\n"
    "v_mfma_f32_16x16x32_bf16 v[228:231], v[184:187], v[164:167], v[228:231]\n"
    "v_and_b32 v12, 3, v12\n"
    "v_mfma_f32_16x16x32_bf16 v[228:231], v[152:155], v[132:135], v[228:231]\n"
    "v_mfma_f32_16x16x32_bf16 v[232:235], v[152:155], v[136:139], v[232:235]\n"
    "v_add_lshl_u32 v12, v12, s41, 12\n"
    "v_mfma_f32_16x16x32_bf16 v[232:235], v[184:187], v[168:171], v[232:235]\n"
    "v_mfma_f32_16x16x32_bf16 v[236:239], v[184:187], v[172:175], v[236:239]\n"
    "v_and_b32 v12, 0x7000, v12\n"
    "v_mfma_f32_16x16x32_bf16 v[236:239], v[152:155], v[140:143], v[236:239]\n"
    "v_mfma_f32_16x16x32_bf16 v[252:255], v[156:159], v[140:143], v[252:255]\n"
    "v_add3_u32 v15, v12, v15, s43\n"
    "v_mfma_f32_16x16x32_bf16 v[252:255], v[188:191], v[172:175], v[252:255]\n"
    "v_mfma_f32_16x16x32_bf16 v[248:251], v[188:191], v[168:171], v[248:251]\n"
    "v_lshrrev_b32 v12, 5, v20\n"
    "v_mfma_f32_16x16x32_bf16 v[248:251], v[156:159], v[136:139], v[248:251]\n"
    "v_mfma_f32_16x16x32_bf16 v[244:247], v[156:159], v[132:135], v[244:247]\n"
    "v_sub_u32 v12, 1, v12\n"
    "v_mfma_f32_16x16x32_bf16 v[244:247], v[188:191], v[164:167], v[244:247]\n"
    "v_mfma_f32_16x16x32_bf16 v[240:243], v[188:191], v[160:163], v[240:243]\n"
    "v_mfma_f32_16x16x32_bf16 v[240:243], v[156:159], v[128:131], v[240:243]\n"
    "s_waitcnt vmcnt(8)\n"
    "s_barrier\n"
    "s_add_u32 m0, s35, 0\n"
    "s_nop 0\n"
    "global_load_lds_dwordx4 v46, s[30:31] offset:0\n"
    "ds_read_b128 v[128:131], v56 offset:49152\n"
    "ds_read_b128 v[132:135], v56 offset:51200\n"
    "ds_read_b128 v[136:139], v56 offset:53248\n"
    "global_load_lds_dwordx4 v47, s[30:31] offset:1024\n"
    "s_add_u32 s30, s30, 212992\n"
    "s_addc_u32 s31, s31, 0\n"
    "ds_read_b128 v[140:143], v56 offset:55296\n"
    "ds_read_b64_tr_b16 v[144:145], v76 offset:49152\n"
    "ds_read_b64_tr_b16 v[146:147], v76 offset:50176\n"
    "s_add_u32 m0, s34, 0\n"
    "s_nop 0\n"
    "global_load_lds_dwordx4 v52, s[36:37] offset:0\n"
    "ds_read_b64_tr_b16 v[148:149], v77 offset:49152\n"
    "ds_read_b64_tr_b16 v[150:151], v77 offset:50176\n"
    "ds_read_b64_tr_b16 v[152:153], v78 offset:49152\n"
    "global_load_lds_dwordx4 v53, s[36:37] offset:1024\n"
    "ds_read_b64_tr_b16 v[154:155], v78 offset:50176\n"
    "ds_read_b64_tr_b16 v[156:157], v79 offset:49152\n"
    "ds_read_b64_tr_b16 v[158:159], v79 offset:50176\n"
    "global_load_lds_dwordx4 v54, s[36:37] offset:2048\n"
    "ds_read_b128 v[160:163], v57 offset:49152\n"
    "ds_read_b128 v[164:167], v57 offset:51200\n"
    "ds_read_b128 v[168:171], v57 offset:53248\n"
    "global_load_lds_dwordx4 v55, s[36:37] offset:3072\n"
    "ds_read_b128 v[172:175], v57 offset:55296\n"
    "ds_read_b64_tr_b16 v[176:177], v76 offset:57344\n"
    "ds_read_b64_tr_b16 v[178:179], v76 offset:58368\n"
    "global_load_dwordx2 v[122:123], v42, s[18:19] offset:64\n"
    "ds_read_b64_tr_b16 v[180:181], v77 offset:57344\n"
    "ds_read_b64_tr_b16 v[182:183], v77 offset:58368\n"
    "ds_read_b64_tr_b16 v[184:185], v78 offset:57344\n"
    "ds_read_b64_tr_b16 v[186:187], v78 offset:58368\n"
    "ds_read_b64_tr_b16 v[188:189], v79 offset:57344\n"
    "ds_read_b64_tr_b16 v[190:191], v79 offset:58368\n"
    "s_waitcnt vmcnt(12) lgkmcnt(0)\n"
    "s_barrier\n"
    "v_mfma_f32_16x16x32_bf16 v[192:195], v[144:147], v[128:131], v[192:195]\n"
    "v_lshlrev_b32 v110, 7, v110\n"
    "v_mfma_f32_16x16x32_bf16 v[192:195], v[176:179], v[160:163], v[192:195]\n"
    "v_add_u32_dpp v52, v109, v48 quad_perm:[0,0,0,0] row_mask:0xf bank_mask:0xf\n"
    "v_mfma_f32_16x16x32_bf16 v[196:199], v[176:179], v[164:167], v[196:199]\n"
    "v_mfma_f32_16x16x32_bf16 v[196:199], v[144:147], v[132:135], v[196:199]\n"
    "v_add_u32_dpp v53, v109, v49 quad_perm:[1,1,1,1] row_mask:0xf bank_mask:0xf\n"
    "v_mfma_f32_16x16x32_bf16 v[200:203], v[144:147], v[136:139], v[200:203]\n"
    "v_mfma_f32_16x16x32_bf16 v[200:203], v[176:179], v[168:171], v[200:203]\n"
    "v_add_u32_dpp v54, v109, v50 quad_perm:[2,2,2,2] row_mask:0xf bank_mask:0xf\n"
    "v_mfma_f32_16x16x32_bf16 v[204:207], v[176:179], v[172:175], v[204:207]\n"
    "v_mfma_f32_16x16x32_bf16 v[204:207], v[144:147], v[140:143], v[204:207]\n"
    "v_add_u32_dpp v55, v109, v51 quad_perm:[3,3,3,3] row_mask:0xf bank_mask:0xf\n"
    "v_mfma_f32_16x16x32_bf16 v[220:223], v[148:151], v[140:143], v[220:223]\n"
    "v_mfma_f32_16x16x32_bf16 v[220:223], v[180:183], v[172:175], v[220:223]\n"
    "v_and_b32 v12, 3, v12\n"
    "v_mfma_f32_16x16x32_bf16 v[216:219], v[180:183], v[168:171], v[216:219]\n"
    "v_mfma_f32_16x16x32_bf16 v[216:219], v[148:151], v[136:139], v[216:219]\n"
    "v_add_lshl_u32 v12, v12, s41, 12\n"
    "v_mfma_f32_16x16x32_bf16 v[212:215], v[148:151], v[132:135], v[212:215]\n"
    "v_mfma_f32_16x16x32_bf16 v[212:215], v[180:183], v[164:167], v[212:215]\n"
    "v_and_b32 v12, 0x7000, v12\n"
    "v_mfma_f32_16x16x32_bf16 v[208:211], v[180:183], v[160:163], v[208:211]\n"
    "v_mfma_f32_16x16x32_bf16 v[208:211], v[148:151], v[128:131], v[208:211]\n"
    "v_add3_u32 v16, v12, v16, s43\n"
    "v_mfma_f32_16x16x32_bf16 v[224:227], v[152:155], v[128:131], v[224:227]\n"
    "v_mfma_f32_16x16x32_bf16 v[224:227], v[184:187], v[160:163], v[224:227]\n"
    "v_add_u32 v16, 3456, v16\n"
    "v_mfma_f32_16x16x32_bf16 v[228:231], v[184:187], v[164:167], v[228:231]\n"
    "v_mfma_f32_16x16x32_bf16 v[228:231], v[152:155], v[132:135], v[228:231]\n"
    "s_mul_i32 s41, s47, 13\n"
    "v_mfma_f32_16x16x32_bf16 v[232:235], v[152:155], v[136:139], v[232:235]\n"
    "v_mfma_f32_16x16x32_bf16 v[232:235], v[184:187], v[168:171], v[232:235]\n"
    "s_sub_u32 s41, s3, s41\n"
    "v_mfma_f32_16x16x32_bf16 v[236:239], v[184:187], v[172:175], v[236:239]\n"
    "v_mfma_f32_16x16x32_bf16 v[236:239], v[152:155], v[140:143], v[236:239]\n"
    "s_lshl1_add_u32 s41, s41, s21\n"
    "v_mfma_f32_16x16x32_bf16 v[252:255], v[156:159], v[140:143], v[252:255]\n"
    "v_mfma_f32_16x16x32_bf16 v[252:255], v[188:191], v[172:175], v[252:255]\n"
    "s_mul_i32 s41, s41, 3\n"
    "v_mfma_f32_16x16x32_bf16 v[248:251], v[188:191], v[168:171], v[248:251]\n"
    "v_mfma_f32_16x16x32_bf16 v[248:251], v[156:159], v[136:139], v[248:251]\n"
    "s_add_u32 s41, s41, s22\n"
    "v_mfma_f32_16x16x32_bf16 v[244:247], v[156:159], v[132:135], v[244:247]\n"
    "v_mfma_f32_16x16x32_bf16 v[244:247], v[188:191], v[164:167], v[244:247]\n"
    "v_mov_b32 v24, s41\n"
    "v_mfma_f32_16x16x32_bf16 v[240:243], v[188:191], v[160:163], v[240:243]\n"
    "v_mfma_f32_16x16x32_bf16 v[240:243], v[156:159], v[128:131], v[240:243]\n"
    "s_waitcnt vmcnt(8)\n"
    "s_barrier\n"
    "s_add_u32 m0, s35, 49152\n"
    "s_nop 0\n"
    "global_load_lds_dwordx4 v46, s[30:31] offset:0\n"
    "ds_read_b128 v[128:131], v58 offset:0\n"
    "ds_read_b128 v[132:135], v58 offset:2048\n"
    "ds_read_b128 v[136:139], v58 offset:4096\n"
    "global_load_lds_dwordx4 v47, s[30:31] offset:1024\n"
    "s_add_u32 s30, s30, 212992\n"
    "s_addc_u32 s31, s31, 0\n"
    "ds_read_b128 v[140:143], v58 offset:6144\n"
    "ds_read_b64_tr_b16 v[144:145], v80 offset:0\n"
    "ds_read_b64_tr_b16 v[146:147], v80 offset:1024\n"
    "s_add_u32 m0, s34, 49152\n"
    "s_nop 0\n"
    "global_load_lds_dwordx4 v52, s[36:37] offset:0\n"
    "ds_read_b64_tr_b16 v[148:149], v81 offset:0\n"
    "ds_read_b64_tr_b16 v[150:151], v81 offset:1024\n"
    "ds_read_b64_tr_b16 v[152:153], v82 offset:0\n"
    "global_load_lds_dwordx4 v53, s[36:37] offset:1024\n"
    "ds_read_b64_tr_b16 v[154:155], v82 offset:1024\n"
    "ds_read_b64_tr_b16 v[156:157], v83 offset:0\n"
    "ds_read_b64_tr_b16 v[158:159], v83 offset:1024\n"
    "global_load_lds_dwordx4 v54, s[36:37] offset:2048\n"
    "ds_read_b128 v[160:163], v59 offset:0\n"
    "ds_read_b128 v[164:167], v59 offset:2048\n"
    "ds_read_b128 v[168:171], v59 offset:4096\n"
    "global_load_lds_dwordx4 v55, s[36:37] offset:3072\n"
    "ds_read_b128 v[172:175], v59 offset:6144\n"
    "ds_read_b64_tr_b16 v[176:177], v80 offset:8192\n"
    "ds_read_b64_tr_b16 v[178:179], v80 offset:9216\n"
    "global_load_dwordx2 v[124:125], v42, s[18:19] offset:96\n"
    "ds_read_b64_tr_b16 v[180:181], v81 offset:8192\n"
    "ds_read_b64_tr_b16 v[182:183], v81 offset:9216\n"
    "ds_read_b64_tr_b16 v[184:185], v82 offset:8192\n"
    "ds_read_b64_tr_b16 v[186:187], v82 offset:9216\n"
    "ds_read_b64_tr_b16 v[188:189], v83 offset:8192\n"
    "ds_read_b64_tr_b16 v[190:191], v83 offset:9216\n"
    "s_waitcnt vmcnt(12) lgkmcnt(0)\n"
    "s_barrier\n"
    "v_mfma_f32_16x16x32_bf16 v[192:195], v[144:147], v[128:131], v[192:195]\n"
    "v_lshlrev_b32 v111, 7, v111\n"
    "v_mfma_f32_16x16x32_bf16 v[192:195], v[176:179], v[160:163], v[192:195]\n"
    "v_mfma_f32_16x16x32_bf16 v[196:199], v[176:179], v[164:167], v[196:199]\n"
    "v_add_u32_dpp v52, v110, v48 quad_perm:[0,0,0,0] row_mask:0xf bank_mask:0xf\n"
    "v_mfma_f32_16x16x32_bf16 v[196:199], v[144:147], v[132:135], v[196:199]\n"
    "v_mfma_f32_16x16x32_bf16 v[200:203], v[144:147], v[136:139], v[200:203]\n"
    "v_add_u32_dpp v53, v110, v49 quad_perm:[1,1,1,1] row_mask:0xf bank_mask:0xf\n"
    "v_mfma_f32_16x16x32_bf16 v[200:203], v[176:179], v[168:171], v[200:203]\n"
    "v_mfma_f32_16x16x32_bf16 v[204:207], v[176:179], v[172:175], v[204:207]\n"
    "v_add_u32_dpp v54, v110, v50 quad_perm:[2,2,2,2] row_mask:0xf bank_mask:0xf\n"
    "v_mfma_f32_16x16x32_bf16 v[204:207], v[144:147], v[140:143], v[204:207]\n"
    "v_mfma_f32_16x16x32_bf16 v[220:223], v[148:151], v[140:143], v[220:223]\n"
    "v_add_u32_dpp v55, v110, v51 quad_perm:[3,3,3,3] row_mask:0xf bank_mask:0xf\n"
    "v_mfma_f32_16x16x32_bf16 v[220:223], v[180:183], v[172:175], v[220:223]\n"
    "v_mfma_f32_16x16x32_bf16 v[216:219], v[180:183], v[168:171], v[216:219]\n"
    "v_mfma_f32_16x16x32_bf16 v[216:219], v[148:151], v[136:139], v[216:219]\n"
    "v_readfirstlane_b32 s40, v24\n"
    "v_mfma_f32_16x16x32_bf16 v[212:215], v[148:151], v[132:135], v[212:215]\n"
    "v_mfma_f32_16x16x32_bf16 v[212:215], v[180:183], v[164:167], v[212:215]\n"
    "s_or_b32 s40, s40, s44\n"
    "v_mfma_f32_16x16x32_bf16 v[208:211], v[180:183], v[160:163], v[208:211]\n"
    "v_mfma_f32_16x16x32_bf16 v[208:211], v[148:151], v[128:131], v[208:211]\n"
    "s_lshl1_add_u32 s41, s40, s54\n"
    "v_mfma_f32_16x16x32_bf16 v[224:227], v[152:155], v[128:131], v[224:227]\n"
    "v_mfma_f32_16x16x32_bf16 v[224:227], v[184:187], v[160:163], v[224:227]\n"
    "s_cmpk_lt_u32 s40, 256\n"
    "v_mfma_f32_16x16x32_bf16 v[228:231], v[184:187], v[164:167], v[228:231]\n"
    "v_mfma_f32_16x16x32_bf16 v[228:231], v[152:155], v[132:135], v[228:231]\n"
    "s_cselect_b32 s41, s41, 2\n"
    "v_mfma_f32_16x16x32_bf16 v[232:235], v[152:155], v[136:139], v[232:235]\n"
    "v_mfma_f32_16x16x32_bf16 v[232:235], v[184:187], v[168:171], v[232:235]\n"
    "v_mfma_f32_16x16x32_bf16 v[236:239], v[184:187], v[172:175], v[236:239]\n"
    "v_mov_b32 v24, s41\n"
    "v_mfma_f32_16x16x32_bf16 v[236:239], v[152:155], v[140:143], v[236:239]\n"
    "v_mfma_f32_16x16x32_bf16 v[252:255], v[156:159], v[140:143], v[252:255]\n"
    "s_mul_i32 s41, s41, 52\n"
    "v_mfma_f32_16x16x32_bf16 v[252:255], v[188:191], v[172:175], v[252:255]\n"
    "v_mfma_f32_16x16x32_bf16 v[248:251], v[188:191], v[168:171], v[248:251]\n"
    "s_add_u32 s38, s4, s41\n"
    "v_mfma_f32_16x16x32_bf16 v[248:251], v[156:159], v[136:139], v[248:251]\n"
    "v_mfma_f32_16x16x32_bf16 v[244:247], v[156:159], v[132:135], v[244:247]\n"
    "s_addc_u32 s39, s5, 0\n"
    "v_mfma_f32_16x16x32_bf16 v[244:247], v[188:191], v[164:167], v[244:247]\n"
    "v_mfma_f32_16x16x32_bf16 v[240:243], v[188:191], v[160:163], v[240:243]\n"
    "v_mfma_f32_16x16x32_bf16 v[240:243], v[156:159], v[128:131], v[240:243]\n"
    "s_waitcnt vmcnt(8)\n"
    "s_barrier\n"
    "s_add_u32 m0, s35, 98304\n"
    "s_nop 0\n"
    "global_load_lds_dwordx4 v46, s[30:31] offset:0\n"
    "ds_read_b128 v[128:131], v56 offset:0\n"
    "ds_read_b128 v[132:135], v56 offset:2048\n"
    "ds_read_b128 v[136:139], v56 offset:4096\n"
    "global_load_lds_dwordx4 v47, s[30:31] offset:1024\n"
    "s_add_u32 s66, s28, 5\n"
    "s_and_b32 s66, s66, 7\n"
    "s_mul_i32 s66, s66, 638976\n"
    "s_add_u32 s30, s6, s66\n"
    "s_addc_u32 s31, s7, 0\n"
    "ds_read_b128 v[140:143], v56 offset:6144\n"
    "ds_read_b64_tr_b16 v[144:145], v76 offset:0\n"
    "ds_read_b64_tr_b16 v[146:147], v76 offset:1024\n"
    "s_add_u32 m0, s34, 98304\n"
    "s_nop 0\n"
    "global_load_lds_dwordx4 v52, s[36:37] offset:0\n"
    "ds_read_b64_tr_b16 v[148:149], v77 offset:0\n"
    "ds_read_b64_tr_b16 v[150:151], v77 offset:1024\n"
    "ds_read_b64_tr_b16 v[152:153], v78 offset:0\n"
    "global_load_lds_dwordx4 v53, s[36:37] offset:1024\n"
    "ds_read_b64_tr_b16 v[154:155], v78 offset:1024\n"
    "ds_read_b64_tr_b16 v[156:157], v79 offset:0\n"
    "ds_read_b64_tr_b16 v[158:159], v79 offset:1024\n"
    "global_load_lds_dwordx4 v54, s[36:37] offset:2048\n"
    "ds_read_b128 v[160:163], v57 offset:0\n"
    "ds_read_b128 v[164:167], v57 offset:2048\n"
    "ds_read_b128 v[168:171], v57 offset:4096\n"
    "global_load_lds_dwordx4 v55, s[36:37] offset:3072\n"
    "ds_read_b128 v[172:175], v57 offset:6144\n"
    "ds_read_b64_tr_b16 v[176:177], v76 offset:8192\n"
    "ds_read_b64_tr_b16 v[178:179], v76 offset:9216\n"
    "global_load_dwordx3 v[36:38], v17, s[38:39] offset:-104\n"
    "ds_read_b64_tr_b16 v[180:181], v77 offset:8192\n"
    "ds_read_b64_tr_b16 v[182:183], v77 offset:9216\n"
    "ds_read_b64_tr_b16 v[184:185], v78 offset:8192\n"
    "ds_read_b64_tr_b16 v[186:187], v78 offset:9216\n"
    "ds_read_b64_tr_b16 v[188:189], v79 offset:8192\n"
    "ds_read_b64_tr_b16 v[190:191], v79 offset:9216\n"
    "s_waitcnt vmcnt(12) lgkmcnt(0)\n"
    "s_barrier\n"
    "v_mfma_f32_16x16x32_bf16 v[192:195], v[144:147], v[128:131], v[192:195]\n"
    "v_lshlrev_b32 v112, 7, v112\n"
    "v_mfma_f32_16x16x32_bf16 v[192:195], v[176:179], v[160:163], v[192:195]\n"
    "v_add_u32_dpp v52, v111, v48 quad_perm:[0,0,0,0] row_mask:0xf bank_mask:0xf\n"
    "v_mfma_f32_16x16x32_bf16 v[196:199], v[176:179], v[164:167], v[196:199]\n"
    "v_add_u32_dpp v53, v111, v49 quad_perm:[1,1,1,1] row_mask:0xf bank_mask:0xf\n"
    "v_mfma_f32_16x16x32_bf16 v[196:199], v[144:147], v[132:135], v[196:199]\n"
    "v_add_u32_dpp v54, v111, v50 quad_perm:[2,2,2,2] row_mask:0xf bank_mask:0xf\n"
    "v_mfma_f32_16x16x32_bf16 v[200:203], v[144:147], v[136:139], v[200:203]\n"
    "v_add_u32_dpp v55, v111, v51 quad_perm:[3,3,3,3] row_mask:0xf bank_mask:0xf\n"
    "v_mfma_f32_16x16x32_bf16 v[200:203], v[176:179], v[168:171], v[200:203]\n"
    "v_lshlrev_b32 v60, 16, v84\n"
    "v_mfma_f32_16x16x32_bf16 v[204:207], v[176:179], v[172:175], v[204:207]\n"
    "v_and_b32 v61, 0xffff0000, v84\n"
    "v_mfma_f32_16x16x32_bf16 v[204:207], v[144:147], v[140:143], v[204:207]\n"
    "v_mfma_f32_16x16x32_bf16 v[220:223], v[148:151], v[140:143], v[220:223]\n"
    "v_lshlrev_b32 v62, 16, v85\n"
    "v_mfma_f32_16x16x32_bf16 v[220:223], v[180:183], v[172:175], v[220:223]\n"
    "v_and_b32 v63, 0xffff0000, v85\n"
    "v_mfma_f32_16x16x32_bf16 v[216:219], v[180:183], v[168:171], v[216:219]\n"
    "v_lshlrev_b32 v64, 16, v86\n"
    "v_mfma_f32_16x16x32_bf16 v[216:219], v[148:151], v[136:139], v[216:219]\n"
    "v_and_b32 v65, 0xffff0000, v86\n"
    "v_mfma_f32_16x16x32_bf16 v[212:215], v[148:151], v[132:135], v[212:215]\n"
    "v_lshlrev_b32 v66, 16, v87\n"
    "v_mfma_f32_16x16x32_bf16 v[212:215], v[180:183], v[164:167], v[212:215]\n"
    "v_and_b32 v67, 0xffff0000, v87\n"
    "v_mfma_f32_16x16x32_bf16 v[208:211], v[180:183], v[160:163], v[208:211]\n"
    "v_lshlrev_b32 v68, 16, v88\n"
    "v_mfma_f32_16x16x32_bf16 v[208:211], v[148:151], v[128:131], v[208:211]\n"
    "v_mfma_f32_16x16x32_bf16 v[224:227], v[152:155], v[128:131], v[224:227]\n"
    "v_and_b32 v69, 0xffff0000, v88\n"
    "v_mfma_f32_16x16x32_bf16 v[224:227], v[184:187], v[160:163], v[224:227]\n"
    "v_lshlrev_b32 v70, 16, v89\n"
    "v_mfma_f32_16x16x32_bf16 v[228:231], v[184:187], v[164:167], v[228:231]\n"
    "v_and_b32 v71, 0xffff0000, v89\n"
    "v_mfma_f32_16x16x32_bf16 v[228:231], v[152:155], v[132:135], v[228:231]\n"
    "v_lshlrev_b32 v72, 16, v90\n"
    "v_mfma_f32_16x16x32_bf16 v[232:235], v[152:155], v[136:139], v[232:235]\n"
    "v_and_b32 v73, 0xffff0000, v90\n"
    "v_mfma_f32_16x16x32_bf16 v[232:235], v[184:187], v[168:171], v[232:235]\n"
    "v_lshlrev_b32 v74, 16, v91\n"
    "v_mfma_f32_16x16x32_bf16 v[236:239], v[184:187], v[172:175], v[236:239]\n"
    "v_and_b32 v75, 0xffff0000, v91\n"
    "v_mfma_f32_16x16x32_bf16 v[236:239], v[152:155], v[140:143], v[236:239]\n"
    "v_mfma_f32_16x16x32_bf16 v[252:255], v[156:159], v[140:143], v[252:255]\n"
    "s_lshl3_add_u32 s40, s2, 4096\n"
    "v_mfma_f32_16x16x32_bf16 v[252:255], v[188:191], v[172:175], v[252:255]\n"
    "s_lshl_b32 s41, s2, 2\n"
    "v_mfma_f32_16x16x32_bf16 v[248:251], v[188:191], v[168:171], v[248:251]\n"
    "v_lshlrev_b32 v11, 3, v1\n"
    "v_mfma_f32_16x16x32_bf16 v[248:251], v[156:159], v[136:139], v[248:251]\n"
    "v_add_u32 v11, 40960, v11\n"
    "v_mfma_f32_16x16x32_bf16 v[244:247], v[156:159], v[132:135], v[244:247]\n"
    "v_writelane_b32 v11, s40, 8\n"
    "v_mfma_f32_16x16x32_bf16 v[244:247], v[188:191], v[164:167], v[244:247]\n"
    "v_writelane_b32 v11, s41, 9\n"
    "v_mfma_f32_16x16x32_bf16 v[240:243], v[188:191], v[160:163], v[240:243]\n"
    "v_mfma_f32_16x16x32_bf16 v[240:243], v[156:159], v[128:131], v[240:243]\n"
    "s_waitcnt vmcnt(8)\n"
    "s_barrier\n"
    "s_add_u32 m0, s35, 0\n"
    "s_nop 0\n"
    "global_load_lds_dwordx4 v46, s[30:31] offset:0\n"
    "ds_read_b128 v[128:131], v56 offset:49152\n"
    "ds_read_b128 v[132:135], v56 offset:51200\n"
    "ds_read_b128 v[136:139], v56 offset:53248\n"
    "global_load_lds_dwordx4 v47, s[30:31] offset:1024\n"
    "s_add_u32 s30, s30, 212992\n"
    "s_addc_u32 s31, s31, 0\n"
    "ds_read_b128 v[140:143], v56 offset:55296\n"
    "ds_read_b64_tr_b16 v[144:145], v76 offset:49152\n"
    "ds_read_b64_tr_b16 v[146:147], v76 offset:50176\n"
    "s_add_u32 m0, s34, 0\n"
    "s_nop 0\n"
    "global_load_lds_dwordx4 v52, s[36:37] offset:0\n"
    "ds_read_b64_tr_b16 v[148:149], v77 offset:49152\n"
    "ds_read_b64_tr_b16 v[150:151], v77 offset:50176\n"
    "ds_read_b64_tr_b16 v[152:153], v78 offset:49152\n"
    "global_load_lds_dwordx4 v53, s[36:37] offset:1024\n"
    "ds_read_b64_tr_b16 v[154:155], v78 offset:50176\n"
    "ds_read_b64_tr_b16 v[156:157], v79 offset:49152\n"
    "ds_read_b64_tr_b16 v[158:159], v79 offset:50176\n"
    "global_load_lds_dwordx4 v54, s[36:37] offset:2048\n"
    "ds_read_b128 v[160:163], v57 offset:49152\n"
    "ds_read_b128 v[164:167], v57 offset:51200\n"
    "ds_read_b128 v[168:171], v57 offset:53248\n"
    "global_load_lds_dwordx4 v55, s[36:37] offset:3072\n"
    "ds_read_b128 v[172:175], v57 offset:55296\n"
    "ds_read_b64_tr_b16 v[176:177], v76 offset:57344\n"
    "ds_read_b64_tr_b16 v[178:179], v76 offset:58368\n"
    "global_load_dword v41, v18, s[38:39] offset:-8\n"
    "ds_read_b64_tr_b16 v[180:181], v77 offset:57344\n"
    "ds_read_b64_tr_b16 v[182:183], v77 offset:58368\n"
    "ds_read_b64_tr_b16 v[184:185], v78 offset:57344\n"
    "ds_read_b64_tr_b16 v[186:187], v78 offset:58368\n"
    "ds_read_b64_tr_b16 v[188:189], v79 offset:57344\n"
    "ds_read_b64_tr_b16 v[190:191], v79 offset:58368\n"
    "s_waitcnt vmcnt(12) lgkmcnt(0)\n"
    "s_barrier\n"
    "v_mfma_f32_16x16x32_bf16 v[192:195], v[144:147], v[128:131], v[192:195]\n"
    "v_mfma_f32_16x16x32_bf16 v[192:195], v[176:179], v[160:163], v[192:195]\n"
    "v_lshlrev_b32 v113, 7, v113\n"
    "v_mfma_f32_16x16x32_bf16 v[196:199], v[176:179], v[164:167], v[196:199]\n"
    "v_mfma_f32_16x16x32_bf16 v[196:199], v[144:147], v[132:135], v[196:199]\n"
    "v_add_u32_dpp v52, v112, v48 quad_perm:[0,0,0,0] row_mask:0xf bank_mask:0xf\n"
    "v_mfma_f32_16x16x32_bf16 v[200:203], v[144:147], v[136:139], v[200:203]\n"
    "v_mfma_f32_16x16x32_bf16 v[200:203], v[176:179], v[168:171], v[200:203]\n"
    "v_add_u32_dpp v53, v112, v49 quad_perm:[1,1,1,1] row_mask:0xf bank_mask:0xf\n"
    "v_mfma_f32_16x16x32_bf16 v[204:207], v[176:179], v[172:175], v[204:207]\n"
    "v_mfma_f32_16x16x32_bf16 v[204:207], v[144:147], v[140:143], v[204:207]\n"
    "v_add_u32_dpp v54, v112, v50 quad_perm:[2,2,2,2] row_mask:0xf bank_mask:0xf\n"
    "v_mfma_f32_16x16x32_bf16 v[220:223], v[148:151], v[140:143], v[220:223]\n"
    "v_mfma_f32_16x16x32_bf16 v[220:223], v[180:183], v[172:175], v[220:223]\n"
    "v_add_u32_dpp v55, v112, v51 quad_perm:[3,3,3,3] row_mask:0xf bank_mask:0xf\n"
    "v_mfma_f32_16x16x32_bf16 v[216:219], v[180:183], v[168:171], v[216:219]\n"
    "v_mfma_f32_16x16x32_bf16 v[216:219], v[148:151], v[136:139], v[216:219]\n"
    "v_mfma_f32_16x16x32_bf16 v[212:215], v[148:151], v[132:135], v[212:215]\n"
    "v_mfma_f32_16x16x32_bf16 v[212:215], v[180:183], v[164:167], v[212:215]\n"
    "v_mfma_f32_16x16x32_bf16 v[208:211], v[180:183], v[160:163], v[208:211]\n"
    "v_add_f32 v192, v192, v60\n"
    "v_mfma_f32_16x16x32_bf16 v[208:211], v[148:151], v[128:131], v[208:211]\n"
    "v_add_f32 v193, v193, v61\n"
    "v_mfma_f32_16x16x32_bf16 v[224:227], v[152:155], v[128:131], v[224:227]\n"
    "v_add_f32 v194, v194, v62\n"
    "v_mfma_f32_16x16x32_bf16 v[224:227], v[184:187], v[160:163], v[224:227]\n"
    "v_add_f32 v195, v195, v63\n"
    "v_mfma_f32_16x16x32_bf16 v[228:231], v[184:187], v[164:167], v[228:231]\n"
    "v_add_f32 v196, v196, v60\n"
    "v_mfma_f32_16x16x32_bf16 v[228:231], v[152:155], v[132:135], v[228:231]\n"
    "v_add_f32 v197, v197, v61\n"
    "v_mfma_f32_16x16x32_bf16 v[232:235], v[152:155], v[136:139], v[232:235]\n"
    "v_add_f32 v198, v198, v62\n"
    "v_mfma_f32_16x16x32_bf16 v[232:235], v[184:187], v[168:171], v[232:235]\n"
    "v_add_f32 v199, v199, v63\n"
    "v_mfma_f32_16x16x32_bf16 v[236:239], v[184:187], v[172:175], v[236:239]\n"
    "v_add_f32 v200, v200, v60\n"
    "v_mfma_f32_16x16x32_bf16 v[236:239], v[152:155], v[140:143], v[236:239]\n"
    "v_add_f32 v201, v201, v61\n"
    "v_mfma_f32_16x16x32_bf16 v[252:255], v[156:159], v[140:143], v[252:255]\n"
    "v_add_f32 v202, v202, v62\n"
    "v_mfma_f32_16x16x32_bf16 v[252:255], v[188:191], v[172:175], v[252:255]\n"
    "v_add_f32 v203, v203, v63\n"
    "v_mfma_f32_16x16x32_bf16 v[248:251], v[188:191], v[168:171], v[248:251]\n"
    "v_add_f32 v204, v204, v60\n"
    "v_mfma_f32_16x16x32_bf16 v[248:251], v[156:159], v[136:139], v[248:251]\n"
    "v_add_f32 v205, v205, v61\n"
    "v_mfma_f32_16x16x32_bf16 v[244:247], v[156:159], v[132:135], v[244:247]\n"
    "v_add_f32 v206, v206, v62\n"
    "v_mfma_f32_16x16x32_bf16 v[244:247], v[188:191], v[164:167], v[244:247]\n"
    "v_add_f32 v207, v207, v63\n"
    "v_mfma_f32_16x16x32_bf16 v[240:243], v[188:191], v[160:163], v[240:243]\n"
    "v_mfma_f32_16x16x32_bf16 v[240:243], v[156:159], v[128:131], v[240:243]\n"
    "s_waitcnt vmcnt(8)\n"
    "s_barrier\n"
    "s_cmp_eq_u32 s20, 3\n"
    "s_cbranch_scc1 .Lxd_on1\n"
    ".Lxd_on1r:\n"
    "s_add_u32 m0, s35, 49152\n"
    "s_nop 0\n"
    "global_load_lds_dwordx4 v46, s[30:31] offset:0\n"
    "ds_read_b128 v[128:131], v58 offset:0\n"
    "ds_read_b128 v[132:135], v58 offset:2048\n"
    "ds_read_b128 v[136:139], v58 offset:4096\n"
    "ds_read_b128 v[140:143], v58 offset:6144\n"
    "global_load_lds_dwordx4 v47, s[30:31] offset:1024\n"
    "s_add_u32 s30, s30, 212992\n"
    "s_addc_u32 s31, s31, 0\n"
    "ds_read_b64_tr_b16 v[144:145], v80 offset:0\n"
    "ds_read_b64_tr_b16 v[146:147], v80 offset:1024\n"
    "ds_read_b64_tr_b16 v[148:149], v81 offset:0\n"
    "ds_read_b64_tr_b16 v[150:151], v81 offset:1024\n"
    "s_add_u32 m0, s34, 49152\n"
    "s_nop 0\n"
    "global_load_lds_dwordx4 v52, s[36:37] offset:0\n"
    "ds_read_b64_tr_b16 v[152:153], v82 offset:0\n"
    "ds_read_b64_tr_b16 v[154:155], v82 offset:1024\n"
    "ds_read_b64_tr_b16 v[156:157], v83 offset:0\n"
    "ds_read_b64_tr_b16 v[158:159], v83 offset:1024\n"
    "global_load_lds_dwordx4 v53, s[36:37] offset:1024\n"
    "ds_read_b128 v[160:163], v59 offset:0\n"
    "ds_read_b128 v[164:167], v59 offset:2048\n"
    "ds_read_b128 v[168:171], v59 offset:4096\n"
    "ds_read_b128 v[172:175], v59 offset:6144\n"
    "global_load_lds_dwordx4 v54, s[36:37] offset:2048\n"
    "ds_read_b64_tr_b16 v[176:177], v80 offset:8192\n"
    "ds_read_b64_tr_b16 v[178:179], v80 offset:9216\n"
    "ds_read_b64_tr_b16 v[180:181], v81 offset:8192\n"
    "ds_read_b64_tr_b16 v[182:183], v81 offset:9216\n"
    "global_load_lds_dwordx4 v55, s[36:37] offset:3072\n"
    "ds_read_b64_tr_b16 v[184:185], v82 offset:8192\n"
    "ds_read_b64_tr_b16 v[186:187], v82 offset:9216\n"
    "ds_read_b64_tr_b16 v[188:189], v83 offset:8192\n"
    "ds_read_b64_tr_b16 v[190:191], v83 offset:9216\n"
    "s_waitcnt vmcnt(11) lgkmcnt(0)\n"
    "s_barrier\n"
    "v_mfma_f32_16x16x32_bf16 v[192:195], v[144:147], v[128:131], v[192:195]\n"
    "v_mfma_f32_16x16x32_bf16 v[192:195], v[176:179], v[160:163], v[192:195]\n"
    "v_lshlrev_b32 v114, 7, v114\n"
    "v_mfma_f32_16x16x32_bf16 v[196:199], v[176:179], v[164:167], v[196:199]\n"
    "v_mfma_f32_16x16x32_bf16 v[196:199], v[144:147], v[132:135], v[196:199]\n"
    "v_add_u32_dpp v52, v113, v48 quad_perm:[0,0,0,0] row_mask:0xf bank_mask:0xf\n"
    "v_mfma_f32_16x16x32_bf16 v[200:203], v[144:147], v[136:139], v[200:203]\n"
    "v_mfma_f32_16x16x32_bf16 v[200:203], v[176:179], v[168:171], v[200:203]\n"
    "v_add_u32_dpp v53, v113, v49 quad_perm:[1,1,1,1] row_mask:0xf bank_mask:0xf\n"
    "v_mfma_f32_16x16x32_bf16 v[204:207], v[176:179], v[172:175], v[204:207]\n"
    "v_mfma_f32_16x16x32_bf16 v[204:207], v[144:147], v[140:143], v[204:207]\n"
    "v_add_u32_dpp v54, v113, v50 quad_perm:[2,2,2,2] row_mask:0xf bank_mask:0xf\n"
    "v_mfma_f32_16x16x32_bf16 v[220:223], v[148:151], v[140:143], v[220:223]\n"
    "v_mfma_f32_16x16x32_bf16 v[220:223], v[180:183], v[172:175], v[220:223]\n"
    "v_add_u32_dpp v55, v113, v51 quad_perm:[3,3,3,3] row_mask:0xf bank_mask:0xf\n"
    "v_mfma_f32_16x16x32_bf16 v[216:219], v[180:183], v[168:171], v[216:219]\n"
    "v_mfma_f32_16x16x32_bf16 v[216:219], v[148:151], v[136:139], v[216:219]\n"
    "v_mfma_f32_16x16x32_bf16 v[212:215], v[148:151], v[132:135], v[212:215]\n"
    "v_mfma_f32_16x16x32_bf16 v[212:215], v[180:183], v[164:167], v[212:215]\n"
    "v_mfma_f32_16x16x32_bf16 v[208:211], v[180:183], v[160:163], v[208:211]\n"
    "v_mfma_f32_16x16x32_bf16 v[208:211], v[148:151], v[128:131], v[208:211]\n"
    "v_mfma_f32_16x16x32_bf16 v[224:227], v[152:155], v[128:131], v[224:227]\n"
    "v_mfma_f32_16x16x32_bf16 v[224:227], v[184:187], v[160:163], v[224:227]\n"
    "v_mfma_f32_16x16x32_bf16 v[228:231], v[184:187], v[164:167], v[228:231]\n"
    "v_add_f32 v222, v222, v66\n"
    "v_add_f32 v223, v223, v67\n"
    "v_mfma_f32_16x16x32_bf16 v[228:231], v[152:155], v[132:135], v[228:231]\n"
    "v_add_f32 v221, v221, v65\n"
    "v_mfma_f32_16x16x32_bf16 v[232:235], v[152:155], v[136:139], v[232:235]\n"
    "v_add_f32 v220, v220, v64\n"
    "v_mfma_f32_16x16x32_bf16 v[232:235], v[184:187], v[168:171], v[232:235]\n"
    "v_add_f32 v219, v219, v67\n"
    "v_mfma_f32_16x16x32_bf16 v[236:239], v[184:187], v[172:175], v[236:239]\n"
    "v_add_f32 v217, v217, v65\n"
    "v_add_f32 v218, v218, v66\n"
    "v_mfma_f32_16x16x32_bf16 v[236:239], v[152:155], v[140:143], v[236:239]\n"
    "v_add_f32 v216, v216, v64\n"
    "v_mfma_f32_16x16x32_bf16 v[252:255], v[156:159], v[140:143], v[252:255]\n"
    "v_add_f32 v215, v215, v67\n"
    "v_mfma_f32_16x16x32_bf16 v[252:255], v[188:191], v[172:175], v[252:255]\n"
    "v_add_f32 v214, v214, v66\n"
    "v_mfma_f32_16x16x32_bf16 v[248:251], v[188:191], v[168:171], v[248:251]\n"
    "v_add_f32 v212, v212, v64\n"
    "v_add_f32 v213, v213, v65\n"
    "v_mfma_f32_16x16x32_bf16 v[248:251], v[156:159], v[136:139], v[248:251]\n"
    "v_add_f32 v211, v211, v67\n"
    "v_mfma_f32_16x16x32_bf16 v[244:247], v[156:159], v[132:135], v[244:247]\n"
    "v_add_f32 v210, v210, v66\n"
    "v_mfma_f32_16x16x32_bf16 v[244:247], v[188:191], v[164:167], v[244:247]\n"
    "v_add_f32 v209, v209, v65\n"
    "v_mfma_f32_16x16x32_bf16 v[240:243], v[188:191], v[160:163], v[240:243]\n"
    "v_add_f32 v208, v208, v64\n"
    "v_mfma_f32_16x16x32_bf16 v[240:243], v[156:159], v[128:131], v[240:243]\n"
    "s_waitcnt vmcnt(7)\n"
    "s_barrier\n"
    "s_add_u32 m0, s35, 98304\n"
    "s_nop 0\n"
    "global_load_lds_dwordx4 v46, s[30:31] offset:0\n"
    "ds_read_b128 v[128:131], v56 offset:0\n"
    "ds_read_b128 v[132:135], v56 offset:2048\n"
    "ds_read_b128 v[136:139], v56 offset:4096\n"
    "ds_read_b128 v[140:143], v56 offset:6144\n"
    "global_load_lds_dwordx4 v47, s[30:31] offset:1024\n"
    "s_add_u32 s66, s28, 6\n"
    "s_and_b32 s66, s66, 7\n"
    "s_mul_i32 s66, s66, 638976\n"
    "s_add_u32 s30, s6, s66\n"
    "s_addc_u32 s31, s7, 0\n"
    "ds_read_b64_tr_b16 v[144:145], v76 offset:0\n"
    "ds_read_b64_tr_b16 v[146:147], v76 offset:1024\n"
    "ds_read_b64_tr_b16 v[148:149], v77 offset:0\n"
    "ds_read_b64_tr_b16 v[150:151], v77 offset:1024\n"
    "s_add_u32 m0, s34, 98304\n"
    "s_nop 0\n"
    "global_load_lds_dwordx4 v52, s[36:37] offset:0\n"
    "ds_read_b64_tr_b16 v[152:153], v78 offset:0\n"
    "ds_read_b64_tr_b16 v[154:155], v78 offset:1024\n"
    "ds_read_b64_tr_b16 v[156:157], v79 offset:0\n"
    "ds_read_b64_tr_b16 v[158:159], v79 offset:1024\n"
    "global_load_lds_dwordx4 v53, s[36:37] offset:1024\n"
    "ds_read_b128 v[160:163], v57 offset:0\n"
    "ds_read_b128 v[164:167], v57 offset:2048\n"
    "ds_read_b128 v[168:171], v57 offset:4096\n"
    "ds_read_b128 v[172:175], v57 offset:6144\n"
    "global_load_lds_dwordx4 v54, s[36:37] offset:2048\n"
    "ds_read_b64_tr_b16 v[176:177], v76 offset:8192\n"
    "ds_read_b64_tr_b16 v[178:179], v76 offset:9216\n"
    "ds_read_b64_tr_b16 v[180:181], v77 offset:8192\n"
    "ds_read_b64_tr_b16 v[182:183], v77 offset:9216\n"
    "global_load_lds_dwordx4 v55, s[36:37] offset:3072\n"
    "ds_read_b64_tr_b16 v[184:185], v78 offset:8192\n"
    "ds_read_b64_tr_b16 v[186:187], v78 offset:9216\n"
    "ds_read_b64_tr_b16 v[188:189], v79 offset:8192\n"
    "ds_read_b64_tr_b16 v[190:191], v79 offset:9216\n"
    "s_waitcnt vmcnt(10) lgkmcnt(0)\n"
    "s_barrier\n"
    "v_mfma_f32_16x16x32_bf16 v[192:195], v[144:147], v[128:131], v[192:195]\n"
    "v_lshlrev_b32 v115, 7, v115\n"
    "v_mfma_f32_16x16x32_bf16 v[192:195], v[176:179], v[160:163], v[192:195]\n"
    "v_add_u32_dpp v52, v114, v48 quad_perm:[0,0,0,0] row_mask:0xf bank_mask:0xf\n"
    "v_mfma_f32_16x16x32_bf16 v[196:199], v[176:179], v[164:167], v[196:199]\n"
    "v_add_u32_dpp v53, v114, v49 quad_perm:[1,1,1,1] row_mask:0xf bank_mask:0xf\n"
    "v_mfma_f32_16x16x32_bf16 v[196:199], v[144:147], v[132:135], v[196:199]\n"
    "v_add_u32_dpp v54, v114, v50 quad_perm:[2,2,2,2] row_mask:0xf bank_mask:0xf\n"
    "v_mfma_f32_16x16x32_bf16 v[200:203], v[144:147], v[136:139], v[200:203]\n"
    "v_add_u32_dpp v55, v114, v51 quad_perm:[3,3,3,3] row_mask:0xf bank_mask:0xf\n"
    "v_mfma_f32_16x16x32_bf16 v[200:203], v[176:179], v[168:171], v[200:203]\n"
    "v_lshl_or_b32 v36, v36, 7, v19\n"
    "v_mfma_f32_16x16x32_bf16 v[204:207], v[176:179], v[172:175], v[204:207]\n"
    "v_mfma_f32_16x16x32_bf16 v[204:207], v[144:147], v[140:143], v[204:207]\n"
    "v_lshl_or_b32 v37, v37, 7, v19\n"
    "v_mfma_f32_16x16x32_bf16 v[220:223], v[148:151], v[140:143], v[220:223]\n"
    "v_lshl_or_b32 v38, v38, 7, v19\n"
    "v_mfma_f32_16x16x32_bf16 v[220:223], v[180:183], v[172:175], v[220:223]\n"
    "v_lshl_or_b32 v41, v41, 7, v20\n"
    "v_mfma_f32_16x16x32_bf16 v[216:219], v[180:183], v[168:171], v[216:219]\n"
    "s_mov_b32 s64, 0xbdd2d3e8\n"
    "v_mfma_f32_16x16x32_bf16 v[216:219], v[148:151], v[136:139], v[216:219]\n"
    "s_mov_b32 s65, 0xc0135761\n"
    "v_mfma_f32_16x16x32_bf16 v[212:215], v[148:151], v[132:135], v[212:215]\n"
    "v_mfma_f32_16x16x32_bf16 v[212:215], v[180:183], v[164:167], v[212:215]\n"
    "s_and_b32 s41, s20, 1\n"
    "v_mfma_f32_16x16x32_bf16 v[208:211], v[180:183], v[160:163], v[208:211]\n"
    "s_lshl1_add_u32 s40, s21, s41\n"
    "v_mfma_f32_16x16x32_bf16 v[208:211], v[148:151], v[128:131], v[208:211]\n"
    "s_lshl_b32 s41, s40, 8\n"
    "v_mfma_f32_16x16x32_bf16 v[224:227], v[152:155], v[128:131], v[224:227]\n"
    "s_add_u32 s3, s41, 149504\n"
    "v_mfma_f32_16x16x32_bf16 v[224:227], v[184:187], v[160:163], v[224:227]\n"
    "s_lshl_b32 s40, s40, 6\n"
    "v_mfma_f32_16x16x32_bf16 v[228:231], v[184:187], v[164:167], v[228:231]\n"
    "v_mfma_f32_16x16x32_bf16 v[228:231], v[152:155], v[132:135], v[228:231]\n"
    "s_add_u32 s40, s40, s24\n"
    "v_mfma_f32_16x16x32_bf16 v[232:235], v[152:155], v[136:139], v[232:235]\n"
    "s_lshl_b32 s41, s23, 12\n"
    "v_mfma_f32_16x16x32_bf16 v[232:235], v[184:187], v[168:171], v[232:235]\n"
    "s_add_u32 s40, s40, s41\n"
    "v_mfma_f32_16x16x32_bf16 v[236:239], v[184:187], v[172:175], v[236:239]\n"
    "s_lshl3_add_u32 s40, s40, 65536\n"
    "v_mfma_f32_16x16x32_bf16 v[236:239], v[152:155], v[140:143], v[236:239]\n"
    "s_add_u32 s48, s40, s16\n"
    "v_mfma_f32_16x16x32_bf16 v[252:255], v[156:159], v[140:143], v[252:255]\n"
    "v_mfma_f32_16x16x32_bf16 v[252:255], v[188:191], v[172:175], v[252:255]\n"
    "s_addc_u32 s49, s17, 0\n"
    "v_mfma_f32_16x16x32_bf16 v[248:251], v[188:191], v[168:171], v[248:251]\n"
    "s_cmp_lt_u32 s22, 2\n"
    "v_mfma_f32_16x16x32_bf16 v[248:251], v[156:159], v[136:139], v[248:251]\n"
    "s_cselect_b32 s24, 16, 0\n"
    "v_mfma_f32_16x16x32_bf16 v[244:247], v[156:159], v[132:135], v[244:247]\n"
    "s_sub_u32 s23, 3, s22\n"
    "v_mfma_f32_16x16x32_bf16 v[244:247], v[188:191], v[164:167], v[244:247]\n"
    "s_and_b32 s23, s23, 3\n"
    "v_mfma_f32_16x16x32_bf16 v[240:243], v[188:191], v[160:163], v[240:243]\n"
    "v_mfma_f32_16x16x32_bf16 v[240:243], v[156:159], v[128:131], v[240:243]\n"
    "s_waitcnt vmcnt(6)\n"
    "s_barrier\n"
    "s_add_u32 m0, s35, 0\n"
    "s_nop 0\n"
    "global_load_lds_dwordx4 v46, s[30:31] offset:0\n"
    "ds_read_b128 v[128:131], v56 offset:49152\n"
    "ds_read_b128 v[132:135], v56 offset:51200\n"
    "ds_read_b128 v[136:139], v56 offset:53248\n"
    "global_load_lds_dwordx4 v47, s[30:31] offset:1024\n"
    "s_add_u32 s30, s30, 212992\n"
    "s_addc_u32 s31, s31, 0\n"
    "ds_read_b128 v[140:143], v56 offset:55296\n"
    "ds_read_b64_tr_b16 v[144:145], v76 offset:49152\n"
    "ds_read_b64_tr_b16 v[146:147], v76 offset:50176\n"
    "s_add_u32 m0, s34, 0\n"
    "s_nop 0\n"
    "global_load_lds_dwordx4 v52, s[36:37] offset:0\n"
    "ds_read_b64_tr_b16 v[148:149], v77 offset:49152\n"
    "ds_read_b64_tr_b16 v[150:151], v77 offset:50176\n"
    "ds_read_b64_tr_b16 v[152:153], v78 offset:49152\n"
    "global_load_lds_dwordx4 v53, s[36:37] offset:1024\n"
    "ds_read_b64_tr_b16 v[154:155], v78 offset:50176\n"
    "ds_read_b64_tr_b16 v[156:157], v79 offset:49152\n"
    "ds_read_b64_tr_b16 v[158:159], v79 offset:50176\n"
    "global_load_lds_dwordx4 v54, s[36:37] offset:2048\n"
    "ds_read_b128 v[160:163], v57 offset:49152\n"
    "ds_read_b128 v[164:167], v57 offset:51200\n"
    "ds_read_b128 v[168:171], v57 offset:53248\n"
    "global_load_lds_dwordx4 v55, s[36:37] offset:3072\n"
    "ds_read_b128 v[172:175], v57 offset:55296\n"
    "ds_read_b64_tr_b16 v[176:177], v76 offset:57344\n"
    "ds_read_b64_tr_b16 v[178:179], v76 offset:58368\n"
    "global_load_dwordx4 v[28:31], v36, s[8:9]\n"
    "ds_read_b64_tr_b16 v[180:181], v77 offset:57344\n"
    "ds_read_b64_tr_b16 v[182:183], v77 offset:58368\n"
    "ds_read_b64_tr_b16 v[184:185], v78 offset:57344\n"
    "ds_read_b64_tr_b16 v[186:187], v78 offset:58368\n"
    "ds_read_b64_tr_b16 v[188:189], v79 offset:57344\n"
    "ds_read_b64_tr_b16 v[190:191], v79 offset:58368\n"
    "s_waitcnt vmcnt(11) lgkmcnt(0)\n"
    "s_barrier\n"
    "v_mfma_f32_16x16x32_bf16 v[192:195], v[144:147], v[128:131], v[192:195]\n"
    "v_mfma_f32_16x16x32_bf16 v[192:195], v[176:179], v[160:163], v[192:195]\n"
    "v_add_f32 v224, v224, v68\n"
    "v_add_f32 v225, v225, v69\n"
    "v_mfma_f32_16x16x32_bf16 v[196:199], v[176:179], v[164:167], v[196:199]\n"
    "v_add_f32 v226, v226, v70\n"
    "v_mfma_f32_16x16x32_bf16 v[196:199], v[144:147], v[132:135], v[196:199]\n"
    "v_add_f32 v227, v227, v71\n"
    "v_mfma_f32_16x16x32_bf16 v[200:203], v[144:147], v[136:139], v[200:203]\n"
    "v_add_f32 v228, v228, v68\n"
    "v_mfma_f32_16x16x32_bf16 v[200:203], v[176:179], v[168:171], v[200:203]\n"
    "v_add_f32 v229, v229, v69\n"
    "v_add_f32 v230, v230, v70\n"
    "v_mfma_f32_16x16x32_bf16 v[204:207], v[176:179], v[172:175], v[204:207]\n"
    "v_add_f32 v231, v231, v71\n"
    "v_mfma_f32_16x16x32_bf16 v[204:207], v[144:147], v[140:143], v[204:207]\n"
    "v_add_f32 v232, v232, v68\n"
    "v_mfma_f32_16x16x32_bf16 v[220:223], v[148:151], v[140:143], v[220:223]\n"
    "v_add_f32 v233, v233, v69\n"
    "v_mfma_f32_16x16x32_bf16 v[220:223], v[180:183], v[172:175], v[220:223]\n"
    "v_add_f32 v234, v234, v70\n"
    "v_add_f32 v235, v235, v71\n"
    "v_mfma_f32_16x16x32_bf16 v[216:219], v[180:183], v[168:171], v[216:219]\n"
    "v_add_f32 v236, v236, v68\n"
    "v_mfma_f32_16x16x32_bf16 v[216:219], v[148:151], v[136:139], v[216:219]\n"
    "v_add_f32 v237, v237, v69\n"
    "v_mfma_f32_16x16x32_bf16 v[212:215], v[148:151], v[132:135], v[212:215]\n"
    "v_add_f32 v238, v238, v70\n"
    "v_mfma_f32_16x16x32_bf16 v[212:215], v[180:183], v[164:167], v[212:215]\n"
    "v_add_f32 v239, v239, v71\n"
    "v_mfma_f32_16x16x32_bf16 v[208:211], v[180:183], v[160:163], v[208:211]\n"
    "v_mfma_f32_16x16x32_bf16 v[208:211], v[148:151], v[128:131], v[208:211]\n"
    "v_mfma_f32_16x16x32_bf16 v[224:227], v[152:155], v[128:131], v[224:227]\n"
    "v_mfma_f32_16x16x32_bf16 v[224:227], v[184:187], v[160:163], v[224:227]\n"
    "v_mfma_f32_16x16x32_bf16 v[228:231], v[184:187], v[164:167], v[228:231]\n"
    "v_lshlrev_b32 v116, 7, v116\n"
    "v_mfma_f32_16x16x32_bf16 v[228:231], v[152:155], v[132:135], v[228:231]\n"
    "v_add_u32_dpp v52, v115, v48 quad_perm:[0,0,0,0] row_mask:0xf bank_mask:0xf\n"
    "v_mfma_f32_16x16x32_bf16 v[232:235], v[152:155], v[136:139], v[232:235]\n"
    "v_add_u32_dpp v53, v115, v49 quad_perm:[1,1,1,1] row_mask:0xf bank_mask:0xf\n"
    "v_mfma_f32_16x16x32_bf16 v[232:235], v[184:187], v[168:171], v[232:235]\n"
    "v_add_u32_dpp v54, v115, v50 quad_perm:[2,2,2,2] row_mask:0xf bank_mask:0xf\n"
    "v_mfma_f32_16x16x32_bf16 v[236:239], v[184:187], v[172:175], v[236:239]\n"
    "v_add_u32_dpp v55, v115, v51 quad_perm:[3,3,3,3] row_mask:0xf bank_mask:0xf\n"
    "v_mfma_f32_16x16x32_bf16 v[236:239], v[152:155], v[140:143], v[236:239]\n"
    "s_getpc_b64 s[10:11]\n"
    ".Lxd_en2:\n"
    "v_mfma_f32_16x16x32_bf16 v[252:255], v[156:159], v[140:143], v[252:255]\n"
    "s_mov_b32 s40, .Lxd_ph1-.Lxd_en2\n"
    "v_mfma_f32_16x16x32_bf16 v[252:255], v[188:191], v[172:175], v[252:255]\n"
    "s_cmp_eq_u32 s22, 1\n"
    "s_cselect_b32 s40, .Lxd_ph0-.Lxd_en2, s40\n"
    "v_mfma_f32_16x16x32_bf16 v[248:251], v[188:191], v[168:171], v[248:251]\n"
    "s_cmp_eq_u32 s22, 2\n"
    "s_cselect_b32 s40, .Lxd_pl3-.Lxd_en2, s40\n"
    "v_mfma_f32_16x16x32_bf16 v[248:251], v[156:159], v[136:139], v[248:251]\n"
    "s_cmp_eq_u32 s22, 3\n"
    "s_cselect_b32 s40, .Lxd_ph2-.Lxd_en2, s40\n"
    "v_mfma_f32_16x16x32_bf16 v[244:247], v[156:159], v[132:135], v[244:247]\n"
    "s_ashr_i32 s41, s40, 31\n"
    "v_mfma_f32_16x16x32_bf16 v[244:247], v[188:191], v[164:167], v[244:247]\n"
    "s_add_u32 s10, s10, s40\n"
    "s_addc_u32 s11, s11, s41\n"
    "v_mfma_f32_16x16x32_bf16 v[240:243], v[188:191], v[160:163], v[240:243]\n"
    "v_mfma_f32_16x16x32_bf16 v[240:243], v[156:159], v[128:131], v[240:243]\n"
    "s_waitcnt vmcnt(7)\n"
    "s_barrier\n"
    "s_add_u32 m0, s35, 49152\n"
    "s_nop 0\n"
    "global_load_lds_dwordx4 v46, s[30:31] offset:0\n"
    "ds_read_b128 v[128:131], v58 offset:0\n"
    "ds_read_b128 v[132:135], v58 offset:2048\n"
    "ds_read_b128 v[136:139], v58 offset:4096\n"
    "global_load_lds_dwordx4 v47, s[30:31] offset:1024\n"
    "s_add_u32 s30, s30, 212992\n"
    "s_addc_u32 s31, s31, 0\n"
    "ds_read_b128 v[140:143], v58 offset:6144\n"
    "ds_read_b64_tr_b16 v[144:145], v80 offset:0\n"
    "ds_read_b64_tr_b16 v[146:147], v80 offset:1024\n"
    "s_add_u32 m0, s34, 49152\n"
    "s_nop 0\n"
    "global_load_lds_dwordx4 v52, s[36:37] offset:0\n"
    "ds_read_b64_tr_b16 v[148:149], v81 offset:0\n"
    "ds_read_b64_tr_b16 v[150:151], v81 offset:1024\n"
    "ds_read_b64_tr_b16 v[152:153], v82 offset:0\n"
    "global_load_lds_dwordx4 v53, s[36:37] offset:1024\n"
    "ds_read_b64_tr_b16 v[154:155], v82 offset:1024\n"
    "ds_read_b64_tr_b16 v[156:157], v83 offset:0\n"
    "ds_read_b64_tr_b16 v[158:159], v83 offset:1024\n"
    "global_load_lds_dwordx4 v54, s[36:37] offset:2048\n"
    "ds_read_b128 v[160:163], v59 offset:0\n"
    "ds_read_b128 v[164:167], v59 offset:2048\n"
    "ds_read_b128 v[168:171], v59 offset:4096\n"
    "global_load_lds_dwordx4 v55, s[36:37] offset:3072\n"
    "ds_read_b128 v[172:175], v59 offset:6144\n"
    "ds_read_b64_tr_b16 v[176:177], v80 offset:8192\n"
    "ds_read_b64_tr_b16 v[178:179], v80 offset:9216\n"
    "global_load_dwordx4 v[32:35], v37, s[8:9]\n"
    "ds_read_b64_tr_b16 v[180:181], v81 offset:8192\n"
    "ds_read_b64_tr_b16 v[182:183], v81 offset:9216\n"
    "ds_read_b64_tr_b16 v[184:185], v82 offset:8192\n"
    "ds_read_b64_tr_b16 v[186:187], v82 offset:9216\n"
    "ds_read_b64_tr_b16 v[188:189], v83 offset:8192\n"
    "ds_read_b64_tr_b16 v[190:191], v83 offset:9216\n"
    "s_waitcnt vmcnt(12) lgkmcnt(0)\n"
    "s_barrier\n"
    "v_mfma_f32_16x16x32_bf16 v[192:195], v[144:147], v[128:131], v[192:195]\n"
    "v_mfma_f32_16x16x32_bf16 v[192:195], v[176:179], v[160:163], v[192:195]\n"
    "v_mfma_f32_16x16x32_bf16 v[196:199], v[176:179], v[164:167], v[196:199]\n"
    "v_mfma_f32_16x16x32_bf16 v[196:199], v[144:147], v[132:135], v[196:199]\n"
    "v_mfma_f32_16x16x32_bf16 v[200:203], v[144:147], v[136:139], v[200:203]\n"
    "v_add_f32 v240, v240, v72\n"
    "v_mfma_f32_16x16x32_bf16 v[200:203], v[176:179], v[168:171], v[200:203]\n"
    "v_add_f32 v241, v241, v73\n"
    "v_mfma_f32_16x16x32_bf16 v[204:207], v[176:179], v[172:175], v[204:207]\n"
    "v_add_f32 v242, v242, v74\n"
    "v_mfma_f32_16x16x32_bf16 v[204:207], v[144:147], v[140:143], v[204:207]\n"
    "v_add_f32 v243, v243, v75\n"
    "v_mfma_f32_16x16x32_bf16 v[220:223], v[148:151], v[140:143], v[220:223]\n"
    "v_add_f32 v244, v244, v72\n"
    "v_mfma_f32_16x16x32_bf16 v[220:223], v[180:183], v[172:175], v[220:223]\n"
    "v_add_f32 v245, v245, v73\n"
    "v_mfma_f32_16x16x32_bf16 v[216:219], v[180:183], v[168:171], v[216:219]\n"
    "v_add_f32 v246, v246, v74\n"
    "v_mfma_f32_16x16x32_bf16 v[216:219], v[148:151], v[136:139], v[216:219]\n"
    "v_add_f32 v247, v247, v75\n"
    "v_mfma_f32_16x16x32_bf16 v[212:215], v[148:151], v[132:135], v[212:215]\n"
    "v_add_f32 v248, v248, v72\n"
    "v_mfma_f32_16x16x32_bf16 v[212:215], v[180:183], v[164:167], v[212:215]\n"
    "v_add_f32 v249, v249, v73\n"
    "v_mfma_f32_16x16x32_bf16 v[208:211], v[180:183], v[160:163], v[208:211]\n"
    "v_add_f32 v250, v250, v74\n"
    "v_mfma_f32_16x16x32_bf16 v[208:211], v[148:151], v[128:131], v[208:211]\n"
    "v_add_f32 v251, v251, v75\n"
    "v_mfma_f32_16x16x32_bf16 v[224:227], v[152:155], v[128:131], v[224:227]\n"
    "v_add_f32 v252, v252, v72\n"
    "v_mfma_f32_16x16x32_bf16 v[224:227], v[184:187], v[160:163], v[224:227]\n"
    "v_add_f32 v253, v253, v73\n"
    "v_mfma_f32_16x16x32_bf16 v[228:231], v[184:187], v[164:167], v[228:231]\n"
    "v_lshlrev_b32 v117, 7, v117\n"
    "v_add_f32 v254, v254, v74\n"
    "v_mfma_f32_16x16x32_bf16 v[228:231], v[152:155], v[132:135], v[228:231]\n"
    "v_add_f32 v255, v255, v75\n"
    "v_mfma_f32_16x16x32_bf16 v[232:235], v[152:155], v[136:139], v[232:235]\n"
    "v_add_u32_dpp v52, v116, v48 quad_perm:[0,0,0,0] row_mask:0xf bank_mask:0xf\n"
    "v_mfma_f32_16x16x32_bf16 v[232:235], v[184:187], v[168:171], v[232:235]\n"
    "v_mfma_f32_16x16x32_bf16 v[236:239], v[184:187], v[172:175], v[236:239]\n"
    "v_mfma_f32_16x16x32_bf16 v[236:239], v[152:155], v[140:143], v[236:239]\n"
    "v_add_u32_dpp v53, v116, v49 quad_perm:[1,1,1,1] row_mask:0xf bank_mask:0xf\n"
    "v_mfma_f32_16x16x32_bf16 v[252:255], v[156:159], v[140:143], v[252:255]\n"
    "v_mfma_f32_16x16x32_bf16 v[252:255], v[188:191], v[172:175], v[252:255]\n"
    "v_add_u32_dpp v54, v116, v50 quad_perm:[2,2,2,2] row_mask:0xf bank_mask:0xf\n"
    "v_mfma_f32_16x16x32_bf16 v[248:251], v[188:191], v[168:171], v[248:251]\n"
    "v_mfma_f32_16x16x32_bf16 v[248:251], v[156:159], v[136:139], v[248:251]\n"
    "v_mfma_f32_16x16x32_bf16 v[244:247], v[156:159], v[132:135], v[244:247]\n"
    "v_add_u32_dpp v55, v116, v51 quad_perm:[3,3,3,3] row_mask:0xf bank_mask:0xf\n"
    "v_mfma_f32_16x16x32_bf16 v[244:247], v[188:191], v[164:167], v[244:247]\n"
    "v_mfma_f32_16x16x32_bf16 v[240:243], v[188:191], v[160:163], v[240:243]\n"
    "v_mfma_f32_16x16x32_bf16 v[240:243], v[156:159], v[128:131], v[240:243]\n"
    "s_waitcnt vmcnt(8)\n"
    "s_barrier\n"
    "s_add_u32 m0, s35, 98304\n"
    "s_nop 0\n"
    "global_load_lds_dwordx4 v46, s[30:31] offset:0\n"
    "ds_read_b128 v[128:131], v56 offset:0\n"
    "ds_read_b128 v[132:135], v56 offset:2048\n"
    "ds_read_b128 v[136:139], v56 offset:4096\n"
    "global_load_lds_dwordx4 v47, s[30:31] offset:1024\n"
    "s_add_u32 s66, s28, 7\n"
    "s_and_b32 s66, s66, 7\n"
    "s_mul_i32 s66, s66, 638976\n"
    "s_add_u32 s30, s6, s66\n"
    "s_addc_u32 s31, s7, 0\n"
    "ds_read_b128 v[140:143], v56 offset:6144\n"
    "ds_read_b64_tr_b16 v[144:145], v76 offset:0\n"
    "ds_read_b64_tr_b16 v[146:147], v76 offset:1024\n"
    "s_add_u32 m0, s34, 98304\n"
    "s_nop 0\n"
    "global_load_lds_dwordx4 v52, s[36:37] offset:0\n"
    "ds_read_b64_tr_b16 v[148:149], v77 offset:0\n"
    "ds_read_b64_tr_b16 v[150:151], v77 offset:1024\n"
    "ds_read_b64_tr_b16 v[152:153], v78 offset:0\n"
    "global_load_lds_dwordx4 v53, s[36:37] offset:1024\n"
    "ds_read_b64_tr_b16 v[154:155], v78 offset:1024\n"
    "ds_read_b64_tr_b16 v[156:157], v79 offset:0\n"
    "ds_read_b64_tr_b16 v[158:159], v79 offset:1024\n"
    "global_load_lds_dwordx4 v54, s[36:37] offset:2048\n"
    "ds_read_b128 v[160:163], v57 offset:0\n"
    "ds_read_b128 v[164:167], v57 offset:2048\n"
    "ds_read_b128 v[168:171], v57 offset:4096\n"
    "global_load_lds_dwordx4 v55, s[36:37] offset:3072\n"
    "ds_read_b128 v[172:175], v57 offset:6144\n"
    "ds_read_b64_tr_b16 v[176:177], v76 offset:8192\n"
    "ds_read_b64_tr_b16 v[178:179], v76 offset:9216\n"
    "global_load_dword v40, v41, s[8:9]\n"
    "ds_read_b64_tr_b16 v[180:181], v77 offset:8192\n"
    "ds_read_b64_tr_b16 v[182:183], v77 offset:9216\n"
    "ds_read_b64_tr_b16 v[184:185], v78 offset:8192\n"
    "ds_read_b64_tr_b16 v[186:187], v78 offset:9216\n"
    "ds_read_b64_tr_b16 v[188:189], v79 offset:8192\n"
    "ds_read_b64_tr_b16 v[190:191], v79 offset:9216\n"
    "s_waitcnt vmcnt(12) lgkmcnt(0)\n"
    "s_barrier\n"
    "v_mfma_f32_16x16x32_bf16 v[192:195], v[144:147], v[128:131], v[192:195]\n"
    "v_lshlrev_b32 v118, 7, v118\n"
    "v_mfma_f32_16x16x32_bf16 v[192:195], v[176:179], v[160:163], v[192:195]\n"
    "v_add_u32_dpp v52, v117, v48 quad_perm:[0,0,0,0] row_mask:0xf bank_mask:0xf\n"
    "v_mfma_f32_16x16x32_bf16 v[196:199], v[176:179], v[164:167], v[196:199]\n"
    "v_add_u32_dpp v53, v117, v49 quad_perm:[1,1,1,1] row_mask:0xf bank_mask:0xf\n"
    "v_mfma_f32_16x16x32_bf16 v[196:199], v[144:147], v[132:135], v[196:199]\n"
    "v_add_u32_dpp v54, v117, v50 quad_perm:[2,2,2,2] row_mask:0xf bank_mask:0xf\n"
    "v_mfma_f32_16x16x32_bf16 v[200:203], v[144:147], v[136:139], v[200:203]\n"
    "v_add_u32_dpp v55, v117, v51 quad_perm:[3,3,3,3] row_mask:0xf bank_mask:0xf\n"
    "v_mfma_f32_16x16x32_bf16 v[200:203], v[176:179], v[168:171], v[200:203]\n"
    "v_readlane_b32 s25, v26, 9\n"
    "v_mfma_f32_16x16x32_bf16 v[204:207], v[176:179], v[172:175], v[204:207]\n"
    "v_readlane_b32 s40, v26, 8\n"
    "v_mfma_f32_16x16x32_bf16 v[204:207], v[144:147], v[140:143], v[204:207]\n"
    "v_readlane_b32 s41, v27, 8\n"
    "v_mfma_f32_16x16x32_bf16 v[220:223], v[148:151], v[140:143], v[220:223]\n"
    "s_mul_i32 s42, s25, 0x9e3779b1\n"
    "v_mfma_f32_16x16x32_bf16 v[220:223], v[180:183], v[172:175], v[220:223]\n"
    "s_lshr_b32 s42, s42, 24\n"
    "v_mfma_f32_16x16x32_bf16 v[216:219], v[180:183], v[168:171], v[216:219]\n"
    "s_xor_b32 s42, s42, s40\n"
    "v_mfma_f32_16x16x32_bf16 v[216:219], v[148:151], v[136:139], v[216:219]\n"
    "s_and_b32 s42, s42, 0xff\n"
    "v_mfma_f32_16x16x32_bf16 v[212:215], v[148:151], v[132:135], v[212:215]\n"
    "s_xor_b32 s41, s41, s25\n"
    "v_mfma_f32_16x16x32_bf16 v[212:215], v[180:183], v[164:167], v[212:215]\n"
    "s_xor_b32 s41, s41, 0xa5c30f96\n"
    "v_mfma_f32_16x16x32_bf16 v[208:211], v[180:183], v[160:163], v[208:211]\n"
    "s_or_b32 s42, s41, s42\n"
    "v_mfma_f32_16x16x32_bf16 v[208:211], v[148:151], v[128:131], v[208:211]\n"
    "v_mfma_f32_16x16x32_bf16 v[224:227], v[152:155], v[128:131], v[224:227]\n"
    "s_lshr_b32 s41, s40, 8\n"
    "v_mfma_f32_16x16x32_bf16 v[224:227], v[184:187], v[160:163], v[224:227]\n"
    "s_mul_i32 s40, s41, 0x9e3779b1\n"
    "v_mfma_f32_16x16x32_bf16 v[228:231], v[184:187], v[164:167], v[228:231]\n"
    "s_xor_b32 s41, s41, 0xa5c30f96\n"
    "v_mfma_f32_16x16x32_bf16 v[228:231], v[152:155], v[132:135], v[228:231]\n"
    "s_lshr_b32 s40, s40, 24\n"
    "v_mfma_f32_16x16x32_bf16 v[232:235], v[152:155], v[136:139], v[232:235]\n"
    "v_xor_b32 v13, s40, v26\n"
    "v_mfma_f32_16x16x32_bf16 v[232:235], v[184:187], v[168:171], v[232:235]\n"
    "v_xor_b32 v22, s41, v27\n"
    "v_mfma_f32_16x16x32_bf16 v[236:239], v[184:187], v[172:175], v[236:239]\n"
    "v_lshl_or_b32 v13, v13, 24, v22\n"
    "v_mfma_f32_16x16x32_bf16 v[236:239], v[152:155], v[140:143], v[236:239]\n"
    "v_cmp_ne_u32 s[66:67], 0, v13\n"
    "v_mfma_f32_16x16x32_bf16 v[252:255], v[156:159], v[140:143], v[252:255]\n"
    "s_and_b32 s66, s66, 0xff\n"
    "v_mfma_f32_16x16x32_bf16 v[252:255], v[188:191], v[172:175], v[252:255]\n"
    "s_or_b32 s42, s42, s66\n"
    "v_mfma_f32_16x16x32_bf16 v[248:251], v[188:191], v[168:171], v[248:251]\n"
    "s_cmp_eq_u32 s20, 3\n"
    "v_mfma_f32_16x16x32_bf16 v[248:251], v[156:159], v[136:139], v[248:251]\n"
    "s_cselect_b32 s42, s42, 0\n"
    "v_mfma_f32_16x16x32_bf16 v[244:247], v[156:159], v[132:135], v[244:247]\n"
    "s_cmp_lg_u32 s42, 0\n"
    "v_mfma_f32_16x16x32_bf16 v[244:247], v[188:191], v[164:167], v[244:247]\n"
    "s_cbranch_scc1 .Lxd_pv3\n"
    ".Lxd_pv3r:\n"
    "v_mfma_f32_16x16x32_bf16 v[240:243], v[188:191], v[160:163], v[240:243]\n"
    "v_mfma_f32_16x16x32_bf16 v[240:243], v[156:159], v[128:131], v[240:243]\n"
    "s_waitcnt vmcnt(8)\n"
    "s_barrier\n"
    "s_add_u32 m0, s35, 0\n"
    "s_nop 0\n"
    "global_load_lds_dwordx4 v46, s[30:31] offset:0\n"
    "ds_read_b128 v[128:131], v56 offset:49152\n"
    "ds_read_b128 v[132:135], v56 offset:51200\n"
    "ds_read_b128 v[136:139], v56 offset:53248\n"
    "global_load_lds_dwordx4 v47, s[30:31] offset:1024\n"
    "s_add_u32 s30, s30, 212992\n"
    "s_addc_u32 s31, s31, 0\n"
    "ds_read_b128 v[140:143], v56 offset:55296\n"
    "ds_read_b64_tr_b16 v[144:145], v76 offset:49152\n"
    "ds_read_b64_tr_b16 v[146:147], v76 offset:50176\n"
    "s_add_u32 m0, s34, 0\n"
    "s_nop 0\n"
    "global_load_lds_dwordx4 v52, s[36:37] offset:0\n"
    "ds_read_b64_tr_b16 v[148:149], v77 offset:49152\n"
    "ds_read_b64_tr_b16 v[150:151], v77 offset:50176\n"
    "ds_read_b64_tr_b16 v[152:153], v78 offset:49152\n"
    "global_load_lds_dwordx4 v53, s[36:37] offset:1024\n"
    "ds_read_b64_tr_b16 v[154:155], v78 offset:50176\n"
    "ds_read_b64_tr_b16 v[156:157], v79 offset:49152\n"
    "ds_read_b64_tr_b16 v[158:159], v79 offset:50176\n"
    "global_load_lds_dwordx4 v54, s[36:37] offset:2048\n"
    "ds_read_b128 v[160:163], v57 offset:49152\n"
    "ds_read_b128 v[164:167], v57 offset:51200\n"
    "ds_read_b128 v[168:171], v57 offset:53248\n"
    "global_load_lds_dwordx4 v55, s[36:37] offset:3072\n"
    "ds_read_b128 v[172:175], v57 offset:55296\n"
    "ds_read_b64_tr_b16 v[176:177], v76 offset:57344\n"
    "ds_read_b64_tr_b16 v[178:179], v76 offset:58368\n"
    "global_load_dwordx4 v[36:39], v38, s[8:9]\n"
    "ds_read_b64_tr_b16 v[180:181], v77 offset:57344\n"
    "ds_read_b64_tr_b16 v[182:183], v77 offset:58368\n"
    "ds_read_b64_tr_b16 v[184:185], v78 offset:57344\n"
    "ds_read_b64_tr_b16 v[186:187], v78 offset:58368\n"
    "ds_read_b64_tr_b16 v[188:189], v79 offset:57344\n"
    "ds_read_b64_tr_b16 v[190:191], v79 offset:58368\n"
    "s_waitcnt vmcnt(12) lgkmcnt(0)\n"
    "s_barrier\n"
    "v_mfma_f32_16x16x32_bf16 v[192:195], v[144:147], v[128:131], v[192:195]\n"
    "v_lshlrev_b32 v119, 7, v119\n"
    "v_mfma_f32_16x16x32_bf16 v[192:195], v[176:179], v[160:163], v[192:195]\n"
    "v_add_u32_dpp v52, v118, v48 quad_perm:[0,0,0,0] row_mask:0xf bank_mask:0xf\n"
    "v_mfma_f32_16x16x32_bf16 v[196:199], v[176:179], v[164:167], v[196:199]\n"
    "v_add_u32_dpp v53, v118, v49 quad_perm:[1,1,1,1] row_mask:0xf bank_mask:0xf\n"
    "v_mfma_f32_16x16x32_bf16 v[196:199], v[144:147], v[132:135], v[196:199]\n"
    "v_mfma_f32_16x16x32_bf16 v[200:203], v[144:147], v[136:139], v[200:203]\n"
    "v_add_u32_dpp v54, v118, v50 quad_perm:[2,2,2,2] row_mask:0xf bank_mask:0xf\n"
    "v_mfma_f32_16x16x32_bf16 v[200:203], v[176:179], v[168:171], v[200:203]\n"
    "v_add_u32_dpp v55, v118, v51 quad_perm:[3,3,3,3] row_mask:0xf bank_mask:0xf\n"
    "v_mfma_f32_16x16x32_bf16 v[204:207], v[176:179], v[172:175], v[204:207]\n"
    "v_mfma_f32_16x16x32_bf16 v[204:207], v[144:147], v[140:143], v[204:207]\n"
    "v_lshlrev_b32 v60, 16, v92\n"
    "v_mfma_f32_16x16x32_bf16 v[220:223], v[148:151], v[140:143], v[220:223]\n"
    "v_and_b32 v61, 0xffff0000, v92\n"
    "v_mfma_f32_16x16x32_bf16 v[220:223], v[180:183], v[172:175], v[220:223]\n"
    "v_mfma_f32_16x16x32_bf16 v[216:219], v[180:183], v[168:171], v[216:219]\n"
    "v_lshlrev_b32 v62, 16, v93\n"
    "v_mfma_f32_16x16x32_bf16 v[216:219], v[148:151], v[136:139], v[216:219]\n"
    "v_and_b32 v63, 0xffff0000, v93\n"
    "v_mfma_f32_16x16x32_bf16 v[212:215], v[148:151], v[132:135], v[212:215]\n"
    "v_mfma_f32_16x16x32_bf16 v[212:215], v[180:183], v[164:167], v[212:215]\n"
    "v_lshlrev_b32 v64, 16, v94\n"
    "v_mfma_f32_16x16x32_bf16 v[208:211], v[180:183], v[160:163], v[208:211]\n"
    "v_and_b32 v65, 0xffff0000, v94\n"
    "v_mfma_f32_16x16x32_bf16 v[208:211], v[148:151], v[128:131], v[208:211]\n"
    "v_mfma_f32_16x16x32_bf16 v[224:227], v[152:155], v[128:131], v[224:227]\n"
    "v_lshlrev_b32 v66, 16, v95\n"
    "v_mfma_f32_16x16x32_bf16 v[224:227], v[184:187], v[160:163], v[224:227]\n"
    "v_and_b32 v67, 0xffff0000, v95\n"
    "v_mfma_f32_16x16x32_bf16 v[228:231], v[184:187], v[164:167], v[228:231]\n"
    "v_mfma_f32_16x16x32_bf16 v[228:231], v[152:155], v[132:135], v[228:231]\n"
    "v_lshlrev_b32 v68, 16, v122\n"
    "v_mfma_f32_16x16x32_bf16 v[232:235], v[152:155], v[136:139], v[232:235]\n"
    "v_and_b32 v69, 0xffff0000, v122\n"
    "v_mfma_f32_16x16x32_bf16 v[232:235], v[184:187], v[168:171], v[232:235]\n"
    "v_mfma_f32_16x16x32_bf16 v[236:239], v[184:187], v[172:175], v[236:239]\n"
    "v_lshlrev_b32 v70, 16, v123\n"
    "v_mfma_f32_16x16x32_bf16 v[236:239], v[152:155], v[140:143], v[236:239]\n"
    "v_and_b32 v71, 0xffff0000, v123\n"
    "v_mfma_f32_16x16x32_bf16 v[252:255], v[156:159], v[140:143], v[252:255]\n"
    "v_mfma_f32_16x16x32_bf16 v[252:255], v[188:191], v[172:175], v[252:255]\n"
    "v_lshlrev_b32 v72, 16, v124\n"
    "v_mfma_f32_16x16x32_bf16 v[248:251], v[188:191], v[168:171], v[248:251]\n"
    "v_and_b32 v73, 0xffff0000, v124\n"
    "v_mfma_f32_16x16x32_bf16 v[248:251], v[156:159], v[136:139], v[248:251]\n"
    "v_mfma_f32_16x16x32_bf16 v[244:247], v[156:159], v[132:135], v[244:247]\n"
    "v_lshlrev_b32 v74, 16, v125\n"
    "v_mfma_f32_16x16x32_bf16 v[244:247], v[188:191], v[164:167], v[244:247]\n"
    "v_and_b32 v75, 0xffff0000, v125\n"
    "v_mfma_f32_16x16x32_bf16 v[240:243], v[188:191], v[160:163], v[240:243]\n"
    "v_mfma_f32_16x16x32_bf16 v[240:243], v[156:159], v[128:131], v[240:243]\n"
    "s_waitcnt vmcnt(8)\n"
    "s_barrier\n"
    "s_add_u32 m0, s35, 49152\n"
    "s_nop 0\n"
    "global_load_lds_dwordx4 v46, s[30:31] offset:0\n"
    "ds_read_b128 v[128:131], v58 offset:0\n"
    "ds_read_b128 v[132:135], v58 offset:2048\n"
    "ds_read_b128 v[136:139], v58 offset:4096\n"
    "ds_read_b128 v[140:143], v58 offset:6144\n"
    "global_load_lds_dwordx4 v47, s[30:31] offset:1024\n"
    "s_add_u32 s30, s30, 212992\n"
    "s_addc_u32 s31, s31, 0\n"
    "ds_read_b64_tr_b16 v[144:145], v80 offset:0\n"
    "ds_read_b64_tr_b16 v[146:147], v80 offset:1024\n"
    "ds_read_b64_tr_b16 v[148:149], v81 offset:0\n"
    "ds_read_b64_tr_b16 v[150:151], v81 offset:1024\n"
    "s_add_u32 m0, s34, 49152\n"
    "s_nop 0\n"
    "global_load_lds_dwordx4 v52, s[36:37] offset:0\n"
    "ds_read_b64_tr_b16 v[152:153], v82 offset:0\n"
    "ds_read_b64_tr_b16 v[154:155], v82 offset:1024\n"
    "ds_read_b64_tr_b16 v[156:157], v83 offset:0\n"
    "ds_read_b64_tr_b16 v[158:159], v83 offset:1024\n"
    "global_load_lds_dwordx4 v53, s[36:37] offset:1024\n"
    "ds_read_b128 v[160:163], v59 offset:0\n"
    "ds_read_b128 v[164:167], v59 offset:2048\n"
    "ds_read_b128 v[168:171], v59 offset:4096\n"
    "ds_read_b128 v[172:175], v59 offset:6144\n"
    "global_load_lds_dwordx4 v54, s[36:37] offset:2048\n"
    "ds_read_b64_tr_b16 v[176:177], v80 offset:8192\n"
    "ds_read_b64_tr_b16 v[178:179], v80 offset:9216\n"
    "ds_read_b64_tr_b16 v[180:181], v81 offset:8192\n"
    "ds_read_b64_tr_b16 v[182:183], v81 offset:9216\n"
    "global_load_lds_dwordx4 v55, s[36:37] offset:3072\n"
    "ds_read_b64_tr_b16 v[184:185], v82 offset:8192\n"
    "ds_read_b64_tr_b16 v[186:187], v82 offset:9216\n"
    "ds_read_b64_tr_b16 v[188:189], v83 offset:8192\n"
    "ds_read_b64_tr_b16 v[190:191], v83 offset:9216\n"
    "s_waitcnt vmcnt(11) lgkmcnt(0)\n"
    "s_barrier\n"
    "v_mfma_f32_16x16x32_bf16 v[192:195], v[144:147], v[128:131], v[192:195]\n"
    "v_lshlrev_b32 v120, 7, v120\n"
    "v_mfma_f32_16x16x32_bf16 v[192:195], v[176:179], v[160:163], v[192:195]\n"
    "v_add_u32_dpp v52, v119, v48 quad_perm:[0,0,0,0] row_mask:0xf bank_mask:0xf\n"
    "v_mfma_f32_16x16x32_bf16 v[196:199], v[176:179], v[164:167], v[196:199]\n"
    "v_add_u32_dpp v53, v119, v49 quad_perm:[1,1,1,1] row_mask:0xf bank_mask:0xf\n"
    "v_mfma_f32_16x16x32_bf16 v[196:199], v[144:147], v[132:135], v[196:199]\n"
    "v_add_u32_dpp v54, v119, v50 quad_perm:[2,2,2,2] row_mask:0xf bank_mask:0xf\n"
    "v_mfma_f32_16x16x32_bf16 v[200:203], v[144:147], v[136:139], v[200:203]\n"
    "v_add_u32_dpp v55, v119, v51 quad_perm:[3,3,3,3] row_mask:0xf bank_mask:0xf\n"
    "v_mfma_f32_16x16x32_bf16 v[200:203], v[176:179], v[168:171], v[200:203]\n"
    "v_lshrrev_b32 v22, 8, v26\n"
    "v_mfma_f32_16x16x32_bf16 v[204:207], v[176:179], v[172:175], v[204:207]\n"
    "v_mfma_f32_16x16x32_bf16 v[204:207], v[144:147], v[140:143], v[204:207]\n"
    "v_mov_b32 v26, s40\n"
    "v_mfma_f32_16x16x32_bf16 v[220:223], v[148:151], v[140:143], v[220:223]\n"
    "v_mov_b32 v27, s41\n"
    "v_mfma_f32_16x16x32_bf16 v[220:223], v[180:183], v[172:175], v[220:223]\n"
    "v_add_u32_dpp v22, v22, v22 row_shl:4 row_mask:0xf bank_mask:0xf\n"
    "v_mfma_f32_16x16x32_bf16 v[216:219], v[180:183], v[168:171], v[216:219]\n"
    "v_add_u32_e64 v43, s25, 1\n"
    "v_mfma_f32_16x16x32_bf16 v[216:219], v[148:151], v[136:139], v[216:219]\n"
    "v_lshlrev_b32 v8, 2, v1\n"
    "v_mfma_f32_16x16x32_bf16 v[212:215], v[148:151], v[132:135], v[212:215]\n"
    "v_mfma_f32_16x16x32_bf16 v[212:215], v[180:183], v[164:167], v[212:215]\n"
    "v_max_u32_dpp v13, v22, v22 quad_perm:[1,0,3,2] row_mask:0xf bank_mask:0xf\n"
    "v_mfma_f32_16x16x32_bf16 v[208:211], v[180:183], v[160:163], v[208:211]\n"
    "v_add_u32 v8, 154112, v8\n"
    "v_mfma_f32_16x16x32_bf16 v[208:211], v[148:151], v[128:131], v[208:211]\n"
    "s_nop 0\n"
    "v_mfma_f32_16x16x32_bf16 v[224:227], v[152:155], v[128:131], v[224:227]\n"
    "v_max_u32_dpp v13, v13, v13 quad_perm:[2,3,0,1] row_mask:0xf bank_mask:0xf\n"
    "v_mfma_f32_16x16x32_bf16 v[224:227], v[184:187], v[160:163], v[224:227]\n"
    "v_sub_u32 v22, v22, v13\n"
    "v_mfma_f32_16x16x32_bf16 v[228:231], v[184:187], v[164:167], v[228:231]\n"
    "v_mfma_f32_16x16x32_bf16 v[228:231], v[152:155], v[132:135], v[228:231]\n"
    "v_subrev_u32 v13, 818, v13\n"
    "v_mfma_f32_16x16x32_bf16 v[232:235], v[152:155], v[136:139], v[232:235]\n"
    "v_ldexp_f32 v22, 1.0, v22\n"
    "v_mfma_f32_16x16x32_bf16 v[232:235], v[184:187], v[168:171], v[232:235]\n"
    "v_rcp_f32 v60, v60\n"
    "v_mfma_f32_16x16x32_bf16 v[236:239], v[184:187], v[172:175], v[236:239]\n"
    "v_rcp_f32 v61, v61\n"
    "v_mfma_f32_16x16x32_bf16 v[236:239], v[152:155], v[140:143], v[236:239]\n"
    "v_rcp_f32 v62, v62\n"
    "v_mfma_f32_16x16x32_bf16 v[252:255], v[156:159], v[140:143], v[252:255]\n"
    "v_mfma_f32_16x16x32_bf16 v[252:255], v[188:191], v[172:175], v[252:255]\n"
    "v_rcp_f32 v63, v63\n"
    "v_mfma_f32_16x16x32_bf16 v[248:251], v[188:191], v[168:171], v[248:251]\n"
    "v_rcp_f32 v64, v64\n"
    "v_mfma_f32_16x16x32_bf16 v[248:251], v[156:159], v[136:139], v[248:251]\n"
    "v_rcp_f32 v65, v65\n"
    "v_mfma_f32_16x16x32_bf16 v[244:247], v[156:159], v[132:135], v[244:247]\n"
    "v_rcp_f32 v66, v66\n"
    "v_mfma_f32_16x16x32_bf16 v[244:247], v[188:191], v[164:167], v[244:247]\n"
    "v_rcp_f32 v67, v67\n"
    "v_mfma_f32_16x16x32_bf16 v[240:243], v[188:191], v[160:163], v[240:243]\n"
    "v_mfma_f32_16x16x32_bf16 v[240:243], v[156:159], v[128:131], v[240:243]\n"
    "s_waitcnt vmcnt(7)\n"
    "s_barrier\n"
    "s_add_u32 m0, s35, 98304\n"
    "s_nop 0\n"
    "global_load_lds_dwordx4 v46, s[30:31] offset:0\n"
    "ds_read_b128 v[128:131], v56 offset:0\n"
    "ds_read_b128 v[132:135], v56 offset:2048\n"
    "ds_read_b128 v[136:139], v56 offset:4096\n"
    "ds_read_b128 v[140:143], v56 offset:6144\n"
    "global_load_lds_dwordx4 v47, s[30:31] offset:1024\n"
    "s_add_u32 s30, s6, 5111808\n"
    "s_addc_u32 s31, s7, 0\n"
    "ds_read_b64_tr_b16 v[144:145], v76 offset:0\n"
    "ds_read_b64_tr_b16 v[146:147], v76 offset:1024\n"
    "ds_read_b64_tr_b16 v[148:149], v77 offset:0\n"
    "ds_read_b64_tr_b16 v[150:151], v77 offset:1024\n"
    "s_add_u32 m0, s34, 98304\n"
    "s_nop 0\n"
    "global_load_lds_dwordx4 v52, s[36:37] offset:0\n"
    "ds_read_b64_tr_b16 v[152:153], v78 offset:0\n"
    "ds_read_b64_tr_b16 v[154:155], v78 offset:1024\n"
    "ds_read_b64_tr_b16 v[156:157], v79 offset:0\n"
    "ds_read_b64_tr_b16 v[158:159], v79 offset:1024\n"
    "global_load_lds_dwordx4 v53, s[36:37] offset:1024\n"
    "ds_read_b128 v[160:163], v57 offset:0\n"
    "ds_read_b128 v[164:167], v57 offset:2048\n"
    "ds_read_b128 v[168:171], v57 offset:4096\n"
    "ds_read_b128 v[172:175], v57 offset:6144\n"
    "global_load_lds_dwordx4 v54, s[36:37] offset:2048\n"
    "ds_read_b64_tr_b16 v[176:177], v76 offset:8192\n"
    "ds_read_b64_tr_b16 v[178:179], v76 offset:9216\n"
    "ds_read_b64_tr_b16 v[180:181], v77 offset:8192\n"
    "ds_read_b64_tr_b16 v[182:183], v77 offset:9216\n"
    "global_load_lds_dwordx4 v55, s[36:37] offset:3072\n"
    "ds_read_b64_tr_b16 v[184:185], v78 offset:8192\n"
    "ds_read_b64_tr_b16 v[186:187], v78 offset:9216\n"
    "ds_read_b64_tr_b16 v[188:189], v79 offset:8192\n"
    "ds_read_b64_tr_b16 v[190:191], v79 offset:9216\n"
    "s_waitcnt vmcnt(10) lgkmcnt(0)\n"
    "s_barrier\n"
    "v_mfma_f32_16x16x32_bf16 v[192:195], v[144:147], v[128:131], v[192:195]\n"
    "v_lshlrev_b32 v121, 7, v121\n"
    "v_mfma_f32_16x16x32_bf16 v[192:195], v[176:179], v[160:163], v[192:195]\n"
    "v_add_u32_dpp v52, v120, v48 quad_perm:[0,0,0,0] row_mask:0xf bank_mask:0xf\n"
    "v_mfma_f32_16x16x32_bf16 v[196:199], v[176:179], v[164:167], v[196:199]\n"
    "v_add_u32_dpp v53, v120, v49 quad_perm:[1,1,1,1] row_mask:0xf bank_mask:0xf\n"
    "v_mfma_f32_16x16x32_bf16 v[196:199], v[144:147], v[132:135], v[196:199]\n"
    "v_add_u32_dpp v54, v120, v50 quad_perm:[2,2,2,2] row_mask:0xf bank_mask:0xf\n"
    "v_mfma_f32_16x16x32_bf16 v[200:203], v[144:147], v[136:139], v[200:203]\n"
    "v_add_u32_dpp v55, v120, v51 quad_perm:[3,3,3,3] row_mask:0xf bank_mask:0xf\n"
    "v_mfma_f32_16x16x32_bf16 v[200:203], v[176:179], v[168:171], v[200:203]\n"
    "s_movk_i32 s43, 15744\n"
    "v_pk_maximum3_f16 v21, v28, v28, s43 op_sel_hi:[1,1,0] neg_lo:[0,1,0] neg_hi:[0,1,0]\n"
    "v_mfma_f32_16x16x32_bf16 v[204:207], v[176:179], v[172:175], v[204:207]\n"
    "v_pk_maximum3_f16 v21, v29, v29, v21 neg_lo:[0,1,0] neg_hi:[0,1,0]\n"
    "v_mfma_f32_16x16x32_bf16 v[204:207], v[144:147], v[140:143], v[204:207]\n"
    "v_mfma_f32_16x16x32_bf16 v[220:223], v[148:151], v[140:143], v[220:223]\n"
    "v_pk_maximum3_f16 v21, v30, v30, v21 neg_lo:[0,1,0] neg_hi:[0,1,0]\n"
    "v_mfma_f32_16x16x32_bf16 v[220:223], v[180:183], v[172:175], v[220:223]\n"
    "v_pk_maximum3_f16 v21, v31, v31, v21 neg_lo:[0,1,0] neg_hi:[0,1,0]\n"
    "v_mfma_f32_16x16x32_bf16 v[216:219], v[180:183], v[168:171], v[216:219]\n"
    "v_pk_maximum3_f16 v21, v32, v32, v21 neg_lo:[0,1,0] neg_hi:[0,1,0]\n"
    "v_mfma_f32_16x16x32_bf16 v[216:219], v[148:151], v[136:139], v[216:219]\n"
    "v_pk_maximum3_f16 v21, v33, v33, v21 neg_lo:[0,1,0] neg_hi:[0,1,0]\n"
    "v_mfma_f32_16x16x32_bf16 v[212:215], v[148:151], v[132:135], v[212:215]\n"
    "v_pk_maximum3_f16 v21, v34, v34, v21 neg_lo:[0,1,0] neg_hi:[0,1,0]\n"
    "v_mfma_f32_16x16x32_bf16 v[212:215], v[180:183], v[164:167], v[212:215]\n"
    "v_pk_maximum3_f16 v21, v35, v35, v21 neg_lo:[0,1,0] neg_hi:[0,1,0]\n"
    "v_mfma_f32_16x16x32_bf16 v[208:211], v[180:183], v[160:163], v[208:211]\n"
    "v_pk_maximum3_f16 v21, v36, v36, v21 neg_lo:[0,1,0] neg_hi:[0,1,0]\n"
    "v_mfma_f32_16x16x32_bf16 v[208:211], v[148:151], v[128:131], v[208:211]\n"
    "v_mfma_f32_16x16x32_bf16 v[224:227], v[152:155], v[128:131], v[224:227]\n"
    "v_pk_maximum3_f16 v21, v37, v37, v21 neg_lo:[0,1,0] neg_hi:[0,1,0]\n"
    "v_mfma_f32_16x16x32_bf16 v[224:227], v[184:187], v[160:163], v[224:227]\n"
    "v_pk_maximum3_f16 v21, v38, v38, v21 neg_lo:[0,1,0] neg_hi:[0,1,0]\n"
    "v_mfma_f32_16x16x32_bf16 v[228:231], v[184:187], v[164:167], v[228:231]\n"
    "v_pk_maximum3_f16 v21, v39, v39, v21 neg_lo:[0,1,0] neg_hi:[0,1,0]\n"
    "v_mfma_f32_16x16x32_bf16 v[228:231], v[152:155], v[132:135], v[228:231]\n"
    "v_pk_maximum3_f16 v21, v40, v40, v21 neg_lo:[0,1,0] neg_hi:[0,1,0]\n"
    "v_mfma_f32_16x16x32_bf16 v[232:235], v[152:155], v[136:139], v[232:235]\n"
    "v_pk_max_u16 v21, v21, v21 op_sel:[0,1] op_sel_hi:[1,0]\n"
    "v_mfma_f32_16x16x32_bf16 v[232:235], v[184:187], v[168:171], v[232:235]\n"
    "v_rcp_f32 v68, v68\n"
    "v_mfma_f32_16x16x32_bf16 v[236:239], v[184:187], v[172:175], v[236:239]\n"
    "v_rcp_f32 v69, v69\n"
    "v_mfma_f32_16x16x32_bf16 v[236:239], v[152:155], v[140:143], v[236:239]\n"
    "v_mfma_f32_16x16x32_bf16 v[252:255], v[156:159], v[140:143], v[252:255]\n"
    "v_rcp_f32 v70, v70\n"
    "v_mfma_f32_16x16x32_bf16 v[252:255], v[188:191], v[172:175], v[252:255]\n"
    "v_rcp_f32 v71, v71\n"
    "v_mfma_f32_16x16x32_bf16 v[248:251], v[188:191], v[168:171], v[248:251]\n"
    "v_rcp_f32 v72, v72\n"
    "v_mfma_f32_16x16x32_bf16 v[248:251], v[156:159], v[136:139], v[248:251]\n"
    "v_rcp_f32 v73, v73\n"
    "v_mfma_f32_16x16x32_bf16 v[244:247], v[156:159], v[132:135], v[244:247]\n"
    "v_rcp_f32 v74, v74\n"
    "v_mfma_f32_16x16x32_bf16 v[244:247], v[188:191], v[164:167], v[244:247]\n"
    "v_rcp_f32 v75, v75\n"
    "v_mfma_f32_16x16x32_bf16 v[240:243], v[188:191], v[160:163], v[240:243]\n"
    "v_mfma_f32_16x16x32_bf16 v[240:243], v[156:159], v[128:131], v[240:243]\n"
    "s_waitcnt vmcnt(6)\n"
    "s_barrier\n"
    "s_add_u32 m0, s35, 0\n"
    "s_nop 0\n"
    "global_load_lds_dwordx4 v46, s[30:31] offset:0\n"
    "ds_read_b128 v[128:131], v56 offset:49152\n"
    "ds_read_b128 v[132:135], v56 offset:51200\n"
    "ds_read_b128 v[136:139], v56 offset:53248\n"
    "ds_read_b128 v[140:143], v56 offset:55296\n"
    "global_load_lds_dwordx4 v47, s[30:31] offset:1024\n"
    "s_add_u32 s30, s30, 212992\n"
    "s_addc_u32 s31, s31, 0\n"
    "ds_read_b64_tr_b16 v[144:145], v76 offset:49152\n"
    "ds_read_b64_tr_b16 v[146:147], v76 offset:50176\n"
    "ds_read_b64_tr_b16 v[148:149], v77 offset:49152\n"
    "ds_read_b64_tr_b16 v[150:151], v77 offset:50176\n"
    "s_add_u32 m0, s34, 0\n"
    "s_nop 0\n"
    "global_load_lds_dwordx4 v52, s[36:37] offset:0\n"
    "ds_read_b64_tr_b16 v[152:153], v78 offset:49152\n"
    "ds_read_b64_tr_b16 v[154:155], v78 offset:50176\n"
    "ds_read_b64_tr_b16 v[156:157], v79 offset:49152\n"
    "ds_read_b64_tr_b16 v[158:159], v79 offset:50176\n"
    "global_load_lds_dwordx4 v53, s[36:37] offset:1024\n"
    "ds_read_b128 v[160:163], v57 offset:49152\n"
    "ds_read_b128 v[164:167], v57 offset:51200\n"
    "ds_read_b128 v[168:171], v57 offset:53248\n"
    "ds_read_b128 v[172:175], v57 offset:55296\n"
    "global_load_lds_dwordx4 v54, s[36:37] offset:2048\n"
    "ds_read_b64_tr_b16 v[176:177], v76 offset:57344\n"
    "ds_read_b64_tr_b16 v[178:179], v76 offset:58368\n"
    "ds_read_b64_tr_b16 v[180:181], v77 offset:57344\n"
    "ds_read_b64_tr_b16 v[182:183], v77 offset:58368\n"
    "global_load_lds_dwordx4 v55, s[36:37] offset:3072\n"
    "ds_read_b64_tr_b16 v[184:185], v78 offset:57344\n"
    "ds_read_b64_tr_b16 v[186:187], v78 offset:58368\n"
    "ds_read_b64_tr_b16 v[188:189], v79 offset:57344\n"
    "ds_read_b64_tr_b16 v[190:191], v79 offset:58368\n"
    "s_waitcnt vmcnt(10) lgkmcnt(0)\n"
    "s_barrier\n"
    "v_mfma_f32_16x16x32_bf16 v[192:195], v[144:147], v[128:131], v[192:195]\n"
    "v_add_u32_dpp v52, v121, v48 quad_perm:[0,0,0,0] row_mask:0xf bank_mask:0xf\n"
    "s_mov_b32 s42, 0x71800000\n"
    "v_mfma_f32_16x16x32_bf16 v[192:195], v[176:179], v[160:163], v[192:195]\n"
    "v_add_u32_dpp v53, v121, v49 quad_perm:[1,1,1,1] row_mask:0xf bank_mask:0xf\n"
    "v_mfma_f32_16x16x32_bf16 v[196:199], v[176:179], v[164:167], v[196:199]\n"
    "v_add_u32_dpp v54, v121, v50 quad_perm:[2,2,2,2] row_mask:0xf bank_mask:0xf\n"
    "v_mfma_f32_16x16x32_bf16 v[196:199], v[144:147], v[132:135], v[196:199]\n"
    "v_add_u32_dpp v55, v121, v51 quad_perm:[3,3,3,3] row_mask:0xf bank_mask:0xf\n"
    "v_mfma_f32_16x16x32_bf16 v[200:203], v[144:147], v[136:139], v[200:203]\n"
    "v_bfe_u32 v41, v1, 4, 1\n"
    "v_mfma_f32_16x16x32_bf16 v[200:203], v[176:179], v[168:171], v[200:203]\n"
    "v_lshlrev_b32 v41, 2, v41\n"
    "v_mfma_f32_16x16x32_bf16 v[204:207], v[176:179], v[172:175], v[204:207]\n"
    "v_add_u32 v41, 154112, v41\n"
    "v_mfma_f32_16x16x32_bf16 v[204:207], v[144:147], v[140:143], v[204:207]\n"
    "v_med3_f32 v60, v60, s42, -s42\n"
    "v_mfma_f32_16x16x32_bf16 v[220:223], v[148:151], v[140:143], v[220:223]\n"
    "v_max_u32_dpp v21, v21, v21 quad_perm:[1,0,3,2] row_mask:0xf bank_mask:0xf\n"
    "v_mfma_f32_16x16x32_bf16 v[220:223], v[180:183], v[172:175], v[220:223]\n"
    "v_med3_f32 v61, v61, s42, -s42\n"
    "v_mfma_f32_16x16x32_bf16 v[216:219], v[180:183], v[168:171], v[216:219]\n"
    "v_max_u32_dpp v21, v21, v21 quad_perm:[2,3,0,1] row_mask:0xf bank_mask:0xf\n"
    "v_mfma_f32_16x16x32_bf16 v[216:219], v[148:151], v[136:139], v[216:219]\n"
    "v_med3_f32 v62, v62, s42, -s42\n"
    "v_mfma_f32_16x16x32_bf16 v[212:215], v[148:151], v[132:135], v[212:215]\n"
    "v_max_u32_dpp v21, v21, v21 row_half_mirror row_mask:0xf bank_mask:0xf\n"
    "v_mfma_f32_16x16x32_bf16 v[212:215], v[180:183], v[164:167], v[212:215]\n"
    "v_med3_f32 v63, v63, s42, -s42\n"
    "v_mfma_f32_16x16x32_bf16 v[208:211], v[180:183], v[160:163], v[208:211]\n"
    "v_max_u32_dpp v21, v21, v21 row_mirror row_mask:0xf bank_mask:0xf\n"
    "v_mfma_f32_16x16x32_bf16 v[208:211], v[148:151], v[128:131], v[208:211]\n"
    "v_med3_f32 v64, v64, s42, -s42\n"
    "v_mfma_f32_16x16x32_bf16 v[224:227], v[152:155], v[128:131], v[224:227]\n"
    "v_max_u32_dpp v21, v21, v21 row_bcast:15 row_mask:0xa bank_mask:0xf\n"
    "v_mfma_f32_16x16x32_bf16 v[224:227], v[184:187], v[160:163], v[224:227]\n"
    "v_med3_f32 v65, v65, s42, -s42\n"
    "v_mfma_f32_16x16x32_bf16 v[228:231], v[184:187], v[164:167], v[228:231]\n"
    "v_max_u32_dpp v21, v21, v21 row_bcast:31 row_mask:0xc bank_mask:0xf\n"
    "v_mfma_f32_16x16x32_bf16 v[228:231], v[152:155], v[132:135], v[228:231]\n"
    "v_med3_f32 v66, v66, s42, -s42\n"
    "v_mfma_f32_16x16x32_bf16 v[232:235], v[152:155], v[136:139], v[232:235]\n"
    "v_readlane_b32 s47, v21, 63\n"
    "v_mfma_f32_16x16x32_bf16 v[232:235], v[184:187], v[168:171], v[232:235]\n"
    "v_med3_f32 v67, v67, s42, -s42\n"
    "v_mfma_f32_16x16x32_bf16 v[236:239], v[184:187], v[172:175], v[236:239]\n"
    "s_and_b32 s47, s47, 0x7f80\n"
    "v_mfma_f32_16x16x32_bf16 v[236:239], v[152:155], v[140:143], v[236:239]\n"
    "s_sub_u32 s50, s47, 2816\n"
    "v_mfma_f32_16x16x32_bf16 v[252:255], v[156:159], v[140:143], v[252:255]\n"
    "s_mul_i32 s51, s50, -8\n"
    "v_med3_f32 v68, v68, s42, -s42\n"
    "v_mfma_f32_16x16x32_bf16 v[252:255], v[188:191], v[172:175], v[252:255]\n"
    "s_lshr_b32 s47, s47, 3\n"
    "v_med3_f32 v69, v69, s42, -s42\n"
    "v_mfma_f32_16x16x32_bf16 v[248:251], v[188:191], v[168:171], v[248:251]\n"
    "v_med3_f32 v70, v70, s42, -s42\n"
    "v_mfma_f32_16x16x32_bf16 v[248:251], v[156:159], v[136:139], v[248:251]\n"
    "v_med3_f32 v71, v71, s42, -s42\n"
    "v_mfma_f32_16x16x32_bf16 v[244:247], v[156:159], v[132:135], v[244:247]\n"
    "v_med3_f32 v72, v72, s42, -s42\n"
    "v_mfma_f32_16x16x32_bf16 v[244:247], v[188:191], v[164:167], v[244:247]\n"
    "v_med3_f32 v73, v73, s42, -s42\n"
    "v_mfma_f32_16x16x32_bf16 v[240:243], v[188:191], v[160:163], v[240:243]\n"
    "v_med3_f32 v74, v74, s42, -s42\n"
    "v_mfma_f32_16x16x32_bf16 v[240:243], v[156:159], v[128:131], v[240:243]\n"
    "v_med3_f32 v75, v75, s42, -s42\n"
    "s_waitcnt vmcnt(6)\n"
    "s_barrier\n"
    "s_add_u32 m0, s35, 49152\n"
    "s_nop 0\n"
    "global_load_lds_dwordx4 v46, s[30:31] offset:0\n"
    "ds_read_b128 v[128:131], v58 offset:0\n"
    "ds_read_b128 v[132:135], v58 offset:2048\n"
    "ds_read_b128 v[136:139], v58 offset:4096\n"
    "ds_read_b128 v[140:143], v58 offset:6144\n"
    "global_load_lds_dwordx4 v47, s[30:31] offset:1024\n"
    "ds_read_b64_tr_b16 v[144:145], v80 offset:0\n"
    "ds_read_b64_tr_b16 v[146:147], v80 offset:1024\n"
    "ds_read_b64_tr_b16 v[148:149], v81 offset:0\n"
    "ds_read_b64_tr_b16 v[150:151], v81 offset:1024\n"
    "s_add_u32 m0, s34, 49152\n"
    "s_nop 0\n"
    "global_load_lds_dwordx4 v52, s[36:37] offset:0\n"
    "ds_read_b64_tr_b16 v[152:153], v82 offset:0\n"
    "ds_read_b64_tr_b16 v[154:155], v82 offset:1024\n"
    "ds_read_b64_tr_b16 v[156:157], v83 offset:0\n"
    "ds_read_b64_tr_b16 v[158:159], v83 offset:1024\n"
    "global_load_lds_dwordx4 v53, s[36:37] offset:1024\n"
    "ds_read_b128 v[160:163], v59 offset:0\n"
    "ds_read_b128 v[164:167], v59 offset:2048\n"
    "ds_read_b128 v[168:171], v59 offset:4096\n"
    "ds_read_b128 v[172:175], v59 offset:6144\n"
    "global_load_lds_dwordx4 v54, s[36:37] offset:2048\n"
    "ds_read_b64_tr_b16 v[176:177], v80 offset:8192\n"
    "ds_read_b64_tr_b16 v[178:179], v80 offset:9216\n"
    "ds_read_b64_tr_b16 v[180:181], v81 offset:8192\n"
    "ds_read_b64_tr_b16 v[182:183], v81 offset:9216\n"
    "global_load_lds_dwordx4 v55, s[36:37] offset:3072\n"
    "ds_read_b64_tr_b16 v[184:185], v82 offset:8192\n"
    "ds_read_b64_tr_b16 v[186:187], v82 offset:9216\n"
    "ds_read_b64_tr_b16 v[188:189], v83 offset:8192\n"
    "ds_read_b64_tr_b16 v[190:191], v83 offset:9216\n"
    "s_waitcnt vmcnt(10) lgkmcnt(0)\n"
    "s_barrier\n"
    "v_mfma_f32_16x16x32_bf16 v[192:195], v[144:147], v[128:131], v[192:195]\n"
    "v_pk_maximum3_f16 v21, v28, v28, s50 op_sel_hi:[1,1,0] neg_lo:[0,1,0] neg_hi:[0,1,0]\n"
    "v_mfma_f32_16x16x32_bf16 v[192:195], v[176:179], v[160:163], v[192:195]\n"
    "v_pk_mad_u16 v21, v21, 8, s51 op_sel_hi:[1,0,0]\n"
    "v_mfma_f32_16x16x32_bf16 v[196:199], v[176:179], v[164:167], v[196:199]\n"
    "v_bfi_b32 v28, s57, v21, v28\n"
    "v_mfma_f32_16x16x32_bf16 v[196:199], v[144:147], v[132:135], v[196:199]\n"
    "v_mfma_f32_16x16x32_bf16 v[200:203], v[144:147], v[136:139], v[200:203]\n"
    "v_pk_maximum3_f16 v12, v29, v29, s50 op_sel_hi:[1,1,0] neg_lo:[0,1,0] neg_hi:[0,1,0]\n"
    "v_mfma_f32_16x16x32_bf16 v[200:203], v[176:179], v[168:171], v[200:203]\n"
    "v_pk_mad_u16 v12, v12, 8, s51 op_sel_hi:[1,0,0]\n"
    "v_mfma_f32_16x16x32_bf16 v[204:207], v[176:179], v[172:175], v[204:207]\n"
    "v_bfi_b32 v29, s57, v12, v29\n"
    "v_mfma_f32_16x16x32_bf16 v[204:207], v[144:147], v[140:143], v[204:207]\n"
    "v_mfma_f32_16x16x32_bf16 v[220:223], v[148:151], v[140:143], v[220:223]\n"
    "v_pk_maximum3_f16 v21, v30, v30, s50 op_sel_hi:[1,1,0] neg_lo:[0,1,0] neg_hi:[0,1,0]\n"
    "v_mfma_f32_16x16x32_bf16 v[220:223], v[180:183], v[172:175], v[220:223]\n"
    "v_pk_mad_u16 v21, v21, 8, s51 op_sel_hi:[1,0,0]\n"
    "v_mfma_f32_16x16x32_bf16 v[216:219], v[180:183], v[168:171], v[216:219]\n"
    "v_bfi_b32 v30, s57, v21, v30\n"
    "v_mfma_f32_16x16x32_bf16 v[216:219], v[148:151], v[136:139], v[216:219]\n"
    "v_mfma_f32_16x16x32_bf16 v[212:215], v[148:151], v[132:135], v[212:215]\n"
    "v_pk_maximum3_f16 v12, v31, v31, s50 op_sel_hi:[1,1,0] neg_lo:[0,1,0] neg_hi:[0,1,0]\n"
    "v_mfma_f32_16x16x32_bf16 v[212:215], v[180:183], v[164:167], v[212:215]\n"
    "v_pk_mad_u16 v12, v12, 8, s51 op_sel_hi:[1,0,0]\n"
    "v_mfma_f32_16x16x32_bf16 v[208:211], v[180:183], v[160:163], v[208:211]\n"
    "v_bfi_b32 v31, s57, v12, v31\n"
    "v_mfma_f32_16x16x32_bf16 v[208:211], v[148:151], v[128:131], v[208:211]\n"
    "v_mfma_f32_16x16x32_bf16 v[224:227], v[152:155], v[128:131], v[224:227]\n"
    "v_pk_maximum3_f16 v21, v32, v32, s50 op_sel_hi:[1,1,0] neg_lo:[0,1,0] neg_hi:[0,1,0]\n"
    "v_mfma_f32_16x16x32_bf16 v[224:227], v[184:187], v[160:163], v[224:227]\n"
    "v_pk_mad_u16 v21, v21, 8, s51 op_sel_hi:[1,0,0]\n"
    "v_mfma_f32_16x16x32_bf16 v[228:231], v[184:187], v[164:167], v[228:231]\n"
    "v_bfi_b32 v32, s57, v21, v32\n"
    "v_mfma_f32_16x16x32_bf16 v[228:231], v[152:155], v[132:135], v[228:231]\n"
    "v_mfma_f32_16x16x32_bf16 v[232:235], v[152:155], v[136:139], v[232:235]\n"
    "v_pk_maximum3_f16 v12, v33, v33, s50 op_sel_hi:[1,1,0] neg_lo:[0,1,0] neg_hi:[0,1,0]\n"
    "v_mfma_f32_16x16x32_bf16 v[232:235], v[184:187], v[168:171], v[232:235]\n"
    "v_pk_mad_u16 v12, v12, 8, s51 op_sel_hi:[1,0,0]\n"
    "v_mfma_f32_16x16x32_bf16 v[236:239], v[184:187], v[172:175], v[236:239]\n"
    "v_bfi_b32 v33, s57, v12, v33\n"
    "v_mfma_f32_16x16x32_bf16 v[236:239], v[152:155], v[140:143], v[236:239]\n"
    "v_mfma_f32_16x16x32_bf16 v[252:255], v[156:159], v[140:143], v[252:255]\n"
    "v_pk_maximum3_f16 v21, v34, v34, s50 op_sel_hi:[1,1,0] neg_lo:[0,1,0] neg_hi:[0,1,0]\n"
    "v_mfma_f32_16x16x32_bf16 v[252:255], v[188:191], v[172:175], v[252:255]\n"
    "v_pk_mad_u16 v21, v21, 8, s51 op_sel_hi:[1,0,0]\n"
    "v_mfma_f32_16x16x32_bf16 v[248:251], v[188:191], v[168:171], v[248:251]\n"
    "v_bfi_b32 v34, s57, v21, v34\n"
    "v_mfma_f32_16x16x32_bf16 v[248:251], v[156:159], v[136:139], v[248:251]\n"
    "v_mfma_f32_16x16x32_bf16 v[244:247], v[156:159], v[132:135], v[244:247]\n"
    "v_lshlrev_b32 v52, 4, v1\n"
    "v_mfma_f32_16x16x32_bf16 v[244:247], v[188:191], v[164:167], v[244:247]\n"
    "v_add_u32 v53, 4096, v52\n"
    "v_mfma_f32_16x16x32_bf16 v[240:243], v[188:191], v[160:163], v[240:243]\n"
    "v_mfma_f32_16x16x32_bf16 v[240:243], v[156:159], v[128:131], v[240:243]\n"
    "s_waitcnt vmcnt(6)\n"
    "s_barrier\n"
    "s_mul_i32 s42, s20, 5376\n"
    "s_add_u32 s40, s42, 1048576\n"
    "s_add_u32 s38, s16, s40\n"
    "s_addc_u32 s39, s17, 0\n"
    "s_add_u32 m0, s42, 98304\n"
    "s_nop 0\n"
    "global_load_lds_dwordx4 v52, s[38:39] offset:0 sc1\n"
    "ds_read_b128 v[128:131], v56 offset:0\n"
    "ds_read_b128 v[132:135], v56 offset:2048\n"
    "ds_read_b128 v[136:139], v56 offset:4096\n"
    "ds_read_b128 v[140:143], v56 offset:6144\n"
    "global_load_lds_dwordx4 v52, s[38:39] offset:1024 sc1\n"
    "ds_read_b64_tr_b16 v[144:145], v76 offset:0\n"
    "ds_read_b64_tr_b16 v[146:147], v76 offset:1024\n"
    "ds_read_b64_tr_b16 v[148:149], v77 offset:0\n"
    "ds_read_b64_tr_b16 v[150:151], v77 offset:1024\n"
    "global_load_lds_dwordx4 v52, s[38:39] offset:2048 sc1\n"
    "ds_read_b64_tr_b16 v[152:153], v78 offset:0\n"
    "ds_read_b64_tr_b16 v[154:155], v78 offset:1024\n"
    "ds_read_b64_tr_b16 v[156:157], v79 offset:0\n"
    "ds_read_b64_tr_b16 v[158:159], v79 offset:1024\n"
    "global_load_lds_dwordx4 v52, s[38:39] offset:3072 sc1\n"
    "ds_read_b128 v[160:163], v57 offset:0\n"
    "ds_read_b128 v[164:167], v57 offset:2048\n"
    "ds_read_b128 v[168:171], v57 offset:4096\n"
    "ds_read_b128 v[172:175], v57 offset:6144\n"
    "s_add_u32 m0, s42, 102400\n"
    "s_nop 0\n"
    "global_load_lds_dwordx4 v53, s[38:39] offset:0 sc1\n"
    "ds_read_b64_tr_b16 v[176:177], v76 offset:8192\n"
    "ds_read_b64_tr_b16 v[178:179], v76 offset:9216\n"
    "ds_read_b64_tr_b16 v[180:181], v77 offset:8192\n"
    "ds_read_b64_tr_b16 v[182:183], v77 offset:9216\n"
    "global_load_lds_dwordx4 v53, s[38:39] offset:256 sc1\n"
    "ds_read_b64_tr_b16 v[184:185], v78 offset:8192\n"
    "ds_read_b64_tr_b16 v[186:187], v78 offset:9216\n"
    "ds_read_b64_tr_b16 v[188:189], v79 offset:8192\n"
    "ds_read_b64_tr_b16 v[190:191], v79 offset:9216\n"
    "s_waitcnt vmcnt(10) lgkmcnt(0)\n"
    "s_barrier\n"
    "v_mfma_f32_16x16x32_bf16 v[192:195], v[144:147], v[128:131], v[192:195]\n"
    "v_pk_maximum3_f16 v12, v35, v35, s50 op_sel_hi:[1,1,0] neg_lo:[0,1,0] neg_hi:[0,1,0]\n"
    "v_mfma_f32_16x16x32_bf16 v[192:195], v[176:179], v[160:163], v[192:195]\n"
    "v_pk_mad_u16 v12, v12, 8, s51 op_sel_hi:[1,0,0]\n"
    "v_mfma_f32_16x16x32_bf16 v[196:199], v[176:179], v[164:167], v[196:199]\n"
    "v_mfma_f32_16x16x32_bf16 v[196:199], v[144:147], v[132:135], v[196:199]\n"
    "v_bfi_b32 v35, s57, v12, v35\n"
    "v_mfma_f32_16x16x32_bf16 v[200:203], v[144:147], v[136:139], v[200:203]\n"
    "v_pk_maximum3_f16 v21, v36, v36, s50 op_sel_hi:[1,1,0] neg_lo:[0,1,0] neg_hi:[0,1,0]\n"
    "v_mfma_f32_16x16x32_bf16 v[200:203], v[176:179], v[168:171], v[200:203]\n"
    "v_mfma_f32_16x16x32_bf16 v[204:207], v[176:179], v[172:175], v[204:207]\n"
    "v_pk_mad_u16 v21, v21, 8, s51 op_sel_hi:[1,0,0]\n"
    "v_mfma_f32_16x16x32_bf16 v[204:207], v[144:147], v[140:143], v[204:207]\n"
    "v_bfi_b32 v36, s57, v21, v36\n"
    "v_mfma_f32_16x16x32_bf16 v[220:223], v[148:151], v[140:143], v[220:223]\n"
    "v_mfma_f32_16x16x32_bf16 v[220:223], v[180:183], v[172:175], v[220:223]\n"
    "v_pk_maximum3_f16 v12, v37, v37, s50 op_sel_hi:[1,1,0] neg_lo:[0,1,0] neg_hi:[0,1,0]\n"
    "v_mfma_f32_16x16x32_bf16 v[216:219], v[180:183], v[168:171], v[216:219]\n"
    "v_pk_mad_u16 v12, v12, 8, s51 op_sel_hi:[1,0,0]\n"
    "v_mfma_f32_16x16x32_bf16 v[216:219], v[148:151], v[136:139], v[216:219]\n"
    "v_mfma_f32_16x16x32_bf16 v[212:215], v[148:151], v[132:135], v[212:215]\n"
    "v_bfi_b32 v37, s57, v12, v37\n"
    "v_mfma_f32_16x16x32_bf16 v[212:215], v[180:183], v[164:167], v[212:215]\n"
    "v_pk_maximum3_f16 v21, v38, v38, s50 op_sel_hi:[1,1,0] neg_lo:[0,1,0] neg_hi:[0,1,0]\n"
    "v_mfma_f32_16x16x32_bf16 v[208:211], v[180:183], v[160:163], v[208:211]\n"
    "v_mfma_f32_16x16x32_bf16 v[208:211], v[148:151], v[128:131], v[208:211]\n"
    "v_pk_mad_u16 v21, v21, 8, s51 op_sel_hi:[1,0,0]\n"
    "v_mfma_f32_16x16x32_bf16 v[224:227], v[152:155], v[128:131], v[224:227]\n"
    "v_mfma_f32_16x16x32_bf16 v[224:227], v[184:187], v[160:163], v[224:227]\n"
    "v_bfi_b32 v38, s57, v21, v38\n"
    "v_mfma_f32_16x16x32_bf16 v[228:231], v[184:187], v[164:167], v[228:231]\n"
    "v_pk_maximum3_f16 v12, v39, v39, s50 op_sel_hi:[1,1,0] neg_lo:[0,1,0] neg_hi:[0,1,0]\n"
    "v_mfma_f32_16x16x32_bf16 v[228:231], v[152:155], v[132:135], v[228:231]\n"
    "v_mfma_f32_16x16x32_bf16 v[232:235], v[152:155], v[136:139], v[232:235]\n"
    "v_pk_mad_u16 v12, v12, 8, s51 op_sel_hi:[1,0,0]\n"
    "v_mfma_f32_16x16x32_bf16 v[232:235], v[184:187], v[168:171], v[232:235]\n"
    "v_bfi_b32 v39, s57, v12, v39\n"
    "v_mfma_f32_16x16x32_bf16 v[236:239], v[184:187], v[172:175], v[236:239]\n"
    "v_mfma_f32_16x16x32_bf16 v[236:239], v[152:155], v[140:143], v[236:239]\n"
    "v_pk_maximum3_f16 v21, v40, v40, s50 op_sel_hi:[1,1,0] neg_lo:[0,1,0] neg_hi:[0,1,0]\n"
    "v_mfma_f32_16x16x32_bf16 v[252:255], v[156:159], v[140:143], v[252:255]\n"
    "v_pk_mad_u16 v21, v21, 8, s51 op_sel_hi:[1,0,0]\n"
    "v_mfma_f32_16x16x32_bf16 v[252:255], v[188:191], v[172:175], v[252:255]\n"
    "v_mfma_f32_16x16x32_bf16 v[248:251], v[188:191], v[168:171], v[248:251]\n"
    "v_bfi_b32 v40, s57, v21, v40\n"
    "v_mfma_f32_16x16x32_bf16 v[248:251], v[156:159], v[136:139], v[248:251]\n"
    "s_sub_u32 s66, 4112, s47\n"
    "s_lshl_b32 s66, s66, 19\n"
    "v_cvt_pkrtz_f16_f32 v12, s66, s66\n"
    "v_mfma_f32_16x16x32_bf16 v[244:247], v[156:159], v[132:135], v[244:247]\n"
    "v_mfma_f32_16x16x32_bf16 v[244:247], v[188:191], v[164:167], v[244:247]\n"
    "v_lshl_or_b32 v21, v12, 16, s47\n"
    "v_mfma_f32_16x16x32_bf16 v[240:243], v[188:191], v[160:163], v[240:243]\n"
    "v_mfma_f32_16x16x32_bf16 v[240:243], v[156:159], v[128:131], v[240:243]\n"
    "s_waitcnt vmcnt(6)\n"
    "s_barrier\n"
    "ds_read_b128 v[128:131], v56 offset:49152\n"
    "ds_read_b128 v[132:135], v56 offset:51200\n"
    "ds_read_b128 v[136:139], v56 offset:53248\n"
    "ds_read_b128 v[140:143], v56 offset:55296\n"
    "ds_read_b64_tr_b16 v[144:145], v76 offset:49152\n"
    "ds_read_b64_tr_b16 v[146:147], v76 offset:50176\n"
    "ds_read_b64_tr_b16 v[148:149], v77 offset:49152\n"
    "ds_read_b64_tr_b16 v[150:151], v77 offset:50176\n"
    "ds_read_b64_tr_b16 v[152:153], v78 offset:49152\n"
    "ds_read_b64_tr_b16 v[154:155], v78 offset:50176\n"
    "ds_read_b64_tr_b16 v[156:157], v79 offset:49152\n"
    "ds_read_b64_tr_b16 v[158:159], v79 offset:50176\n"
    "ds_read_b128 v[160:163], v57 offset:49152\n"
    "ds_read_b128 v[164:167], v57 offset:51200\n"
    "ds_read_b128 v[168:171], v57 offset:53248\n"
    "ds_read_b128 v[172:175], v57 offset:55296\n"
    "ds_read_b64_tr_b16 v[176:177], v76 offset:57344\n"
    "ds_read_b64_tr_b16 v[178:179], v76 offset:58368\n"
    "ds_read_b64_tr_b16 v[180:181], v77 offset:57344\n"
    "ds_read_b64_tr_b16 v[182:183], v77 offset:58368\n"
    "ds_read_b64_tr_b16 v[184:185], v78 offset:57344\n"
    "ds_read_b64_tr_b16 v[186:187], v78 offset:58368\n"
    "ds_read_b64_tr_b16 v[188:189], v79 offset:57344\n"
    "ds_read_b64_tr_b16 v[190:191], v79 offset:58368\n"
    "s_cmp_lg_u32 s20, 3\n"
    "s_cbranch_scc1 .Lxd_on4\n"
    "s_mov_b64 exec, 1\n"
    "ds_write_b64 v8, v[26:27]\n"
    "s_mov_b64 exec, 15\n"
    "ds_write2_b32 v8, v22, v13 offset0:4 offset1:8\n"
    "s_mov_b64 exec, -1\n"
    ".Lxd_on4:\n"
    "s_waitcnt vmcnt(0) lgkmcnt(0)\n"
    "s_cmp_lg_u32 s20, 3\n"
    "s_cbranch_scc1 .Lxd_on5\n"
    "s_mov_b64 exec, 0x200\n"
    "global_store_dword v11, v43, s[16:17] sc1\n"
    "s_mov_b64 exec, -1\n"
    ".Lxd_on5:\n"
    "s_barrier\n"
    "s_setprio 3\n"
    "v_mfma_f32_16x16x32_bf16 v[192:195], v[144:147], v[128:131], v[192:195]\n"
    "v_add_u32 v42, 98304, v52\n"
    "v_mfma_f32_16x16x32_bf16 v[192:195], v[176:179], v[160:163], v[192:195]\n"
    "v_lshl_add_u32 v11, v1, 2, s3\n"
    "v_mfma_f32_16x16x32_bf16 v[196:199], v[176:179], v[164:167], v[196:199]\n"
    "v_mov_b32 v13, s58\n"
    "v_mfma_f32_16x16x32_bf16 v[196:199], v[144:147], v[132:135], v[196:199]\n"
    "s_mov_b32 s36, 0\n"
    "v_mfma_f32_16x16x32_bf16 v[200:203], v[144:147], v[136:139], v[200:203]\n"
    "v_mov_b32 v84, s65\n"
    "v_mfma_f32_16x16x32_bf16 v[200:203], v[176:179], v[168:171], v[200:203]\n"
    "v_lshl_add_u32 v7, s23, 12, v3\n"
    "v_mfma_f32_16x16x32_bf16 v[204:207], v[176:179], v[172:175], v[204:207]\n"
    "v_lshl_add_u32 v8, s23, 12, v4\n"
    "v_mfma_f32_16x16x32_bf16 v[204:207], v[144:147], v[140:143], v[204:207]\n"
    "v_lshl_add_u32 v9, s23, 12, v5\n"
    "v_mfma_f32_16x16x32_bf16 v[220:223], v[148:151], v[140:143], v[220:223]\n"
    "v_lshl_add_u32 v10, s23, 12, v6\n"
    "v_mfma_f32_16x16x32_bf16 v[220:223], v[180:183], v[172:175], v[220:223]\n"
    "v_mfma_f32_16x16x32_bf16 v[216:219], v[180:183], v[168:171], v[216:219]\n"
    "v_mul_f32 v76, v192, v192\n"
    "v_mul_f32 v77, v193, v193\n"
    "v_mfma_f32_16x16x32_bf16 v[216:219], v[148:151], v[136:139], v[216:219]\n"
    "v_mul_f32 v78, v196, v196\n"
    "v_mul_f32 v79, v197, v197\n"
    "v_mfma_f32_16x16x32_bf16 v[212:215], v[148:151], v[132:135], v[212:215]\n"
    "v_mul_f32 v80, v200, v200\n"
    "v_mul_f32 v81, v201, v201\n"
    "v_mfma_f32_16x16x32_bf16 v[212:215], v[180:183], v[164:167], v[212:215]\n"
    "v_mul_f32 v82, v204, v204\n"
    "v_mul_f32 v83, v205, v205\n"
    "v_mfma_f32_16x16x32_bf16 v[208:211], v[180:183], v[160:163], v[208:211]\n"
    "v_fma_f32 v76, v76, s64, v84\n"
    "v_fma_f32 v77, v77, s64, v84\n"
    "v_mfma_f32_16x16x32_bf16 v[208:211], v[148:151], v[128:131], v[208:211]\n"
    "v_fma_f32 v78, v78, s64, v84\n"
    "v_fma_f32 v79, v79, s64, v84\n"
    "v_mfma_f32_16x16x32_bf16 v[224:227], v[152:155], v[128:131], v[224:227]\n"
    "v_fma_f32 v80, v80, s64, v84\n"
    "v_fma_f32 v81, v81, s64, v84\n"
    "v_mfma_f32_16x16x32_bf16 v[224:227], v[184:187], v[160:163], v[224:227]\n"
    "v_fma_f32 v82, v82, s64, v84\n"
    "v_fma_f32 v83, v83, s64, v84\n"
    "v_mfma_f32_16x16x32_bf16 v[228:231], v[184:187], v[164:167], v[228:231]\n"
    "v_mul_f32 v76, v76, v192\n"
    "v_mul_f32 v77, v77, v193\n"
    "v_mfma_f32_16x16x32_bf16 v[228:231], v[152:155], v[132:135], v[228:231]\n"
    "v_mul_f32 v78, v78, v196\n"
    "v_mul_f32 v79, v79, v197\n"
    "v_mfma_f32_16x16x32_bf16 v[232:235], v[152:155], v[136:139], v[232:235]\n"
    "v_mul_f32 v80, v80, v200\n"
    "v_mul_f32 v81, v81, v201\n"
    "v_mfma_f32_16x16x32_bf16 v[232:235], v[184:187], v[168:171], v[232:235]\n"
    "v_mul_f32 v82, v82, v204\n"
    "v_mul_f32 v83, v83, v205\n"
    "v_mfma_f32_16x16x32_bf16 v[236:239], v[184:187], v[172:175], v[236:239]\n"
    "v_exp_f32 v76, v76\n"
    "v_mfma_f32_16x16x32_bf16 v[236:239], v[152:155], v[140:143], v[236:239]\n"
    "v_exp_f32 v77, v77\n"
    "v_mfma_f32_16x16x32_bf16 v[252:255], v[156:159], v[140:143], v[252:255]\n"
    "v_exp_f32 v78, v78\n"
    "v_mfma_f32_16x16x32_bf16 v[252:255], v[188:191], v[172:175], v[252:255]\n"
    "v_exp_f32 v79, v79\n"
    "v_mfma_f32_16x16x32_bf16 v[248:251], v[188:191], v[168:171], v[248:251]\n"
    "v_exp_f32 v80, v80\n"
    "v_mfma_f32_16x16x32_bf16 v[248:251], v[156:159], v[136:139], v[248:251]\n"
    "v_exp_f32 v81, v81\n"
    "v_mfma_f32_16x16x32_bf16 v[244:247], v[156:159], v[132:135], v[244:247]\n"
    "v_exp_f32 v82, v82\n"
    "v_mfma_f32_16x16x32_bf16 v[244:247], v[188:191], v[164:167], v[244:247]\n"
    "v_exp_f32 v83, v83\n"
    "v_mfma_f32_16x16x32_bf16 v[240:243], v[188:191], v[160:163], v[240:243]\n"
    "v_mfma_f32_16x16x32_bf16 v[240:243], v[156:159], v[128:131], v[240:243]\n"
    "s_cmp_lg_u32 s21, 0\n"
    "s_cbranch_scc1 .Lxd_g1\n"
    "s_barrier\n"
    ".Lxd_g1:\n"
    "s_setprio 2\n"
    "s_cmp_eq_u32 s22, 3\n"
    "s_cbranch_scc1 .Lxd_np\n"
    "ds_write_b32 v25, v24 offset:20\n"
    "ds_write_b128 v15, v[28:31] offset:0\n"
    "ds_write_b128 v15, v[32:35] offset:144\n"
    "ds_write_b128 v15, v[36:39] offset:288\n"
    "ds_write2_b32 v16, v40, v12 offset1:72\n"
    "ds_or_b32 v25, v21 offset:532\n"
    ".Lxd_np:\n"
    "ds_read2_b32 v[26:27], v41 offset1:1\n"
    "ds_read_b32 v0, v41 offset:16\n"
    "ds_read_b32 v23, v41 offset:24\n"
    "s_load_dword s26, s[68:69], 0\n"
    "v_add_u32 v25, s24, v25\n"
    "ds_read_b128 v[92:95], v42 offset:0\n"
    "ds_read_b128 v[96:99], v42 offset:1024\n"
    "ds_read_b128 v[100:103], v42 offset:2048\n"
    "ds_read_b128 v[104:107], v42 offset:3072\n"
    "ds_read_b128 v[108:111], v42 offset:4096\n"
    "ds_read_b128 v[112:115], v42 offset:5120\n"
    "ds_read_b128 v[116:119], v42 offset:6144\n"
    "ds_read_b128 v[120:123], v42 offset:7168\n"
    "ds_read_b128 v[124:127], v42 offset:8192\n"
    "v_pk_fma_f32 v[76:77], v[76:77], v[60:61], v[60:61]\n"
    "v_pk_fma_f32 v[78:79], v[78:79], v[60:61], v[60:61]\n"
    "v_pk_fma_f32 v[80:81], v[80:81], v[60:61], v[60:61]\n"
    "v_pk_fma_f32 v[82:83], v[82:83], v[60:61], v[60:61]\n"
    "v_rcp_f32 v76, v76\n"
    "v_rcp_f32 v77, v77\n"
    "v_rcp_f32 v78, v78\n"
    "v_rcp_f32 v79, v79\n"
    "v_rcp_f32 v80, v80\n"
    "v_rcp_f32 v81, v81\n"
    "v_rcp_f32 v82, v82\n"
    "v_rcp_f32 v83, v83\n"
    "v_pk_mul_f32 v[84:85], v[192:193], v[76:77]\n"
    "v_pk_mul_f32 v[86:87], v[196:197], v[78:79]\n"
    "v_pk_mul_f32 v[88:89], v[200:201], v[80:81]\n"
    "v_pk_mul_f32 v[90:91], v[204:205], v[82:83]\n"
    "s_cmp_eq_u32 s22, 3\n"
    "s_cbranch_scc1 .Lxd_on6\n"
    ".Lxd_on6r:\n"
    "s_setprio 3\n"
    "v_pk_mul_f32 v[76:77], v[194:195], v[194:195]\n"
    "v_pk_mul_f32 v[78:79], v[198:199], v[198:199]\n"
    "v_pk_mul_f32 v[80:81], v[202:203], v[202:203]\n"
    "v_pk_mul_f32 v[82:83], v[206:207], v[206:207]\n"
    "v_pk_fma_f32 v[76:77], v[76:77], s[64:65], s[64:65] op_sel:[0,0,1] op_sel_hi:[1,0,1]\n"
    "v_pk_fma_f32 v[78:79], v[78:79], s[64:65], s[64:65] op_sel:[0,0,1] op_sel_hi:[1,0,1]\n"
    "v_pk_fma_f32 v[80:81], v[80:81], s[64:65], s[64:65] op_sel:[0,0,1] op_sel_hi:[1,0,1]\n"
    "v_pk_fma_f32 v[82:83], v[82:83], s[64:65], s[64:65] op_sel:[0,0,1] op_sel_hi:[1,0,1]\n"
    "v_pk_mul_f32 v[76:77], v[76:77], v[194:195]\n"
    "v_pk_mul_f32 v[78:79], v[78:79], v[198:199]\n"
    "v_pk_mul_f32 v[80:81], v[80:81], v[202:203]\n"
    "v_pk_mul_f32 v[82:83], v[82:83], v[206:207]\n"
    "v_exp_f32 v76, v76\n"
    "v_exp_f32 v77, v77\n"
    "v_exp_f32 v78, v78\n"
    "v_exp_f32 v79, v79\n"
    "v_exp_f32 v80, v80\n"
    "v_exp_f32 v81, v81\n"
    "v_exp_f32 v82, v82\n"
    "v_exp_f32 v83, v83\n"
    "v_pk_fma_f32 v[76:77], v[76:77], v[62:63], v[62:63]\n"
    "v_pk_fma_f32 v[78:79], v[78:79], v[62:63], v[62:63]\n"
    "v_pk_fma_f32 v[80:81], v[80:81], v[62:63], v[62:63]\n"
    "v_pk_fma_f32 v[82:83], v[82:83], v[62:63], v[62:63]\n"
    "v_rcp_f32 v76, v76\n"
    "v_rcp_f32 v77, v77\n"
    "v_rcp_f32 v78, v78\n"
    "v_rcp_f32 v79, v79\n"
    "v_rcp_f32 v80, v80\n"
    "v_rcp_f32 v81, v81\n"
    "v_rcp_f32 v82, v82\n"
    "v_rcp_f32 v83, v83\n"
    "v_pk_fma_f32 v[84:85], v[194:195], v[76:77], v[84:85]\n"
    "v_pk_fma_f32 v[86:87], v[198:199], v[78:79], v[86:87]\n"
    "v_pk_fma_f32 v[88:89], v[202:203], v[80:81], v[88:89]\n"
    "v_pk_fma_f32 v[90:91], v[206:207], v[82:83], v[90:91]\n"
    "ds_read_b32 v22, v13 offset:512\n"
    "v_pk_mul_f32 v[76:77], v[208:209], v[208:209]\n"
    "v_pk_mul_f32 v[78:79], v[212:213], v[212:213]\n"
    "v_pk_mul_f32 v[80:81], v[216:217], v[216:217]\n"
    "v_pk_mul_f32 v[82:83], v[220:221], v[220:221]\n"
    "v_pk_fma_f32 v[76:77], v[76:77], s[64:65], s[64:65] op_sel:[0,0,1] op_sel_hi:[1,0,1]\n"
    "v_pk_fma_f32 v[78:79], v[78:79], s[64:65], s[64:65] op_sel:[0,0,1] op_sel_hi:[1,0,1]\n"
    "v_pk_fma_f32 v[80:81], v[80:81], s[64:65], s[64:65] op_sel:[0,0,1] op_sel_hi:[1,0,1]\n"
    "v_pk_fma_f32 v[82:83], v[82:83], s[64:65], s[64:65] op_sel:[0,0,1] op_sel_hi:[1,0,1]\n"
    "v_pk_mul_f32 v[76:77], v[76:77], v[208:209]\n"
    "v_pk_mul_f32 v[78:79], v[78:79], v[212:213]\n"
    "v_pk_mul_f32 v[80:81], v[80:81], v[216:217]\n"
    "v_pk_mul_f32 v[82:83], v[82:83], v[220:221]\n"
    "v_exp_f32 v76, v76\n"
    "v_exp_f32 v77, v77\n"
    "ds_read_b128 v[128:131], v42 offset:9216\n"
    "v_exp_f32 v78, v78\n"
    "v_exp_f32 v79, v79\n"
    "v_exp_f32 v80, v80\n"
    "ds_read_b128 v[132:135], v42 offset:10240\n"
    "v_exp_f32 v81, v81\n"
    "v_exp_f32 v82, v82\n"
    "v_exp_f32 v83, v83\n"
    "ds_read_b128 v[136:139], v42 offset:11264\n"
    "v_pk_fma_f32 v[76:77], v[76:77], v[64:65], v[64:65]\n"
    "v_pk_fma_f32 v[78:79], v[78:79], v[64:65], v[64:65]\n"
    "v_pk_fma_f32 v[80:81], v[80:81], v[64:65], v[64:65]\n"
    "v_pk_fma_f32 v[82:83], v[82:83], v[64:65], v[64:65]\n"
    "v_rcp_f32 v76, v76\n"
    "v_rcp_f32 v77, v77\n"
    "ds_read_b128 v[140:143], v42 offset:12288\n"
    "v_rcp_f32 v78, v78\n"
    "v_rcp_f32 v79, v79\n"
    "v_rcp_f32 v80, v80\n"
    "ds_read_b128 v[144:147], v42 offset:13312\n"
    "v_rcp_f32 v81, v81\n"
    "v_rcp_f32 v82, v82\n"
    "v_rcp_f32 v83, v83\n"
    "ds_read_b128 v[148:151], v42 offset:14336\n"
    "v_pk_fma_f32 v[84:85], v[208:209], v[76:77], v[84:85]\n"
    "v_pk_fma_f32 v[86:87], v[212:213], v[78:79], v[86:87]\n"
    "v_pk_fma_f32 v[88:89], v[216:217], v[80:81], v[88:89]\n"
    "v_pk_fma_f32 v[90:91], v[220:221], v[82:83], v[90:91]\n"
    "s_waitcnt lgkmcnt(6)\n"
    "v_readfirstlane_b32 s45, v22\n"
    "s_cmp_lt_i32 s45, 16\n"
    "s_cbranch_scc1 .Lxd_ck7\n"
    ".Lxd_ck7r:\n"
    "ds_read_b64_tr_b16 v[44:45], v9 offset:0\n"
    "ds_read_b64_tr_b16 v[46:47], v10 offset:0\n"
    "ds_read_b64_tr_b16 v[48:49], v7 offset:0\n"
    "ds_read_b64_tr_b16 v[50:51], v8 offset:576\n"
    "ds_read_b64_tr_b16 v[52:53], v8 offset:1152\n"
    "ds_read_b64_tr_b16 v[54:55], v8 offset:1728\n"
    "ds_read_b64_tr_b16 v[56:57], v8 offset:2304\n"
    "ds_read_b64_tr_b16 v[58:59], v8 offset:2880\n"
    "v_readfirstlane_b32 s28, v26\n"
    "v_readfirstlane_b32 s29, v27\n"
    "s_setprio 2\n"
    "v_pk_mul_f32 v[76:77], v[210:211], v[210:211]\n"
    "v_pk_mul_f32 v[78:79], v[214:215], v[214:215]\n"
    "v_pk_mul_f32 v[80:81], v[218:219], v[218:219]\n"
    "v_pk_mul_f32 v[82:83], v[222:223], v[222:223]\n"
    "v_pk_fma_f32 v[76:77], v[76:77], s[64:65], s[64:65] op_sel:[0,0,1] op_sel_hi:[1,0,1]\n"
    "v_pk_fma_f32 v[78:79], v[78:79], s[64:65], s[64:65] op_sel:[0,0,1] op_sel_hi:[1,0,1]\n"
    "v_pk_fma_f32 v[80:81], v[80:81], s[64:65], s[64:65] op_sel:[0,0,1] op_sel_hi:[1,0,1]\n"
    "v_pk_fma_f32 v[82:83], v[82:83], s[64:65], s[64:65] op_sel:[0,0,1] op_sel_hi:[1,0,1]\n"
    "v_pk_mul_f32 v[76:77], v[76:77], v[210:211]\n"
    "v_pk_mul_f32 v[78:79], v[78:79], v[214:215]\n"
    "v_pk_mul_f32 v[80:81], v[80:81], v[218:219]\n"
    "v_pk_mul_f32 v[82:83], v[82:83], v[222:223]\n"
    "v_exp_f32 v76, v76\n"
    "v_exp_f32 v77, v77\n"
    "ds_read_b128 v[152:155], v42 offset:15360\n"
    "v_exp_f32 v78, v78\n"
    "v_exp_f32 v79, v79\n"
    "v_exp_f32 v80, v80\n"
    "ds_read_b128 v[156:159], v42 offset:16384\n"
    "v_exp_f32 v81, v81\n"
    "v_exp_f32 v82, v82\n"
    "v_exp_f32 v83, v83\n"
    "ds_read_b128 v[160:163], v42 offset:17408\n"
    "v_pk_fma_f32 v[76:77], v[76:77], v[66:67], v[66:67]\n"
    "v_pk_fma_f32 v[78:79], v[78:79], v[66:67], v[66:67]\n"
    "v_pk_fma_f32 v[80:81], v[80:81], v[66:67], v[66:67]\n"
    "v_pk_fma_f32 v[82:83], v[82:83], v[66:67], v[66:67]\n"
    "v_rcp_f32 v76, v76\n"
    "v_rcp_f32 v77, v77\n"
    "ds_read_b128 v[164:167], v42 offset:18432\n"
    "v_rcp_f32 v78, v78\n"
    "v_rcp_f32 v79, v79\n"
    "v_rcp_f32 v80, v80\n"
    "ds_read_b128 v[168:171], v42 offset:19456\n"
    "v_rcp_f32 v81, v81\n"
    "v_rcp_f32 v82, v82\n"
    "v_rcp_f32 v83, v83\n"
    "ds_read_b128 v[172:175], v42 offset:20480\n"
    "v_pk_fma_f32 v[84:85], v[210:211], v[76:77], v[84:85]\n"
    "v_pk_fma_f32 v[86:87], v[214:215], v[78:79], v[86:87]\n"
    "v_pk_fma_f32 v[88:89], v[218:219], v[80:81], v[88:89]\n"
    "v_pk_fma_f32 v[90:91], v[222:223], v[82:83], v[90:91]\n"
    "s_cmp_eq_u32 s22, 3\n"
    "s_cbranch_scc1 .Lxd_on9\n"
    ".Lxd_on9r:\n"
    "s_cmp_eq_u32 s22, 0\n"
    "s_cbranch_scc1 .Lxd_on10\n"
    ".Lxd_on10r:\n"
    "v_pk_mul_f32 v[76:77], v[224:225], v[224:225]\n"
    "v_pk_mul_f32 v[78:79], v[228:229], v[228:229]\n"
    "v_pk_mul_f32 v[80:81], v[232:233], v[232:233]\n"
    "v_pk_mul_f32 v[82:83], v[236:237], v[236:237]\n"
    "v_pk_fma_f32 v[76:77], v[76:77], s[64:65], s[64:65] op_sel:[0,0,1] op_sel_hi:[1,0,1]\n"
    "v_pk_fma_f32 v[78:79], v[78:79], s[64:65], s[64:65] op_sel:[0,0,1] op_sel_hi:[1,0,1]\n"
    "v_pk_fma_f32 v[80:81], v[80:81], s[64:65], s[64:65] op_sel:[0,0,1] op_sel_hi:[1,0,1]\n"
    "v_pk_fma_f32 v[82:83], v[82:83], s[64:65], s[64:65] op_sel:[0,0,1] op_sel_hi:[1,0,1]\n"
    "v_pk_mul_f32 v[76:77], v[76:77], v[224:225]\n"
    "v_pk_mul_f32 v[78:79], v[78:79], v[228:229]\n"
    "v_pk_mul_f32 v[80:81], v[80:81], v[232:233]\n"
    "v_pk_mul_f32 v[82:83], v[82:83], v[236:237]\n"
    "v_exp_f32 v76, v76\n"
    "v_exp_f32 v77, v77\n"
    "ds_read_b128 v[176:179], v42 offset:21504\n"
    "v_exp_f32 v78, v78\n"
    "v_exp_f32 v79, v79\n"
    "v_exp_f32 v80, v80\n"
    "ds_read_b128 v[180:183], v42 offset:22528\n"
    "v_exp_f32 v81, v81\n"
    "v_exp_f32 v82, v82\n"
    "v_exp_f32 v83, v83\n"
    "ds_read_b128 v[184:187], v42 offset:23552\n"
    "v_pk_fma_f32 v[76:77], v[76:77], v[68:69], v[68:69]\n"
    "v_pk_fma_f32 v[78:79], v[78:79], v[68:69], v[68:69]\n"
    "v_pk_fma_f32 v[80:81], v[80:81], v[68:69], v[68:69]\n"
    "v_pk_fma_f32 v[82:83], v[82:83], v[68:69], v[68:69]\n"
    "v_rcp_f32 v76, v76\n"
    "v_rcp_f32 v77, v77\n"
    "ds_read_b128 v[188:191], v42 offset:24576\n"
    "v_rcp_f32 v78, v78\n"
    "v_rcp_f32 v79, v79\n"
    "v_rcp_f32 v80, v80\n"
    "ds_read_b128 v[192:195], v42 offset:25600\n"
    "v_rcp_f32 v81, v81\n"
    "v_rcp_f32 v82, v82\n"
    "v_rcp_f32 v83, v83\n"
    "ds_read_b128 v[196:199], v42 offset:26624\n"
    "v_pk_fma_f32 v[84:85], v[224:225], v[76:77], v[84:85]\n"
    "v_pk_fma_f32 v[86:87], v[228:229], v[78:79], v[86:87]\n"
    "v_pk_fma_f32 v[88:89], v[232:233], v[80:81], v[88:89]\n"
    "v_pk_fma_f32 v[90:91], v[236:237], v[82:83], v[90:91]\n"
    "s_setprio 1\n"
    "v_pk_mul_f32 v[76:77], v[226:227], v[226:227]\n"
    "v_pk_mul_f32 v[78:79], v[230:231], v[230:231]\n"
    "v_pk_mul_f32 v[80:81], v[234:235], v[234:235]\n"
    "v_pk_mul_f32 v[82:83], v[238:239], v[238:239]\n"
    "v_pk_fma_f32 v[76:77], v[76:77], s[64:65], s[64:65] op_sel:[0,0,1] op_sel_hi:[1,0,1]\n"
    "v_pk_fma_f32 v[78:79], v[78:79], s[64:65], s[64:65] op_sel:[0,0,1] op_sel_hi:[1,0,1]\n"
    "v_pk_fma_f32 v[80:81], v[80:81], s[64:65], s[64:65] op_sel:[0,0,1] op_sel_hi:[1,0,1]\n"
    "v_pk_fma_f32 v[82:83], v[82:83], s[64:65], s[64:65] op_sel:[0,0,1] op_sel_hi:[1,0,1]\n"
    "v_pk_mul_f32 v[76:77], v[76:77], v[226:227]\n"
    "v_pk_mul_f32 v[78:79], v[78:79], v[230:231]\n"
    "v_pk_mul_f32 v[80:81], v[80:81], v[234:235]\n"
    "v_pk_mul_f32 v[82:83], v[82:83], v[238:239]\n"
    "v_exp_f32 v76, v76\n"
    "v_exp_f32 v77, v77\n"
    "ds_read_b128 v[200:203], v42 offset:27648\n"
    "v_exp_f32 v78, v78\n"
    "v_exp_f32 v79, v79\n"
    "v_exp_f32 v80, v80\n"
    "ds_read_b128 v[204:207], v42 offset:28672\n"
    "v_exp_f32 v81, v81\n"
    "v_exp_f32 v82, v82\n"
    "v_exp_f32 v83, v83\n"
    "ds_read_b128 v[208:211], v42 offset:29696\n"
    "v_pk_fma_f32 v[76:77], v[76:77], v[70:71], v[70:71]\n"
    "v_pk_fma_f32 v[78:79], v[78:79], v[70:71], v[70:71]\n"
    "v_pk_fma_f32 v[80:81], v[80:81], v[70:71], v[70:71]\n"
    "v_pk_fma_f32 v[82:83], v[82:83], v[70:71], v[70:71]\n"
    "v_rcp_f32 v76, v76\n"
    "v_rcp_f32 v77, v77\n"
    "ds_read_b128 v[212:215], v42 offset:30720\n"
    "v_rcp_f32 v78, v78\n"
    "v_rcp_f32 v79, v79\n"
    "v_rcp_f32 v80, v80\n"
    "ds_read_b128 v[216:219], v42 offset:31744\n"
    "v_rcp_f32 v81, v81\n"
    "v_rcp_f32 v82, v82\n"
    "v_rcp_f32 v83, v83\n"
    "ds_read_b128 v[220:223], v42 offset:32768\n"
    "v_pk_fma_f32 v[84:85], v[226:227], v[76:77], v[84:85]\n"
    "v_pk_fma_f32 v[86:87], v[230:231], v[78:79], v[86:87]\n"
    "v_pk_fma_f32 v[88:89], v[234:235], v[80:81], v[88:89]\n"
    "v_pk_fma_f32 v[90:91], v[238:239], v[82:83], v[90:91]\n"
    "v_pk_mul_f32 v[76:77], v[240:241], v[240:241]\n"
    "v_pk_mul_f32 v[78:79], v[244:245], v[244:245]\n"
    "v_pk_mul_f32 v[80:81], v[248:249], v[248:249]\n"
    "v_pk_mul_f32 v[82:83], v[252:253], v[252:253]\n"
    "v_pk_fma_f32 v[76:77], v[76:77], s[64:65], s[64:65] op_sel:[0,0,1] op_sel_hi:[1,0,1]\n"
    "v_pk_fma_f32 v[78:79], v[78:79], s[64:65], s[64:65] op_sel:[0,0,1] op_sel_hi:[1,0,1]\n"
    "v_pk_fma_f32 v[80:81], v[80:81], s[64:65], s[64:65] op_sel:[0,0,1] op_sel_hi:[1,0,1]\n"
    "v_pk_fma_f32 v[82:83], v[82:83], s[64:65], s[64:65] op_sel:[0,0,1] op_sel_hi:[1,0,1]\n"
    "v_pk_mul_f32 v[76:77], v[76:77], v[240:241]\n"
    "v_pk_mul_f32 v[78:79], v[78:79], v[244:245]\n"
    "v_pk_mul_f32 v[80:81], v[80:81], v[248:249]\n"
    "v_pk_mul_f32 v[82:83], v[82:83], v[252:253]\n"
    "v_exp_f32 v76, v76\n"
    "v_exp_f32 v77, v77\n"
    "v_exp_f32 v78, v78\n"
    "v_exp_f32 v79, v79\n"
    "v_exp_f32 v80, v80\n"
    "v_exp_f32 v81, v81\n"
    "v_exp_f32 v82, v82\n"
    "v_exp_f32 v83, v83\n"
    "v_pk_fma_f32 v[76:77], v[76:77], v[72:73], v[72:73]\n"
    "v_pk_fma_f32 v[78:79], v[78:79], v[72:73], v[72:73]\n"
    "v_pk_fma_f32 v[80:81], v[80:81], v[72:73], v[72:73]\n"
    "v_pk_fma_f32 v[82:83], v[82:83], v[72:73], v[72:73]\n"
    "v_rcp_f32 v76, v76\n"
    "v_rcp_f32 v77, v77\n"
    "v_rcp_f32 v78, v78\n"
    "v_rcp_f32 v79, v79\n"
    "v_rcp_f32 v80, v80\n"
    "v_rcp_f32 v81, v81\n"
    "v_rcp_f32 v82, v82\n"
    "v_rcp_f32 v83, v83\n"
    "v_pk_fma_f32 v[84:85], v[240:241], v[76:77], v[84:85]\n"
    "v_pk_fma_f32 v[86:87], v[244:245], v[78:79], v[86:87]\n"
    "v_pk_fma_f32 v[88:89], v[248:249], v[80:81], v[88:89]\n"
    "v_pk_fma_f32 v[90:91], v[252:253], v[82:83], v[90:91]\n"
    "s_cmp_eq_u32 s22, 3\n"
    "s_cbranch_scc1 .Lxd_on11\n"
    ".Lxd_on11r:\n"
    "s_cmp_eq_u32 s22, 0\n"
    "s_cbranch_scc1 .Lxd_on13\n"
    ".Lxd_on13r:\n"
    "s_cmp_eq_u32 s22, 1\n"
    "s_cbranch_scc1 .Lxd_on14\n"
    ".Lxd_on14r:\n"
    "s_setprio 0\n"
    "v_pk_mul_f32 v[76:77], v[242:243], v[242:243]\n"
    "v_pk_mul_f32 v[78:79], v[246:247], v[246:247]\n"
    "v_pk_mul_f32 v[80:81], v[250:251], v[250:251]\n"
    "v_pk_mul_f32 v[82:83], v[254:255], v[254:255]\n"
    "v_pk_fma_f32 v[76:77], v[76:77], s[64:65], s[64:65] op_sel:[0,0,1] op_sel_hi:[1,0,1]\n"
    "v_pk_fma_f32 v[78:79], v[78:79], s[64:65], s[64:65] op_sel:[0,0,1] op_sel_hi:[1,0,1]\n"
    "v_pk_fma_f32 v[80:81], v[80:81], s[64:65], s[64:65] op_sel:[0,0,1] op_sel_hi:[1,0,1]\n"
    "v_pk_fma_f32 v[82:83], v[82:83], s[64:65], s[64:65] op_sel:[0,0,1] op_sel_hi:[1,0,1]\n"
    "v_pk_mul_f32 v[76:77], v[76:77], v[242:243]\n"
    "v_pk_mul_f32 v[78:79], v[78:79], v[246:247]\n"
    "v_pk_mul_f32 v[80:81], v[80:81], v[250:251]\n"
    "v_pk_mul_f32 v[82:83], v[82:83], v[254:255]\n"
    "v_exp_f32 v76, v76\n"
    "v_exp_f32 v77, v77\n"
    "v_exp_f32 v78, v78\n"
    "v_exp_f32 v79, v79\n"
    "v_exp_f32 v80, v80\n"
    "v_exp_f32 v81, v81\n"
    "v_exp_f32 v82, v82\n"
    "v_exp_f32 v83, v83\n"
    "v_pk_fma_f32 v[76:77], v[76:77], v[74:75], v[74:75]\n"
    "v_pk_fma_f32 v[78:79], v[78:79], v[74:75], v[74:75]\n"
    "v_pk_fma_f32 v[80:81], v[80:81], v[74:75], v[74:75]\n"
    "v_pk_fma_f32 v[82:83], v[82:83], v[74:75], v[74:75]\n"
    "v_rcp_f32 v76, v76\n"
    "v_rcp_f32 v77, v77\n"
    "v_rcp_f32 v78, v78\n"
    "v_rcp_f32 v79, v79\n"
    "v_rcp_f32 v80, v80\n"
    "v_rcp_f32 v81, v81\n"
    "v_rcp_f32 v82, v82\n"
    "v_rcp_f32 v83, v83\n"
    "v_pk_fma_f32 v[84:85], v[242:243], v[76:77], v[84:85]\n"
    "v_pk_fma_f32 v[86:87], v[246:247], v[78:79], v[86:87]\n"
    "v_pk_fma_f32 v[88:89], v[250:251], v[80:81], v[88:89]\n"
    "v_pk_fma_f32 v[90:91], v[254:255], v[82:83], v[90:91]\n"
    "v_add_f32 v84, v84, v85\n"
    "v_add_f32 v86, v86, v87\n"
    "v_add_f32 v88, v88, v89\n"
    "v_add_f32 v90, v90, v91\n"
    "s_waitcnt lgkmcnt(15)\n"
    "v_pk_mul_f16 v60, v44, s45 op_sel:[0,1]\n"
    "v_pk_mul_f16 v61, v45, s45 op_sel:[0,1]\n"
    "v_permlane32_swap_b32 v84, v88\n"
    "v_permlane32_swap_b32 v86, v90\n"
    "v_add_f32 v84, v84, v88\n"
    "v_add_f32 v86, v86, v90\n"
    "v_pk_mul_f16 v62, v46, s45 op_sel:[0,1]\n"
    "v_pk_mul_f16 v63, v47, s45 op_sel:[0,1]\n"
    "v_permlane16_swap_b32 v84, v86\n"
    "v_add_f32 v84, v84, v86\n"
    "v_and_b32 v26, -2, v84\n"
    "s_waitcnt lgkmcnt(0)\n"
    "ds_wrxchg_rtn_b32 v27, v11, v26\n"
    "ds_read_b128 v[224:227], v42 offset:33792\n"
    "ds_read_b128 v[228:231], v42 offset:34816\n"
    "ds_read_b128 v[232:235], v42 offset:35840\n"
    "ds_read_b128 v[236:239], v42 offset:36864\n"
    "ds_read_b128 v[240:243], v42 offset:37888\n"
    "ds_read_b128 v[244:247], v42 offset:38912\n"
    "ds_read_b128 v[248:251], v42 offset:39936\n"
    "ds_read_b128 v[252:255], v42 offset:40960\n"
    "v_pk_mul_f16 v64, v44, v46\n"
    "v_pk_mul_f16 v65, v45, v47\n"
    "v_pk_mul_f16 v66, v48, v48 op_sel_hi:[0,1]\n"
    "v_pk_mul_f16 v67, v48, v49 op_sel_hi:[0,1]\n"
    "v_mov_b64 v[88:89], 0\n"
    "v_mov_b64 v[90:91], 0\n"
    "s_mov_b32 s46, -1\n"
    ".Lxd_enter:\n"
    "s_cmp_eq_u32 s21, 0\n"
    "s_cselect_b64 vcc, 0, -1\n"
    "s_cbranch_scc1 .Lxd_pr\n"
    "s_setprio 1\n"
    ".Lxd_pr:\n"
    "s_cmp_lg_u32 s36, 0\n"
    "s_cbranch_scc1 .Lxd_el\n"
    "s_setpc_b64 s[10:11]\n"
    ".Lxd_el:\n"
    "s_cmp_eq_u32 s22, 0\n"
    "s_cbranch_scc1 .Lxd_pl1\n"
    "s_branch .Lxd_pl2\n"
    ".Lxd_ph0:\n"
    "v_smfmac_f32_16x16x64_f16 v[88:91], v[112:115], v[60:67], v2\n"
    "s_cbranch_vccnz .Lxd_pr15\n"
    "s_setprio 2\n"
    ".Lxd_pr15:\n"
    "v_pk_mul_f16 v68, v58, v58 op_sel_hi:[0,1]\n"
    "v_pk_mul_f16 v69, v58, v49 op_sel_hi:[0,1]\n"
    "v_mfma_f32_16x16x32_f16 v[84:87], v[108:111], v[64:67], 0\n"
    "v_fmac_f32 v11, v76, v80\n"
    "v_fmac_f32 v11, v77, v81\n"
    "v_mfma_f32_16x16x32_f16 v[84:87], v[96:99], v[60:63], v[84:87]\n"
    "v_fmac_f32 v11, v78, v82\n"
    "v_fma_f32 v10, v79, v83, v11\n"
    "v_mfma_f32_16x16x32_f16 v[80:83], v[92:95], v[60:63], 0\n"
    "s_waitcnt lgkmcnt(0)\n"
    "v_pk_mul_f16 v70, v48, v58 op_sel_hi:[0,1]\n"
    "v_pk_mul_f16 v71, v48, v59 op_sel_hi:[0,1]\n"
    "v_mfma_f32_16x16x32_f16 v[80:83], v[104:107], v[64:67], v[80:83]\n"
    "s_waitcnt vmcnt(0)\n"
    "v_pk_mul_f16 v72, v50, v58 op_sel_hi:[0,1]\n"
    "v_pk_mul_f16 v73, v50, v59 op_sel_hi:[0,1]\n"
    "v_mfma_f32_16x16x32_f16 v[76:79], v[100:103], v[64:67], 0\n"
    "s_cmp_lt_i32 s46, 0\n"
    "v_pk_mul_f16 v74, v52, v58 op_sel_hi:[0,1]\n"
    "v_pk_mul_f16 v75, v52, v59 op_sel_hi:[0,1]\n"
    "v_mfma_f32_16x16x32_f16 v[76:79], v[116:119], v[68:71], v[76:79]\n"
    "s_cbranch_scc1 .Lxd_ht18\n"
    ".Lxd_ht18r:\n"
    "v_pk_mul_f16 v60, v54, v58 op_sel_hi:[0,1]\n"
    "v_pk_mul_f16 v61, v54, v59 op_sel_hi:[0,1]\n"
    "v_mfma_f32_16x16x32_f16 v[76:79], v[128:131], v[72:75], v[76:79]\n"
    "v_pk_mul_f16 v62, v56, v58 op_sel_hi:[0,1]\n"
    "v_readfirstlane_b32 s40, v24\n"
    "v_mfma_f32_16x16x32_f16 v[80:83], v[132:135], v[72:75], v[80:83]\n"
    "s_or_b32 s40, s40, s44\n"
    "v_pk_mul_f16 v63, v56, v59 op_sel_hi:[0,1]\n"
    "v_permlane32_swap_b32 v7, v8\n"
    "v_mfma_f32_16x16x32_f16 v[80:83], v[120:123], v[68:71], v[80:83]\n"
    "s_lshl1_add_u32 s41, s40, s54\n"
    "v_pk_mul_f16 v64, v56, v56 op_sel_hi:[0,1]\n"
    "v_permlane32_swap_b32 v9, v10\n"
    "v_mfma_f32_16x16x32_f16 v[84:87], v[124:127], v[68:71], v[84:87]\n"
    "s_cmpk_lt_u32 s40, 256\n"
    "v_pk_mul_f16 v65, v56, v49 op_sel_hi:[0,1]\n"
    "v_mul_f32 v7, v0, v7\n"
    "v_mfma_f32_16x16x32_f16 v[84:87], v[136:139], v[72:75], v[84:87]\n"
    "s_cselect_b32 s41, s41, 2\n"
    "v_pk_mul_f16 v66, v48, v56 op_sel_hi:[0,1]\n"
    "v_mov_b32 v12, s41\n"
    "v_smfmac_f32_16x16x64_f16 v[88:91], v[140:143], v[68:75], v2\n"
    "s_cbranch_scc0 .Lxd_tf17\n"
    ".Lxd_tf17r:\n"
    "v_pk_mul_f16 v67, v48, v57 op_sel_hi:[0,1]\n"
    "v_mul_f32 v9, v0, v9\n"
    "v_mfma_f32_16x16x32_f16 v[76:79], v[144:147], v[60:63], v[76:79]\n"
    "s_mul_i32 s41, s41, 52\n"
    "v_pk_mul_f16 v68, v50, v56 op_sel_hi:[0,1]\n"
    "v_pk_mul_f16 v69, v50, v57 op_sel_hi:[0,1]\n"
    "v_mfma_f32_16x16x32_f16 v[76:79], v[156:159], v[64:67], v[76:79]\n"
    "s_add_u32 s38, s4, s41\n"
    "v_pk_mul_f16 v70, v52, v56 op_sel_hi:[0,1]\n"
    "v_fmac_f32 v7, v23, v8\n"
    "v_mfma_f32_16x16x32_f16 v[80:83], v[160:163], v[64:67], v[80:83]\n"
    "s_addc_u32 s39, s5, 0\n"
    "v_pk_mul_f16 v71, v52, v57 op_sel_hi:[0,1]\n"
    "v_fmac_f32 v9, v23, v10\n"
    "v_mfma_f32_16x16x32_f16 v[80:83], v[148:151], v[60:63], v[80:83]\n"
    "ds_read_b32 v22, v25 offset:516\n"
    "v_pk_mul_f16 v72, v54, v56 op_sel_hi:[0,1]\n"
    "v_permlane16_swap_b32 v7, v9\n"
    "v_mfma_f32_16x16x32_f16 v[84:87], v[152:155], v[60:63], v[84:87]\n"
    "global_load_dwordx3 v[36:38], v17, s[38:39] offset:-104\n"
    "v_pk_mul_f16 v73, v54, v57 op_sel_hi:[0,1]\n"
    "v_add_f32 v7, v7, v9\n"
    "v_mfma_f32_16x16x32_f16 v[84:87], v[164:167], v[64:67], v[84:87]\n"
    "global_load_dword v41, v18, s[38:39] offset:-8\n"
    "v_pk_mul_f16 v74, v54, v54 op_sel_hi:[0,1]\n"
    "v_add_f32_dpp v7, v7, v7 quad_perm:[1,0,3,2] row_mask:0xf bank_mask:0xf\n"
    "v_smfmac_f32_16x16x64_f16 v[88:91], v[168:171], v[60:67], v2\n"
    "ds_write_b32 v25, v12 offset:20\n"
    "v_pk_mul_f16 v75, v54, v49 op_sel_hi:[0,1]\n"
    "v_add_f32_dpp v7, v7, v7 quad_perm:[2,3,0,1] row_mask:0xf bank_mask:0xf\n"
    "v_mfma_f32_16x16x32_f16 v[76:79], v[172:175], v[68:71], v[76:79]\n"
    "v_pk_mul_f16 v60, v48, v54 op_sel_hi:[0,1]\n"
    "v_pk_mul_f16 v61, v48, v55 op_sel_hi:[0,1]\n"
    "v_mfma_f32_16x16x32_f16 v[76:79], v[184:187], v[72:75], v[76:79]\n"
    "s_waitcnt lgkmcnt(1)\n"
    "v_pk_mul_f16 v62, v50, v54 op_sel_hi:[0,1]\n"
    "v_readfirstlane_b32 s45, v22\n"
    "v_mfma_f32_16x16x32_f16 v[80:83], v[188:191], v[72:75], v[80:83]\n"
    "s_cmp_lt_i32 s45, 16\n"
    "v_pk_mul_f16 v63, v50, v55 op_sel_hi:[0,1]\n"
    "v_add_f32_dpp v7, v7, v7 row_half_mirror row_mask:0xf bank_mask:0xf\n"
    "v_mfma_f32_16x16x32_f16 v[80:83], v[176:179], v[68:71], v[80:83]\n"
    "s_cbranch_scc1 .Lxd_ck19\n"
    ".Lxd_ck19r:\n"
    "v_pk_mul_f16 v64, v52, v54 op_sel_hi:[0,1]\n"
    "v_add_f32_dpp v7, v7, v7 row_mirror row_mask:0xf bank_mask:0xf\n"
    "v_mfma_f32_16x16x32_f16 v[84:87], v[180:183], v[68:71], v[84:87]\n"
    "ds_read_b64_tr_b16 v[44:45], v5 offset:12288\n"
    "v_pk_mul_f16 v65, v52, v55 op_sel_hi:[0,1]\n"
    "v_mfma_f32_16x16x32_f16 v[84:87], v[192:195], v[72:75], v[84:87]\n"
    "ds_read_b64_tr_b16 v[46:47], v6 offset:12288\n"
    "v_pk_mul_f16 v66, v52, v52 op_sel_hi:[0,1]\n"
    "v_smfmac_f32_16x16x64_f16 v[88:91], v[196:199], v[68:75], v2\n"
    "ds_write_b32 v14, v7\n"
    "v_pk_mul_f16 v67, v52, v49 op_sel_hi:[0,1]\n"
    "v_add_u32 v14, 64, v14\n"
    "v_mfma_f32_16x16x32_f16 v[76:79], v[200:203], v[60:63], v[76:79]\n"
    "v_pk_mul_f16 v68, v48, v52 op_sel_hi:[0,1]\n"
    "v_pk_mul_f16 v69, v48, v53 op_sel_hi:[0,1]\n"
    "v_mfma_f32_16x16x32_f16 v[76:79], v[212:215], v[64:67], v[76:79]\n"
    "v_pk_mul_f16 v70, v50, v52 op_sel_hi:[0,1]\n"
    "v_mfma_f32_16x16x32_f16 v[80:83], v[216:219], v[64:67], v[80:83]\n"
    "s_cbranch_vccnz .Lxd_pr16\n"
    "s_setprio 0\n"
    ".Lxd_pr16:\n"
    "v_pk_mul_f16 v71, v50, v53 op_sel_hi:[0,1]\n"
    "v_mfma_f32_16x16x32_f16 v[80:83], v[204:207], v[60:63], v[80:83]\n"
    "v_pk_mul_f16 v72, v50, v50 op_sel_hi:[0,1]\n"
    "v_mfma_f32_16x16x32_f16 v[84:87], v[208:211], v[60:63], v[84:87]\n"
    "v_pk_mul_f16 v73, v50, v49 op_sel_hi:[0,1]\n"
    "v_pk_mul_f16 v74, v48, v50 op_sel_hi:[0,1]\n"
    "v_mfma_f32_16x16x32_f16 v[84:87], v[220:223], v[64:67], v[84:87]\n"
    "v_pk_mul_f16 v75, v48, v51 op_sel_hi:[0,1]\n"
    "v_smfmac_f32_16x16x64_f16 v[88:91], v[224:227], v[60:67], v2\n"
    "ds_read_b64_tr_b16 v[48:49], v3 offset:12288\n"
    "ds_read_b64_tr_b16 v[58:59], v4 offset:15168\n"
    "v_smfmac_f32_16x16x64_f16 v[88:91], v[252:255], v[68:75], v2\n"
    "s_waitcnt lgkmcnt(3)\n"
    "v_pk_mul_f16 v60, v44, s45 op_sel:[0,1]\n"
    "v_pk_mul_f16 v61, v45, s45 op_sel:[0,1]\n"
    "v_mfma_f32_16x16x32_f16 v[84:87], v[236:239], v[68:71], v[84:87]\n"
    "ds_read_b64_tr_b16 v[50:51], v4 offset:12864\n"
    "v_pk_mul_f16 v62, v46, s45 op_sel:[0,1]\n"
    "v_pk_mul_f16 v63, v47, s45 op_sel:[0,1]\n"
    "v_mfma_f32_16x16x32_f16 v[84:87], v[248:251], v[72:75], v[84:87]\n"
    "ds_read_b64_tr_b16 v[52:53], v4 offset:13440\n"
    "v_pk_mul_f16 v64, v44, v46\n"
    "v_pk_mul_f16 v65, v45, v47\n"
    "v_mfma_f32_16x16x32_f16 v[76:79], v[240:243], v[72:75], v[76:79]\n"
    "ds_read_b64_tr_b16 v[54:55], v4 offset:14016\n"
    "v_add_f32 v11, v88, v89\n"
    "v_add_f32 v13, v90, v91\n"
    "v_mfma_f32_16x16x32_f16 v[76:79], v[228:231], v[68:71], v[76:79]\n"
    "ds_read_b64_tr_b16 v[56:57], v4 offset:14592\n"
    "v_mov_b64 v[88:89], 0\n"
    "v_mov_b64 v[90:91], 0\n"
    "v_mfma_f32_16x16x32_f16 v[80:83], v[232:235], v[68:71], v[80:83]\n"
    "s_waitcnt lgkmcnt(4)\n"
    "v_pk_mul_f16 v66, v48, v48 op_sel_hi:[0,1]\n"
    "v_pk_mul_f16 v67, v48, v49 op_sel_hi:[0,1]\n"
    "v_mfma_f32_16x16x32_f16 v[80:83], v[244:247], v[72:75], v[80:83]\n"
    "s_cmp_lg_u32 s36, 0\n"
    "v_mul_f32 v11, v11, v13\n"
    "v_fmac_f32 v11, v84, v85\n"
    "v_fmac_f32 v11, v86, v87\n"
    "s_cbranch_scc1 .Lxd_pl1\n"
    ".Lxd_ph1:\n"
    "v_smfmac_f32_16x16x64_f16 v[88:91], v[112:115], v[60:67], v2\n"
    "s_cbranch_vccnz .Lxd_pr20\n"
    "s_setprio 2\n"
    ".Lxd_pr20:\n"
    "v_pk_mul_f16 v68, v58, v58 op_sel_hi:[0,1]\n"
    "v_pk_mul_f16 v69, v58, v49 op_sel_hi:[0,1]\n"
    "v_mfma_f32_16x16x32_f16 v[84:87], v[108:111], v[64:67], 0\n"
    "v_fmac_f32 v11, v76, v80\n"
    "v_fmac_f32 v11, v77, v81\n"
    "v_mfma_f32_16x16x32_f16 v[84:87], v[96:99], v[60:63], v[84:87]\n"
    "v_fmac_f32 v11, v78, v82\n"
    "v_fma_f32 v7, v79, v83, v11\n"
    "v_mfma_f32_16x16x32_f16 v[80:83], v[92:95], v[60:63], 0\n"
    "s_waitcnt lgkmcnt(0)\n"
    "v_pk_mul_f16 v70, v48, v58 op_sel_hi:[0,1]\n"
    "v_pk_mul_f16 v71, v48, v59 op_sel_hi:[0,1]\n"
    "v_mfma_f32_16x16x32_f16 v[80:83], v[104:107], v[64:67], v[80:83]\n"
    "s_waitcnt vmcnt(0)\n"
    "v_pk_mul_f16 v72, v50, v58 op_sel_hi:[0,1]\n"
    "v_pk_mul_f16 v73, v50, v59 op_sel_hi:[0,1]\n"
    "v_mfma_f32_16x16x32_f16 v[76:79], v[100:103], v[64:67], 0\n"
    "s_cmp_lg_u32 s46, 0\n"
    "v_pk_mul_f16 v74, v52, v58 op_sel_hi:[0,1]\n"
    "v_pk_mul_f16 v75, v52, v59 op_sel_hi:[0,1]\n"
    "v_mfma_f32_16x16x32_f16 v[76:79], v[116:119], v[68:71], v[76:79]\n"
    "s_cbranch_scc1 .Lxd_ht22\n"
    ".Lxd_ht22r:\n"
    "v_pk_mul_f16 v60, v54, v58 op_sel_hi:[0,1]\n"
    "v_pk_mul_f16 v61, v54, v59 op_sel_hi:[0,1]\n"
    "v_mfma_f32_16x16x32_f16 v[76:79], v[128:131], v[72:75], v[76:79]\n"
    "v_pk_mul_f16 v62, v56, v58 op_sel_hi:[0,1]\n"
    "v_lshl_or_b32 v36, v36, 7, v19\n"
    "v_mfma_f32_16x16x32_f16 v[80:83], v[132:135], v[72:75], v[80:83]\n"
    "global_load_dwordx4 v[28:31], v36, s[8:9]\n"
    "v_pk_mul_f16 v63, v56, v59 op_sel_hi:[0,1]\n"
    "v_lshl_or_b32 v37, v37, 7, v19\n"
    "v_mfma_f32_16x16x32_f16 v[80:83], v[120:123], v[68:71], v[80:83]\n"
    "global_load_dwordx4 v[32:35], v37, s[8:9]\n"
    "v_pk_mul_f16 v64, v56, v56 op_sel_hi:[0,1]\n"
    "v_lshl_or_b32 v41, v41, 7, v20\n"
    "v_mfma_f32_16x16x32_f16 v[84:87], v[124:127], v[68:71], v[84:87]\n"
    "global_load_dword v40, v41, s[8:9]\n"
    "v_pk_mul_f16 v65, v56, v49 op_sel_hi:[0,1]\n"
    "v_lshl_or_b32 v38, v38, 7, v19\n"
    "v_mfma_f32_16x16x32_f16 v[84:87], v[136:139], v[72:75], v[84:87]\n"
    "global_load_dwordx4 v[36:39], v38, s[8:9]\n"
    "v_pk_mul_f16 v66, v48, v56 op_sel_hi:[0,1]\n"
    "v_xor_b32 v15, 0x4000, v15\n"
    "v_smfmac_f32_16x16x64_f16 v[88:91], v[140:143], v[68:75], v2\n"
    "v_pk_mul_f16 v67, v48, v57 op_sel_hi:[0,1]\n"
    "v_xor_b32 v16, 0x4000, v16\n"
    "v_mfma_f32_16x16x32_f16 v[76:79], v[144:147], v[60:63], v[76:79]\n"
    "v_pk_mul_f16 v68, v50, v56 op_sel_hi:[0,1]\n"
    "v_pk_mul_f16 v69, v50, v57 op_sel_hi:[0,1]\n"
    "v_mfma_f32_16x16x32_f16 v[76:79], v[156:159], v[64:67], v[76:79]\n"
    "v_pk_mul_f16 v70, v52, v56 op_sel_hi:[0,1]\n"
    "v_xor_b32 v3, 0x4000, v3\n"
    "v_mfma_f32_16x16x32_f16 v[80:83], v[160:163], v[64:67], v[80:83]\n"
    "v_pk_mul_f16 v71, v52, v57 op_sel_hi:[0,1]\n"
    "v_xor_b32 v4, 0x4000, v4\n"
    "v_mfma_f32_16x16x32_f16 v[80:83], v[148:151], v[60:63], v[80:83]\n"
    "ds_read_b32 v22, v25 offset:520\n"
    "v_pk_mul_f16 v72, v54, v56 op_sel_hi:[0,1]\n"
    "v_xor_b32 v5, 0x4000, v5\n"
    "v_mfma_f32_16x16x32_f16 v[84:87], v[152:155], v[60:63], v[84:87]\n"
    "v_pk_mul_f16 v73, v54, v57 op_sel_hi:[0,1]\n"
    "v_xor_b32 v6, 0x4000, v6\n"
    "v_mfma_f32_16x16x32_f16 v[84:87], v[164:167], v[64:67], v[84:87]\n"
    "v_pk_mul_f16 v74, v54, v54 op_sel_hi:[0,1]\n"
    "v_smfmac_f32_16x16x64_f16 v[88:91], v[168:171], v[60:67], v2\n"
    "v_pk_mul_f16 v75, v54, v49 op_sel_hi:[0,1]\n"
    "v_mfma_f32_16x16x32_f16 v[76:79], v[172:175], v[68:71], v[76:79]\n"
    "v_pk_mul_f16 v60, v48, v54 op_sel_hi:[0,1]\n"
    "v_pk_mul_f16 v61, v48, v55 op_sel_hi:[0,1]\n"
    "v_mfma_f32_16x16x32_f16 v[76:79], v[184:187], v[72:75], v[76:79]\n"
    "s_waitcnt lgkmcnt(0)\n"
    "v_pk_mul_f16 v62, v50, v54 op_sel_hi:[0,1]\n"
    "v_readfirstlane_b32 s45, v22\n"
    "v_mfma_f32_16x16x32_f16 v[80:83], v[188:191], v[72:75], v[80:83]\n"
    "s_cmp_lt_i32 s45, 16\n"
    "v_pk_mul_f16 v63, v50, v55 op_sel_hi:[0,1]\n"
    "v_mfma_f32_16x16x32_f16 v[80:83], v[176:179], v[68:71], v[80:83]\n"
    "s_cbranch_scc1 .Lxd_ck23\n"
    ".Lxd_ck23r:\n"
    "v_pk_mul_f16 v64, v52, v54 op_sel_hi:[0,1]\n"
    "v_mfma_f32_16x16x32_f16 v[84:87], v[180:183], v[68:71], v[84:87]\n"
    "ds_read_b64_tr_b16 v[44:45], v5 offset:0\n"
    "v_pk_mul_f16 v65, v52, v55 op_sel_hi:[0,1]\n"
    "v_mfma_f32_16x16x32_f16 v[84:87], v[192:195], v[72:75], v[84:87]\n"
    "ds_read_b64_tr_b16 v[46:47], v6 offset:0\n"
    "v_pk_mul_f16 v66, v52, v52 op_sel_hi:[0,1]\n"
    "v_smfmac_f32_16x16x64_f16 v[88:91], v[196:199], v[68:75], v2\n"
    "v_pk_mul_f16 v67, v52, v49 op_sel_hi:[0,1]\n"
    "v_mfma_f32_16x16x32_f16 v[76:79], v[200:203], v[60:63], v[76:79]\n"
    "v_pk_mul_f16 v68, v48, v52 op_sel_hi:[0,1]\n"
    "v_pk_mul_f16 v69, v48, v53 op_sel_hi:[0,1]\n"
    "v_mfma_f32_16x16x32_f16 v[76:79], v[212:215], v[64:67], v[76:79]\n"
    "v_pk_mul_f16 v70, v50, v52 op_sel_hi:[0,1]\n"
    "v_mfma_f32_16x16x32_f16 v[80:83], v[216:219], v[64:67], v[80:83]\n"
    "s_cbranch_vccnz .Lxd_pr21\n"
    "s_setprio 0\n"
    ".Lxd_pr21:\n"
    "v_pk_mul_f16 v71, v50, v53 op_sel_hi:[0,1]\n"
    "v_mfma_f32_16x16x32_f16 v[80:83], v[204:207], v[60:63], v[80:83]\n"
    "v_pk_mul_f16 v72, v50, v50 op_sel_hi:[0,1]\n"
    "v_mfma_f32_16x16x32_f16 v[84:87], v[208:211], v[60:63], v[84:87]\n"
    "v_pk_mul_f16 v73, v50, v49 op_sel_hi:[0,1]\n"
    "v_pk_mul_f16 v74, v48, v50 op_sel_hi:[0,1]\n"
    "v_mfma_f32_16x16x32_f16 v[84:87], v[220:223], v[64:67], v[84:87]\n"
    "v_pk_mul_f16 v75, v48, v51 op_sel_hi:[0,1]\n"
    "v_smfmac_f32_16x16x64_f16 v[88:91], v[224:227], v[60:67], v2\n"
    "ds_read_b64_tr_b16 v[48:49], v3 offset:0\n"
    "ds_read_b64_tr_b16 v[58:59], v4 offset:2880\n"
    "v_smfmac_f32_16x16x64_f16 v[88:91], v[252:255], v[68:75], v2\n"
    "s_waitcnt lgkmcnt(2)\n"
    "v_pk_mul_f16 v60, v44, s45 op_sel:[0,1]\n"
    "v_pk_mul_f16 v61, v45, s45 op_sel:[0,1]\n"
    "v_mfma_f32_16x16x32_f16 v[84:87], v[236:239], v[68:71], v[84:87]\n"
    "ds_read_b64_tr_b16 v[50:51], v4 offset:576\n"
    "v_pk_mul_f16 v62, v46, s45 op_sel:[0,1]\n"
    "v_pk_mul_f16 v63, v47, s45 op_sel:[0,1]\n"
    "v_mfma_f32_16x16x32_f16 v[84:87], v[248:251], v[72:75], v[84:87]\n"
    "ds_read_b64_tr_b16 v[52:53], v4 offset:1152\n"
    "v_pk_mul_f16 v64, v44, v46\n"
    "v_pk_mul_f16 v65, v45, v47\n"
    "v_mfma_f32_16x16x32_f16 v[76:79], v[240:243], v[72:75], v[76:79]\n"
    "ds_read_b64_tr_b16 v[54:55], v4 offset:1728\n"
    "v_add_f32 v11, v88, v89\n"
    "v_add_f32 v13, v90, v91\n"
    "v_mfma_f32_16x16x32_f16 v[76:79], v[228:231], v[68:71], v[76:79]\n"
    "ds_read_b64_tr_b16 v[56:57], v4 offset:2304\n"
    "v_mov_b64 v[88:89], 0\n"
    "v_mov_b64 v[90:91], 0\n"
    "v_mfma_f32_16x16x32_f16 v[80:83], v[232:235], v[68:71], v[80:83]\n"
    "s_waitcnt lgkmcnt(4)\n"
    "v_pk_mul_f16 v66, v48, v48 op_sel_hi:[0,1]\n"
    "v_pk_mul_f16 v67, v48, v49 op_sel_hi:[0,1]\n"
    "v_mfma_f32_16x16x32_f16 v[80:83], v[244:247], v[72:75], v[80:83]\n"
    "v_mul_f32 v11, v11, v13\n"
    "v_fmac_f32 v11, v84, v85\n"
    "v_fmac_f32 v11, v86, v87\n"
    ".Lxd_ph2:\n"
    "v_smfmac_f32_16x16x64_f16 v[88:91], v[112:115], v[60:67], v2\n"
    "s_cbranch_vccnz .Lxd_pr24\n"
    "s_setprio 2\n"
    ".Lxd_pr24:\n"
    "v_pk_mul_f16 v68, v58, v58 op_sel_hi:[0,1]\n"
    "v_pk_mul_f16 v69, v58, v49 op_sel_hi:[0,1]\n"
    "v_mfma_f32_16x16x32_f16 v[84:87], v[108:111], v[64:67], 0\n"
    "v_fmac_f32 v11, v76, v80\n"
    "v_fmac_f32 v11, v77, v81\n"
    "v_mfma_f32_16x16x32_f16 v[84:87], v[96:99], v[60:63], v[84:87]\n"
    "s_movk_i32 s43, 15744\n"
    "v_fmac_f32 v11, v78, v82\n"
    "v_fma_f32 v8, v79, v83, v11\n"
    "v_mfma_f32_16x16x32_f16 v[80:83], v[92:95], v[60:63], 0\n"
    "s_waitcnt lgkmcnt(0)\n"
    "v_pk_mul_f16 v70, v48, v58 op_sel_hi:[0,1]\n"
    "v_pk_mul_f16 v71, v48, v59 op_sel_hi:[0,1]\n"
    "v_mfma_f32_16x16x32_f16 v[80:83], v[104:107], v[64:67], v[80:83]\n"
    "s_waitcnt vmcnt(0)\n"
    "v_pk_mul_f16 v72, v50, v58 op_sel_hi:[0,1]\n"
    "v_pk_mul_f16 v73, v50, v59 op_sel_hi:[0,1]\n"
    "v_mfma_f32_16x16x32_f16 v[76:79], v[100:103], v[64:67], 0\n"
    "s_cmp_lg_u32 s46, 0\n"
    "v_pk_mul_f16 v74, v52, v58 op_sel_hi:[0,1]\n"
    "v_pk_mul_f16 v75, v52, v59 op_sel_hi:[0,1]\n"
    "v_mfma_f32_16x16x32_f16 v[76:79], v[116:119], v[68:71], v[76:79]\n"
    "s_cbranch_scc1 .Lxd_ht26\n"
    ".Lxd_ht26r:\n"
    "v_pk_mul_f16 v60, v54, v58 op_sel_hi:[0,1]\n"
    "v_pk_mul_f16 v61, v54, v59 op_sel_hi:[0,1]\n"
    "v_mfma_f32_16x16x32_f16 v[76:79], v[128:131], v[72:75], v[76:79]\n"
    "v_pk_mul_f16 v62, v56, v58 op_sel_hi:[0,1]\n"
    "v_pk_maximum3_f16 v21, v28, v28, s43 op_sel_hi:[1,1,0] neg_lo:[0,1,0] neg_hi:[0,1,0]\n"
    "v_pk_maximum3_f16 v21, v29, v29, v21 neg_lo:[0,1,0] neg_hi:[0,1,0]\n"
    "v_mfma_f32_16x16x32_f16 v[80:83], v[132:135], v[72:75], v[80:83]\n"
    "v_pk_mul_f16 v63, v56, v59 op_sel_hi:[0,1]\n"
    "v_pk_maximum3_f16 v21, v30, v30, v21 neg_lo:[0,1,0] neg_hi:[0,1,0]\n"
    "v_mfma_f32_16x16x32_f16 v[80:83], v[120:123], v[68:71], v[80:83]\n"
    "v_pk_mul_f16 v64, v56, v56 op_sel_hi:[0,1]\n"
    "v_pk_maximum3_f16 v21, v31, v31, v21 neg_lo:[0,1,0] neg_hi:[0,1,0]\n"
    "v_mfma_f32_16x16x32_f16 v[84:87], v[124:127], v[68:71], v[84:87]\n"
    "v_pk_mul_f16 v65, v56, v49 op_sel_hi:[0,1]\n"
    "v_pk_maximum3_f16 v21, v32, v32, v21 neg_lo:[0,1,0] neg_hi:[0,1,0]\n"
    "v_mfma_f32_16x16x32_f16 v[84:87], v[136:139], v[72:75], v[84:87]\n"
    "v_pk_mul_f16 v66, v48, v56 op_sel_hi:[0,1]\n"
    "v_pk_maximum3_f16 v21, v33, v33, v21 neg_lo:[0,1,0] neg_hi:[0,1,0]\n"
    "v_smfmac_f32_16x16x64_f16 v[88:91], v[140:143], v[68:75], v2\n"
    "v_pk_mul_f16 v67, v48, v57 op_sel_hi:[0,1]\n"
    "v_pk_maximum3_f16 v21, v34, v34, v21 neg_lo:[0,1,0] neg_hi:[0,1,0]\n"
    "v_mfma_f32_16x16x32_f16 v[76:79], v[144:147], v[60:63], v[76:79]\n"
    "v_pk_mul_f16 v68, v50, v56 op_sel_hi:[0,1]\n"
    "v_pk_mul_f16 v69, v50, v57 op_sel_hi:[0,1]\n"
    "v_pk_maximum3_f16 v21, v35, v35, v21 neg_lo:[0,1,0] neg_hi:[0,1,0]\n"
    "v_mfma_f32_16x16x32_f16 v[76:79], v[156:159], v[64:67], v[76:79]\n"
    "v_pk_mul_f16 v70, v52, v56 op_sel_hi:[0,1]\n"
    "v_pk_maximum3_f16 v21, v36, v36, v21 neg_lo:[0,1,0] neg_hi:[0,1,0]\n"
    "v_mfma_f32_16x16x32_f16 v[80:83], v[160:163], v[64:67], v[80:83]\n"
    "v_pk_mul_f16 v71, v52, v57 op_sel_hi:[0,1]\n"
    "v_pk_maximum3_f16 v21, v37, v37, v21 neg_lo:[0,1,0] neg_hi:[0,1,0]\n"
    "v_mfma_f32_16x16x32_f16 v[80:83], v[148:151], v[60:63], v[80:83]\n"
    "ds_read_b32 v22, v25 offset:524\n"
    "v_pk_mul_f16 v72, v54, v56 op_sel_hi:[0,1]\n"
    "v_pk_maximum3_f16 v21, v38, v38, v21 neg_lo:[0,1,0] neg_hi:[0,1,0]\n"
    "v_mfma_f32_16x16x32_f16 v[84:87], v[152:155], v[60:63], v[84:87]\n"
    "v_pk_mul_f16 v73, v54, v57 op_sel_hi:[0,1]\n"
    "v_pk_maximum3_f16 v21, v39, v39, v21 neg_lo:[0,1,0] neg_hi:[0,1,0]\n"
    "v_mfma_f32_16x16x32_f16 v[84:87], v[164:167], v[64:67], v[84:87]\n"
    "v_pk_mul_f16 v74, v54, v54 op_sel_hi:[0,1]\n"
    "v_pk_maximum3_f16 v21, v40, v40, v21 neg_lo:[0,1,0] neg_hi:[0,1,0]\n"
    "v_smfmac_f32_16x16x64_f16 v[88:91], v[168:171], v[60:67], v2\n"
    "v_pk_mul_f16 v75, v54, v49 op_sel_hi:[0,1]\n"
    "v_pk_max_u16 v21, v21, v21 op_sel:[0,1] op_sel_hi:[1,0]\n"
    "v_mfma_f32_16x16x32_f16 v[76:79], v[172:175], v[68:71], v[76:79]\n"
    "v_pk_mul_f16 v60, v48, v54 op_sel_hi:[0,1]\n"
    "v_pk_mul_f16 v61, v48, v55 op_sel_hi:[0,1]\n"
    "v_mfma_f32_16x16x32_f16 v[76:79], v[184:187], v[72:75], v[76:79]\n"
    "s_waitcnt lgkmcnt(0)\n"
    "v_pk_mul_f16 v62, v50, v54 op_sel_hi:[0,1]\n"
    "v_readfirstlane_b32 s45, v22\n"
    "v_mfma_f32_16x16x32_f16 v[80:83], v[188:191], v[72:75], v[80:83]\n"
    "s_cmp_lt_i32 s45, 16\n"
    "v_pk_mul_f16 v63, v50, v55 op_sel_hi:[0,1]\n"
    "v_max_u32_dpp v21, v21, v21 quad_perm:[1,0,3,2] row_mask:0xf bank_mask:0xf\n"
    "v_mfma_f32_16x16x32_f16 v[80:83], v[176:179], v[68:71], v[80:83]\n"
    "s_cbranch_scc1 .Lxd_ck27\n"
    ".Lxd_ck27r:\n"
    "v_pk_mul_f16 v64, v52, v54 op_sel_hi:[0,1]\n"
    "v_max_u32_dpp v21, v21, v21 quad_perm:[2,3,0,1] row_mask:0xf bank_mask:0xf\n"
    "v_mfma_f32_16x16x32_f16 v[84:87], v[180:183], v[68:71], v[84:87]\n"
    "ds_read_b64_tr_b16 v[44:45], v5 offset:4096\n"
    "v_pk_mul_f16 v65, v52, v55 op_sel_hi:[0,1]\n"
    "v_max_u32_dpp v21, v21, v21 row_half_mirror row_mask:0xf bank_mask:0xf\n"
    "v_mfma_f32_16x16x32_f16 v[84:87], v[192:195], v[72:75], v[84:87]\n"
    "ds_read_b64_tr_b16 v[46:47], v6 offset:4096\n"
    "v_pk_mul_f16 v66, v52, v52 op_sel_hi:[0,1]\n"
    "v_max_u32_dpp v21, v21, v21 row_mirror row_mask:0xf bank_mask:0xf\n"
    "v_smfmac_f32_16x16x64_f16 v[88:91], v[196:199], v[68:75], v2\n"
    "v_pk_mul_f16 v67, v52, v49 op_sel_hi:[0,1]\n"
    "v_max_u32_dpp v21, v21, v21 row_bcast:15 row_mask:0xa bank_mask:0xf\n"
    "v_mfma_f32_16x16x32_f16 v[76:79], v[200:203], v[60:63], v[76:79]\n"
    "v_pk_mul_f16 v68, v48, v52 op_sel_hi:[0,1]\n"
    "v_pk_mul_f16 v69, v48, v53 op_sel_hi:[0,1]\n"
    "v_mfma_f32_16x16x32_f16 v[76:79], v[212:215], v[64:67], v[76:79]\n"
    "v_pk_mul_f16 v70, v50, v52 op_sel_hi:[0,1]\n"
    "v_max_u32_dpp v21, v21, v21 row_bcast:31 row_mask:0xc bank_mask:0xf\n"
    "v_mfma_f32_16x16x32_f16 v[80:83], v[216:219], v[64:67], v[80:83]\n"
    "s_cbranch_vccnz .Lxd_pr25\n"
    "s_setprio 0\n"
    ".Lxd_pr25:\n"
    "v_pk_mul_f16 v71, v50, v53 op_sel_hi:[0,1]\n"
    "v_readlane_b32 s47, v21, 63\n"
    "v_mfma_f32_16x16x32_f16 v[80:83], v[204:207], v[60:63], v[80:83]\n"
    "s_and_b32 s47, s47, 0x7f80\n"
    "v_pk_mul_f16 v72, v50, v50 op_sel_hi:[0,1]\n"
    "v_mfma_f32_16x16x32_f16 v[84:87], v[208:211], v[60:63], v[84:87]\n"
    "s_sub_u32 s50, s47, 2816\n"
    "v_pk_mul_f16 v73, v50, v49 op_sel_hi:[0,1]\n"
    "v_pk_mul_f16 v74, v48, v50 op_sel_hi:[0,1]\n"
    "v_pk_maximum3_f16 v21, v28, v28, s50 op_sel_hi:[1,1,0] neg_lo:[0,1,0] neg_hi:[0,1,0]\n"
    "v_mfma_f32_16x16x32_f16 v[84:87], v[220:223], v[64:67], v[84:87]\n"
    "s_mul_i32 s51, s50, -8\n"
    "v_pk_mul_f16 v75, v48, v51 op_sel_hi:[0,1]\n"
    "v_pk_mad_u16 v21, v21, 8, s51 op_sel_hi:[1,0,0]\n"
    "v_smfmac_f32_16x16x64_f16 v[88:91], v[224:227], v[60:67], v2\n"
    "ds_read_b64_tr_b16 v[48:49], v3 offset:4096\n"
    "ds_read_b64_tr_b16 v[58:59], v4 offset:6976\n"
    "v_bfi_b32 v28, s57, v21, v28\n"
    "v_smfmac_f32_16x16x64_f16 v[88:91], v[252:255], v[68:75], v2\n"
    "s_waitcnt lgkmcnt(2)\n"
    "v_pk_mul_f16 v60, v44, s45 op_sel:[0,1]\n"
    "v_pk_mul_f16 v61, v45, s45 op_sel:[0,1]\n"
    "v_mfma_f32_16x16x32_f16 v[84:87], v[236:239], v[68:71], v[84:87]\n"
    "ds_read_b64_tr_b16 v[50:51], v4 offset:4672\n"
    "v_pk_mul_f16 v62, v46, s45 op_sel:[0,1]\n"
    "v_pk_mul_f16 v63, v47, s45 op_sel:[0,1]\n"
    "v_mfma_f32_16x16x32_f16 v[84:87], v[248:251], v[72:75], v[84:87]\n"
    "ds_read_b64_tr_b16 v[52:53], v4 offset:5248\n"
    "v_pk_mul_f16 v64, v44, v46\n"
    "v_pk_mul_f16 v65, v45, v47\n"
    "v_mfma_f32_16x16x32_f16 v[76:79], v[240:243], v[72:75], v[76:79]\n"
    "ds_read_b64_tr_b16 v[54:55], v4 offset:5824\n"
    "v_add_f32 v11, v88, v89\n"
    "v_add_f32 v13, v90, v91\n"
    "v_mfma_f32_16x16x32_f16 v[76:79], v[228:231], v[68:71], v[76:79]\n"
    "ds_read_b64_tr_b16 v[56:57], v4 offset:6400\n"
    "v_mov_b64 v[88:89], 0\n"
    "v_mov_b64 v[90:91], 0\n"
    "v_mfma_f32_16x16x32_f16 v[80:83], v[232:235], v[68:71], v[80:83]\n"
    "s_waitcnt lgkmcnt(4)\n"
    "v_pk_mul_f16 v66, v48, v48 op_sel_hi:[0,1]\n"
    "v_pk_mul_f16 v67, v48, v49 op_sel_hi:[0,1]\n"
    "v_mfma_f32_16x16x32_f16 v[80:83], v[244:247], v[72:75], v[80:83]\n"
    "s_lshr_b32 s47, s47, 3\n"
    "v_mul_f32 v11, v11, v13\n"
    "v_fmac_f32 v11, v84, v85\n"
    "v_fmac_f32 v11, v86, v87\n"
    ".Lxd_ph3:\n"
    "v_smfmac_f32_16x16x64_f16 v[88:91], v[112:115], v[60:67], v2\n"
    "s_cbranch_vccnz .Lxd_pr28\n"
    "s_setprio 2\n"
    ".Lxd_pr28:\n"
    "v_pk_mul_f16 v68, v58, v58 op_sel_hi:[0,1]\n"
    "v_pk_mul_f16 v69, v58, v49 op_sel_hi:[0,1]\n"
    "v_mfma_f32_16x16x32_f16 v[84:87], v[108:111], v[64:67], 0\n"
    "s_cmp_lt_u32 s55, 32\n"
    "v_fmac_f32 v11, v76, v80\n"
    "v_fmac_f32 v11, v77, v81\n"
    "v_pk_maximum3_f16 v12, v29, v29, s50 op_sel_hi:[1,1,0] neg_lo:[0,1,0] neg_hi:[0,1,0]\n"
    "v_mfma_f32_16x16x32_f16 v[84:87], v[96:99], v[60:63], v[84:87]\n"
    "s_cselect_b32 s44, 0, -1\n"
    "v_fmac_f32 v11, v78, v82\n"
    "v_fma_f32 v9, v79, v83, v11\n"
    "v_mfma_f32_16x16x32_f16 v[80:83], v[92:95], v[60:63], 0\n"
    "s_waitcnt lgkmcnt(0)\n"
    "v_pk_mul_f16 v70, v48, v58 op_sel_hi:[0,1]\n"
    "v_pk_mul_f16 v71, v48, v59 op_sel_hi:[0,1]\n"
    "v_pk_mad_u16 v12, v12, 8, s51 op_sel_hi:[1,0,0]\n"
    "v_mfma_f32_16x16x32_f16 v[80:83], v[104:107], v[64:67], v[80:83]\n"
    "s_add_u32 s55, s55, 4\n"
    "v_pk_mul_f16 v72, v50, v58 op_sel_hi:[0,1]\n"
    "v_pk_mul_f16 v73, v50, v59 op_sel_hi:[0,1]\n"
    "v_mfma_f32_16x16x32_f16 v[76:79], v[100:103], v[64:67], 0\n"
    "v_pk_mul_f16 v74, v52, v58 op_sel_hi:[0,1]\n"
    "v_pk_mul_f16 v75, v52, v59 op_sel_hi:[0,1]\n"
    "v_bfi_b32 v29, s57, v12, v29\n"
    "v_mfma_f32_16x16x32_f16 v[76:79], v[116:119], v[68:71], v[76:79]\n"
    "v_pk_mul_f16 v60, v54, v58 op_sel_hi:[0,1]\n"
    "v_pk_mul_f16 v61, v54, v59 op_sel_hi:[0,1]\n"
    "v_pk_maximum3_f16 v21, v30, v30, s50 op_sel_hi:[1,1,0] neg_lo:[0,1,0] neg_hi:[0,1,0]\n"
    "v_mfma_f32_16x16x32_f16 v[76:79], v[128:131], v[72:75], v[76:79]\n"
    "v_pk_mul_f16 v62, v56, v58 op_sel_hi:[0,1]\n"
    "v_add_u32_e64 v24, s44, 1\n"
    "v_mfma_f32_16x16x32_f16 v[80:83], v[132:135], v[72:75], v[80:83]\n"
    "s_sub_u32 s66, 4112, s47\n"
    "v_pk_mul_f16 v63, v56, v59 op_sel_hi:[0,1]\n"
    "v_pk_mad_u16 v21, v21, 8, s51 op_sel_hi:[1,0,0]\n"
    "v_bfi_b32 v30, s57, v21, v30\n"
    "v_mfma_f32_16x16x32_f16 v[80:83], v[120:123], v[68:71], v[80:83]\n"
    "s_lshl_b32 s66, s66, 19\n"
    "v_pk_mul_f16 v64, v56, v56 op_sel_hi:[0,1]\n"
    "v_pk_maximum3_f16 v12, v31, v31, s50 op_sel_hi:[1,1,0] neg_lo:[0,1,0] neg_hi:[0,1,0]\n"
    "v_pk_mad_u16 v12, v12, 8, s51 op_sel_hi:[1,0,0]\n"
    "v_mfma_f32_16x16x32_f16 v[84:87], v[124:127], v[68:71], v[84:87]\n"
    "v_pk_mul_f16 v65, v56, v49 op_sel_hi:[0,1]\n"
    "v_bfi_b32 v31, s57, v12, v31\n"
    "v_mfma_f32_16x16x32_f16 v[84:87], v[136:139], v[72:75], v[84:87]\n"
    "ds_write_b128 v15, v[28:31] offset:0\n"
    "v_pk_mul_f16 v66, v48, v56 op_sel_hi:[0,1]\n"
    "v_pk_maximum3_f16 v21, v32, v32, s50 op_sel_hi:[1,1,0] neg_lo:[0,1,0] neg_hi:[0,1,0]\n"
    "v_pk_mad_u16 v21, v21, 8, s51 op_sel_hi:[1,0,0]\n"
    "v_smfmac_f32_16x16x64_f16 v[88:91], v[140:143], v[68:75], v2\n"
    "v_pk_mul_f16 v67, v48, v57 op_sel_hi:[0,1]\n"
    "v_bfi_b32 v32, s57, v21, v32\n"
    "v_mfma_f32_16x16x32_f16 v[76:79], v[144:147], v[60:63], v[76:79]\n"
    "v_pk_mul_f16 v68, v50, v56 op_sel_hi:[0,1]\n"
    "v_pk_mul_f16 v69, v50, v57 op_sel_hi:[0,1]\n"
    "v_pk_maximum3_f16 v12, v33, v33, s50 op_sel_hi:[1,1,0] neg_lo:[0,1,0] neg_hi:[0,1,0]\n"
    "v_mfma_f32_16x16x32_f16 v[76:79], v[156:159], v[64:67], v[76:79]\n"
    "v_pk_mul_f16 v70, v52, v56 op_sel_hi:[0,1]\n"
    "v_pk_mad_u16 v12, v12, 8, s51 op_sel_hi:[1,0,0]\n"
    "v_bfi_b32 v33, s57, v12, v33\n"
    "v_mfma_f32_16x16x32_f16 v[80:83], v[160:163], v[64:67], v[80:83]\n"
    "v_pk_mul_f16 v71, v52, v57 op_sel_hi:[0,1]\n"
    "v_pk_maximum3_f16 v21, v34, v34, s50 op_sel_hi:[1,1,0] neg_lo:[0,1,0] neg_hi:[0,1,0]\n"
    "v_mfma_f32_16x16x32_f16 v[80:83], v[148:151], v[60:63], v[80:83]\n"
    "ds_read_b32 v22, v25 offset:528\n"
    "v_pk_mul_f16 v72, v54, v56 op_sel_hi:[0,1]\n"
    "v_pk_mad_u16 v21, v21, 8, s51 op_sel_hi:[1,0,0]\n"
    "v_bfi_b32 v34, s57, v21, v34\n"
    "v_mfma_f32_16x16x32_f16 v[84:87], v[152:155], v[60:63], v[84:87]\n"
    "v_pk_mul_f16 v73, v54, v57 op_sel_hi:[0,1]\n"
    "v_pk_maximum3_f16 v12, v35, v35, s50 op_sel_hi:[1,1,0] neg_lo:[0,1,0] neg_hi:[0,1,0]\n"
    "v_pk_mad_u16 v12, v12, 8, s51 op_sel_hi:[1,0,0]\n"
    "v_mfma_f32_16x16x32_f16 v[84:87], v[164:167], v[64:67], v[84:87]\n"
    "v_pk_mul_f16 v74, v54, v54 op_sel_hi:[0,1]\n"
    "v_bfi_b32 v35, s57, v12, v35\n"
    "v_smfmac_f32_16x16x64_f16 v[88:91], v[168:171], v[60:67], v2\n"
    "ds_write_b128 v15, v[32:35] offset:144\n"
    "v_pk_mul_f16 v75, v54, v49 op_sel_hi:[0,1]\n"
    "v_pk_maximum3_f16 v21, v36, v36, s50 op_sel_hi:[1,1,0] neg_lo:[0,1,0] neg_hi:[0,1,0]\n"
    "v_pk_mad_u16 v21, v21, 8, s51 op_sel_hi:[1,0,0]\n"
    "v_mfma_f32_16x16x32_f16 v[76:79], v[172:175], v[68:71], v[76:79]\n"
    "v_pk_mul_f16 v60, v48, v54 op_sel_hi:[0,1]\n"
    "v_pk_mul_f16 v61, v48, v55 op_sel_hi:[0,1]\n"
    "v_mfma_f32_16x16x32_f16 v[76:79], v[184:187], v[72:75], v[76:79]\n"
    "s_waitcnt lgkmcnt(1)\n"
    "v_pk_mul_f16 v62, v50, v54 op_sel_hi:[0,1]\n"
    "v_readfirstlane_b32 s45, v22\n"
    "v_bfi_b32 v36, s57, v21, v36\n"
    "v_mfma_f32_16x16x32_f16 v[80:83], v[188:191], v[72:75], v[80:83]\n"
    "s_cmp_lt_i32 s45, 16\n"
    "v_pk_mul_f16 v63, v50, v55 op_sel_hi:[0,1]\n"
    "v_pk_maximum3_f16 v12, v37, v37, s50 op_sel_hi:[1,1,0] neg_lo:[0,1,0] neg_hi:[0,1,0]\n"
    "v_pk_mad_u16 v12, v12, 8, s51 op_sel_hi:[1,0,0]\n"
    "v_mfma_f32_16x16x32_f16 v[80:83], v[176:179], v[68:71], v[80:83]\n"
    "s_cbranch_scc1 .Lxd_ck31\n"
    ".Lxd_ck31r:\n"
    "v_pk_mul_f16 v64, v52, v54 op_sel_hi:[0,1]\n"
    "v_bfi_b32 v37, s57, v12, v37\n"
    "v_mfma_f32_16x16x32_f16 v[84:87], v[180:183], v[68:71], v[84:87]\n"
    "ds_read_b64_tr_b16 v[44:45], v5 offset:8192\n"
    "v_pk_mul_f16 v65, v52, v55 op_sel_hi:[0,1]\n"
    "v_pk_maximum3_f16 v21, v38, v38, s50 op_sel_hi:[1,1,0] neg_lo:[0,1,0] neg_hi:[0,1,0]\n"
    "v_pk_mad_u16 v21, v21, 8, s51 op_sel_hi:[1,0,0]\n"
    "v_mfma_f32_16x16x32_f16 v[84:87], v[192:195], v[72:75], v[84:87]\n"
    "ds_read_b64_tr_b16 v[46:47], v6 offset:8192\n"
    "v_pk_mul_f16 v66, v52, v52 op_sel_hi:[0,1]\n"
    "v_bfi_b32 v38, s57, v21, v38\n"
    "v_pk_maximum3_f16 v12, v39, v39, s50 op_sel_hi:[1,1,0] neg_lo:[0,1,0] neg_hi:[0,1,0]\n"
    "v_smfmac_f32_16x16x64_f16 v[88:91], v[196:199], v[68:75], v2\n"
    "v_pk_mul_f16 v67, v52, v49 op_sel_hi:[0,1]\n"
    "v_pk_mad_u16 v12, v12, 8, s51 op_sel_hi:[1,0,0]\n"
    "v_mfma_f32_16x16x32_f16 v[76:79], v[200:203], v[60:63], v[76:79]\n"
    "s_and_b32 s40, s45, 15\n"
    "v_pk_mul_f16 v68, v48, v52 op_sel_hi:[0,1]\n"
    "v_pk_mul_f16 v69, v48, v53 op_sel_hi:[0,1]\n"
    "v_bfi_b32 v39, s57, v12, v39\n"
    "v_mfma_f32_16x16x32_f16 v[76:79], v[212:215], v[64:67], v[76:79]\n"
    "s_cbranch_scc1 .Lxd_dr30\n"
    "s_mov_b64 exec, 1\n"
    "global_atomic_add v24, v17, v24, s[52:53] sc0 sc1\n"
    "s_mov_b64 exec, -1\n"
    ".Lxd_dr30r:\n"
    "v_pk_mul_f16 v70, v50, v52 op_sel_hi:[0,1]\n"
    "v_pk_maximum3_f16 v21, v40, v40, s50 op_sel_hi:[1,1,0] neg_lo:[0,1,0] neg_hi:[0,1,0]\n"
    "v_mfma_f32_16x16x32_f16 v[80:83], v[216:219], v[64:67], v[80:83]\n"
    "s_cbranch_vccnz .Lxd_pr29\n"
    "s_setprio 0\n"
    ".Lxd_pr29:\n"
    "v_pk_mul_f16 v71, v50, v53 op_sel_hi:[0,1]\n"
    "v_pk_mad_u16 v21, v21, 8, s51 op_sel_hi:[1,0,0]\n"
    "v_bfi_b32 v40, s57, v21, v40\n"
    "v_mfma_f32_16x16x32_f16 v[80:83], v[204:207], v[60:63], v[80:83]\n"
    "ds_write_b128 v15, v[36:39] offset:288\n"
    "v_pk_mul_f16 v72, v50, v50 op_sel_hi:[0,1]\n"
    "v_cvt_pkrtz_f16_f32 v12, s66, s66\n"
    "v_lshl_or_b32 v21, v12, 16, s47\n"
    "v_mfma_f32_16x16x32_f16 v[84:87], v[208:211], v[60:63], v[84:87]\n"
    "ds_write2_b32 v16, v40, v12 offset1:72\n"
    "v_pk_mul_f16 v73, v50, v49 op_sel_hi:[0,1]\n"
    "v_pk_mul_f16 v74, v48, v50 op_sel_hi:[0,1]\n"
    "v_mfma_f32_16x16x32_f16 v[84:87], v[220:223], v[64:67], v[84:87]\n"
    "ds_or_b32 v25, v21 offset:532\n"
    "v_pk_mul_f16 v75, v48, v51 op_sel_hi:[0,1]\n"
    "v_smfmac_f32_16x16x64_f16 v[88:91], v[224:227], v[60:67], v2\n"
    "ds_read_b64_tr_b16 v[48:49], v3 offset:8192\n"
    "ds_read_b64_tr_b16 v[58:59], v4 offset:11072\n"
    "v_add_u32 v25, 16, v25\n"
    "v_smfmac_f32_16x16x64_f16 v[88:91], v[252:255], v[68:75], v2\n"
    "s_waitcnt lgkmcnt(5)\n"
    "v_pk_mul_f16 v60, v44, s45 op_sel:[0,1]\n"
    "v_pk_mul_f16 v61, v45, s45 op_sel:[0,1]\n"
    "v_mfma_f32_16x16x32_f16 v[84:87], v[236:239], v[68:71], v[84:87]\n"
    "ds_read_b64_tr_b16 v[50:51], v4 offset:8768\n"
    "v_pk_mul_f16 v62, v46, s45 op_sel:[0,1]\n"
    "v_pk_mul_f16 v63, v47, s45 op_sel:[0,1]\n"
    "v_mfma_f32_16x16x32_f16 v[84:87], v[248:251], v[72:75], v[84:87]\n"
    "ds_read_b64_tr_b16 v[52:53], v4 offset:9344\n"
    "v_pk_mul_f16 v64, v44, v46\n"
    "v_pk_mul_f16 v65, v45, v47\n"
    "v_mfma_f32_16x16x32_f16 v[76:79], v[240:243], v[72:75], v[76:79]\n"
    "ds_read_b64_tr_b16 v[54:55], v4 offset:9920\n"
    "v_add_f32 v11, v88, v89\n"
    "v_add_f32 v13, v90, v91\n"
    "v_mfma_f32_16x16x32_f16 v[76:79], v[228:231], v[68:71], v[76:79]\n"
    "ds_read_b64_tr_b16 v[56:57], v4 offset:10496\n"
    "v_mov_b64 v[88:89], 0\n"
    "v_mov_b64 v[90:91], 0\n"
    "v_mfma_f32_16x16x32_f16 v[80:83], v[232:235], v[68:71], v[80:83]\n"
    "s_waitcnt lgkmcnt(4)\n"
    "v_pk_mul_f16 v66, v48, v48 op_sel_hi:[0,1]\n"
    "v_pk_mul_f16 v67, v48, v49 op_sel_hi:[0,1]\n"
    "v_mfma_f32_16x16x32_f16 v[80:83], v[244:247], v[72:75], v[80:83]\n"
    "s_cmp_eq_u32 s36, 0\n"
    "v_mul_f32 v11, v11, v13\n"
    "v_fmac_f32 v11, v84, v85\n"
    "v_fmac_f32 v11, v86, v87\n"
    "s_cbranch_scc1 .Lxd_ph0\n"
    ".Lxd_pl0:\n"
    "v_smfmac_f32_16x16x64_f16 v[88:91], v[112:115], v[60:67], v2\n"
    "s_cbranch_vccnz .Lxd_pr32\n"
    "s_setprio 2\n"
    ".Lxd_pr32:\n"
    "v_pk_mul_f16 v68, v58, v58 op_sel_hi:[0,1]\n"
    "v_pk_mul_f16 v69, v58, v49 op_sel_hi:[0,1]\n"
    "v_mfma_f32_16x16x32_f16 v[84:87], v[108:111], v[64:67], 0\n"
    "v_fmac_f32 v11, v76, v80\n"
    "v_fmac_f32 v11, v77, v81\n"
    "v_mfma_f32_16x16x32_f16 v[84:87], v[96:99], v[60:63], v[84:87]\n"
    "v_fmac_f32 v11, v78, v82\n"
    "v_fma_f32 v10, v79, v83, v11\n"
    "v_mfma_f32_16x16x32_f16 v[80:83], v[92:95], v[60:63], 0\n"
    "s_waitcnt lgkmcnt(0)\n"
    "v_pk_mul_f16 v70, v48, v58 op_sel_hi:[0,1]\n"
    "v_pk_mul_f16 v71, v48, v59 op_sel_hi:[0,1]\n"
    "v_mfma_f32_16x16x32_f16 v[80:83], v[104:107], v[64:67], v[80:83]\n"
    "v_pk_mul_f16 v72, v50, v58 op_sel_hi:[0,1]\n"
    "v_pk_mul_f16 v73, v50, v59 op_sel_hi:[0,1]\n"
    "v_mfma_f32_16x16x32_f16 v[76:79], v[100:103], v[64:67], 0\n"
    "v_pk_mul_f16 v74, v52, v58 op_sel_hi:[0,1]\n"
    "v_pk_mul_f16 v75, v52, v59 op_sel_hi:[0,1]\n"
    "v_mfma_f32_16x16x32_f16 v[76:79], v[116:119], v[68:71], v[76:79]\n"
    "v_pk_mul_f16 v60, v54, v58 op_sel_hi:[0,1]\n"
    "v_pk_mul_f16 v61, v54, v59 op_sel_hi:[0,1]\n"
    "v_mfma_f32_16x16x32_f16 v[76:79], v[128:131], v[72:75], v[76:79]\n"
    "v_pk_mul_f16 v62, v56, v58 op_sel_hi:[0,1]\n"
    "v_permlane32_swap_b32 v7, v8\n"
    "v_mfma_f32_16x16x32_f16 v[80:83], v[132:135], v[72:75], v[80:83]\n"
    "v_pk_mul_f16 v63, v56, v59 op_sel_hi:[0,1]\n"
    "v_permlane32_swap_b32 v9, v10\n"
    "v_mfma_f32_16x16x32_f16 v[80:83], v[120:123], v[68:71], v[80:83]\n"
    "v_pk_mul_f16 v64, v56, v56 op_sel_hi:[0,1]\n"
    "v_mul_f32 v7, v0, v7\n"
    "v_mfma_f32_16x16x32_f16 v[84:87], v[124:127], v[68:71], v[84:87]\n"
    "v_pk_mul_f16 v65, v56, v49 op_sel_hi:[0,1]\n"
    "v_mul_f32 v9, v0, v9\n"
    "v_mfma_f32_16x16x32_f16 v[84:87], v[136:139], v[72:75], v[84:87]\n"
    "v_pk_mul_f16 v66, v48, v56 op_sel_hi:[0,1]\n"
    "v_fmac_f32 v7, v23, v8\n"
    "v_smfmac_f32_16x16x64_f16 v[88:91], v[140:143], v[68:75], v2\n"
    "v_pk_mul_f16 v67, v48, v57 op_sel_hi:[0,1]\n"
    "v_fmac_f32 v9, v23, v10\n"
    "v_mfma_f32_16x16x32_f16 v[76:79], v[144:147], v[60:63], v[76:79]\n"
    "v_pk_mul_f16 v68, v50, v56 op_sel_hi:[0,1]\n"
    "v_pk_mul_f16 v69, v50, v57 op_sel_hi:[0,1]\n"
    "v_mfma_f32_16x16x32_f16 v[76:79], v[156:159], v[64:67], v[76:79]\n"
    "v_pk_mul_f16 v70, v52, v56 op_sel_hi:[0,1]\n"
    "v_permlane16_swap_b32 v7, v9\n"
    "v_mfma_f32_16x16x32_f16 v[80:83], v[160:163], v[64:67], v[80:83]\n"
    "v_pk_mul_f16 v71, v52, v57 op_sel_hi:[0,1]\n"
    "v_add_f32 v7, v7, v9\n"
    "v_mfma_f32_16x16x32_f16 v[80:83], v[148:151], v[60:63], v[80:83]\n"
    "ds_read_b32 v22, v25 offset:516\n"
    "v_pk_mul_f16 v72, v54, v56 op_sel_hi:[0,1]\n"
    "v_add_f32_dpp v7, v7, v7 quad_perm:[1,0,3,2] row_mask:0xf bank_mask:0xf\n"
    "v_mfma_f32_16x16x32_f16 v[84:87], v[152:155], v[60:63], v[84:87]\n"
    "v_pk_mul_f16 v73, v54, v57 op_sel_hi:[0,1]\n"
    "v_add_f32_dpp v7, v7, v7 quad_perm:[2,3,0,1] row_mask:0xf bank_mask:0xf\n"
    "v_mfma_f32_16x16x32_f16 v[84:87], v[164:167], v[64:67], v[84:87]\n"
    "v_pk_mul_f16 v74, v54, v54 op_sel_hi:[0,1]\n"
    "v_add_f32_dpp v7, v7, v7 row_half_mirror row_mask:0xf bank_mask:0xf\n"
    "v_smfmac_f32_16x16x64_f16 v[88:91], v[168:171], v[60:67], v2\n"
    "v_pk_mul_f16 v75, v54, v49 op_sel_hi:[0,1]\n"
    "v_add_f32_dpp v7, v7, v7 row_mirror row_mask:0xf bank_mask:0xf\n"
    "v_mfma_f32_16x16x32_f16 v[76:79], v[172:175], v[68:71], v[76:79]\n"
    "ds_write_b32 v14, v7\n"
    "v_pk_mul_f16 v60, v48, v54 op_sel_hi:[0,1]\n"
    "v_pk_mul_f16 v61, v48, v55 op_sel_hi:[0,1]\n"
    "v_mfma_f32_16x16x32_f16 v[76:79], v[184:187], v[72:75], v[76:79]\n"
    "s_waitcnt lgkmcnt(1)\n"
    "v_pk_mul_f16 v62, v50, v54 op_sel_hi:[0,1]\n"
    "v_readfirstlane_b32 s45, v22\n"
    "v_mfma_f32_16x16x32_f16 v[80:83], v[188:191], v[72:75], v[80:83]\n"
    "s_cmp_lt_i32 s45, 16\n"
    "v_pk_mul_f16 v63, v50, v55 op_sel_hi:[0,1]\n"
    "v_add_u32 v14, 64, v14\n"
    "v_mfma_f32_16x16x32_f16 v[80:83], v[176:179], v[68:71], v[80:83]\n"
    "s_cbranch_scc1 .Lxd_ck34\n"
    ".Lxd_ck34r:\n"
    "v_pk_mul_f16 v64, v52, v54 op_sel_hi:[0,1]\n"
    "v_mfma_f32_16x16x32_f16 v[84:87], v[180:183], v[68:71], v[84:87]\n"
    "ds_read_b64_tr_b16 v[44:45], v5 offset:12288\n"
    "v_pk_mul_f16 v65, v52, v55 op_sel_hi:[0,1]\n"
    "v_mfma_f32_16x16x32_f16 v[84:87], v[192:195], v[72:75], v[84:87]\n"
    "ds_read_b64_tr_b16 v[46:47], v6 offset:12288\n"
    "v_pk_mul_f16 v66, v52, v52 op_sel_hi:[0,1]\n"
    "v_smfmac_f32_16x16x64_f16 v[88:91], v[196:199], v[68:75], v2\n"
    "v_pk_mul_f16 v67, v52, v49 op_sel_hi:[0,1]\n"
    "v_mfma_f32_16x16x32_f16 v[76:79], v[200:203], v[60:63], v[76:79]\n"
    "v_pk_mul_f16 v68, v48, v52 op_sel_hi:[0,1]\n"
    "v_pk_mul_f16 v69, v48, v53 op_sel_hi:[0,1]\n"
    "v_mfma_f32_16x16x32_f16 v[76:79], v[212:215], v[64:67], v[76:79]\n"
    "v_pk_mul_f16 v70, v50, v52 op_sel_hi:[0,1]\n"
    "v_mfma_f32_16x16x32_f16 v[80:83], v[216:219], v[64:67], v[80:83]\n"
    "s_cbranch_vccnz .Lxd_pr33\n"
    "s_setprio 0\n"
    ".Lxd_pr33:\n"
    "v_pk_mul_f16 v71, v50, v53 op_sel_hi:[0,1]\n"
    "v_mfma_f32_16x16x32_f16 v[80:83], v[204:207], v[60:63], v[80:83]\n"
    "v_pk_mul_f16 v72, v50, v50 op_sel_hi:[0,1]\n"
    "v_mfma_f32_16x16x32_f16 v[84:87], v[208:211], v[60:63], v[84:87]\n"
    "v_pk_mul_f16 v73, v50, v49 op_sel_hi:[0,1]\n"
    "v_pk_mul_f16 v74, v48, v50 op_sel_hi:[0,1]\n"
    "v_mfma_f32_16x16x32_f16 v[84:87], v[220:223], v[64:67], v[84:87]\n"
    "v_pk_mul_f16 v75, v48, v51 op_sel_hi:[0,1]\n"
    "v_smfmac_f32_16x16x64_f16 v[88:91], v[224:227], v[60:67], v2\n"
    "ds_read_b64_tr_b16 v[48:49], v3 offset:12288\n"
    "ds_read_b64_tr_b16 v[58:59], v4 offset:15168\n"
    "v_smfmac_f32_16x16x64_f16 v[88:91], v[252:255], v[68:75], v2\n"
    "s_waitcnt lgkmcnt(2)\n"
    "v_pk_mul_f16 v60, v44, s45 op_sel:[0,1]\n"
    "v_pk_mul_f16 v61, v45, s45 op_sel:[0,1]\n"
    "v_mfma_f32_16x16x32_f16 v[84:87], v[236:239], v[68:71], v[84:87]\n"
    "ds_read_b64_tr_b16 v[50:51], v4 offset:12864\n"
    "v_pk_mul_f16 v62, v46, s45 op_sel:[0,1]\n"
    "v_pk_mul_f16 v63, v47, s45 op_sel:[0,1]\n"
    "v_mfma_f32_16x16x32_f16 v[84:87], v[248:251], v[72:75], v[84:87]\n"
    "ds_read_b64_tr_b16 v[52:53], v4 offset:13440\n"
    "v_pk_mul_f16 v64, v44, v46\n"
    "v_pk_mul_f16 v65, v45, v47\n"
    "v_mfma_f32_16x16x32_f16 v[76:79], v[240:243], v[72:75], v[76:79]\n"
    "ds_read_b64_tr_b16 v[54:55], v4 offset:14016\n"
    "v_add_f32 v11, v88, v89\n"
    "v_add_f32 v13, v90, v91\n"
    "v_mfma_f32_16x16x32_f16 v[76:79], v[228:231], v[68:71], v[76:79]\n"
    "ds_read_b64_tr_b16 v[56:57], v4 offset:14592\n"
    "v_mov_b64 v[88:89], 0\n"
    "v_mov_b64 v[90:91], 0\n"
    "v_mfma_f32_16x16x32_f16 v[80:83], v[232:235], v[68:71], v[80:83]\n"
    "s_waitcnt lgkmcnt(4)\n"
    "v_pk_mul_f16 v66, v48, v48 op_sel_hi:[0,1]\n"
    "v_pk_mul_f16 v67, v48, v49 op_sel_hi:[0,1]\n"
    "v_mfma_f32_16x16x32_f16 v[80:83], v[244:247], v[72:75], v[80:83]\n"
    "v_mul_f32 v11, v11, v13\n"
    "v_fmac_f32 v11, v84, v85\n"
    "v_fmac_f32 v11, v86, v87\n"
    ".Lxd_pl1:\n"
    "v_smfmac_f32_16x16x64_f16 v[88:91], v[112:115], v[60:67], v2\n"
    "s_cbranch_vccnz .Lxd_pr35\n"
    "s_setprio 2\n"
    ".Lxd_pr35:\n"
    "v_pk_mul_f16 v68, v58, v58 op_sel_hi:[0,1]\n"
    "v_pk_mul_f16 v69, v58, v49 op_sel_hi:[0,1]\n"
    "v_mfma_f32_16x16x32_f16 v[84:87], v[108:111], v[64:67], 0\n"
    "v_fmac_f32 v11, v76, v80\n"
    "v_fmac_f32 v11, v77, v81\n"
    "v_mfma_f32_16x16x32_f16 v[84:87], v[96:99], v[60:63], v[84:87]\n"
    "v_fmac_f32 v11, v78, v82\n"
    "v_fma_f32 v7, v79, v83, v11\n"
    "v_mfma_f32_16x16x32_f16 v[80:83], v[92:95], v[60:63], 0\n"
    "s_waitcnt lgkmcnt(0)\n"
    "v_pk_mul_f16 v70, v48, v58 op_sel_hi:[0,1]\n"
    "v_pk_mul_f16 v71, v48, v59 op_sel_hi:[0,1]\n"
    "v_mfma_f32_16x16x32_f16 v[80:83], v[104:107], v[64:67], v[80:83]\n"
    "v_pk_mul_f16 v72, v50, v58 op_sel_hi:[0,1]\n"
    "v_pk_mul_f16 v73, v50, v59 op_sel_hi:[0,1]\n"
    "v_mfma_f32_16x16x32_f16 v[76:79], v[100:103], v[64:67], 0\n"
    "s_cmp_lg_u32 s46, 0\n"
    "v_pk_mul_f16 v74, v52, v58 op_sel_hi:[0,1]\n"
    "v_pk_mul_f16 v75, v52, v59 op_sel_hi:[0,1]\n"
    "v_mfma_f32_16x16x32_f16 v[76:79], v[116:119], v[68:71], v[76:79]\n"
    "s_cbranch_scc1 .Lxd_ht37\n"
    ".Lxd_ht37r:\n"
    "v_pk_mul_f16 v60, v54, v58 op_sel_hi:[0,1]\n"
    "v_pk_mul_f16 v61, v54, v59 op_sel_hi:[0,1]\n"
    "v_mfma_f32_16x16x32_f16 v[76:79], v[128:131], v[72:75], v[76:79]\n"
    "v_pk_mul_f16 v62, v56, v58 op_sel_hi:[0,1]\n"
    "v_xor_b32 v3, 0x4000, v3\n"
    "v_mfma_f32_16x16x32_f16 v[80:83], v[132:135], v[72:75], v[80:83]\n"
    "v_pk_mul_f16 v63, v56, v59 op_sel_hi:[0,1]\n"
    "v_xor_b32 v4, 0x4000, v4\n"
    "v_mfma_f32_16x16x32_f16 v[80:83], v[120:123], v[68:71], v[80:83]\n"
    "v_pk_mul_f16 v64, v56, v56 op_sel_hi:[0,1]\n"
    "v_xor_b32 v5, 0x4000, v5\n"
    "v_mfma_f32_16x16x32_f16 v[84:87], v[124:127], v[68:71], v[84:87]\n"
    "v_pk_mul_f16 v65, v56, v49 op_sel_hi:[0,1]\n"
    "v_xor_b32 v6, 0x4000, v6\n"
    "v_mfma_f32_16x16x32_f16 v[84:87], v[136:139], v[72:75], v[84:87]\n"
    "v_pk_mul_f16 v66, v48, v56 op_sel_hi:[0,1]\n"
    "v_smfmac_f32_16x16x64_f16 v[88:91], v[140:143], v[68:75], v2\n"
    "v_pk_mul_f16 v67, v48, v57 op_sel_hi:[0,1]\n"
    "v_mfma_f32_16x16x32_f16 v[76:79], v[144:147], v[60:63], v[76:79]\n"
    "v_pk_mul_f16 v68, v50, v56 op_sel_hi:[0,1]\n"
    "v_pk_mul_f16 v69, v50, v57 op_sel_hi:[0,1]\n"
    "v_mfma_f32_16x16x32_f16 v[76:79], v[156:159], v[64:67], v[76:79]\n"
    "v_pk_mul_f16 v70, v52, v56 op_sel_hi:[0,1]\n"
    "v_mfma_f32_16x16x32_f16 v[80:83], v[160:163], v[64:67], v[80:83]\n"
    "v_pk_mul_f16 v71, v52, v57 op_sel_hi:[0,1]\n"
    "v_mfma_f32_16x16x32_f16 v[80:83], v[148:151], v[60:63], v[80:83]\n"
    "ds_read_b32 v22, v25 offset:520\n"
    "v_pk_mul_f16 v72, v54, v56 op_sel_hi:[0,1]\n"
    "v_mfma_f32_16x16x32_f16 v[84:87], v[152:155], v[60:63], v[84:87]\n"
    "v_pk_mul_f16 v73, v54, v57 op_sel_hi:[0,1]\n"
    "v_mfma_f32_16x16x32_f16 v[84:87], v[164:167], v[64:67], v[84:87]\n"
    "v_pk_mul_f16 v74, v54, v54 op_sel_hi:[0,1]\n"
    "v_smfmac_f32_16x16x64_f16 v[88:91], v[168:171], v[60:67], v2\n"
    "v_pk_mul_f16 v75, v54, v49 op_sel_hi:[0,1]\n"
    "v_mfma_f32_16x16x32_f16 v[76:79], v[172:175], v[68:71], v[76:79]\n"
    "v_pk_mul_f16 v60, v48, v54 op_sel_hi:[0,1]\n"
    "v_pk_mul_f16 v61, v48, v55 op_sel_hi:[0,1]\n"
    "v_mfma_f32_16x16x32_f16 v[76:79], v[184:187], v[72:75], v[76:79]\n"
    "s_waitcnt lgkmcnt(0)\n"
    "v_pk_mul_f16 v62, v50, v54 op_sel_hi:[0,1]\n"
    "v_readfirstlane_b32 s45, v22\n"
    "v_mfma_f32_16x16x32_f16 v[80:83], v[188:191], v[72:75], v[80:83]\n"
    "s_cmp_lt_i32 s45, 16\n"
    "v_pk_mul_f16 v63, v50, v55 op_sel_hi:[0,1]\n"
    "v_mfma_f32_16x16x32_f16 v[80:83], v[176:179], v[68:71], v[80:83]\n"
    "s_cbranch_scc1 .Lxd_ck38\n"
    ".Lxd_ck38r:\n"
    "v_pk_mul_f16 v64, v52, v54 op_sel_hi:[0,1]\n"
    "v_mfma_f32_16x16x32_f16 v[84:87], v[180:183], v[68:71], v[84:87]\n"
    "ds_read_b64_tr_b16 v[44:45], v5 offset:0\n"
    "v_pk_mul_f16 v65, v52, v55 op_sel_hi:[0,1]\n"
    "v_mfma_f32_16x16x32_f16 v[84:87], v[192:195], v[72:75], v[84:87]\n"
    "ds_read_b64_tr_b16 v[46:47], v6 offset:0\n"
    "v_pk_mul_f16 v66, v52, v52 op_sel_hi:[0,1]\n"
    "v_smfmac_f32_16x16x64_f16 v[88:91], v[196:199], v[68:75], v2\n"
    "v_pk_mul_f16 v67, v52, v49 op_sel_hi:[0,1]\n"
    "v_mfma_f32_16x16x32_f16 v[76:79], v[200:203], v[60:63], v[76:79]\n"
    "v_pk_mul_f16 v68, v48, v52 op_sel_hi:[0,1]\n"
    "v_pk_mul_f16 v69, v48, v53 op_sel_hi:[0,1]\n"
    "v_mfma_f32_16x16x32_f16 v[76:79], v[212:215], v[64:67], v[76:79]\n"
    "v_pk_mul_f16 v70, v50, v52 op_sel_hi:[0,1]\n"
    "v_mfma_f32_16x16x32_f16 v[80:83], v[216:219], v[64:67], v[80:83]\n"
    "s_cbranch_vccnz .Lxd_pr36\n"
    "s_setprio 0\n"
    ".Lxd_pr36:\n"
    "v_pk_mul_f16 v71, v50, v53 op_sel_hi:[0,1]\n"
    "v_mfma_f32_16x16x32_f16 v[80:83], v[204:207], v[60:63], v[80:83]\n"
    "v_pk_mul_f16 v72, v50, v50 op_sel_hi:[0,1]\n"
    "v_mfma_f32_16x16x32_f16 v[84:87], v[208:211], v[60:63], v[84:87]\n"
    "v_pk_mul_f16 v73, v50, v49 op_sel_hi:[0,1]\n"
    "v_pk_mul_f16 v74, v48, v50 op_sel_hi:[0,1]\n"
    "v_mfma_f32_16x16x32_f16 v[84:87], v[220:223], v[64:67], v[84:87]\n"
    "v_pk_mul_f16 v75, v48, v51 op_sel_hi:[0,1]\n"
    "v_smfmac_f32_16x16x64_f16 v[88:91], v[224:227], v[60:67], v2\n"
    "ds_read_b64_tr_b16 v[48:49], v3 offset:0\n"
    "ds_read_b64_tr_b16 v[58:59], v4 offset:2880\n"
    "v_smfmac_f32_16x16x64_f16 v[88:91], v[252:255], v[68:75], v2\n"
    "s_waitcnt lgkmcnt(2)\n"
    "v_pk_mul_f16 v60, v44, s45 op_sel:[0,1]\n"
    "v_pk_mul_f16 v61, v45, s45 op_sel:[0,1]\n"
    "v_mfma_f32_16x16x32_f16 v[84:87], v[236:239], v[68:71], v[84:87]\n"
    "ds_read_b64_tr_b16 v[50:51], v4 offset:576\n"
    "v_pk_mul_f16 v62, v46, s45 op_sel:[0,1]\n"
    "v_pk_mul_f16 v63, v47, s45 op_sel:[0,1]\n"
    "v_mfma_f32_16x16x32_f16 v[84:87], v[248:251], v[72:75], v[84:87]\n"
    "ds_read_b64_tr_b16 v[52:53], v4 offset:1152\n"
    "v_pk_mul_f16 v64, v44, v46\n"
    "v_pk_mul_f16 v65, v45, v47\n"
    "v_mfma_f32_16x16x32_f16 v[76:79], v[240:243], v[72:75], v[76:79]\n"
    "ds_read_b64_tr_b16 v[54:55], v4 offset:1728\n"
    "v_add_f32 v11, v88, v89\n"
    "v_add_f32 v13, v90, v91\n"
    "v_mfma_f32_16x16x32_f16 v[76:79], v[228:231], v[68:71], v[76:79]\n"
    "ds_read_b64_tr_b16 v[56:57], v4 offset:2304\n"
    "v_mov_b64 v[88:89], 0\n"
    "v_mov_b64 v[90:91], 0\n"
    "v_mfma_f32_16x16x32_f16 v[80:83], v[232:235], v[68:71], v[80:83]\n"
    "s_waitcnt lgkmcnt(4)\n"
    "v_pk_mul_f16 v66, v48, v48 op_sel_hi:[0,1]\n"
    "v_pk_mul_f16 v67, v48, v49 op_sel_hi:[0,1]\n"
    "v_mfma_f32_16x16x32_f16 v[80:83], v[244:247], v[72:75], v[80:83]\n"
    "v_mul_f32 v11, v11, v13\n"
    "v_fmac_f32 v11, v84, v85\n"
    "v_fmac_f32 v11, v86, v87\n"
    ".Lxd_pl2:\n"
    "v_smfmac_f32_16x16x64_f16 v[88:91], v[112:115], v[60:67], v2\n"
    "s_cbranch_vccnz .Lxd_pr39\n"
    "s_setprio 2\n"
    ".Lxd_pr39:\n"
    "v_pk_mul_f16 v68, v58, v58 op_sel_hi:[0,1]\n"
    "v_pk_mul_f16 v69, v58, v49 op_sel_hi:[0,1]\n"
    "v_mfma_f32_16x16x32_f16 v[84:87], v[108:111], v[64:67], 0\n"
    "v_fmac_f32 v11, v76, v80\n"
    "v_fmac_f32 v11, v77, v81\n"
    "v_mfma_f32_16x16x32_f16 v[84:87], v[96:99], v[60:63], v[84:87]\n"
    "v_fmac_f32 v11, v78, v82\n"
    "v_fma_f32 v8, v79, v83, v11\n"
    "v_mfma_f32_16x16x32_f16 v[80:83], v[92:95], v[60:63], 0\n"
    "s_waitcnt lgkmcnt(0)\n"
    "v_pk_mul_f16 v70, v48, v58 op_sel_hi:[0,1]\n"
    "v_pk_mul_f16 v71, v48, v59 op_sel_hi:[0,1]\n"
    "v_mfma_f32_16x16x32_f16 v[80:83], v[104:107], v[64:67], v[80:83]\n"
    "v_pk_mul_f16 v72, v50, v58 op_sel_hi:[0,1]\n"
    "v_pk_mul_f16 v73, v50, v59 op_sel_hi:[0,1]\n"
    "v_mfma_f32_16x16x32_f16 v[76:79], v[100:103], v[64:67], 0\n"
    "s_cmp_lg_u32 s46, 0\n"
    "v_pk_mul_f16 v74, v52, v58 op_sel_hi:[0,1]\n"
    "v_pk_mul_f16 v75, v52, v59 op_sel_hi:[0,1]\n"
    "v_mfma_f32_16x16x32_f16 v[76:79], v[116:119], v[68:71], v[76:79]\n"
    "s_cbranch_scc1 .Lxd_ht41\n"
    ".Lxd_ht41r:\n"
    "v_pk_mul_f16 v60, v54, v58 op_sel_hi:[0,1]\n"
    "v_pk_mul_f16 v61, v54, v59 op_sel_hi:[0,1]\n"
    "v_mfma_f32_16x16x32_f16 v[76:79], v[128:131], v[72:75], v[76:79]\n"
    "v_pk_mul_f16 v62, v56, v58 op_sel_hi:[0,1]\n"
    "v_mfma_f32_16x16x32_f16 v[80:83], v[132:135], v[72:75], v[80:83]\n"
    "v_pk_mul_f16 v63, v56, v59 op_sel_hi:[0,1]\n"
    "v_mfma_f32_16x16x32_f16 v[80:83], v[120:123], v[68:71], v[80:83]\n"
    "v_pk_mul_f16 v64, v56, v56 op_sel_hi:[0,1]\n"
    "v_mfma_f32_16x16x32_f16 v[84:87], v[124:127], v[68:71], v[84:87]\n"
    "v_pk_mul_f16 v65, v56, v49 op_sel_hi:[0,1]\n"
    "v_mfma_f32_16x16x32_f16 v[84:87], v[136:139], v[72:75], v[84:87]\n"
    "v_pk_mul_f16 v66, v48, v56 op_sel_hi:[0,1]\n"
    "v_smfmac_f32_16x16x64_f16 v[88:91], v[140:143], v[68:75], v2\n"
    "v_pk_mul_f16 v67, v48, v57 op_sel_hi:[0,1]\n"
    "v_mfma_f32_16x16x32_f16 v[76:79], v[144:147], v[60:63], v[76:79]\n"
    "v_pk_mul_f16 v68, v50, v56 op_sel_hi:[0,1]\n"
    "v_pk_mul_f16 v69, v50, v57 op_sel_hi:[0,1]\n"
    "v_mfma_f32_16x16x32_f16 v[76:79], v[156:159], v[64:67], v[76:79]\n"
    "v_pk_mul_f16 v70, v52, v56 op_sel_hi:[0,1]\n"
    "v_mfma_f32_16x16x32_f16 v[80:83], v[160:163], v[64:67], v[80:83]\n"
    "v_pk_mul_f16 v71, v52, v57 op_sel_hi:[0,1]\n"
    "v_mfma_f32_16x16x32_f16 v[80:83], v[148:151], v[60:63], v[80:83]\n"
    "ds_read_b32 v22, v25 offset:524\n"
    "v_pk_mul_f16 v72, v54, v56 op_sel_hi:[0,1]\n"
    "v_mfma_f32_16x16x32_f16 v[84:87], v[152:155], v[60:63], v[84:87]\n"
    "v_pk_mul_f16 v73, v54, v57 op_sel_hi:[0,1]\n"
    "v_mfma_f32_16x16x32_f16 v[84:87], v[164:167], v[64:67], v[84:87]\n"
    "v_pk_mul_f16 v74, v54, v54 op_sel_hi:[0,1]\n"
    "v_smfmac_f32_16x16x64_f16 v[88:91], v[168:171], v[60:67], v2\n"
    "v_pk_mul_f16 v75, v54, v49 op_sel_hi:[0,1]\n"
    "v_mfma_f32_16x16x32_f16 v[76:79], v[172:175], v[68:71], v[76:79]\n"
    "v_pk_mul_f16 v60, v48, v54 op_sel_hi:[0,1]\n"
    "v_pk_mul_f16 v61, v48, v55 op_sel_hi:[0,1]\n"
    "v_mfma_f32_16x16x32_f16 v[76:79], v[184:187], v[72:75], v[76:79]\n"
    "s_waitcnt lgkmcnt(0)\n"
    "v_pk_mul_f16 v62, v50, v54 op_sel_hi:[0,1]\n"
    "v_readfirstlane_b32 s45, v22\n"
    "v_mfma_f32_16x16x32_f16 v[80:83], v[188:191], v[72:75], v[80:83]\n"
    "s_cmp_lt_i32 s45, 16\n"
    "v_pk_mul_f16 v63, v50, v55 op_sel_hi:[0,1]\n"
    "v_mfma_f32_16x16x32_f16 v[80:83], v[176:179], v[68:71], v[80:83]\n"
    "s_cbranch_scc1 .Lxd_ck42\n"
    ".Lxd_ck42r:\n"
    "v_pk_mul_f16 v64, v52, v54 op_sel_hi:[0,1]\n"
    "v_mfma_f32_16x16x32_f16 v[84:87], v[180:183], v[68:71], v[84:87]\n"
    "ds_read_b64_tr_b16 v[44:45], v5 offset:4096\n"
    "v_pk_mul_f16 v65, v52, v55 op_sel_hi:[0,1]\n"
    "v_mfma_f32_16x16x32_f16 v[84:87], v[192:195], v[72:75], v[84:87]\n"
    "ds_read_b64_tr_b16 v[46:47], v6 offset:4096\n"
    "v_pk_mul_f16 v66, v52, v52 op_sel_hi:[0,1]\n"
    "v_smfmac_f32_16x16x64_f16 v[88:91], v[196:199], v[68:75], v2\n"
    "v_pk_mul_f16 v67, v52, v49 op_sel_hi:[0,1]\n"
    "v_mfma_f32_16x16x32_f16 v[76:79], v[200:203], v[60:63], v[76:79]\n"
    "v_pk_mul_f16 v68, v48, v52 op_sel_hi:[0,1]\n"
    "v_pk_mul_f16 v69, v48, v53 op_sel_hi:[0,1]\n"
    "v_mfma_f32_16x16x32_f16 v[76:79], v[212:215], v[64:67], v[76:79]\n"
    "v_pk_mul_f16 v70, v50, v52 op_sel_hi:[0,1]\n"
    "v_mfma_f32_16x16x32_f16 v[80:83], v[216:219], v[64:67], v[80:83]\n"
    "s_cbranch_vccnz .Lxd_pr40\n"
    "s_setprio 0\n"
    ".Lxd_pr40:\n"
    "v_pk_mul_f16 v71, v50, v53 op_sel_hi:[0,1]\n"
    "v_mfma_f32_16x16x32_f16 v[80:83], v[204:207], v[60:63], v[80:83]\n"
    "v_pk_mul_f16 v72, v50, v50 op_sel_hi:[0,1]\n"
    "v_mfma_f32_16x16x32_f16 v[84:87], v[208:211], v[60:63], v[84:87]\n"
    "v_pk_mul_f16 v73, v50, v49 op_sel_hi:[0,1]\n"
    "v_pk_mul_f16 v74, v48, v50 op_sel_hi:[0,1]\n"
    "v_mfma_f32_16x16x32_f16 v[84:87], v[220:223], v[64:67], v[84:87]\n"
    "v_pk_mul_f16 v75, v48, v51 op_sel_hi:[0,1]\n"
    "v_smfmac_f32_16x16x64_f16 v[88:91], v[224:227], v[60:67], v2\n"
    "ds_read_b64_tr_b16 v[48:49], v3 offset:4096\n"
    "ds_read_b64_tr_b16 v[58:59], v4 offset:6976\n"
    "v_smfmac_f32_16x16x64_f16 v[88:91], v[252:255], v[68:75], v2\n"
    "s_waitcnt lgkmcnt(2)\n"
    "v_pk_mul_f16 v60, v44, s45 op_sel:[0,1]\n"
    "v_pk_mul_f16 v61, v45, s45 op_sel:[0,1]\n"
    "v_mfma_f32_16x16x32_f16 v[84:87], v[236:239], v[68:71], v[84:87]\n"
    "ds_read_b64_tr_b16 v[50:51], v4 offset:4672\n"
    "v_pk_mul_f16 v62, v46, s45 op_sel:[0,1]\n"
    "v_pk_mul_f16 v63, v47, s45 op_sel:[0,1]\n"
    "v_mfma_f32_16x16x32_f16 v[84:87], v[248:251], v[72:75], v[84:87]\n"
    "ds_read_b64_tr_b16 v[52:53], v4 offset:5248\n"
    "v_pk_mul_f16 v64, v44, v46\n"
    "v_pk_mul_f16 v65, v45, v47\n"
    "v_mfma_f32_16x16x32_f16 v[76:79], v[240:243], v[72:75], v[76:79]\n"
    "ds_read_b64_tr_b16 v[54:55], v4 offset:5824\n"
    "v_add_f32 v11, v88, v89\n"
    "v_add_f32 v13, v90, v91\n"
    "v_mfma_f32_16x16x32_f16 v[76:79], v[228:231], v[68:71], v[76:79]\n"
    "ds_read_b64_tr_b16 v[56:57], v4 offset:6400\n"
    "v_mov_b64 v[88:89], 0\n"
    "v_mov_b64 v[90:91], 0\n"
    "v_mfma_f32_16x16x32_f16 v[80:83], v[232:235], v[68:71], v[80:83]\n"
    "s_waitcnt lgkmcnt(4)\n"
    "v_pk_mul_f16 v66, v48, v48 op_sel_hi:[0,1]\n"
    "v_pk_mul_f16 v67, v48, v49 op_sel_hi:[0,1]\n"
    "v_mfma_f32_16x16x32_f16 v[80:83], v[244:247], v[72:75], v[80:83]\n"
    "v_mul_f32 v11, v11, v13\n"
    "v_fmac_f32 v11, v84, v85\n"
    "v_fmac_f32 v11, v86, v87\n"
    ".Lxd_pl3:\n"
    "v_smfmac_f32_16x16x64_f16 v[88:91], v[112:115], v[60:67], v2\n"
    "s_cbranch_vccnz .Lxd_pr43\n"
    "s_setprio 2\n"
    ".Lxd_pr43:\n"
    "v_pk_mul_f16 v68, v58, v58 op_sel_hi:[0,1]\n"
    "v_pk_mul_f16 v69, v58, v49 op_sel_hi:[0,1]\n"
    "v_mfma_f32_16x16x32_f16 v[84:87], v[108:111], v[64:67], 0\n"
    "v_fmac_f32 v11, v76, v80\n"
    "v_fmac_f32 v11, v77, v81\n"
    "v_mfma_f32_16x16x32_f16 v[84:87], v[96:99], v[60:63], v[84:87]\n"
    "v_fmac_f32 v11, v78, v82\n"
    "v_fma_f32 v9, v79, v83, v11\n"
    "v_mfma_f32_16x16x32_f16 v[80:83], v[92:95], v[60:63], 0\n"
    "s_waitcnt lgkmcnt(0)\n"
    "v_pk_mul_f16 v70, v48, v58 op_sel_hi:[0,1]\n"
    "v_pk_mul_f16 v71, v48, v59 op_sel_hi:[0,1]\n"
    "v_mfma_f32_16x16x32_f16 v[80:83], v[104:107], v[64:67], v[80:83]\n"
    "v_pk_mul_f16 v72, v50, v58 op_sel_hi:[0,1]\n"
    "v_pk_mul_f16 v73, v50, v59 op_sel_hi:[0,1]\n"
    "v_mfma_f32_16x16x32_f16 v[76:79], v[100:103], v[64:67], 0\n"
    "s_cmp_lt_i32 s46, 0\n"
    "v_pk_mul_f16 v74, v52, v58 op_sel_hi:[0,1]\n"
    "v_pk_mul_f16 v75, v52, v59 op_sel_hi:[0,1]\n"
    "v_mfma_f32_16x16x32_f16 v[76:79], v[116:119], v[68:71], v[76:79]\n"
    "s_cbranch_scc1 .Lxd_ht45\n"
    ".Lxd_ht45r:\n"
    "v_pk_mul_f16 v60, v54, v58 op_sel_hi:[0,1]\n"
    "v_pk_mul_f16 v61, v54, v59 op_sel_hi:[0,1]\n"
    "v_mfma_f32_16x16x32_f16 v[76:79], v[128:131], v[72:75], v[76:79]\n"
    "v_pk_mul_f16 v62, v56, v58 op_sel_hi:[0,1]\n"
    "v_mfma_f32_16x16x32_f16 v[80:83], v[132:135], v[72:75], v[80:83]\n"
    "v_pk_mul_f16 v63, v56, v59 op_sel_hi:[0,1]\n"
    "v_mfma_f32_16x16x32_f16 v[80:83], v[120:123], v[68:71], v[80:83]\n"
    "v_pk_mul_f16 v64, v56, v56 op_sel_hi:[0,1]\n"
    "v_mfma_f32_16x16x32_f16 v[84:87], v[124:127], v[68:71], v[84:87]\n"
    "v_pk_mul_f16 v65, v56, v49 op_sel_hi:[0,1]\n"
    "v_mfma_f32_16x16x32_f16 v[84:87], v[136:139], v[72:75], v[84:87]\n"
    "v_pk_mul_f16 v66, v48, v56 op_sel_hi:[0,1]\n"
    "v_smfmac_f32_16x16x64_f16 v[88:91], v[140:143], v[68:75], v2\n"
    "v_pk_mul_f16 v67, v48, v57 op_sel_hi:[0,1]\n"
    "v_mfma_f32_16x16x32_f16 v[76:79], v[144:147], v[60:63], v[76:79]\n"
    "v_pk_mul_f16 v68, v50, v56 op_sel_hi:[0,1]\n"
    "v_pk_mul_f16 v69, v50, v57 op_sel_hi:[0,1]\n"
    "v_mfma_f32_16x16x32_f16 v[76:79], v[156:159], v[64:67], v[76:79]\n"
    "s_cmp_eq_u32 s36, 0\n"
    "s_cbranch_scc1 .Lxd_on47\n"
    ".Lxd_on47r:\n"
    "v_pk_mul_f16 v70, v52, v56 op_sel_hi:[0,1]\n"
    "v_mfma_f32_16x16x32_f16 v[80:83], v[160:163], v[64:67], v[80:83]\n"
    "v_pk_mul_f16 v71, v52, v57 op_sel_hi:[0,1]\n"
    "v_mfma_f32_16x16x32_f16 v[80:83], v[148:151], v[60:63], v[80:83]\n"
    "ds_read_b32 v22, v25 offset:528\n"
    "v_pk_mul_f16 v72, v54, v56 op_sel_hi:[0,1]\n"
    "v_mfma_f32_16x16x32_f16 v[84:87], v[152:155], v[60:63], v[84:87]\n"
    "v_pk_mul_f16 v73, v54, v57 op_sel_hi:[0,1]\n"
    "v_mfma_f32_16x16x32_f16 v[84:87], v[164:167], v[64:67], v[84:87]\n"
    "v_pk_mul_f16 v74, v54, v54 op_sel_hi:[0,1]\n"
    "v_smfmac_f32_16x16x64_f16 v[88:91], v[168:171], v[60:67], v2\n"
    "v_pk_mul_f16 v75, v54, v49 op_sel_hi:[0,1]\n"
    "v_mfma_f32_16x16x32_f16 v[76:79], v[172:175], v[68:71], v[76:79]\n"
    "v_pk_mul_f16 v60, v48, v54 op_sel_hi:[0,1]\n"
    "v_pk_mul_f16 v61, v48, v55 op_sel_hi:[0,1]\n"
    "v_mfma_f32_16x16x32_f16 v[76:79], v[184:187], v[72:75], v[76:79]\n"
    "s_waitcnt lgkmcnt(0)\n"
    "v_pk_mul_f16 v62, v50, v54 op_sel_hi:[0,1]\n"
    "v_readfirstlane_b32 s45, v22\n"
    "v_mfma_f32_16x16x32_f16 v[80:83], v[188:191], v[72:75], v[80:83]\n"
    "s_cmp_lt_i32 s45, 16\n"
    "v_pk_mul_f16 v63, v50, v55 op_sel_hi:[0,1]\n"
    "v_mfma_f32_16x16x32_f16 v[80:83], v[176:179], v[68:71], v[80:83]\n"
    "s_cbranch_scc1 .Lxd_ck48\n"
    ".Lxd_ck48r:\n"
    "v_pk_mul_f16 v64, v52, v54 op_sel_hi:[0,1]\n"
    "v_mfma_f32_16x16x32_f16 v[84:87], v[180:183], v[68:71], v[84:87]\n"
    "ds_read_b64_tr_b16 v[44:45], v5 offset:8192\n"
    "v_pk_mul_f16 v65, v52, v55 op_sel_hi:[0,1]\n"
    "v_mfma_f32_16x16x32_f16 v[84:87], v[192:195], v[72:75], v[84:87]\n"
    "ds_read_b64_tr_b16 v[46:47], v6 offset:8192\n"
    "v_pk_mul_f16 v66, v52, v52 op_sel_hi:[0,1]\n"
    "v_smfmac_f32_16x16x64_f16 v[88:91], v[196:199], v[68:75], v2\n"
    "v_pk_mul_f16 v67, v52, v49 op_sel_hi:[0,1]\n"
    "v_mfma_f32_16x16x32_f16 v[76:79], v[200:203], v[60:63], v[76:79]\n"
    "v_pk_mul_f16 v68, v48, v52 op_sel_hi:[0,1]\n"
    "v_pk_mul_f16 v69, v48, v53 op_sel_hi:[0,1]\n"
    "v_mfma_f32_16x16x32_f16 v[76:79], v[212:215], v[64:67], v[76:79]\n"
    "v_pk_mul_f16 v70, v50, v52 op_sel_hi:[0,1]\n"
    "v_mfma_f32_16x16x32_f16 v[80:83], v[216:219], v[64:67], v[80:83]\n"
    "s_cbranch_vccnz .Lxd_pr44\n"
    "s_setprio 0\n"
    ".Lxd_pr44:\n"
    "v_pk_mul_f16 v71, v50, v53 op_sel_hi:[0,1]\n"
    "v_mfma_f32_16x16x32_f16 v[80:83], v[204:207], v[60:63], v[80:83]\n"
    "v_pk_mul_f16 v72, v50, v50 op_sel_hi:[0,1]\n"
    "v_mfma_f32_16x16x32_f16 v[84:87], v[208:211], v[60:63], v[84:87]\n"
    "v_pk_mul_f16 v73, v50, v49 op_sel_hi:[0,1]\n"
    "v_pk_mul_f16 v74, v48, v50 op_sel_hi:[0,1]\n"
    "v_mfma_f32_16x16x32_f16 v[84:87], v[220:223], v[64:67], v[84:87]\n"
    "v_pk_mul_f16 v75, v48, v51 op_sel_hi:[0,1]\n"
    "v_smfmac_f32_16x16x64_f16 v[88:91], v[224:227], v[60:67], v2\n"
    "ds_read_b64_tr_b16 v[48:49], v3 offset:8192\n"
    "ds_read_b64_tr_b16 v[58:59], v4 offset:11072\n"
    "v_add_u32 v25, 16, v25\n"
    "v_smfmac_f32_16x16x64_f16 v[88:91], v[252:255], v[68:75], v2\n"
    "s_waitcnt lgkmcnt(2)\n"
    "v_pk_mul_f16 v60, v44, s45 op_sel:[0,1]\n"
    "v_pk_mul_f16 v61, v45, s45 op_sel:[0,1]\n"
    "v_mfma_f32_16x16x32_f16 v[84:87], v[236:239], v[68:71], v[84:87]\n"
    "ds_read_b64_tr_b16 v[50:51], v4 offset:8768\n"
    "v_pk_mul_f16 v62, v46, s45 op_sel:[0,1]\n"
    "v_pk_mul_f16 v63, v47, s45 op_sel:[0,1]\n"
    "v_mfma_f32_16x16x32_f16 v[84:87], v[248:251], v[72:75], v[84:87]\n"
    "ds_read_b64_tr_b16 v[52:53], v4 offset:9344\n"
    "v_pk_mul_f16 v64, v44, v46\n"
    "v_pk_mul_f16 v65, v45, v47\n"
    "v_mfma_f32_16x16x32_f16 v[76:79], v[240:243], v[72:75], v[76:79]\n"
    "ds_read_b64_tr_b16 v[54:55], v4 offset:9920\n"
    "v_add_f32 v11, v88, v89\n"
    "v_add_f32 v13, v90, v91\n"
    "v_mfma_f32_16x16x32_f16 v[76:79], v[228:231], v[68:71], v[76:79]\n"
    "ds_read_b64_tr_b16 v[56:57], v4 offset:10496\n"
    "v_mov_b64 v[88:89], 0\n"
    "v_mov_b64 v[90:91], 0\n"
    "v_mfma_f32_16x16x32_f16 v[80:83], v[232:235], v[68:71], v[80:83]\n"
    "s_waitcnt lgkmcnt(4)\n"
    "v_pk_mul_f16 v66, v48, v48 op_sel_hi:[0,1]\n"
    "v_pk_mul_f16 v67, v48, v49 op_sel_hi:[0,1]\n"
    "v_mfma_f32_16x16x32_f16 v[80:83], v[244:247], v[72:75], v[80:83]\n"
    "s_cmp_eq_u32 s36, 0\n"
    "v_mul_f32 v11, v11, v13\n"
    "v_fmac_f32 v11, v84, v85\n"
    "v_fmac_f32 v11, v86, v87\n"
    "s_cbranch_scc1 .Lxd_ph0\n"
    "s_branch .Lxd_pl0\n"
    ".Lxd_last:\n"
    "s_cbranch_vccnz .Lxd_pr49\n"
    "s_setprio 0\n"
    ".Lxd_pr49:\n"
    "v_pk_mul_f16 v64, v52, v54 op_sel_hi:[0,1]\n"
    "v_mfma_f32_16x16x32_f16 v[84:87], v[180:183], v[68:71], v[84:87]\n"
    "v_pk_mul_f16 v65, v52, v55 op_sel_hi:[0,1]\n"
    "v_mfma_f32_16x16x32_f16 v[84:87], v[192:195], v[72:75], v[84:87]\n"
    "v_pk_mul_f16 v66, v52, v52 op_sel_hi:[0,1]\n"
    "v_smfmac_f32_16x16x64_f16 v[88:91], v[196:199], v[68:75], v2\n"
    "v_pk_mul_f16 v67, v52, v49 op_sel_hi:[0,1]\n"
    "v_mfma_f32_16x16x32_f16 v[76:79], v[200:203], v[60:63], v[76:79]\n"
    "v_pk_mul_f16 v68, v48, v52 op_sel_hi:[0,1]\n"
    "v_pk_mul_f16 v69, v48, v53 op_sel_hi:[0,1]\n"
    "v_mfma_f32_16x16x32_f16 v[76:79], v[212:215], v[64:67], v[76:79]\n"
    "v_pk_mul_f16 v70, v50, v52 op_sel_hi:[0,1]\n"
    "v_mfma_f32_16x16x32_f16 v[80:83], v[216:219], v[64:67], v[80:83]\n"
    "v_pk_mul_f16 v71, v50, v53 op_sel_hi:[0,1]\n"
    "v_mfma_f32_16x16x32_f16 v[80:83], v[204:207], v[60:63], v[80:83]\n"
    "v_pk_mul_f16 v72, v50, v50 op_sel_hi:[0,1]\n"
    "v_mfma_f32_16x16x32_f16 v[84:87], v[208:211], v[60:63], v[84:87]\n"
    "v_pk_mul_f16 v73, v50, v49 op_sel_hi:[0,1]\n"
    "v_pk_mul_f16 v74, v48, v50 op_sel_hi:[0,1]\n"
    "v_mfma_f32_16x16x32_f16 v[84:87], v[220:223], v[64:67], v[84:87]\n"
    "v_pk_mul_f16 v75, v48, v51 op_sel_hi:[0,1]\n"
    "v_smfmac_f32_16x16x64_f16 v[88:91], v[224:227], v[60:67], v2\n"
    "s_nop 0\n"
    "v_smfmac_f32_16x16x64_f16 v[88:91], v[252:255], v[68:75], v2\n"
    "v_mfma_f32_16x16x32_f16 v[84:87], v[236:239], v[68:71], v[84:87]\n"
    "v_mfma_f32_16x16x32_f16 v[84:87], v[248:251], v[72:75], v[84:87]\n"
    "v_mfma_f32_16x16x32_f16 v[76:79], v[240:243], v[72:75], v[76:79]\n"
    "v_mfma_f32_16x16x32_f16 v[76:79], v[228:231], v[68:71], v[76:79]\n"
    "v_mfma_f32_16x16x32_f16 v[80:83], v[232:235], v[68:71], v[80:83]\n"
    "v_mfma_f32_16x16x32_f16 v[80:83], v[244:247], v[72:75], v[80:83]\n"
    "s_nop 1\n"
    "v_add_f32 v11, v88, v89\n"
    "v_add_f32 v13, v90, v91\n"
    "v_mul_f32 v11, v11, v13\n"
    "v_fmac_f32 v11, v84, v85\n"
    "v_fmac_f32 v11, v86, v87\n"
    "s_nop 0\n"
    "v_fmac_f32 v11, v76, v80\n"
    "v_fmac_f32 v11, v77, v81\n"
    "v_fmac_f32 v11, v78, v82\n"
    "s_setpc_b64 s[60:61]\n"
    ".Lxd_fin:\n"
    "v_lshl_add_u32 v68, v18, 6, v20\n"
    "v_mov_b32 v67, 154144\n"
    "v_permlane32_swap_b32 v7, v8\n"
    "v_permlane32_swap_b32 v9, v10\n"
    "v_mul_f32 v7, v0, v7\n"
    "v_mul_f32 v9, v0, v9\n"
    "v_fmac_f32 v7, v23, v8\n"
    "v_fmac_f32 v9, v23, v10\n"
    "ds_read_b32 v67, v67\n"
    "v_lshlrev_b32 v69, 2, v68\n"
    "v_permlane16_swap_b32 v7, v9\n"
    "v_add_f32 v7, v7, v9\n"
    "v_add_u32 v68, 150528, v68\n"
    "v_add_u32 v69, 152128, v69\n"
    "v_add_f32_dpp v7, v7, v7 quad_perm:[1,0,3,2] row_mask:0xf bank_mask:0xf\n"
    "s_lshl_b32 s26, s26, 16\n"
    "s_nop 0\n"
    "v_add_f32_dpp v7, v7, v7 quad_perm:[2,3,0,1] row_mask:0xf bank_mask:0xf\n"
    "s_nop 1\n"
    "v_add_f32_dpp v7, v7, v7 row_half_mirror row_mask:0xf bank_mask:0xf\n"
    "s_nop 1\n"
    "v_add_f32_dpp v7, v7, v7 row_mirror row_mask:0xf bank_mask:0xf\n"
    "ds_write_b32 v14, v7\n"
    "s_cmp_lg_u32 s56, 2\n"
    "s_cbranch_scc1 .Lxd_f2\n"
    ".Lxd_f3:\n"
    "s_waitcnt vmcnt(0) lgkmcnt(0)\n"
    "s_barrier\n"
    "s_cmp_lg_u32 s20, 0\n"
    "s_cbranch_scc1 .Lxd_done\n"
    "ds_read_b128 v[60:63], v69\n"
    "ds_read_b32 v64, v68\n"
    "ds_read_u16 v65, v68 offset:512\n"
    "ds_read_b32 v66, v68 offset:1024\n"
    "s_waitcnt lgkmcnt(3)\n"
    "v_pk_add_f32 v[60:61], v[60:61], v[62:63]\n"
    "s_waitcnt lgkmcnt(0)\n"
    "v_cmp_lt_i32 vcc, 0, v64\n"
    "v_lshrrev_b32 v65, 4, v65\n"
    "v_add_f32 v60, v60, v61\n"
    "v_lshl_add_u32 v65, v65, 2, v67\n"
    "v_add_f32 v66, s26, v66\n"
    "v_ldexp_f32 v60, v60, v65\n"
    "s_mov_b64 exec, vcc\n"
    "v_add_f32 v60, v60, v66\n"
    "v_cvt_pk_bf16_f32 v60, v60, v60\n"
    "global_store_short v64, v60, s[70:71] offset:-2\n"
    "s_mov_b64 exec, -1\n"
    ".Lxd_done:\n"
    "s_endpgm\n"
    ".Lxd_cin:\n"
    "s_lshr_b32 s20, s43, 6\n"
    "v_and_b32 v1, 63, v0\n"
    "v_lshlrev_b32 v42, 4, v1\n"
    "s_and_b32 s40, s2, 3\n"
    "s_lshl2_add_u32 s23, s40, s20\n"
    "s_lshl1_add_u32 s41, s40, s20\n"
    "s_add_u32 s41, s41, 12\n"
    "s_cmp_ge_u32 s20, 4\n"
    "s_cselect_b32 s23, s41, s23\n"
    "s_add_u32 s41, s40, 24\n"
    "s_cmp_ge_u32 s20, 6\n"
    "s_cselect_b32 s23, s41, s23\n"
    "s_cselect_b32 s24, 1, 0\n"
    "s_min_u32 s34, s23, 25\n"
    "s_lshr_b32 s35, s2, 2\n"
    "v_and_b32 v190, 31, v0\n"
    "v_min_u32 v190, 25, v190\n"
    "v_mul_u32_u24 v190, 52, v190\n"
    "v_bfe_u32 v191, v0, 5, 1\n"
    "v_lshl_add_u32 v190, v191, 4, v190\n"
    "s_mul_i32 s40, s34, 1352\n"
    "s_xor_b32 s66, s20, 7\n"
    "s_or_b32 s66, s66, s2\n"
    "s_waitcnt lgkmcnt(0)\n"
    "s_load_dwordx4 s[68:71], s[0:1], 64\n"
    "s_add_u32 s38, s12, s40\n"
    "s_addc_u32 s39, s13, 0\n"
    "s_cmp_lg_u32 s66, 0\n"
    "s_cbranch_scc1 .Lxd_on50\n"
    "v_and_b32 v188, 31, v0\n"
    "v_lshlrev_b32 v188, 7, v188\n"
    "v_add_u32 v188, 32768, v188\n"
    "v_mov_b32 v176, 114\n"
    "global_store_dword v188, v176, s[16:17] sc1\n"
    "global_load_dwordx4 v[180:183], v42, s[16:17] sc1\n"
    ".Lxd_on50:\n"
    "global_load_dwordx4 v[80:83], v190, s[38:39] offset:0\n"
    "global_load_dwordx4 v[84:87], v190, s[38:39] offset:32\n"
    "s_getpc_b64 s[60:61]\n"
    "s_add_u32 s60, s60, xd_tab@rel32@lo+4\n"
    "s_addc_u32 s61, s61, xd_tab@rel32@hi+12\n"
    "v_lshl_add_u32 v188, s24, 10, v42\n"
    "s_lshl1_add_u32 s40, s24, s35\n"
    "v_lshl_add_u32 v189, s40, 11, v42\n"
    "s_load_dword s25, s[16:17], 0\n"
    "global_load_dwordx4 v[44:47], v188, s[60:61]\n"
    "global_load_dwordx4 v[48:51], v189, s[60:61] offset:2048\n"
    "global_load_dwordx4 v[52:55], v189, s[60:61] offset:3072\n"
    "s_setprio 0\n"
    "v_mov_b64 v[188:189], 0\n"
    "v_lshlrev_b32 v190, 3, v0\n"
    "v_lshrrev_b32 v186, 5, v1\n"
    "ds_write_b64 v190, v[188:189] offset:36864\n"
    "v_bfe_u32 v190, v0, 1, 4\n"
    "v_lshlrev_b32 v72, 2, v186\n"
    "v_sub_u32 v190, v190, v72\n"
    "v_and_b32 v72, 16, v42\n"
    "s_movk_i32 s40, 0x3f80\n"
    "v_lshlrev_b32_e64 v191, v72, s40\n"
    "v_cmp_eq_u32 s[60:61], 0, v190\n"
    "v_cmp_eq_u32 s[62:63], 1, v190\n"
    "v_cmp_eq_u32 s[64:65], 2, v190\n"
    "v_cmp_eq_u32 s[50:51], 3, v190\n"
    "v_cndmask_b32 v72, 0, v191, s[60:61]\n"
    "v_cndmask_b32 v73, 0, v191, s[62:63]\n"
    "v_cndmask_b32 v74, 0, v191, s[64:65]\n"
    "v_cndmask_b32 v75, 0, v191, s[50:51]\n"
    "v_cmp_eq_u32 s[60:61], 8, v190\n"
    "v_cmp_eq_u32 s[62:63], 9, v190\n"
    "v_cmp_eq_u32 s[64:65], 10, v190\n"
    "v_cmp_eq_u32 s[50:51], 11, v190\n"
    "v_cndmask_b32 v76, 0, v191, s[60:61]\n"
    "v_cndmask_b32 v77, 0, v191, s[62:63]\n"
    "v_cndmask_b32 v78, 0, v191, s[64:65]\n"
    "v_cndmask_b32 v79, 0, v191, s[50:51]\n"
    "s_lshl_b32 s30, s20, 12\n"
    "s_add_u32 s30, s30, 65536\n"
    "v_and_b32 v188, 31, v0\n"
    "v_mul_u32_u24 v192, 528, v186\n"
    "v_lshl_add_u32 v192, v188, 2, v192\n"
    "v_add_u32 v192, s30, v192\n"
    "s_lshl_b32 s40, s35, 6\n"
    "s_add_u32 s40, s40, s20\n"
    "s_lshl1_add_u32 s41, s20, s35\n"
    "s_add_u32 s41, s41, 120\n"
    "s_cmp_ge_u32 s20, 4\n"
    "s_cselect_b32 s40, s41, s40\n"
    "s_lshl1_add_u32 s41, s35, 384\n"
    "s_cmp_ge_u32 s20, 6\n"
    "s_cselect_b32 s40, s41, s40\n"
    "s_cselect_b32 s42, 1, 192\n"
    "s_and_b32 s41, s2, 3\n"
    "s_lshl2_add_u32 s40, s41, s40\n"
    "v_lshrrev_b32 v188, 3, v1\n"
    "v_mul_u32_u24 v188, 448, v188\n"
    "v_bfe_u32 v189, v0, 2, 1\n"
    "v_mad_u32_u24 v188, v189, s42, v188\n"
    "v_and_b32 v189, 48, v42\n"
    "v_add3_u32 v188, v188, v189, s40\n"
    "v_lshlrev_b32 v193, 4, v188\n"
    "v_mov_b32 v177, 0\n"
    "s_waitcnt vmcnt(0) lgkmcnt(0)\n"
    "s_cmp_lg_u32 s66, 0\n"
    "s_cbranch_scc1 .Lxd_on51\n"
    "s_mov_b32 s40, 0x9e3779b1\n"
    "v_mul_lo_u32 v144, v180, s40\n"
    "v_xor_b32 v145, 0xa5c30f96, v180\n"
    "v_alignbit_b32 v144, s25, v144, 24\n"
    "v_mul_lo_u32 v146, v181, s40\n"
    "v_xor_b32 v147, 0xa5c30f96, v181\n"
    "v_alignbit_b32 v146, s25, v146, 24\n"
    "v_mul_lo_u32 v148, v182, s40\n"
    "v_xor_b32 v149, 0xa5c30f96, v182\n"
    "v_alignbit_b32 v148, s25, v148, 24\n"
    "v_mul_lo_u32 v150, v183, s40\n"
    "v_xor_b32 v151, 0xa5c30f96, v183\n"
    "v_alignbit_b32 v150, s25, v150, 24\n"
    "v_lshlrev_b32 v180, 1, v42\n"
    "s_add_u32 s40, s16, 4096\n"
    "s_addc_u32 s41, s17, 0\n"
    "global_store_dwordx4 v180, v[144:147], s[40:41] sc1\n"
    "global_store_dwordx4 v180, v[148:151], s[40:41] offset:16 sc1\n"
    ".Lxd_on51:\n"
    "s_barrier\n"
    "s_branch .Lxd_bD\n"
    ".Lxd_s2:\n"
    "s_cmp_lt_u32 s2, 4\n"
    "s_cbranch_scc1 .Lxd_cin\n"
    ".Lxd_bR:\n"
    "s_lshr_b32 s20, s43, 6\n"
    "v_and_b32 v1, 63, v0\n"
    "v_lshlrev_b32 v42, 4, v1\n"
    "s_mul_i32 s40, s20, 4480\n"
    "s_min_u32 s40, s40, 30672\n"
    "s_mov_b32 m0, s40\n"
    "s_add_u32 s40, s40, 70304\n"
    "s_waitcnt lgkmcnt(0)\n"
    "s_load_dwordx4 s[68:71], s[0:1], 64\n"
    "s_add_u32 s38, s12, s40\n"
    "s_addc_u32 s39, s13, 0\n"
    "global_load_lds_dwordx4 v42, s[38:39] offset:0\n"
    "global_load_lds_dwordx4 v42, s[38:39] offset:1024\n"
    "global_load_lds_dwordx4 v42, s[38:39] offset:2048\n"
    "global_load_lds_dwordx4 v42, s[38:39] offset:3072\n"
    "global_load_lds_dwordx4 v42, s[38:39] offset:3456\n"
    "s_and_b32 s40, s2, 3\n"
    "s_lshl2_add_u32 s23, s40, s20\n"
    "s_lshl1_add_u32 s41, s40, s20\n"
    "s_add_u32 s41, s41, 12\n"
    "s_cmp_ge_u32 s20, 4\n"
    "s_cselect_b32 s23, s41, s23\n"
    "s_add_u32 s41, s40, 24\n"
    "s_cmp_ge_u32 s20, 6\n"
    "s_cselect_b32 s23, s41, s23\n"
    "s_cselect_b32 s24, 1, 0\n"
    "s_min_u32 s34, s23, 25\n"
    "s_lshr_b32 s35, s2, 2\n"
    "v_lshrrev_b32 v190, 2, v1\n"
    "v_and_b32 v188, 48, v42\n"
    "s_movk_i32 s42, 1352\n"
    "v_min_u32 v191, 9, v190\n"
    "v_mad_u32_u24 v190, v190, s42, v188\n"
    "v_mad_u32_u24 v191, v191, s42, v188\n"
    "v_add_u32 v191, 20608, v191\n"
    "s_mul_i32 s41, s34, 52\n"
    "s_add_u32 s41, s41, 35152\n"
    "s_cmp_lg_u32 s20, 7\n"
    "s_cbranch_scc1 .Lxd_on52\n"
    "v_min_u32 v188, 38, v1\n"
    "s_mov_b32 m0, 40960\n"
    "v_lshlrev_b32 v188, 2, v188\n"
    "global_load_lds_dword v188, s[14:15]\n"
    ".Lxd_on52:\n"
    "s_lshl_b32 s28, s20, 11\n"
    "s_add_u32 s28, s28, 49152\n"
    "s_mov_b32 m0, s28\n"
    "s_add_u32 s38, s12, s41\n"
    "s_addc_u32 s39, s13, 0\n"
    "global_load_lds_dwordx4 v190, s[38:39] offset:0\n"
    "global_load_lds_dwordx4 v191, s[38:39] offset:1024\n"
    "s_getpc_b64 s[60:61]\n"
    "s_add_u32 s60, s60, xd_tab@rel32@lo+4\n"
    "s_addc_u32 s61, s61, xd_tab@rel32@hi+12\n"
    "v_lshl_add_u32 v188, s24, 10, v42\n"
    "s_lshl1_add_u32 s40, s24, s35\n"
    "v_lshl_add_u32 v189, s40, 11, v42\n"
    "s_load_dword s25, s[16:17], 0\n"
    "global_load_dwordx4 v[44:47], v188, s[60:61]\n"
    "global_load_dwordx4 v[48:51], v189, s[60:61] offset:2048\n"
    "global_load_dwordx4 v[52:55], v189, s[60:61] offset:3072\n"
    "s_setprio 0\n"
    "v_mov_b64 v[188:189], 0\n"
    "v_lshlrev_b32 v190, 3, v0\n"
    "v_lshrrev_b32 v186, 5, v1\n"
    "ds_write_b64 v190, v[188:189] offset:36864\n"
    "v_lshrrev_b32 v184, 4, v1\n"
    "v_and_b32 v185, 15, v0\n"
    "v_lshrrev_b32 v190, 2, v185\n"
    "v_lshl_add_u32 v64, v184, 3, v190\n"
    "v_add_u32 v65, 4, v64\n"
    "s_lshr_b32 s21, s20, 2\n"
    "s_and_b32 s43, s20, 3\n"
    "s_mul_i32 s40, s21, 13\n"
    "s_lshl2_add_u32 s40, s43, s40\n"
    "v_and_b32 v191, 3, v0\n"
    "v_add_lshl_u32 v188, v191, s40, 3\n"
    "v_min_u32 v64, 25, v64\n"
    "v_mad_u32_u24 v64, v64, s42, v188\n"
    "v_min_u32 v65, 25, v65\n"
    "v_mad_u32_u24 v65, v65, s42, v188\n"
    "v_lshl_add_u32 v188, s43, 4, v185\n"
    "v_cmp_lt_u32 vcc, 25, v188\n"
    "v_cmp_lt_u32 s[60:61], 51, v188\n"
    "v_lshl_add_u32 v66, s21, 6, v188\n"
    "v_cndmask_b32_e64 v189, 0, 6, vcc\n"
    "v_add_lshl_u32 v66, v66, v189, 1\n"
    "v_mov_b32 v189, 0x40000\n"
    "v_cmp_gt_u32 vcc, 26, v1\n"
    "v_cndmask_b32 v66, v66, v189, s[60:61]\n"
    "v_lshlrev_b32 v67, 6, v1\n"
    "v_cndmask_b32 v67, v189, v67, vcc\n"
    "v_lshlrev_b32 v68, 4, v184\n"
    "v_add_u32 v68, 40960, v68\n"
    "v_lshlrev_b32 v69, 1, v1\n"
    "v_lshl_add_u32 v190, v186, 3, v190\n"
    "v_and_b32 v188, 1, v184\n"
    "v_lshl_add_u32 v188, v188, 2, v191\n"
    "v_cmp_eq_u32 vcc, 7, v188\n"
    "v_lshlrev_b32 v188, 3, v188\n"
    "v_lshl_add_u32 v70, v190, 6, v188\n"
    "v_add_u32 v71, s28, v70\n"
    "v_mov_b32 v188, 38912\n"
    "v_cndmask_b32 v71, v71, v188, vcc\n"
    "v_cmp_gt_u32 vcc, 3, v184\n"
    "s_waitcnt vmcnt(5) lgkmcnt(0)\n"
    "s_barrier\n"
    "ds_read2_b64 v[176:179], v68 offset0:13 offset1:14\n"
    "ds_read_u16 v68, v69 offset:41012\n"
    "ds_read_u16 v69, v69 offset:40960\n"
    "ds_read_b64_tr_b16 v[112:113], v64 offset:0\n"
    "ds_read_b64_tr_b16 v[114:115], v65 offset:0\n"
    "ds_read_b64_tr_b16 v[116:117], v64 offset:208\n"
    "ds_read_b64_tr_b16 v[118:119], v65 offset:208\n"
    "ds_read_b64_tr_b16 v[120:121], v64 offset:416\n"
    "ds_read_b64_tr_b16 v[122:123], v65 offset:416\n"
    "ds_read_b64_tr_b16 v[124:125], v64 offset:624\n"
    "ds_read_b64_tr_b16 v[126:127], v65 offset:624\n"
    "ds_read_b64_tr_b16 v[128:129], v64 offset:832\n"
    "ds_read_b64_tr_b16 v[130:131], v65 offset:832\n"
    "ds_read_b64_tr_b16 v[132:133], v64 offset:1040\n"
    "ds_read_b64_tr_b16 v[134:135], v65 offset:1040\n"
    "ds_read_b64_tr_b16 v[136:137], v64 offset:1248\n"
    "ds_read_b64_tr_b16 v[138:139], v65 offset:1248\n"
    "s_waitcnt lgkmcnt(14)\n"
    "v_cndmask_b32 v177, 0, v177, vcc\n"
    "v_cndmask_b32 v178, 0, v178, vcc\n"
    "v_cndmask_b32 v179, 0, v179, vcc\n"
    "ds_write_b16 v67, v68 offset:36920\n"
    "s_waitcnt lgkmcnt(13)\n"
    "v_mfma_f32_16x16x32_bf16 v[144:147], v[176:179], v[112:115], 0\n"
    "s_waitcnt lgkmcnt(11)\n"
    "v_mfma_f32_16x16x32_bf16 v[148:151], v[176:179], v[116:119], 0\n"
    "s_waitcnt lgkmcnt(9)\n"
    "v_mfma_f32_16x16x32_bf16 v[152:155], v[176:179], v[120:123], 0\n"
    "s_waitcnt lgkmcnt(7)\n"
    "v_mfma_f32_16x16x32_bf16 v[156:159], v[176:179], v[124:127], 0\n"
    "s_waitcnt lgkmcnt(5)\n"
    "v_mfma_f32_16x16x32_bf16 v[160:163], v[176:179], v[128:131], 0\n"
    "s_waitcnt lgkmcnt(3)\n"
    "v_mfma_f32_16x16x32_bf16 v[164:167], v[176:179], v[132:135], 0\n"
    "s_waitcnt lgkmcnt(1)\n"
    "v_mfma_f32_16x16x32_bf16 v[168:171], v[176:179], v[136:139], 0\n"
    "s_waitcnt vmcnt(3)\n"
    "ds_read_b64_tr_b16 v[80:81], v71 offset:0\n"
    "ds_read_b64_tr_b16 v[82:83], v71 offset:256\n"
    "ds_read_b64_tr_b16 v[84:85], v71 offset:1024\n"
    "ds_read_b64_tr_b16 v[86:87], v71 offset:1280\n"
    "v_cvt_pk_bf16_f32 v144, v144, v144\n"
    "ds_write_b16 v66, v144 offset:36864\n"
    "v_cvt_pk_bf16_f32 v148, v148, v148\n"
    "ds_write_b16 v66, v148 offset:37120\n"
    "v_cvt_pk_bf16_f32 v152, v152, v152\n"
    "ds_write_b16 v66, v152 offset:37376\n"
    "v_cvt_pk_bf16_f32 v156, v156, v156\n"
    "ds_write_b16 v66, v156 offset:37632\n"
    "v_cvt_pk_bf16_f32 v160, v160, v160\n"
    "ds_write_b16 v66, v160 offset:37888\n"
    "v_cvt_pk_bf16_f32 v164, v164, v164\n"
    "ds_write_b16 v66, v164 offset:38144\n"
    "s_cmp_lg_u32 s21, 0\n"
    "s_cbranch_scc1 .Lxd_on53\n"
    "v_cvt_pk_bf16_f32 v168, v168, v168\n"
    "ds_write_b16 v66, v168 offset:38400\n"
    ".Lxd_on53:\n"
    "v_lshlrev_b32 v177, 16, v69\n"
    "s_waitcnt lgkmcnt(0)\n"
    "s_barrier\n"
    "ds_read_b64_tr_b16 v[72:73], v70 offset:36864\n"
    "ds_read_b64_tr_b16 v[74:75], v70 offset:37120\n"
    "ds_read_b64_tr_b16 v[76:77], v70 offset:37888\n"
    "ds_read_b64_tr_b16 v[78:79], v70 offset:38144\n"
    "s_lshl_b32 s30, s20, 12\n"
    "s_add_u32 s30, s30, 65536\n"
    "v_and_b32 v188, 31, v0\n"
    "v_mul_u32_u24 v192, 528, v186\n"
    "v_lshl_add_u32 v192, v188, 2, v192\n"
    "v_add_u32 v192, s30, v192\n"
    "s_lshl_b32 s40, s35, 6\n"
    "s_add_u32 s40, s40, s20\n"
    "s_lshl1_add_u32 s41, s20, s35\n"
    "s_add_u32 s41, s41, 120\n"
    "s_cmp_ge_u32 s20, 4\n"
    "s_cselect_b32 s40, s41, s40\n"
    "s_lshl1_add_u32 s41, s35, 384\n"
    "s_cmp_ge_u32 s20, 6\n"
    "s_cselect_b32 s40, s41, s40\n"
    "s_cselect_b32 s42, 1, 192\n"
    "s_and_b32 s41, s2, 3\n"
    "s_lshl2_add_u32 s40, s41, s40\n"
    "v_lshrrev_b32 v188, 3, v1\n"
    "v_mul_u32_u24 v188, 448, v188\n"
    "v_bfe_u32 v189, v0, 2, 1\n"
    "v_mad_u32_u24 v188, v189, s42, v188\n"
    "v_and_b32 v189, 48, v42\n"
    "v_add3_u32 v188, v188, v189, s40\n"
    "v_lshlrev_b32 v193, 4, v188\n"
    "s_waitcnt vmcnt(0) lgkmcnt(0)\n"
    ".Lxd_bD:\n"
    "v_mfma_f32_32x32x16_bf16 v[96:111], v[72:75], v[80:83], 0\n"
    "v_mfma_f32_32x32x16_bf16 v[96:111], v[80:83], v[72:75], v[96:111]\n"
    "v_mfma_f32_32x32x16_bf16 v[96:111], v[76:79], v[84:87], v[96:111]\n"
    "v_mfma_f32_32x32x16_bf16 v[96:111], v[84:87], v[76:79], v[96:111]\n"
    "v_readlane_b32 s43, v177, s34\n"
    "v_mad_u32_u16 v64, v44, 4, s30 op_sel:[0,0,0,0]\n"
    "v_mad_u32_u16 v65, v44, 4, s30 op_sel:[1,0,0,0]\n"
    "v_mad_u32_u16 v66, v45, 4, s30 op_sel:[0,0,0,0]\n"
    "v_mad_u32_u16 v67, v45, 4, s30 op_sel:[1,0,0,0]\n"
    "v_mad_u32_u16 v68, v46, 4, s30 op_sel:[0,0,0,0]\n"
    "v_mad_u32_u16 v69, v46, 4, s30 op_sel:[1,0,0,0]\n"
    "v_mad_u32_u16 v70, v47, 4, s30 op_sel:[0,0,0,0]\n"
    "v_mad_u32_u16 v71, v47, 4, s30 op_sel:[1,0,0,0]\n"
    "v_mov_b32 v184, 40944\n"
    "s_and_b32 s25, s25, 0xffffff\n"
    "s_mul_i32 s40, s25, 0x9e3779b1\n"
    "s_xor_b32 s41, s25, 0xa5c30f96\n"
    "s_lshr_b32 s40, s40, 24\n"
    "v_writelane_b32 v108, s43, 60\n"
    "ds_write2_b32 v192, v96, v97 offset0:0 offset1:33\n"
    "ds_write2_b32 v192, v98, v99 offset0:66 offset1:99\n"
    "v_add_u32 v192, 1056, v192\n"
    "ds_write2_b32 v192, v100, v101 offset0:0 offset1:33\n"
    "ds_write2_b32 v192, v102, v103 offset0:66 offset1:99\n"
    "v_add_u32 v192, 1056, v192\n"
    "ds_write2_b32 v192, v104, v105 offset0:0 offset1:33\n"
    "ds_write2_b32 v192, v106, v107 offset0:66 offset1:99\n"
    "v_add_u32 v192, 1056, v192\n"
    "ds_write2_b32 v192, v108, v109 offset0:0 offset1:33\n"
    "ds_read_b32 v56, v64\n"
    "ds_read_b32 v57, v65\n"
    "ds_read_b32 v58, v66\n"
    "ds_read_b32 v59, v67\n"
    "ds_read_b32 v60, v68\n"
    "ds_read_b32 v61, v69\n"
    "ds_read_b32 v62, v70\n"
    "ds_read_b32 v63, v71\n"
    "s_waitcnt lgkmcnt(6)\n"
    "v_pk_mul_f32 v[56:57], v[56:57], v[48:49]\n"
    "v_max3_f32 v188, |v56|, |v57|, 0\n"
    "s_waitcnt lgkmcnt(4)\n"
    "v_pk_mul_f32 v[58:59], v[58:59], v[50:51]\n"
    "v_max3_f32 v188, |v58|, |v59|, v188\n"
    "s_waitcnt lgkmcnt(2)\n"
    "v_pk_mul_f32 v[60:61], v[60:61], v[52:53]\n"
    "v_max3_f32 v188, |v60|, |v61|, v188\n"
    "s_waitcnt lgkmcnt(0)\n"
    "v_pk_mul_f32 v[62:63], v[62:63], v[54:55]\n"
    "v_max3_f32 v188, |v62|, |v63|, v188\n"
    "s_nop 1\n"
    "v_max_u32_dpp v188, v188, v188 quad_perm:[1,0,3,2] row_mask:0xf bank_mask:0xf\n"
    "s_nop 1\n"
    "v_max_u32_dpp v188, v188, v188 quad_perm:[2,3,0,1] row_mask:0xf bank_mask:0xf\n"
    "s_nop 1\n"
    "v_max_u32_dpp v188, v188, v188 row_half_mirror row_mask:0xf bank_mask:0xf\n"
    "s_nop 1\n"
    "v_max_u32_dpp v188, v188, v188 row_mirror row_mask:0xf bank_mask:0xf\n"
    "s_mov_b32 exec_lo, 0x10001\n"
    "s_mov_b32 exec_hi, 1\n"
    "ds_max_u32 v184, v188\n"
    "s_mov_b64 exec, -1\n"
    "s_add_u32 s38, s16, 1047552\n"
    "s_addc_u32 s39, s17, 0\n"
    "s_cmp_lt_u32 s23, 26\n"
    "s_cselect_b32 s43, 0xfff8fff8, 0\n"
    "s_waitcnt lgkmcnt(0)\n"
    "s_barrier\n"
    "ds_read_b32 v188, v184\n"
    "v_cmp_le_u32 vcc, 0x400, v193\n"
    "s_cmp_eq_u32 s20, 7\n"
    "s_cselect_b32 s42, 0, 48\n"
    "s_bfm_b64 s[60:61], s42, 0\n"
    "s_and_b64 s[60:61], s[60:61], vcc\n"
    "s_waitcnt lgkmcnt(0)\n"
    "v_lshrrev_b32 v188, 23, v188\n"
    "v_max_u32 v188, 40, v188\n"
    "v_min_u32 v188, 250, v188\n"
    "v_sub_u32 v190, 268, v188\n"
    "v_lshlrev_b32 v190, 23, v190\n"
    "v_pk_mul_f32 v[56:57], v[56:57], v[190:191] op_sel_hi:[1,0]\n"
    "v_cvt_pkrtz_f16_f32 v56, v56, v57\n"
    "v_add_u32 v56, 0x40004, v56\n"
    "v_and_b32 v56, s43, v56\n"
    "v_pk_mul_f32 v[58:59], v[58:59], v[190:191] op_sel_hi:[1,0]\n"
    "v_cvt_pkrtz_f16_f32 v57, v58, v59\n"
    "v_add_u32 v57, 0x40004, v57\n"
    "v_and_b32 v57, s43, v57\n"
    "v_pk_mul_f32 v[60:61], v[60:61], v[190:191] op_sel_hi:[1,0]\n"
    "v_cvt_pkrtz_f16_f32 v58, v60, v61\n"
    "v_add_u32 v58, 0x40004, v58\n"
    "v_and_b32 v58, s43, v58\n"
    "v_pk_mul_f32 v[62:63], v[62:63], v[190:191] op_sel_hi:[1,0]\n"
    "v_cvt_pkrtz_f16_f32 v59, v62, v63\n"
    "v_add_u32 v59, 0x40004, v59\n"
    "v_and_b32 v59, s43, v59\n"
    "v_lshl_or_b32 v188, v188, 8, s40\n"
    "v_mov_b32 v189, s41\n"
    "s_lshl3_add_u32 s40, s2, 40960\n"
    "v_mov_b32 v190, s40\n"
    "s_cmp_eq_u32 s20, 7\n"
    "s_cbranch_scc1 .Lxd_b3\n"
    "s_mov_b64 exec, s[60:61]\n"
    "global_store_dwordx4 v193, v[56:59], s[38:39] sc1\n"
    "s_mov_b64 exec, -1\n"
    ".Lxd_b3:\n"
    "s_waitcnt vmcnt(0)\n"
    "s_barrier\n"
    "s_cmp_lg_u32 s20, 0\n"
    "s_cbranch_scc1 .Lxd_on54\n"
    "s_mov_b64 exec, 1\n"
    "global_store_dwordx2 v190, v[188:189], s[16:17] sc1\n"
    "s_mov_b64 exec, -1\n"
    ".Lxd_on54:\n"
    "s_branch .Lxd_h1\n"
    ".Lxd_help:\n"
    "s_lshr_b32 s20, s43, 6\n"
    "v_and_b32 v1, 63, v0\n"
    "v_lshlrev_b32 v42, 4, v1\n"
    "s_lshr_b32 s41, s2, 3\n"
    "s_sub_u32 s41, s41, 1\n"
    "s_cmp_lg_u32 s20, 7\n"
    "s_cbranch_scc1 .Lxd_on56\n"
    "s_mul_i32 s40, s41, 60\n"
    "v_min_u32 v7, 59, v1\n"
    "v_add_lshl_u32 v7, v7, s40, 7\n"
    "s_getpc_b64 s[60:61]\n"
    ".Lxd_pc55:\n"
    "s_sub_u32 s60, s60, .Lxd_pc55-.Lxd_top\n"
    "s_subb_u32 s61, s61, 0\n"
    "v_min_u32 v7, .Lxd_end-.Lxd_top-4, v7\n"
    "global_load_dword v7, v7, s[60:61]\n"
    ".Lxd_on56:\n"
    "v_mul_u32_u24 v68, 52, v1\n"
    "v_lshrrev_b32 v68, 8, v68\n"
    "v_mad_i32_i24 v69, v68, -5, v1\n"
    "s_and_b32 s40, s2, 0xf8\n"
    "s_add_u32 s40, s40, s20\n"
    "s_mul_i32 s40, s40, 13\n"
    "s_sub_u32 s40, s40, 104\n"
    "v_add_u32 v68, s40, v68\n"
    "v_min_u32 v68, 0x1ff, v68\n"
    "v_lshrrev_b32 v70, 8, v68\n"
    "s_movk_i32 s40, 1792\n"
    "v_mad_u32_u24 v68, v70, s40, v68\n"
    "s_and_b32 s40, s2, 7\n"
    "v_lshl_add_u32 v68, s40, 8, v68\n"
    "v_mul_u32_u24 v68, 104, v68\n"
    "s_mul_i32 s40, s40, 3\n"
    "v_add3_u32 v70, v69, s40, 2\n"
    "v_subrev_u32 v71, 24, v70\n"
    "v_min_u32 v70, v70, v71\n"
    "v_lshl_add_u32 v70, v70, 2, v68\n"
    "s_waitcnt lgkmcnt(0)\n"
    "s_load_dwordx4 s[68:71], s[0:1], 64\n"
    "global_load_dword v60, v70, s[4:5]\n"
    "v_add3_u32 v70, v69, s40, 7\n"
    "v_subrev_u32 v71, 24, v70\n"
    "v_min_u32 v70, v70, v71\n"
    "v_lshl_add_u32 v70, v70, 2, v68\n"
    "global_load_dword v61, v70, s[4:5]\n"
    "v_add3_u32 v70, v69, s40, 12\n"
    "v_subrev_u32 v71, 24, v70\n"
    "v_min_u32 v70, v70, v71\n"
    "v_lshl_add_u32 v70, v70, 2, v68\n"
    "global_load_dword v62, v70, s[4:5]\n"
    "v_add3_u32 v70, v69, s40, 17\n"
    "v_subrev_u32 v71, 24, v70\n"
    "v_min_u32 v70, v70, v71\n"
    "v_lshl_add_u32 v70, v70, 2, v68\n"
    "global_load_dword v63, v70, s[4:5]\n"
    "v_add3_u32 v70, v69, s40, 22\n"
    "v_subrev_u32 v71, 24, v70\n"
    "v_min_u32 v70, v70, v71\n"
    "v_add_u32 v71, 22, v69\n"
    "v_cmp_lt_u32 vcc, 23, v71\n"
    "v_min_u32 v71, 25, v71\n"
    "s_nop 0\n"
    "v_cndmask_b32 v70, v70, v71, vcc\n"
    "v_lshl_add_u32 v70, v70, 2, v68\n"
    "global_load_dword v64, v70, s[4:5]\n"
    "s_cmp_lg_u32 s20, 6\n"
    "s_cbranch_scc1 .Lxd_on57\n"
    "v_min_u32 v12, 25, v1\n"
    "s_mov_b32 m0, 159744\n"
    "v_lshlrev_b32 v12, 7, v12\n"
    "global_load_lds_dword v12, s[10:11]\n"
    "global_load_lds_dword v12, s[18:19]\n"
    ".Lxd_on57:\n"
    ".Lxd_h1:\n"
    "s_lshr_b32 s3, s2, 4\n"
    "s_mul_i32 s40, s3, 3\n"
    "s_add_u32 s47, s2, s40\n"
    "s_and_b32 s47, s47, 15\n"
    "s_lshr_b32 s21, s20, 2\n"
    "s_lshl1_add_u32 s22, s21, s20\n"
    "s_and_b32 s22, s22, 3\n"
    "s_lshl_b32 s58, s21, 8\n"
    "s_add_u32 s58, s58, 150528\n"
    "s_mov_b32 s40, s47\n"
    "s_mul_i32 s54, s40, 512\n"
    "s_add_u32 s54, s54, 2\n"
    "s_lshl_b32 s40, s40, 7\n"
    "s_add_u32 s40, s40, 32768\n"
    "s_add_u32 s52, s16, s40\n"
    "s_addc_u32 s53, s17, 0\n"
    "s_mov_b32 s57, 0x7fff7fff\n"
    "s_mov_b32 s56, 0\n"
    "s_mov_b32 s27, 0\n"
    "s_mov_b32 s55, 9\n"
    "s_mov_b32 s44, 0\n"
    "s_mov_b32 s46, 0\n"
    "v_lshrrev_b32 v17, 3, v1\n"
    "v_mul_u32_u24 v17, 12, v17\n"
    "v_and_b32 v19, 7, v1\n"
    "v_lshlrev_b32 v19, 4, v19\n"
    "v_lshrrev_b32 v18, 5, v1\n"
    "v_lshlrev_b32 v18, 2, v18\n"
    "v_and_b32 v20, 31, v1\n"
    "v_lshlrev_b32 v20, 2, v20\n"
    "s_lshl2_add_u32 s40, s22, s58\n"
    "s_sub_u32 s40, s40, 20\n"
    "v_lshl_add_u32 v25, v1, 18, s40\n"
    "s_lshl1_add_u32 s23, s3, s21\n"
    "s_mul_i32 s23, s23, 6\n"
    "s_add_u32 s23, s23, s22\n"
    "s_add_u32 s23, s23, 78\n"
    "v_mov_b32 v24, s23\n"
    "v_lshlrev_b32 v13, 2, v0\n"
    "v_readfirstlane_b32 s40, v24\n"
    "s_or_b32 s40, s40, s44\n"
    "s_lshl1_add_u32 s41, s40, s54\n"
    "s_cmpk_lt_u32 s40, 256\n"
    "s_cselect_b32 s41, s41, 2\n"
    "v_mov_b32 v12, s41\n"
    "s_mul_i32 s41, s41, 52\n"
    "s_add_u32 s38, s4, s41\n"
    "s_addc_u32 s39, s5, 0\n"
    "global_load_dwordx3 v[36:38], v17, s[38:39] offset:-104\n"
    "global_load_dword v41, v18, s[38:39] offset:-8\n"
    "s_setprio 0\n"
    "v_mov_b32 v21, 0\n"
    "v_add_u32 v13, 149504, v13\n"
    "ds_write_b32 v13, v21\n"
    "s_cmp_lt_u32 s2, 8\n"
    "s_cbranch_scc1 .Lxd_hp58\n"
    "s_waitcnt vmcnt(2)\n"
    "s_mov_b32 m0, 159744\n"
    "v_lshlrev_b32 v60, 7, v60\n"
    "global_load_lds_dword v60, s[8:9]\n"
    "s_mov_b32 m0, 159744\n"
    "v_lshlrev_b32 v61, 7, v61\n"
    "global_load_lds_dword v61, s[8:9]\n"
    "s_waitcnt vmcnt(2)\n"
    "s_branch .Lxd_h3\n"
    ".Lxd_hp58:\n"
    "s_waitcnt vmcnt(0)\n"
    ".Lxd_h3:\n"
    "v_lshl_or_b32 v36, v36, 7, v19\n"
    "v_lshl_or_b32 v37, v37, 7, v19\n"
    "v_lshl_or_b32 v38, v38, 7, v19\n"
    "v_lshl_or_b32 v41, v41, 7, v20\n"
    "global_load_dwordx4 v[28:31], v36, s[8:9]\n"
    "global_load_dwordx4 v[32:35], v37, s[8:9]\n"
    "global_load_dword v40, v41, s[8:9]\n"
    "global_load_dwordx4 v[36:39], v38, s[8:9]\n"
    "s_waitcnt lgkmcnt(0)\n"
    "s_barrier\n"
    "ds_write_b32 v25, v12 offset:20\n"
    "s_load_dword s26, s[68:69], 0\n"
    "s_lshl_b32 s43, s21, 16\n"
    "v_lshrrev_b32 v12, 4, v1\n"
    "v_bfe_u32 v13, v1, 2, 2\n"
    "v_and_b32 v21, 3, v1\n"
    "s_mul_i32 s40, s22, 0x2020\n"
    "s_and_b32 s40, s40, 0x4060\n"
    "s_add_u32 s40, s40, s43\n"
    "v_lshl_add_u32 v21, v21, 3, s40\n"
    "s_movk_i32 s41, 144\n"
    "v_add_u32 v22, v12, v13\n"
    "v_and_b32 v22, 3, v22\n"
    "v_mad_u32_u24 v4, v22, s41, v21\n"
    "v_cmp_gt_u32 vcc, 2, v13\n"
    "v_add_u32 v24, 22, v13\n"
    "v_lshlrev_b32 v13, 3, v13\n"
    "v_cndmask_b32 v22, v24, v22, vcc\n"
    "v_mad_u32_u24 v3, v22, s41, v21\n"
    "v_cmp_eq_u32 vcc, 3, v12\n"
    "v_xor_b32 v22, 0, v12\n"
    "v_and_b32 v22, 1, v22\n"
    "v_lshlrev_b32 v22, 1, v22\n"
    "v_lshl_add_u32 v22, v12, 3, v22\n"
    "s_mov_b32 s40, 0x05040100\n"
    "v_bfe_u32 v24, s40, v13, 8\n"
    "v_add_u32 v22, v22, v24\n"
    "s_mov_b32 s40, 0x1a191818\n"
    "v_bfe_u32 v24, s40, v13, 8\n"
    "v_cndmask_b32 v22, v22, v24, vcc\n"
    "v_mad_u32_u24 v5, v22, s41, v21\n"
    "v_xor_b32 v22, 1, v12\n"
    "v_and_b32 v22, 1, v22\n"
    "v_lshlrev_b32 v22, 1, v22\n"
    "v_lshl_add_u32 v22, v12, 3, v22\n"
    "s_mov_b32 s40, 0x05040100\n"
    "v_bfe_u32 v24, s40, v13, 8\n"
    "v_add_u32 v22, v22, v24\n"
    "s_mov_b32 s40, 0x1a191918\n"
    "v_bfe_u32 v24, s40, v13, 8\n"
    "v_cndmask_b32 v22, v22, v24, vcc\n"
    "v_mad_u32_u24 v6, v22, s41, v21\n"
    "v_bfrev_b32 v14, v12\n"
    "v_lshrrev_b32 v14, 26, v14\n"
    "s_add_u32 s40, s22, 3\n"
    "s_and_b32 s40, s40, 3\n"
    "s_lshl2_add_u32 s40, s40, s22\n"
    "s_add_u32 s40, s40, s58\n"
    "s_lshl_b32 s40, s40, 2\n"
    "s_add_u32 s40, s40, -450048\n"
    "v_add_u32 v14, s40, v14\n"
    "v_and_b32 v2, 1, v1\n"
    "v_mul_u32_u24 v2, 0xaaaa, v2\n"
    "v_add_u32 v2, 0x4444, v2\n"
    "v_mad_u32_u24 v15, v17, 36, v19\n"
    "v_mad_u32_u24 v16, v18, 36, v20\n"
    "s_add_u32 s41, s22, 2\n"
    "v_lshrrev_b32 v12, 5, v19\n"
    "v_sub_u32 v12, 1, v12\n"
    "v_and_b32 v12, 3, v12\n"
    "v_add_lshl_u32 v12, v12, s41, 12\n"
    "v_and_b32 v12, 0x7000, v12\n"
    "v_add3_u32 v15, v12, v15, s43\n"
    "v_lshrrev_b32 v12, 5, v20\n"
    "v_sub_u32 v12, 1, v12\n"
    "v_and_b32 v12, 3, v12\n"
    "v_add_lshl_u32 v12, v12, s41, 12\n"
    "v_and_b32 v12, 0x7000, v12\n"
    "v_add3_u32 v16, v12, v16, s43\n"
    "v_add_u32 v16, 3456, v16\n"
    "s_lshl3_add_u32 s40, s2, 4096\n"
    "s_lshl_b32 s41, s2, 2\n"
    "v_lshlrev_b32 v11, 3, v1\n"
    "v_add_u32 v11, 40960, v11\n"
    "v_writelane_b32 v11, s40, 8\n"
    "v_writelane_b32 v11, s41, 9\n"
    "s_getpc_b64 s[10:11]\n"
    ".Lxd_en59:\n"
    "s_mov_b32 s40, .Lxd_ph1-.Lxd_en59\n"
    "s_cmp_eq_u32 s22, 1\n"
    "s_cselect_b32 s40, .Lxd_ph0-.Lxd_en59, s40\n"
    "s_cmp_eq_u32 s22, 2\n"
    "s_cselect_b32 s40, .Lxd_pl3-.Lxd_en59, s40\n"
    "s_cmp_eq_u32 s22, 3\n"
    "s_cselect_b32 s40, .Lxd_pl2-.Lxd_en59, s40\n"
    "s_ashr_i32 s41, s40, 31\n"
    "s_add_u32 s10, s10, s40\n"
    "s_addc_u32 s11, s11, s41\n"
    "v_mov_b32 v68, v42\n"
    "v_add_u32 v69, 4096, v42\n"
    "v_add_u32 v42, 98304, v42\n"
    "v_bfe_u32 v70, v1, 4, 1\n"
    "v_lshlrev_b32 v70, 2, v70\n"
    "v_add_u32 v70, 154112, v70\n"
    "s_mov_b32 s36, 0\n"
    "v_mov_b64 v[88:89], 0\n"
    "v_mov_b64 v[90:91], 0\n"
    "s_waitcnt vmcnt(0)\n"
    "s_movk_i32 s43, 15744\n"
    "v_pk_maximum3_f16 v21, v28, v28, s43 op_sel_hi:[1,1,0] neg_lo:[0,1,0] neg_hi:[0,1,0]\n"
    "v_pk_maximum3_f16 v21, v29, v29, v21 neg_lo:[0,1,0] neg_hi:[0,1,0]\n"
    "v_pk_maximum3_f16 v21, v30, v30, v21 neg_lo:[0,1,0] neg_hi:[0,1,0]\n"
    "v_pk_maximum3_f16 v21, v31, v31, v21 neg_lo:[0,1,0] neg_hi:[0,1,0]\n"
    "v_pk_maximum3_f16 v21, v32, v32, v21 neg_lo:[0,1,0] neg_hi:[0,1,0]\n"
    "v_pk_maximum3_f16 v21, v33, v33, v21 neg_lo:[0,1,0] neg_hi:[0,1,0]\n"
    "v_pk_maximum3_f16 v21, v34, v34, v21 neg_lo:[0,1,0] neg_hi:[0,1,0]\n"
    "v_pk_maximum3_f16 v21, v35, v35, v21 neg_lo:[0,1,0] neg_hi:[0,1,0]\n"
    "v_pk_maximum3_f16 v21, v36, v36, v21 neg_lo:[0,1,0] neg_hi:[0,1,0]\n"
    "v_pk_maximum3_f16 v21, v37, v37, v21 neg_lo:[0,1,0] neg_hi:[0,1,0]\n"
    "v_pk_maximum3_f16 v21, v38, v38, v21 neg_lo:[0,1,0] neg_hi:[0,1,0]\n"
    "v_pk_maximum3_f16 v21, v39, v39, v21 neg_lo:[0,1,0] neg_hi:[0,1,0]\n"
    "v_pk_maximum3_f16 v21, v40, v40, v21 neg_lo:[0,1,0] neg_hi:[0,1,0]\n"
    "v_pk_max_u16 v21, v21, v21 op_sel:[0,1] op_sel_hi:[1,0]\n"
    "s_nop 1\n"
    "v_max_u32_dpp v21, v21, v21 quad_perm:[1,0,3,2] row_mask:0xf bank_mask:0xf\n"
    "s_nop 1\n"
    "v_max_u32_dpp v21, v21, v21 quad_perm:[2,3,0,1] row_mask:0xf bank_mask:0xf\n"
    "s_nop 1\n"
    "v_max_u32_dpp v21, v21, v21 row_half_mirror row_mask:0xf bank_mask:0xf\n"
    "s_nop 1\n"
    "v_max_u32_dpp v21, v21, v21 row_mirror row_mask:0xf bank_mask:0xf\n"
    "s_nop 1\n"
    "v_max_u32_dpp v21, v21, v21 row_bcast:15 row_mask:0xa bank_mask:0xf\n"
    "s_nop 1\n"
    "v_max_u32_dpp v21, v21, v21 row_bcast:31 row_mask:0xc bank_mask:0xf\n"
    "s_nop 1\n"
    "v_readlane_b32 s47, v21, 63\n"
    "s_and_b32 s47, s47, 0x7f80\n"
    "s_sub_u32 s50, s47, 2816\n"
    "s_mul_i32 s51, s50, -8\n"
    "s_lshr_b32 s47, s47, 3\n"
    "v_pk_maximum3_f16 v21, v28, v28, s50 op_sel_hi:[1,1,0] neg_lo:[0,1,0] neg_hi:[0,1,0]\n"
    "v_pk_mad_u16 v21, v21, 8, s51 op_sel_hi:[1,0,0]\n"
    "v_bfi_b32 v28, s57, v21, v28\n"
    "v_pk_maximum3_f16 v12, v29, v29, s50 op_sel_hi:[1,1,0] neg_lo:[0,1,0] neg_hi:[0,1,0]\n"
    "v_pk_mad_u16 v12, v12, 8, s51 op_sel_hi:[1,0,0]\n"
    "v_bfi_b32 v29, s57, v12, v29\n"
    "v_pk_maximum3_f16 v21, v30, v30, s50 op_sel_hi:[1,1,0] neg_lo:[0,1,0] neg_hi:[0,1,0]\n"
    "v_pk_mad_u16 v21, v21, 8, s51 op_sel_hi:[1,0,0]\n"
    "v_bfi_b32 v30, s57, v21, v30\n"
    "v_pk_maximum3_f16 v12, v31, v31, s50 op_sel_hi:[1,1,0] neg_lo:[0,1,0] neg_hi:[0,1,0]\n"
    "v_pk_mad_u16 v12, v12, 8, s51 op_sel_hi:[1,0,0]\n"
    "v_bfi_b32 v31, s57, v12, v31\n"
    "v_pk_maximum3_f16 v21, v32, v32, s50 op_sel_hi:[1,1,0] neg_lo:[0,1,0] neg_hi:[0,1,0]\n"
    "v_pk_mad_u16 v21, v21, 8, s51 op_sel_hi:[1,0,0]\n"
    "v_bfi_b32 v32, s57, v21, v32\n"
    "v_pk_maximum3_f16 v12, v33, v33, s50 op_sel_hi:[1,1,0] neg_lo:[0,1,0] neg_hi:[0,1,0]\n"
    "v_pk_mad_u16 v12, v12, 8, s51 op_sel_hi:[1,0,0]\n"
    "v_bfi_b32 v33, s57, v12, v33\n"
    "s_cmp_lg_u32 s20, 7\n"
    "s_cbranch_scc1 .Lxd_on60\n"
    "s_mov_b64 exec, 0x3ff\n"
    "global_load_dwordx2 v[26:27], v11, s[16:17] sc1\n"
    "s_mov_b64 exec, -1\n"
    ".Lxd_on60:\n"
    "v_pk_maximum3_f16 v21, v34, v34, s50 op_sel_hi:[1,1,0] neg_lo:[0,1,0] neg_hi:[0,1,0]\n"
    "v_pk_mad_u16 v21, v21, 8, s51 op_sel_hi:[1,0,0]\n"
    "v_bfi_b32 v34, s57, v21, v34\n"
    "v_pk_maximum3_f16 v12, v35, v35, s50 op_sel_hi:[1,1,0] neg_lo:[0,1,0] neg_hi:[0,1,0]\n"
    "v_pk_mad_u16 v12, v12, 8, s51 op_sel_hi:[1,0,0]\n"
    "v_bfi_b32 v35, s57, v12, v35\n"
    "v_pk_maximum3_f16 v21, v36, v36, s50 op_sel_hi:[1,1,0] neg_lo:[0,1,0] neg_hi:[0,1,0]\n"
    "v_pk_mad_u16 v21, v21, 8, s51 op_sel_hi:[1,0,0]\n"
    "v_bfi_b32 v36, s57, v21, v36\n"
    "v_pk_maximum3_f16 v12, v37, v37, s50 op_sel_hi:[1,1,0] neg_lo:[0,1,0] neg_hi:[0,1,0]\n"
    "v_pk_mad_u16 v12, v12, 8, s51 op_sel_hi:[1,0,0]\n"
    "v_bfi_b32 v37, s57, v12, v37\n"
    "v_pk_maximum3_f16 v21, v38, v38, s50 op_sel_hi:[1,1,0] neg_lo:[0,1,0] neg_hi:[0,1,0]\n"
    "v_pk_mad_u16 v21, v21, 8, s51 op_sel_hi:[1,0,0]\n"
    "v_bfi_b32 v38, s57, v21, v38\n"
    "v_pk_maximum3_f16 v12, v39, v39, s50 op_sel_hi:[1,1,0] neg_lo:[0,1,0] neg_hi:[0,1,0]\n"
    "v_pk_mad_u16 v12, v12, 8, s51 op_sel_hi:[1,0,0]\n"
    "v_bfi_b32 v39, s57, v12, v39\n"
    "v_pk_maximum3_f16 v21, v40, v40, s50 op_sel_hi:[1,1,0] neg_lo:[0,1,0] neg_hi:[0,1,0]\n"
    "v_pk_mad_u16 v21, v21, 8, s51 op_sel_hi:[1,0,0]\n"
    "v_bfi_b32 v40, s57, v21, v40\n"
    "s_sub_u32 s66, 4112, s47\n"
    "s_lshl_b32 s66, s66, 19\n"
    "v_cvt_pkrtz_f16_f32 v12, s66, s66\n"
    "v_lshl_or_b32 v21, v12, 16, s47\n"
    "ds_write_b128 v15, v[28:31] offset:0\n"
    "ds_write_b128 v15, v[32:35] offset:144\n"
    "ds_write_b128 v15, v[36:39] offset:288\n"
    "ds_write2_b32 v16, v40, v12 offset1:72\n"
    "ds_or_b32 v25, v21 offset:532\n"
    "s_cmp_lg_u32 s20, 7\n"
    "s_cbranch_scc1 .Lxd_on62\n"
    "s_waitcnt vmcnt(0)\n"
    "v_readlane_b32 s25, v26, 9\n"
    "v_readlane_b32 s40, v26, 8\n"
    "v_readlane_b32 s41, v27, 8\n"
    "s_mul_i32 s42, s25, 0x9e3779b1\n"
    "s_lshr_b32 s42, s42, 24\n"
    "s_xor_b32 s42, s42, s40\n"
    "s_and_b32 s42, s42, 0xff\n"
    "s_xor_b32 s41, s41, s25\n"
    "s_xor_b32 s41, s41, 0xa5c30f96\n"
    "s_or_b32 s42, s41, s42\n"
    "s_lshr_b32 s41, s40, 8\n"
    "s_mul_i32 s40, s41, 0x9e3779b1\n"
    "s_xor_b32 s41, s41, 0xa5c30f96\n"
    "s_lshr_b32 s40, s40, 24\n"
    "v_xor_b32 v13, s40, v26\n"
    "v_xor_b32 v22, s41, v27\n"
    "v_lshl_or_b32 v13, v13, 24, v22\n"
    "v_cmp_ne_u32 s[66:67], 0, v13\n"
    "s_and_b32 s66, s66, 0xff\n"
    "s_or_b32 s42, s42, s66\n"
    "s_cmp_eq_u32 s20, 7\n"
    "s_cselect_b32 s42, s42, 0\n"
    "s_cmp_lg_u32 s42, 0\n"
    "s_cbranch_scc1 .Lxd_pv61\n"
    ".Lxd_pv61r:\n"
    "s_mov_b64 s[64:65], s[40:41]\n"
    ".Lxd_on62:\n"
    "s_waitcnt lgkmcnt(0)\n"
    "s_barrier\n"
    "s_mul_i32 s42, s20, 5376\n"
    "s_add_u32 s40, s42, 1048576\n"
    "s_add_u32 s38, s16, s40\n"
    "s_addc_u32 s39, s17, 0\n"
    "s_add_u32 m0, s42, 98304\n"
    "s_nop 0\n"
    "global_load_lds_dwordx4 v68, s[38:39] offset:0 sc1\n"
    "global_load_lds_dwordx4 v68, s[38:39] offset:1024 sc1\n"
    "global_load_lds_dwordx4 v68, s[38:39] offset:2048 sc1\n"
    "global_load_lds_dwordx4 v68, s[38:39] offset:3072 sc1\n"
    "s_add_u32 m0, s42, 102400\n"
    "s_nop 0\n"
    "global_load_lds_dwordx4 v69, s[38:39] offset:0 sc1\n"
    "global_load_lds_dwordx4 v69, s[38:39] offset:256 sc1\n"
    "s_cmp_lg_u32 s20, 7\n"
    "s_cbranch_scc1 .Lxd_on63\n"
    "s_mov_b64 s[40:41], s[64:65]\n"
    "v_lshrrev_b32 v22, 8, v26\n"
    "v_mov_b32 v26, s40\n"
    "v_mov_b32 v27, s41\n"
    "v_add_u32_dpp v22, v22, v22 row_shl:4 row_mask:0xf bank_mask:0xf\n"
    "v_add_u32_e64 v43, s25, 1\n"
    "v_lshlrev_b32 v8, 2, v1\n"
    "v_max_u32_dpp v13, v22, v22 quad_perm:[1,0,3,2] row_mask:0xf bank_mask:0xf\n"
    "v_add_u32 v8, 154112, v8\n"
    "s_nop 0\n"
    "v_max_u32_dpp v13, v13, v13 quad_perm:[2,3,0,1] row_mask:0xf bank_mask:0xf\n"
    "v_sub_u32 v22, v22, v13\n"
    "v_subrev_u32 v13, 818, v13\n"
    "v_ldexp_f32 v22, 1.0, v22\n"
    "s_mov_b64 exec, 1\n"
    "ds_write_b64 v8, v[26:27]\n"
    "s_mov_b64 exec, 15\n"
    "ds_write2_b32 v8, v22, v13 offset0:4 offset1:8\n"
    "s_mov_b64 exec, -1\n"
    ".Lxd_on63:\n"
    "s_cmp_lt_u32 s22, 2\n"
    "s_cselect_b32 s40, 16, 0\n"
    "v_add_u32 v25, s40, v25\n"
    "s_add_u32 s23, s23, 4\n"
    "v_mov_b32 v24, s23\n"
    "v_mov_b32 v13, s58\n"
    "s_sub_u32 s40, 3, s22\n"
    "s_and_b32 s40, s40, 3\n"
    "v_lshl_add_u32 v7, s40, 12, v3\n"
    "v_lshl_add_u32 v8, s40, 12, v4\n"
    "v_lshl_add_u32 v9, s40, 12, v5\n"
    "v_lshl_add_u32 v10, s40, 12, v6\n"
    "ds_read_b32 v22, v13 offset:512\n"
    "ds_read_b64_tr_b16 v[44:45], v9 offset:0\n"
    "ds_read_b64_tr_b16 v[46:47], v10 offset:0\n"
    "ds_read_b64_tr_b16 v[48:49], v7 offset:0\n"
    "ds_read_b64_tr_b16 v[50:51], v8 offset:576\n"
    "ds_read_b64_tr_b16 v[52:53], v8 offset:1152\n"
    "ds_read_b64_tr_b16 v[54:55], v8 offset:1728\n"
    "ds_read_b64_tr_b16 v[56:57], v8 offset:2304\n"
    "ds_read_b64_tr_b16 v[58:59], v8 offset:2880\n"
    "s_cmp_lg_u32 s22, 0\n"
    "s_cbranch_scc1 .Lxd_on64\n"
    "v_readfirstlane_b32 s40, v24\n"
    "s_or_b32 s40, s40, s44\n"
    "s_lshl1_add_u32 s41, s40, s54\n"
    "s_cmpk_lt_u32 s40, 256\n"
    "s_cselect_b32 s41, s41, 2\n"
    "v_mov_b32 v12, s41\n"
    "s_mul_i32 s41, s41, 52\n"
    "s_add_u32 s38, s4, s41\n"
    "s_addc_u32 s39, s5, 0\n"
    "ds_write_b32 v25, v12 offset:20\n"
    "global_load_dwordx3 v[36:38], v17, s[38:39] offset:-104\n"
    "global_load_dword v41, v18, s[38:39] offset:-8\n"
    ".Lxd_on64:\n"
    "s_waitcnt vmcnt(0) lgkmcnt(0)\n"
    "s_barrier\n"
    "ds_read2_b32 v[26:27], v70 offset1:1\n"
    "ds_read_b32 v0, v70 offset:16\n"
    "ds_read_b32 v23, v70 offset:24\n"
    "s_cmp_lg_u32 s20, 7\n"
    "s_cbranch_scc1 .Lxd_on65\n"
    "s_mov_b64 exec, 0x200\n"
    "global_store_dword v11, v43, s[16:17] sc1\n"
    "s_mov_b64 exec, -1\n"
    ".Lxd_on65:\n"
    "ds_read_b128 v[92:95], v42 offset:0\n"
    "ds_read_b128 v[96:99], v42 offset:1024\n"
    "ds_read_b128 v[100:103], v42 offset:2048\n"
    "ds_read_b128 v[104:107], v42 offset:3072\n"
    "ds_read_b128 v[108:111], v42 offset:4096\n"
    "ds_read_b128 v[112:115], v42 offset:5120\n"
    "ds_read_b128 v[116:119], v42 offset:6144\n"
    "ds_read_b128 v[120:123], v42 offset:7168\n"
    "ds_read_b128 v[124:127], v42 offset:8192\n"
    "ds_read_b128 v[128:131], v42 offset:9216\n"
    "ds_read_b128 v[132:135], v42 offset:10240\n"
    "ds_read_b128 v[136:139], v42 offset:11264\n"
    "ds_read_b128 v[140:143], v42 offset:12288\n"
    "ds_read_b128 v[144:147], v42 offset:13312\n"
    "s_cmp_ge_u32 s2, 8\n"
    "s_cbranch_scc1 .Lxd_h4\n"
    "s_lshl3_add_u32 s40, s2, s20\n"
    "s_mul_i32 s40, s40, 52\n"
    "s_add_u32 s40, s40, 39936\n"
    "v_min_u32 v68, 51, v1\n"
    "s_mov_b32 m0, 159744\n"
    "v_add_lshl_u32 v68, v68, s40, 7\n"
    "global_load_lds_dword v68, s[6:7]\n"
    ".Lxd_h4:\n"
    "ds_read_b128 v[148:151], v42 offset:14336\n"
    "ds_read_b128 v[152:155], v42 offset:15360\n"
    "ds_read_b128 v[156:159], v42 offset:16384\n"
    "ds_read_b128 v[160:163], v42 offset:17408\n"
    "ds_read_b128 v[164:167], v42 offset:18432\n"
    "ds_read_b128 v[168:171], v42 offset:19456\n"
    "ds_read_b128 v[172:175], v42 offset:20480\n"
    "ds_read_b128 v[176:179], v42 offset:21504\n"
    "ds_read_b128 v[180:183], v42 offset:22528\n"
    "ds_read_b128 v[184:187], v42 offset:23552\n"
    "ds_read_b128 v[188:191], v42 offset:24576\n"
    "ds_read_b128 v[192:195], v42 offset:25600\n"
    "ds_read_b128 v[196:199], v42 offset:26624\n"
    "ds_read_b128 v[200:203], v42 offset:27648\n"
    "ds_read_b128 v[204:207], v42 offset:28672\n"
    "ds_read_b128 v[208:211], v42 offset:29696\n"
    "ds_read_b128 v[212:215], v42 offset:30720\n"
    "ds_read_b128 v[216:219], v42 offset:31744\n"
    "ds_read_b128 v[220:223], v42 offset:32768\n"
    "ds_read_b128 v[224:227], v42 offset:33792\n"
    "ds_read_b128 v[228:231], v42 offset:34816\n"
    "ds_read_b128 v[232:235], v42 offset:35840\n"
    "ds_read_b128 v[236:239], v42 offset:36864\n"
    "ds_read_b128 v[240:243], v42 offset:37888\n"
    "ds_read_b128 v[244:247], v42 offset:38912\n"
    "ds_read_b128 v[248:251], v42 offset:39936\n"
    "ds_read_b128 v[252:255], v42 offset:40960\n"
    "s_waitcnt lgkmcnt(15)\n"
    "v_readfirstlane_b32 s45, v22\n"
    "v_readfirstlane_b32 s28, v26\n"
    "v_readfirstlane_b32 s29, v27\n"
    "s_cmp_lt_u32 s2, 8\n"
    "s_cbranch_scc1 .Lxd_hp67\n"
    "s_mov_b32 m0, 159744\n"
    "s_mov_b32 s46, 3\n"
    "v_lshlrev_b32 v26, 7, v62\n"
    "v_lshlrev_b32 v27, 7, v63\n"
    "v_lshlrev_b32 v42, 7, v64\n"
    ".Lxd_hp67:\n"
    "v_pk_mul_f16 v64, v44, v46\n"
    "v_pk_mul_f16 v65, v45, v47\n"
    "v_pk_mul_f16 v66, v48, v48 op_sel_hi:[0,1]\n"
    "v_pk_mul_f16 v67, v48, v49 op_sel_hi:[0,1]\n"
    "v_pk_mul_f16 v60, v44, s45 op_sel:[0,1]\n"
    "v_pk_mul_f16 v61, v45, s45 op_sel:[0,1]\n"
    "v_pk_mul_f16 v62, v46, s45 op_sel:[0,1]\n"
    "v_pk_mul_f16 v63, v47, s45 op_sel:[0,1]\n"
    "s_waitcnt lgkmcnt(0)\n"
    "s_branch .Lxd_enter\n"
    ".Lxd_slow:\n"
    "s_cmp_eq_u32 s45, -1\n"
    "s_cbranch_scc1 .Lxd_sl7\n"
    "s_mov_b32 s59, 0x100000\n"
    "s_and_b32 s40, s45, 0xff0\n"
    "s_cbranch_scc0 .Lxd_sl1\n"
    ".Lxd_sl2:\n"
    "s_cmp_ge_i32 s45, -16\n"
    "s_cbranch_scc1 .Lxd_sl3\n"
    "s_bitset0_b32 s45, 31\n"
    ".Lxd_sl6:\n"
    "s_cmp_lg_u32 s56, 0\n"
    "s_cbranch_scc1 .Lxd_sl9\n"
    "v_readfirstlane_b32 s40, v13\n"
    "s_lshl4_add_u32 s41, s22, s58\n"
    "s_mov_b32 s56, 2\n"
    "s_cmp_lt_u32 s40, s41\n"
    "s_cbranch_scc0 .Lxd_gran\n"
    ".Lxd_sl9:\n"
    "s_setpc_b64 s[60:61]\n"
    ".Lxd_sl1:\n"
    "s_sleep 1\n"
    "ds_read_b32 v22, v13 offset:512\n"
    "s_waitcnt lgkmcnt(0)\n"
    "v_readfirstlane_b32 s45, v22\n"
    "s_sub_u32 s59, s59, 1\n"
    "s_cbranch_scc1 .Lxd_sl5\n"
    "s_and_b32 s40, s45, 0xff0\n"
    "s_cbranch_scc0 .Lxd_sl1\n"
    "s_cmp_gt_i32 s45, 0\n"
    "s_cbranch_scc0 .Lxd_sl2\n"
    "s_setpc_b64 s[60:61]\n"
    ".Lxd_sl3:\n"
    "v_and_b32 v13, 0x3ffff, v13\n"
    "v_lshl_add_u32 v13, v1, 2, v13\n"
    "s_mov_b64 exec, 7\n"
    ".Lxd_sl4:\n"
    "ds_read_b32 v22, v13 offset:4\n"
    "s_waitcnt lgkmcnt(0)\n"
    "v_cmp_eq_u32 s[40:41], 0, v22\n"
    "v_cmp_ne_u32 s[42:43], -1, v22\n"
    "s_sub_u32 s59, s59, 1\n"
    "s_cbranch_scc1 .Lxd_sl5\n"
    "s_cmp_lg_u32 s40, 0\n"
    "s_cbranch_scc1 .Lxd_sl4\n"
    "s_cmp_eq_u32 s42, 0\n"
    "s_cbranch_scc1 .Lxd_sl5\n"
    "s_mov_b64 exec, -1\n"
    "s_mov_b32 s45, 1\n"
    "s_mov_b32 s56, 0\n"
    "s_setpc_b64 s[60:61]\n"
    ".Lxd_sl5:\n"
    "s_mov_b64 exec, -1\n"
    "s_mov_b32 s45, -1\n"
    ".Lxd_sl7:\n"
    "s_cmp_lg_u32 s56, 1\n"
    "s_cbranch_scc1 .Lxd_sl6\n"
    "s_branch .Lxd_dsum\n"
    ".Lxd_gran:\n"
    "s_mov_b32 s46, 0\n"
    "s_lshl4_add_u32 s40, s22, s58\n"
    "v_lshrrev_b32 v13, 4, v1\n"
    "v_lshl_add_u32 v13, v13, 2, s40\n"
    "s_waitcnt vmcnt(0)\n"
    "ds_read2_b32 v[26:27], v13 offset1:16\n"
    "v_and_b32 v13, 15, v1\n"
    "v_lshlrev_b32 v13, 15, v13\n"
    "s_mov_b32 s42, 0x1fff1fff\n"
    "s_mov_b32 s43, s42\n"
    "s_add_u32 s40, s16, 65528\n"
    "s_addc_u32 s41, s17, 0\n"
    "v_mov_b32 v42, 0\n"
    "s_mov_b32 s56, 2\n"
    "s_waitcnt lgkmcnt(0)\n"
    "v_cmp_lt_i32 s[30:31], 0, v26\n"
    "v_cmp_lt_i32 s[64:65], 0, v27\n"
    "v_lshl_add_u32 v43, v26, 2, v13\n"
    "v_lshl_add_u32 v27, v27, 2, v13\n"
    "v_mov_b32 v26, 0\n"
    "s_and_b64 s[30:31], s[30:31], s[42:43]\n"
    "s_cbranch_scc0 .Lxd_gr30\n"
    "s_mov_b64 exec, s[30:31]\n"
    "global_load_dwordx2 v[42:43], v43, s[40:41] sc1\n"
    "s_mov_b64 exec, -1\n"
    "s_mov_b32 s56, 1\n"
    ".Lxd_gr30:\n"
    "s_and_b64 s[64:65], s[64:65], s[42:43]\n"
    "s_cbranch_scc0 .Lxd_gr64\n"
    "s_mov_b64 exec, s[64:65]\n"
    "global_load_dwordx2 v[26:27], v27, s[40:41] sc1\n"
    "s_mov_b64 exec, -1\n"
    "s_mov_b32 s56, 1\n"
    ".Lxd_gr64:\n"
    "s_setpc_b64 s[60:61]\n"
    ".Lxd_dsum:\n"
    "s_lshl4_add_u32 s42, s22, s58\n"
    "s_waitcnt vmcnt(0)\n"
    "s_cmp_eq_u64 s[30:31], 0\n"
    "s_cbranch_scc1 .Lxd_ds30\n"
    "v_xor_b32 v13, s28, v42\n"
    "v_xor_b32 v22, s29, v43\n"
    "v_lshl_or_b32 v13, v13, 24, v22\n"
    "v_and_b32 v22, 0xffffff00, v42\n"
    "v_cmp_ne_u32 s[40:41], 0, v13\n"
    "v_lshrrev_b32 v13, 4, v1\n"
    "v_add_f32_dpp v22, v22, v22 quad_perm:[1,0,3,2] row_mask:0xf bank_mask:0xf\n"
    "s_and_b64 s[40:41], s[40:41], s[30:31]\n"
    "s_cbranch_scc1 .Lxd_ds9\n"
    "v_add_f32_dpp v22, v22, v22 quad_perm:[2,3,0,1] row_mask:0xf bank_mask:0xf\n"
    "v_lshl_add_u32 v13, v13, 2, s42\n"
    "s_nop 0\n"
    "v_add_f32_dpp v22, v22, v22 row_half_mirror row_mask:0xf bank_mask:0xf\n"
    "s_nop 1\n"
    "v_add_f32_dpp v22, v22, v22 row_mirror row_mask:0xf bank_mask:0xf\n"
    "ds_write_b32 v13, v22 offset:1024\n"
    ".Lxd_ds30:\n"
    "s_cmp_eq_u64 s[64:65], 0\n"
    "s_cbranch_scc1 .Lxd_ds64\n"
    "v_xor_b32 v13, s28, v26\n"
    "v_xor_b32 v22, s29, v27\n"
    "v_lshl_or_b32 v13, v13, 24, v22\n"
    "v_and_b32 v22, 0xffffff00, v26\n"
    "v_cmp_ne_u32 s[40:41], 0, v13\n"
    "v_lshrrev_b32 v13, 4, v1\n"
    "v_add_f32_dpp v22, v22, v22 quad_perm:[1,0,3,2] row_mask:0xf bank_mask:0xf\n"
    "s_and_b64 s[40:41], s[40:41], s[64:65]\n"
    "s_cbranch_scc1 .Lxd_ds9\n"
    "v_add_f32_dpp v22, v22, v22 quad_perm:[2,3,0,1] row_mask:0xf bank_mask:0xf\n"
    "v_lshl_add_u32 v13, v13, 2, s42\n"
    "s_nop 0\n"
    "v_add_f32_dpp v22, v22, v22 row_half_mirror row_mask:0xf bank_mask:0xf\n"
    "s_nop 1\n"
    "v_add_f32_dpp v22, v22, v22 row_mirror row_mask:0xf bank_mask:0xf\n"
    "ds_write_b32 v13, v22 offset:1088\n"
    ".Lxd_ds64:\n"
    "s_mov_b32 s56, 2\n"
    ".Lxd_ds9:\n"
    "s_setpc_b64 s[60:61]\n"
    ".Lxd_part:\n"
    "v_cmp_ne_u32 s[40:41], -1, v27\n"
    "s_mov_b32 s46, 0\n"
    "s_cmp_eq_u64 s[40:41], 0\n"
    "s_cbranch_scc1 .Lxd_pa1\n"
    "v_add_f32 v26, v26, v27\n"
    "v_lshlrev_b32 v42, 3, v1\n"
    "v_add_u32 v26, 0x80, v26\n"
    "v_mov_b32 v27, s29\n"
    "v_and_b32 v26, 0xffffff00, v26\n"
    "s_mov_b64 exec, s[40:41]\n"
    "v_or_b32 v26, s28, v26\n"
    "global_store_dwordx2 v42, v[26:27], s[48:49] sc1\n"
    "s_mov_b64 exec, -1\n"
    ".Lxd_pa1:\n"
    "s_setpc_b64 s[60:61]\n"
    ".Lxd_mark:\n"
    "v_and_b32 v13, 0x3ffff, v13\n"
    "v_lshl_add_u32 v13, v1, 2, v13\n"
    "s_add_u32 s40, s58, 248\n"
    "v_mov_b32 v24, -1\n"
    "v_cmp_gt_u32 s[42:43], s40, v13\n"
    "s_mov_b32 s62, 0x88888888\n"
    "s_mov_b32 s63, s62\n"
    "s_and_b64 exec, s[42:43], s[62:63]\n"
    "ds_write_b32 v13, v24 offset:8\n"
    "s_mov_b64 exec, s[42:43]\n"
    "v_cndmask_b32 v24, 0, -16, s[62:63]\n"
    "s_brev_b32 s40, 1\n"
    "v_writelane_b32 v24, s40, 2\n"
    "s_lshl_b32 s40, 1, s22\n"
    "v_or_b32 v24, s40, v24\n"
    "s_mov_b32 s36, 1\n"
    "ds_or_b32 v13, v24 offset:520\n"
    "s_mov_b64 exec, -1\n"
    "s_setpc_b64 s[60:61]\n"
    ".Lxd_tf17:\n"
    "v_mov_b32 v12, -1\n"
    "v_mov_b32 v13, v25\n"
    "s_call_b64 s[60:61], .Lxd_mark\n"
    "s_branch .Lxd_tf17r\n"
    ".Lxd_ht18:\n"
    "s_call_b64 s[60:61], .Lxd_part\n"
    "s_branch .Lxd_ht18r\n"
    ".Lxd_ck19:\n"
    "v_add_u32 v13, 4, v25\n"
    "s_call_b64 s[60:61], .Lxd_slow\n"
    "s_cmp_eq_u32 s45, -1\n"
    "s_cbranch_scc0 .Lxd_ck19r\n"
    "s_call_b64 s[60:61], .Lxd_last\n"
    "v_fma_f32 v7, v79, v83, v11\n"
    "s_branch .Lxd_fin\n"
    ".Lxd_ht22:\n"
    "s_cmp_lt_i32 s46, 0\n"
    "s_cbranch_scc1 .Lxd_ht22s\n"
    "global_load_lds_dword v26, s[8:9]\n"
    "v_mov_b32 v26, v27\n"
    "v_mov_b32 v27, v42\n"
    "s_sub_u32 s46, s46, 1\n"
    "s_branch .Lxd_ht22r\n"
    ".Lxd_ht22s:\n"
    "s_call_b64 s[60:61], .Lxd_part\n"
    "s_branch .Lxd_ht22r\n"
    ".Lxd_ck23:\n"
    "v_add_u32 v13, 8, v25\n"
    "s_call_b64 s[60:61], .Lxd_slow\n"
    "s_cmp_eq_u32 s45, -1\n"
    "s_cbranch_scc0 .Lxd_ck23r\n"
    "s_call_b64 s[60:61], .Lxd_last\n"
    "v_fma_f32 v8, v79, v83, v11\n"
    "s_branch .Lxd_fin\n"
    ".Lxd_ht26:\n"
    "s_cmp_lt_i32 s46, 0\n"
    "s_cbranch_scc1 .Lxd_ht26s\n"
    "global_load_lds_dword v26, s[8:9]\n"
    "v_mov_b32 v26, v27\n"
    "v_mov_b32 v27, v42\n"
    "s_sub_u32 s46, s46, 1\n"
    "s_branch .Lxd_ht26r\n"
    ".Lxd_ht26s:\n"
    "s_call_b64 s[60:61], .Lxd_part\n"
    "s_branch .Lxd_ht26r\n"
    ".Lxd_ck27:\n"
    "v_add_u32 v13, 12, v25\n"
    "s_call_b64 s[60:61], .Lxd_slow\n"
    "s_cmp_eq_u32 s45, -1\n"
    "s_cbranch_scc0 .Lxd_ck27r\n"
    "s_call_b64 s[60:61], .Lxd_last\n"
    "v_fma_f32 v9, v79, v83, v11\n"
    "s_branch .Lxd_fin\n"
    ".Lxd_dr30:\n"
    "v_add_u32 v13, 16, v25\n"
    "s_call_b64 s[60:61], .Lxd_mark\n"
    "s_branch .Lxd_dr30r\n"
    ".Lxd_ck31:\n"
    "v_add_u32 v13, 16, v25\n"
    "s_call_b64 s[60:61], .Lxd_slow\n"
    "s_cmp_eq_u32 s45, -1\n"
    "s_cbranch_scc0 .Lxd_ck31r\n"
    "s_call_b64 s[60:61], .Lxd_last\n"
    "v_fma_f32 v10, v79, v83, v11\n"
    "s_branch .Lxd_fin\n"
    ".Lxd_ck34:\n"
    "v_add_u32 v13, 4, v25\n"
    "s_call_b64 s[60:61], .Lxd_slow\n"
    "s_cmp_eq_u32 s45, -1\n"
    "s_cbranch_scc0 .Lxd_ck34r\n"
    "s_call_b64 s[60:61], .Lxd_last\n"
    "v_fma_f32 v7, v79, v83, v11\n"
    "s_branch .Lxd_fin\n"
    ".Lxd_ht37:\n"
    "s_cmp_lt_i32 s46, 0\n"
    "s_cbranch_scc1 .Lxd_ht37s\n"
    "global_load_lds_dword v26, s[8:9]\n"
    "v_mov_b32 v26, v27\n"
    "v_mov_b32 v27, v42\n"
    "s_sub_u32 s46, s46, 1\n"
    "s_branch .Lxd_ht37r\n"
    ".Lxd_ht37s:\n"
    "s_call_b64 s[60:61], .Lxd_part\n"
    "s_branch .Lxd_ht37r\n"
    ".Lxd_ck38:\n"
    "v_add_u32 v13, 8, v25\n"
    "s_call_b64 s[60:61], .Lxd_slow\n"
    "s_cmp_eq_u32 s45, -1\n"
    "s_cbranch_scc0 .Lxd_ck38r\n"
    "s_call_b64 s[60:61], .Lxd_last\n"
    "v_fma_f32 v8, v79, v83, v11\n"
    "s_branch .Lxd_fin\n"
    ".Lxd_ht41:\n"
    "s_cmp_lt_i32 s46, 0\n"
    "s_cbranch_scc1 .Lxd_ht41s\n"
    "global_load_lds_dword v26, s[8:9]\n"
    "v_mov_b32 v26, v27\n"
    "v_mov_b32 v27, v42\n"
    "s_sub_u32 s46, s46, 1\n"
    "s_branch .Lxd_ht41r\n"
    ".Lxd_ht41s:\n"
    "s_call_b64 s[60:61], .Lxd_part\n"
    "s_branch .Lxd_ht41r\n"
    ".Lxd_ck42:\n"
    "v_add_u32 v13, 12, v25\n"
    "s_call_b64 s[60:61], .Lxd_slow\n"
    "s_cmp_eq_u32 s45, -1\n"
    "s_cbranch_scc0 .Lxd_ck42r\n"
    "s_call_b64 s[60:61], .Lxd_last\n"
    "v_fma_f32 v9, v79, v83, v11\n"
    "s_branch .Lxd_fin\n"
    ".Lxd_ht45:\n"
    "s_call_b64 s[60:61], .Lxd_part\n"
    "s_branch .Lxd_ht45r\n"
    ".Lxd_dr46:\n"
    "v_add_u32 v13, 16, v25\n"
    "s_call_b64 s[60:61], .Lxd_mark\n"
    "s_branch .Lxd_dr46r\n"
    ".Lxd_on47:\n"
    "v_mov_b32 v24, 1\n"
    "s_and_b32 s40, s45, 15\n"
    "s_cbranch_scc1 .Lxd_dr46\n"
    "s_mov_b64 exec, 1\n"
    "global_atomic_add v24, v17, v24, s[52:53] sc0 sc1\n"
    "s_mov_b64 exec, -1\n"
    ".Lxd_dr46r:\n"
    "s_branch .Lxd_on47r\n"
    ".Lxd_ck48:\n"
    "v_add_u32 v13, 16, v25\n"
    "s_call_b64 s[60:61], .Lxd_slow\n"
    "s_cmp_eq_u32 s45, -1\n"
    "s_cbranch_scc0 .Lxd_ck48r\n"
    "s_call_b64 s[60:61], .Lxd_last\n"
    "v_fma_f32 v10, v79, v83, v11\n"
    "s_branch .Lxd_fin\n"
    ".Lxd_f2:\n"
    "s_mov_b32 s59, 0x20000\n"
    ".Lxd_f4:\n"
    "s_call_b64 s[60:61], .Lxd_dsum\n"
    "s_cmp_eq_u32 s56, 2\n"
    "s_cbranch_scc1 .Lxd_f3\n"
    "s_sleep 2\n"
    "s_call_b64 s[60:61], .Lxd_gran\n"
    "s_sub_u32 s59, s59, 1\n"
    "s_cbranch_scc0 .Lxd_f4\n"
    "s_waitcnt vmcnt(0)\n"
    "s_branch .Lxd_f3\n"
    ".Lxd_on1:\n"
    "s_mov_b64 exec, 0x3ff\n"
    "global_load_dwordx2 v[26:27], v11, s[16:17] sc1\n"
    "s_mov_b64 exec, -1\n"
    "s_branch .Lxd_on1r\n"
    ".Lxd_pv3:\n"
    "s_mov_b32 s59, 0x20000\n"
    ".Lxd_pv3l:\n"
    "s_sleep 1\n"
    "s_mov_b64 exec, 0x3ff\n"
    "global_load_dwordx2 v[26:27], v11, s[16:17] sc1\n"
    "s_mov_b64 exec, -1\n"
    "s_waitcnt vmcnt(0)\n"
    "v_readlane_b32 s25, v26, 9\n"
    "v_readlane_b32 s40, v26, 8\n"
    "v_readlane_b32 s41, v27, 8\n"
    "s_mul_i32 s42, s25, 0x9e3779b1\n"
    "s_lshr_b32 s42, s42, 24\n"
    "s_xor_b32 s42, s42, s40\n"
    "s_and_b32 s42, s42, 0xff\n"
    "s_xor_b32 s41, s41, s25\n"
    "s_xor_b32 s41, s41, 0xa5c30f96\n"
    "s_or_b32 s42, s41, s42\n"
    "s_lshr_b32 s41, s40, 8\n"
    "s_mul_i32 s40, s41, 0x9e3779b1\n"
    "s_xor_b32 s41, s41, 0xa5c30f96\n"
    "s_lshr_b32 s40, s40, 24\n"
    "v_xor_b32 v13, s40, v26\n"
    "v_xor_b32 v22, s41, v27\n"
    "v_lshl_or_b32 v13, v13, 24, v22\n"
    "v_cmp_ne_u32 s[66:67], 0, v13\n"
    "s_and_b32 s66, s66, 0xff\n"
    "s_or_b32 s42, s42, s66\n"
    "s_cmp_eq_u32 s42, 0\n"
    "s_cbranch_scc1 .Lxd_pv3r\n"
    "s_sub_u32 s59, s59, 1\n"
    "s_cbranch_scc0 .Lxd_pv3l\n"
    "s_branch .Lxd_pv3r\n"
    ".Lxd_on6:\n"
    "v_mov_b32 v24, 1\n"
    "s_mov_b64 exec, 1\n"
    "global_atomic_add v24, v17, v24, s[52:53] sc0 sc1\n"
    "s_mov_b64 exec, -1\n"
    "s_branch .Lxd_on6r\n"
    ".Lxd_ck7:\n"
    "v_mov_b32 v13, s58\n"
    "s_call_b64 s[60:61], .Lxd_slow\n"
    "s_branch .Lxd_ck7r\n"
    ".Lxd_tf8:\n"
    "v_mov_b32 v12, -1\n"
    "v_mov_b32 v13, v25\n"
    "s_call_b64 s[60:61], .Lxd_mark\n"
    "s_branch .Lxd_tf8r\n"
    ".Lxd_on9:\n"
    "s_waitcnt vmcnt(0)\n"
    "v_readfirstlane_b32 s40, v24\n"
    "s_or_b32 s40, s40, s44\n"
    "s_lshl1_add_u32 s41, s40, s54\n"
    "s_cmpk_lt_u32 s40, 256\n"
    "s_cselect_b32 s41, s41, 2\n"
    "v_mov_b32 v12, s41\n"
    "s_cbranch_scc0 .Lxd_tf8\n"
    ".Lxd_tf8r:\n"
    "s_mul_i32 s41, s41, 52\n"
    "s_add_u32 s38, s4, s41\n"
    "s_addc_u32 s39, s5, 0\n"
    "ds_write_b32 v25, v12 offset:20\n"
    "global_load_dwordx3 v[36:38], v17, s[38:39] offset:-104\n"
    "global_load_dword v41, v18, s[38:39] offset:-8\n"
    "v_mov_b32 v13, s58\n"
    "s_branch .Lxd_on9r\n"
    ".Lxd_on10:\n"
    "v_mov_b32 v24, 1\n"
    "s_mov_b64 exec, 1\n"
    "global_atomic_add v24, v17, v24, s[52:53] sc0 sc1\n"
    "s_mov_b64 exec, -1\n"
    "s_branch .Lxd_on10r\n"
    ".Lxd_on11:\n"
    "s_waitcnt vmcnt(0)\n"
    "v_lshl_or_b32 v36, v36, 7, v19\n"
    "v_lshl_or_b32 v37, v37, 7, v19\n"
    "v_lshl_or_b32 v38, v38, 7, v19\n"
    "v_lshl_or_b32 v41, v41, 7, v20\n"
    "global_load_dwordx4 v[28:31], v36, s[8:9]\n"
    "global_load_dwordx4 v[32:35], v37, s[8:9]\n"
    "global_load_dword v40, v41, s[8:9]\n"
    "global_load_dwordx4 v[36:39], v38, s[8:9]\n"
    "s_branch .Lxd_on11r\n"
    ".Lxd_tf12:\n"
    "v_mov_b32 v12, -1\n"
    "v_mov_b32 v13, v25\n"
    "s_call_b64 s[60:61], .Lxd_mark\n"
    "s_branch .Lxd_tf12r\n"
    ".Lxd_on13:\n"
    "s_waitcnt vmcnt(0)\n"
    "v_readfirstlane_b32 s40, v24\n"
    "s_or_b32 s40, s40, s44\n"
    "s_lshl1_add_u32 s41, s40, s54\n"
    "s_cmpk_lt_u32 s40, 256\n"
    "s_cselect_b32 s41, s41, 2\n"
    "v_mov_b32 v12, s41\n"
    "s_cbranch_scc0 .Lxd_tf12\n"
    ".Lxd_tf12r:\n"
    "s_mul_i32 s41, s41, 52\n"
    "s_add_u32 s38, s4, s41\n"
    "s_addc_u32 s39, s5, 0\n"
    "ds_write_b32 v25, v12 offset:20\n"
    "global_load_dwordx3 v[36:38], v17, s[38:39] offset:-104\n"
    "global_load_dword v41, v18, s[38:39] offset:-8\n"
    "s_branch .Lxd_on13r\n"
    ".Lxd_on14:\n"
    "v_mov_b32 v24, 1\n"
    "s_mov_b64 exec, 1\n"
    "global_atomic_add v24, v17, v24, s[52:53] sc0 sc1\n"
    "s_mov_b64 exec, -1\n"
    "s_branch .Lxd_on14r\n"
    ".Lxd_pv61:\n"
    "s_mov_b32 s59, 0x20000\n"
    ".Lxd_pv61l:\n"
    "s_sleep 1\n"
    "s_mov_b64 exec, 0x3ff\n"
    "global_load_dwordx2 v[26:27], v11, s[16:17] sc1\n"
    "s_mov_b64 exec, -1\n"
    "s_waitcnt vmcnt(0)\n"
    "v_readlane_b32 s25, v26, 9\n"
    "v_readlane_b32 s40, v26, 8\n"
    "v_readlane_b32 s41, v27, 8\n"
    "s_mul_i32 s42, s25, 0x9e3779b1\n"
    "s_lshr_b32 s42, s42, 24\n"
    "s_xor_b32 s42, s42, s40\n"
    "s_and_b32 s42, s42, 0xff\n"
    "s_xor_b32 s41, s41, s25\n"
    "s_xor_b32 s41, s41, 0xa5c30f96\n"
    "s_or_b32 s42, s41, s42\n"
    "s_lshr_b32 s41, s40, 8\n"
    "s_mul_i32 s40, s41, 0x9e3779b1\n"
    "s_xor_b32 s41, s41, 0xa5c30f96\n"
    "s_lshr_b32 s40, s40, 24\n"
    "v_xor_b32 v13, s40, v26\n"
    "v_xor_b32 v22, s41, v27\n"
    "v_lshl_or_b32 v13, v13, 24, v22\n"
    "v_cmp_ne_u32 s[66:67], 0, v13\n"
    "s_and_b32 s66, s66, 0xff\n"
    "s_or_b32 s42, s42, s66\n"
    "s_cmp_eq_u32 s42, 0\n"
    "s_cbranch_scc1 .Lxd_pv61r\n"
    "s_sub_u32 s59, s59, 1\n"
    "s_cbranch_scc0 .Lxd_pv61l\n"
    "s_branch .Lxd_pv61r\n"
    ".Lxd_end:\n"
    : "+{v0}"(tid) : "{s[0:1]}"(__builtin_amdgcn_kernarg_segment_ptr()), "{s2}"(blockIdx.x)
    : "memory", "vcc", "scc", "v1", "v2", "v3", "v4", "v5", "v6", "v7", "v8", "v9", "v10", "v11", "v12", "v13", "v14", "v15", "v16", "v17", "v18", "v19", "v20", "v21", "v22", "v23", "v24", "v25", "v26", "v27", "v28", "v29", "v30", "v31", "v32", "v33", "v34", "v35", "v36", "v37", "v38", "v39", "v40", "v41", "v42", "v43", "v44", "v45", "v46", "v47", "v48", "v49", "v50", "v51", "v52", "v53", "v54", "v55", "v56", "v57", "v58", "v59", "v60", "v61", "v62", "v63", "v64", "v65", "v66", "v67", "v68", "v69", "v70", "v71", "v72", "v73", "v74", "v75", "v76", "v77", "v78", "v79", "v80", "v81", "v82", "v83", "v84", "v85", "v86", "v87", "v88", "v89", "v90", "v91", "v92", "v93", "v94", "v95", "v96", "v97", "v98", "v99", "v100", "v101", "v102", "v103", "v104", "v105", "v106", "v107", "v108", "v109", "v110", "v111", "v112", "v113", "v114", "v115", "v116", "v117", "v118", "v119", "v120", "v121", "v122", "v123", "v124", "v125", "v126", "v127", "v128", "v129", "v130", "v131", "v132", "v133", "v134", "v135", "v136", "v137", "v138", "v139", "v140", "v141", "v142", "v143", "v144", "v145", "v146", "v147", "v148", "v149", "v150", "v151", "v152", "v153", "v154", "v155", "v156", "v157", "v158", "v159", "v160", "v161", "v162", "v163", "v164", "v165", "v166", "v167", "v168", "v169", "v170", "v171", "v172", "v173", "v174", "v175", "v176", "v177", "v178", "v179", "v180", "v181", "v182", "v183", "v184", "v185", "v186", "v187", "v188", "v189", "v190", "v191", "v192", "v193", "v194", "v195", "v196", "v197", "v198", "v199", "v200", "v201", "v202", "v203", "v204", "v205", "v206", "v207", "v208", "v209", "v210", "v211", "v212", "v213", "v214", "v215", "v216", "v217", "v218", "v219", "v220", "v221", "v222", "v223", "v224", "v225", "v226", "v227", "v228", "v229", "v230", "v231", "v232", "v233", "v234", "v235", "v236", "v237", "v238", "v239", "v240", "v241", "v242", "v243", "v244", "v245", "v246", "v247", "v248", "v249", "v250", "v251", "v252", "v253", "v254", "v255", "s3", "s4", "s5", "s6", "s7", "s8", "s9", "s10", "s11", "s12", "s13", "s14", "s15", "s16", "s17", "s18", "s19", "s20", "s21", "s22", "s23", "s24", "s25", "s26", "s27", "s28", "s29", "s30", "s31", "s34", "s35", "s36", "s37", "s38", "s39", "s40", "s41", "s42", "s43", "s44", "s45", "s46", "s47", "s48", "s49", "s50", "s51", "s52", "s53", "s54", "s55", "s56", "s57", "s58", "s59", "s60", "s61", "s62", "s63", "s64", "s65", "s66", "s67", "s68", "s69", "s70", "s71");
  asm volatile("" :: "v"((__attribute__((address_space(3))) char*)shm));
}

extern "C" void kernel_launch(void* const* d_in, const int* in_sizes, int n_in, void* d_out, int out_size,
                              void* d_ws, size_t ws_size, hipStream_t stream) {
  xd_kernel<<<256, 512, 0, stream>>>((const int*)d_in[0], (const u16*)d_in[4], (const u16*)d_in[1], (const u16*)d_in[5], (const u16*)d_in[2], (const u16*)d_in[3], (char*)d_ws,
                                     (const u16*)d_in[6], (const u16*)d_in[7], (u16*)d_out);
}
```

```cpp
#include <hip/hip_runtime.h>
typedef unsigned short u16;

__device__ const __attribute__((aligned(16), used, visibility("hidden"))) u16 xd_tab[5120] asm("xd_tab") = {924, 925, 928, 929, 926, 927, 930, 931, 934, 935, 938, 939, 932, 933, 936, 937, 940, 941, 944, 945, 942, 943, 946, 947, 948, 0, 949, 0, 0, 0, 0, 0, 2, 36, 138, 172, 0, 1, 24, 25, 338, 372, 474, 508, 34, 35, 57, 58, 546, 580, 682, 716, 68, 69, 90, 91, 816, 817, 850, 952, 102, 99, 123, 124, 680, 681, 684, 685, 20, 21, 22, 23, 714, 715, 717, 718, 54, 55, 56, 53, 748, 749, 750, 751, 88, 89, 86, 87, 782, 779, 783, 784, 122, 119, 120, 121, 152, 153, 154, 155, 284, 285, 286, 287, 186, 187, 188, 185, 318, 319, 320, 317, 220, 221, 218, 219, 352, 353, 350, 351, 254, 251, 252, 253, 386, 383, 384, 385, 416, 417, 418, 419, 548, 549, 550, 551, 450, 451, 452, 449, 582, 583, 584, 581, 484, 485, 482, 483, 616, 617, 614, 615, 518, 515, 516, 517, 650, 647, 648, 649, 544, 545, 552, 553, 16, 17, 18, 19, 578, 579, 585, 586, 50, 51, 52, 49, 612, 613, 618, 619, 84, 85, 82, 83, 646, 643, 651, 652, 118, 115, 116, 117, 148, 149, 150, 151, 280, 281, 282, 283, 182, 183, 184, 181, 314, 315, 316, 313, 216, 217, 214, 215, 348, 349, 346, 347, 250, 247, 248, 249, 382, 379, 380, 381, 412, 413, 414, 415, 408, 409, 420, 421, 446, 447, 448, 445, 442, 443, 453, 454, 480, 481, 478, 479, 476, 477, 486, 487, 514, 511, 512, 513, 510, 507, 519, 520, 12, 13, 14, 15, 144, 145, 146, 147, 46, 47, 48, 45, 178, 179, 180, 177, 80, 81, 78, 79, 212, 213, 210, 211, 114, 111, 112, 113, 246, 243, 244, 245, 276, 277, 278, 279, 272, 273, 288, 289, 310, 311, 312, 309, 306, 307, 321, 322, 344, 345, 342, 343, 340, 341, 354, 355, 378, 375, 376, 377, 374, 371, 387, 388, 8, 9, 10, 11, 140, 141, 142, 143, 42, 43, 44, 41, 174, 175, 176, 173, 76, 77, 74, 75, 208, 209, 206, 207, 110, 107, 108, 109, 242, 239, 240, 241, 136, 137, 156, 157, 4, 5, 6, 7, 170, 171, 189, 190, 38, 39, 40, 37, 204, 205, 222, 223, 72, 73, 70, 71, 238, 235, 255, 256, 106, 103, 104, 105, 0, 0, 0, 0, 0, 0, 0, 0, 0, 0, 0, 0, 0, 0, 0, 0, 0, 0, 0, 0, 0, 0, 0, 0, 0, 0, 0, 0, 0, 0, 0, 0, 0, 0, 0, 0, 0, 0, 0, 0, 0, 0, 0, 0, 0, 0, 0, 0, 0, 0, 0, 0, 0, 0, 0, 0, 0, 0, 0, 0, 0, 0, 0, 0, 0, 0, 0, 0, 0, 0, 0, 0, 0, 0, 0, 0, 0, 0, 0, 0, 0, 0, 0, 0, 0, 0, 0, 0, 0, 0, 0, 0, 0, 0, 0, 0, 0, 0, 0, 0, 0, 0, 0, 0, 0, 0, 0, 0, 0, 0, 0, 0, 0, 0, 0, 0, 0, 0, 0, 0, 0, 0, 0, 0, 0, 0, 0, 0, 924, 925, 926, 927, 934, 935, 932, 933, 940, 941, 942, 943, 948, 0, 0, 0, 2, 36, 0, 1, 338, 372, 34, 35, 546, 580, 68, 69, 816, 817, 102, 99, 928, 929, 930, 931, 938, 939, 936, 937, 944, 945, 946, 947, 949, 0, 0, 0, 138, 172, 24, 25, 474, 508, 57, 58, 682, 716, 90, 91, 850, 952, 123, 124, 680, 681, 20, 21, 714, 715, 54, 55, 748, 749, 88, 89, 782, 779, 122, 119, 152, 153, 284, 285, 186, 187, 318, 319, 220, 221, 352, 353, 254, 251, 386, 383, 684, 685, 22, 23, 717, 718, 56, 53, 750, 751, 86, 87, 783, 784, 120, 121, 154, 155, 286, 287, 188, 185, 320, 317, 218, 219, 350, 351, 252, 253, 384, 385, 416, 417, 548, 549, 450, 451, 582, 583, 484, 485, 616, 617, 518, 515, 650, 647, 544, 545, 16, 17, 578, 579, 50, 51, 612, 613, 84, 85, 646, 643, 118, 115, 418, 419, 550, 551, 452, 449, 584, 581, 482, 483, 614, 615, 516, 517, 648, 649, 552, 553, 18, 19, 585, 586, 52, 49, 618, 619, 82, 83, 651, 652, 116, 117, 148, 149, 280, 281, 182, 183, 314, 315, 216, 217, 348, 349, 250, 247, 382, 379, 412, 413, 408, 409, 446, 447, 442, 443, 480, 481, 476, 477, 514, 511, 510, 507, 150, 151, 282, 283, 184, 181, 316, 313, 214, 215, 346, 347, 248, 249, 380, 381, 414, 415, 420, 421, 448, 445, 453, 454, 478, 479, 486, 487, 512, 513, 519, 520, 12, 13, 144, 145, 46, 47, 178, 179, 80, 81, 212, 213, 114, 111, 246, 243, 276, 277, 272, 273, 310, 311, 306, 307, 344, 345, 340, 341, 378, 375, 374, 371, 14, 15, 146, 147, 48, 45, 180, 177, 78, 79, 210, 211, 112, 113, 244, 245, 278, 279, 288, 289, 312, 309, 321, 322, 342, 343, 354, 355, 376, 377, 387, 388, 8, 9, 140, 141, 42, 43, 174, 175, 76, 77, 208, 209, 110, 107, 242, 239, 136, 137, 4, 5, 170, 171, 38, 39, 204, 205, 72, 73, 238, 235, 106, 103, 10, 11, 142, 143, 44, 41, 176, 173, 74, 75, 206, 207, 108, 109, 240, 241, 156, 157, 6, 7, 189, 190, 40, 37, 222, 223, 70, 71, 255, 256, 104, 105, 0, 0, 0, 0, 0, 0, 0, 0, 0, 0, 0, 0, 0, 0, 0, 0, 0, 0, 0, 0, 0, 0, 0, 0, 0, 0, 0, 0, 0, 0, 0, 0, 0, 0, 0, 0, 0, 0, 0, 0, 0, 0, 0, 0, 0, 0, 0, 0, 0, 0, 0, 0, 0, 0, 0, 0, 0, 0, 0, 0, 0, 0, 0, 0, 0, 0, 0, 0, 0, 0, 0, 0, 0, 0, 0, 0, 0, 0, 0, 0, 0, 0, 0, 0, 0, 0, 0, 0, 0, 0, 0, 0, 0, 0, 0, 0, 0, 0, 0, 0, 0, 0, 0, 0, 0, 0, 0, 0, 0, 0, 0, 0, 0, 0, 0, 0, 0, 0, 0, 0, 0, 0, 0, 0, 0, 0, 0, 0, 0, 0, 0, 0, 0, 0, 0, 0, 0, 0, 0, 0, 0, 0, 0, 0, 0, 0, 0, 0, 0, 0, 0, 0, 0, 0, 0, 0, 0, 0, 0, 0, 0, 16256, 0, 16256, 0, 16256, 0, 16256, 0, 16256, 0, 16256, 0, 16256, 0, 16256, 0, 16256, 0, 16256, 0, 16256, 0, 16256, 0, 16128, 0, 16256, 0, 16128, 0, 0, 0, 16128, 0, 16256, 0, 16256, 0, 16256, 0, 16128, 0, 16256, 0, 16256, 0, 16256, 0, 16128, 0, 16256, 0, 16256, 0, 16256, 0, 16128, 0, 16256, 0, 16256, 0, 16256, 0, 16256, 0, 16256, 0, 16256, 0, 16256, 0, 16256, 0, 16256, 0, 16256, 0, 16256, 0, 16256, 0, 16256, 0, 16256, 0, 16256, 0, 16256, 0, 16256, 0, 16256, 0, 16256, 0, 16256, 0, 16256, 0, 16256, 0, 16256, 0, 16256, 0, 16256, 0, 16256, 0, 16256, 0, 16256, 0, 16256, 0, 16256, 0, 16256, 0, 16256, 0, 16256, 0, 16256, 0, 16256, 0, 16128, 0, 16256, 0, 16256, 0, 16256, 0, 16128, 0, 16256, 0, 16256, 0, 16256, 0, 16128, 0, 16256, 0, 16256, 0, 16256, 0, 16128, 0, 16256, 0, 16256, 0, 16256, 0, 16256, 0, 16256, 0, 16256, 0, 16256, 0, 16256, 0, 16256, 0, 16256, 0, 16256, 0, 16256, 0, 16256, 0, 16256, 0, 16256, 0, 16256, 0, 16256, 0, 16256, 0, 16256, 0, 16256, 0, 16256, 0, 16256, 0, 16256, 0, 16256, 0, 16256, 0, 16256, 0, 16256, 0, 16256, 0, 16256, 0, 16256, 0, 16256, 0, 16256, 0, 16256, 0, 16256, 0, 16256, 0, 16256, 0, 16256, 0, 16256, 0, 16256, 0, 16256, 0, 16256, 0, 16256, 0, 16256, 0, 16256, 0, 16256, 0, 16256, 0, 16256, 0, 16256, 0, 16256, 0, 16256, 0, 16256, 0, 16256, 0, 16256, 0, 16256, 0, 16256, 0, 16256, 0, 16256, 0, 16256, 0, 16256, 0, 16256, 0, 16256, 0, 16256, 0, 16256, 0, 16256, 0, 16256, 0, 16256, 0, 16256, 0, 16256, 0, 16256, 0, 16256, 0, 16256, 0, 16256, 0, 16256, 0, 16256, 0, 16256, 0, 16256, 0, 16256, 0, 16256, 0, 16256, 0, 16256, 0, 16256, 0, 16256, 0, 16256, 0, 16128, 0, 16256, 0, 16256, 0, 16256, 0, 16128, 0, 16256, 0, 16256, 0, 16256, 0, 16128, 0, 16256, 0, 16256, 0, 16256, 0, 16128, 0, 16256, 0, 16256, 0, 16256, 0, 0, 0, 0, 0, 0, 0, 0, 0, 0, 0, 0, 0, 0, 0, 0, 0, 0, 0, 0, 0, 0, 0, 0, 0, 0, 0, 0, 0, 0, 0, 0, 0, 0, 0, 0, 0, 0, 0, 0, 0, 0, 0, 0, 0, 0, 0, 0, 0, 0, 0, 0, 0, 0, 0, 0, 0, 0, 0, 0, 0, 0, 0, 0, 0, 0, 0, 0, 0, 0, 0, 0, 0, 0, 0, 0, 0, 0, 0, 0, 0, 0, 0, 0, 0, 0, 0, 0, 0, 0, 0, 0, 0, 0, 0, 0, 0, 0, 0, 0, 0, 0, 0, 0, 0, 0, 0, 0, 0, 0, 0, 0, 0, 0, 0, 0, 0, 0, 0, 0, 0, 0, 0, 0, 0, 0, 0, 0, 0, 0, 0, 0, 0, 0, 0, 0, 0, 0, 0, 0, 0, 0, 0, 0, 0, 0, 0, 0, 0, 0, 0, 0, 0, 0, 0, 0, 0, 0, 0, 0, 0, 16128, 0, 16256, 0, 16256, 0, 16256, 0, 16128, 0, 16256, 0, 16256, 0, 16256, 0, 16128, 0, 16256, 0, 16256, 0, 16256, 0, 16128, 0, 16256, 0, 16256, 0, 16256, 0, 16256, 0, 16256, 0, 16256, 0, 16256, 0, 16256, 0, 16256, 0, 16256, 0, 16256, 0, 16256, 0, 16256, 0, 16256, 0, 16256, 0, 16256, 0, 16256, 0, 16256, 0, 16256, 0, 16256, 0, 16256, 0, 16256, 0, 16256, 0, 16256, 0, 16256, 0, 16256, 0, 16256, 0, 16256, 0, 16256, 0, 16256, 0, 16256, 0, 16256, 0, 16256, 0, 16256, 0, 16256, 0, 16256, 0, 16256, 0, 16256, 0, 16256, 0, 16256, 0, 16256, 0, 16256, 0, 16256, 0, 16256, 0, 16256, 0, 16256, 0, 16256, 0, 16256, 0, 16256, 0, 16256, 0, 16256, 0, 16256, 0, 16256, 0, 16256, 0, 16256, 0, 16256, 0, 16256, 0, 16256, 0, 16256, 0, 16256, 0, 16256, 0, 16256, 0, 16256, 0, 16256, 0, 16256, 0, 16256, 0, 16256, 0, 16256, 0, 16256, 0, 16256, 0, 16256, 0, 16256, 0, 16256, 0, 16256, 0, 16256, 0, 16256, 0, 16256, 0, 16256, 0, 16256, 0, 16256, 0, 16256, 0, 16256, 0, 16256, 0, 16128, 0, 16256, 0, 16256, 0, 16256, 0, 16128, 0, 16256, 0, 16256, 0, 16256, 0, 16128, 0, 16256, 0, 16256, 0, 16256, 0, 16128, 0, 16256, 0, 16256, 0, 16256, 0, 16256, 0, 16256, 0, 16256, 0, 16256, 0, 16256, 0, 16256, 0, 16256, 0, 16256, 0, 16256, 0, 16256, 0, 16256, 0, 16256, 0, 16256, 0, 16256, 0, 16256, 0, 16256, 0, 16128, 0, 16256, 0, 16256, 0, 16256, 0, 16128, 0, 16256, 0, 16256, 0, 16256, 0, 16128, 0, 16256, 0, 16256, 0, 16256, 0, 16128, 0, 16256, 0, 16256, 0, 16256, 0, 16256, 0, 16256, 0, 16256, 0, 16256, 0, 16256, 0, 16256, 0, 16256, 0, 16256, 0, 16256, 0, 16256, 0, 16256, 0, 16256, 0, 16256, 0, 16256, 0, 16256, 0, 16256, 0, 16256, 0, 16256, 0, 16256, 0, 16256, 0, 16256, 0, 16256, 0, 16256, 0, 16256, 0, 16256, 0, 16256, 0, 16256, 0, 16256, 0, 16256, 0, 16256, 0, 16256, 0, 16256, 0, 0, 0, 0, 0, 0, 0, 0, 0, 0, 0, 0, 0, 0, 0, 0, 0, 0, 0, 0, 0, 0, 0, 0, 0, 0, 0, 0, 0, 0, 0, 0, 0, 0, 0, 0, 0, 0, 0, 0, 0, 0, 0, 0, 0, 0, 0, 0, 0, 0, 0, 0, 0, 0, 0, 0, 0, 0, 0, 0, 0, 0, 0, 0, 0, 0, 0, 0, 0, 0, 0, 0, 0, 0, 0, 0, 0, 0, 0, 0, 0, 0, 0, 0, 0, 0, 0, 0, 0, 0, 0, 0, 0, 0, 0, 0, 0, 0, 0, 0, 0, 0, 0, 0, 0, 0, 0, 0, 0, 0, 0, 0, 0, 0, 0, 0, 0, 0, 0, 0, 0, 0, 0, 0, 0, 0, 0, 0, 0, 16768, 0, 16768, 0, 16768, 0, 16768, 0, 16768, 0, 16768, 0, 16768, 0, 16768, 0, 16768, 0, 16768, 0, 16768, 0, 16768, 0, 16768, 0, 0, 0, 16768, 0, 0, 0, 16256, 0, 16256, 0, 16256, 0, 16256, 0, 16256, 0, 16256, 0, 16256, 0, 16256, 0, 16256, 0, 16256, 0, 16256, 0, 16256, 0, 16128, 0, 16256, 0, 16128, 0, 17280, 0, 16128, 0, 16256, 0, 16256, 0, 16256, 0, 16128, 0, 16256, 0, 16256, 0, 16256, 0, 16128, 0, 16256, 0, 16256, 0, 16256, 0, 16128, 0, 16256, 0, 16256, 0, 16256, 0, 16256, 0, 16256, 0, 16256, 0, 16256, 0, 16256, 0, 16256, 0, 16256, 0, 16256, 0, 16256, 0, 16256, 0, 16256, 0, 16256, 0, 16256, 0, 16256, 0, 16256, 0, 16256, 0, 16256, 0, 16256, 0, 16256, 0, 16256, 0, 16256, 0, 16256, 0, 16256, 0, 16256, 0, 16256, 0, 16256, 0, 16256, 0, 16256, 0, 16256, 0, 16256, 0, 16256, 0, 16256, 0, 16128, 0, 16256, 0, 16256, 0, 16256, 0, 16128, 0, 16256, 0, 16256, 0, 16256, 0, 16128, 0, 16256, 0, 16256, 0, 16256, 0, 16128, 0, 16256, 0, 16256, 0, 16256, 0, 16256, 0, 16256, 0, 16256, 0, 16256, 0, 16256, 0, 16256, 0, 16256, 0, 16256, 0, 16256, 0, 16256, 0, 16256, 0, 16256, 0, 16256, 0, 16256, 0, 16256, 0, 16256, 0, 16256, 0, 16256, 0, 16256, 0, 16256, 0, 16256, 0, 16256, 0, 16256, 0, 16256, 0, 16256, 0, 16256, 0, 16256, 0, 16256, 0, 16256, 0, 16256, 0, 16256, 0, 16256, 0, 16256, 0, 16256, 0, 16256, 0, 16256, 0, 16256, 0, 16256, 0, 16256, 0, 16256, 0, 16256, 0, 16256, 0, 16256, 0, 16256, 0, 16256, 0, 16256, 0, 16256, 0, 16256, 0, 16256, 0, 16256, 0, 16256, 0, 16256, 0, 16256, 0, 16256, 0, 16256, 0, 16256, 0, 16256, 0, 16256, 0, 16256, 0, 16256, 0, 16256, 0, 16256, 0, 16256, 0, 16256, 0, 16256, 0, 16256, 0, 16256, 0, 16256, 0, 16256, 0, 16256, 0, 16256, 0, 16256, 0, 16256, 0, 16256, 0, 16256, 0, 16256, 0, 16256, 0, 16256, 0, 16256, 0, 16256, 0, 16128, 0, 16256, 0, 16256, 0, 16256, 0, 16128, 0, 16256, 0, 16256, 0, 16256, 0, 16128, 0, 16256, 0, 16256, 0, 16256, 0, 16128, 0, 16256, 0, 16256, 0, 16256, 0, 0, 0, 0, 0, 0, 0, 0, 0, 0, 0, 0, 0, 0, 0, 0, 0, 0, 0, 0, 0, 0, 0, 0, 0, 0, 0, 0, 0, 0, 0, 0, 0, 0, 0, 0, 0, 0, 0, 0, 0, 0, 0, 0, 0, 0, 0, 0, 0, 0, 0, 0, 0, 0, 0, 0, 0, 0, 0, 0, 0, 0, 0, 0, 0, 0, 0, 0, 0, 0, 0, 0, 0, 0, 0, 0, 0, 0, 0, 0, 0, 0, 0, 0, 0, 0, 0, 0, 0, 0, 0, 0, 0, 0, 0, 0, 0, 0, 0, 0, 0, 0, 0, 0, 0, 0, 0, 0, 0, 0, 0, 0, 0, 0, 0, 0, 0, 0, 0, 0, 0, 0, 0, 0, 0, 0, 0, 0, 0, 16768, 0, 16768, 0, 16768, 0, 16768, 0, 16768, 0, 16768, 0, 16768, 0, 16768, 0, 16768, 0, 16768, 0, 16768, 0, 16768, 0, 0, 0, 0, 0, 0, 0, 0, 0, 16128, 0, 16256, 0, 16256, 0, 16256, 0, 16128, 0, 16256, 0, 16256, 0, 16256, 0, 16128, 0, 16256, 0, 16256, 0, 16256, 0, 16128, 0, 16256, 0, 16256, 0, 16256, 0, 16256, 0, 16256, 0, 16256, 0, 16256, 0, 16256, 0, 16256, 0, 16256, 0, 16256, 0, 16256, 0, 16256, 0, 16256, 0, 16256, 0, 16256, 0, 16256, 0, 16256, 0, 16256, 0, 16256, 0, 16256, 0, 16256, 0, 16256, 0, 16256, 0, 16256, 0, 16256, 0, 16256, 0, 16256, 0, 16256, 0, 16256, 0, 16256, 0, 16256, 0, 16256, 0, 16256, 0, 16256, 0, 16256, 0, 16256, 0, 16256, 0, 16256, 0, 16256, 0, 16256, 0, 16256, 0, 16256, 0, 16256, 0, 16256, 0, 16256, 0, 16256, 0, 16256, 0, 16256, 0, 16256, 0, 16256, 0, 16256, 0, 16256, 0, 16256, 0, 16256, 0, 16256, 0, 16256, 0, 16256, 0, 16256, 0, 16256, 0, 16256, 0, 16256, 0, 16256, 0, 16256, 0, 16256, 0, 16256, 0, 16256, 0, 16256, 0, 16256, 0, 16256, 0, 16256, 0, 16256, 0, 16256, 0, 16256, 0, 16256, 0, 16256, 0, 16256, 0, 16256, 0, 16256, 0, 16256, 0, 16256, 0, 16256, 0, 16256, 0, 16128, 0, 16256, 0, 16256, 0, 16256, 0, 16128, 0, 16256, 0, 16256, 0, 16256, 0, 16128, 0, 16256, 0, 16256, 0, 16256, 0, 16128, 0, 16256, 0, 16256, 0, 16256, 0, 16256, 0, 16256, 0, 16256, 0, 16256, 0, 16256, 0, 16256, 0, 16256, 0, 16256, 0, 16256, 0, 16256, 0, 16256, 0, 16256, 0, 16256, 0, 16256, 0, 16256, 0, 16256, 0, 16128, 0, 16256, 0, 16256, 0, 16256, 0, 16128, 0, 16256, 0, 16256, 0, 16256, 0, 16128, 0, 16256, 0, 16256, 0, 16256, 0, 16128, 0, 16256, 0, 16256, 0, 16256, 0, 16256, 0, 16256, 0, 16256, 0, 16256, 0, 16256, 0, 16256, 0, 16256, 0, 16256, 0, 16256, 0, 16256, 0, 16256, 0, 16256, 0, 16256, 0, 16256, 0, 16256, 0, 16256, 0, 16256, 0, 16256, 0, 16256, 0, 16256, 0, 16256, 0, 16256, 0, 16256, 0, 16256, 0, 16256, 0, 16256, 0, 16256, 0, 16256, 0, 16256, 0, 16256, 0, 16256, 0, 16256, 0, 0, 0, 0, 0, 0, 0, 0, 0, 0, 0, 0, 0, 0, 0, 0, 0, 0, 0, 0, 0, 0, 0, 0, 0, 0, 0, 0, 0, 0, 0, 0, 0, 0, 0, 0, 0, 0, 0, 0, 0, 0, 0, 0, 0, 0, 0, 0, 0, 0, 0, 0, 0, 0, 0, 0, 0, 0, 0, 0, 0, 0, 0, 0, 0, 0, 0, 0, 0, 0, 0, 0, 0, 0, 0, 0, 0, 0, 0, 0, 0, 0, 0, 0, 0, 0, 0, 0, 0, 0, 0, 0, 0, 0, 0, 0, 0, 0, 0, 0, 0, 0, 0, 0, 0, 0, 0, 0, 0, 0, 0, 0, 0, 0, 0, 0, 0, 0, 0, 0, 0, 0, 0, 0, 0, 0, 0, 0, 0, 0, 0, 0, 0, 0, 0, 0, 0, 0, 0, 0, 0, 0, 0, 0, 0, 16256, 0, 16256, 0, 16128, 0, 16256, 0, 16256, 0, 16256, 0, 16128, 0, 16256, 0, 0, 0, 0, 0, 0, 0, 0, 0, 0, 0, 0, 0, 0, 0, 0, 0, 16256, 0, 16256, 0, 16256, 0, 16256, 0, 16256, 0, 16256, 0, 16256, 0, 16256, 0, 16128, 0, 16256, 0, 16256, 0, 16256, 0, 16128, 0, 16256, 0, 16256, 0, 16256, 0, 16256, 0, 16256, 0, 16256, 0, 16256, 0, 16256, 0, 16256, 0, 16256, 0, 16256, 0, 16256, 0, 16256, 0, 16256, 0, 16256, 0, 16256, 0, 16256, 0, 16256, 0, 16256, 0, 16256, 0, 16256, 0, 16256, 0, 16256, 0, 16256, 0, 16256, 0, 16256, 0, 16256, 0, 16256, 0, 16256, 0, 16256, 0, 16256, 0, 16256, 0, 16256, 0, 16256, 0, 16256, 0, 16128, 0, 16256, 0, 16256, 0, 16256, 0, 16128, 0, 16256, 0, 16256, 0, 16256, 0, 16256, 0, 16256, 0, 16256, 0, 16256, 0, 16256, 0, 16256, 0, 16256, 0, 16256, 0, 16256, 0, 16256, 0, 16256, 0, 16256, 0, 16256, 0, 16256, 0, 16256, 0, 16256, 0, 16256, 0, 16256, 0, 16256, 0, 16256, 0, 16256, 0, 16256, 0, 16256, 0, 16256, 0, 16256, 0, 16256, 0, 16128, 0, 16256, 0, 16256, 0, 16256, 0, 16128, 0, 16256, 0, 16256, 0, 16256, 0, 16256, 0, 16256, 0, 16256, 0, 16256, 0, 16256, 0, 16256, 0, 16256, 0, 16256, 0, 16256, 0, 16256, 0, 16256, 0, 16256, 0, 16256, 0, 16256, 0, 16256, 0, 16256, 0, 16256, 0, 16256, 0, 16256, 0, 16256, 0, 16256, 0, 16256, 0, 16256, 0, 16256, 0, 16128, 0, 16256, 0, 16256, 0, 16256, 0, 16128, 0, 16256, 0, 16256, 0, 16256, 0, 16256, 0, 16256, 0, 16256, 0, 16256, 0, 16256, 0, 16256, 0, 16256, 0, 16256, 0, 16256, 0, 16256, 0, 16256, 0, 16256, 0, 16256, 0, 16256, 0, 16256, 0, 16256, 0, 16256, 0, 16256, 0, 16256, 0, 16256, 0, 16256, 0, 16256, 0, 16128, 0, 16256, 0, 16256, 0, 16256, 0, 16128, 0, 16256, 0, 16256, 0, 16256, 0, 16256, 0, 16256, 0, 16256, 0, 16256, 0, 16256, 0, 16256, 0, 16256, 0, 16256, 0, 16256, 0, 16256, 0, 16256, 0, 16256, 0, 16256, 0, 16256, 0, 16256, 0, 16256, 0, 0, 0, 0, 0, 0, 0, 0, 0, 0, 0, 0, 0, 0, 0, 0, 0, 0, 0, 0, 0, 0, 0, 0, 0, 0, 0, 0, 0, 0, 0, 0, 0, 0, 0, 0, 0, 0, 0, 0, 0, 0, 0, 0, 0, 0, 0, 0, 0, 0, 0, 0, 0, 0, 0, 0, 0, 0, 0, 0, 0, 0, 0, 0, 0, 0, 0, 0, 0, 0, 0, 0, 0, 0, 0, 0, 0, 0, 0, 0, 0, 0, 0, 0, 0, 0, 0, 0, 0, 0, 0, 0, 0, 0, 0, 0, 0, 0, 0, 0, 0, 0, 0, 0, 0, 0, 0, 0, 0, 0, 0, 0, 0, 0, 0, 0, 0, 0, 0, 0, 0, 0, 0, 0, 0, 0, 0, 0, 0, 0, 0, 0, 0, 0, 0, 0, 0, 0, 0, 0, 0, 0, 0, 0, 0, 16256, 0, 16256, 0, 16128, 0, 16256, 0, 16128, 0, 16256, 0, 16128, 0, 16256, 0, 0, 0, 0, 0, 0, 0, 0, 0, 0, 0, 0, 0, 0, 0, 0, 0, 16256, 0, 16256, 0, 16256, 0, 16256, 0, 16128, 0, 0, 0, 16256, 0, 16256, 0, 16128, 0, 16256, 0, 16256, 0, 16256, 0, 16128, 0, 16256, 0, 16256, 0, 16256, 0, 16256, 0, 16256, 0, 16256, 0, 16256, 0, 16256, 0, 16256, 0, 16256, 0, 16256, 0, 16256, 0, 16256, 0, 16256, 0, 16256, 0, 16256, 0, 16256, 0, 16256, 0, 16256, 0, 16256, 0, 16256, 0, 16256, 0, 16256, 0, 16256, 0, 16256, 0, 16256, 0, 16256, 0, 16256, 0, 16256, 0, 16256, 0, 16256, 0, 16256, 0, 16256, 0, 16256, 0, 16256, 0, 16128, 0, 16256, 0, 16256, 0, 16256, 0, 16128, 0, 16256, 0, 16256, 0, 16256, 0, 16256, 0, 16256, 0, 16256, 0, 16256, 0, 16256, 0, 16256, 0, 16256, 0, 16256, 0, 16256, 0, 16256, 0, 16256, 0, 16256, 0, 16256, 0, 16256, 0, 16256, 0, 16256, 0, 16256, 0, 16256, 0, 16256, 0, 16256, 0, 16256, 0, 16256, 0, 16256, 0, 16256, 0, 16256, 0, 16256, 0, 16128, 0, 16256, 0, 16256, 0, 16256, 0, 16128, 0, 16256, 0, 16256, 0, 16256, 0, 16256, 0, 16256, 0, 16256, 0, 16256, 0, 16256, 0, 16256, 0, 16256, 0, 16256, 0, 16256, 0, 16256, 0, 16256, 0, 16256, 0, 16256, 0, 16256, 0, 16256, 0, 16256, 0, 16256, 0, 16256, 0, 16256, 0, 16256, 0, 16256, 0, 16256, 0, 16256, 0, 16256, 0, 16128, 0, 16256, 0, 16256, 0, 16256, 0, 16128, 0, 16256, 0, 16256, 0, 16256, 0, 16256, 0, 16256, 0, 16256, 0, 16256, 0, 16256, 0, 16256, 0, 16256, 0, 16256, 0, 16256, 0, 16256, 0, 16256, 0, 16256, 0, 16256, 0, 16256, 0, 16256, 0, 16256, 0, 16256, 0, 16256, 0, 16256, 0, 16256, 0, 16256, 0, 16256, 0, 16128, 0, 16256, 0, 16256, 0, 16256, 0, 16128, 0, 16256, 0, 16256, 0, 16256, 0, 16256, 0, 16256, 0, 16256, 0, 16256, 0, 16256, 0, 16256, 0, 16256, 0, 16256, 0, 16256, 0, 16256, 0, 16256, 0, 16256, 0, 16256, 0, 16256, 0, 16256, 0, 16256, 0, 0, 0, 0, 0, 0, 0, 0, 0, 0, 0, 0, 0, 0, 0, 0, 0, 0, 0, 0, 0, 0, 0, 0, 0, 0, 0, 0, 0, 0, 0, 0, 0, 0, 0, 0, 0, 0, 0, 0, 0, 0, 0, 0, 0, 0, 0, 0, 0, 0, 0, 0, 0, 0, 0, 0, 0, 0, 0, 0, 0, 0, 0, 0, 0, 0, 0, 0, 0, 0, 0, 0, 0, 0, 0, 0, 0, 0, 0, 0, 0, 0, 0, 0, 0, 0, 0, 0, 0, 0, 0, 0, 0, 0, 0, 0, 0, 0, 0, 0, 0, 0, 0, 0, 0, 0, 0, 0, 0, 0, 0, 0, 0, 0, 0, 0, 0, 0, 0, 0, 0, 0, 0, 0, 0, 0, 0, 0, 0, 16768, 0, 16768, 0, 16768, 0, 16768, 0, 16768, 0, 16768, 0, 16768, 0, 16768, 0, 16256, 0, 16256, 0, 16128, 0, 16256, 0, 16256, 0, 16256, 0, 16128, 0, 16256, 0, 16768, 0, 16768, 0, 16768, 0, 16768, 0, 16768, 0, 16768, 0, 16768, 0, 16768, 0, 16256, 0, 16256, 0, 16256, 0, 16256, 0, 16256, 0, 16256, 0, 16256, 0, 16256, 0, 16128, 0, 16256, 0, 16256, 0, 16256, 0, 16128, 0, 16256, 0, 16256, 0, 16256, 0, 16256, 0, 16256, 0, 16256, 0, 16256, 0, 16256, 0, 16256, 0, 16256, 0, 16256, 0, 16256, 0, 16256, 0, 16256, 0, 16256, 0, 16256, 0, 16256, 0, 16256, 0, 16256, 0, 16256, 0, 16256, 0, 16256, 0, 16256, 0, 16256, 0, 16256, 0, 16256, 0, 16256, 0, 16256, 0, 16256, 0, 16256, 0, 16256, 0, 16256, 0, 16256, 0, 16256, 0, 16256, 0, 16128, 0, 16256, 0, 16256, 0, 16256, 0, 16128, 0, 16256, 0, 16256, 0, 16256, 0, 16256, 0, 16256, 0, 16256, 0, 16256, 0, 16256, 0, 16256, 0, 16256, 0, 16256, 0, 16256, 0, 16256, 0, 16256, 0, 16256, 0, 16256, 0, 16256, 0, 16256, 0, 16256, 0, 16256, 0, 16256, 0, 16256, 0, 16256, 0, 16256, 0, 16256, 0, 16256, 0, 16256, 0, 16256, 0, 16256, 0, 16128, 0, 16256, 0, 16256, 0, 16256, 0, 16128, 0, 16256, 0, 16256, 0, 16256, 0, 16256, 0, 16256, 0, 16256, 0, 16256, 0, 16256, 0, 16256, 0, 16256, 0, 16256, 0, 16256, 0, 16256, 0, 16256, 0, 16256, 0, 16256, 0, 16256, 0, 16256, 0, 16256, 0, 16256, 0, 16256, 0, 16256, 0, 16256, 0, 16256, 0, 16256, 0, 16256, 0, 16256, 0, 16128, 0, 16256, 0, 16256, 0, 16256, 0, 16128, 0, 16256, 0, 16256, 0, 16256, 0, 16256, 0, 16256, 0, 16256, 0, 16256, 0, 16256, 0, 16256, 0, 16256, 0, 16256, 0, 16256, 0, 16256, 0, 16256, 0, 16256, 0, 16256, 0, 16256, 0, 16256, 0, 16256, 0, 16256, 0, 16256, 0, 16256, 0, 16256, 0, 16256, 0, 16256, 0, 16128, 0, 16256, 0, 16256, 0, 16256, 0, 16128, 0, 16256, 0, 16256, 0, 16256, 0, 16256, 0, 16256, 0, 16256, 0, 16256, 0, 16256, 0, 16256, 0, 16256, 0, 16256, 0, 16256, 0, 16256, 0, 16256, 0, 16256, 0, 16256, 0, 16256, 0, 16256, 0, 16256, 0, 0, 0, 0, 0, 0, 0, 0, 0, 0, 0, 0, 0, 0, 0, 0, 0, 0, 0, 0, 0, 0, 0, 0, 0, 0, 0, 0, 0, 0, 0, 0, 0, 0, 0, 0, 0, 0, 0, 0, 0, 0, 0, 0, 0, 0, 0, 0, 0, 0, 0, 0, 0, 0, 0, 0, 0, 0, 0, 0, 0, 0, 0, 0, 0, 0, 0, 0, 0, 0, 0, 0, 0, 0, 0, 0, 0, 0, 0, 0, 0, 0, 0, 0, 0, 0, 0, 0, 0, 0, 0, 0, 0, 0, 0, 0, 0, 0, 0, 0, 0, 0, 0, 0, 0, 0, 0, 0, 0, 0, 0, 0, 0, 0, 0, 0, 0, 0, 0, 0, 0, 0, 0, 0, 0, 0, 0, 0, 0, 16768, 0, 16768, 0, 16768, 0, 16768, 0, 16768, 0, 0, 0, 0, 0, 0, 0, 16256, 0, 16256, 0, 16128, 0, 16256, 0, 16128, 0, 16256, 0, 16128, 0, 16256, 0, 16768, 0, 16768, 0, 16768, 0, 16768, 0, 16768, 0, 0, 0, 0, 0, 0, 0, 16256, 0, 16256, 0, 16256, 0, 16256, 0, 16128, 0, 17280, 0, 16256, 0, 16256, 0, 16128, 0, 16256, 0, 16256, 0, 16256, 0, 16128, 0, 16256, 0, 16256, 0, 16256, 0, 16256, 0, 16256, 0, 16256, 0, 16256, 0, 16256, 0, 16256, 0, 16256, 0, 16256, 0, 16256, 0, 16256, 0, 16256, 0, 16256, 0, 16256, 0, 16256, 0, 16256, 0, 16256, 0, 16256, 0, 16256, 0, 16256, 0, 16256, 0, 16256, 0, 16256, 0, 16256, 0, 16256, 0, 16256, 0, 16256, 0, 16256, 0, 16256, 0, 16256, 0, 16256, 0, 16256, 0, 16256, 0, 16128, 0, 16256, 0, 16256, 0, 16256, 0, 16128, 0, 16256, 0, 16256, 0, 16256, 0, 16256, 0, 16256, 0, 16256, 0, 16256, 0, 16256, 0, 16256, 0, 16256, 0, 16256, 0, 16256, 0, 16256, 0, 16256, 0, 16256, 0, 16256, 0, 16256, 0, 16256, 0, 16256, 0, 16256, 0, 16256, 0, 16256, 0, 16256, 0, 16256, 0, 16256, 0, 16256, 0, 16256, 0, 16256, 0, 16256, 0, 16128, 0, 16256, 0, 16256, 0, 16256, 0, 16128, 0, 16256, 0, 16256, 0, 16256, 0, 16256, 0, 16256, 0, 16256, 0, 16256, 0, 16256, 0, 16256, 0, 16256, 0, 16256, 0, 16256, 0, 16256, 0, 16256, 0, 16256, 0, 16256, 0, 16256, 0, 16256, 0, 16256, 0, 16256, 0, 16256, 0, 16256, 0, 16256, 0, 16256, 0, 16256, 0, 16256, 0, 16256, 0, 16128, 0, 16256, 0, 16256, 0, 16256, 0, 16128, 0, 16256, 0, 16256, 0, 16256, 0, 16256, 0, 16256, 0, 16256, 0, 16256, 0, 16256, 0, 16256, 0, 16256, 0, 16256, 0, 16256, 0, 16256, 0, 16256, 0, 16256, 0, 16256, 0, 16256, 0, 16256, 0, 16256, 0, 16256, 0, 16256, 0, 16256, 0, 16256, 0, 16256, 0, 16256, 0, 16128, 0, 16256, 0, 16256, 0, 16256, 0, 16128, 0, 16256, 0, 16256, 0, 16256, 0, 16256, 0, 16256, 0, 16256, 0, 16256, 0, 16256, 0, 16256, 0, 16256, 0, 16256, 0, 16256, 0, 16256, 0, 16256, 0, 16256, 0, 16256, 0, 16256, 0, 16256, 0, 16256, 0, 0, 0, 0, 0, 0, 0, 0, 0, 0, 0, 0, 0, 0, 0, 0, 0, 0, 0, 0, 0, 0, 0, 0, 0, 0, 0, 0, 0, 0, 0, 0, 0, 0, 0, 0, 0, 0, 0, 0, 0, 0, 0, 0, 0, 0, 0, 0, 0, 0, 0, 0, 0, 0, 0, 0, 0, 0, 0, 0, 0, 0, 0, 0, 0, 0, 0, 0, 0, 0, 0, 0, 0, 0, 0, 0, 0, 0, 0, 0, 0, 0, 0, 0, 0, 0, 0, 0, 0, 0, 0, 0, 0, 0, 0, 0, 0, 0, 0, 0, 0, 0, 0, 0, 0, 0, 0, 0, 0, 0, 0, 0, 0, 0, 0, 0, 0, 0, 0, 0, 0, 0, 0, 0, 0, 0, 0, 0};

__global__ __launch_bounds__(512) void xd_kernel(const int* idx, const u16* w1, const u16* table, const u16* b1, const u16* ws_cin, const u16* wout, char* ws, const u16* w2, const u16* b_out, u16* out) {
  __shared__ __attribute__((aligned(1024))) char shm[163840];
  unsigned tid = threadIdx.x;
  asm volatile(
    "s_load_dwordx16 s[4:19], s[0:1], 0\n"
    ".Lxd_top:\n"
    "v_readfirstlane_b32 s43, v0\n"
    "s_setprio 3\n"
    "s_cmp_lt_u32 s2, 8\n"
    "s_cbranch_scc1 .Lxd_s2\n"
    "s_cmp_lt_u32 s2, 48\n"
    "s_cbranch_scc1 .Lxd_help\n"
    "s_and_b32 s24, s2, 15\n"
    "s_mul_i32 s40, s43, 36\n"
    "s_mul_i32 s41, s24, 26624\n"
    "s_add_u32 s38, s40, s41\n"
    "v_lshlrev_b32 v86, 4, v0\n"
    "s_lshl_b32 s34, s43, 6\n"
    "s_add_u32 m0, s34, 98304\n"
    "s_waitcnt lgkmcnt(0)\n"
    "s_add_u32 s38, s4, s38\n"
    "s_addc_u32 s39, s5, 0\n"
    "global_load_lds_dwordx4 v86, s[38:39] offset:0\n"
    "global_load_lds_dwordx4 v86, s[38:39] offset:1024\n"
    "global_load_lds_dwordx4 v86, s[38:39] offset:2048\n"
    "global_load_lds_dwordx4 v86, s[38:39] offset:2304\n"
    "s_sub_u32 s3, s2, 48\n"
    "s_and_b32 s28, s2, 7\n"
    "s_lshr_b32 s23, s3, 4\n"
    "s_mul_i32 s42, s28, 3\n"
    "v_bfe_u32 v85, v0, 4, 2\n"
    "v_lshlrev_b32 v87, 1, v0\n"
    "v_and_b32 v87, 0xe0, v87\n"
    "v_and_b32 v88, 0xf0, v86\n"
    "v_xor_b32 v87, v87, v88\n"
    "s_movk_i32 s41, 3328\n"
    "v_mad_u32_u24 v46, v85, s41, v87\n"
    "v_add_u32 v47, 12288, v46\n"
    "s_mul_i32 s41, s43, 416\n"
    "s_lshl_b32 s40, s23, 8\n"
    "s_add_u32 s41, s41, s40\n"
    "s_mul_i32 s30, s42, 212992\n"
    "s_lshl_b32 s35, s43, 5\n"
    "s_bitset1_b32 s35, 15\n"
    "s_add_u32 s6, s6, s41\n"
    "s_addc_u32 s7, s7, 0\n"
    "s_add_u32 s30, s6, s30\n"
    "s_addc_u32 s31, s7, 0\n"
    "s_add_u32 m0, s35, 0\n"
    "s_nop 0\n"
    "global_load_lds_dwordx4 v46, s[30:31] offset:0\n"
    "global_load_lds_dwordx4 v47, s[30:31] offset:1024\n"
    "s_add_u32 s30, s30, 212992\n"
    "s_addc_u32 s31, s31, 0\n"
    "s_setprio 2\n"
    "v_and_b32 v84, 3, v0\n"
    "v_bfe_u32 v44, v0, 3, 3\n"
    "v_lshl_add_u32 v44, v84, 3, v44\n"
    "s_add_u32 s40, s34, 98304\n"
    "v_mul_u32_u24 v44, 104, v44\n"
    "v_add_u32 v44, s40, v44\n"
    "v_lshl_add_u32 v45, s42, 2, v44\n"
    "v_xor_b32 v51, v0, v86\n"
    "v_and_b32 v86, 0x70, v86\n"
    "v_bfi_b32 v86, 48, v51, v86\n"
    "v_xor_b32 v51, 64, v86\n"
    "v_add_u32 v48, 3072, v86\n"
    "v_add_u32 v49, 2048, v51\n"
    "v_add_u32 v50, 1024, v86\n"
    "s_sub_u32 s36, s8, 3072\n"
    "s_subb_u32 s37, s9, 0\n"
    "s_add_u32 m0, s35, 49152\n"
    "s_nop 0\n"
    "global_load_lds_dwordx4 v46, s[30:31] offset:0\n"
    "global_load_lds_dwordx4 v47, s[30:31] offset:1024\n"
    "s_add_u32 s30, s30, 212992\n"
    "s_addc_u32 s31, s31, 0\n"
    "s_add_u32 m0, s35, 98304\n"
    "s_nop 0\n"
    "global_load_lds_dwordx4 v46, s[30:31] offset:0\n"
    "global_load_lds_dwordx4 v47, s[30:31] offset:1024\n"
    "s_add_u32 s66, s28, 1\n"
    "s_and_b32 s66, s66, 7\n"
    "s_mul_i32 s66, s66, 638976\n"
    "s_add_u32 s30, s6, s66\n"
    "s_addc_u32 s31, s7, 0\n"
    "s_waitcnt vmcnt(6)\n"
    "ds_read2_b32 v[96:97], v45 offset0:0 offset1:1\n"
    "ds_read_b32 v98, v45 offset:8\n"
    "s_lshr_b32 s20, s43, 6\n"
    "s_lshr_b32 s21, s43, 8\n"
    "v_and_b32 v1, 63, v0\n"
    "s_waitcnt lgkmcnt(0)\n"
    "v_lshlrev_b32 v96, 7, v96\n"
    "v_lshlrev_b32 v97, 7, v97\n"
    "v_lshlrev_b32 v98, 7, v98\n"
    "v_add_u32_dpp v52, v96, v48 quad_perm:[0,0,0,0] row_mask:0xf bank_mask:0xf\n"
    "v_add_u32_dpp v53, v96, v49 quad_perm:[1,1,1,1] row_mask:0xf bank_mask:0xf\n"
    "v_add_u32_dpp v54, v96, v50 quad_perm:[2,2,2,2] row_mask:0xf bank_mask:0xf\n"
    "v_add_u32_dpp v55, v96, v51 quad_perm:[3,3,3,3] row_mask:0xf bank_mask:0xf\n"
    "s_add_u32 m0, s34, 0\n"
    "s_nop 0\n"
    "global_load_lds_dwordx4 v52, s[36:37] offset:0\n"
    "global_load_lds_dwordx4 v53, s[36:37] offset:1024\n"
    "global_load_lds_dwordx4 v54, s[36:37] offset:2048\n"
    "global_load_lds_dwordx4 v55, s[36:37] offset:3072\n"
    "s_setprio 1\n"
    "v_add_u32_dpp v52, v97, v48 quad_perm:[0,0,0,0] row_mask:0xf bank_mask:0xf\n"
    "v_add_u32_dpp v53, v97, v49 quad_perm:[1,1,1,1] row_mask:0xf bank_mask:0xf\n"
    "v_add_u32_dpp v54, v97, v50 quad_perm:[2,2,2,2] row_mask:0xf bank_mask:0xf\n"
    "v_add_u32_dpp v55, v97, v51 quad_perm:[3,3,3,3] row_mask:0xf bank_mask:0xf\n"
    "s_load_dwordx4 s[68:71], s[0:1], 64\n"
    "s_add_u32 s40, s28, 1\n"
    "s_and_b32 s40, s40, 7\n"
    "v_mad_u32_u24 v45, s40, 12, v44\n"
    "ds_read2_b32 v[100:101], v45 offset0:1 offset1:2\n"
    "ds_read_b32 v99, v45 offset:0\n"
    "s_add_u32 s40, s28, 2\n"
    "s_and_b32 s40, s40, 7\n"
    "v_mad_u32_u24 v45, s40, 12, v44\n"
    "ds_read2_b32 v[102:103], v45 offset0:0 offset1:1\n"
    "ds_read_b32 v104, v45 offset:8\n"
    "s_add_u32 s40, s28, 3\n"
    "s_and_b32 s40, s40, 7\n"
    "v_mad_u32_u24 v45, s40, 12, v44\n"
    "ds_read2_b32 v[106:107], v45 offset0:1 offset1:2\n"
    "ds_read_b32 v105, v45 offset:0\n"
    "s_add_u32 s40, s28, 4\n"
    "s_and_b32 s40, s40, 7\n"
    "v_mad_u32_u24 v45, s40, 12, v44\n"
    "ds_read2_b32 v[108:109], v45 offset0:0 offset1:1\n"
    "ds_read_b32 v110, v45 offset:8\n"
    "s_add_u32 s40, s28, 5\n"
    "s_and_b32 s40, s40, 7\n"
    "v_mad_u32_u24 v45, s40, 12, v44\n"
    "ds_read2_b32 v[112:113], v45 offset0:1 offset1:2\n"
    "ds_read_b32 v111, v45 offset:0\n"
    "s_add_u32 s40, s28, 6\n"
    "s_and_b32 s40, s40, 7\n"
    "v_mad_u32_u24 v45, s40, 12, v44\n"
    "ds_read2_b32 v[114:115], v45 offset0:0 offset1:1\n"
    "ds_read_b32 v116, v45 offset:8\n"
    "s_add_u32 s40, s28, 7\n"
    "s_and_b32 s40, s40, 7\n"
    "v_mad_u32_u24 v45, s40, 12, v44\n"
    "ds_read2_b32 v[118:119], v45 offset0:1 offset1:2\n"
    "ds_read_b32 v117, v45 offset:0\n"
    "ds_read_b64 v[120:121], v44 offset:96\n"
    "v_lshrrev_b32 v86, 5, v88\n"
    "v_xor_b32 v86, v86, v85\n"
    "v_lshlrev_b32 v86, 4, v86\n"
    "v_lshl_add_u32 v86, v88, 3, v86\n"
    "s_and_b32 s41, s20, 1\n"
    "s_lshl1_add_u32 s40, s21, s41\n"
    "v_lshl_add_u32 v56, s40, 13, v86\n"
    "v_xor_b32 v57, 64, v56\n"
    "v_lshrrev_b32 v86, 6, v88\n"
    "v_lshl_add_u32 v88, v85, 3, v86\n"
    "v_bfe_u32 v86, v0, 2, 3\n"
    "s_and_b32 s40, s20, 2\n"
    "s_lshl1_add_u32 s40, s40, 1024\n"
    "v_xor_b32 v86, s40, v86\n"
    "v_lshlrev_b32 v86, 5, v86\n"
    "v_lshl_add_u32 v86, v88, 8, v86\n"
    "v_lshl_add_u32 v76, v84, 3, v86\n"
    "v_xor_b32 v77, 32, v76\n"
    "v_xor_b32 v78, 64, v76\n"
    "v_xor_b32 v79, 96, v76\n"
    "v_lshlrev_b32 v92, 2, v0\n"
    "v_add_u32_e64 v93, s21, -1\n"
    "v_add_u32 v92, 149504, v92\n"
    "ds_write_b32 v92, v93\n"
    "s_waitcnt lgkmcnt(0)\n"
    "v_lshlrev_b32 v99, 7, v99\n"
    "s_setprio 0\n"
    "s_cmp_eq_u32 s21, 0\n"
    "s_cbranch_scc1 .Lxd_g0\n"
    "s_waitcnt vmcnt(8)\n"
    "s_barrier\n"
    ".Lxd_g0:\n"
    "s_add_u32 m0, s34, 49152\n"
    "s_nop 0\n"
    "global_load_lds_dwordx4 v52, s[36:37] offset:0\n"
    "global_load_lds_dwordx4 v53, s[36:37] offset:1024\n"
    "global_load_lds_dwordx4 v54, s[36:37] offset:2048\n"
    "global_load_lds_dwordx4 v55, s[36:37] offset:3072\n"
    "v_add_u32_dpp v52, v98, v48 quad_perm:[0,0,0,0] row_mask:0xf bank_mask:0xf\n"
    "v_add_u32_dpp v53, v98, v49 quad_perm:[1,1,1,1] row_mask:0xf bank_mask:0xf\n"
    "v_add_u32_dpp v54, v98, v50 quad_perm:[2,2,2,2] row_mask:0xf bank_mask:0xf\n"
    "v_add_u32_dpp v55, v98, v51 quad_perm:[3,3,3,3] row_mask:0xf bank_mask:0xf\n"
    "s_waitcnt vmcnt(4) lgkmcnt(0)\n"
    "s_barrier\n"
    "ds_read_b128 v[128:131], v56 offset:0\n"
    "ds_read_b128 v[132:135], v56 offset:2048\n"
    "ds_read_b128 v[136:139], v56 offset:4096\n"
    "ds_read_b128 v[140:143], v56 offset:6144\n"
    "ds_read_b64_tr_b16 v[144:145], v76 offset:0\n"
    "ds_read_b64_tr_b16 v[146:147], v76 offset:1024\n"
    "ds_read_b64_tr_b16 v[148:149], v77 offset:0\n"
    "ds_read_b64_tr_b16 v[150:151], v77 offset:1024\n"
    "ds_read_b64_tr_b16 v[152:153], v78 offset:0\n"
    "ds_read_b64_tr_b16 v[154:155], v78 offset:1024\n"
    "ds_read_b64_tr_b16 v[156:157], v79 offset:0\n"
    "ds_read_b64_tr_b16 v[158:159], v79 offset:1024\n"
    "ds_read_b128 v[160:163], v57 offset:0\n"
    "ds_read_b128 v[164:167], v57 offset:2048\n"
    "ds_read_b128 v[168:171], v57 offset:4096\n"
    "ds_read_b128 v[172:175], v57 offset:6144\n"
    "ds_read_b64_tr_b16 v[176:177], v76 offset:8192\n"
    "ds_read_b64_tr_b16 v[178:179], v76 offset:9216\n"
    "ds_read_b64_tr_b16 v[180:181], v77 offset:8192\n"
    "ds_read_b64_tr_b16 v[182:183], v77 offset:9216\n"
    "ds_read_b64_tr_b16 v[184:185], v78 offset:8192\n"
    "ds_read_b64_tr_b16 v[186:187], v78 offset:9216\n"
    "ds_read_b64_tr_b16 v[188:189], v79 offset:8192\n"
    "ds_read_b64_tr_b16 v[190:191], v79 offset:9216\n"
    "s_add_u32 m0, s34, 98304\n"
    "s_nop 0\n"
    "global_load_lds_dwordx4 v52, s[36:37] offset:0\n"
    "global_load_lds_dwordx4 v53, s[36:37] offset:1024\n"
    "global_load_lds_dwordx4 v54, s[36:37] offset:2048\n"
    "global_load_lds_dwordx4 v55, s[36:37] offset:3072\n"
    "s_waitcnt lgkmcnt(0)\n"
    "s_barrier\n"
    "v_mfma_f32_16x16x32_bf16 v[192:195], v[144:147], v[128:131], 0\n"
    "v_lshlrev_b32 v100, 7, v100\n"
    "v_mfma_f32_16x16x32_bf16 v[192:195], v[176:179], v[160:163], v[192:195]\n"
    "v_add_u32_dpp v52, v99, v48 quad_perm:[0,0,0,0] row_mask:0xf bank_mask:0xf\n"
    "v_mfma_f32_16x16x32_bf16 v[196:199], v[176:179], v[164:167], 0\n"
    "v_mfma_f32_16x16x32_bf16 v[196:199], v[144:147], v[132:135], v[196:199]\n"
    "v_add_u32_dpp v53, v99, v49 quad_perm:[1,1,1,1] row_mask:0xf bank_mask:0xf\n"
    "v_mfma_f32_16x16x32_bf16 v[200:203], v[144:147], v[136:139], 0\n"
    "v_mfma_f32_16x16x32_bf16 v[200:203], v[176:179], v[168:171], v[200:203]\n"
    "v_add_u32_dpp v54, v99, v50 quad_perm:[2,2,2,2] row_mask:0xf bank_mask:0xf\n"
    "v_mfma_f32_16x16x32_bf16 v[204:207], v[176:179], v[172:175], 0\n"
    "v_mfma_f32_16x16x32_bf16 v[204:207], v[144:147], v[140:143], v[204:207]\n"
    "v_add_u32_dpp v55, v99, v51 quad_perm:[3,3,3,3] row_mask:0xf bank_mask:0xf\n"
    "v_mfma_f32_16x16x32_bf16 v[220:223], v[148:151], v[140:143], 0\n"
    "v_mfma_f32_16x16x32_bf16 v[220:223], v[180:183], v[172:175], v[220:223]\n"
    "s_bfe_u32 s40, s34, 0x1000d\n"
    "v_mfma_f32_16x16x32_bf16 v[216:219], v[180:183], v[168:171], 0\n"
    "v_mfma_f32_16x16x32_bf16 v[216:219], v[148:151], v[136:139], v[216:219]\n"
    "s_lshl1_add_u32 s40, s23, s40\n"
    "v_mfma_f32_16x16x32_bf16 v[212:215], v[148:151], v[132:135], 0\n"
    "v_mfma_f32_16x16x32_bf16 v[212:215], v[180:183], v[164:167], v[212:215]\n"
    "s_lshl_b32 s40, s40, 7\n"
    "v_mfma_f32_16x16x32_bf16 v[208:211], v[180:183], v[160:163], 0\n"
    "v_mfma_f32_16x16x32_bf16 v[208:211], v[148:151], v[128:131], v[208:211]\n"
    "v_lshl_add_u32 v42, v85, 3, s40\n"
    "v_mfma_f32_16x16x32_bf16 v[224:227], v[152:155], v[128:131], 0\n"
    "v_mfma_f32_16x16x32_bf16 v[224:227], v[184:187], v[160:163], v[224:227]\n"
    "s_lshl_b32 s24, s24, 8\n"
    "v_mfma_f32_16x16x32_bf16 v[228:231], v[184:187], v[164:167], 0\n"
    "v_mfma_f32_16x16x32_bf16 v[228:231], v[152:155], v[132:135], v[228:231]\n"
    "v_add_u32 v58, 98304, v56\n"
    "v_mfma_f32_16x16x32_bf16 v[232:235], v[152:155], v[136:139], 0\n"
    "v_mfma_f32_16x16x32_bf16 v[232:235], v[184:187], v[168:171], v[232:235]\n"
    "v_add_u32 v59, 98304, v57\n"
    "v_mfma_f32_16x16x32_bf16 v[236:239], v[184:187], v[172:175], 0\n"
    "v_mfma_f32_16x16x32_bf16 v[236:239], v[152:155], v[140:143], v[236:239]\n"
    "v_add_u32 v80, 98304, v76\n"
    "v_mfma_f32_16x16x32_bf16 v[252:255], v[156:159], v[140:143], 0\n"
    "v_mfma_f32_16x16x32_bf16 v[252:255], v[188:191], v[172:175], v[252:255]\n"
    "v_add_u32 v81, 98304, v77\n"
    "v_mfma_f32_16x16x32_bf16 v[248:251], v[188:191], v[168:171], 0\n"
    "v_mfma_f32_16x16x32_bf16 v[248:251], v[156:159], v[136:139], v[248:251]\n"
    "v_add_u32 v82, 98304, v78\n"
    "v_mfma_f32_16x16x32_bf16 v[244:247], v[156:159], v[132:135], 0\n"
    "v_mfma_f32_16x16x32_bf16 v[244:247], v[188:191], v[164:167], v[244:247]\n"
    "v_add_u32 v83, 98304, v79\n"
    "v_mfma_f32_16x16x32_bf16 v[240:243], v[188:191], v[160:163], 0\n"
    "v_mfma_f32_16x16x32_bf16 v[240:243], v[156:159], v[128:131], v[240:243]\n"
    "s_waitcnt vmcnt(4)\n"
    "s_barrier\n"
    "s_add_u32 m0, s35, 0\n"
    "s_nop 0\n"
    "global_load_lds_dwordx4 v46, s[30:31] offset:0\n"
    "ds_read_b128 v[128:131], v56 offset:49152\n"
    "ds_read_b128 v[132:135], v56 offset:51200\n"
    "ds_read_b128 v[136:139], v56 offset:53248\n"
    "ds_read_b128 v[140:143], v56 offset:55296\n"
    "global_load_lds_dwordx4 v47, s[30:31] offset:1024\n"
    "s_add_u32 s30, s30, 212992\n"
    "s_addc_u32 s31, s31, 0\n"
    "ds_read_b64_tr_b16 v[144:145], v76 offset:49152\n"
    "ds_read_b64_tr_b16 v[146:147], v76 offset:50176\n"
    "ds_read_b64_tr_b16 v[148:149], v77 offset:49152\n"
    "ds_read_b64_tr_b16 v[150:151], v77 offset:50176\n"
    "s_add_u32 m0, s34, 0\n"
    "s_nop 0\n"
    "global_load_lds_dwordx4 v52, s[36:37] offset:0\n"
    "ds_read_b64_tr_b16 v[152:153], v78 offset:49152\n"
    "ds_read_b64_tr_b16 v[154:155], v78 offset:50176\n"
    "ds_read_b64_tr_b16 v[156:157], v79 offset:49152\n"
    "ds_read_b64_tr_b16 v[158:159], v79 offset:50176\n"
    "global_load_lds_dwordx4 v53, s[36:37] offset:1024\n"
    "ds_read_b128 v[160:163], v57 offset:49152\n"
    "ds_read_b128 v[164:167], v57 offset:51200\n"
    "ds_read_b128 v[168:171], v57 offset:53248\n"
    "ds_read_b128 v[172:175], v57 offset:55296\n"
    "global_load_lds_dwordx4 v54, s[36:37] offset:2048\n"
    "ds_read_b64_tr_b16 v[176:177], v76 offset:57344\n"
    "ds_read_b64_tr_b16 v[178:179], v76 offset:58368\n"
    "ds_read_b64_tr_b16 v[180:181], v77 offset:57344\n"
    "ds_read_b64_tr_b16 v[182:183], v77 offset:58368\n"
    "global_load_lds_dwordx4 v55, s[36:37] offset:3072\n"
    "ds_read_b64_tr_b16 v[184:185], v78 offset:57344\n"
    "ds_read_b64_tr_b16 v[186:187], v78 offset:58368\n"
    "ds_read_b64_tr_b16 v[188:189], v79 offset:57344\n"
    "ds_read_b64_tr_b16 v[190:191], v79 offset:58368\n"
    "s_waitcnt lgkmcnt(0)\n"
    "s_barrier\n"
    "v_mfma_f32_16x16x32_bf16 v[192:195], v[144:147], v[128:131], v[192:195]\n"
    "v_lshlrev_b32 v101, 7, v101\n"
    "v_mfma_f32_16x16x32_bf16 v[192:195], v[176:179], v[160:163], v[192:195]\n"
    "v_mfma_f32_16x16x32_bf16 v[196:199], v[176:179], v[164:167], v[196:199]\n"
    "v_add_u32_dpp v52, v100, v48 quad_perm:[0,0,0,0] row_mask:0xf bank_mask:0xf\n"
    "v_mfma_f32_16x16x32_bf16 v[196:199], v[144:147], v[132:135], v[196:199]\n"
    "v_mfma_f32_16x16x32_bf16 v[200:203], v[144:147], v[136:139], v[200:203]\n"
    "v_add_u32_dpp v53, v100, v49 quad_perm:[1,1,1,1] row_mask:0xf bank_mask:0xf\n"
    "v_mfma_f32_16x16x32_bf16 v[200:203], v[176:179], v[168:171], v[200:203]\n"
    "v_mfma_f32_16x16x32_bf16 v[204:207], v[176:179], v[172:175], v[204:207]\n"
    "v_add_u32_dpp v54, v100, v50 quad_perm:[2,2,2,2] row_mask:0xf bank_mask:0xf\n"
    "v_mfma_f32_16x16x32_bf16 v[204:207], v[144:147], v[140:143], v[204:207]\n"
    "v_mfma_f32_16x16x32_bf16 v[220:223], v[148:151], v[140:143], v[220:223]\n"
    "v_add_u32_dpp v55, v100, v51 quad_perm:[3,3,3,3] row_mask:0xf bank_mask:0xf\n"
    "v_mfma_f32_16x16x32_bf16 v[220:223], v[180:183], v[172:175], v[220:223]\n"
    "v_mfma_f32_16x16x32_bf16 v[216:219], v[180:183], v[168:171], v[216:219]\n"
    "s_mul_i32 s47, s3, 5042\n"
    "v_mfma_f32_16x16x32_bf16 v[216:219], v[148:151], v[136:139], v[216:219]\n"
    "v_mfma_f32_16x16x32_bf16 v[212:215], v[148:151], v[132:135], v[212:215]\n"
    "s_lshr_b32 s47, s47, 16\n"
    "v_mfma_f32_16x16x32_bf16 v[212:215], v[180:183], v[164:167], v[212:215]\n"
    "v_mfma_f32_16x16x32_bf16 v[208:211], v[180:183], v[160:163], v[208:211]\n"
    "s_lshr_b32 s21, s20, 2\n"
    "v_mfma_f32_16x16x32_bf16 v[208:211], v[148:151], v[128:131], v[208:211]\n"
    "v_mfma_f32_16x16x32_bf16 v[224:227], v[152:155], v[128:131], v[224:227]\n"
    "s_lshl1_add_u32 s22, s21, s20\n"
    "v_mfma_f32_16x16x32_bf16 v[224:227], v[184:187], v[160:163], v[224:227]\n"
    "v_mfma_f32_16x16x32_bf16 v[228:231], v[184:187], v[164:167], v[228:231]\n"
    "s_and_b32 s22, s22, 3\n"
    "v_mfma_f32_16x16x32_bf16 v[228:231], v[152:155], v[132:135], v[228:231]\n"
    "v_mfma_f32_16x16x32_bf16 v[232:235], v[152:155], v[136:139], v[232:235]\n"
    "s_lshl_b32 s58, s21, 8\n"
    "v_mfma_f32_16x16x32_bf16 v[232:235], v[184:187], v[168:171], v[232:235]\n"
    "v_mfma_f32_16x16x32_bf16 v[236:239], v[184:187], v[172:175], v[236:239]\n"
    "s_add_u32 s58, s58, 150528\n"
    "v_mfma_f32_16x16x32_bf16 v[236:239], v[152:155], v[140:143], v[236:239]\n"
    "v_mfma_f32_16x16x32_bf16 v[252:255], v[156:159], v[140:143], v[252:255]\n"
    "s_mov_b32 s40, s47\n"
    "v_mfma_f32_16x16x32_bf16 v[252:255], v[188:191], v[172:175], v[252:255]\n"
    "v_mfma_f32_16x16x32_bf16 v[248:251], v[188:191], v[168:171], v[248:251]\n"
    "s_mul_i32 s54, s40, 512\n"
    "v_mfma_f32_16x16x32_bf16 v[248:251], v[156:159], v[136:139], v[248:251]\n"
    "v_mfma_f32_16x16x32_bf16 v[244:247], v[156:159], v[132:135], v[244:247]\n"
    "s_add_u32 s54, s54, 2\n"
    "v_mfma_f32_16x16x32_bf16 v[244:247], v[188:191], v[164:167], v[244:247]\n"
    "v_mfma_f32_16x16x32_bf16 v[240:243], v[188:191], v[160:163], v[240:243]\n"
    "v_mfma_f32_16x16x32_bf16 v[240:243], v[156:159], v[128:131], v[240:243]\n"
    "s_waitcnt vmcnt(6)\n"
    "s_barrier\n"
    "s_add_u32 m0, s35, 49152\n"
    "s_nop 0\n"
    "global_load_lds_dwordx4 v46, s[30:31] offset:0\n"
    "ds_read_b128 v[128:131], v58 offset:0\n"
    "ds_read_b128 v[132:135], v58 offset:2048\n"
    "ds_read_b128 v[136:139], v58 offset:4096\n"
    "ds_read_b128 v[140:143], v58 offset:6144\n"
    "global_load_lds_dwordx4 v47, s[30:31] offset:1024\n"
    "s_add_u32 s30, s30, 212992\n"
    "s_addc_u32 s31, s31, 0\n"
    "ds_read_b64_tr_b16 v[144:145], v80 offset:0\n"
    "ds_read_b64_tr_b16 v[146:147], v80 offset:1024\n"
    "ds_read_b64_tr_b16 v[148:149], v81 offset:0\n"
    "ds_read_b64_tr_b16 v[150:151], v81 offset:1024\n"
    "s_add_u32 m0, s34, 49152\n"
    "s_nop 0\n"
    "global_load_lds_dwordx4 v52, s[36:37] offset:0\n"
    "ds_read_b64_tr_b16 v[152:153], v82 offset:0\n"
    "ds_read_b64_tr_b16 v[154:155], v82 offset:1024\n"
    "ds_read_b64_tr_b16 v[156:157], v83 offset:0\n"
    "ds_read_b64_tr_b16 v[158:159], v83 offset:1024\n"
    "global_load_lds_dwordx4 v53, s[36:37] offset:1024\n"
    "ds_read_b128 v[160:163], v59 offset:0\n"
    "ds_read_b128 v[164:167], v59 offset:2048\n"
    "ds_read_b128 v[168:171], v59 offset:4096\n"
    "ds_read_b128 v[172:175], v59 offset:6144\n"
    "global_load_lds_dwordx4 v54, s[36:37] offset:2048\n"
    "ds_read_b64_tr_b16 v[176:177], v80 offset:8192\n"
    "ds_read_b64_tr_b16 v[178:179], v80 offset:9216\n"
    "ds_read_b64_tr_b16 v[180:181], v81 offset:8192\n"
    "ds_read_b64_tr_b16 v[182:183], v81 offset:9216\n"
    "global_load_lds_dwordx4 v55, s[36:37] offset:3072\n"
    "ds_read_b64_tr_b16 v[184:185], v82 offset:8192\n"
    "ds_read_b64_tr_b16 v[186:187], v82 offset:9216\n"
    "ds_read_b64_tr_b16 v[188:189], v83 offset:8192\n"
    "ds_read_b64_tr_b16 v[190:191], v83 offset:9216\n"
    "s_waitcnt vmcnt(10) lgkmcnt(0)\n"
    "s_barrier\n"
    "v_mfma_f32_16x16x32_bf16 v[192:195], v[144:147], v[128:131], v[192:195]\n"
    "v_lshlrev_b32 v102, 7, v102\n"
    "v_mfma_f32_16x16x32_bf16 v[192:195], v[176:179], v[160:163], v[192:195]\n"
    "v_mfma_f32_16x16x32_bf16 v[196:199], v[176:179], v[164:167], v[196:199]\n"
    "v_add_u32_dpp v52, v101, v48 quad_perm:[0,0,0,0] row_mask:0xf bank_mask:0xf\n"
    "v_mfma_f32_16x16x32_bf16 v[196:199], v[144:147], v[132:135], v[196:199]\n"
    "v_mfma_f32_16x16x32_bf16 v[200:203], v[144:147], v[136:139], v[200:203]\n"
    "v_add_u32_dpp v53, v101, v49 quad_perm:[1,1,1,1] row_mask:0xf bank_mask:0xf\n"
    "v_mfma_f32_16x16x32_bf16 v[200:203], v[176:179], v[168:171], v[200:203]\n"
    "v_mfma_f32_16x16x32_bf16 v[204:207], v[176:179], v[172:175], v[204:207]\n"
    "v_add_u32_dpp v54, v101, v50 quad_perm:[2,2,2,2] row_mask:0xf bank_mask:0xf\n"
    "v_mfma_f32_16x16x32_bf16 v[204:207], v[144:147], v[140:143], v[204:207]\n"
    "v_mfma_f32_16x16x32_bf16 v[220:223], v[148:151], v[140:143], v[220:223]\n"
    "v_add_u32_dpp v55, v101, v51 quad_perm:[3,3,3,3] row_mask:0xf bank_mask:0xf\n"
    "v_mfma_f32_16x16x32_bf16 v[220:223], v[180:183], v[172:175], v[220:223]\n"
    "v_mfma_f32_16x16x32_bf16 v[216:219], v[180:183], v[168:171], v[216:219]\n"
    "s_lshl_b32 s40, s40, 7\n"
    "v_mfma_f32_16x16x32_bf16 v[216:219], v[148:151], v[136:139], v[216:219]\n"
    "v_mfma_f32_16x16x32_bf16 v[212:215], v[148:151], v[132:135], v[212:215]\n"
    "s_add_u32 s40, s40, 32768\n"
    "v_mfma_f32_16x16x32_bf16 v[212:215], v[180:183], v[164:167], v[212:215]\n"
    "v_mfma_f32_16x16x32_bf16 v[208:211], v[180:183], v[160:163], v[208:211]\n"
    "s_add_u32 s52, s16, s40\n"
    "s_addc_u32 s53, s17, 0\n"
    "v_mfma_f32_16x16x32_bf16 v[208:211], v[148:151], v[128:131], v[208:211]\n"
    "v_mfma_f32_16x16x32_bf16 v[224:227], v[152:155], v[128:131], v[224:227]\n"
    "s_mov_b32 s57, 0x7fff7fff\n"
    "v_mfma_f32_16x16x32_bf16 v[224:227], v[184:187], v[160:163], v[224:227]\n"
    "v_mfma_f32_16x16x32_bf16 v[228:231], v[184:187], v[164:167], v[228:231]\n"
    "s_mov_b32 s56, 0\n"
    "v_mfma_f32_16x16x32_bf16 v[228:231], v[152:155], v[132:135], v[228:231]\n"
    "v_mfma_f32_16x16x32_bf16 v[232:235], v[152:155], v[136:139], v[232:235]\n"
    "s_mov_b32 s27, 0\n"
    "v_mfma_f32_16x16x32_bf16 v[232:235], v[184:187], v[168:171], v[232:235]\n"
    "v_mfma_f32_16x16x32_bf16 v[236:239], v[184:187], v[172:175], v[236:239]\n"
    "s_mov_b32 s55, 9\n"
    "v_mfma_f32_16x16x32_bf16 v[236:239], v[152:155], v[140:143], v[236:239]\n"
    "v_mfma_f32_16x16x32_bf16 v[252:255], v[156:159], v[140:143], v[252:255]\n"
    "s_mov_b32 s44, 0\n"
    "v_mfma_f32_16x16x32_bf16 v[252:255], v[188:191], v[172:175], v[252:255]\n"
    "v_mfma_f32_16x16x32_bf16 v[248:251], v[188:191], v[168:171], v[248:251]\n"
    "s_mov_b32 s46, 0\n"
    "v_mfma_f32_16x16x32_bf16 v[248:251], v[156:159], v[136:139], v[248:251]\n"
    "v_mfma_f32_16x16x32_bf16 v[244:247], v[156:159], v[132:135], v[244:247]\n"
    "v_lshrrev_b32 v17, 3, v1\n"
    "v_mfma_f32_16x16x32_bf16 v[244:247], v[188:191], v[164:167], v[244:247]\n"
    "v_mfma_f32_16x16x32_bf16 v[240:243], v[188:191], v[160:163], v[240:243]\n"
    "v_mfma_f32_16x16x32_bf16 v[240:243], v[156:159], v[128:131], v[240:243]\n"
    "s_waitcnt vmcnt(6)\n"
    "s_barrier\n"
    "s_add_u32 m0, s35, 98304\n"
    "s_nop 0\n"
    "global_load_lds_dwordx4 v46, s[30:31] offset:0\n"
    "ds_read_b128 v[128:131], v56 offset:0\n"
    "ds_read_b128 v[132:135], v56 offset:2048\n"
    "ds_read_b128 v[136:139], v56 offset:4096\n"
    "ds_read_b128 v[140:143], v56 offset:6144\n"
    "global_load_lds_dwordx4 v47, s[30:31] offset:1024\n"
    "s_add_u32 s66, s28, 2\n"
    "s_and_b32 s66, s66, 7\n"
    "s_mul_i32 s66, s66, 638976\n"
    "s_add_u32 s30, s6, s66\n"
    "s_addc_u32 s31, s7, 0\n"
    "ds_read_b64_tr_b16 v[144:145], v76 offset:0\n"
    "ds_read_b64_tr_b16 v[146:147], v76 offset:1024\n"
    "ds_read_b64_tr_b16 v[148:149], v77 offset:0\n"
    "ds_read_b64_tr_b16 v[150:151], v77 offset:1024\n"
    "s_add_u32 m0, s34, 98304\n"
    "s_nop 0\n"
    "global_load_lds_dwordx4 v52, s[36:37] offset:0\n"
    "ds_read_b64_tr_b16 v[152:153], v78 offset:0\n"
    "ds_read_b64_tr_b16 v[154:155], v78 offset:1024\n"
    "ds_read_b64_tr_b16 v[156:157], v79 offset:0\n"
    "ds_read_b64_tr_b16 v[158:159], v79 offset:1024\n"
    "global_load_lds_dwordx4 v53, s[36:37] offset:1024\n"
    "ds_read_b128 v[160:163], v57 offset:0\n"
    "ds_read_b128 v[164:167], v57 offset:2048\n"
    "ds_read_b128 v[168:171], v57 offset:4096\n"
    "ds_read_b128 v[172:175], v57 offset:6144\n"
    "global_load_lds_dwordx4 v54, s[36:37] offset:2048\n"
    "ds_read_b64_tr_b16 v[176:177], v76 offset:8192\n"
    "ds_read_b64_tr_b16 v[178:179], v76 offset:9216\n"
    "ds_read_b64_tr_b16 v[180:181], v77 offset:8192\n"
    "ds_read_b64_tr_b16 v[182:183], v77 offset:9216\n"
    "global_load_lds_dwordx4 v55, s[36:37] offset:3072\n"
    "ds_read_b64_tr_b16 v[184:185], v78 offset:8192\n"
    "ds_read_b64_tr_b16 v[186:187], v78 offset:9216\n"
    "ds_read_b64_tr_b16 v[188:189], v79 offset:8192\n"
    "ds_read_b64_tr_b16 v[190:191], v79 offset:9216\n"
    "s_waitcnt vmcnt(10) lgkmcnt(0)\n"
    "s_barrier\n"
    "v_mfma_f32_16x16x32_bf16 v[192:195], v[144:147], v[128:131], v[192:195]\n"
    "v_lshlrev_b32 v103, 7, v103\n"
    "v_mfma_f32_16x16x32_bf16 v[192:195], v[176:179], v[160:163], v[192:195]\n"
    "v_add_u32_dpp v52, v102, v48 quad_perm:[0,0,0,0] row_mask:0xf bank_mask:0xf\n"
    "v_mfma_f32_16x16x32_bf16 v[196:199], v[176:179], v[164:167], v[196:199]\n"
    "v_mfma_f32_16x16x32_bf16 v[196:199], v[144:147], v[132:135], v[196:199]\n"
    "v_add_u32_dpp v53, v102, v49 quad_perm:[1,1,1,1] row_mask:0xf bank_mask:0xf\n"
    "v_mfma_f32_16x16x32_bf16 v[200:203], v[144:147], v[136:139], v[200:203]\n"
    "v_mfma_f32_16x16x32_bf16 v[200:203], v[176:179], v[168:171], v[200:203]\n"
    "v_add_u32_dpp v54, v102, v50 quad_perm:[2,2,2,2] row_mask:0xf bank_mask:0xf\n"
    "v_mfma_f32_16x16x32_bf16 v[204:207], v[176:179], v[172:175], v[204:207]\n"
    "v_mfma_f32_16x16x32_bf16 v[204:207], v[144:147], v[140:143], v[204:207]\n"
    "v_add_u32_dpp v55, v102, v51 quad_perm:[3,3,3,3] row_mask:0xf bank_mask:0xf\n"
    "v_mfma_f32_16x16x32_bf16 v[220:223], v[148:151], v[140:143], v[220:223]\n"
    "v_mfma_f32_16x16x32_bf16 v[220:223], v[180:183], v[172:175], v[220:223]\n"
    "v_mul_u32_u24 v17, 12, v17\n"
    "v_mfma_f32_16x16x32_bf16 v[216:219], v[180:183], v[168:171], v[216:219]\n"
    "v_mfma_f32_16x16x32_bf16 v[216:219], v[148:151], v[136:139], v[216:219]\n"
    "v_and_b32 v19, 7, v1\n"
    "v_mfma_f32_16x16x32_bf16 v[212:215], v[148:151], v[132:135], v[212:215]\n"
    "v_mfma_f32_16x16x32_bf16 v[212:215], v[180:183], v[164:167], v[212:215]\n"
    "v_lshlrev_b32 v19, 4, v19\n"
    "v_mfma_f32_16x16x32_bf16 v[208:211], v[180:183], v[160:163], v[208:211]\n"
    "v_mfma_f32_16x16x32_bf16 v[208:211], v[148:151], v[128:131], v[208:211]\n"
    "v_lshrrev_b32 v18, 5, v1\n"
    "v_mfma_f32_16x16x32_bf16 v[224:227], v[152:155], v[128:131], v[224:227]\n"
    "v_mfma_f32_16x16x32_bf16 v[224:227], v[184:187], v[160:163], v[224:227]\n"
    "v_lshlrev_b32 v18, 2, v18\n"
    "v_mfma_f32_16x16x32_bf16 v[228:231], v[184:187], v[164:167], v[228:231]\n"
    "v_mfma_f32_16x16x32_bf16 v[228:231], v[152:155], v[132:135], v[228:231]\n"
    "v_and_b32 v20, 31, v1\n"
    "v_mfma_f32_16x16x32_bf16 v[232:235], v[152:155], v[136:139], v[232:235]\n"
    "v_mfma_f32_16x16x32_bf16 v[232:235], v[184:187], v[168:171], v[232:235]\n"
    "v_lshlrev_b32 v20, 2, v20\n"
    "v_mfma_f32_16x16x32_bf16 v[236:239], v[184:187], v[172:175], v[236:239]\n"
    "v_mfma_f32_16x16x32_bf16 v[236:239], v[152:155], v[140:143], v[236:239]\n"
    "s_lshl2_add_u32 s40, s22, s58\n"
    "v_mfma_f32_16x16x32_bf16 v[252:255], v[156:159], v[140:143], v[252:255]\n"
    "v_mfma_f32_16x16x32_bf16 v[252:255], v[188:191], v[172:175], v[252:255]\n"
    "s_sub_u32 s40, s40, 20\n"
    "v_mfma_f32_16x16x32_bf16 v[248:251], v[188:191], v[168:171], v[248:251]\n"
    "v_mfma_f32_16x16x32_bf16 v[248:251], v[156:159], v[136:139], v[248:251]\n"
    "v_lshl_add_u32 v25, v1, 18, s40\n"
    "v_mfma_f32_16x16x32_bf16 v[244:247], v[156:159], v[132:135], v[244:247]\n"
    "v_mfma_f32_16x16x32_bf16 v[244:247], v[188:191], v[164:167], v[244:247]\n"
    "s_lshl_b32 s43, s21, 16\n"
    "v_mfma_f32_16x16x32_bf16 v[240:243], v[188:191], v[160:163], v[240:243]\n"
    "v_mfma_f32_16x16x32_bf16 v[240:243], v[156:159], v[128:131], v[240:243]\n"
    "s_waitcnt vmcnt(6)\n"
    "s_barrier\n"
    "s_add_u32 m0, s35, 0\n"
    "s_nop 0\n"
    "global_load_lds_dwordx4 v46, s[30:31] offset:0\n"
    "ds_read_b128 v[128:131], v56 offset:49152\n"
    "ds_read_b128 v[132:135], v56 offset:51200\n"
    "ds_read_b128 v[136:139], v56 offset:53248\n"
    "global_load_lds_dwordx4 v47, s[30:31] offset:1024\n"
    "s_add_u32 s30, s30, 212992\n"
    "s_addc_u32 s31, s31, 0\n"
    "ds_read_b128 v[140:143], v56 offset:55296\n"
    "ds_read_b64_tr_b16 v[144:145], v76 offset:49152\n"
    "ds_read_b64_tr_b16 v[146:147], v76 offset:50176\n"
    "s_add_u32 m0, s34, 0\n"
    "s_nop 0\n"
    "global_load_lds_dwordx4 v52, s[36:37] offset:0\n"
    "ds_read_b64_tr_b16 v[148:149], v77 offset:49152\n"
    "ds_read_b64_tr_b16 v[150:151], v77 offset:50176\n"
    "ds_read_b64_tr_b16 v[152:153], v78 offset:49152\n"
    "global_load_lds_dwordx4 v53, s[36:37] offset:1024\n"
    "ds_read_b64_tr_b16 v[154:155], v78 offset:50176\n"
    "ds_read_b64_tr_b16 v[156:157], v79 offset:49152\n"
    "ds_read_b64_tr_b16 v[158:159], v79 offset:50176\n"
    "global_load_lds_dwordx4 v54, s[36:37] offset:2048\n"
    "ds_read_b128 v[160:163], v57 offset:49152\n"
    "ds_read_b128 v[164:167], v57 offset:51200\n"
    "ds_read_b128 v[168:171], v57 offset:53248\n"
    "global_load_lds_dwordx4 v55, s[36:37] offset:3072\n"
    "ds_read_b128 v[172:175], v57 offset:55296\n"
    "ds_read_b64_tr_b16 v[176:177], v76 offset:57344\n"
    "ds_read_b64_tr_b16 v[178:179], v76 offset:58368\n"
    "global_load_dwordx2 v[84:85], v42, s[10:11] offset:0\n"
    "ds_read_b64_tr_b16 v[180:181], v77 offset:57344\n"
    "ds_read_b64_tr_b16 v[182:183], v77 offset:58368\n"
    "ds_read_b64_tr_b16 v[184:185], v78 offset:57344\n"
    "ds_read_b64_tr_b16 v[186:187], v78 offset:58368\n"
    "ds_read_b64_tr_b16 v[188:189], v79 offset:57344\n"
    "ds_read_b64_tr_b16 v[190:191], v79 offset:58368\n"
    "s_waitcnt vmcnt(11) lgkmcnt(0)\n"
    "s_barrier\n"
    "v_mfma_f32_16x16x32_bf16 v[192:195], v[144:147], v[128:131], v[192:195]\n"
    "v_lshlrev_b32 v104, 7, v104\n"
    "v_mfma_f32_16x16x32_bf16 v[192:195], v[176:179], v[160:163], v[192:195]\n"
    "v_mfma_f32_16x16x32_bf16 v[196:199], v[176:179], v[164:167], v[196:199]\n"
    "v_add_u32_dpp v52, v103, v48 quad_perm:[0,0,0,0] row_mask:0xf bank_mask:0xf\n"
    "v_mfma_f32_16x16x32_bf16 v[196:199], v[144:147], v[132:135], v[196:199]\n"
    "v_mfma_f32_16x16x32_bf16 v[200:203], v[144:147], v[136:139], v[200:203]\n"
    "v_add_u32_dpp v53, v103, v49 quad_perm:[1,1,1,1] row_mask:0xf bank_mask:0xf\n"
    "v_mfma_f32_16x16x32_bf16 v[200:203], v[176:179], v[168:171], v[200:203]\n"
    "v_mfma_f32_16x16x32_bf16 v[204:207], v[176:179], v[172:175], v[204:207]\n"
    "v_add_u32_dpp v54, v103, v50 quad_perm:[2,2,2,2] row_mask:0xf bank_mask:0xf\n"
    "v_mfma_f32_16x16x32_bf16 v[204:207], v[144:147], v[140:143], v[204:207]\n"
    "v_mfma_f32_16x16x32_bf16 v[220:223], v[148:151], v[140:143], v[220:223]\n"
    "v_add_u32_dpp v55, v103, v51 quad_perm:[3,3,3,3] row_mask:0xf bank_mask:0xf\n"
    "v_mfma_f32_16x16x32_bf16 v[220:223], v[180:183], v[172:175], v[220:223]\n"
    "v_mfma_f32_16x16x32_bf16 v[216:219], v[180:183], v[168:171], v[216:219]\n"
    "v_lshrrev_b32 v12, 4, v1\n"
    "v_mfma_f32_16x16x32_bf16 v[216:219], v[148:151], v[136:139], v[216:219]\n"
    "v_mfma_f32_16x16x32_bf16 v[212:215], v[148:151], v[132:135], v[212:215]\n"
    "v_bfe_u32 v13, v1, 2, 2\n"
    "v_mfma_f32_16x16x32_bf16 v[212:215], v[180:183], v[164:167], v[212:215]\n"
    "v_mfma_f32_16x16x32_bf16 v[208:211], v[180:183], v[160:163], v[208:211]\n"
    "v_and_b32 v21, 3, v1\n"
    "v_mfma_f32_16x16x32_bf16 v[208:211], v[148:151], v[128:131], v[208:211]\n"
    "v_mfma_f32_16x16x32_bf16 v[224:227], v[152:155], v[128:131], v[224:227]\n"
    "s_mul_i32 s40, s22, 0x2020\n"
    "v_mfma_f32_16x16x32_bf16 v[224:227], v[184:187], v[160:163], v[224:227]\n"
    "v_mfma_f32_16x16x32_bf16 v[228:231], v[184:187], v[164:167], v[228:231]\n"
    "s_and_b32 s40, s40, 0x4060\n"
    "v_mfma_f32_16x16x32_bf16 v[228:231], v[152:155], v[132:135], v[228:231]\n"
    "v_mfma_f32_16x16x32_bf16 v[232:235], v[152:155], v[136:139], v[232:235]\n"
    "s_add_u32 s40, s40, s43\n"
    "v_mfma_f32_16x16x32_bf16 v[232:235], v[184:187], v[168:171], v[232:235]\n"
    "v_mfma_f32_16x16x32_bf16 v[236:239], v[184:187], v[172:175], v[236:239]\n"
    "v_lshl_add_u32 v21, v21, 3, s40\n"
    "v_mfma_f32_16x16x32_bf16 v[236:239], v[152:155], v[140:143], v[236:239]\n"
    "v_mfma_f32_16x16x32_bf16 v[252:255], v[156:159], v[140:143], v[252:255]\n"
    "s_movk_i32 s41, 144\n"
    "v_mfma_f32_16x16x32_bf16 v[252:255], v[188:191], v[172:175], v[252:255]\n"
    "v_mfma_f32_16x16x32_bf16 v[248:251], v[188:191], v[168:171], v[248:251]\n"
    "v_add_u32 v22, v12, v13\n"
    "v_mfma_f32_16x16x32_bf16 v[248:251], v[156:159], v[136:139], v[248:251]\n"
    "v_mfma_f32_16x16x32_bf16 v[244:247], v[156:159], v[132:135], v[244:247]\n"
    "v_and_b32 v22, 3, v22\n"
    "v_mfma_f32_16x16x32_bf16 v[244:247], v[188:191], v[164:167], v[244:247]\n"
    "v_mfma_f32_16x16x32_bf16 v[240:243], v[188:191], v[160:163], v[240:243]\n"
    "v_mfma_f32_16x16x32_bf16 v[240:243], v[156:159], v[128:131], v[240:243]\n"
    "s_waitcnt vmcnt(7)\n"
    "s_barrier\n"
    "s_add_u32 m0, s35, 49152\n"
    "s_nop 0\n"
    "global_load_lds_dwordx4 v46, s[30:31] offset:0\n"
    "ds_read_b128 v[128:131], v58 offset:0\n"
    "ds_read_b128 v[132:135], v58 offset:2048\n"
    "ds_read_b128 v[136:139], v58 offset:4096\n"
    "global_load_lds_dwordx4 v47, s[30:31] offset:1024\n"
    "s_add_u32 s30, s30, 212992\n"
    "s_addc_u32 s31, s31, 0\n"
    "ds_read_b128 v[140:143], v58 offset:6144\n"
    "ds_read_b64_tr_b16 v[144:145], v80 offset:0\n"
    "ds_read_b64_tr_b16 v[146:147], v80 offset:1024\n"
    "s_add_u32 m0, s34, 49152\n"
    "s_nop 0\n"
    "global_load_lds_dwordx4 v52, s[36:37] offset:0\n"
    "ds_read_b64_tr_b16 v[148:149], v81 offset:0\n"
    "ds_read_b64_tr_b16 v[150:151], v81 offset:1024\n"
    "ds_read_b64_tr_b16 v[152:153], v82 offset:0\n"
    "global_load_lds_dwordx4 v53, s[36:37] offset:1024\n"
    "ds_read_b64_tr_b16 v[154:155], v82 offset:1024\n"
    "ds_read_b64_tr_b16 v[156:157], v83 offset:0\n"
    "ds_read_b64_tr_b16 v[158:159], v83 offset:1024\n"
    "global_load_lds_dwordx4 v54, s[36:37] offset:2048\n"
    "ds_read_b128 v[160:163], v59 offset:0\n"
    "ds_read_b128 v[164:167], v59 offset:2048\n"
    "ds_read_b128 v[168:171], v59 offset:4096\n"
    "global_load_lds_dwordx4 v55, s[36:37] offset:3072\n"
    "ds_read_b128 v[172:175], v59 offset:6144\n"
    "ds_read_b64_tr_b16 v[176:177], v80 offset:8192\n"
    "ds_read_b64_tr_b16 v[178:179], v80 offset:9216\n"
    "global_load_dwordx2 v[86:87], v42, s[10:11] offset:32\n"
    "ds_read_b64_tr_b16 v[180:181], v81 offset:8192\n"
    "ds_read_b64_tr_b16 v[182:183], v81 offset:9216\n"
    "ds_read_b64_tr_b16 v[184:185], v82 offset:8192\n"
    "ds_read_b64_tr_b16 v[186:187], v82 offset:9216\n"
    "ds_read_b64_tr_b16 v[188:189], v83 offset:8192\n"
    "ds_read_b64_tr_b16 v[190:191], v83 offset:9216\n"
    "s_waitcnt vmcnt(12) lgkmcnt(0)\n"
    "s_barrier\n"
    "v_mfma_f32_16x16x32_bf16 v[192:195], v[144:147], v[128:131], v[192:195]\n"
    "v_lshlrev_b32 v105, 7, v105\n"
    "v_mfma_f32_16x16x32_bf16 v[192:195], v[176:179], v[160:163], v[192:195]\n"
    "v_add_u32_dpp v52, v104, v48 quad_perm:[0,0,0,0] row_mask:0xf bank_mask:0xf\n"
    "v_mfma_f32_16x16x32_bf16 v[196:199], v[176:179], v[164:167], v[196:199]\n"
    "v_mfma_f32_16x16x32_bf16 v[196:199], v[144:147], v[132:135], v[196:199]\n"
    "v_add_u32_dpp v53, v104, v49 quad_perm:[1,1,1,1] row_mask:0xf bank_mask:0xf\n"
    "v_mfma_f32_16x16x32_bf16 v[200:203], v[144:147], v[136:139], v[200:203]\n"
    "v_mfma_f32_16x16x32_bf16 v[200:203], v[176:179], v[168:171], v[200:203]\n"
    "v_add_u32_dpp v54, v104, v50 quad_perm:[2,2,2,2] row_mask:0xf bank_mask:0xf\n"
    "v_mfma_f32_16x16x32_bf16 v[204:207], v[176:179], v[172:175], v[204:207]\n"
    "v_mfma_f32_16x16x32_bf16 v[204:207], v[144:147], v[140:143], v[204:207]\n"
    "v_add_u32_dpp v55, v104, v51 quad_perm:[3,3,3,3] row_mask:0xf bank_mask:0xf\n"
    "v_mfma_f32_16x16x32_bf16 v[220:223], v[148:151], v[140:143], v[220:223]\n"
    "v_mfma_f32_16x16x32_bf16 v[220:223], v[180:183], v[172:175], v[220:223]\n"
    "v_mad_u32_u24 v4, v22, s41, v21\n"
    "v_mfma_f32_16x16x32_bf16 v[216:219], v[180:183], v[168:171], v[216:219]\n"
    "v_mfma_f32_16x16x32_bf16 v[216:219], v[148:151], v[136:139], v[216:219]\n"
    "v_cmp_gt_u32 vcc, 2, v13\n"
    "v_mfma_f32_16x16x32_bf16 v[212:215], v[148:151], v[132:135], v[212:215]\n"
    "v_mfma_f32_16x16x32_bf16 v[212:215], v[180:183], v[164:167], v[212:215]\n"
    "v_add_u32 v24, 22, v13\n"
    "v_mfma_f32_16x16x32_bf16 v[208:211], v[180:183], v[160:163], v[208:211]\n"
    "v_mfma_f32_16x16x32_bf16 v[208:211], v[148:151], v[128:131], v[208:211]\n"
    "v_lshlrev_b32 v13, 3, v13\n"
    "v_mfma_f32_16x16x32_bf16 v[224:227], v[152:155], v[128:131], v[224:227]\n"
    "v_mfma_f32_16x16x32_bf16 v[224:227], v[184:187], v[160:163], v[224:227]\n"
    "v_cndmask_b32 v22, v24, v22, vcc\n"
    "v_mfma_f32_16x16x32_bf16 v[228:231], v[184:187], v[164:167], v[228:231]\n"
    "v_mfma_f32_16x16x32_bf16 v[228:231], v[152:155], v[132:135], v[228:231]\n"
    "v_mad_u32_u24 v3, v22, s41, v21\n"
    "v_mfma_f32_16x16x32_bf16 v[232:235], v[152:155], v[136:139], v[232:235]\n"
    "v_mfma_f32_16x16x32_bf16 v[232:235], v[184:187], v[168:171], v[232:235]\n"
    "v_cmp_eq_u32 vcc, 3, v12\n"
    "v_mfma_f32_16x16x32_bf16 v[236:239], v[184:187], v[172:175], v[236:239]\n"
    "v_mfma_f32_16x16x32_bf16 v[236:239], v[152:155], v[140:143], v[236:239]\n"
    "v_xor_b32 v22, 0, v12\n"
    "v_mfma_f32_16x16x32_bf16 v[252:255], v[156:159], v[140:143], v[252:255]\n"
    "v_mfma_f32_16x16x32_bf16 v[252:255], v[188:191], v[172:175], v[252:255]\n"
    "v_and_b32 v22, 1, v22\n"
    "v_mfma_f32_16x16x32_bf16 v[248:251], v[188:191], v[168:171], v[248:251]\n"
    "v_mfma_f32_16x16x32_bf16 v[248:251], v[156:159], v[136:139], v[248:251]\n"
    "v_lshlrev_b32 v22, 1, v22\n"
    "v_mfma_f32_16x16x32_bf16 v[244:247], v[156:159], v[132:135], v[244:247]\n"
    "v_mfma_f32_16x16x32_bf16 v[244:247], v[188:191], v[164:167], v[244:247]\n"
    "v_lshl_add_u32 v22, v12, 3, v22\n"
    "v_mfma_f32_16x16x32_bf16 v[240:243], v[188:191], v[160:163], v[240:243]\n"
    "v_mfma_f32_16x16x32_bf16 v[240:243], v[156:159], v[128:131], v[240:243]\n"
    "s_waitcnt vmcnt(8)\n"
    "s_barrier\n"
    "s_add_u32 m0, s35, 98304\n"
    "s_nop 0\n"
    "global_load_lds_dwordx4 v46, s[30:31] offset:0\n"
    "ds_read_b128 v[128:131], v56 offset:0\n"
    "ds_read_b128 v[132:135], v56 offset:2048\n"
    "ds_read_b128 v[136:139], v56 offset:4096\n"
    "global_load_lds_dwordx4 v47, s[30:31] offset:1024\n"
    "s_add_u32 s66, s28, 3\n"
    "s_and_b32 s66, s66, 7\n"
    "s_mul_i32 s66, s66, 638976\n"
    "s_add_u32 s30, s6, s66\n"
    "s_addc_u32 s31, s7, 0\n"
    "ds_read_b128 v[140:143], v56 offset:6144\n"
    "ds_read_b64_tr_b16 v[144:145], v76 offset:0\n"
    "ds_read_b64_tr_b16 v[146:147], v76 offset:1024\n"
    "s_add_u32 m0, s34, 98304\n"
    "s_nop 0\n"
    "global_load_lds_dwordx4 v52, s[36:37] offset:0\n"
    "ds_read_b64_tr_b16 v[148:149], v77 offset:0\n"
    "ds_read_b64_tr_b16 v[150:151], v77 offset:1024\n"
    "ds_read_b64_tr_b16 v[152:153], v78 offset:0\n"
    "global_load_lds_dwordx4 v53, s[36:37] offset:1024\n"
    "ds_read_b64_tr_b16 v[154:155], v78 offset:1024\n"
    "ds_read_b64_tr_b16 v[156:157], v79 offset:0\n"
    "ds_read_b64_tr_b16 v[158:159], v79 offset:1024\n"
    "global_load_lds_dwordx4 v54, s[36:37] offset:2048\n"
    "ds_read_b128 v[160:163], v57 offset:0\n"
    "ds_read_b128 v[164:167], v57 offset:2048\n"
    "ds_read_b128 v[168:171], v57 offset:4096\n"
    "global_load_lds_dwordx4 v55, s[36:37] offset:3072\n"
    "ds_read_b128 v[172:175], v57 offset:6144\n"
    "ds_read_b64_tr_b16 v[176:177], v76 offset:8192\n"
    "ds_read_b64_tr_b16 v[178:179], v76 offset:9216\n"
    "global_load_dwordx2 v[88:89], v42, s[10:11] offset:64\n"
    "ds_read_b64_tr_b16 v[180:181], v77 offset:8192\n"
    "ds_read_b64_tr_b16 v[182:183], v77 offset:9216\n"
    "ds_read_b64_tr_b16 v[184:185], v78 offset:8192\n"
    "ds_read_b64_tr_b16 v[186:187], v78 offset:9216\n"
    "ds_read_b64_tr_b16 v[188:189], v79 offset:8192\n"
    "ds_read_b64_tr_b16 v[190:191], v79 offset:9216\n"
    "s_waitcnt vmcnt(12) lgkmcnt(0)\n"
    "s_barrier\n"
    "v_mfma_f32_16x16x32_bf16 v[192:195], v[144:147], v[128:131], v[192:195]\n"
    "v_lshlrev_b32 v106, 7, v106\n"
    "v_mfma_f32_16x16x32_bf16 v[192:195], v[176:179], v[160:163], v[192:195]\n"
    "v_mfma_f32_16x16x32_bf16 v[196:199], v[176:179], v[164:167], v[196:199]\n"
    "v_add_u32_dpp v52, v105, v48 quad_perm:[0,0,0,0] row_mask:0xf bank_mask:0xf\n"
    "v_mfma_f32_16x16x32_bf16 v[196:199], v[144:147], v[132:135], v[196:199]\n"
    "v_mfma_f32_16x16x32_bf16 v[200:203], v[144:147], v[136:139], v[200:203]\n"
    "v_add_u32_dpp v53, v105, v49 quad_perm:[1,1,1,1] row_mask:0xf bank_mask:0xf\n"
    "v_mfma_f32_16x16x32_bf16 v[200:203], v[176:179], v[168:171], v[200:203]\n"
    "v_mfma_f32_16x16x32_bf16 v[204:207], v[176:179], v[172:175], v[204:207]\n"
    "v_add_u32_dpp v54, v105, v50 quad_perm:[2,2,2,2] row_mask:0xf bank_mask:0xf\n"
    "v_mfma_f32_16x16x32_bf16 v[204:207], v[144:147], v[140:143], v[204:207]\n"
    "v_mfma_f32_16x16x32_bf16 v[220:223], v[148:151], v[140:143], v[220:223]\n"
    "v_add_u32_dpp v55, v105, v51 quad_perm:[3,3,3,3] row_mask:0xf bank_mask:0xf\n"
    "v_mfma_f32_16x16x32_bf16 v[220:223], v[180:183], v[172:175], v[220:223]\n"
    "v_mfma_f32_16x16x32_bf16 v[216:219], v[180:183], v[168:171], v[216:219]\n"
    "s_mov_b32 s40, 0x05040100\n"
    "v_mfma_f32_16x16x32_bf16 v[216:219], v[148:151], v[136:139], v[216:219]\n"
    "v_mfma_f32_16x16x32_bf16 v[212:215], v[148:151], v[132:135], v[212:215]\n"
    "v_bfe_u32 v24, s40, v13, 8\n"
    "v_mfma_f32_16x16x32_bf16 v[212:215], v[180:183], v[164:167], v[212:215]\n"
    "v_mfma_f32_16x16x32_bf16 v[208:211], v[180:183], v[160:163], v[208:211]\n"
    "v_add_u32 v22, v22, v24\n"
    "v_mfma_f32_16x16x32_bf16 v[208:211], v[148:151], v[128:131], v[208:211]\n"
    "v_mfma_f32_16x16x32_bf16 v[224:227], v[152:155], v[128:131], v[224:227]\n"
    "s_mov_b32 s40, 0x1a191818\n"
    "v_mfma_f32_16x16x32_bf16 v[224:227], v[184:187], v[160:163], v[224:227]\n"
    "v_mfma_f32_16x16x32_bf16 v[228:231], v[184:187], v[164:167], v[228:231]\n"
    "v_bfe_u32 v24, s40, v13, 8\n"
    "v_mfma_f32_16x16x32_bf16 v[228:231], v[152:155], v[132:135], v[228:231]\n"
    "v_mfma_f32_16x16x32_bf16 v[232:235], v[152:155], v[136:139], v[232:235]\n"
    "v_cndmask_b32 v22, v22, v24, vcc\n"
    "v_mfma_f32_16x16x32_bf16 v[232:235], v[184:187], v[168:171], v[232:235]\n"
    "v_mfma_f32_16x16x32_bf16 v[236:239], v[184:187], v[172:175], v[236:239]\n"
    "v_mad_u32_u24 v5, v22, s41, v21\n"
    "v_mfma_f32_16x16x32_bf16 v[236:239], v[152:155], v[140:143], v[236:239]\n"
    "v_mfma_f32_16x16x32_bf16 v[252:255], v[156:159], v[140:143], v[252:255]\n"
    "v_xor_b32 v22, 1, v12\n"
    "v_mfma_f32_16x16x32_bf16 v[252:255], v[188:191], v[172:175], v[252:255]\n"
    "v_mfma_f32_16x16x32_bf16 v[248:251], v[188:191], v[168:171], v[248:251]\n"
    "v_and_b32 v22, 1, v22\n"
    "v_mfma_f32_16x16x32_bf16 v[248:251], v[156:159], v[136:139], v[248:251]\n"
    "v_mfma_f32_16x16x32_bf16 v[244:247], v[156:159], v[132:135], v[244:247]\n"
    "v_lshlrev_b32 v22, 1, v22\n"
    "v_mfma_f32_16x16x32_bf16 v[244:247], v[188:191], v[164:167], v[244:247]\n"
    "v_mfma_f32_16x16x32_bf16 v[240:243], v[188:191], v[160:163], v[240:243]\n"
    "v_mfma_f32_16x16x32_bf16 v[240:243], v[156:159], v[128:131], v[240:243]\n"
    "s_waitcnt vmcnt(8)\n"
    "s_barrier\n"
    "s_add_u32 m0, s35, 0\n"
    "s_nop 0\n"
    "global_load_lds_dwordx4 v46, s[30:31] offset:0\n"
    "ds_read_b128 v[128:131], v56 offset:49152\n"
    "ds_read_b128 v[132:135], v56 offset:51200\n"
    "ds_read_b128 v[136:139], v56 offset:53248\n"
    "global_load_lds_dwordx4 v47, s[30:31] offset:1024\n"
    "s_add_u32 s30, s30, 212992\n"
    "s_addc_u32 s31, s31, 0\n"
    "ds_read_b128 v[140:143], v56 offset:55296\n"
    "ds_read_b64_tr_b16 v[144:145], v76 offset:49152\n"
    "ds_read_b64_tr_b16 v[146:147], v76 offset:50176\n"
    "s_add_u32 m0, s34, 0\n"
    "s_nop 0\n"
    "global_load_lds_dwordx4 v52, s[36:37] offset:0\n"
    "ds_read_b64_tr_b16 v[148:149], v77 offset:49152\n"
    "ds_read_b64_tr_b16 v[150:151], v77 offset:50176\n"
    "ds_read_b64_tr_b16 v[152:153], v78 offset:49152\n"
    "global_load_lds_dwordx4 v53, s[36:37] offset:1024\n"
    "ds_read_b64_tr_b16 v[154:155], v78 offset:50176\n"
    "ds_read_b64_tr_b16 v[156:157], v79 offset:49152\n"
    "ds_read_b64_tr_b16 v[158:159], v79 offset:50176\n"
    "global_load_lds_dwordx4 v54, s[36:37] offset:2048\n"
    "ds_read_b128 v[160:163], v57 offset:49152\n"
    "ds_read_b128 v[164:167], v57 offset:51200\n"
    "ds_read_b128 v[168:171], v57 offset:53248\n"
    "global_load_lds_dwordx4 v55, s[36:37] offset:3072\n"
    "ds_read_b128 v[172:175], v57 offset:55296\n"
    "ds_read_b64_tr_b16 v[176:177], v76 offset:57344\n"
    "ds_read_b64_tr_b16 v[178:179], v76 offset:58368\n"
    "global_load_dwordx2 v[90:91], v42, s[10:11] offset:96\n"
    "ds_read_b64_tr_b16 v[180:181], v77 offset:57344\n"
    "ds_read_b64_tr_b16 v[182:183], v77 offset:58368\n"
    "ds_read_b64_tr_b16 v[184:185], v78 offset:57344\n"
    "ds_read_b64_tr_b16 v[186:187], v78 offset:58368\n"
    "ds_read_b64_tr_b16 v[188:189], v79 offset:57344\n"
    "ds_read_b64_tr_b16 v[190:191], v79 offset:58368\n"
    "s_waitcnt vmcnt(12) lgkmcnt(0)\n"
    "s_barrier\n"
    "v_mfma_f32_16x16x32_bf16 v[192:195], v[144:147], v[128:131], v[192:195]\n"
    "v_lshlrev_b32 v107, 7, v107\n"
    "v_mfma_f32_16x16x32_bf16 v[192:195], v[176:179], v[160:163], v[192:195]\n"
    "v_mfma_f32_16x16x32_bf16 v[196:199], v[176:179], v[164:167], v[196:199]\n"
    "v_add_u32_dpp v52, v106, v48 quad_perm:[0,0,0,0] row_mask:0xf bank_mask:0xf\n"
    "v_mfma_f32_16x16x32_bf16 v[196:199], v[144:147], v[132:135], v[196:199]\n"
    "v_mfma_f32_16x16x32_bf16 v[200:203], v[144:147], v[136:139], v[200:203]\n"
    "v_add_u32_dpp v53, v106, v49 quad_perm:[1,1,1,1] row_mask:0xf bank_mask:0xf\n"
    "v_mfma_f32_16x16x32_bf16 v[200:203], v[176:179], v[168:171], v[200:203]\n"
    "v_mfma_f32_16x16x32_bf16 v[204:207], v[176:179], v[172:175], v[204:207]\n"
    "v_add_u32_dpp v54, v106, v50 quad_perm:[2,2,2,2] row_mask:0xf bank_mask:0xf\n"
    "v_mfma_f32_16x16x32_bf16 v[204:207], v[144:147], v[140:143], v[204:207]\n"
    "v_mfma_f32_16x16x32_bf16 v[220:223], v[148:151], v[140:143], v[220:223]\n"
    "v_add_u32_dpp v55, v106, v51 quad_perm:[3,3,3,3] row_mask:0xf bank_mask:0xf\n"
    "v_mfma_f32_16x16x32_bf16 v[220:223], v[180:183], v[172:175], v[220:223]\n"
    "v_mfma_f32_16x16x32_bf16 v[216:219], v[180:183], v[168:171], v[216:219]\n"
    "v_lshl_add_u32 v22, v12, 3, v22\n"
    "v_mfma_f32_16x16x32_bf16 v[216:219], v[148:151], v[136:139], v[216:219]\n"
    "v_mfma_f32_16x16x32_bf16 v[212:215], v[148:151], v[132:135], v[212:215]\n"
    "s_mov_b32 s40, 0x05040100\n"
    "v_mfma_f32_16x16x32_bf16 v[212:215], v[180:183], v[164:167], v[212:215]\n"
    "v_mfma_f32_16x16x32_bf16 v[208:211], v[180:183], v[160:163], v[208:211]\n"
    "v_bfe_u32 v24, s40, v13, 8\n"
    "v_mfma_f32_16x16x32_bf16 v[208:211], v[148:151], v[128:131], v[208:211]\n"
    "v_mfma_f32_16x16x32_bf16 v[224:227], v[152:155], v[128:131], v[224:227]\n"
    "v_add_u32 v22, v22, v24\n"
    "v_mfma_f32_16x16x32_bf16 v[224:227], v[184:187], v[160:163], v[224:227]\n"
    "v_mfma_f32_16x16x32_bf16 v[228:231], v[184:187], v[164:167], v[228:231]\n"
    "s_mov_b32 s40, 0x1a191918\n"
    "v_mfma_f32_16x16x32_bf16 v[228:231], v[152:155], v[132:135], v[228:231]\n"
    "v_mfma_f32_16x16x32_bf16 v[232:235], v[152:155], v[136:139], v[232:235]\n"
    "v_bfe_u32 v24, s40, v13, 8\n"
    "v_mfma_f32_16x16x32_bf16 v[232:235], v[184:187], v[168:171], v[232:235]\n"
    "v_mfma_f32_16x16x32_bf16 v[236:239], v[184:187], v[172:175], v[236:239]\n"
    "v_cndmask_b32 v22, v22, v24, vcc\n"
    "v_mfma_f32_16x16x32_bf16 v[236:239], v[152:155], v[140:143], v[236:239]\n"
    "v_mfma_f32_16x16x32_bf16 v[252:255], v[156:159], v[140:143], v[252:255]\n"
    "v_mad_u32_u24 v6, v22, s41, v21\n"
    "v_mfma_f32_16x16x32_bf16 v[252:255], v[188:191], v[172:175], v[252:255]\n"
    "v_mfma_f32_16x16x32_bf16 v[248:251], v[188:191], v[168:171], v[248:251]\n"
    "v_bfrev_b32 v14, v12\n"
    "v_mfma_f32_16x16x32_bf16 v[248:251], v[156:159], v[136:139], v[248:251]\n"
    "v_mfma_f32_16x16x32_bf16 v[244:247], v[156:159], v[132:135], v[244:247]\n"
    "v_lshrrev_b32 v14, 26, v14\n"
    "v_mfma_f32_16x16x32_bf16 v[244:247], v[188:191], v[164:167], v[244:247]\n"
    "v_mfma_f32_16x16x32_bf16 v[240:243], v[188:191], v[160:163], v[240:243]\n"
    "v_mfma_f32_16x16x32_bf16 v[240:243], v[156:159], v[128:131], v[240:243]\n"
    "s_waitcnt vmcnt(8)\n"
    "s_barrier\n"
    "s_add_u32 m0, s35, 49152\n"
    "s_nop 0\n"
    "global_load_lds_dwordx4 v46, s[30:31] offset:0\n"
    "ds_read_b128 v[128:131], v58 offset:0\n"
    "ds_read_b128 v[132:135], v58 offset:2048\n"
    "ds_read_b128 v[136:139], v58 offset:4096\n"
    "global_load_lds_dwordx4 v47, s[30:31] offset:1024\n"
    "s_add_u32 s30, s30, 212992\n"
    "s_addc_u32 s31, s31, 0\n"
    "ds_read_b128 v[140:143], v58 offset:6144\n"
    "ds_read_b64_tr_b16 v[144:145], v80 offset:0\n"
    "ds_read_b64_tr_b16 v[146:147], v80 offset:1024\n"
    "s_add_u32 m0, s34, 49152\n"
    "s_nop 0\n"
    "global_load_lds_dwordx4 v52, s[36:37] offset:0\n"
    "ds_read_b64_tr_b16 v[148:149], v81 offset:0\n"
    "ds_read_b64_tr_b16 v[150:151], v81 offset:1024\n"
    "ds_read_b64_tr_b16 v[152:153], v82 offset:0\n"
    "global_load_lds_dwordx4 v53, s[36:37] offset:1024\n"
    "ds_read_b64_tr_b16 v[154:155], v82 offset:1024\n"
    "ds_read_b64_tr_b16 v[156:157], v83 offset:0\n"
    "ds_read_b64_tr_b16 v[158:159], v83 offset:1024\n"
    "global_load_lds_dwordx4 v54, s[36:37] offset:2048\n"
    "ds_read_b128 v[160:163], v59 offset:0\n"
    "ds_read_b128 v[164:167], v59 offset:2048\n"
    "ds_read_b128 v[168:171], v59 offset:4096\n"
    "global_load_lds_dwordx4 v55, s[36:37] offset:3072\n"
    "ds_read_b128 v[172:175], v59 offset:6144\n"
    "ds_read_b64_tr_b16 v[176:177], v80 offset:8192\n"
    "ds_read_b64_tr_b16 v[178:179], v80 offset:9216\n"
    "global_load_dwordx2 v[92:93], v42, s[18:19] offset:0\n"
    "ds_read_b64_tr_b16 v[180:181], v81 offset:8192\n"
    "ds_read_b64_tr_b16 v[182:183], v81 offset:9216\n"
    "ds_read_b64_tr_b16 v[184:185], v82 offset:8192\n"
    "ds_read_b64_tr_b16 v[186:187], v82 offset:9216\n"
    "ds_read_b64_tr_b16 v[188:189], v83 offset:8192\n"
    "ds_read_b64_tr_b16 v[190:191], v83 offset:9216\n"
    "s_waitcnt vmcnt(12) lgkmcnt(0)\n"
    "s_barrier\n"
    "v_mfma_f32_16x16x32_bf16 v[192:195], v[144:147], v[128:131], v[192:195]\n"
    "v_lshlrev_b32 v108, 7, v108\n"
    "v_mfma_f32_16x16x32_bf16 v[192:195], v[176:179], v[160:163], v[192:195]\n"
    "v_add_u32_dpp v52, v107, v48 quad_perm:[0,0,0,0] row_mask:0xf bank_mask:0xf\n"
    "v_mfma_f32_16x16x32_bf16 v[196:199], v[176:179], v[164:167], v[196:199]\n"
    "v_mfma_f32_16x16x32_bf16 v[196:199], v[144:147], v[132:135], v[196:199]\n"
    "v_add_u32_dpp v53, v107, v49 quad_perm:[1,1,1,1] row_mask:0xf bank_mask:0xf\n"
    "v_mfma_f32_16x16x32_bf16 v[200:203], v[144:147], v[136:139], v[200:203]\n"
    "v_mfma_f32_16x16x32_bf16 v[200:203], v[176:179], v[168:171], v[200:203]\n"
    "v_add_u32_dpp v54, v107, v50 quad_perm:[2,2,2,2] row_mask:0xf bank_mask:0xf\n"
    "v_mfma_f32_16x16x32_bf16 v[204:207], v[176:179], v[172:175], v[204:207]\n"
    "v_mfma_f32_16x16x32_bf16 v[204:207], v[144:147], v[140:143], v[204:207]\n"
    "v_add_u32_dpp v55, v107, v51 quad_perm:[3,3,3,3] row_mask:0xf bank_mask:0xf\n"
    "v_mfma_f32_16x16x32_bf16 v[220:223], v[148:151], v[140:143], v[220:223]\n"
    "v_mfma_f32_16x16x32_bf16 v[220:223], v[180:183], v[172:175], v[220:223]\n"
    "s_add_u32 s40, s22, 3\n"
    "v_mfma_f32_16x16x32_bf16 v[216:219], v[180:183], v[168:171], v[216:219]\n"
    "v_mfma_f32_16x16x32_bf16 v[216:219], v[148:151], v[136:139], v[216:219]\n"
    "s_and_b32 s40, s40, 3\n"
    "v_mfma_f32_16x16x32_bf16 v[212:215], v[148:151], v[132:135], v[212:215]\n"
    "v_mfma_f32_16x16x32_bf16 v[212:215], v[180:183], v[164:167], v[212:215]\n"
    "s_lshl2_add_u32 s40, s40, s22\n"
    "v_mfma_f32_16x16x32_bf16 v[208:211], v[180:183], v[160:163], v[208:211]\n"
    "v_mfma_f32_16x16x32_bf16 v[208:211], v[148:151], v[128:131], v[208:211]\n"
    "s_add_u32 s40, s40, s58\n"
    "v_mfma_f32_16x16x32_bf16 v[224:227], v[152:155], v[128:131], v[224:227]\n"
    "v_mfma_f32_16x16x32_bf16 v[224:227], v[184:187], v[160:163], v[224:227]\n"
    "s_lshl_b32 s40, s40, 2\n"
    "v_mfma_f32_16x16x32_bf16 v[228:231], v[184:187], v[164:167], v[228:231]\n"
    "v_mfma_f32_16x16x32_bf16 v[228:231], v[152:155], v[132:135], v[228:231]\n"
    "s_add_u32 s40, s40, -450048\n"
    "v_mfma_f32_16x16x32_bf16 v[232:235], v[152:155], v[136:139], v[232:235]\n"
    "v_mfma_f32_16x16x32_bf16 v[232:235], v[184:187], v[168:171], v[232:235]\n"
    "v_add_u32 v14, s40, v14\n"
    "v_mfma_f32_16x16x32_bf16 v[236:239], v[184:187], v[172:175], v[236:239]\n"
    "v_mfma_f32_16x16x32_bf16 v[236:239], v[152:155], v[140:143], v[236:239]\n"
    "v_and_b32 v2, 1, v1\n"
    "v_mfma_f32_16x16x32_bf16 v[252:255], v[156:159], v[140:143], v[252:255]\n"
    "v_mfma_f32_16x16x32_bf16 v[252:255], v[188:191], v[172:175], v[252:255]\n"
    "v_mul_u32_u24 v2, 0xaaaa, v2\n"
    "v_mfma_f32_16x16x32_bf16 v[248:251], v[188:191], v[168:171], v[248:251]\n"
    "v_mfma_f32_16x16x32_bf16 v[248:251], v[156:159], v[136:139], v[248:251]\n"
    "v_add_u32 v2, 0x4444, v2\n"
    "v_mfma_f32_16x16x32_bf16 v[244:247], v[156:159], v[132:135], v[244:247]\n"
    "v_mfma_f32_16x16x32_bf16 v[244:247], v[188:191], v[164:167], v[244:247]\n"
    "v_mad_u32_u24 v15, v17, 36, v19\n"
    "v_mfma_f32_16x16x32_bf16 v[240:243], v[188:191], v[160:163], v[240:243]\n"
    "v_mfma_f32_16x16x32_bf16 v[240:243], v[156:159], v[128:131], v[240:243]\n"
    "s_waitcnt vmcnt(8)\n"
    "s_barrier\n"
    "s_add_u32 m0, s35, 98304\n"
    "s_nop 0\n"
    "global_load_lds_dwordx4 v46, s[30:31] offset:0\n"
    "ds_read_b128 v[128:131], v56 offset:0\n"
    "ds_read_b128 v[132:135], v56 offset:2048\n"
    "ds_read_b128 v[136:139], v56 offset:4096\n"
    "global_load_lds_dwordx4 v47, s[30:31] offset:1024\n"
    "s_add_u32 s66, s28, 4\n"
    "s_and_b32 s66, s66, 7\n"
    "s_mul_i32 s66, s66, 638976\n"
    "s_add_u32 s30, s6, s66\n"
    "s_addc_u32 s31, s7, 0\n"
    "ds_read_b128 v[140:143], v56 offset:6144\n"
    "ds_read_b64_tr_b16 v[144:145], v76 offset:0\n"
    "ds_read_b64_tr_b16 v[146:147], v76 offset:1024\n"
    "s_add_u32 m0, s34, 98304\n"
    "s_nop 0\n"
    "global_load_lds_dwordx4 v52, s[36:37] offset:0\n"
    "ds_read_b64_tr_b16 v[148:149], v77 offset:0\n"
    "ds_read_b64_tr_b16 v[150:151], v77 offset:1024\n"
    "ds_read_b64_tr_b16 v[152:153], v78 offset:0\n"
    "global_load_lds_dwordx4 v53, s[36:37] offset:1024\n"
    "ds_read_b64_tr_b16 v[154:155], v78 offset:1024\n"
    "ds_read_b64_tr_b16 v[156:157], v79 offset:0\n"
    "ds_read_b64_tr_b16 v[158:159], v79 offset:1024\n"
    "global_load_lds_dwordx4 v54, s[36:37] offset:2048\n"
    "ds_read_b128 v[160:163], v57 offset:0\n"
    "ds_read_b128 v[164:167], v57 offset:2048\n"
    "ds_read_b128 v[168:171], v57 offset:4096\n"
    "global_load_lds_dwordx4 v55, s[36:37] offset:3072\n"
    "ds_read_b128 v[172:175], v57 offset:6144\n"
    "ds_read_b64_tr_b16 v[176:177], v76 offset:8192\n"
    "ds_read_b64_tr_b16 v[178:179], v76 offset:9216\n"
    "global_load_dwordx2 v[94:95], v42, s[18:19] offset:32\n"
    "ds_read_b64_tr_b16 v[180:181], v77 offset:8192\n"
    "ds_read_b64_tr_b16 v[182:183], v77 offset:9216\n"
    "ds_read_b64_tr_b16 v[184:185], v78 offset:8192\n"
    "ds_read_b64_tr_b16 v[186:187], v78 offset:9216\n"
    "ds_read_b64_tr_b16 v[188:189], v79 offset:8192\n"
    "ds_read_b64_tr_b16 v[190:191], v79 offset:9216\n"
    "s_waitcnt vmcnt(12) lgkmcnt(0)\n"
    "s_barrier\n"
    "v_mfma_f32_16x16x32_bf16 v[192:195], v[144:147], v[128:131], v[192:195]\n"
    "v_lshlrev_b32 v109, 7, v109\n"
    "v_mfma_f32_16x16x32_bf16 v[192:195], v[176:179], v[160:163], v[192:195]\n"
    "v_mfma_f32_16x16x32_bf16 v[196:199], v[176:179], v[164:167], v[196:199]\n"
    "v_add_u32_dpp v52, v108, v48 quad_perm:[0,0,0,0] row_mask:0xf bank_mask:0xf\n"
    "v_mfma_f32_16x16x32_bf16 v[196:199], v[144:147], v[132:135], v[196:199]\n"
    "v_mfma_f32_16x16x32_bf16 v[200:203], v[144:147], v[136:139], v[200:203]\n"
    "v_add_u32_dpp v53, v108, v49 quad_perm:[1,1,1,1] row_mask:0xf bank_mask:0xf\n"
    "v_mfma_f32_16x16x32_bf16 v[200:203], v[176:179], v[168:171], v[200:203]\n"
    "v_mfma_f32_16x16x32_bf16 v[204:207], v[176:179], v[172:175], v[204:207]\n"
    "v_add_u32_dpp v54, v108, v50 quad_perm:[2,2,2,2] row_mask:0xf bank_mask:0xf\n"
    "v_mfma_f32_16x16x32_bf16 v[204:207], v[144:147], v[140:143], v[204:207]\n"
    "v_mfma_f32_16x16x32_bf16 v[220:223], v[148:151], v[140:143], v[220:223]\n"
    "v_add_u32_dpp v55, v108, v51 quad_perm:[3,3,3,3] row_mask:0xf bank_mask:0xf\n"
    "v_mfma_f32_16x16x32_bf16 v[220:223], v[180:183], v[172:175], v[220:223]\n"
    "v_mfma_f32_16x16x32_bf16 v[216:219], v[180:183], v[168:171], v[216:219]\n"
    "v_mad_u32_u24 v16, v18, 36, v20\n"
    "v_mfma_f32_16x16x32_bf16 v[216:219], v[148:151], v[136:139], v[216:219]\n"
    "v_mfma_f32_16x16x32_bf16 v[212:215], v[148:151], v[132:135], v[212:215]\n"
    "s_add_u32 s41, s22, 2\n"
    "v_mfma_f32_16x16x32_bf16 v[212:215], v[180:183], v[164:167], v[212:215]\n"
    "v_mfma_f32_16x16x32_bf16 v[208:211], v[180:183], v[160:163], v[208:211]\n"
    "v_lshrrev_b32 v12, 5, v19\n"
    "v_mfma_f32_16x16x32_bf16 v[208:211], v[148:151], v[128:131], v[208:211]\n"
    "v_mfma_f32_16x16x32_bf16 v[224:227], v[152:155], v[128:131], v[224:227]\n"
    "v_sub_u32 v12, 1, v12\n"
    "v_mfma_f32_16x16x32_bf16 v[224:227], v[184:187], v[160:163], v[224:227]\n"
    "v_mfma_f32_16x16x32_bf16 v[228:231], v[184:187], v[164:167], v[228:231]\n"
    "v_and_b32 v12, 3, v12\n"
    "v_mfma_f32_16x16x32_bf16 v[228:231], v[152:155], v[132:135], v[228:231]\n"
    "v_mfma_f32_16x16x32_bf16 v[232:235], v[152:155], v[136:139], v[232:235]\n"
    "v_add_lshl_u32 v12, v12, s41, 12\n"
    "v_mfma_f32_16x16x32_bf16 v[232:235], v[184:187], v[168:171], v[232:235]\n"
    "v_mfma_f32_16x16x32_bf16 v[236:239], v[184:187], v[172:175], v[236:239]\n"
    "v_and_b32 v12, 0x7000, v12\n"
    "v_mfma_f32_16x16x32_bf16 v[236:239], v[152:155], v[140:143], v[236:239]\n"
    "v_mfma_f32_16x16x32_bf16 v[252:255], v[156:159], v[140:143], v[252:255]\n"
    "v_add3_u32 v15, v12, v15, s43\n"
    "v_mfma_f32_16x16x32_bf16 v[252:255], v[188:191], v[172:175], v[252:255]\n"
    "v_mfma_f32_16x16x32_bf16 v[248:251], v[188:191], v[168:171], v[248:251]\n"
    "v_lshrrev_b32 v12, 5, v20\n"
    "v_mfma_f32_16x16x32_bf16 v[248:251], v[156:159], v[136:139], v[248:251]\n"
    "v_mfma_f32_16x16x32_bf16 v[244:247], v[156:159], v[132:135], v[244:247]\n"
    "v_sub_u32 v12, 1, v12\n"
    "v_mfma_f32_16x16x32_bf16 v[244:247], v[188:191], v[164:167], v[244:247]\n"
    "v_mfma_f32_16x16x32_bf16 v[240:243], v[188:191], v[160:163], v[240:243]\n"
    "v_mfma_f32_16x16x32_bf16 v[240:243], v[156:159], v[128:131], v[240:243]\n"
    "s_waitcnt vmcnt(8)\n"
    "s_barrier\n"
    "s_add_u32 m0, s35, 0\n"
    "s_nop 0\n"
    "global_load_lds_dwordx4 v46, s[30:31] offset:0\n"
    "ds_read_b128 v[128:131], v56 offset:49152\n"
    "ds_read_b128 v[132:135], v56 offset:51200\n"
    "ds_read_b128 v[136:139], v56 offset:53248\n"
    "global_load_lds_dwordx4 v47, s[30:31] offset:1024\n"
    "s_add_u32 s30, s30, 212992\n"
    "s_addc_u32 s31, s31, 0\n"
    "ds_read_b128 v[140:143], v56 offset:55296\n"
    "ds_read_b64_tr_b16 v[144:145], v76 offset:49152\n"
    "ds_read_b64_tr_b16 v[146:147], v76 offset:50176\n"
    "s_add_u32 m0, s34, 0\n"
    "s_nop 0\n"
    "global_load_lds_dwordx4 v52, s[36:37] offset:0\n"
    "ds_read_b64_tr_b16 v[148:149], v77 offset:49152\n"
    "ds_read_b64_tr_b16 v[150:151], v77 offset:50176\n"
    "ds_read_b64_tr_b16 v[152:153], v78 offset:49152\n"
    "global_load_lds_dwordx4 v53, s[36:37] offset:1024\n"
    "ds_read_b64_tr_b16 v[154:155], v78 offset:50176\n"
    "ds_read_b64_tr_b16 v[156:157], v79 offset:49152\n"
    "ds_read_b64_tr_b16 v[158:159], v79 offset:50176\n"
    "global_load_lds_dwordx4 v54, s[36:37] offset:2048\n"
    "ds_read_b128 v[160:163], v57 offset:49152\n"
    "ds_read_b128 v[164:167], v57 offset:51200\n"
    "ds_read_b128 v[168:171], v57 offset:53248\n"
    "global_load_lds_dwordx4 v55, s[36:37] offset:3072\n"
    "ds_read_b128 v[172:175], v57 offset:55296\n"
    "ds_read_b64_tr_b16 v[176:177], v76 offset:57344\n"
    "ds_read_b64_tr_b16 v[178:179], v76 offset:58368\n"
    "global_load_dwordx2 v[122:123], v42, s[18:19] offset:64\n"
    "ds_read_b64_tr_b16 v[180:181], v77 offset:57344\n"
    "ds_read_b64_tr_b16 v[182:183], v77 offset:58368\n"
    "ds_read_b64_tr_b16 v[184:185], v78 offset:57344\n"
    "ds_read_b64_tr_b16 v[186:187], v78 offset:58368\n"
    "ds_read_b64_tr_b16 v[188:189], v79 offset:57344\n"
    "ds_read_b64_tr_b16 v[190:191], v79 offset:58368\n"
    "s_waitcnt vmcnt(12) lgkmcnt(0)\n"
    "s_barrier\n"
    "v_mfma_f32_16x16x32_bf16 v[192:195], v[144:147], v[128:131], v[192:195]\n"
    "v_lshlrev_b32 v110, 7, v110\n"
    "v_mfma_f32_16x16x32_bf16 v[192:195], v[176:179], v[160:163], v[192:195]\n"
    "v_add_u32_dpp v52, v109, v48 quad_perm:[0,0,0,0] row_mask:0xf bank_mask:0xf\n"
    "v_mfma_f32_16x16x32_bf16 v[196:199], v[176:179], v[164:167], v[196:199]\n"
    "v_mfma_f32_16x16x32_bf16 v[196:199], v[144:147], v[132:135], v[196:199]\n"
    "v_add_u32_dpp v53, v109, v49 quad_perm:[1,1,1,1] row_mask:0xf bank_mask:0xf\n"
    "v_mfma_f32_16x16x32_bf16 v[200:203], v[144:147], v[136:139], v[200:203]\n"
    "v_mfma_f32_16x16x32_bf16 v[200:203], v[176:179], v[168:171], v[200:203]\n"
    "v_add_u32_dpp v54, v109, v50 quad_perm:[2,2,2,2] row_mask:0xf bank_mask:0xf\n"
    "v_mfma_f32_16x16x32_bf16 v[204:207], v[176:179], v[172:175], v[204:207]\n"
    "v_mfma_f32_16x16x32_bf16 v[204:207], v[144:147], v[140:143], v[204:207]\n"
    "v_add_u32_dpp v55, v109, v51 quad_perm:[3,3,3,3] row_mask:0xf bank_mask:0xf\n"
    "v_mfma_f32_16x16x32_bf16 v[220:223], v[148:151], v[140:143], v[220:223]\n"
    "v_mfma_f32_16x16x32_bf16 v[220:223], v[180:183], v[172:175], v[220:223]\n"
    "v_and_b32 v12, 3, v12\n"
    "v_mfma_f32_16x16x32_bf16 v[216:219], v[180:183], v[168:171], v[216:219]\n"
    "v_mfma_f32_16x16x32_bf16 v[216:219], v[148:151], v[136:139], v[216:219]\n"
    "v_add_lshl_u32 v12, v12, s41, 12\n"
    "v_mfma_f32_16x16x32_bf16 v[212:215], v[148:151], v[132:135], v[212:215]\n"
    "v_mfma_f32_16x16x32_bf16 v[212:215], v[180:183], v[164:167], v[212:215]\n"
    "v_and_b32 v12, 0x7000, v12\n"
    "v_mfma_f32_16x16x32_bf16 v[208:211], v[180:183], v[160:163], v[208:211]\n"
    "v_mfma_f32_16x16x32_bf16 v[208:211], v[148:151], v[128:131], v[208:211]\n"
    "v_add3_u32 v16, v12, v16, s43\n"
    "v_mfma_f32_16x16x32_bf16 v[224:227], v[152:155], v[128:131], v[224:227]\n"
    "v_mfma_f32_16x16x32_bf16 v[224:227], v[184:187], v[160:163], v[224:227]\n"
    "v_add_u32 v16, 3456, v16\n"
    "v_mfma_f32_16x16x32_bf16 v[228:231], v[184:187], v[164:167], v[228:231]\n"
    "v_mfma_f32_16x16x32_bf16 v[228:231], v[152:155], v[132:135], v[228:231]\n"
    "s_mul_i32 s41, s47, 13\n"
    "v_mfma_f32_16x16x32_bf16 v[232:235], v[152:155], v[136:139], v[232:235]\n"
    "v_mfma_f32_16x16x32_bf16 v[232:235], v[184:187], v[168:171], v[232:235]\n"
    "s_sub_u32 s41, s3, s41\n"
    "v_mfma_f32_16x16x32_bf16 v[236:239], v[184:187], v[172:175], v[236:239]\n"
    "v_mfma_f32_16x16x32_bf16 v[236:239], v[152:155], v[140:143], v[236:239]\n"
    "s_lshl1_add_u32 s41, s41, s21\n"
    "v_mfma_f32_16x16x32_bf16 v[252:255], v[156:159], v[140:143], v[252:255]\n"
    "v_mfma_f32_16x16x32_bf16 v[252:255], v[188:191], v[172:175], v[252:255]\n"
    "s_mul_i32 s41, s41, 3\n"
    "v_mfma_f32_16x16x32_bf16 v[248:251], v[188:191], v[168:171], v[248:251]\n"
    "v_mfma_f32_16x16x32_bf16 v[248:251], v[156:159], v[136:139], v[248:251]\n"
    "s_add_u32 s41, s41, s22\n"
    "v_mfma_f32_16x16x32_bf16 v[244:247], v[156:159], v[132:135], v[244:247]\n"
    "v_mfma_f32_16x16x32_bf16 v[244:247], v[188:191], v[164:167], v[244:247]\n"
    "v_mov_b32 v24, s41\n"
    "v_mfma_f32_16x16x32_bf16 v[240:243], v[188:191], v[160:163], v[240:243]\n"
    "v_mfma_f32_16x16x32_bf16 v[240:243], v[156:159], v[128:131], v[240:243]\n"
    "s_waitcnt vmcnt(8)\n"
    "s_barrier\n"
    "s_add_u32 m0, s35, 49152\n"
    "s_nop 0\n"
    "global_load_lds_dwordx4 v46, s[30:31] offset:0\n"
    "ds_read_b128 v[128:131], v58 offset:0\n"
    "ds_read_b128 v[132:135], v58 offset:2048\n"
    "ds_read_b128 v[136:139], v58 offset:4096\n"
    "global_load_lds_dwordx4 v47, s[30:31] offset:1024\n"
    "s_add_u32 s30, s30, 212992\n"
    "s_addc_u32 s31, s31, 0\n"
    "ds_read_b128 v[140:143], v58 offset:6144\n"
    "ds_read_b64_tr_b16 v[144:145], v80 offset:0\n"
    "ds_read_b64_tr_b16 v[146:147], v80 offset:1024\n"
    "s_add_u32 m0, s34, 49152\n"
    "s_nop 0\n"
    "global_load_lds_dwordx4 v52, s[36:37] offset:0\n"
    "ds_read_b64_tr_b16 v[148:149], v81 offset:0\n"
    "ds_read_b64_tr_b16 v[150:151], v81 offset:1024\n"
    "ds_read_b64_tr_b16 v[152:153], v82 offset:0\n"
    "global_load_lds_dwordx4 v53, s[36:37] offset:1024\n"
    "ds_read_b64_tr_b16 v[154:155], v82 offset:1024\n"
    "ds_read_b64_tr_b16 v[156:157], v83 offset:0\n"
    "ds_read_b64_tr_b16 v[158:159], v83 offset:1024\n"
    "global_load_lds_dwordx4 v54, s[36:37] offset:2048\n"
    "ds_read_b128 v[160:163], v59 offset:0\n"
    "ds_read_b128 v[164:167], v59 offset:2048\n"
    "ds_read_b128 v[168:171], v59 offset:4096\n"
    "global_load_lds_dwordx4 v55, s[36:37] offset:3072\n"
    "ds_read_b128 v[172:175], v59 offset:6144\n"
    "ds_read_b64_tr_b16 v[176:177], v80 offset:8192\n"
    "ds_read_b64_tr_b16 v[178:179], v80 offset:9216\n"
    "global_load_dwordx2 v[124:125], v42, s[18:19] offset:96\n"
    "ds_read_b64_tr_b16 v[180:181], v81 offset:8192\n"
    "ds_read_b64_tr_b16 v[182:183], v81 offset:9216\n"
    "ds_read_b64_tr_b16 v[184:185], v82 offset:8192\n"
    "ds_read_b64_tr_b16 v[186:187], v82 offset:9216\n"
    "ds_read_b64_tr_b16 v[188:189], v83 offset:8192\n"
    "ds_read_b64_tr_b16 v[190:191], v83 offset:9216\n"
    "s_waitcnt vmcnt(12) lgkmcnt(0)\n"
    "s_barrier\n"
    "v_mfma_f32_16x16x32_bf16 v[192:195], v[144:147], v[128:131], v[192:195]\n"
    "v_lshlrev_b32 v111, 7, v111\n"
    "v_mfma_f32_16x16x32_bf16 v[192:195], v[176:179], v[160:163], v[192:195]\n"
    "v_mfma_f32_16x16x32_bf16 v[196:199], v[176:179], v[164:167], v[196:199]\n"
    "v_add_u32_dpp v52, v110, v48 quad_perm:[0,0,0,0] row_mask:0xf bank_mask:0xf\n"
    "v_mfma_f32_16x16x32_bf16 v[196:199], v[144:147], v[132:135], v[196:199]\n"
    "v_mfma_f32_16x16x32_bf16 v[200:203], v[144:147], v[136:139], v[200:203]\n"
    "v_add_u32_dpp v53, v110, v49 quad_perm:[1,1,1,1] row_mask:0xf bank_mask:0xf\n"
    "v_mfma_f32_16x16x32_bf16 v[200:203], v[176:179], v[168:171], v[200:203]\n"
    "v_mfma_f32_16x16x32_bf16 v[204:207], v[176:179], v[172:175], v[204:207]\n"
    "v_add_u32_dpp v54, v110, v50 quad_perm:[2,2,2,2] row_mask:0xf bank_mask:0xf\n"
    "v_mfma_f32_16x16x32_bf16 v[204:207], v[144:147], v[140:143], v[204:207]\n"
    "v_mfma_f32_16x16x32_bf16 v[220:223], v[148:151], v[140:143], v[220:223]\n"
    "v_add_u32_dpp v55, v110, v51 quad_perm:[3,3,3,3] row_mask:0xf bank_mask:0xf\n"
    "v_mfma_f32_16x16x32_bf16 v[220:223], v[180:183], v[172:175], v[220:223]\n"
    "v_mfma_f32_16x16x32_bf16 v[216:219], v[180:183], v[168:171], v[216:219]\n"
    "v_mfma_f32_16x16x32_bf16 v[216:219], v[148:151], v[136:139], v[216:219]\n"
    "v_readfirstlane_b32 s40, v24\n"
    "v_mfma_f32_16x16x32_bf16 v[212:215], v[148:151], v[132:135], v[212:215]\n"
    "v_mfma_f32_16x16x32_bf16 v[212:215], v[180:183], v[164:167], v[212:215]\n"
    "s_or_b32 s40, s40, s44\n"
    "v_mfma_f32_16x16x32_bf16 v[208:211], v[180:183], v[160:163], v[208:211]\n"
    "v_mfma_f32_16x16x32_bf16 v[208:211], v[148:151], v[128:131], v[208:211]\n"
    "s_lshl1_add_u32 s41, s40, s54\n"
    "v_mfma_f32_16x16x32_bf16 v[224:227], v[152:155], v[128:131], v[224:227]\n"
    "v_mfma_f32_16x16x32_bf16 v[224:227], v[184:187], v[160:163], v[224:227]\n"
    "s_cmpk_lt_u32 s40, 256\n"
    "v_mfma_f32_16x16x32_bf16 v[228:231], v[184:187], v[164:167], v[228:231]\n"
    "v_mfma_f32_16x16x32_bf16 v[228:231], v[152:155], v[132:135], v[228:231]\n"
    "s_cselect_b32 s41, s41, 2\n"
    "v_mfma_f32_16x16x32_bf16 v[232:235], v[152:155], v[136:139], v[232:235]\n"
    "v_mfma_f32_16x16x32_bf16 v[232:235], v[184:187], v[168:171], v[232:235]\n"
    "v_mfma_f32_16x16x32_bf16 v[236:239], v[184:187], v[172:175], v[236:239]\n"
    "v_mov_b32 v24, s41\n"
    "v_mfma_f32_16x16x32_bf16 v[236:239], v[152:155], v[140:143], v[236:239]\n"
    "v_mfma_f32_16x16x32_bf16 v[252:255], v[156:159], v[140:143], v[252:255]\n"
    "s_mul_i32 s41, s41, 52\n"
    "v_mfma_f32_16x16x32_bf16 v[252:255], v[188:191], v[172:175], v[252:255]\n"
    "v_mfma_f32_16x16x32_bf16 v[248:251], v[188:191], v[168:171], v[248:251]\n"
    "s_add_u32 s38, s4, s41\n"
    "v_mfma_f32_16x16x32_bf16 v[248:251], v[156:159], v[136:139], v[248:251]\n"
    "v_mfma_f32_16x16x32_bf16 v[244:247], v[156:159], v[132:135], v[244:247]\n"
    "s_addc_u32 s39, s5, 0\n"
    "v_mfma_f32_16x16x32_bf16 v[244:247], v[188:191], v[164:167], v[244:247]\n"
    "v_mfma_f32_16x16x32_bf16 v[240:243], v[188:191], v[160:163], v[240:243]\n"
    "v_mfma_f32_16x16x32_bf16 v[240:243], v[156:159], v[128:131], v[240:243]\n"
    "s_waitcnt vmcnt(8)\n"
    "s_barrier\n"
    "s_add_u32 m0, s35, 98304\n"
    "s_nop 0\n"
    "global_load_lds_dwordx4 v46, s[30:31] offset:0\n"
    "ds_read_b128 v[128:131], v56 offset:0\n"
    "ds_read_b128 v[132:135], v56 offset:2048\n"
    "ds_read_b128 v[136:139], v56 offset:4096\n"
    "global_load_lds_dwordx4 v47, s[30:31] offset:1024\n"
    "s_add_u32 s66, s28, 5\n"
    "s_and_b32 s66, s66, 7\n"
    "s_mul_i32 s66, s66, 638976\n"
    "s_add_u32 s30, s6, s66\n"
    "s_addc_u32 s31, s7, 0\n"
    "ds_read_b128 v[140:143], v56 offset:6144\n"
    "ds_read_b64_tr_b16 v[144:145], v76 offset:0\n"
    "ds_read_b64_tr_b16 v[146:147], v76 offset:1024\n"
    "s_add_u32 m0, s34, 98304\n"
    "s_nop 0\n"
    "global_load_lds_dwordx4 v52, s[36:37] offset:0\n"
    "ds_read_b64_tr_b16 v[148:149], v77 offset:0\n"
    "ds_read_b64_tr_b16 v[150:151], v77 offset:1024\n"
    "ds_read_b64_tr_b16 v[152:153], v78 offset:0\n"
    "global_load_lds_dwordx4 v53, s[36:37] offset:1024\n"
    "ds_read_b64_tr_b16 v[154:155], v78 offset:1024\n"
    "ds_read_b64_tr_b16 v[156:157], v79 offset:0\n"
    "ds_read_b64_tr_b16 v[158:159], v79 offset:1024\n"
    "global_load_lds_dwordx4 v54, s[36:37] offset:2048\n"
    "ds_read_b128 v[160:163], v57 offset:0\n"
    "ds_read_b128 v[164:167], v57 offset:2048\n"
    "ds_read_b128 v[168:171], v57 offset:4096\n"
    "global_load_lds_dwordx4 v55, s[36:37] offset:3072\n"
    "ds_read_b128 v[172:175], v57 offset:6144\n"
    "ds_read_b64_tr_b16 v[176:177], v76 offset:8192\n"
    "ds_read_b64_tr_b16 v[178:179], v76 offset:9216\n"
    "global_load_dwordx3 v[36:38], v17, s[38:39] offset:-104\n"
    "ds_read_b64_tr_b16 v[180:181], v77 offset:8192\n"
    "ds_read_b64_tr_b16 v[182:183], v77 offset:9216\n"
    "ds_read_b64_tr_b16 v[184:185], v78 offset:8192\n"
    "ds_read_b64_tr_b16 v[186:187], v78 offset:9216\n"
    "ds_read_b64_tr_b16 v[188:189], v79 offset:8192\n"
    "ds_read_b64_tr_b16 v[190:191], v79 offset:9216\n"
    "s_waitcnt vmcnt(12) lgkmcnt(0)\n"
    "s_barrier\n"
    "v_mfma_f32_16x16x32_bf16 v[192:195], v[144:147], v[128:131], v[192:195]\n"
    "v_lshlrev_b32 v112, 7, v112\n"
    "v_mfma_f32_16x16x32_bf16 v[192:195], v[176:179], v[160:163], v[192:195]\n"
    "v_add_u32_dpp v52, v111, v48 quad_perm:[0,0,0,0] row_mask:0xf bank_mask:0xf\n"
    "v_mfma_f32_16x16x32_bf16 v[196:199], v[176:179], v[164:167], v[196:199]\n"
    "v_add_u32_dpp v53, v111, v49 quad_perm:[1,1,1,1] row_mask:0xf bank_mask:0xf\n"
    "v_mfma_f32_16x16x32_bf16 v[196:199], v[144:147], v[132:135], v[196:199]\n"
    "v_add_u32_dpp v54, v111, v50 quad_perm:[2,2,2,2] row_mask:0xf bank_mask:0xf\n"
    "v_mfma_f32_16x16x32_bf16 v[200:203], v[144:147], v[136:139], v[200:203]\n"
    "v_add_u32_dpp v55, v111, v51 quad_perm:[3,3,3,3] row_mask:0xf bank_mask:0xf\n"
    "v_mfma_f32_16x16x32_bf16 v[200:203], v[176:179], v[168:171], v[200:203]\n"
    "v_lshlrev_b32 v60, 16, v84\n"
    "v_mfma_f32_16x16x32_bf16 v[204:207], v[176:179], v[172:175], v[204:207]\n"
    "v_and_b32 v61, 0xffff0000, v84\n"
    "v_mfma_f32_16x16x32_bf16 v[204:207], v[144:147], v[140:143], v[204:207]\n"
    "v_mfma_f32_16x16x32_bf16 v[220:223], v[148:151], v[140:143], v[220:223]\n"
    "v_lshlrev_b32 v62, 16, v85\n"
    "v_mfma_f32_16x16x32_bf16 v[220:223], v[180:183], v[172:175], v[220:223]\n"
    "v_and_b32 v63, 0xffff0000, v85\n"
    "v_mfma_f32_16x16x32_bf16 v[216:219], v[180:183], v[168:171], v[216:219]\n"
    "v_lshlrev_b32 v64, 16, v86\n"
    "v_mfma_f32_16x16x32_bf16 v[216:219], v[148:151], v[136:139], v[216:219]\n"
    "v_and_b32 v65, 0xffff0000, v86\n"
    "v_mfma_f32_16x16x32_bf16 v[212:215], v[148:151], v[132:135], v[212:215]\n"
    "v_lshlrev_b32 v66, 16, v87\n"
    "v_mfma_f32_16x16x32_bf16 v[212:215], v[180:183], v[164:167], v[212:215]\n"
    "v_and_b32 v67, 0xffff0000, v87\n"
    "v_mfma_f32_16x16x32_bf16 v[208:211], v[180:183], v[160:163], v[208:211]\n"
    "v_lshlrev_b32 v68, 16, v88\n"
    "v_mfma_f32_16x16x32_bf16 v[208:211], v[148:151], v[128:131], v[208:211]\n"
    "v_mfma_f32_16x16x32_bf16 v[224:227], v[152:155], v[128:131], v[224:227]\n"
    "v_and_b32 v69, 0xffff0000, v88\n"
    "v_mfma_f32_16x16x32_bf16 v[224:227], v[184:187], v[160:163], v[224:227]\n"
    "v_lshlrev_b32 v70, 16, v89\n"
    "v_mfma_f32_16x16x32_bf16 v[228:231], v[184:187], v[164:167], v[228:231]\n"
    "v_and_b32 v71, 0xffff0000, v89\n"
    "v_mfma_f32_16x16x32_bf16 v[228:231], v[152:155], v[132:135], v[228:231]\n"
    "v_lshlrev_b32 v72, 16, v90\n"
    "v_mfma_f32_16x16x32_bf16 v[232:235], v[152:155], v[136:139], v[232:235]\n"
    "v_and_b32 v73, 0xffff0000, v90\n"
    "v_mfma_f32_16x16x32_bf16 v[232:235], v[184:187], v[168:171], v[232:235]\n"
    "v_lshlrev_b32 v74, 16, v91\n"
    "v_mfma_f32_16x16x32_bf16 v[236:239], v[184:187], v[172:175], v[236:239]\n"
    "v_and_b32 v75, 0xffff0000, v91\n"
    "v_mfma_f32_16x16x32_bf16 v[236:239], v[152:155], v[140:143], v[236:239]\n"
    "v_mfma_f32_16x16x32_bf16 v[252:255], v[156:159], v[140:143], v[252:255]\n"
    "s_lshl3_add_u32 s40, s2, 4096\n"
    "v_mfma_f32_16x16x32_bf16 v[252:255], v[188:191], v[172:175], v[252:255]\n"
    "s_lshl_b32 s41, s2, 2\n"
    "v_mfma_f32_16x16x32_bf16 v[248:251], v[188:191], v[168:171], v[248:251]\n"
    "v_lshlrev_b32 v11, 3, v1\n"
    "v_mfma_f32_16x16x32_bf16 v[248:251], v[156:159], v[136:139], v[248:251]\n"
    "v_add_u32 v11, 40960, v11\n"
    "v_mfma_f32_16x16x32_bf16 v[244:247], v[156:159], v[132:135], v[244:247]\n"
    "v_writelane_b32 v11, s40, 8\n"
    "v_mfma_f32_16x16x32_bf16 v[244:247], v[188:191], v[164:167], v[244:247]\n"
    "v_writelane_b32 v11, s41, 9\n"
    "v_mfma_f32_16x16x32_bf16 v[240:243], v[188:191], v[160:163], v[240:243]\n"
    "v_mfma_f32_16x16x32_bf16 v[240:243], v[156:159], v[128:131], v[240:243]\n"
    "s_waitcnt vmcnt(8)\n"
    "s_barrier\n"
    "s_add_u32 m0, s35, 0\n"
    "s_nop 0\n"
    "global_load_lds_dwordx4 v46, s[30:31] offset:0\n"
    "ds_read_b128 v[128:131], v56 offset:49152\n"
    "ds_read_b128 v[132:135], v56 offset:51200\n"
    "ds_read_b128 v[136:139], v56 offset:53248\n"
    "global_load_lds_dwordx4 v47, s[30:31] offset:1024\n"
    "s_add_u32 s30, s30, 212992\n"
    "s_addc_u32 s31, s31, 0\n"
    "ds_read_b128 v[140:143], v56 offset:55296\n"
    "ds_read_b64_tr_b16 v[144:145], v76 offset:49152\n"
    "ds_read_b64_tr_b16 v[146:147], v76 offset:50176\n"
    "s_add_u32 m0, s34, 0\n"
    "s_nop 0\n"
    "global_load_lds_dwordx4 v52, s[36:37] offset:0\n"
    "ds_read_b64_tr_b16 v[148:149], v77 offset:49152\n"
    "ds_read_b64_tr_b16 v[150:151], v77 offset:50176\n"
    "ds_read_b64_tr_b16 v[152:153], v78 offset:49152\n"
    "global_load_lds_dwordx4 v53, s[36:37] offset:1024\n"
    "ds_read_b64_tr_b16 v[154:155], v78 offset:50176\n"
    "ds_read_b64_tr_b16 v[156:157], v79 offset:49152\n"
    "ds_read_b64_tr_b16 v[158:159], v79 offset:50176\n"
    "global_load_lds_dwordx4 v54, s[36:37] offset:2048\n"
    "ds_read_b128 v[160:163], v57 offset:49152\n"
    "ds_read_b128 v[164:167], v57 offset:51200\n"
    "ds_read_b128 v[168:171], v57 offset:53248\n"
    "global_load_lds_dwordx4 v55, s[36:37] offset:3072\n"
    "ds_read_b128 v[172:175], v57 offset:55296\n"
    "ds_read_b64_tr_b16 v[176:177], v76 offset:57344\n"
    "ds_read_b64_tr_b16 v[178:179], v76 offset:58368\n"
    "global_load_dword v41, v18, s[38:39] offset:-8\n"
    "ds_read_b64_tr_b16 v[180:181], v77 offset:57344\n"
    "ds_read_b64_tr_b16 v[182:183], v77 offset:58368\n"
    "ds_read_b64_tr_b16 v[184:185], v78 offset:57344\n"
    "ds_read_b64_tr_b16 v[186:187], v78 offset:58368\n"
    "ds_read_b64_tr_b16 v[188:189], v79 offset:57344\n"
    "ds_read_b64_tr_b16 v[190:191], v79 offset:58368\n"
    "s_waitcnt vmcnt(12) lgkmcnt(0)\n"
    "s_barrier\n"
    "v_mfma_f32_16x16x32_bf16 v[192:195], v[144:147], v[128:131], v[192:195]\n"
    "v_mfma_f32_16x16x32_bf16 v[192:195], v[176:179], v[160:163], v[192:195]\n"
    "v_lshlrev_b32 v113, 7, v113\n"
    "v_mfma_f32_16x16x32_bf16 v[196:199], v[176:179], v[164:167], v[196:199]\n"
    "v_mfma_f32_16x16x32_bf16 v[196:199], v[144:147], v[132:135], v[196:199]\n"
    "v_add_u32_dpp v52, v112, v48 quad_perm:[0,0,0,0] row_mask:0xf bank_mask:0xf\n"
    "v_mfma_f32_16x16x32_bf16 v[200:203], v[144:147], v[136:139], v[200:203]\n"
    "v_mfma_f32_16x16x32_bf16 v[200:203], v[176:179], v[168:171], v[200:203]\n"
    "v_add_u32_dpp v53, v112, v49 quad_perm:[1,1,1,1] row_mask:0xf bank_mask:0xf\n"
    "v_mfma_f32_16x16x32_bf16 v[204:207], v[176:179], v[172:175], v[204:207]\n"
    "v_mfma_f32_16x16x32_bf16 v[204:207], v[144:147], v[140:143], v[204:207]\n"
    "v_add_u32_dpp v54, v112, v50 quad_perm:[2,2,2,2] row_mask:0xf bank_mask:0xf\n"
    "v_mfma_f32_16x16x32_bf16 v[220:223], v[148:151], v[140:143], v[220:223]\n"
    "v_mfma_f32_16x16x32_bf16 v[220:223], v[180:183], v[172:175], v[220:223]\n"
    "v_add_u32_dpp v55, v112, v51 quad_perm:[3,3,3,3] row_mask:0xf bank_mask:0xf\n"
    "v_mfma_f32_16x16x32_bf16 v[216:219], v[180:183], v[168:171], v[216:219]\n"
    "v_mfma_f32_16x16x32_bf16 v[216:219], v[148:151], v[136:139], v[216:219]\n"
    "v_mfma_f32_16x16x32_bf16 v[212:215], v[148:151], v[132:135], v[212:215]\n"
    "v_mfma_f32_16x16x32_bf16 v[212:215], v[180:183], v[164:167], v[212:215]\n"
    "v_mfma_f32_16x16x32_bf16 v[208:211], v[180:183], v[160:163], v[208:211]\n"
    "v_add_f32 v192, v192, v60\n"
    "v_mfma_f32_16x16x32_bf16 v[208:211], v[148:151], v[128:131], v[208:211]\n"
    "v_add_f32 v193, v193, v61\n"
    "v_mfma_f32_16x16x32_bf16 v[224:227], v[152:155], v[128:131], v[224:227]\n"
    "v_add_f32 v194, v194, v62\n"
    "v_mfma_f32_16x16x32_bf16 v[224:227], v[184:187], v[160:163], v[224:227]\n"
    "v_add_f32 v195, v195, v63\n"
    "v_mfma_f32_16x16x32_bf16 v[228:231], v[184:187], v[164:167], v[228:231]\n"
    "v_add_f32 v196, v196, v60\n"
    "v_mfma_f32_16x16x32_bf16 v[228:231], v[152:155], v[132:135], v[228:231]\n"
    "v_add_f32 v197, v197, v61\n"
    "v_mfma_f32_16x16x32_bf16 v[232:235], v[152:155], v[136:139], v[232:235]\n"
    "v_add_f32 v198, v198, v62\n"
    "v_mfma_f32_16x16x32_bf16 v[232:235], v[184:187], v[168:171], v[232:235]\n"
    "v_add_f32 v199, v199, v63\n"
    "v_mfma_f32_16x16x32_bf16 v[236:239], v[184:187], v[172:175], v[236:239]\n"
    "v_add_f32 v200, v200, v60\n"
    "v_mfma_f32_16x16x32_bf16 v[236:239], v[152:155], v[140:143], v[236:239]\n"
    "v_add_f32 v201, v201, v61\n"
    "v_mfma_f32_16x16x32_bf16 v[252:255], v[156:159], v[140:143], v[252:255]\n"
    "v_add_f32 v202, v202, v62\n"
    "v_mfma_f32_16x16x32_bf16 v[252:255], v[188:191], v[172:175], v[252:255]\n"
    "v_add_f32 v203, v203, v63\n"
    "v_mfma_f32_16x16x32_bf16 v[248:251], v[188:191], v[168:171], v[248:251]\n"
    "v_add_f32 v204, v204, v60\n"
    "v_mfma_f32_16x16x32_bf16 v[248:251], v[156:159], v[136:139], v[248:251]\n"
    "v_add_f32 v205, v205, v61\n"
    "v_mfma_f32_16x16x32_bf16 v[244:247], v[156:159], v[132:135], v[244:247]\n"
    "v_add_f32 v206, v206, v62\n"
    "v_mfma_f32_16x16x32_bf16 v[244:247], v[188:191], v[164:167], v[244:247]\n"
    "v_add_f32 v207, v207, v63\n"
    "v_mfma_f32_16x16x32_bf16 v[240:243], v[188:191], v[160:163], v[240:243]\n"
    "v_mfma_f32_16x16x32_bf16 v[240:243], v[156:159], v[128:131], v[240:243]\n"
    "s_waitcnt vmcnt(8)\n"
    "s_barrier\n"
    "s_cmp_eq_u32 s20, 3\n"
    "s_cbranch_scc1 .Lxd_on1\n"
    ".Lxd_on1r:\n"
    "s_add_u32 m0, s35, 49152\n"
    "s_nop 0\n"
    "global_load_lds_dwordx4 v46, s[30:31] offset:0\n"
    "ds_read_b128 v[128:131], v58 offset:0\n"
    "ds_read_b128 v[132:135], v58 offset:2048\n"
    "ds_read_b128 v[136:139], v58 offset:4096\n"
    "ds_read_b128 v[140:143], v58 offset:6144\n"
    "global_load_lds_dwordx4 v47, s[30:31] offset:1024\n"
    "s_add_u32 s30, s30, 212992\n"
    "s_addc_u32 s31, s31, 0\n"
    "ds_read_b64_tr_b16 v[144:145], v80 offset:0\n"
    "ds_read_b64_tr_b16 v[146:147], v80 offset:1024\n"
    "ds_read_b64_tr_b16 v[148:149], v81 offset:0\n"
    "ds_read_b64_tr_b16 v[150:151], v81 offset:1024\n"
    "s_add_u32 m0, s34, 49152\n"
    "s_nop 0\n"
    "global_load_lds_dwordx4 v52, s[36:37] offset:0\n"
    "ds_read_b64_tr_b16 v[152:153], v82 offset:0\n"
    "ds_read_b64_tr_b16 v[154:155], v82 offset:1024\n"
    "ds_read_b64_tr_b16 v[156:157], v83 offset:0\n"
    "ds_read_b64_tr_b16 v[158:159], v83 offset:1024\n"
    "global_load_lds_dwordx4 v53, s[36:37] offset:1024\n"
    "ds_read_b128 v[160:163], v59 offset:0\n"
    "ds_read_b128 v[164:167], v59 offset:2048\n"
    "ds_read_b128 v[168:171], v59 offset:4096\n"
    "ds_read_b128 v[172:175], v59 offset:6144\n"
    "global_load_lds_dwordx4 v54, s[36:37] offset:2048\n"
    "ds_read_b64_tr_b16 v[176:177], v80 offset:8192\n"
    "ds_read_b64_tr_b16 v[178:179], v80 offset:9216\n"
    "ds_read_b64_tr_b16 v[180:181], v81 offset:8192\n"
    "ds_read_b64_tr_b16 v[182:183], v81 offset:9216\n"
    "global_load_lds_dwordx4 v55, s[36:37] offset:3072\n"
    "ds_read_b64_tr_b16 v[184:185], v82 offset:8192\n"
    "ds_read_b64_tr_b16 v[186:187], v82 offset:9216\n"
    "ds_read_b64_tr_b16 v[188:189], v83 offset:8192\n"
    "ds_read_b64_tr_b16 v[190:191], v83 offset:9216\n"
    "s_waitcnt vmcnt(11) lgkmcnt(0)\n"
    "s_barrier\n"
    "v_mfma_f32_16x16x32_bf16 v[192:195], v[144:147], v[128:131], v[192:195]\n"
    "v_mfma_f32_16x16x32_bf16 v[192:195], v[176:179], v[160:163], v[192:195]\n"
    "v_lshlrev_b32 v114, 7, v114\n"
    "v_mfma_f32_16x16x32_bf16 v[196:199], v[176:179], v[164:167], v[196:199]\n"
    "v_mfma_f32_16x16x32_bf16 v[196:199], v[144:147], v[132:135], v[196:199]\n"
    "v_add_u32_dpp v52, v113, v48 quad_perm:[0,0,0,0] row_mask:0xf bank_mask:0xf\n"
    "v_mfma_f32_16x16x32_bf16 v[200:203], v[144:147], v[136:139], v[200:203]\n"
    "v_mfma_f32_16x16x32_bf16 v[200:203], v[176:179], v[168:171], v[200:203]\n"
    "v_add_u32_dpp v53, v113, v49 quad_perm:[1,1,1,1] row_mask:0xf bank_mask:0xf\n"
    "v_mfma_f32_16x16x32_bf16 v[204:207], v[176:179], v[172:175], v[204:207]\n"
    "v_mfma_f32_16x16x32_bf16 v[204:207], v[144:147], v[140:143], v[204:207]\n"
    "v_add_u32_dpp v54, v113, v50 quad_perm:[2,2,2,2] row_mask:0xf bank_mask:0xf\n"
    "v_mfma_f32_16x16x32_bf16 v[220:223], v[148:151], v[140:143], v[220:223]\n"
    "v_mfma_f32_16x16x32_bf16 v[220:223], v[180:183], v[172:175], v[220:223]\n"
    "v_add_u32_dpp v55, v113, v51 quad_perm:[3,3,3,3] row_mask:0xf bank_mask:0xf\n"
    "v_mfma_f32_16x16x32_bf16 v[216:219], v[180:183], v[168:171], v[216:219]\n"
    "v_mfma_f32_16x16x32_bf16 v[216:219], v[148:151], v[136:139], v[216:219]\n"
    "v_mfma_f32_16x16x32_bf16 v[212:215], v[148:151], v[132:135], v[212:215]\n"
    "v_mfma_f32_16x16x32_bf16 v[212:215], v[180:183], v[164:167], v[212:215]\n"
    "v_mfma_f32_16x16x32_bf16 v[208:211], v[180:183], v[160:163], v[208:211]\n"
    "v_mfma_f32_16x16x32_bf16 v[208:211], v[148:151], v[128:131], v[208:211]\n"
    "v_mfma_f32_16x16x32_bf16 v[224:227], v[152:155], v[128:131], v[224:227]\n"
    "v_mfma_f32_16x16x32_bf16 v[224:227], v[184:187], v[160:163], v[224:227]\n"
    "v_mfma_f32_16x16x32_bf16 v[228:231], v[184:187], v[164:167], v[228:231]\n"
    "v_add_f32 v222, v222, v66\n"
    "v_add_f32 v223, v223, v67\n"
    "v_mfma_f32_16x16x32_bf16 v[228:231], v[152:155], v[132:135], v[228:231]\n"
    "v_add_f32 v221, v221, v65\n"
    "v_mfma_f32_16x16x32_bf16 v[232:235], v[152:155], v[136:139], v[232:235]\n"
    "v_add_f32 v220, v220, v64\n"
    "v_mfma_f32_16x16x32_bf16 v[232:235], v[184:187], v[168:171], v[232:235]\n"
    "v_add_f32 v219, v219, v67\n"
    "v_mfma_f32_16x16x32_bf16 v[236:239], v[184:187], v[172:175], v[236:239]\n"
    "v_add_f32 v217, v217, v65\n"
    "v_add_f32 v218, v218, v66\n"
    "v_mfma_f32_16x16x32_bf16 v[236:239], v[152:155], v[140:143], v[236:239]\n"
    "v_add_f32 v216, v216, v64\n"
    "v_mfma_f32_16x16x32_bf16 v[252:255], v[156:159], v[140:143], v[252:255]\n"
    "v_add_f32 v215, v215, v67\n"
    "v_mfma_f32_16x16x32_bf16 v[252:255], v[188:191], v[172:175], v[252:255]\n"
    "v_add_f32 v214, v214, v66\n"
    "v_mfma_f32_16x16x32_bf16 v[248:251], v[188:191], v[168:171], v[248:251]\n"
    "v_add_f32 v212, v212, v64\n"
    "v_add_f32 v213, v213, v65\n"
    "v_mfma_f32_16x16x32_bf16 v[248:251], v[156:159], v[136:139], v[248:251]\n"
    "v_add_f32 v211, v211, v67\n"
    "v_mfma_f32_16x16x32_bf16 v[244:247], v[156:159], v[132:135], v[244:247]\n"
    "v_add_f32 v210, v210, v66\n"
    "v_mfma_f32_16x16x32_bf16 v[244:247], v[188:191], v[164:167], v[244:247]\n"
    "v_add_f32 v209, v209, v65\n"
    "v_mfma_f32_16x16x32_bf16 v[240:243], v[188:191], v[160:163], v[240:243]\n"
    "v_add_f32 v208, v208, v64\n"
    "v_mfma_f32_16x16x32_bf16 v[240:243], v[156:159], v[128:131], v[240:243]\n"
    "s_waitcnt vmcnt(7)\n"
    "s_barrier\n"
    "s_add_u32 m0, s35, 98304\n"
    "s_nop 0\n"
    "global_load_lds_dwordx4 v46, s[30:31] offset:0\n"
    "ds_read_b128 v[128:131], v56 offset:0\n"
    "ds_read_b128 v[132:135], v56 offset:2048\n"
    "ds_read_b128 v[136:139], v56 offset:4096\n"
    "ds_read_b128 v[140:143], v56 offset:6144\n"
    "global_load_lds_dwordx4 v47, s[30:31] offset:1024\n"
    "s_add_u32 s66, s28, 6\n"
    "s_and_b32 s66, s66, 7\n"
    "s_mul_i32 s66, s66, 638976\n"
    "s_add_u32 s30, s6, s66\n"
    "s_addc_u32 s31, s7, 0\n"
    "ds_read_b64_tr_b16 v[144:145], v76 offset:0\n"
    "ds_read_b64_tr_b16 v[146:147], v76 offset:1024\n"
    "ds_read_b64_tr_b16 v[148:149], v77 offset:0\n"
    "ds_read_b64_tr_b16 v[150:151], v77 offset:1024\n"
    "s_add_u32 m0, s34, 98304\n"
    "s_nop 0\n"
    "global_load_lds_dwordx4 v52, s[36:37] offset:0\n"
    "ds_read_b64_tr_b16 v[152:153], v78 offset:0\n"
    "ds_read_b64_tr_b16 v[154:155], v78 offset:1024\n"
    "ds_read_b64_tr_b16 v[156:157], v79 offset:0\n"
    "ds_read_b64_tr_b16 v[158:159], v79 offset:1024\n"
    "global_load_lds_dwordx4 v53, s[36:37] offset:1024\n"
    "ds_read_b128 v[160:163], v57 offset:0\n"
    "ds_read_b128 v[164:167], v57 offset:2048\n"
    "ds_read_b128 v[168:171], v57 offset:4096\n"
    "ds_read_b128 v[172:175], v57 offset:6144\n"
    "global_load_lds_dwordx4 v54, s[36:37] offset:2048\n"
    "ds_read_b64_tr_b16 v[176:177], v76 offset:8192\n"
    "ds_read_b64_tr_b16 v[178:179], v76 offset:9216\n"
    "ds_read_b64_tr_b16 v[180:181], v77 offset:8192\n"
    "ds_read_b64_tr_b16 v[182:183], v77 offset:9216\n"
    "global_load_lds_dwordx4 v55, s[36:37] offset:3072\n"
    "ds_read_b64_tr_b16 v[184:185], v78 offset:8192\n"
    "ds_read_b64_tr_b16 v[186:187], v78 offset:9216\n"
    "ds_read_b64_tr_b16 v[188:189], v79 offset:8192\n"
    "ds_read_b64_tr_b16 v[190:191], v79 offset:9216\n"
    "s_waitcnt vmcnt(10) lgkmcnt(0)\n"
    "s_barrier\n"
    "v_mfma_f32_16x16x32_bf16 v[192:195], v[144:147], v[128:131], v[192:195]\n"
    "v_lshlrev_b32 v115, 7, v115\n"
    "v_mfma_f32_16x16x32_bf16 v[192:195], v[176:179], v[160:163], v[192:195]\n"
    "v_add_u32_dpp v52, v114, v48 quad_perm:[0,0,0,0] row_mask:0xf bank_mask:0xf\n"
    "v_mfma_f32_16x16x32_bf16 v[196:199], v[176:179], v[164:167], v[196:199]\n"
    "v_add_u32_dpp v53, v114, v49 quad_perm:[1,1,1,1] row_mask:0xf bank_mask:0xf\n"
    "v_mfma_f32_16x16x32_bf16 v[196:199], v[144:147], v[132:135], v[196:199]\n"
    "v_add_u32_dpp v54, v114, v50 quad_perm:[2,2,2,2] row_mask:0xf bank_mask:0xf\n"
    "v_mfma_f32_16x16x32_bf16 v[200:203], v[144:147], v[136:139], v[200:203]\n"
    "v_add_u32_dpp v55, v114, v51 quad_perm:[3,3,3,3] row_mask:0xf bank_mask:0xf\n"
    "v_mfma_f32_16x16x32_bf16 v[200:203], v[176:179], v[168:171], v[200:203]\n"
    "v_lshl_or_b32 v36, v36, 7, v19\n"
    "v_mfma_f32_16x16x32_bf16 v[204:207], v[176:179], v[172:175], v[204:207]\n"
    "v_mfma_f32_16x16x32_bf16 v[204:207], v[144:147], v[140:143], v[204:207]\n"
    "v_lshl_or_b32 v37, v37, 7, v19\n"
    "v_mfma_f32_16x16x32_bf16 v[220:223], v[148:151], v[140:143], v[220:223]\n"
    "v_lshl_or_b32 v38, v38, 7, v19\n"
    "v_mfma_f32_16x16x32_bf16 v[220:223], v[180:183], v[172:175], v[220:223]\n"
    "v_lshl_or_b32 v41, v41, 7, v20\n"
    "v_mfma_f32_16x16x32_bf16 v[216:219], v[180:183], v[168:171], v[216:219]\n"
    "s_mov_b32 s64, 0xbdd2d3e8\n"
    "v_mfma_f32_16x16x32_bf16 v[216:219], v[148:151], v[136:139], v[216:219]\n"
    "s_mov_b32 s65, 0xc0135761\n"
    "v_mfma_f32_16x16x32_bf16 v[212:215], v[148:151], v[132:135], v[212:215]\n"
    "v_mfma_f32_16x16x32_bf16 v[212:215], v[180:183], v[164:167], v[212:215]\n"
    "s_and_b32 s41, s20, 1\n"
    "v_mfma_f32_16x16x32_bf16 v[208:211], v[180:183], v[160:163], v[208:211]\n"
    "s_lshl1_add_u32 s40, s21, s41\n"
    "v_mfma_f32_16x16x32_bf16 v[208:211], v[148:151], v[128:131], v[208:211]\n"
    "s_lshl_b32 s41, s40, 8\n"
    "v_mfma_f32_16x16x32_bf16 v[224:227], v[152:155], v[128:131], v[224:227]\n"
    "s_add_u32 s3, s41, 149504\n"
    "v_mfma_f32_16x16x32_bf16 v[224:227], v[184:187], v[160:163], v[224:227]\n"
    "s_lshl_b32 s40, s40, 6\n"
    "v_mfma_f32_16x16x32_bf16 v[228:231], v[184:187], v[164:167], v[228:231]\n"
    "v_mfma_f32_16x16x32_bf16 v[228:231], v[152:155], v[132:135], v[228:231]\n"
    "s_add_u32 s40, s40, s24\n"
    "v_mfma_f32_16x16x32_bf16 v[232:235], v[152:155], v[136:139], v[232:235]\n"
    "s_lshl_b32 s41, s23, 12\n"
    "v_mfma_f32_16x16x32_bf16 v[232:235], v[184:187], v[168:171], v[232:235]\n"
    "s_add_u32 s40, s40, s41\n"
    "v_mfma_f32_16x16x32_bf16 v[236:239], v[184:187], v[172:175], v[236:239]\n"
    "s_lshl3_add_u32 s40, s40, 65536\n"
    "v_mfma_f32_16x16x32_bf16 v[236:239], v[152:155], v[140:143], v[236:239]\n"
    "s_add_u32 s48, s40, s16\n"
    "v_mfma_f32_16x16x32_bf16 v[252:255], v[156:159], v[140:143], v[252:255]\n"
    "v_mfma_f32_16x16x32_bf16 v[252:255], v[188:191], v[172:175], v[252:255]\n"
    "s_addc_u32 s49, s17, 0\n"
    "v_mfma_f32_16x16x32_bf16 v[248:251], v[188:191], v[168:171], v[248:251]\n"
    "s_cmp_lt_u32 s22, 2\n"
    "v_mfma_f32_16x16x32_bf16 v[248:251], v[156:159], v[136:139], v[248:251]\n"
    "s_cselect_b32 s24, 16, 0\n"
    "v_mfma_f32_16x16x32_bf16 v[244:247], v[156:159], v[132:135], v[244:247]\n"
    "s_sub_u32 s23, 3, s22\n"
    "v_mfma_f32_16x16x32_bf16 v[244:247], v[188:191], v[164:167], v[244:247]\n"
    "s_and_b32 s23, s23, 3\n"
    "v_mfma_f32_16x16x32_bf16 v[240:243], v[188:191], v[160:163], v[240:243]\n"
    "v_mfma_f32_16x16x32_bf16 v[240:243], v[156:159], v[128:131], v[240:243]\n"
    "s_waitcnt vmcnt(6)\n"
    "s_barrier\n"
    "s_add_u32 m0, s35, 0\n"
    "s_nop 0\n"
    "global_load_lds_dwordx4 v46, s[30:31] offset:0\n"
    "ds_read_b128 v[128:131], v56 offset:49152\n"
    "ds_read_b128 v[132:135], v56 offset:51200\n"
    "ds_read_b128 v[136:139], v56 offset:53248\n"
    "global_load_lds_dwordx4 v47, s[30:31] offset:1024\n"
    "s_add_u32 s30, s30, 212992\n"
    "s_addc_u32 s31, s31, 0\n"
    "ds_read_b128 v[140:143], v56 offset:55296\n"
    "ds_read_b64_tr_b16 v[144:145], v76 offset:49152\n"
    "ds_read_b64_tr_b16 v[146:147], v76 offset:50176\n"
    "s_add_u32 m0, s34, 0\n"
    "s_nop 0\n"
    "global_load_lds_dwordx4 v52, s[36:37] offset:0\n"
    "ds_read_b64_tr_b16 v[148:149], v77 offset:49152\n"
    "ds_read_b64_tr_b16 v[150:151], v77 offset:50176\n"
    "ds_read_b64_tr_b16 v[152:153], v78 offset:49152\n"
    "global_load_lds_dwordx4 v53, s[36:37] offset:1024\n"
    "ds_read_b64_tr_b16 v[154:155], v78 offset:50176\n"
    "ds_read_b64_tr_b16 v[156:157], v79 offset:49152\n"
    "ds_read_b64_tr_b16 v[158:159], v79 offset:50176\n"
    "global_load_lds_dwordx4 v54, s[36:37] offset:2048\n"
    "ds_read_b128 v[160:163], v57 offset:49152\n"
    "ds_read_b128 v[164:167], v57 offset:51200\n"
    "ds_read_b128 v[168:171], v57 offset:53248\n"
    "global_load_lds_dwordx4 v55, s[36:37] offset:3072\n"
    "ds_read_b128 v[172:175], v57 offset:55296\n"
    "ds_read_b64_tr_b16 v[176:177], v76 offset:57344\n"
    "ds_read_b64_tr_b16 v[178:179], v76 offset:58368\n"
    "global_load_dwordx4 v[28:31], v36, s[8:9]\n"
    "ds_read_b64_tr_b16 v[180:181], v77 offset:57344\n"
    "ds_read_b64_tr_b16 v[182:183], v77 offset:58368\n"
    "ds_read_b64_tr_b16 v[184:185], v78 offset:57344\n"
    "ds_read_b64_tr_b16 v[186:187], v78 offset:58368\n"
    "ds_read_b64_tr_b16 v[188:189], v79 offset:57344\n"
    "ds_read_b64_tr_b16 v[190:191], v79 offset:58368\n"
    "s_waitcnt vmcnt(11) lgkmcnt(0)\n"
    "s_barrier\n"
    "v_mfma_f32_16x16x32_bf16 v[192:195], v[144:147], v[128:131], v[192:195]\n"
    "v_mfma_f32_16x16x32_bf16 v[192:195], v[176:179], v[160:163], v[192:195]\n"
    "v_add_f32 v224, v224, v68\n"
    "v_add_f32 v225, v225, v69\n"
    "v_mfma_f32_16x16x32_bf16 v[196:199], v[176:179], v[164:167], v[196:199]\n"
    "v_add_f32 v226, v226, v70\n"
    "v_mfma_f32_16x16x32_bf16 v[196:199], v[144:147], v[132:135], v[196:199]\n"
    "v_add_f32 v227, v227, v71\n"
    "v_mfma_f32_16x16x32_bf16 v[200:203], v[144:147], v[136:139], v[200:203]\n"
    "v_add_f32 v228, v228, v68\n"
    "v_mfma_f32_16x16x32_bf16 v[200:203], v[176:179], v[168:171], v[200:203]\n"
    "v_add_f32 v229, v229, v69\n"
    "v_add_f32 v230, v230, v70\n"
    "v_mfma_f32_16x16x32_bf16 v[204:207], v[176:179], v[172:175], v[204:207]\n"
    "v_add_f32 v231, v231, v71\n"
    "v_mfma_f32_16x16x32_bf16 v[204:207], v[144:147], v[140:143], v[204:207]\n"
    "v_add_f32 v232, v232, v68\n"
    "v_mfma_f32_16x16x32_bf16 v[220:223], v[148:151], v[140:143], v[220:223]\n"
    "v_add_f32 v233, v233, v69\n"
    "v_mfma_f32_16x16x32_bf16 v[220:223], v[180:183], v[172:175], v[220:223]\n"
    "v_add_f32 v234, v234, v70\n"
    "v_add_f32 v235, v235, v71\n"
    "v_mfma_f32_16x16x32_bf16 v[216:219], v[180:183], v[168:171], v[216:219]\n"
    "v_add_f32 v236, v236, v68\n"
    "v_mfma_f32_16x16x32_bf16 v[216:219], v[148:151], v[136:139], v[216:219]\n"
    "v_add_f32 v237, v237, v69\n"
    "v_mfma_f32_16x16x32_bf16 v[212:215], v[148:151], v[132:135], v[212:215]\n"
    "v_add_f32 v238, v238, v70\n"
    "v_mfma_f32_16x16x32_bf16 v[212:215], v[180:183], v[164:167], v[212:215]\n"
    "v_add_f32 v239, v239, v71\n"
    "v_mfma_f32_16x16x32_bf16 v[208:211], v[180:183], v[160:163], v[208:211]\n"
    "v_mfma_f32_16x16x32_bf16 v[208:211], v[148:151], v[128:131], v[208:211]\n"
    "v_mfma_f32_16x16x32_bf16 v[224:227], v[152:155], v[128:131], v[224:227]\n"
    "v_mfma_f32_16x16x32_bf16 v[224:227], v[184:187], v[160:163], v[224:227]\n"
    "v_mfma_f32_16x16x32_bf16 v[228:231], v[184:187], v[164:167], v[228:231]\n"
    "v_lshlrev_b32 v116, 7, v116\n"
    "v_mfma_f32_16x16x32_bf16 v[228:231], v[152:155], v[132:135], v[228:231]\n"
    "v_add_u32_dpp v52, v115, v48 quad_perm:[0,0,0,0] row_mask:0xf bank_mask:0xf\n"
    "v_mfma_f32_16x16x32_bf16 v[232:235], v[152:155], v[136:139], v[232:235]\n"
    "v_add_u32_dpp v53, v115, v49 quad_perm:[1,1,1,1] row_mask:0xf bank_mask:0xf\n"
    "v_mfma_f32_16x16x32_bf16 v[232:235], v[184:187], v[168:171], v[232:235]\n"
    "v_add_u32_dpp v54, v115, v50 quad_perm:[2,2,2,2] row_mask:0xf bank_mask:0xf\n"
    "v_mfma_f32_16x16x32_bf16 v[236:239], v[184:187], v[172:175], v[236:239]\n"
    "v_add_u32_dpp v55, v115, v51 quad_perm:[3,3,3,3] row_mask:0xf bank_mask:0xf\n"
    "v_mfma_f32_16x16x32_bf16 v[236:239], v[152:155], v[140:143], v[236:239]\n"
    "s_getpc_b64 s[10:11]\n"
    ".Lxd_en2:\n"
    "v_mfma_f32_16x16x32_bf16 v[252:255], v[156:159], v[140:143], v[252:255]\n"
    "s_mov_b32 s40, .Lxd_ph1-.Lxd_en2\n"
    "v_mfma_f32_16x16x32_bf16 v[252:255], v[188:191], v[172:175], v[252:255]\n"
    "s_cmp_eq_u32 s22, 1\n"
    "s_cselect_b32 s40, .Lxd_ph0-.Lxd_en2, s40\n"
    "v_mfma_f32_16x16x32_bf16 v[248:251], v[188:191], v[168:171], v[248:251]\n"
    "s_cmp_eq_u32 s22, 2\n"
    "s_cselect_b32 s40, .Lxd_pl3-.Lxd_en2, s40\n"
    "v_mfma_f32_16x16x32_bf16 v[248:251], v[156:159], v[136:139], v[248:251]\n"
    "s_cmp_eq_u32 s22, 3\n"
    "s_cselect_b32 s40, .Lxd_ph2-.Lxd_en2, s40\n"
    "v_mfma_f32_16x16x32_bf16 v[244:247], v[156:159], v[132:135], v[244:247]\n"
    "s_ashr_i32 s41, s40, 31\n"
    "v_mfma_f32_16x16x32_bf16 v[244:247], v[188:191], v[164:167], v[244:247]\n"
    "s_add_u32 s10, s10, s40\n"
    "s_addc_u32 s11, s11, s41\n"
    "v_mfma_f32_16x16x32_bf16 v[240:243], v[188:191], v[160:163], v[240:243]\n"
    "v_mfma_f32_16x16x32_bf16 v[240:243], v[156:159], v[128:131], v[240:243]\n"
    "s_waitcnt vmcnt(7)\n"
    "s_barrier\n"
    "s_add_u32 m0, s35, 49152\n"
    "s_nop 0\n"
    "global_load_lds_dwordx4 v46, s[30:31] offset:0\n"
    "ds_read_b128 v[128:131], v58 offset:0\n"
    "ds_read_b128 v[132:135], v58 offset:2048\n"
    "ds_read_b128 v[136:139], v58 offset:4096\n"
    "global_load_lds_dwordx4 v47, s[30:31] offset:1024\n"
    "s_add_u32 s30, s30, 212992\n"
    "s_addc_u32 s31, s31, 0\n"
    "ds_read_b128 v[140:143], v58 offset:6144\n"
    "ds_read_b64_tr_b16 v[144:145], v80 offset:0\n"
    "ds_read_b64_tr_b16 v[146:147], v80 offset:1024\n"
    "s_add_u32 m0, s34, 49152\n"
    "s_nop 0\n"
    "global_load_lds_dwordx4 v52, s[36:37] offset:0\n"
    "ds_read_b64_tr_b16 v[148:149], v81 offset:0\n"
    "ds_read_b64_tr_b16 v[150:151], v81 offset:1024\n"
    "ds_read_b64_tr_b16 v[152:153], v82 offset:0\n"
    "global_load_lds_dwordx4 v53, s[36:37] offset:1024\n"
    "ds_read_b64_tr_b16 v[154:155], v82 offset:1024\n"
    "ds_read_b64_tr_b16 v[156:157], v83 offset:0\n"
    "ds_read_b64_tr_b16 v[158:159], v83 offset:1024\n"
    "global_load_lds_dwordx4 v54, s[36:37] offset:2048\n"
    "ds_read_b128 v[160:163], v59 offset:0\n"
    "ds_read_b128 v[164:167], v59 offset:2048\n"
    "ds_read_b128 v[168:171], v59 offset:4096\n"
    "global_load_lds_dwordx4 v55, s[36:37] offset:3072\n"
    "ds_read_b128 v[172:175], v59 offset:6144\n"
    "ds_read_b64_tr_b16 v[176:177], v80 offset:8192\n"
    "ds_read_b64_tr_b16 v[178:179], v80 offset:9216\n"
    "global_load_dwordx4 v[32:35], v37, s[8:9]\n"
    "ds_read_b64_tr_b16 v[180:181], v81 offset:8192\n"
    "ds_read_b64_tr_b16 v[182:183], v81 offset:9216\n"
    "ds_read_b64_tr_b16 v[184:185], v82 offset:8192\n"
    "ds_read_b64_tr_b16 v[186:187], v82 offset:9216\n"
    "ds_read_b64_tr_b16 v[188:189], v83 offset:8192\n"
    "ds_read_b64_tr_b16 v[190:191], v83 offset:9216\n"
    "s_waitcnt vmcnt(12) lgkmcnt(0)\n"
    "s_barrier\n"
    "v_mfma_f32_16x16x32_bf16 v[192:195], v[144:147], v[128:131], v[192:195]\n"
    "v_mfma_f32_16x16x32_bf16 v[192:195], v[176:179], v[160:163], v[192:195]\n"
    "v_mfma_f32_16x16x32_bf16 v[196:199], v[176:179], v[164:167], v[196:199]\n"
    "v_mfma_f32_16x16x32_bf16 v[196:199], v[144:147], v[132:135], v[196:199]\n"
    "v_mfma_f32_16x16x32_bf16 v[200:203], v[144:147], v[136:139], v[200:203]\n"
    "v_add_f32 v240, v240, v72\n"
    "v_mfma_f32_16x16x32_bf16 v[200:203], v[176:179], v[168:171], v[200:203]\n"
    "v_add_f32 v241, v241, v73\n"
    "v_mfma_f32_16x16x32_bf16 v[204:207], v[176:179], v[172:175], v[204:207]\n"
    "v_add_f32 v242, v242, v74\n"
    "v_mfma_f32_16x16x32_bf16 v[204:207], v[144:147], v[140:143], v[204:207]\n"
    "v_add_f32 v243, v243, v75\n"
    "v_mfma_f32_16x16x32_bf16 v[220:223], v[148:151], v[140:143], v[220:223]\n"
    "v_add_f32 v244, v244, v72\n"
    "v_mfma_f32_16x16x32_bf16 v[220:223], v[180:183], v[172:175], v[220:223]\n"
    "v_add_f32 v245, v245, v73\n"
    "v_mfma_f32_16x16x32_bf16 v[216:219], v[180:183], v[168:171], v[216:219]\n"
    "v_add_f32 v246, v246, v74\n"
    "v_mfma_f32_16x16x32_bf16 v[216:219], v[148:151], v[136:139], v[216:219]\n"
    "v_add_f32 v247, v247, v75\n"
    "v_mfma_f32_16x16x32_bf16 v[212:215], v[148:151], v[132:135], v[212:215]\n"
    "v_add_f32 v248, v248, v72\n"
    "v_mfma_f32_16x16x32_bf16 v[212:215], v[180:183], v[164:167], v[212:215]\n"
    "v_add_f32 v249, v249, v73\n"
    "v_mfma_f32_16x16x32_bf16 v[208:211], v[180:183], v[160:163], v[208:211]\n"
    "v_add_f32 v250, v250, v74\n"
    "v_mfma_f32_16x16x32_bf16 v[208:211], v[148:151], v[128:131], v[208:211]\n"
    "v_add_f32 v251, v251, v75\n"
    "v_mfma_f32_16x16x32_bf16 v[224:227], v[152:155], v[128:131], v[224:227]\n"
    "v_add_f32 v252, v252, v72\n"
    "v_mfma_f32_16x16x32_bf16 v[224:227], v[184:187], v[160:163], v[224:227]\n"
    "v_add_f32 v253, v253, v73\n"
    "v_mfma_f32_16x16x32_bf16 v[228:231], v[184:187], v[164:167], v[228:231]\n"
    "v_lshlrev_b32 v117, 7, v117\n"
    "v_add_f32 v254, v254, v74\n"
    "v_mfma_f32_16x16x32_bf16 v[228:231], v[152:155], v[132:135], v[228:231]\n"
    "v_add_f32 v255, v255, v75\n"
    "v_mfma_f32_16x16x32_bf16 v[232:235], v[152:155], v[136:139], v[232:235]\n"
    "v_add_u32_dpp v52, v116, v48 quad_perm:[0,0,0,0] row_mask:0xf bank_mask:0xf\n"
    "v_mfma_f32_16x16x32_bf16 v[232:235], v[184:187], v[168:171], v[232:235]\n"
    "v_mfma_f32_16x16x32_bf16 v[236:239], v[184:187], v[172:175], v[236:239]\n"
    "v_mfma_f32_16x16x32_bf16 v[236:239], v[152:155], v[140:143], v[236:239]\n"
    "v_add_u32_dpp v53, v116, v49 quad_perm:[1,1,1,1] row_mask:0xf bank_mask:0xf\n"
    "v_mfma_f32_16x16x32_bf16 v[252:255], v[156:159], v[140:143], v[252:255]\n"
    "v_mfma_f32_16x16x32_bf16 v[252:255], v[188:191], v[172:175], v[252:255]\n"
    "v_add_u32_dpp v54, v116, v50 quad_perm:[2,2,2,2] row_mask:0xf bank_mask:0xf\n"
    "v_mfma_f32_16x16x32_bf16 v[248:251], v[188:191], v[168:171], v[248:251]\n"
    "v_mfma_f32_16x16x32_bf16 v[248:251], v[156:159], v[136:139], v[248:251]\n"
    "v_mfma_f32_16x16x32_bf16 v[244:247], v[156:159], v[132:135], v[244:247]\n"
    "v_add_u32_dpp v55, v116, v51 quad_perm:[3,3,3,3] row_mask:0xf bank_mask:0xf\n"
    "v_mfma_f32_16x16x32_bf16 v[244:247], v[188:191], v[164:167], v[244:247]\n"
    "v_mfma_f32_16x16x32_bf16 v[240:243], v[188:191], v[160:163], v[240:243]\n"
    "v_mfma_f32_16x16x32_bf16 v[240:243], v[156:159], v[128:131], v[240:243]\n"
    "s_waitcnt vmcnt(8)\n"
    "s_barrier\n"
    "s_add_u32 m0, s35, 98304\n"
    "s_nop 0\n"
    "global_load_lds_dwordx4 v46, s[30:31] offset:0\n"
    "ds_read_b128 v[128:131], v56 offset:0\n"
    "ds_read_b128 v[132:135], v56 offset:2048\n"
    "ds_read_b128 v[136:139], v56 offset:4096\n"
    "global_load_lds_dwordx4 v47, s[30:31] offset:1024\n"
    "s_add_u32 s66, s28, 7\n"
    "s_and_b32 s66, s66, 7\n"
    "s_mul_i32 s66, s66, 638976\n"
    "s_add_u32 s30, s6, s66\n"
    "s_addc_u32 s31, s7, 0\n"
    "ds_read_b128 v[140:143], v56 offset:6144\n"
    "ds_read_b64_tr_b16 v[144:145], v76 offset:0\n"
    "ds_read_b64_tr_b16 v[146:147], v76 offset:1024\n"
    "s_add_u32 m0, s34, 98304\n"
    "s_nop 0\n"
    "global_load_lds_dwordx4 v52, s[36:37] offset:0\n"
    "ds_read_b64_tr_b16 v[148:149], v77 offset:0\n"
    "ds_read_b64_tr_b16 v[150:151], v77 offset:1024\n"
    "ds_read_b64_tr_b16 v[152:153], v78 offset:0\n"
    "global_load_lds_dwordx4 v53, s[36:37] offset:1024\n"
    "ds_read_b64_tr_b16 v[154:155], v78 offset:1024\n"
    "ds_read_b64_tr_b16 v[156:157], v79 offset:0\n"
    "ds_read_b64_tr_b16 v[158:159], v79 offset:1024\n"
    "global_load_lds_dwordx4 v54, s[36:37] offset:2048\n"
    "ds_read_b128 v[160:163], v57 offset:0\n"
    "ds_read_b128 v[164:167], v57 offset:2048\n"
    "ds_read_b128 v[168:171], v57 offset:4096\n"
    "global_load_lds_dwordx4 v55, s[36:37] offset:3072\n"
    "ds_read_b128 v[172:175], v57 offset:6144\n"
    "ds_read_b64_tr_b16 v[176:177], v76 offset:8192\n"
    "ds_read_b64_tr_b16 v[178:179], v76 offset:9216\n"
    "global_load_dword v40, v41, s[8:9]\n"
    "ds_read_b64_tr_b16 v[180:181], v77 offset:8192\n"
    "ds_read_b64_tr_b16 v[182:183], v77 offset:9216\n"
    "ds_read_b64_tr_b16 v[184:185], v78 offset:8192\n"
    "ds_read_b64_tr_b16 v[186:187], v78 offset:9216\n"
    "ds_read_b64_tr_b16 v[188:189], v79 offset:8192\n"
    "ds_read_b64_tr_b16 v[190:191], v79 offset:9216\n"
    "s_waitcnt vmcnt(12) lgkmcnt(0)\n"
    "s_barrier\n"
    "v_mfma_f32_16x16x32_bf16 v[192:195], v[144:147], v[128:131], v[192:195]\n"
    "v_lshlrev_b32 v118, 7, v118\n"
    "v_mfma_f32_16x16x32_bf16 v[192:195], v[176:179], v[160:163], v[192:195]\n"
    "v_add_u32_dpp v52, v117, v48 quad_perm:[0,0,0,0] row_mask:0xf bank_mask:0xf\n"
    "v_mfma_f32_16x16x32_bf16 v[196:199], v[176:179], v[164:167], v[196:199]\n"
    "v_add_u32_dpp v53, v117, v49 quad_perm:[1,1,1,1] row_mask:0xf bank_mask:0xf\n"
    "v_mfma_f32_16x16x32_bf16 v[196:199], v[144:147], v[132:135], v[196:199]\n"
    "v_add_u32_dpp v54, v117, v50 quad_perm:[2,2,2,2] row_mask:0xf bank_mask:0xf\n"
    "v_mfma_f32_16x16x32_bf16 v[200:203], v[144:147], v[136:139], v[200:203]\n"
    "v_add_u32_dpp v55, v117, v51 quad_perm:[3,3,3,3] row_mask:0xf bank_mask:0xf\n"
    "v_mfma_f32_16x16x32_bf16 v[200:203], v[176:179], v[168:171], v[200:203]\n"
    "v_readlane_b32 s25, v26, 9\n"
    "v_mfma_f32_16x16x32_bf16 v[204:207], v[176:179], v[172:175], v[204:207]\n"
    "v_readlane_b32 s40, v26, 8\n"
    "v_mfma_f32_16x16x32_bf16 v[204:207], v[144:147], v[140:143], v[204:207]\n"
    "v_readlane_b32 s41, v27, 8\n"
    "v_mfma_f32_16x16x32_bf16 v[220:223], v[148:151], v[140:143], v[220:223]\n"
    "s_mul_i32 s42, s25, 0x9e3779b1\n"
    "v_mfma_f32_16x16x32_bf16 v[220:223], v[180:183], v[172:175], v[220:223]\n"
    "s_lshr_b32 s42, s42, 24\n"
    "v_mfma_f32_16x16x32_bf16 v[216:219], v[180:183], v[168:171], v[216:219]\n"
    "s_xor_b32 s42, s42, s40\n"
    "v_mfma_f32_16x16x32_bf16 v[216:219], v[148:151], v[136:139], v[216:219]\n"
    "s_and_b32 s42, s42, 0xff\n"
    "v_mfma_f32_16x16x32_bf16 v[212:215], v[148:151], v[132:135], v[212:215]\n"
    "s_xor_b32 s41, s41, s25\n"
    "v_mfma_f32_16x16x32_bf16 v[212:215], v[180:183], v[164:167], v[212:215]\n"
    "s_xor_b32 s41, s41, 0xa5c30f96\n"
    "v_mfma_f32_16x16x32_bf16 v[208:211], v[180:183], v[160:163], v[208:211]\n"
    "s_or_b32 s42, s41, s42\n"
    "v_mfma_f32_16x16x32_bf16 v[208:211], v[148:151], v[128:131], v[208:211]\n"
    "v_mfma_f32_16x16x32_bf16 v[224:227], v[152:155], v[128:131], v[224:227]\n"
    "s_lshr_b32 s41, s40, 8\n"
    "v_mfma_f32_16x16x32_bf16 v[224:227], v[184:187], v[160:163], v[224:227]\n"
    "s_mul_i32 s40, s41, 0x9e3779b1\n"
    "v_mfma_f32_16x16x32_bf16 v[228:231], v[184:187], v[164:167], v[228:231]\n"
    "s_xor_b32 s41, s41, 0xa5c30f96\n"
    "v_mfma_f32_16x16x32_bf16 v[228:231], v[152:155], v[132:135], v[228:231]\n"
    "s_lshr_b32 s40, s40, 24\n"
    "v_mfma_f32_16x16x32_bf16 v[232:235], v[152:155], v[136:139], v[232:235]\n"
    "v_xor_b32 v13, s40, v26\n"
    "v_mfma_f32_16x16x32_bf16 v[232:235], v[184:187], v[168:171], v[232:235]\n"
    "v_xor_b32 v22, s41, v27\n"
    "v_mfma_f32_16x16x32_bf16 v[236:239], v[184:187], v[172:175], v[236:239]\n"
    "v_lshl_or_b32 v13, v13, 24, v22\n"
    "v_mfma_f32_16x16x32_bf16 v[236:239], v[152:155], v[140:143], v[236:239]\n"
    "v_cmp_ne_u32 s[66:67], 0, v13\n"
    "v_mfma_f32_16x16x32_bf16 v[252:255], v[156:159], v[140:143], v[252:255]\n"
    "s_and_b32 s66, s66, 0xff\n"
    "v_mfma_f32_16x16x32_bf16 v[252:255], v[188:191], v[172:175], v[252:255]\n"
    "s_or_b32 s42, s42, s66\n"
    "v_mfma_f32_16x16x32_bf16 v[248:251], v[188:191], v[168:171], v[248:251]\n"
    "s_cmp_eq_u32 s20, 3\n"
    "v_mfma_f32_16x16x32_bf16 v[248:251], v[156:159], v[136:139], v[248:251]\n"
    "s_cselect_b32 s42, s42, 0\n"
    "v_mfma_f32_16x16x32_bf16 v[244:247], v[156:159], v[132:135], v[244:247]\n"
    "s_cmp_lg_u32 s42, 0\n"
    "v_mfma_f32_16x16x32_bf16 v[244:247], v[188:191], v[164:167], v[244:247]\n"
    "s_cbranch_scc1 .Lxd_pv3\n"
    ".Lxd_pv3r:\n"
    "v_mfma_f32_16x16x32_bf16 v[240:243], v[188:191], v[160:163], v[240:243]\n"
    "v_mfma_f32_16x16x32_bf16 v[240:243], v[156:159], v[128:131], v[240:243]\n"
    "s_waitcnt vmcnt(8)\n"
    "s_barrier\n"
    "s_add_u32 m0, s35, 0\n"
    "s_nop 0\n"
    "global_load_lds_dwordx4 v46, s[30:31] offset:0\n"
    "ds_read_b128 v[128:131], v56 offset:49152\n"
    "ds_read_b128 v[132:135], v56 offset:51200\n"
    "ds_read_b128 v[136:139], v56 offset:53248\n"
    "global_load_lds_dwordx4 v47, s[30:31] offset:1024\n"
    "s_add_u32 s30, s30, 212992\n"
    "s_addc_u32 s31, s31, 0\n"
    "ds_read_b128 v[140:143], v56 offset:55296\n"
    "ds_read_b64_tr_b16 v[144:145], v76 offset:49152\n"
    "ds_read_b64_tr_b16 v[146:147], v76 offset:50176\n"
    "s_add_u32 m0, s34, 0\n"
    "s_nop 0\n"
    "global_load_lds_dwordx4 v52, s[36:37] offset:0\n"
    "ds_read_b64_tr_b16 v[148:149], v77 offset:49152\n"
    "ds_read_b64_tr_b16 v[150:151], v77 offset:50176\n"
    "ds_read_b64_tr_b16 v[152:153], v78 offset:49152\n"
    "global_load_lds_dwordx4 v53, s[36:37] offset:1024\n"
    "ds_read_b64_tr_b16 v[154:155], v78 offset:50176\n"
    "ds_read_b64_tr_b16 v[156:157], v79 offset:49152\n"
    "ds_read_b64_tr_b16 v[158:159], v79 offset:50176\n"
    "global_load_lds_dwordx4 v54, s[36:37] offset:2048\n"
    "ds_read_b128 v[160:163], v57 offset:49152\n"
    "ds_read_b128 v[164:167], v57 offset:51200\n"
    "ds_read_b128 v[168:171], v57 offset:53248\n"
    "global_load_lds_dwordx4 v55, s[36:37] offset:3072\n"
    "ds_read_b128 v[172:175], v57 offset:55296\n"
    "ds_read_b64_tr_b16 v[176:177], v76 offset:57344\n"
    "ds_read_b64_tr_b16 v[178:179], v76 offset:58368\n"
    "global_load_dwordx4 v[36:39], v38, s[8:9]\n"
    "ds_read_b64_tr_b16 v[180:181], v77 offset:57344\n"
    "ds_read_b64_tr_b16 v[182:183], v77 offset:58368\n"
    "ds_read_b64_tr_b16 v[184:185], v78 offset:57344\n"
    "ds_read_b64_tr_b16 v[186:187], v78 offset:58368\n"
    "ds_read_b64_tr_b16 v[188:189], v79 offset:57344\n"
    "ds_read_b64_tr_b16 v[190:191], v79 offset:58368\n"
    "s_waitcnt vmcnt(12) lgkmcnt(0)\n"
    "s_barrier\n"
    "v_mfma_f32_16x16x32_bf16 v[192:195], v[144:147], v[128:131], v[192:195]\n"
    "v_lshlrev_b32 v119, 7, v119\n"
    "v_mfma_f32_16x16x32_bf16 v[192:195], v[176:179], v[160:163], v[192:195]\n"
    "v_add_u32_dpp v52, v118, v48 quad_perm:[0,0,0,0] row_mask:0xf bank_mask:0xf\n"
    "v_mfma_f32_16x16x32_bf16 v[196:199], v[176:179], v[164:167], v[196:199]\n"
    "v_add_u32_dpp v53, v118, v49 quad_perm:[1,1,1,1] row_mask:0xf bank_mask:0xf\n"
    "v_mfma_f32_16x16x32_bf16 v[196:199], v[144:147], v[132:135], v[196:199]\n"
    "v_mfma_f32_16x16x32_bf16 v[200:203], v[144:147], v[136:139], v[200:203]\n"
    "v_add_u32_dpp v54, v118, v50 quad_perm:[2,2,2,2] row_mask:0xf bank_mask:0xf\n"
    "v_mfma_f32_16x16x32_bf16 v[200:203], v[176:179], v[168:171], v[200:203]\n"
    "v_add_u32_dpp v55, v118, v51 quad_perm:[3,3,3,3] row_mask:0xf bank_mask:0xf\n"
    "v_mfma_f32_16x16x32_bf16 v[204:207], v[176:179], v[172:175], v[204:207]\n"
    "v_mfma_f32_16x16x32_bf16 v[204:207], v[144:147], v[140:143], v[204:207]\n"
    "v_lshlrev_b32 v60, 16, v92\n"
    "v_mfma_f32_16x16x32_bf16 v[220:223], v[148:151], v[140:143], v[220:223]\n"
    "v_and_b32 v61, 0xffff0000, v92\n"
    "v_mfma_f32_16x16x32_bf16 v[220:223], v[180:183], v[172:175], v[220:223]\n"
    "v_mfma_f32_16x16x32_bf16 v[216:219], v[180:183], v[168:171], v[216:219]\n"
    "v_lshlrev_b32 v62, 16, v93\n"
    "v_mfma_f32_16x16x32_bf16 v[216:219], v[148:151], v[136:139], v[216:219]\n"
    "v_and_b32 v63, 0xffff0000, v93\n"
    "v_mfma_f32_16x16x32_bf16 v[212:215], v[148:151], v[132:135], v[212:215]\n"
    "v_mfma_f32_16x16x32_bf16 v[212:215], v[180:183], v[164:167], v[212:215]\n"
    "v_lshlrev_b32 v64, 16, v94\n"
    "v_mfma_f32_16x16x32_bf16 v[208:211], v[180:183], v[160:163], v[208:211]\n"
    "v_and_b32 v65, 0xffff0000, v94\n"
    "v_mfma_f32_16x16x32_bf16 v[208:211], v[148:151], v[128:131], v[208:211]\n"
    "v_mfma_f32_16x16x32_bf16 v[224:227], v[152:155], v[128:131], v[224:227]\n"
    "v_lshlrev_b32 v66, 16, v95\n"
    "v_mfma_f32_16x16x32_bf16 v[224:227], v[184:187], v[160:163], v[224:227]\n"
    "v_and_b32 v67, 0xffff0000, v95\n"
    "v_mfma_f32_16x16x32_bf16 v[228:231], v[184:187], v[164:167], v[228:231]\n"
    "v_mfma_f32_16x16x32_bf16 v[228:231], v[152:155], v[132:135], v[228:231]\n"
    "v_lshlrev_b32 v68, 16, v122\n"
    "v_mfma_f32_16x16x32_bf16 v[232:235], v[152:155], v[136:139], v[232:235]\n"
    "v_and_b32 v69, 0xffff0000, v122\n"
    "v_mfma_f32_16x16x32_bf16 v[232:235], v[184:187], v[168:171], v[232:235]\n"
    "v_mfma_f32_16x16x32_bf16 v[236:239], v[184:187], v[172:175], v[236:239]\n"
    "v_lshlrev_b32 v70, 16, v123\n"
    "v_mfma_f32_16x16x32_bf16 v[236:239], v[152:155], v[140:143], v[236:239]\n"
    "v_and_b32 v71, 0xffff0000, v123\n"
    "v_mfma_f32_16x16x32_bf16 v[252:255], v[156:159], v[140:143], v[252:255]\n"
    "v_mfma_f32_16x16x32_bf16 v[252:255], v[188:191], v[172:175], v[252:255]\n"
    "v_lshlrev_b32 v72, 16, v124\n"
    "v_mfma_f32_16x16x32_bf16 v[248:251], v[188:191], v[168:171], v[248:251]\n"
    "v_and_b32 v73, 0xffff0000, v124\n"
    "v_mfma_f32_16x16x32_bf16 v[248:251], v[156:159], v[136:139], v[248:251]\n"
    "v_mfma_f32_16x16x32_bf16 v[244:247], v[156:159], v[132:135], v[244:247]\n"
    "v_lshlrev_b32 v74, 16, v125\n"
    "v_mfma_f32_16x16x32_bf16 v[244:247], v[188:191], v[164:167], v[244:247]\n"
    "v_and_b32 v75, 0xffff0000, v125\n"
    "v_mfma_f32_16x16x32_bf16 v[240:243], v[188:191], v[160:163], v[240:243]\n"
    "v_mfma_f32_16x16x32_bf16 v[240:243], v[156:159], v[128:131], v[240:243]\n"
    "s_waitcnt vmcnt(8)\n"
    "s_barrier\n"
    "s_add_u32 m0, s35, 49152\n"
    "s_nop 0\n"
    "global_load_lds_dwordx4 v46, s[30:31] offset:0\n"
    "ds_read_b128 v[128:131], v58 offset:0\n"
    "ds_read_b128 v[132:135], v58 offset:2048\n"
    "ds_read_b128 v[136:139], v58 offset:4096\n"
    "ds_read_b128 v[140:143], v58 offset:6144\n"
    "global_load_lds_dwordx4 v47, s[30:31] offset:1024\n"
    "s_add_u32 s30, s30, 212992\n"
    "s_addc_u32 s31, s31, 0\n"
    "ds_read_b64_tr_b16 v[144:145], v80 offset:0\n"
    "ds_read_b64_tr_b16 v[146:147], v80 offset:1024\n"
    "ds_read_b64_tr_b16 v[148:149], v81 offset:0\n"
    "ds_read_b64_tr_b16 v[150:151], v81 offset:1024\n"
    "s_add_u32 m0, s34, 49152\n"
    "s_nop 0\n"
    "global_load_lds_dwordx4 v52, s[36:37] offset:0\n"
    "ds_read_b64_tr_b16 v[152:153], v82 offset:0\n"
    "ds_read_b64_tr_b16 v[154:155], v82 offset:1024\n"
    "ds_read_b64_tr_b16 v[156:157], v83 offset:0\n"
    "ds_read_b64_tr_b16 v[158:159], v83 offset:1024\n"
    "global_load_lds_dwordx4 v53, s[36:37] offset:1024\n"
    "ds_read_b128 v[160:163], v59 offset:0\n"
    "ds_read_b128 v[164:167], v59 offset:2048\n"
    "ds_read_b128 v[168:171], v59 offset:4096\n"
    "ds_read_b128 v[172:175], v59 offset:6144\n"
    "global_load_lds_dwordx4 v54, s[36:37] offset:2048\n"
    "ds_read_b64_tr_b16 v[176:177], v80 offset:8192\n"
    "ds_read_b64_tr_b16 v[178:179], v80 offset:9216\n"
    "ds_read_b64_tr_b16 v[180:181], v81 offset:8192\n"
    "ds_read_b64_tr_b16 v[182:183], v81 offset:9216\n"
    "global_load_lds_dwordx4 v55, s[36:37] offset:3072\n"
    "ds_read_b64_tr_b16 v[184:185], v82 offset:8192\n"
    "ds_read_b64_tr_b16 v[186:187], v82 offset:9216\n"
    "ds_read_b64_tr_b16 v[188:189], v83 offset:8192\n"
    "ds_read_b64_tr_b16 v[190:191], v83 offset:9216\n"
    "s_waitcnt vmcnt(11) lgkmcnt(0)\n"
    "s_barrier\n"
    "v_mfma_f32_16x16x32_bf16 v[192:195], v[144:147], v[128:131], v[192:195]\n"
    "v_lshlrev_b32 v120, 7, v120\n"
    "v_mfma_f32_16x16x32_bf16 v[192:195], v[176:179], v[160:163], v[192:195]\n"
    "v_add_u32_dpp v52, v119, v48 quad_perm:[0,0,0,0] row_mask:0xf bank_mask:0xf\n"
    "v_mfma_f32_16x16x32_bf16 v[196:199], v[176:179], v[164:167], v[196:199]\n"
    "v_add_u32_dpp v53, v119, v49 quad_perm:[1,1,1,1] row_mask:0xf bank_mask:0xf\n"
    "v_mfma_f32_16x16x32_bf16 v[196:199], v[144:147], v[132:135], v[196:199]\n"
    "v_add_u32_dpp v54, v119, v50 quad_perm:[2,2,2,2] row_mask:0xf bank_mask:0xf\n"
    "v_mfma_f32_16x16x32_bf16 v[200:203], v[144:147], v[136:139], v[200:203]\n"
    "v_add_u32_dpp v55, v119, v51 quad_perm:[3,3,3,3] row_mask:0xf bank_mask:0xf\n"
    "v_mfma_f32_16x16x32_bf16 v[200:203], v[176:179], v[168:171], v[200:203]\n"
    "v_lshrrev_b32 v22, 8, v26\n"
    "v_mfma_f32_16x16x32_bf16 v[204:207], v[176:179], v[172:175], v[204:207]\n"
    "v_mfma_f32_16x16x32_bf16 v[204:207], v[144:147], v[140:143], v[204:207]\n"
    "v_mov_b32 v26, s40\n"
    "v_mfma_f32_16x16x32_bf16 v[220:223], v[148:151], v[140:143], v[220:223]\n"
    "v_mov_b32 v27, s41\n"
    "v_mfma_f32_16x16x32_bf16 v[220:223], v[180:183], v[172:175], v[220:223]\n"
    "v_add_u32_dpp v22, v22, v22 row_shl:4 row_mask:0xf bank_mask:0xf\n"
    "v_mfma_f32_16x16x32_bf16 v[216:219], v[180:183], v[168:171], v[216:219]\n"
    "v_add_u32_e64 v43, s25, 1\n"
    "v_mfma_f32_16x16x32_bf16 v[216:219], v[148:151], v[136:139], v[216:219]\n"
    "v_lshlrev_b32 v8, 2, v1\n"
    "v_mfma_f32_16x16x32_bf16 v[212:215], v[148:151], v[132:135], v[212:215]\n"
    "v_mfma_f32_16x16x32_bf16 v[212:215], v[180:183], v[164:167], v[212:215]\n"
    "v_max_u32_dpp v13, v22, v22 quad_perm:[1,0,3,2] row_mask:0xf bank_mask:0xf\n"
    "v_mfma_f32_16x16x32_bf16 v[208:211], v[180:183], v[160:163], v[208:211]\n"
    "v_add_u32 v8, 154112, v8\n"
    "v_mfma_f32_16x16x32_bf16 v[208:211], v[148:151], v[128:131], v[208:211]\n"
    "s_nop 0\n"
    "v_mfma_f32_16x16x32_bf16 v[224:227], v[152:155], v[128:131], v[224:227]\n"
    "v_max_u32_dpp v13, v13, v13 quad_perm:[2,3,0,1] row_mask:0xf bank_mask:0xf\n"
    "v_mfma_f32_16x16x32_bf16 v[224:227], v[184:187], v[160:163], v[224:227]\n"
    "v_sub_u32 v22, v22, v13\n"
    "v_mfma_f32_16x16x32_bf16 v[228:231], v[184:187], v[164:167], v[228:231]\n"
    "v_mfma_f32_16x16x32_bf16 v[228:231], v[152:155], v[132:135], v[228:231]\n"
    "v_subrev_u32 v13, 818, v13\n"
    "v_mfma_f32_16x16x32_bf16 v[232:235], v[152:155], v[136:139], v[232:235]\n"
    "v_ldexp_f32 v22, 1.0, v22\n"
    "v_mfma_f32_16x16x32_bf16 v[232:235], v[184:187], v[168:171], v[232:235]\n"
    "v_rcp_f32 v60, v60\n"
    "v_mfma_f32_16x16x32_bf16 v[236:239], v[184:187], v[172:175], v[236:239]\n"
    "v_rcp_f32 v61, v61\n"
    "v_mfma_f32_16x16x32_bf16 v[236:239], v[152:155], v[140:143], v[236:239]\n"
    "v_rcp_f32 v62, v62\n"
    "v_mfma_f32_16x16x32_bf16 v[252:255], v[156:159], v[140:143], v[252:255]\n"
    "v_mfma_f32_16x16x32_bf16 v[252:255], v[188:191], v[172:175], v[252:255]\n"
    "v_rcp_f32 v63, v63\n"
    "v_mfma_f32_16x16x32_bf16 v[248:251], v[188:191], v[168:171], v[248:251]\n"
    "v_rcp_f32 v64, v64\n"
    "v_mfma_f32_16x16x32_bf16 v[248:251], v[156:159], v[136:139], v[248:251]\n"
    "v_rcp_f32 v65, v65\n"
    "v_mfma_f32_16x16x32_bf16 v[244:247], v[156:159], v[132:135], v[244:247]\n"
    "v_rcp_f32 v66, v66\n"
    "v_mfma_f32_16x16x32_bf16 v[244:247], v[188:191], v[164:167], v[244:247]\n"
    "v_rcp_f32 v67, v67\n"
    "v_mfma_f32_16x16x32_bf16 v[240:243], v[188:191], v[160:163], v[240:243]\n"
    "v_mfma_f32_16x16x32_bf16 v[240:243], v[156:159], v[128:131], v[240:243]\n"
    "s_waitcnt vmcnt(7)\n"
    "s_barrier\n"
    "s_add_u32 m0, s35, 98304\n"
    "s_nop 0\n"
    "global_load_lds_dwordx4 v46, s[30:31] offset:0\n"
    "ds_read_b128 v[128:131], v56 offset:0\n"
    "ds_read_b128 v[132:135], v56 offset:2048\n"
    "ds_read_b128 v[136:139], v56 offset:4096\n"
    "ds_read_b128 v[140:143], v56 offset:6144\n"
    "global_load_lds_dwordx4 v47, s[30:31] offset:1024\n"
    "s_add_u32 s30, s6, 5111808\n"
    "s_addc_u32 s31, s7, 0\n"
    "ds_read_b64_tr_b16 v[144:145], v76 offset:0\n"
    "ds_read_b64_tr_b16 v[146:147], v76 offset:1024\n"
    "ds_read_b64_tr_b16 v[148:149], v77 offset:0\n"
    "ds_read_b64_tr_b16 v[150:151], v77 offset:1024\n"
    "s_add_u32 m0, s34, 98304\n"
    "s_nop 0\n"
    "global_load_lds_dwordx4 v52, s[36:37] offset:0\n"
    "ds_read_b64_tr_b16 v[152:153], v78 offset:0\n"
    "ds_read_b64_tr_b16 v[154:155], v78 offset:1024\n"
    "ds_read_b64_tr_b16 v[156:157], v79 offset:0\n"
    "ds_read_b64_tr_b16 v[158:159], v79 offset:1024\n"
    "global_load_lds_dwordx4 v53, s[36:37] offset:1024\n"
    "ds_read_b128 v[160:163], v57 offset:0\n"
    "ds_read_b128 v[164:167], v57 offset:2048\n"
    "ds_read_b128 v[168:171], v57 offset:4096\n"
    "ds_read_b128 v[172:175], v57 offset:6144\n"
    "global_load_lds_dwordx4 v54, s[36:37] offset:2048\n"
    "ds_read_b64_tr_b16 v[176:177], v76 offset:8192\n"
    "ds_read_b64_tr_b16 v[178:179], v76 offset:9216\n"
    "ds_read_b64_tr_b16 v[180:181], v77 offset:8192\n"
    "ds_read_b64_tr_b16 v[182:183], v77 offset:9216\n"
    "global_load_lds_dwordx4 v55, s[36:37] offset:3072\n"
    "ds_read_b64_tr_b16 v[184:185], v78 offset:8192\n"
    "ds_read_b64_tr_b16 v[186:187], v78 offset:9216\n"
    "ds_read_b64_tr_b16 v[188:189], v79 offset:8192\n"
    "ds_read_b64_tr_b16 v[190:191], v79 offset:9216\n"
    "s_waitcnt vmcnt(10) lgkmcnt(0)\n"
    "s_barrier\n"
    "v_mfma_f32_16x16x32_bf16 v[192:195], v[144:147], v[128:131], v[192:195]\n"
    "v_lshlrev_b32 v121, 7, v121\n"
    "v_mfma_f32_16x16x32_bf16 v[192:195], v[176:179], v[160:163], v[192:195]\n"
    "v_add_u32_dpp v52, v120, v48 quad_perm:[0,0,0,0] row_mask:0xf bank_mask:0xf\n"
    "v_mfma_f32_16x16x32_bf16 v[196:199], v[176:179], v[164:167], v[196:199]\n"
    "v_add_u32_dpp v53, v120, v49 quad_perm:[1,1,1,1] row_mask:0xf bank_mask:0xf\n"
    "v_mfma_f32_16x16x32_bf16 v[196:199], v[144:147], v[132:135], v[196:199]\n"
    "v_add_u32_dpp v54, v120, v50 quad_perm:[2,2,2,2] row_mask:0xf bank_mask:0xf\n"
    "v_mfma_f32_16x16x32_bf16 v[200:203], v[144:147], v[136:139], v[200:203]\n"
    "v_add_u32_dpp v55, v120, v51 quad_perm:[3,3,3,3] row_mask:0xf bank_mask:0xf\n"
    "v_mfma_f32_16x16x32_bf16 v[200:203], v[176:179], v[168:171], v[200:203]\n"
    "s_movk_i32 s43, 15744\n"
    "v_pk_maximum3_f16 v21, v28, v28, s43 op_sel_hi:[1,1,0] neg_lo:[0,1,0] neg_hi:[0,1,0]\n"
    "v_mfma_f32_16x16x32_bf16 v[204:207], v[176:179], v[172:175], v[204:207]\n"
    "v_pk_maximum3_f16 v21, v29, v29, v21 neg_lo:[0,1,0] neg_hi:[0,1,0]\n"
    "v_mfma_f32_16x16x32_bf16 v[204:207], v[144:147], v[140:143], v[204:207]\n"
    "v_mfma_f32_16x16x32_bf16 v[220:223], v[148:151], v[140:143], v[220:223]\n"
    "v_pk_maximum3_f16 v21, v30, v30, v21 neg_lo:[0,1,0] neg_hi:[0,1,0]\n"
    "v_mfma_f32_16x16x32_bf16 v[220:223], v[180:183], v[172:175], v[220:223]\n"
    "v_pk_maximum3_f16 v21, v31, v31, v21 neg_lo:[0,1,0] neg_hi:[0,1,0]\n"
    "v_mfma_f32_16x16x32_bf16 v[216:219], v[180:183], v[168:171], v[216:219]\n"
    "v_pk_maximum3_f16 v21, v32, v32, v21 neg_lo:[0,1,0] neg_hi:[0,1,0]\n"
    "v_mfma_f32_16x16x32_bf16 v[216:219], v[148:151], v[136:139], v[216:219]\n"
    "v_pk_maximum3_f16 v21, v33, v33, v21 neg_lo:[0,1,0] neg_hi:[0,1,0]\n"
    "v_mfma_f32_16x16x32_bf16 v[212:215], v[148:151], v[132:135], v[212:215]\n"
    "v_pk_maximum3_f16 v21, v34, v34, v21 neg_lo:[0,1,0] neg_hi:[0,1,0]\n"
    "v_mfma_f32_16x16x32_bf16 v[212:215], v[180:183], v[164:167], v[212:215]\n"
    "v_pk_maximum3_f16 v21, v35, v35, v21 neg_lo:[0,1,0] neg_hi:[0,1,0]\n"
    "v_mfma_f32_16x16x32_bf16 v[208:211], v[180:183], v[160:163], v[208:211]\n"
    "v_pk_maximum3_f16 v21, v36, v36, v21 neg_lo:[0,1,0] neg_hi:[0,1,0]\n"
    "v_mfma_f32_16x16x32_bf16 v[208:211], v[148:151], v[128:131], v[208:211]\n"
    "v_mfma_f32_16x16x32_bf16 v[224:227], v[152:155], v[128:131], v[224:227]\n"
    "v_pk_maximum3_f16 v21, v37, v37, v21 neg_lo:[0,1,0] neg_hi:[0,1,0]\n"
    "v_mfma_f32_16x16x32_bf16 v[224:227], v[184:187], v[160:163], v[224:227]\n"
    "v_pk_maximum3_f16 v21, v38, v38, v21 neg_lo:[0,1,0] neg_hi:[0,1,0]\n"
    "v_mfma_f32_16x16x32_bf16 v[228:231], v[184:187], v[164:167], v[228:231]\n"
    "v_pk_maximum3_f16 v21, v39, v39, v21 neg_lo:[0,1,0] neg_hi:[0,1,0]\n"
    "v_mfma_f32_16x16x32_bf16 v[228:231], v[152:155], v[132:135], v[228:231]\n"
    "v_pk_maximum3_f16 v21, v40, v40, v21 neg_lo:[0,1,0] neg_hi:[0,1,0]\n"
    "v_mfma_f32_16x16x32_bf16 v[232:235], v[152:155], v[136:139], v[232:235]\n"
    "v_pk_max_u16 v21, v21, v21 op_sel:[0,1] op_sel_hi:[1,0]\n"
    "v_mfma_f32_16x16x32_bf16 v[232:235], v[184:187], v[168:171], v[232:235]\n"
    "v_rcp_f32 v68, v68\n"
    "v_mfma_f32_16x16x32_bf16 v[236:239], v[184:187], v[172:175], v[236:239]\n"
    "v_rcp_f32 v69, v69\n"
    "v_mfma_f32_16x16x32_bf16 v[236:239], v[152:155], v[140:143], v[236:239]\n"
    "v_mfma_f32_16x16x32_bf16 v[252:255], v[156:159], v[140:143], v[252:255]\n"
    "v_rcp_f32 v70, v70\n"
    "v_mfma_f32_16x16x32_bf16 v[252:255], v[188:191], v[172:175], v[252:255]\n"
    "v_rcp_f32 v71, v71\n"
    "v_mfma_f32_16x16x32_bf16 v[248:251], v[188:191], v[168:171], v[248:251]\n"
    "v_rcp_f32 v72, v72\n"
    "v_mfma_f32_16x16x32_bf16 v[248:251], v[156:159], v[136:139], v[248:251]\n"
    "v_rcp_f32 v73, v73\n"
    "v_mfma_f32_16x16x32_bf16 v[244:247], v[156:159], v[132:135], v[244:247]\n"
    "v_rcp_f32 v74, v74\n"
    "v_mfma_f32_16x16x32_bf16 v[244:247], v[188:191], v[164:167], v[244:247]\n"
    "v_rcp_f32 v75, v75\n"
    "v_mfma_f32_16x16x32_bf16 v[240:243], v[188:191], v[160:163], v[240:243]\n"
    "v_mfma_f32_16x16x32_bf16 v[240:243], v[156:159], v[128:131], v[240:243]\n"
    "s_waitcnt vmcnt(6)\n"
    "s_barrier\n"
    "s_add_u32 m0, s35, 0\n"
    "s_nop 0\n"
    "global_load_lds_dwordx4 v46, s[30:31] offset:0\n"
    "ds_read_b128 v[128:131], v56 offset:49152\n"
    "ds_read_b128 v[132:135], v56 offset:51200\n"
    "ds_read_b128 v[136:139], v56 offset:53248\n"
    "ds_read_b128 v[140:143], v56 offset:55296\n"
    "global_load_lds_dwordx4 v47, s[30:31] offset:1024\n"
    "s_add_u32 s30, s30, 212992\n"
    "s_addc_u32 s31, s31, 0\n"
    "ds_read_b64_tr_b16 v[144:145], v76 offset:49152\n"
    "ds_read_b64_tr_b16 v[146:147], v76 offset:50176\n"
    "ds_read_b64_tr_b16 v[148:149], v77 offset:49152\n"
    "ds_read_b64_tr_b16 v[150:151], v77 offset:50176\n"
    "s_add_u32 m0, s34, 0\n"
    "s_nop 0\n"
    "global_load_lds_dwordx4 v52, s[36:37] offset:0\n"
    "ds_read_b64_tr_b16 v[152:153], v78 offset:49152\n"
    "ds_read_b64_tr_b16 v[154:155], v78 offset:50176\n"
    "ds_read_b64_tr_b16 v[156:157], v79 offset:49152\n"
    "ds_read_b64_tr_b16 v[158:159], v79 offset:50176\n"
    "global_load_lds_dwordx4 v53, s[36:37] offset:1024\n"
    "ds_read_b128 v[160:163], v57 offset:49152\n"
    "ds_read_b128 v[164:167], v57 offset:51200\n"
    "ds_read_b128 v[168:171], v57 offset:53248\n"
    "ds_read_b128 v[172:175], v57 offset:55296\n"
    "global_load_lds_dwordx4 v54, s[36:37] offset:2048\n"
    "ds_read_b64_tr_b16 v[176:177], v76 offset:57344\n"
    "ds_read_b64_tr_b16 v[178:179], v76 offset:58368\n"
    "ds_read_b64_tr_b16 v[180:181], v77 offset:57344\n"
    "ds_read_b64_tr_b16 v[182:183], v77 offset:58368\n"
    "global_load_lds_dwordx4 v55, s[36:37] offset:3072\n"
    "ds_read_b64_tr_b16 v[184:185], v78 offset:57344\n"
    "ds_read_b64_tr_b16 v[186:187], v78 offset:58368\n"
    "ds_read_b64_tr_b16 v[188:189], v79 offset:57344\n"
    "ds_read_b64_tr_b16 v[190:191], v79 offset:58368\n"
    "s_waitcnt vmcnt(10) lgkmcnt(0)\n"
    "s_barrier\n"
    "v_mfma_f32_16x16x32_bf16 v[192:195], v[144:147], v[128:131], v[192:195]\n"
    "v_add_u32_dpp v52, v121, v48 quad_perm:[0,0,0,0] row_mask:0xf bank_mask:0xf\n"
    "s_mov_b32 s42, 0x71800000\n"
    "v_mfma_f32_16x16x32_bf16 v[192:195], v[176:179], v[160:163], v[192:195]\n"
    "v_add_u32_dpp v53, v121, v49 quad_perm:[1,1,1,1] row_mask:0xf bank_mask:0xf\n"
    "v_mfma_f32_16x16x32_bf16 v[196:199], v[176:179], v[164:167], v[196:199]\n"
    "v_add_u32_dpp v54, v121, v50 quad_perm:[2,2,2,2] row_mask:0xf bank_mask:0xf\n"
    "v_mfma_f32_16x16x32_bf16 v[196:199], v[144:147], v[132:135], v[196:199]\n"
    "v_add_u32_dpp v55, v121, v51 quad_perm:[3,3,3,3] row_mask:0xf bank_mask:0xf\n"
    "v_mfma_f32_16x16x32_bf16 v[200:203], v[144:147], v[136:139], v[200:203]\n"
    "v_bfe_u32 v41, v1, 4, 1\n"
    "v_mfma_f32_16x16x32_bf16 v[200:203], v[176:179], v[168:171], v[200:203]\n"
    "v_lshlrev_b32 v41, 2, v41\n"
    "v_mfma_f32_16x16x32_bf16 v[204:207], v[176:179], v[172:175], v[204:207]\n"
    "v_add_u32 v41, 154112, v41\n"
    "v_mfma_f32_16x16x32_bf16 v[204:207], v[144:147], v[140:143], v[204:207]\n"
    "v_med3_f32 v60, v60, s42, -s42\n"
    "v_mfma_f32_16x16x32_bf16 v[220:223], v[148:151], v[140:143], v[220:223]\n"
    "v_max_u32_dpp v21, v21, v21 quad_perm:[1,0,3,2] row_mask:0xf bank_mask:0xf\n"
    "v_mfma_f32_16x16x32_bf16 v[220:223], v[180:183], v[172:175], v[220:223]\n"
    "v_med3_f32 v61, v61, s42, -s42\n"
    "v_mfma_f32_16x16x32_bf16 v[216:219], v[180:183], v[168:171], v[216:219]\n"
    "v_max_u32_dpp v21, v21, v21 quad_perm:[2,3,0,1] row_mask:0xf bank_mask:0xf\n"
    "v_mfma_f32_16x16x32_bf16 v[216:219], v[148:151], v[136:139], v[216:219]\n"
    "v_med3_f32 v62, v62, s42, -s42\n"
    "v_mfma_f32_16x16x32_bf16 v[212:215], v[148:151], v[132:135], v[212:215]\n"
    "v_max_u32_dpp v21, v21, v21 row_half_mirror row_mask:0xf bank_mask:0xf\n"
    "v_mfma_f32_16x16x32_bf16 v[212:215], v[180:183], v[164:167], v[212:215]\n"
    "v_med3_f32 v63, v63, s42, -s42\n"
    "v_mfma_f32_16x16x32_bf16 v[208:211], v[180:183], v[160:163], v[208:211]\n"
    "v_max_u32_dpp v21, v21, v21 row_mirror row_mask:0xf bank_mask:0xf\n"
    "v_mfma_f32_16x16x32_bf16 v[208:211], v[148:151], v[128:131], v[208:211]\n"
    "v_med3_f32 v64, v64, s42, -s42\n"
    "v_mfma_f32_16x16x32_bf16 v[224:227], v[152:155], v[128:131], v[224:227]\n"
    "v_max_u32_dpp v21, v21, v21 row_bcast:15 row_mask:0xa bank_mask:0xf\n"
    "v_mfma_f32_16x16x32_bf16 v[224:227], v[184:187], v[160:163], v[224:227]\n"
    "v_med3_f32 v65, v65, s42, -s42\n"
    "v_mfma_f32_16x16x32_bf16 v[228:231], v[184:187], v[164:167], v[228:231]\n"
    "v_max_u32_dpp v21, v21, v21 row_bcast:31 row_mask:0xc bank_mask:0xf\n"
    "v_mfma_f32_16x16x32_bf16 v[228:231], v[152:155], v[132:135], v[228:231]\n"
    "v_med3_f32 v66, v66, s42, -s42\n"
    "v_mfma_f32_16x16x32_bf16 v[232:235], v[152:155], v[136:139], v[232:235]\n"
    "v_readlane_b32 s47, v21, 63\n"
    "v_mfma_f32_16x16x32_bf16 v[232:235], v[184:187], v[168:171], v[232:235]\n"
    "v_med3_f32 v67, v67, s42, -s42\n"
    "v_mfma_f32_16x16x32_bf16 v[236:239], v[184:187], v[172:175], v[236:239]\n"
    "s_and_b32 s47, s47, 0x7f80\n"
    "v_mfma_f32_16x16x32_bf16 v[236:239], v[152:155], v[140:143], v[236:239]\n"
    "s_sub_u32 s50, s47, 2816\n"
    "v_mfma_f32_16x16x32_bf16 v[252:255], v[156:159], v[140:143], v[252:255]\n"
    "s_mul_i32 s51, s50, -8\n"
    "v_med3_f32 v68, v68, s42, -s42\n"
    "v_mfma_f32_16x16x32_bf16 v[252:255], v[188:191], v[172:175], v[252:255]\n"
    "s_lshr_b32 s47, s47, 3\n"
    "v_med3_f32 v69, v69, s42, -s42\n"
    "v_mfma_f32_16x16x32_bf16 v[248:251], v[188:191], v[168:171], v[248:251]\n"
    "v_med3_f32 v70, v70, s42, -s42\n"
    "v_mfma_f32_16x16x32_bf16 v[248:251], v[156:159], v[136:139], v[248:251]\n"
    "v_med3_f32 v71, v71, s42, -s42\n"
    "v_mfma_f32_16x16x32_bf16 v[244:247], v[156:159], v[132:135], v[244:247]\n"
    "v_med3_f32 v72, v72, s42, -s42\n"
    "v_mfma_f32_16x16x32_bf16 v[244:247], v[188:191], v[164:167], v[244:247]\n"
    "v_med3_f32 v73, v73, s42, -s42\n"
    "v_mfma_f32_16x16x32_bf16 v[240:243], v[188:191], v[160:163], v[240:243]\n"
    "v_med3_f32 v74, v74, s42, -s42\n"
    "v_mfma_f32_16x16x32_bf16 v[240:243], v[156:159], v[128:131], v[240:243]\n"
    "v_med3_f32 v75, v75, s42, -s42\n"
    "s_waitcnt vmcnt(6)\n"
    "s_barrier\n"
    "s_add_u32 m0, s35, 49152\n"
    "s_nop 0\n"
    "global_load_lds_dwordx4 v46, s[30:31] offset:0\n"
    "ds_read_b128 v[128:131], v58 offset:0\n"
    "ds_read_b128 v[132:135], v58 offset:2048\n"
    "ds_read_b128 v[136:139], v58 offset:4096\n"
    "ds_read_b128 v[140:143], v58 offset:6144\n"
    "global_load_lds_dwordx4 v47, s[30:31] offset:1024\n"
    "ds_read_b64_tr_b16 v[144:145], v80 offset:0\n"
    "ds_read_b64_tr_b16 v[146:147], v80 offset:1024\n"
    "ds_read_b64_tr_b16 v[148:149], v81 offset:0\n"
    "ds_read_b64_tr_b16 v[150:151], v81 offset:1024\n"
    "s_add_u32 m0, s34, 49152\n"
    "s_nop 0\n"
    "global_load_lds_dwordx4 v52, s[36:37] offset:0\n"
    "ds_read_b64_tr_b16 v[152:153], v82 offset:0\n"
    "ds_read_b64_tr_b16 v[154:155], v82 offset:1024\n"
    "ds_read_b64_tr_b16 v[156:157], v83 offset:0\n"
    "ds_read_b64_tr_b16 v[158:159], v83 offset:1024\n"
    "global_load_lds_dwordx4 v53, s[36:37] offset:1024\n"
    "ds_read_b128 v[160:163], v59 offset:0\n"
    "ds_read_b128 v[164:167], v59 offset:2048\n"
    "ds_read_b128 v[168:171], v59 offset:4096\n"
    "ds_read_b128 v[172:175], v59 offset:6144\n"
    "global_load_lds_dwordx4 v54, s[36:37] offset:2048\n"
    "ds_read_b64_tr_b16 v[176:177], v80 offset:8192\n"
    "ds_read_b64_tr_b16 v[178:179], v80 offset:9216\n"
    "ds_read_b64_tr_b16 v[180:181], v81 offset:8192\n"
    "ds_read_b64_tr_b16 v[182:183], v81 offset:9216\n"
    "global_load_lds_dwordx4 v55, s[36:37] offset:3072\n"
    "ds_read_b64_tr_b16 v[184:185], v82 offset:8192\n"
    "ds_read_b64_tr_b16 v[186:187], v82 offset:9216\n"
    "ds_read_b64_tr_b16 v[188:189], v83 offset:8192\n"
    "ds_read_b64_tr_b16 v[190:191], v83 offset:9216\n"
    "s_waitcnt vmcnt(10) lgkmcnt(0)\n"
    "s_barrier\n"
    "v_mfma_f32_16x16x32_bf16 v[192:195], v[144:147], v[128:131], v[192:195]\n"
    "v_pk_maximum3_f16 v21, v28, v28, s50 op_sel_hi:[1,1,0] neg_lo:[0,1,0] neg_hi:[0,1,0]\n"
    "v_mfma_f32_16x16x32_bf16 v[192:195], v[176:179], v[160:163], v[192:195]\n"
    "v_pk_mad_u16 v21, v21, 8, s51 op_sel_hi:[1,0,0]\n"
    "v_mfma_f32_16x16x32_bf16 v[196:199], v[176:179], v[164:167], v[196:199]\n"
    "v_bfi_b32 v28, s57, v21, v28\n"
    "v_mfma_f32_16x16x32_bf16 v[196:199], v[144:147], v[132:135], v[196:199]\n"
    "v_mfma_f32_16x16x32_bf16 v[200:203], v[144:147], v[136:139], v[200:203]\n"
    "v_pk_maximum3_f16 v12, v29, v29, s50 op_sel_hi:[1,1,0] neg_lo:[0,1,0] neg_hi:[0,1,0]\n"
    "v_mfma_f32_16x16x32_bf16 v[200:203], v[176:179], v[168:171], v[200:203]\n"
    "v_pk_mad_u16 v12, v12, 8, s51 op_sel_hi:[1,0,0]\n"
    "v_mfma_f32_16x16x32_bf16 v[204:207], v[176:179], v[172:175], v[204:207]\n"
    "v_bfi_b32 v29, s57, v12, v29\n"
    "v_mfma_f32_16x16x32_bf16 v[204:207], v[144:147], v[140:143], v[204:207]\n"
    "v_mfma_f32_16x16x32_bf16 v[220:223], v[148:151], v[140:143], v[220:223]\n"
    "v_pk_maximum3_f16 v21, v30, v30, s50 op_sel_hi:[1,1,0] neg_lo:[0,1,0] neg_hi:[0,1,0]\n"
    "v_mfma_f32_16x16x32_bf16 v[220:223], v[180:183], v[172:175], v[220:223]\n"
    "v_pk_mad_u16 v21, v21, 8, s51 op_sel_hi:[1,0,0]\n"
    "v_mfma_f32_16x16x32_bf16 v[216:219], v[180:183], v[168:171], v[216:219]\n"
    "v_bfi_b32 v30, s57, v21, v30\n"
    "v_mfma_f32_16x16x32_bf16 v[216:219], v[148:151], v[136:139], v[216:219]\n"
    "v_mfma_f32_16x16x32_bf16 v[212:215], v[148:151], v[132:135], v[212:215]\n"
    "v_pk_maximum3_f16 v12, v31, v31, s50 op_sel_hi:[1,1,0] neg_lo:[0,1,0] neg_hi:[0,1,0]\n"
    "v_mfma_f32_16x16x32_bf16 v[212:215], v[180:183], v[164:167], v[212:215]\n"
    "v_pk_mad_u16 v12, v12, 8, s51 op_sel_hi:[1,0,0]\n"
    "v_mfma_f32_16x16x32_bf16 v[208:211], v[180:183], v[160:163], v[208:211]\n"
    "v_bfi_b32 v31, s57, v12, v31\n"
    "v_mfma_f32_16x16x32_bf16 v[208:211], v[148:151], v[128:131], v[208:211]\n"
    "v_mfma_f32_16x16x32_bf16 v[224:227], v[152:155], v[128:131], v[224:227]\n"
    "v_pk_maximum3_f16 v21, v32, v32, s50 op_sel_hi:[1,1,0] neg_lo:[0,1,0] neg_hi:[0,1,0]\n"
    "v_mfma_f32_16x16x32_bf16 v[224:227], v[184:187], v[160:163], v[224:227]\n"
    "v_pk_mad_u16 v21, v21, 8, s51 op_sel_hi:[1,0,0]\n"
    "v_mfma_f32_16x16x32_bf16 v[228:231], v[184:187], v[164:167], v[228:231]\n"
    "v_bfi_b32 v32, s57, v21, v32\n"
    "v_mfma_f32_16x16x32_bf16 v[228:231], v[152:155], v[132:135], v[228:231]\n"
    "v_mfma_f32_16x16x32_bf16 v[232:235], v[152:155], v[136:139], v[232:235]\n"
    "v_pk_maximum3_f16 v12, v33, v33, s50 op_sel_hi:[1,1,0] neg_lo:[0,1,0] neg_hi:[0,1,0]\n"
    "v_mfma_f32_16x16x32_bf16 v[232:235], v[184:187], v[168:171], v[232:235]\n"
    "v_pk_mad_u16 v12, v12, 8, s51 op_sel_hi:[1,0,0]\n"
    "v_mfma_f32_16x16x32_bf16 v[236:239], v[184:187], v[172:175], v[236:239]\n"
    "v_bfi_b32 v33, s57, v12, v33\n"
    "v_mfma_f32_16x16x32_bf16 v[236:239], v[152:155], v[140:143], v[236:239]\n"
    "v_mfma_f32_16x16x32_bf16 v[252:255], v[156:159], v[140:143], v[252:255]\n"
    "v_pk_maximum3_f16 v21, v34, v34, s50 op_sel_hi:[1,1,0] neg_lo:[0,1,0] neg_hi:[0,1,0]\n"
    "v_mfma_f32_16x16x32_bf16 v[252:255], v[188:191], v[172:175], v[252:255]\n"
    "v_pk_mad_u16 v21, v21, 8, s51 op_sel_hi:[1,0,0]\n"
    "v_mfma_f32_16x16x32_bf16 v[248:251], v[188:191], v[168:171], v[248:251]\n"
    "v_bfi_b32 v34, s57, v21, v34\n"
    "v_mfma_f32_16x16x32_bf16 v[248:251], v[156:159], v[136:139], v[248:251]\n"
    "v_mfma_f32_16x16x32_bf16 v[244:247], v[156:159], v[132:135], v[244:247]\n"
    "v_lshlrev_b32 v52, 4, v1\n"
    "v_mfma_f32_16x16x32_bf16 v[244:247], v[188:191], v[164:167], v[244:247]\n"
    "v_add_u32 v53, 4096, v52\n"
    "v_mfma_f32_16x16x32_bf16 v[240:243], v[188:191], v[160:163], v[240:243]\n"
    "v_mfma_f32_16x16x32_bf16 v[240:243], v[156:159], v[128:131], v[240:243]\n"
    "s_waitcnt vmcnt(6)\n"
    "s_barrier\n"
    "s_mul_i32 s42, s20, 5376\n"
    "s_add_u32 s40, s42, 1048576\n"
    "s_add_u32 s38, s16, s40\n"
    "s_addc_u32 s39, s17, 0\n"
    "s_add_u32 m0, s42, 98304\n"
    "s_nop 0\n"
    "global_load_lds_dwordx4 v52, s[38:39] offset:0 sc1\n"
    "ds_read_b128 v[128:131], v56 offset:0\n"
    "ds_read_b128 v[132:135], v56 offset:2048\n"
    "ds_read_b128 v[136:139], v56 offset:4096\n"
    "ds_read_b128 v[140:143], v56 offset:6144\n"
    "global_load_lds_dwordx4 v52, s[38:39] offset:1024 sc1\n"
    "ds_read_b64_tr_b16 v[144:145], v76 offset:0\n"
    "ds_read_b64_tr_b16 v[146:147], v76 offset:1024\n"
    "ds_read_b64_tr_b16 v[148:149], v77 offset:0\n"
    "ds_read_b64_tr_b16 v[150:151], v77 offset:1024\n"
    "global_load_lds_dwordx4 v52, s[38:39] offset:2048 sc1\n"
    "ds_read_b64_tr_b16 v[152:153], v78 offset:0\n"
    "ds_read_b64_tr_b16 v[154:155], v78 offset:1024\n"
    "ds_read_b64_tr_b16 v[156:157], v79 offset:0\n"
    "ds_read_b64_tr_b16 v[158:159], v79 offset:1024\n"
    "global_load_lds_dwordx4 v52, s[38:39] offset:3072 sc1\n"
    "ds_read_b128 v[160:163], v57 offset:0\n"
    "ds_read_b128 v[164:167], v57 offset:2048\n"
    "ds_read_b128 v[168:171], v57 offset:4096\n"
    "ds_read_b128 v[172:175], v57 offset:6144\n"
    "s_add_u32 m0, s42, 102400\n"
    "s_nop 0\n"
    "global_load_lds_dwordx4 v53, s[38:39] offset:0 sc1\n"
    "ds_read_b64_tr_b16 v[176:177], v76 offset:8192\n"
    "ds_read_b64_tr_b16 v[178:179], v76 offset:9216\n"
    "ds_read_b64_tr_b16 v[180:181], v77 offset:8192\n"
    "ds_read_b64_tr_b16 v[182:183], v77 offset:9216\n"
    "global_load_lds_dwordx4 v53, s[38:39] offset:256 sc1\n"
    "ds_read_b64_tr_b16 v[184:185], v78 offset:8192\n"
    "ds_read_b64_tr_b16 v[186:187], v78 offset:9216\n"
    "ds_read_b64_tr_b16 v[188:189], v79 offset:8192\n"
    "ds_read_b64_tr_b16 v[190:191], v79 offset:9216\n"
    "s_waitcnt vmcnt(10) lgkmcnt(0)\n"
    "s_barrier\n"
    "v_mfma_f32_16x16x32_bf16 v[192:195], v[144:147], v[128:131], v[192:195]\n"
    "v_add_u32 v42, 98304, v52\n"
    "v_mfma_f32_16x16x32_bf16 v[192:195], v[176:179], v[160:163], v[192:195]\n"
    "s_mov_b32 s36, 0\n"
    "v_mfma_f32_16x16x32_bf16 v[196:199], v[176:179], v[164:167], v[196:199]\n"
    "v_mov_b32 v84, s65\n"
    "v_mfma_f32_16x16x32_bf16 v[196:199], v[144:147], v[132:135], v[196:199]\n"
    "v_mfma_f32_16x16x32_bf16 v[200:203], v[144:147], v[136:139], v[200:203]\n"
    "v_pk_maximum3_f16 v12, v35, v35, s50 op_sel_hi:[1,1,0] neg_lo:[0,1,0] neg_hi:[0,1,0]\n"
    "v_mfma_f32_16x16x32_bf16 v[200:203], v[176:179], v[168:171], v[200:203]\n"
    "v_pk_mad_u16 v12, v12, 8, s51 op_sel_hi:[1,0,0]\n"
    "v_mfma_f32_16x16x32_bf16 v[204:207], v[176:179], v[172:175], v[204:207]\n"
    "v_bfi_b32 v35, s57, v12, v35\n"
    "v_mfma_f32_16x16x32_bf16 v[204:207], v[144:147], v[140:143], v[204:207]\n"
    "v_mfma_f32_16x16x32_bf16 v[220:223], v[148:151], v[140:143], v[220:223]\n"
    "v_pk_maximum3_f16 v21, v36, v36, s50 op_sel_hi:[1,1,0] neg_lo:[0,1,0] neg_hi:[0,1,0]\n"
    "v_mfma_f32_16x16x32_bf16 v[220:223], v[180:183], v[172:175], v[220:223]\n"
    "v_pk_mad_u16 v21, v21, 8, s51 op_sel_hi:[1,0,0]\n"
    "v_mfma_f32_16x16x32_bf16 v[216:219], v[180:183], v[168:171], v[216:219]\n"
    "v_bfi_b32 v36, s57, v21, v36\n"
    "v_mfma_f32_16x16x32_bf16 v[216:219], v[148:151], v[136:139], v[216:219]\n"
    "v_mfma_f32_16x16x32_bf16 v[212:215], v[148:151], v[132:135], v[212:215]\n"
    "v_pk_maximum3_f16 v12, v37, v37, s50 op_sel_hi:[1,1,0] neg_lo:[0,1,0] neg_hi:[0,1,0]\n"
    "v_mfma_f32_16x16x32_bf16 v[212:215], v[180:183], v[164:167], v[212:215]\n"
    "v_pk_mad_u16 v12, v12, 8, s51 op_sel_hi:[1,0,0]\n"
    "v_mfma_f32_16x16x32_bf16 v[208:211], v[180:183], v[160:163], v[208:211]\n"
    "v_bfi_b32 v37, s57, v12, v37\n"
    "v_mfma_f32_16x16x32_bf16 v[208:211], v[148:151], v[128:131], v[208:211]\n"
    "v_mfma_f32_16x16x32_bf16 v[224:227], v[152:155], v[128:131], v[224:227]\n"
    "v_pk_maximum3_f16 v21, v38, v38, s50 op_sel_hi:[1,1,0] neg_lo:[0,1,0] neg_hi:[0,1,0]\n"
    "v_mfma_f32_16x16x32_bf16 v[224:227], v[184:187], v[160:163], v[224:227]\n"
    "v_pk_mad_u16 v21, v21, 8, s51 op_sel_hi:[1,0,0]\n"
    "v_mfma_f32_16x16x32_bf16 v[228:231], v[184:187], v[164:167], v[228:231]\n"
    "v_bfi_b32 v38, s57, v21, v38\n"
    "v_mfma_f32_16x16x32_bf16 v[228:231], v[152:155], v[132:135], v[228:231]\n"
    "v_mfma_f32_16x16x32_bf16 v[232:235], v[152:155], v[136:139], v[232:235]\n"
    "v_pk_maximum3_f16 v12, v39, v39, s50 op_sel_hi:[1,1,0] neg_lo:[0,1,0] neg_hi:[0,1,0]\n"
    "v_mfma_f32_16x16x32_bf16 v[232:235], v[184:187], v[168:171], v[232:235]\n"
    "v_pk_mad_u16 v12, v12, 8, s51 op_sel_hi:[1,0,0]\n"
    "v_mfma_f32_16x16x32_bf16 v[236:239], v[184:187], v[172:175], v[236:239]\n"
    "v_bfi_b32 v39, s57, v12, v39\n"
    "v_mfma_f32_16x16x32_bf16 v[236:239], v[152:155], v[140:143], v[236:239]\n"
    "v_mfma_f32_16x16x32_bf16 v[252:255], v[156:159], v[140:143], v[252:255]\n"
    "v_pk_maximum3_f16 v21, v40, v40, s50 op_sel_hi:[1,1,0] neg_lo:[0,1,0] neg_hi:[0,1,0]\n"
    "v_mfma_f32_16x16x32_bf16 v[252:255], v[188:191], v[172:175], v[252:255]\n"
    "v_pk_mad_u16 v21, v21, 8, s51 op_sel_hi:[1,0,0]\n"
    "v_mfma_f32_16x16x32_bf16 v[248:251], v[188:191], v[168:171], v[248:251]\n"
    "v_bfi_b32 v40, s57, v21, v40\n"
    "v_mfma_f32_16x16x32_bf16 v[248:251], v[156:159], v[136:139], v[248:251]\n"
    "v_mfma_f32_16x16x32_bf16 v[244:247], v[156:159], v[132:135], v[244:247]\n"
    "s_sub_u32 s66, 4112, s47\n"
    "s_lshl_b32 s66, s66, 19\n"
    "v_cvt_pkrtz_f16_f32 v12, s66, s66\n"
    "v_mfma_f32_16x16x32_bf16 v[244:247], v[188:191], v[164:167], v[244:247]\n"
    "v_lshl_or_b32 v21, v12, 16, s47\n"
    "v_mfma_f32_16x16x32_bf16 v[240:243], v[188:191], v[160:163], v[240:243]\n"
    "v_mfma_f32_16x16x32_bf16 v[240:243], v[156:159], v[128:131], v[240:243]\n"
    "s_waitcnt vmcnt(6)\n"
    "s_barrier\n"
    "ds_read_b128 v[128:131], v56 offset:49152\n"
    "ds_read_b128 v[132:135], v56 offset:51200\n"
    "ds_read_b128 v[136:139], v56 offset:53248\n"
    "ds_read_b128 v[140:143], v56 offset:55296\n"
    "ds_read_b64_tr_b16 v[144:145], v76 offset:49152\n"
    "ds_read_b64_tr_b16 v[146:147], v76 offset:50176\n"
    "ds_read_b64_tr_b16 v[148:149], v77 offset:49152\n"
    "ds_read_b64_tr_b16 v[150:151], v77 offset:50176\n"
    "ds_read_b64_tr_b16 v[152:153], v78 offset:49152\n"
    "ds_read_b64_tr_b16 v[154:155], v78 offset:50176\n"
    "ds_read_b64_tr_b16 v[156:157], v79 offset:49152\n"
    "ds_read_b64_tr_b16 v[158:159], v79 offset:50176\n"
    "ds_read_b128 v[160:163], v57 offset:49152\n"
    "ds_read_b128 v[164:167], v57 offset:51200\n"
    "ds_read_b128 v[168:171], v57 offset:53248\n"
    "ds_read_b128 v[172:175], v57 offset:55296\n"
    "ds_read_b64_tr_b16 v[176:177], v76 offset:57344\n"
    "ds_read_b64_tr_b16 v[178:179], v76 offset:58368\n"
    "ds_read_b64_tr_b16 v[180:181], v77 offset:57344\n"
    "ds_read_b64_tr_b16 v[182:183], v77 offset:58368\n"
    "ds_read_b64_tr_b16 v[184:185], v78 offset:57344\n"
    "ds_read_b64_tr_b16 v[186:187], v78 offset:58368\n"
    "ds_read_b64_tr_b16 v[188:189], v79 offset:57344\n"
    "ds_read_b64_tr_b16 v[190:191], v79 offset:58368\n"
    "s_cmp_lg_u32 s20, 3\n"
    "s_cbranch_scc1 .Lxd_on4\n"
    "s_mov_b64 exec, 1\n"
    "ds_write_b64 v8, v[26:27]\n"
    "s_mov_b64 exec, 15\n"
    "ds_write2_b32 v8, v22, v13 offset0:4 offset1:8\n"
    "s_mov_b64 exec, -1\n"
    ".Lxd_on4:\n"
    "s_waitcnt vmcnt(0) lgkmcnt(0)\n"
    "s_cmp_lg_u32 s20, 3\n"
    "s_cbranch_scc1 .Lxd_on5\n"
    "s_mov_b64 exec, 0x200\n"
    "global_store_dword v11, v43, s[16:17] sc1\n"
    "s_mov_b64 exec, -1\n"
    ".Lxd_on5:\n"
    "s_barrier\n"
    "s_setprio 3\n"
    "v_mfma_f32_16x16x32_bf16 v[192:195], v[144:147], v[128:131], v[192:195]\n"
    "v_lshl_add_u32 v11, v1, 2, s3\n"
    "v_mfma_f32_16x16x32_bf16 v[192:195], v[176:179], v[160:163], v[192:195]\n"
    "v_mov_b32 v13, s58\n"
    "v_mfma_f32_16x16x32_bf16 v[196:199], v[176:179], v[164:167], v[196:199]\n"
    "v_mfma_f32_16x16x32_bf16 v[196:199], v[144:147], v[132:135], v[196:199]\n"
    "v_lshl_add_u32 v7, s23, 12, v3\n"
    "v_mfma_f32_16x16x32_bf16 v[200:203], v[144:147], v[136:139], v[200:203]\n"
    "v_mfma_f32_16x16x32_bf16 v[200:203], v[176:179], v[168:171], v[200:203]\n"
    "v_lshl_add_u32 v8, s23, 12, v4\n"
    "v_mfma_f32_16x16x32_bf16 v[204:207], v[176:179], v[172:175], v[204:207]\n"
    "v_lshl_add_u32 v9, s23, 12, v5\n"
    "v_mfma_f32_16x16x32_bf16 v[204:207], v[144:147], v[140:143], v[204:207]\n"
    "v_mfma_f32_16x16x32_bf16 v[220:223], v[148:151], v[140:143], v[220:223]\n"
    "v_lshl_add_u32 v10, s23, 12, v6\n"
    "v_mfma_f32_16x16x32_bf16 v[220:223], v[180:183], v[172:175], v[220:223]\n"
    "v_mfma_f32_16x16x32_bf16 v[216:219], v[180:183], v[168:171], v[216:219]\n"
    "v_mul_f32 v76, v192, v192\n"
    "v_mul_f32 v77, v193, v193\n"
    "v_mfma_f32_16x16x32_bf16 v[216:219], v[148:151], v[136:139], v[216:219]\n"
    "v_mul_f32 v78, v196, v196\n"
    "v_mul_f32 v79, v197, v197\n"
    "v_mfma_f32_16x16x32_bf16 v[212:215], v[148:151], v[132:135], v[212:215]\n"
    "v_mul_f32 v80, v200, v200\n"
    "v_mul_f32 v81, v201, v201\n"
    "v_mfma_f32_16x16x32_bf16 v[212:215], v[180:183], v[164:167], v[212:215]\n"
    "v_mul_f32 v82, v204, v204\n"
    "v_mul_f32 v83, v205, v205\n"
    "v_mfma_f32_16x16x32_bf16 v[208:211], v[180:183], v[160:163], v[208:211]\n"
    "v_fma_f32 v76, v76, s64, v84\n"
    "v_fma_f32 v77, v77, s64, v84\n"
    "v_mfma_f32_16x16x32_bf16 v[208:211], v[148:151], v[128:131], v[208:211]\n"
    "v_fma_f32 v78, v78, s64, v84\n"
    "v_fma_f32 v79, v79, s64, v84\n"
    "v_mfma_f32_16x16x32_bf16 v[224:227], v[152:155], v[128:131], v[224:227]\n"
    "v_fma_f32 v80, v80, s64, v84\n"
    "v_fma_f32 v81, v81, s64, v84\n"
    "v_mfma_f32_16x16x32_bf16 v[224:227], v[184:187], v[160:163], v[224:227]\n"
    "v_fma_f32 v82, v82, s64, v84\n"
    "v_fma_f32 v83, v83, s64, v84\n"
    "v_mfma_f32_16x16x32_bf16 v[228:231], v[184:187], v[164:167], v[228:231]\n"
    "v_mul_f32 v76, v76, v192\n"
    "v_mul_f32 v77, v77, v193\n"
    "v_mfma_f32_16x16x32_bf16 v[228:231], v[152:155], v[132:135], v[228:231]\n"
    "v_mul_f32 v78, v78, v196\n"
    "v_mul_f32 v79, v79, v197\n"
    "v_mfma_f32_16x16x32_bf16 v[232:235], v[152:155], v[136:139], v[232:235]\n"
    "v_mul_f32 v80, v80, v200\n"
    "v_mul_f32 v81, v81, v201\n"
    "v_mfma_f32_16x16x32_bf16 v[232:235], v[184:187], v[168:171], v[232:235]\n"
    "v_mul_f32 v82, v82, v204\n"
    "v_mul_f32 v83, v83, v205\n"
    "v_mfma_f32_16x16x32_bf16 v[236:239], v[184:187], v[172:175], v[236:239]\n"
    "v_exp_f32 v76, v76\n"
    "v_mfma_f32_16x16x32_bf16 v[236:239], v[152:155], v[140:143], v[236:239]\n"
    "v_exp_f32 v77, v77\n"
    "v_mfma_f32_16x16x32_bf16 v[252:255], v[156:159], v[140:143], v[252:255]\n"
    "v_exp_f32 v78, v78\n"
    "v_mfma_f32_16x16x32_bf16 v[252:255], v[188:191], v[172:175], v[252:255]\n"
    "v_exp_f32 v79, v79\n"
    "v_mfma_f32_16x16x32_bf16 v[248:251], v[188:191], v[168:171], v[248:251]\n"
    "v_exp_f32 v80, v80\n"
    "v_mfma_f32_16x16x32_bf16 v[248:251], v[156:159], v[136:139], v[248:251]\n"
    "v_exp_f32 v81, v81\n"
    "v_mfma_f32_16x16x32_bf16 v[244:247], v[156:159], v[132:135], v[244:247]\n"
    "v_exp_f32 v82, v82\n"
    "v_mfma_f32_16x16x32_bf16 v[244:247], v[188:191], v[164:167], v[244:247]\n"
    "v_exp_f32 v83, v83\n"
    "v_mfma_f32_16x16x32_bf16 v[240:243], v[188:191], v[160:163], v[240:243]\n"
    "v_mfma_f32_16x16x32_bf16 v[240:243], v[156:159], v[128:131], v[240:243]\n"
    "s_cmp_lg_u32 s21, 0\n"
    "s_cbranch_scc1 .Lxd_g1\n"
    "s_barrier\n"
    ".Lxd_g1:\n"
    "s_setprio 2\n"
    "s_cmp_eq_u32 s22, 3\n"
    "s_cbranch_scc1 .Lxd_np\n"
    "ds_write_b32 v25, v24 offset:20\n"
    "ds_write_b128 v15, v[28:31] offset:0\n"
    "ds_write_b128 v15, v[32:35] offset:144\n"
    "ds_write_b128 v15, v[36:39] offset:288\n"
    "ds_write2_b32 v16, v40, v12 offset1:72\n"
    "ds_or_b32 v25, v21 offset:532\n"
    ".Lxd_np:\n"
    "ds_read2_b32 v[26:27], v41 offset1:1\n"
    "ds_read_b32 v0, v41 offset:16\n"
    "ds_read_b32 v23, v41 offset:24\n"
    "s_load_dword s26, s[68:69], 0\n"
    "v_add_u32 v25, s24, v25\n"
    "ds_read_b128 v[92:95], v42 offset:0\n"
    "ds_read_b128 v[96:99], v42 offset:1024\n"
    "ds_read_b128 v[100:103], v42 offset:2048\n"
    "ds_read_b128 v[104:107], v42 offset:3072\n"
    "ds_read_b128 v[108:111], v42 offset:4096\n"
    "ds_read_b128 v[112:115], v42 offset:5120\n"
    "ds_read_b128 v[116:119], v42 offset:6144\n"
    "ds_read_b128 v[120:123], v42 offset:7168\n"
    "ds_read_b128 v[124:127], v42 offset:8192\n"
    "s_cmp_eq_u32 s22, 3\n"
    "s_cbranch_scc1 .Lxd_on6\n"
    ".Lxd_on6r:\n"
    "v_pk_fma_f32 v[76:77], v[76:77], v[60:61], v[60:61]\n"
    "v_pk_fma_f32 v[78:79], v[78:79], v[60:61], v[60:61]\n"
    "v_pk_fma_f32 v[80:81], v[80:81], v[60:61], v[60:61]\n"
    "v_pk_fma_f32 v[82:83], v[82:83], v[60:61], v[60:61]\n"
    "v_rcp_f32 v76, v76\n"
    "v_rcp_f32 v77, v77\n"
    "v_rcp_f32 v78, v78\n"
    "v_rcp_f32 v79, v79\n"
    "v_rcp_f32 v80, v80\n"
    "v_rcp_f32 v81, v81\n"
    "v_rcp_f32 v82, v82\n"
    "v_rcp_f32 v83, v83\n"
    "v_pk_mul_f32 v[84:85], v[192:193], v[76:77]\n"
    "v_pk_mul_f32 v[86:87], v[196:197], v[78:79]\n"
    "v_pk_mul_f32 v[88:89], v[200:201], v[80:81]\n"
    "v_pk_mul_f32 v[90:91], v[204:205], v[82:83]\n"
    "s_setprio 3\n"
    "v_pk_mul_f32 v[76:77], v[194:195], v[194:195]\n"
    "v_pk_mul_f32 v[78:79], v[198:199], v[198:199]\n"
    "v_pk_mul_f32 v[80:81], v[202:203], v[202:203]\n"
    "v_pk_mul_f32 v[82:83], v[206:207], v[206:207]\n"
    "v_pk_fma_f32 v[76:77], v[76:77], s[64:65], s[64:65] op_sel:[0,0,1] op_sel_hi:[1,0,1]\n"
    "v_pk_fma_f32 v[78:79], v[78:79], s[64:65], s[64:65] op_sel:[0,0,1] op_sel_hi:[1,0,1]\n"
    "v_pk_fma_f32 v[80:81], v[80:81], s[64:65], s[64:65] op_sel:[0,0,1] op_sel_hi:[1,0,1]\n"
    "v_pk_fma_f32 v[82:83], v[82:83], s[64:65], s[64:65] op_sel:[0,0,1] op_sel_hi:[1,0,1]\n"
    "v_pk_mul_f32 v[76:77], v[76:77], v[194:195]\n"
    "v_pk_mul_f32 v[78:79], v[78:79], v[198:199]\n"
    "v_pk_mul_f32 v[80:81], v[80:81], v[202:203]\n"
    "v_pk_mul_f32 v[82:83], v[82:83], v[206:207]\n"
    "v_exp_f32 v76, v76\n"
    "v_exp_f32 v77, v77\n"
    "v_exp_f32 v78, v78\n"
    "v_exp_f32 v79, v79\n"
    "v_exp_f32 v80, v80\n"
    "v_exp_f32 v81, v81\n"
    "v_exp_f32 v82, v82\n"
    "v_exp_f32 v83, v83\n"
    "v_pk_fma_f32 v[76:77], v[76:77], v[62:63], v[62:63]\n"
    "v_pk_fma_f32 v[78:79], v[78:79], v[62:63], v[62:63]\n"
    "v_pk_fma_f32 v[80:81], v[80:81], v[62:63], v[62:63]\n"
    "v_pk_fma_f32 v[82:83], v[82:83], v[62:63], v[62:63]\n"
    "v_rcp_f32 v76, v76\n"
    "v_rcp_f32 v77, v77\n"
    "v_rcp_f32 v78, v78\n"
    "v_rcp_f32 v79, v79\n"
    "v_rcp_f32 v80, v80\n"
    "v_rcp_f32 v81, v81\n"
    "v_rcp_f32 v82, v82\n"
    "v_rcp_f32 v83, v83\n"
    "v_pk_fma_f32 v[84:85], v[194:195], v[76:77], v[84:85]\n"
    "v_pk_fma_f32 v[86:87], v[198:199], v[78:79], v[86:87]\n"
    "v_pk_fma_f32 v[88:89], v[202:203], v[80:81], v[88:89]\n"
    "v_pk_fma_f32 v[90:91], v[206:207], v[82:83], v[90:91]\n"
    "ds_read_b32 v22, v13 offset:512\n"
    "v_pk_mul_f32 v[76:77], v[208:209], v[208:209]\n"
    "v_pk_mul_f32 v[78:79], v[212:213], v[212:213]\n"
    "v_pk_mul_f32 v[80:81], v[216:217], v[216:217]\n"
    "v_pk_mul_f32 v[82:83], v[220:221], v[220:221]\n"
    "v_pk_fma_f32 v[76:77], v[76:77], s[64:65], s[64:65] op_sel:[0,0,1] op_sel_hi:[1,0,1]\n"
    "v_pk_fma_f32 v[78:79], v[78:79], s[64:65], s[64:65] op_sel:[0,0,1] op_sel_hi:[1,0,1]\n"
    "v_pk_fma_f32 v[80:81], v[80:81], s[64:65], s[64:65] op_sel:[0,0,1] op_sel_hi:[1,0,1]\n"
    "v_pk_fma_f32 v[82:83], v[82:83], s[64:65], s[64:65] op_sel:[0,0,1] op_sel_hi:[1,0,1]\n"
    "v_pk_mul_f32 v[76:77], v[76:77], v[208:209]\n"
    "v_pk_mul_f32 v[78:79], v[78:79], v[212:213]\n"
    "v_pk_mul_f32 v[80:81], v[80:81], v[216:217]\n"
    "v_pk_mul_f32 v[82:83], v[82:83], v[220:221]\n"
    "v_exp_f32 v76, v76\n"
    "v_exp_f32 v77, v77\n"
    "ds_read_b128 v[128:131], v42 offset:9216\n"
    "v_exp_f32 v78, v78\n"
    "v_exp_f32 v79, v79\n"
    "v_exp_f32 v80, v80\n"
    "ds_read_b128 v[132:135], v42 offset:10240\n"
    "v_exp_f32 v81, v81\n"
    "v_exp_f32 v82, v82\n"
    "v_exp_f32 v83, v83\n"
    "ds_read_b128 v[136:139], v42 offset:11264\n"
    "v_pk_fma_f32 v[76:77], v[76:77], v[64:65], v[64:65]\n"
    "v_pk_fma_f32 v[78:79], v[78:79], v[64:65], v[64:65]\n"
    "v_pk_fma_f32 v[80:81], v[80:81], v[64:65], v[64:65]\n"
    "v_pk_fma_f32 v[82:83], v[82:83], v[64:65], v[64:65]\n"
    "v_rcp_f32 v76, v76\n"
    "v_rcp_f32 v77, v77\n"
    "ds_read_b128 v[140:143], v42 offset:12288\n"
    "v_rcp_f32 v78, v78\n"
    "v_rcp_f32 v79, v79\n"
    "v_rcp_f32 v80, v80\n"
    "ds_read_b128 v[144:147], v42 offset:13312\n"
    "v_rcp_f32 v81, v81\n"
    "v_rcp_f32 v82, v82\n"
    "v_rcp_f32 v83, v83\n"
    "ds_read_b128 v[148:151], v42 offset:14336\n"
    "v_pk_fma_f32 v[84:85], v[208:209], v[76:77], v[84:85]\n"
    "v_pk_fma_f32 v[86:87], v[212:213], v[78:79], v[86:87]\n"
    "v_pk_fma_f32 v[88:89], v[216:217], v[80:81], v[88:89]\n"
    "v_pk_fma_f32 v[90:91], v[220:221], v[82:83], v[90:91]\n"
    "s_waitcnt lgkmcnt(6)\n"
    "v_readfirstlane_b32 s45, v22\n"
    "s_cmp_lt_i32 s45, 16\n"
    "s_cbranch_scc1 .Lxd_ck7\n"
    ".Lxd_ck7r:\n"
    "ds_read_b64_tr_b16 v[44:45], v9 offset:0\n"
    "ds_read_b64_tr_b16 v[46:47], v10 offset:0\n"
    "ds_read_b64_tr_b16 v[48:49], v7 offset:0\n"
    "ds_read_b64_tr_b16 v[50:51], v8 offset:576\n"
    "ds_read_b64_tr_b16 v[52:53], v8 offset:1152\n"
    "ds_read_b64_tr_b16 v[54:55], v8 offset:1728\n"
    "ds_read_b64_tr_b16 v[56:57], v8 offset:2304\n"
    "ds_read_b64_tr_b16 v[58:59], v8 offset:2880\n"
    "v_readfirstlane_b32 s28, v26\n"
    "v_readfirstlane_b32 s29, v27\n"
    "s_cmp_eq_u32 s22, 3\n"
    "s_cbranch_scc1 .Lxd_on9\n"
    ".Lxd_on9r:\n"
    "s_cmp_eq_u32 s22, 0\n"
    "s_cbranch_scc1 .Lxd_on10\n"
    ".Lxd_on10r:\n"
    "v_pk_mul_f32 v[76:77], v[210:211], v[210:211]\n"
    "v_pk_mul_f32 v[78:79], v[214:215], v[214:215]\n"
    "v_pk_mul_f32 v[80:81], v[218:219], v[218:219]\n"
    "v_pk_mul_f32 v[82:83], v[222:223], v[222:223]\n"
    "v_pk_fma_f32 v[76:77], v[76:77], s[64:65], s[64:65] op_sel:[0,0,1] op_sel_hi:[1,0,1]\n"
    "v_pk_fma_f32 v[78:79], v[78:79], s[64:65], s[64:65] op_sel:[0,0,1] op_sel_hi:[1,0,1]\n"
    "v_pk_fma_f32 v[80:81], v[80:81], s[64:65], s[64:65] op_sel:[0,0,1] op_sel_hi:[1,0,1]\n"
    "v_pk_fma_f32 v[82:83], v[82:83], s[64:65], s[64:65] op_sel:[0,0,1] op_sel_hi:[1,0,1]\n"
    "v_pk_mul_f32 v[76:77], v[76:77], v[210:211]\n"
    "v_pk_mul_f32 v[78:79], v[78:79], v[214:215]\n"
    "v_pk_mul_f32 v[80:81], v[80:81], v[218:219]\n"
    "v_pk_mul_f32 v[82:83], v[82:83], v[222:223]\n"
    "v_exp_f32 v76, v76\n"
    "v_exp_f32 v77, v77\n"
    "ds_read_b128 v[152:155], v42 offset:15360\n"
    "v_exp_f32 v78, v78\n"
    "v_exp_f32 v79, v79\n"
    "v_exp_f32 v80, v80\n"
    "ds_read_b128 v[156:159], v42 offset:16384\n"
    "v_exp_f32 v81, v81\n"
    "v_exp_f32 v82, v82\n"
    "v_exp_f32 v83, v83\n"
    "ds_read_b128 v[160:163], v42 offset:17408\n"
    "v_pk_fma_f32 v[76:77], v[76:77], v[66:67], v[66:67]\n"
    "v_pk_fma_f32 v[78:79], v[78:79], v[66:67], v[66:67]\n"
    "v_pk_fma_f32 v[80:81], v[80:81], v[66:67], v[66:67]\n"
    "v_pk_fma_f32 v[82:83], v[82:83], v[66:67], v[66:67]\n"
    "v_rcp_f32 v76, v76\n"
    "v_rcp_f32 v77, v77\n"
    "ds_read_b128 v[164:167], v42 offset:18432\n"
    "v_rcp_f32 v78, v78\n"
    "v_rcp_f32 v79, v79\n"
    "v_rcp_f32 v80, v80\n"
    "ds_read_b128 v[168:171], v42 offset:19456\n"
    "v_rcp_f32 v81, v81\n"
    "v_rcp_f32 v82, v82\n"
    "v_rcp_f32 v83, v83\n"
    "ds_read_b128 v[172:175], v42 offset:20480\n"
    "v_pk_fma_f32 v[84:85], v[210:211], v[76:77], v[84:85]\n"
    "v_pk_fma_f32 v[86:87], v[214:215], v[78:79], v[86:87]\n"
    "v_pk_fma_f32 v[88:89], v[218:219], v[80:81], v[88:89]\n"
    "v_pk_fma_f32 v[90:91], v[222:223], v[82:83], v[90:91]\n"
    "s_setprio 2\n"
    "v_pk_mul_f32 v[76:77], v[224:225], v[224:225]\n"
    "v_pk_mul_f32 v[78:79], v[228:229], v[228:229]\n"
    "v_pk_mul_f32 v[80:81], v[232:233], v[232:233]\n"
    "v_pk_mul_f32 v[82:83], v[236:237], v[236:237]\n"
    "v_pk_fma_f32 v[76:77], v[76:77], s[64:65], s[64:65] op_sel:[0,0,1] op_sel_hi:[1,0,1]\n"
    "v_pk_fma_f32 v[78:79], v[78:79], s[64:65], s[64:65] op_sel:[0,0,1] op_sel_hi:[1,0,1]\n"
    "v_pk_fma_f32 v[80:81], v[80:81], s[64:65], s[64:65] op_sel:[0,0,1] op_sel_hi:[1,0,1]\n"
    "v_pk_fma_f32 v[82:83], v[82:83], s[64:65], s[64:65] op_sel:[0,0,1] op_sel_hi:[1,0,1]\n"
    "v_pk_mul_f32 v[76:77], v[76:77], v[224:225]\n"
    "v_pk_mul_f32 v[78:79], v[78:79], v[228:229]\n"
    "v_pk_mul_f32 v[80:81], v[80:81], v[232:233]\n"
    "v_pk_mul_f32 v[82:83], v[82:83], v[236:237]\n"
    "v_exp_f32 v76, v76\n"
    "v_exp_f32 v77, v77\n"
    "ds_read_b128 v[176:179], v42 offset:21504\n"
    "v_exp_f32 v78, v78\n"
    "v_exp_f32 v79, v79\n"
    "v_exp_f32 v80, v80\n"
    "ds_read_b128 v[180:183], v42 offset:22528\n"
    "v_exp_f32 v81, v81\n"
    "v_exp_f32 v82, v82\n"
    "v_exp_f32 v83, v83\n"
    "ds_read_b128 v[184:187], v42 offset:23552\n"
    "v_pk_fma_f32 v[76:77], v[76:77], v[68:69], v[68:69]\n"
    "v_pk_fma_f32 v[78:79], v[78:79], v[68:69], v[68:69]\n"
    "v_pk_fma_f32 v[80:81], v[80:81], v[68:69], v[68:69]\n"
    "v_pk_fma_f32 v[82:83], v[82:83], v[68:69], v[68:69]\n"
    "v_rcp_f32 v76, v76\n"
    "v_rcp_f32 v77, v77\n"
    "ds_read_b128 v[188:191], v42 offset:24576\n"
    "v_rcp_f32 v78, v78\n"
    "v_rcp_f32 v79, v79\n"
    "v_rcp_f32 v80, v80\n"
    "ds_read_b128 v[192:195], v42 offset:25600\n"
    "v_rcp_f32 v81, v81\n"
    "v_rcp_f32 v82, v82\n"
    "v_rcp_f32 v83, v83\n"
    "ds_read_b128 v[196:199], v42 offset:26624\n"
    "v_pk_fma_f32 v[84:85], v[224:225], v[76:77], v[84:85]\n"
    "v_pk_fma_f32 v[86:87], v[228:229], v[78:79], v[86:87]\n"
    "v_pk_fma_f32 v[88:89], v[232:233], v[80:81], v[88:89]\n"
    "v_pk_fma_f32 v[90:91], v[236:237], v[82:83], v[90:91]\n"
    "v_pk_mul_f32 v[76:77], v[226:227], v[226:227]\n"
    "v_pk_mul_f32 v[78:79], v[230:231], v[230:231]\n"
    "v_pk_mul_f32 v[80:81], v[234:235], v[234:235]\n"
    "v_pk_mul_f32 v[82:83], v[238:239], v[238:239]\n"
    "v_pk_fma_f32 v[76:77], v[76:77], s[64:65], s[64:65] op_sel:[0,0,1] op_sel_hi:[1,0,1]\n"
    "v_pk_fma_f32 v[78:79], v[78:79], s[64:65], s[64:65] op_sel:[0,0,1] op_sel_hi:[1,0,1]\n"
    "v_pk_fma_f32 v[80:81], v[80:81], s[64:65], s[64:65] op_sel:[0,0,1] op_sel_hi:[1,0,1]\n"
    "v_pk_fma_f32 v[82:83], v[82:83], s[64:65], s[64:65] op_sel:[0,0,1] op_sel_hi:[1,0,1]\n"
    "v_pk_mul_f32 v[76:77], v[76:77], v[226:227]\n"
    "v_pk_mul_f32 v[78:79], v[78:79], v[230:231]\n"
    "v_pk_mul_f32 v[80:81], v[80:81], v[234:235]\n"
    "v_pk_mul_f32 v[82:83], v[82:83], v[238:239]\n"
    "v_exp_f32 v76, v76\n"
    "v_exp_f32 v77, v77\n"
    "ds_read_b128 v[200:203], v42 offset:27648\n"
    "v_exp_f32 v78, v78\n"
    "v_exp_f32 v79, v79\n"
    "v_exp_f32 v80, v80\n"
    "ds_read_b128 v[204:207], v42 offset:28672\n"
    "v_exp_f32 v81, v81\n"
    "v_exp_f32 v82, v82\n"
    "v_exp_f32 v83, v83\n"
    "ds_read_b128 v[208:211], v42 offset:29696\n"
    "v_pk_fma_f32 v[76:77], v[76:77], v[70:71], v[70:71]\n"
    "v_pk_fma_f32 v[78:79], v[78:79], v[70:71], v[70:71]\n"
    "v_pk_fma_f32 v[80:81], v[80:81], v[70:71], v[70:71]\n"
    "v_pk_fma_f32 v[82:83], v[82:83], v[70:71], v[70:71]\n"
    "v_rcp_f32 v76, v76\n"
    "v_rcp_f32 v77, v77\n"
    "ds_read_b128 v[212:215], v42 offset:30720\n"
    "v_rcp_f32 v78, v78\n"
    "v_rcp_f32 v79, v79\n"
    "v_rcp_f32 v80, v80\n"
    "ds_read_b128 v[216:219], v42 offset:31744\n"
    "v_rcp_f32 v81, v81\n"
    "v_rcp_f32 v82, v82\n"
    "v_rcp_f32 v83, v83\n"
    "ds_read_b128 v[220:223], v42 offset:32768\n"
    "v_pk_fma_f32 v[84:85], v[226:227], v[76:77], v[84:85]\n"
    "v_pk_fma_f32 v[86:87], v[230:231], v[78:79], v[86:87]\n"
    "v_pk_fma_f32 v[88:89], v[234:235], v[80:81], v[88:89]\n"
    "v_pk_fma_f32 v[90:91], v[238:239], v[82:83], v[90:91]\n"
    "s_cmp_eq_u32 s22, 3\n"
    "s_cbranch_scc1 .Lxd_on11\n"
    ".Lxd_on11r:\n"
    "s_cmp_eq_u32 s22, 0\n"
    "s_cbranch_scc1 .Lxd_on13\n"
    ".Lxd_on13r:\n"
    "s_cmp_eq_u32 s22, 1\n"
    "s_cbranch_scc1 .Lxd_on14\n"
    ".Lxd_on14r:\n"
    "s_setprio 1\n"
    "v_pk_mul_f32 v[76:77], v[240:241], v[240:241]\n"
    "v_pk_mul_f32 v[78:79], v[244:245], v[244:245]\n"
    "v_pk_mul_f32 v[80:81], v[248:249], v[248:249]\n"
    "v_pk_mul_f32 v[82:83], v[252:253], v[252:253]\n"
    "v_pk_fma_f32 v[76:77], v[76:77], s[64:65], s[64:65] op_sel:[0,0,1] op_sel_hi:[1,0,1]\n"
    "v_pk_fma_f32 v[78:79], v[78:79], s[64:65], s[64:65] op_sel:[0,0,1] op_sel_hi:[1,0,1]\n"
    "v_pk_fma_f32 v[80:81], v[80:81], s[64:65], s[64:65] op_sel:[0,0,1] op_sel_hi:[1,0,1]\n"
    "v_pk_fma_f32 v[82:83], v[82:83], s[64:65], s[64:65] op_sel:[0,0,1] op_sel_hi:[1,0,1]\n"
    "v_pk_mul_f32 v[76:77], v[76:77], v[240:241]\n"
    "v_pk_mul_f32 v[78:79], v[78:79], v[244:245]\n"
    "v_pk_mul_f32 v[80:81], v[80:81], v[248:249]\n"
    "v_pk_mul_f32 v[82:83], v[82:83], v[252:253]\n"
    "v_exp_f32 v76, v76\n"
    "v_exp_f32 v77, v77\n"
    "v_exp_f32 v78, v78\n"
    "v_exp_f32 v79, v79\n"
    "v_exp_f32 v80, v80\n"
    "v_exp_f32 v81, v81\n"
    "v_exp_f32 v82, v82\n"
    "v_exp_f32 v83, v83\n"
    "v_pk_fma_f32 v[76:77], v[76:77], v[72:73], v[72:73]\n"
    "v_pk_fma_f32 v[78:79], v[78:79], v[72:73], v[72:73]\n"
    "v_pk_fma_f32 v[80:81], v[80:81], v[72:73], v[72:73]\n"
    "v_pk_fma_f32 v[82:83], v[82:83], v[72:73], v[72:73]\n"
    "v_rcp_f32 v76, v76\n"
    "v_rcp_f32 v77, v77\n"
    "v_rcp_f32 v78, v78\n"
    "v_rcp_f32 v79, v79\n"
    "v_rcp_f32 v80, v80\n"
    "v_rcp_f32 v81, v81\n"
    "v_rcp_f32 v82, v82\n"
    "v_rcp_f32 v83, v83\n"
    "v_pk_fma_f32 v[84:85], v[240:241], v[76:77], v[84:85]\n"
    "v_pk_fma_f32 v[86:87], v[244:245], v[78:79], v[86:87]\n"
    "v_pk_fma_f32 v[88:89], v[248:249], v[80:81], v[88:89]\n"
    "v_pk_fma_f32 v[90:91], v[252:253], v[82:83], v[90:91]\n"
    "s_setprio 0\n"
    "v_pk_mul_f32 v[76:77], v[242:243], v[242:243]\n"
    "v_pk_mul_f32 v[78:79], v[246:247], v[246:247]\n"
    "v_pk_mul_f32 v[80:81], v[250:251], v[250:251]\n"
    "v_pk_mul_f32 v[82:83], v[254:255], v[254:255]\n"
    "v_pk_fma_f32 v[76:77], v[76:77], s[64:65], s[64:65] op_sel:[0,0,1] op_sel_hi:[1,0,1]\n"
    "v_pk_fma_f32 v[78:79], v[78:79], s[64:65], s[64:65] op_sel:[0,0,1] op_sel_hi:[1,0,1]\n"
    "v_pk_fma_f32 v[80:81], v[80:81], s[64:65], s[64:65] op_sel:[0,0,1] op_sel_hi:[1,0,1]\n"
    "v_pk_fma_f32 v[82:83], v[82:83], s[64:65], s[64:65] op_sel:[0,0,1] op_sel_hi:[1,0,1]\n"
    "v_pk_mul_f32 v[76:77], v[76:77], v[242:243]\n"
    "v_pk_mul_f32 v[78:79], v[78:79], v[246:247]\n"
    "v_pk_mul_f32 v[80:81], v[80:81], v[250:251]\n"
    "v_pk_mul_f32 v[82:83], v[82:83], v[254:255]\n"
    "v_exp_f32 v76, v76\n"
    "v_exp_f32 v77, v77\n"
    "v_exp_f32 v78, v78\n"
    "v_exp_f32 v79, v79\n"
    "v_exp_f32 v80, v80\n"
    "v_exp_f32 v81, v81\n"
    "v_exp_f32 v82, v82\n"
    "v_exp_f32 v83, v83\n"
    "v_pk_fma_f32 v[76:77], v[76:77], v[74:75], v[74:75]\n"
    "v_pk_fma_f32 v[78:79], v[78:79], v[74:75], v[74:75]\n"
    "v_pk_fma_f32 v[80:81], v[80:81], v[74:75], v[74:75]\n"
    "v_pk_fma_f32 v[82:83], v[82:83], v[74:75], v[74:75]\n"
    "v_rcp_f32 v76, v76\n"
    "v_rcp_f32 v77, v77\n"
    "v_rcp_f32 v78, v78\n"
    "v_rcp_f32 v79, v79\n"
    "v_rcp_f32 v80, v80\n"
    "v_rcp_f32 v81, v81\n"
    "v_rcp_f32 v82, v82\n"
    "v_rcp_f32 v83, v83\n"
    "v_pk_fma_f32 v[84:85], v[242:243], v[76:77], v[84:85]\n"
    "v_pk_fma_f32 v[86:87], v[246:247], v[78:79], v[86:87]\n"
    "v_pk_fma_f32 v[88:89], v[250:251], v[80:81], v[88:89]\n"
    "v_pk_fma_f32 v[90:91], v[254:255], v[82:83], v[90:91]\n"
    "v_add_f32 v84, v84, v85\n"
    "v_add_f32 v86, v86, v87\n"
    "v_add_f32 v88, v88, v89\n"
    "v_add_f32 v90, v90, v91\n"
    "s_waitcnt lgkmcnt(15)\n"
    "v_pk_mul_f16 v60, v44, s45 op_sel:[0,1]\n"
    "v_pk_mul_f16 v61, v45, s45 op_sel:[0,1]\n"
    "v_permlane32_swap_b32 v84, v88\n"
    "v_permlane32_swap_b32 v86, v90\n"
    "v_add_f32 v84, v84, v88\n"
    "v_add_f32 v86, v86, v90\n"
    "v_pk_mul_f16 v62, v46, s45 op_sel:[0,1]\n"
    "v_pk_mul_f16 v63, v47, s45 op_sel:[0,1]\n"
    "v_permlane16_swap_b32 v84, v86\n"
    "v_add_f32 v84, v84, v86\n"
    "v_and_b32 v26, -2, v84\n"
    "s_waitcnt lgkmcnt(0)\n"
    "ds_wrxchg_rtn_b32 v27, v11, v26\n"
    "ds_read_b128 v[224:227], v42 offset:33792\n"
    "ds_read_b128 v[228:231], v42 offset:34816\n"
    "ds_read_b128 v[232:235], v42 offset:35840\n"
    "ds_read_b128 v[236:239], v42 offset:36864\n"
    "ds_read_b128 v[240:243], v42 offset:37888\n"
    "ds_read_b128 v[244:247], v42 offset:38912\n"
    "ds_read_b128 v[248:251], v42 offset:39936\n"
    "ds_read_b128 v[252:255], v42 offset:40960\n"
    "v_pk_mul_f16 v64, v44, v46\n"
    "v_pk_mul_f16 v65, v45, v47\n"
    "v_pk_mul_f16 v66, v48, v48 op_sel_hi:[0,1]\n"
    "v_pk_mul_f16 v67, v48, v49 op_sel_hi:[0,1]\n"
    "v_mov_b64 v[88:89], 0\n"
    "v_mov_b64 v[90:91], 0\n"
    "s_mov_b32 s46, -1\n"
    ".Lxd_enter:\n"
    "s_cmp_eq_u32 s21, 0\n"
    "s_cselect_b64 vcc, 0, -1\n"
    "s_cbranch_scc1 .Lxd_pr\n"
    "s_setprio 1\n"
    ".Lxd_pr:\n"
    "s_cmp_lg_u32 s36, 0\n"
    "s_cbranch_scc1 .Lxd_el\n"
    "s_setpc_b64 s[10:11]\n"
    ".Lxd_el:\n"
    "s_cmp_eq_u32 s22, 0\n"
    "s_cbranch_scc1 .Lxd_pl1\n"
    "s_branch .Lxd_pl2\n"
    ".Lxd_ph0:\n"
    "v_smfmac_f32_16x16x64_f16 v[88:91], v[112:115], v[60:67], v2\n"
    "s_cbranch_vccnz .Lxd_pr15\n"
    "s_setprio 2\n"
    ".Lxd_pr15:\n"
    "v_pk_mul_f16 v68, v58, v58 op_sel_hi:[0,1]\n"
    "v_pk_mul_f16 v69, v58, v49 op_sel_hi:[0,1]\n"
    "v_mfma_f32_16x16x32_f16 v[84:87], v[108:111], v[64:67], 0\n"
    "v_fmac_f32 v11, v76, v80\n"
    "v_fmac_f32 v11, v77, v81\n"
    "v_mfma_f32_16x16x32_f16 v[84:87], v[96:99], v[60:63], v[84:87]\n"
    "v_fmac_f32 v11, v78, v82\n"
    "v_fma_f32 v10, v79, v83, v11\n"
    "v_mfma_f32_16x16x32_f16 v[80:83], v[92:95], v[60:63], 0\n"
    "s_waitcnt lgkmcnt(0)\n"
    "v_pk_mul_f16 v70, v48, v58 op_sel_hi:[0,1]\n"
    "v_pk_mul_f16 v71, v48, v59 op_sel_hi:[0,1]\n"
    "v_mfma_f32_16x16x32_f16 v[80:83], v[104:107], v[64:67], v[80:83]\n"
    "s_waitcnt vmcnt(0)\n"
    "v_pk_mul_f16 v72, v50, v58 op_sel_hi:[0,1]\n"
    "v_pk_mul_f16 v73, v50, v59 op_sel_hi:[0,1]\n"
    "v_mfma_f32_16x16x32_f16 v[76:79], v[100:103], v[64:67], 0\n"
    "s_cmp_lt_i32 s46, 0\n"
    "v_pk_mul_f16 v74, v52, v58 op_sel_hi:[0,1]\n"
    "v_pk_mul_f16 v75, v52, v59 op_sel_hi:[0,1]\n"
    "v_mfma_f32_16x16x32_f16 v[76:79], v[116:119], v[68:71], v[76:79]\n"
    "s_cbranch_scc1 .Lxd_ht18\n"
    ".Lxd_ht18r:\n"
    "v_pk_mul_f16 v60, v54, v58 op_sel_hi:[0,1]\n"
    "v_pk_mul_f16 v61, v54, v59 op_sel_hi:[0,1]\n"
    "v_mfma_f32_16x16x32_f16 v[76:79], v[128:131], v[72:75], v[76:79]\n"
    "v_pk_mul_f16 v62, v56, v58 op_sel_hi:[0,1]\n"
    "v_readfirstlane_b32 s40, v24\n"
    "v_mfma_f32_16x16x32_f16 v[80:83], v[132:135], v[72:75], v[80:83]\n"
    "s_or_b32 s40, s40, s44\n"
    "v_pk_mul_f16 v63, v56, v59 op_sel_hi:[0,1]\n"
    "v_permlane32_swap_b32 v7, v8\n"
    "v_mfma_f32_16x16x32_f16 v[80:83], v[120:123], v[68:71], v[80:83]\n"
    "s_lshl1_add_u32 s41, s40, s54\n"
    "v_pk_mul_f16 v64, v56, v56 op_sel_hi:[0,1]\n"
    "v_permlane32_swap_b32 v9, v10\n"
    "v_mfma_f32_16x16x32_f16 v[84:87], v[124:127], v[68:71], v[84:87]\n"
    "s_cmpk_lt_u32 s40, 256\n"
    "v_pk_mul_f16 v65, v56, v49 op_sel_hi:[0,1]\n"
    "v_mul_f32 v7, v0, v7\n"
    "v_mfma_f32_16x16x32_f16 v[84:87], v[136:139], v[72:75], v[84:87]\n"
    "s_cselect_b32 s41, s41, 2\n"
    "v_pk_mul_f16 v66, v48, v56 op_sel_hi:[0,1]\n"
    "v_mov_b32 v12, s41\n"
    "v_smfmac_f32_16x16x64_f16 v[88:91], v[140:143], v[68:75], v2\n"
    "s_cbranch_scc0 .Lxd_tf17\n"
    ".Lxd_tf17r:\n"
    "v_pk_mul_f16 v67, v48, v57 op_sel_hi:[0,1]\n"
    "v_mul_f32 v9, v0, v9\n"
    "v_mfma_f32_16x16x32_f16 v[76:79], v[144:147], v[60:63], v[76:79]\n"
    "s_mul_i32 s41, s41, 52\n"
    "v_pk_mul_f16 v68, v50, v56 op_sel_hi:[0,1]\n"
    "v_pk_mul_f16 v69, v50, v57 op_sel_hi:[0,1]\n"
    "v_mfma_f32_16x16x32_f16 v[76:79], v[156:159], v[64:67], v[76:79]\n"
    "s_add_u32 s38, s4, s41\n"
    "v_pk_mul_f16 v70, v52, v56 op_sel_hi:[0,1]\n"
    "v_fmac_f32 v7, v23, v8\n"
    "v_mfma_f32_16x16x32_f16 v[80:83], v[160:163], v[64:67], v[80:83]\n"
    "s_addc_u32 s39, s5, 0\n"
    "v_pk_mul_f16 v71, v52, v57 op_sel_hi:[0,1]\n"
    "v_fmac_f32 v9, v23, v10\n"
    "v_mfma_f32_16x16x32_f16 v[80:83], v[148:151], v[60:63], v[80:83]\n"
    "ds_read_b32 v22, v25 offset:516\n"
    "v_pk_mul_f16 v72, v54, v56 op_sel_hi:[0,1]\n"
    "v_permlane16_swap_b32 v7, v9\n"
    "v_mfma_f32_16x16x32_f16 v[84:87], v[152:155], v[60:63], v[84:87]\n"
    "global_load_dwordx3 v[36:38], v17, s[38:39] offset:-104\n"
    "v_pk_mul_f16 v73, v54, v57 op_sel_hi:[0,1]\n"
    "v_add_f32 v7, v7, v9\n"
    "v_mfma_f32_16x16x32_f16 v[84:87], v[164:167], v[64:67], v[84:87]\n"
    "global_load_dword v41, v18, s[38:39] offset:-8\n"
    "v_pk_mul_f16 v74, v54, v54 op_sel_hi:[0,1]\n"
    "v_add_f32_dpp v7, v7, v7 quad_perm:[1,0,3,2] row_mask:0xf bank_mask:0xf\n"
    "v_smfmac_f32_16x16x64_f16 v[88:91], v[168:171], v[60:67], v2\n"
    "ds_write_b32 v25, v12 offset:20\n"
    "v_pk_mul_f16 v75, v54, v49 op_sel_hi:[0,1]\n"
    "v_add_f32_dpp v7, v7, v7 quad_perm:[2,3,0,1] row_mask:0xf bank_mask:0xf\n"
    "v_mfma_f32_16x16x32_f16 v[76:79], v[172:175], v[68:71], v[76:79]\n"
    "v_pk_mul_f16 v60, v48, v54 op_sel_hi:[0,1]\n"
    "v_pk_mul_f16 v61, v48, v55 op_sel_hi:[0,1]\n"
    "v_mfma_f32_16x16x32_f16 v[76:79], v[184:187], v[72:75], v[76:79]\n"
    "s_waitcnt lgkmcnt(1)\n"
    "v_pk_mul_f16 v62, v50, v54 op_sel_hi:[0,1]\n"
    "v_readfirstlane_b32 s45, v22\n"
    "v_mfma_f32_16x16x32_f16 v[80:83], v[188:191], v[72:75], v[80:83]\n"
    "s_cmp_lt_i32 s45, 16\n"
    "v_pk_mul_f16 v63, v50, v55 op_sel_hi:[0,1]\n"
    "v_add_f32_dpp v7, v7, v7 row_half_mirror row_mask:0xf bank_mask:0xf\n"
    "v_mfma_f32_16x16x32_f16 v[80:83], v[176:179], v[68:71], v[80:83]\n"
    "s_cbranch_scc1 .Lxd_ck19\n"
    ".Lxd_ck19r:\n"
    "v_pk_mul_f16 v64, v52, v54 op_sel_hi:[0,1]\n"
    "v_add_f32_dpp v7, v7, v7 row_mirror row_mask:0xf bank_mask:0xf\n"
    "v_mfma_f32_16x16x32_f16 v[84:87], v[180:183], v[68:71], v[84:87]\n"
    "ds_read_b64_tr_b16 v[44:45], v5 offset:12288\n"
    "v_pk_mul_f16 v65, v52, v55 op_sel_hi:[0,1]\n"
    "v_mfma_f32_16x16x32_f16 v[84:87], v[192:195], v[72:75], v[84:87]\n"
    "ds_read_b64_tr_b16 v[46:47], v6 offset:12288\n"
    "v_pk_mul_f16 v66, v52, v52 op_sel_hi:[0,1]\n"
    "v_smfmac_f32_16x16x64_f16 v[88:91], v[196:199], v[68:75], v2\n"
    "ds_write_b32 v14, v7\n"
    "v_pk_mul_f16 v67, v52, v49 op_sel_hi:[0,1]\n"
    "v_add_u32 v14, 64, v14\n"
    "v_mfma_f32_16x16x32_f16 v[76:79], v[200:203], v[60:63], v[76:79]\n"
    "v_pk_mul_f16 v68, v48, v52 op_sel_hi:[0,1]\n"
    "v_pk_mul_f16 v69, v48, v53 op_sel_hi:[0,1]\n"
    "v_mfma_f32_16x16x32_f16 v[76:79], v[212:215], v[64:67], v[76:79]\n"
    "v_pk_mul_f16 v70, v50, v52 op_sel_hi:[0,1]\n"
    "v_mfma_f32_16x16x32_f16 v[80:83], v[216:219], v[64:67], v[80:83]\n"
    "s_cbranch_vccnz .Lxd_pr16\n"
    "s_setprio 0\n"
    ".Lxd_pr16:\n"
    "v_pk_mul_f16 v71, v50, v53 op_sel_hi:[0,1]\n"
    "v_mfma_f32_16x16x32_f16 v[80:83], v[204:207], v[60:63], v[80:83]\n"
    "v_pk_mul_f16 v72, v50, v50 op_sel_hi:[0,1]\n"
    "v_mfma_f32_16x16x32_f16 v[84:87], v[208:211], v[60:63], v[84:87]\n"
    "v_pk_mul_f16 v73, v50, v49 op_sel_hi:[0,1]\n"
    "v_pk_mul_f16 v74, v48, v50 op_sel_hi:[0,1]\n"
    "v_mfma_f32_16x16x32_f16 v[84:87], v[220:223], v[64:67], v[84:87]\n"
    "v_pk_mul_f16 v75, v48, v51 op_sel_hi:[0,1]\n"
    "v_smfmac_f32_16x16x64_f16 v[88:91], v[224:227], v[60:67], v2\n"
    "ds_read_b64_tr_b16 v[48:49], v3 offset:12288\n"
    "ds_read_b64_tr_b16 v[58:59], v4 offset:15168\n"
    "v_smfmac_f32_16x16x64_f16 v[88:91], v[252:255], v[68:75], v2\n"
    "s_waitcnt lgkmcnt(3)\n"
    "v_pk_mul_f16 v60, v44, s45 op_sel:[0,1]\n"
    "v_pk_mul_f16 v61, v45, s45 op_sel:[0,1]\n"
    "v_mfma_f32_16x16x32_f16 v[84:87], v[236:239], v[68:71], v[84:87]\n"
    "ds_read_b64_tr_b16 v[50:51], v4 offset:12864\n"
    "v_pk_mul_f16 v62, v46, s45 op_sel:[0,1]\n"
    "v_pk_mul_f16 v63, v47, s45 op_sel:[0,1]\n"
    "v_mfma_f32_16x16x32_f16 v[84:87], v[248:251], v[72:75], v[84:87]\n"
    "ds_read_b64_tr_b16 v[52:53], v4 offset:13440\n"
    "v_pk_mul_f16 v64, v44, v46\n"
    "v_pk_mul_f16 v65, v45, v47\n"
    "v_mfma_f32_16x16x32_f16 v[76:79], v[240:243], v[72:75], v[76:79]\n"
    "ds_read_b64_tr_b16 v[54:55], v4 offset:14016\n"
    "v_add_f32 v11, v88, v89\n"
    "v_add_f32 v13, v90, v91\n"
    "v_mfma_f32_16x16x32_f16 v[76:79], v[228:231], v[68:71], v[76:79]\n"
    "ds_read_b64_tr_b16 v[56:57], v4 offset:14592\n"
    "v_mov_b64 v[88:89], 0\n"
    "v_mov_b64 v[90:91], 0\n"
    "v_mfma_f32_16x16x32_f16 v[80:83], v[232:235], v[68:71], v[80:83]\n"
    "s_waitcnt lgkmcnt(4)\n"
    "v_pk_mul_f16 v66, v48, v48 op_sel_hi:[0,1]\n"
    "v_pk_mul_f16 v67, v48, v49 op_sel_hi:[0,1]\n"
    "v_mfma_f32_16x16x32_f16 v[80:83], v[244:247], v[72:75], v[80:83]\n"
    "s_cmp_lg_u32 s36, 0\n"
    "v_mul_f32 v11, v11, v13\n"
    "v_fmac_f32 v11, v84, v85\n"
    "v_fmac_f32 v11, v86, v87\n"
    "s_cbranch_scc1 .Lxd_pl1\n"
    ".Lxd_ph1:\n"
    "v_smfmac_f32_16x16x64_f16 v[88:91], v[112:115], v[60:67], v2\n"
    "s_cbranch_vccnz .Lxd_pr20\n"
    "s_setprio 2\n"
    ".Lxd_pr20:\n"
    "v_pk_mul_f16 v68, v58, v58 op_sel_hi:[0,1]\n"
    "v_pk_mul_f16 v69, v58, v49 op_sel_hi:[0,1]\n"
    "v_mfma_f32_16x16x32_f16 v[84:87], v[108:111], v[64:67], 0\n"
    "v_fmac_f32 v11, v76, v80\n"
    "v_fmac_f32 v11, v77, v81\n"
    "v_mfma_f32_16x16x32_f16 v[84:87], v[96:99], v[60:63], v[84:87]\n"
    "v_fmac_f32 v11, v78, v82\n"
    "v_fma_f32 v7, v79, v83, v11\n"
    "v_mfma_f32_16x16x32_f16 v[80:83], v[92:95], v[60:63], 0\n"
    "s_waitcnt lgkmcnt(0)\n"
    "v_pk_mul_f16 v70, v48, v58 op_sel_hi:[0,1]\n"
    "v_pk_mul_f16 v71, v48, v59 op_sel_hi:[0,1]\n"
    "v_mfma_f32_16x16x32_f16 v[80:83], v[104:107], v[64:67], v[80:83]\n"
    "s_waitcnt vmcnt(0)\n"
    "v_pk_mul_f16 v72, v50, v58 op_sel_hi:[0,1]\n"
    "v_pk_mul_f16 v73, v50, v59 op_sel_hi:[0,1]\n"
    "v_mfma_f32_16x16x32_f16 v[76:79], v[100:103], v[64:67], 0\n"
    "s_cmp_lg_u32 s46, 0\n"
    "v_pk_mul_f16 v74, v52, v58 op_sel_hi:[0,1]\n"
    "v_pk_mul_f16 v75, v52, v59 op_sel_hi:[0,1]\n"
    "v_mfma_f32_16x16x32_f16 v[76:79], v[116:119], v[68:71], v[76:79]\n"
    "s_cbranch_scc1 .Lxd_ht22\n"
    ".Lxd_ht22r:\n"
    "v_pk_mul_f16 v60, v54, v58 op_sel_hi:[0,1]\n"
    "v_pk_mul_f16 v61, v54, v59 op_sel_hi:[0,1]\n"
    "v_mfma_f32_16x16x32_f16 v[76:79], v[128:131], v[72:75], v[76:79]\n"
    "v_pk_mul_f16 v62, v56, v58 op_sel_hi:[0,1]\n"
    "v_lshl_or_b32 v36, v36, 7, v19\n"
    "v_mfma_f32_16x16x32_f16 v[80:83], v[132:135], v[72:75], v[80:83]\n"
    "global_load_dwordx4 v[28:31], v36, s[8:9]\n"
    "v_pk_mul_f16 v63, v56, v59 op_sel_hi:[0,1]\n"
    "v_lshl_or_b32 v37, v37, 7, v19\n"
    "v_mfma_f32_16x16x32_f16 v[80:83], v[120:123], v[68:71], v[80:83]\n"
    "global_load_dwordx4 v[32:35], v37, s[8:9]\n"
    "v_pk_mul_f16 v64, v56, v56 op_sel_hi:[0,1]\n"
    "v_lshl_or_b32 v41, v41, 7, v20\n"
    "v_mfma_f32_16x16x32_f16 v[84:87], v[124:127], v[68:71], v[84:87]\n"
    "global_load_dword v40, v41, s[8:9]\n"
    "v_pk_mul_f16 v65, v56, v49 op_sel_hi:[0,1]\n"
    "v_lshl_or_b32 v38, v38, 7, v19\n"
    "v_mfma_f32_16x16x32_f16 v[84:87], v[136:139], v[72:75], v[84:87]\n"
    "global_load_dwordx4 v[36:39], v38, s[8:9]\n"
    "v_pk_mul_f16 v66, v48, v56 op_sel_hi:[0,1]\n"
    "v_xor_b32 v15, 0x4000, v15\n"
    "v_smfmac_f32_16x16x64_f16 v[88:91], v[140:143], v[68:75], v2\n"
    "v_pk_mul_f16 v67, v48, v57 op_sel_hi:[0,1]\n"
    "v_xor_b32 v16, 0x4000, v16\n"
    "v_mfma_f32_16x16x32_f16 v[76:79], v[144:147], v[60:63], v[76:79]\n"
    "v_pk_mul_f16 v68, v50, v56 op_sel_hi:[0,1]\n"
    "v_pk_mul_f16 v69, v50, v57 op_sel_hi:[0,1]\n"
    "v_mfma_f32_16x16x32_f16 v[76:79], v[156:159], v[64:67], v[76:79]\n"
    "v_pk_mul_f16 v70, v52, v56 op_sel_hi:[0,1]\n"
    "v_xor_b32 v3, 0x4000, v3\n"
    "v_mfma_f32_16x16x32_f16 v[80:83], v[160:163], v[64:67], v[80:83]\n"
    "v_pk_mul_f16 v71, v52, v57 op_sel_hi:[0,1]\n"
    "v_xor_b32 v4, 0x4000, v4\n"
    "v_mfma_f32_16x16x32_f16 v[80:83], v[148:151], v[60:63], v[80:83]\n"
    "ds_read_b32 v22, v25 offset:520\n"
    "v_pk_mul_f16 v72, v54, v56 op_sel_hi:[0,1]\n"
    "v_xor_b32 v5, 0x4000, v5\n"
    "v_mfma_f32_16x16x32_f16 v[84:87], v[152:155], v[60:63], v[84:87]\n"
    "v_pk_mul_f16 v73, v54, v57 op_sel_hi:[0,1]\n"
    "v_xor_b32 v6, 0x4000, v6\n"
    "v_mfma_f32_16x16x32_f16 v[84:87], v[164:167], v[64:67], v[84:87]\n"
    "v_pk_mul_f16 v74, v54, v54 op_sel_hi:[0,1]\n"
    "v_smfmac_f32_16x16x64_f16 v[88:91], v[168:171], v[60:67], v2\n"
    "v_pk_mul_f16 v75, v54, v49 op_sel_hi:[0,1]\n"
    "v_mfma_f32_16x16x32_f16 v[76:79], v[172:175], v[68:71], v[76:79]\n"
    "v_pk_mul_f16 v60, v48, v54 op_sel_hi:[0,1]\n"
    "v_pk_mul_f16 v61, v48, v55 op_sel_hi:[0,1]\n"
    "v_mfma_f32_16x16x32_f16 v[76:79], v[184:187], v[72:75], v[76:79]\n"
    "s_waitcnt lgkmcnt(0)\n"
    "v_pk_mul_f16 v62, v50, v54 op_sel_hi:[0,1]\n"
    "v_readfirstlane_b32 s45, v22\n"
    "v_mfma_f32_16x16x32_f16 v[80:83], v[188:191], v[72:75], v[80:83]\n"
    "s_cmp_lt_i32 s45, 16\n"
    "v_pk_mul_f16 v63, v50, v55 op_sel_hi:[0,1]\n"
    "v_mfma_f32_16x16x32_f16 v[80:83], v[176:179], v[68:71], v[80:83]\n"
    "s_cbranch_scc1 .Lxd_ck23\n"
    ".Lxd_ck23r:\n"
    "v_pk_mul_f16 v64, v52, v54 op_sel_hi:[0,1]\n"
    "v_mfma_f32_16x16x32_f16 v[84:87], v[180:183], v[68:71], v[84:87]\n"
    "ds_read_b64_tr_b16 v[44:45], v5 offset:0\n"
    "v_pk_mul_f16 v65, v52, v55 op_sel_hi:[0,1]\n"
    "v_mfma_f32_16x16x32_f16 v[84:87], v[192:195], v[72:75], v[84:87]\n"
    "ds_read_b64_tr_b16 v[46:47], v6 offset:0\n"
    "v_pk_mul_f16 v66, v52, v52 op_sel_hi:[0,1]\n"
    "v_smfmac_f32_16x16x64_f16 v[88:91], v[196:199], v[68:75], v2\n"
    "v_pk_mul_f16 v67, v52, v49 op_sel_hi:[0,1]\n"
    "v_mfma_f32_16x16x32_f16 v[76:79], v[200:203], v[60:63], v[76:79]\n"
    "v_pk_mul_f16 v68, v48, v52 op_sel_hi:[0,1]\n"
    "v_pk_mul_f16 v69, v48, v53 op_sel_hi:[0,1]\n"
    "v_mfma_f32_16x16x32_f16 v[76:79], v[212:215], v[64:67], v[76:79]\n"
    "v_pk_mul_f16 v70, v50, v52 op_sel_hi:[0,1]\n"
    "v_mfma_f32_16x16x32_f16 v[80:83], v[216:219], v[64:67], v[80:83]\n"
    "s_cbranch_vccnz .Lxd_pr21\n"
    "s_setprio 0\n"
    ".Lxd_pr21:\n"
    "v_pk_mul_f16 v71, v50, v53 op_sel_hi:[0,1]\n"
    "v_mfma_f32_16x16x32_f16 v[80:83], v[204:207], v[60:63], v[80:83]\n"
    "v_pk_mul_f16 v72, v50, v50 op_sel_hi:[0,1]\n"
    "v_mfma_f32_16x16x32_f16 v[84:87], v[208:211], v[60:63], v[84:87]\n"
    "v_pk_mul_f16 v73, v50, v49 op_sel_hi:[0,1]\n"
    "v_pk_mul_f16 v74, v48, v50 op_sel_hi:[0,1]\n"
    "v_mfma_f32_16x16x32_f16 v[84:87], v[220:223], v[64:67], v[84:87]\n"
    "v_pk_mul_f16 v75, v48, v51 op_sel_hi:[0,1]\n"
    "v_smfmac_f32_16x16x64_f16 v[88:91], v[224:227], v[60:67], v2\n"
    "ds_read_b64_tr_b16 v[48:49], v3 offset:0\n"
    "ds_read_b64_tr_b16 v[58:59], v4 offset:2880\n"
    "v_smfmac_f32_16x16x64_f16 v[88:91], v[252:255], v[68:75], v2\n"
    "s_waitcnt lgkmcnt(2)\n"
    "v_pk_mul_f16 v60, v44, s45 op_sel:[0,1]\n"
    "v_pk_mul_f16 v61, v45, s45 op_sel:[0,1]\n"
    "v_mfma_f32_16x16x32_f16 v[84:87], v[236:239], v[68:71], v[84:87]\n"
    "ds_read_b64_tr_b16 v[50:51], v4 offset:576\n"
    "v_pk_mul_f16 v62, v46, s45 op_sel:[0,1]\n"
    "v_pk_mul_f16 v63, v47, s45 op_sel:[0,1]\n"
    "v_mfma_f32_16x16x32_f16 v[84:87], v[248:251], v[72:75], v[84:87]\n"
    "ds_read_b64_tr_b16 v[52:53], v4 offset:1152\n"
    "v_pk_mul_f16 v64, v44, v46\n"
    "v_pk_mul_f16 v65, v45, v47\n"
    "v_mfma_f32_16x16x32_f16 v[76:79], v[240:243], v[72:75], v[76:79]\n"
    "ds_read_b64_tr_b16 v[54:55], v4 offset:1728\n"
    "v_add_f32 v11, v88, v89\n"
    "v_add_f32 v13, v90, v91\n"
    "v_mfma_f32_16x16x32_f16 v[76:79], v[228:231], v[68:71], v[76:79]\n"
    "ds_read_b64_tr_b16 v[56:57], v4 offset:2304\n"
    "v_mov_b64 v[88:89], 0\n"
    "v_mov_b64 v[90:91], 0\n"
    "v_mfma_f32_16x16x32_f16 v[80:83], v[232:235], v[68:71], v[80:83]\n"
    "s_waitcnt lgkmcnt(4)\n"
    "v_pk_mul_f16 v66, v48, v48 op_sel_hi:[0,1]\n"
    "v_pk_mul_f16 v67, v48, v49 op_sel_hi:[0,1]\n"
    "v_mfma_f32_16x16x32_f16 v[80:83], v[244:247], v[72:75], v[80:83]\n"
    "v_mul_f32 v11, v11, v13\n"
    "v_fmac_f32 v11, v84, v85\n"
    "v_fmac_f32 v11, v86, v87\n"
    ".Lxd_ph2:\n"
    "v_smfmac_f32_16x16x64_f16 v[88:91], v[112:115], v[60:67], v2\n"
    "s_cbranch_vccnz .Lxd_pr24\n"
    "s_setprio 2\n"
    ".Lxd_pr24:\n"
    "v_pk_mul_f16 v68, v58, v58 op_sel_hi:[0,1]\n"
    "v_pk_mul_f16 v69, v58, v49 op_sel_hi:[0,1]\n"
    "v_mfma_f32_16x16x32_f16 v[84:87], v[108:111], v[64:67], 0\n"
    "v_fmac_f32 v11, v76, v80\n"
    "v_fmac_f32 v11, v77, v81\n"
    "v_mfma_f32_16x16x32_f16 v[84:87], v[96:99], v[60:63], v[84:87]\n"
    "s_movk_i32 s43, 15744\n"
    "v_fmac_f32 v11, v78, v82\n"
    "v_fma_f32 v8, v79, v83, v11\n"
    "v_mfma_f32_16x16x32_f16 v[80:83], v[92:95], v[60:63], 0\n"
    "s_waitcnt lgkmcnt(0)\n"
    "v_pk_mul_f16 v70, v48, v58 op_sel_hi:[0,1]\n"
    "v_pk_mul_f16 v71, v48, v59 op_sel_hi:[0,1]\n"
    "v_mfma_f32_16x16x32_f16 v[80:83], v[104:107], v[64:67], v[80:83]\n"
    "s_waitcnt vmcnt(0)\n"
    "v_pk_mul_f16 v72, v50, v58 op_sel_hi:[0,1]\n"
    "v_pk_mul_f16 v73, v50, v59 op_sel_hi:[0,1]\n"
    "v_mfma_f32_16x16x32_f16 v[76:79], v[100:103], v[64:67], 0\n"
    "s_cmp_lg_u32 s46, 0\n"
    "v_pk_mul_f16 v74, v52, v58 op_sel_hi:[0,1]\n"
    "v_pk_mul_f16 v75, v52, v59 op_sel_hi:[0,1]\n"
    "v_mfma_f32_16x16x32_f16 v[76:79], v[116:119], v[68:71], v[76:79]\n"
    "s_cbranch_scc1 .Lxd_ht26\n"
    ".Lxd_ht26r:\n"
    "v_pk_mul_f16 v60, v54, v58 op_sel_hi:[0,1]\n"
    "v_pk_mul_f16 v61, v54, v59 op_sel_hi:[0,1]\n"
    "v_mfma_f32_16x16x32_f16 v[76:79], v[128:131], v[72:75], v[76:79]\n"
    "v_pk_mul_f16 v62, v56, v58 op_sel_hi:[0,1]\n"
    "v_pk_maximum3_f16 v21, v28, v28, s43 op_sel_hi:[1,1,0] neg_lo:[0,1,0] neg_hi:[0,1,0]\n"
    "v_pk_maximum3_f16 v21, v29, v29, v21 neg_lo:[0,1,0] neg_hi:[0,1,0]\n"
    "v_mfma_f32_16x16x32_f16 v[80:83], v[132:135], v[72:75], v[80:83]\n"
    "v_pk_mul_f16 v63, v56, v59 op_sel_hi:[0,1]\n"
    "v_pk_maximum3_f16 v21, v30, v30, v21 neg_lo:[0,1,0] neg_hi:[0,1,0]\n"
    "v_mfma_f32_16x16x32_f16 v[80:83], v[120:123], v[68:71], v[80:83]\n"
    "v_pk_mul_f16 v64, v56, v56 op_sel_hi:[0,1]\n"
    "v_pk_maximum3_f16 v21, v31, v31, v21 neg_lo:[0,1,0] neg_hi:[0,1,0]\n"
    "v_mfma_f32_16x16x32_f16 v[84:87], v[124:127], v[68:71], v[84:87]\n"
    "v_pk_mul_f16 v65, v56, v49 op_sel_hi:[0,1]\n"
    "v_pk_maximum3_f16 v21, v32, v32, v21 neg_lo:[0,1,0] neg_hi:[0,1,0]\n"
    "v_mfma_f32_16x16x32_f16 v[84:87], v[136:139], v[72:75], v[84:87]\n"
    "v_pk_mul_f16 v66, v48, v56 op_sel_hi:[0,1]\n"
    "v_pk_maximum3_f16 v21, v33, v33, v21 neg_lo:[0,1,0] neg_hi:[0,1,0]\n"
    "v_smfmac_f32_16x16x64_f16 v[88:91], v[140:143], v[68:75], v2\n"
    "v_pk_mul_f16 v67, v48, v57 op_sel_hi:[0,1]\n"
    "v_pk_maximum3_f16 v21, v34, v34, v21 neg_lo:[0,1,0] neg_hi:[0,1,0]\n"
    "v_mfma_f32_16x16x32_f16 v[76:79], v[144:147], v[60:63], v[76:79]\n"
    "v_pk_mul_f16 v68, v50, v56 op_sel_hi:[0,1]\n"
    "v_pk_mul_f16 v69, v50, v57 op_sel_hi:[0,1]\n"
    "v_pk_maximum3_f16 v21, v35, v35, v21 neg_lo:[0,1,0] neg_hi:[0,1,0]\n"
    "v_mfma_f32_16x16x32_f16 v[76:79], v[156:159], v[64:67], v[76:79]\n"
    "v_pk_mul_f16 v70, v52, v56 op_sel_hi:[0,1]\n"
    "v_pk_maximum3_f16 v21, v36, v36, v21 neg_lo:[0,1,0] neg_hi:[0,1,0]\n"
    "v_mfma_f32_16x16x32_f16 v[80:83], v[160:163], v[64:67], v[80:83]\n"
    "v_pk_mul_f16 v71, v52, v57 op_sel_hi:[0,1]\n"
    "v_pk_maximum3_f16 v21, v37, v37, v21 neg_lo:[0,1,0] neg_hi:[0,1,0]\n"
    "v_mfma_f32_16x16x32_f16 v[80:83], v[148:151], v[60:63], v[80:83]\n"
    "ds_read_b32 v22, v25 offset:524\n"
    "v_pk_mul_f16 v72, v54, v56 op_sel_hi:[0,1]\n"
    "v_pk_maximum3_f16 v21, v38, v38, v21 neg_lo:[0,1,0] neg_hi:[0,1,0]\n"
    "v_mfma_f32_16x16x32_f16 v[84:87], v[152:155], v[60:63], v[84:87]\n"
    "v_pk_mul_f16 v73, v54, v57 op_sel_hi:[0,1]\n"
    "v_pk_maximum3_f16 v21, v39, v39, v21 neg_lo:[0,1,0] neg_hi:[0,1,0]\n"
    "v_mfma_f32_16x16x32_f16 v[84:87], v[164:167], v[64:67], v[84:87]\n"
    "v_pk_mul_f16 v74, v54, v54 op_sel_hi:[0,1]\n"
    "v_pk_maximum3_f16 v21, v40, v40, v21 neg_lo:[0,1,0] neg_hi:[0,1,0]\n"
    "v_smfmac_f32_16x16x64_f16 v[88:91], v[168:171], v[60:67], v2\n"
    "v_pk_mul_f16 v75, v54, v49 op_sel_hi:[0,1]\n"
    "v_pk_max_u16 v21, v21, v21 op_sel:[0,1] op_sel_hi:[1,0]\n"
    "v_mfma_f32_16x16x32_f16 v[76:79], v[172:175], v[68:71], v[76:79]\n"
    "v_pk_mul_f16 v60, v48, v54 op_sel_hi:[0,1]\n"
    "v_pk_mul_f16 v61, v48, v55 op_sel_hi:[0,1]\n"
    "v_mfma_f32_16x16x32_f16 v[76:79], v[184:187], v[72:75], v[76:79]\n"
    "s_waitcnt lgkmcnt(0)\n"
    "v_pk_mul_f16 v62, v50, v54 op_sel_hi:[0,1]\n"
    "v_readfirstlane_b32 s45, v22\n"
    "v_mfma_f32_16x16x32_f16 v[80:83], v[188:191], v[72:75], v[80:83]\n"
    "s_cmp_lt_i32 s45, 16\n"
    "v_pk_mul_f16 v63, v50, v55 op_sel_hi:[0,1]\n"
    "v_max_u32_dpp v21, v21, v21 quad_perm:[1,0,3,2] row_mask:0xf bank_mask:0xf\n"
    "v_mfma_f32_16x16x32_f16 v[80:83], v[176:179], v[68:71], v[80:83]\n"
    "s_cbranch_scc1 .Lxd_ck27\n"
    ".Lxd_ck27r:\n"
    "v_pk_mul_f16 v64, v52, v54 op_sel_hi:[0,1]\n"
    "v_max_u32_dpp v21, v21, v21 quad_perm:[2,3,0,1] row_mask:0xf bank_mask:0xf\n"
    "v_mfma_f32_16x16x32_f16 v[84:87], v[180:183], v[68:71], v[84:87]\n"
    "ds_read_b64_tr_b16 v[44:45], v5 offset:4096\n"
    "v_pk_mul_f16 v65, v52, v55 op_sel_hi:[0,1]\n"
    "v_max_u32_dpp v21, v21, v21 row_half_mirror row_mask:0xf bank_mask:0xf\n"
    "v_mfma_f32_16x16x32_f16 v[84:87], v[192:195], v[72:75], v[84:87]\n"
    "ds_read_b64_tr_b16 v[46:47], v6 offset:4096\n"
    "v_pk_mul_f16 v66, v52, v52 op_sel_hi:[0,1]\n"
    "v_max_u32_dpp v21, v21, v21 row_mirror row_mask:0xf bank_mask:0xf\n"
    "v_smfmac_f32_16x16x64_f16 v[88:91], v[196:199], v[68:75], v2\n"
    "v_pk_mul_f16 v67, v52, v49 op_sel_hi:[0,1]\n"
    "v_max_u32_dpp v21, v21, v21 row_bcast:15 row_mask:0xa bank_mask:0xf\n"
    "v_mfma_f32_16x16x32_f16 v[76:79], v[200:203], v[60:63], v[76:79]\n"
    "v_pk_mul_f16 v68, v48, v52 op_sel_hi:[0,1]\n"
    "v_pk_mul_f16 v69, v48, v53 op_sel_hi:[0,1]\n"
    "v_mfma_f32_16x16x32_f16 v[76:79], v[212:215], v[64:67], v[76:79]\n"
    "v_pk_mul_f16 v70, v50, v52 op_sel_hi:[0,1]\n"
    "v_max_u32_dpp v21, v21, v21 row_bcast:31 row_mask:0xc bank_mask:0xf\n"
    "v_mfma_f32_16x16x32_f16 v[80:83], v[216:219], v[64:67], v[80:83]\n"
    "s_cbranch_vccnz .Lxd_pr25\n"
    "s_setprio 0\n"
    ".Lxd_pr25:\n"
    "v_pk_mul_f16 v71, v50, v53 op_sel_hi:[0,1]\n"
    "v_readlane_b32 s47, v21, 63\n"
    "v_mfma_f32_16x16x32_f16 v[80:83], v[204:207], v[60:63], v[80:83]\n"
    "s_and_b32 s47, s47, 0x7f80\n"
    "v_pk_mul_f16 v72, v50, v50 op_sel_hi:[0,1]\n"
    "v_mfma_f32_16x16x32_f16 v[84:87], v[208:211], v[60:63], v[84:87]\n"
    "s_sub_u32 s50, s47, 2816\n"
    "v_pk_mul_f16 v73, v50, v49 op_sel_hi:[0,1]\n"
    "v_pk_mul_f16 v74, v48, v50 op_sel_hi:[0,1]\n"
    "v_pk_maximum3_f16 v21, v28, v28, s50 op_sel_hi:[1,1,0] neg_lo:[0,1,0] neg_hi:[0,1,0]\n"
    "v_mfma_f32_16x16x32_f16 v[84:87], v[220:223], v[64:67], v[84:87]\n"
    "s_mul_i32 s51, s50, -8\n"
    "v_pk_mul_f16 v75, v48, v51 op_sel_hi:[0,1]\n"
    "v_pk_mad_u16 v21, v21, 8, s51 op_sel_hi:[1,0,0]\n"
    "v_smfmac_f32_16x16x64_f16 v[88:91], v[224:227], v[60:67], v2\n"
    "ds_read_b64_tr_b16 v[48:49], v3 offset:4096\n"
    "ds_read_b64_tr_b16 v[58:59], v4 offset:6976\n"
    "v_bfi_b32 v28, s57, v21, v28\n"
    "v_smfmac_f32_16x16x64_f16 v[88:91], v[252:255], v[68:75], v2\n"
    "s_waitcnt lgkmcnt(2)\n"
    "v_pk_mul_f16 v60, v44, s45 op_sel:[0,1]\n"
    "v_pk_mul_f16 v61, v45, s45 op_sel:[0,1]\n"
    "v_mfma_f32_16x16x32_f16 v[84:87], v[236:239], v[68:71], v[84:87]\n"
    "ds_read_b64_tr_b16 v[50:51], v4 offset:4672\n"
    "v_pk_mul_f16 v62, v46, s45 op_sel:[0,1]\n"
    "v_pk_mul_f16 v63, v47, s45 op_sel:[0,1]\n"
    "v_mfma_f32_16x16x32_f16 v[84:87], v[248:251], v[72:75], v[84:87]\n"
    "ds_read_b64_tr_b16 v[52:53], v4 offset:5248\n"
    "v_pk_mul_f16 v64, v44, v46\n"
    "v_pk_mul_f16 v65, v45, v47\n"
    "v_mfma_f32_16x16x32_f16 v[76:79], v[240:243], v[72:75], v[76:79]\n"
    "ds_read_b64_tr_b16 v[54:55], v4 offset:5824\n"
    "v_add_f32 v11, v88, v89\n"
    "v_add_f32 v13, v90, v91\n"
    "v_mfma_f32_16x16x32_f16 v[76:79], v[228:231], v[68:71], v[76:79]\n"
    "ds_read_b64_tr_b16 v[56:57], v4 offset:6400\n"
    "v_mov_b64 v[88:89], 0\n"
    "v_mov_b64 v[90:91], 0\n"
    "v_mfma_f32_16x16x32_f16 v[80:83], v[232:235], v[68:71], v[80:83]\n"
    "s_waitcnt lgkmcnt(4)\n"
    "v_pk_mul_f16 v66, v48, v48 op_sel_hi:[0,1]\n"
    "v_pk_mul_f16 v67, v48, v49 op_sel_hi:[0,1]\n"
    "v_mfma_f32_16x16x32_f16 v[80:83], v[244:247], v[72:75], v[80:83]\n"
    "s_lshr_b32 s47, s47, 3\n"
    "v_mul_f32 v11, v11, v13\n"
    "v_fmac_f32 v11, v84, v85\n"
    "v_fmac_f32 v11, v86, v87\n"
    ".Lxd_ph3:\n"
    "v_smfmac_f32_16x16x64_f16 v[88:91], v[112:115], v[60:67], v2\n"
    "s_cbranch_vccnz .Lxd_pr28\n"
    "s_setprio 2\n"
    ".Lxd_pr28:\n"
    "v_pk_mul_f16 v68, v58, v58 op_sel_hi:[0,1]\n"
    "v_pk_mul_f16 v69, v58, v49 op_sel_hi:[0,1]\n"
    "v_mfma_f32_16x16x32_f16 v[84:87], v[108:111], v[64:67], 0\n"
    "s_cmp_lt_u32 s55, 32\n"
    "v_fmac_f32 v11, v76, v80\n"
    "v_fmac_f32 v11, v77, v81\n"
    "v_pk_maximum3_f16 v12, v29, v29, s50 op_sel_hi:[1,1,0] neg_lo:[0,1,0] neg_hi:[0,1,0]\n"
    "v_mfma_f32_16x16x32_f16 v[84:87], v[96:99], v[60:63], v[84:87]\n"
    "s_cselect_b32 s44, 0, -1\n"
    "v_fmac_f32 v11, v78, v82\n"
    "v_fma_f32 v9, v79, v83, v11\n"
    "v_mfma_f32_16x16x32_f16 v[80:83], v[92:95], v[60:63], 0\n"
    "s_waitcnt lgkmcnt(0)\n"
    "v_pk_mul_f16 v70, v48, v58 op_sel_hi:[0,1]\n"
    "v_pk_mul_f16 v71, v48, v59 op_sel_hi:[0,1]\n"
    "v_pk_mad_u16 v12, v12, 8, s51 op_sel_hi:[1,0,0]\n"
    "v_mfma_f32_16x16x32_f16 v[80:83], v[104:107], v[64:67], v[80:83]\n"
    "s_add_u32 s55, s55, 4\n"
    "v_pk_mul_f16 v72, v50, v58 op_sel_hi:[0,1]\n"
    "v_pk_mul_f16 v73, v50, v59 op_sel_hi:[0,1]\n"
    "v_mfma_f32_16x16x32_f16 v[76:79], v[100:103], v[64:67], 0\n"
    "v_pk_mul_f16 v74, v52, v58 op_sel_hi:[0,1]\n"
    "v_pk_mul_f16 v75, v52, v59 op_sel_hi:[0,1]\n"
    "v_bfi_b32 v29, s57, v12, v29\n"
    "v_mfma_f32_16x16x32_f16 v[76:79], v[116:119], v[68:71], v[76:79]\n"
    "v_pk_mul_f16 v60, v54, v58 op_sel_hi:[0,1]\n"
    "v_pk_mul_f16 v61, v54, v59 op_sel_hi:[0,1]\n"
    "v_pk_maximum3_f16 v21, v30, v30, s50 op_sel_hi:[1,1,0] neg_lo:[0,1,0] neg_hi:[0,1,0]\n"
    "v_mfma_f32_16x16x32_f16 v[76:79], v[128:131], v[72:75], v[76:79]\n"
    "v_pk_mul_f16 v62, v56, v58 op_sel_hi:[0,1]\n"
    "v_add_u32_e64 v24, s44, 1\n"
    "v_mfma_f32_16x16x32_f16 v[80:83], v[132:135], v[72:75], v[80:83]\n"
    "s_sub_u32 s66, 4112, s47\n"
    "v_pk_mul_f16 v63, v56, v59 op_sel_hi:[0,1]\n"
    "v_pk_mad_u16 v21, v21, 8, s51 op_sel_hi:[1,0,0]\n"
    "v_bfi_b32 v30, s57, v21, v30\n"
    "v_mfma_f32_16x16x32_f16 v[80:83], v[120:123], v[68:71], v[80:83]\n"
    "s_lshl_b32 s66, s66, 19\n"
    "v_pk_mul_f16 v64, v56, v56 op_sel_hi:[0,1]\n"
    "v_pk_maximum3_f16 v12, v31, v31, s50 op_sel_hi:[1,1,0] neg_lo:[0,1,0] neg_hi:[0,1,0]\n"
    "v_pk_mad_u16 v12, v12, 8, s51 op_sel_hi:[1,0,0]\n"
    "v_mfma_f32_16x16x32_f16 v[84:87], v[124:127], v[68:71], v[84:87]\n"
    "v_pk_mul_f16 v65, v56, v49 op_sel_hi:[0,1]\n"
    "v_bfi_b32 v31, s57, v12, v31\n"
    "v_mfma_f32_16x16x32_f16 v[84:87], v[136:139], v[72:75], v[84:87]\n"
    "ds_write_b128 v15, v[28:31] offset:0\n"
    "v_pk_mul_f16 v66, v48, v56 op_sel_hi:[0,1]\n"
    "v_pk_maximum3_f16 v21, v32, v32, s50 op_sel_hi:[1,1,0] neg_lo:[0,1,0] neg_hi:[0,1,0]\n"
    "v_pk_mad_u16 v21, v21, 8, s51 op_sel_hi:[1,0,0]\n"
    "v_smfmac_f32_16x16x64_f16 v[88:91], v[140:143], v[68:75], v2\n"
    "v_pk_mul_f16 v67, v48, v57 op_sel_hi:[0,1]\n"
    "v_bfi_b32 v32, s57, v21, v32\n"
    "v_mfma_f32_16x16x32_f16 v[76:79], v[144:147], v[60:63], v[76:79]\n"
    "v_pk_mul_f16 v68, v50, v56 op_sel_hi:[0,1]\n"
    "v_pk_mul_f16 v69, v50, v57 op_sel_hi:[0,1]\n"
    "v_pk_maximum3_f16 v12, v33, v33, s50 op_sel_hi:[1,1,0] neg_lo:[0,1,0] neg_hi:[0,1,0]\n"
    "v_mfma_f32_16x16x32_f16 v[76:79], v[156:159], v[64:67], v[76:79]\n"
    "v_pk_mul_f16 v70, v52, v56 op_sel_hi:[0,1]\n"
    "v_pk_mad_u16 v12, v12, 8, s51 op_sel_hi:[1,0,0]\n"
    "v_bfi_b32 v33, s57, v12, v33\n"
    "v_mfma_f32_16x16x32_f16 v[80:83], v[160:163], v[64:67], v[80:83]\n"
    "v_pk_mul_f16 v71, v52, v57 op_sel_hi:[0,1]\n"
    "v_pk_maximum3_f16 v21, v34, v34, s50 op_sel_hi:[1,1,0] neg_lo:[0,1,0] neg_hi:[0,1,0]\n"
    "v_mfma_f32_16x16x32_f16 v[80:83], v[148:151], v[60:63], v[80:83]\n"
    "ds_read_b32 v22, v25 offset:528\n"
    "v_pk_mul_f16 v72, v54, v56 op_sel_hi:[0,1]\n"
    "v_pk_mad_u16 v21, v21, 8, s51 op_sel_hi:[1,0,0]\n"
    "v_bfi_b32 v34, s57, v21, v34\n"
    "v_mfma_f32_16x16x32_f16 v[84:87], v[152:155], v[60:63], v[84:87]\n"
    "v_pk_mul_f16 v73, v54, v57 op_sel_hi:[0,1]\n"
    "v_pk_maximum3_f16 v12, v35, v35, s50 op_sel_hi:[1,1,0] neg_lo:[0,1,0] neg_hi:[0,1,0]\n"
    "v_pk_mad_u16 v12, v12, 8, s51 op_sel_hi:[1,0,0]\n"
    "v_mfma_f32_16x16x32_f16 v[84:87], v[164:167], v[64:67], v[84:87]\n"
    "v_pk_mul_f16 v74, v54, v54 op_sel_hi:[0,1]\n"
    "v_bfi_b32 v35, s57, v12, v35\n"
    "v_smfmac_f32_16x16x64_f16 v[88:91], v[168:171], v[60:67], v2\n"
    "ds_write_b128 v15, v[32:35] offset:144\n"
    "v_pk_mul_f16 v75, v54, v49 op_sel_hi:[0,1]\n"
    "v_pk_maximum3_f16 v21, v36, v36, s50 op_sel_hi:[1,1,0] neg_lo:[0,1,0] neg_hi:[0,1,0]\n"
    "v_pk_mad_u16 v21, v21, 8, s51 op_sel_hi:[1,0,0]\n"
    "v_mfma_f32_16x16x32_f16 v[76:79], v[172:175], v[68:71], v[76:79]\n"
    "v_pk_mul_f16 v60, v48, v54 op_sel_hi:[0,1]\n"
    "v_pk_mul_f16 v61, v48, v55 op_sel_hi:[0,1]\n"
    "v_mfma_f32_16x16x32_f16 v[76:79], v[184:187], v[72:75], v[76:79]\n"
    "s_waitcnt lgkmcnt(1)\n"
    "v_pk_mul_f16 v62, v50, v54 op_sel_hi:[0,1]\n"
    "v_readfirstlane_b32 s45, v22\n"
    "v_bfi_b32 v36, s57, v21, v36\n"
    "v_mfma_f32_16x16x32_f16 v[80:83], v[188:191], v[72:75], v[80:83]\n"
    "s_cmp_lt_i32 s45, 16\n"
    "v_pk_mul_f16 v63, v50, v55 op_sel_hi:[0,1]\n"
    "v_pk_maximum3_f16 v12, v37, v37, s50 op_sel_hi:[1,1,0] neg_lo:[0,1,0] neg_hi:[0,1,0]\n"
    "v_pk_mad_u16 v12, v12, 8, s51 op_sel_hi:[1,0,0]\n"
    "v_mfma_f32_16x16x32_f16 v[80:83], v[176:179], v[68:71], v[80:83]\n"
    "s_cbranch_scc1 .Lxd_ck31\n"
    ".Lxd_ck31r:\n"
    "v_pk_mul_f16 v64, v52, v54 op_sel_hi:[0,1]\n"
    "v_bfi_b32 v37, s57, v12, v37\n"
    "v_mfma_f32_16x16x32_f16 v[84:87], v[180:183], v[68:71], v[84:87]\n"
    "ds_read_b64_tr_b16 v[44:45], v5 offset:8192\n"
    "v_pk_mul_f16 v65, v52, v55 op_sel_hi:[0,1]\n"
    "v_pk_maximum3_f16 v21, v38, v38, s50 op_sel_hi:[1,1,0] neg_lo:[0,1,0] neg_hi:[0,1,0]\n"
    "v_pk_mad_u16 v21, v21, 8, s51 op_sel_hi:[1,0,0]\n"
    "v_mfma_f32_16x16x32_f16 v[84:87], v[192:195], v[72:75], v[84:87]\n"
    "ds_read_b64_tr_b16 v[46:47], v6 offset:8192\n"
    "v_pk_mul_f16 v66, v52, v52 op_sel_hi:[0,1]\n"
    "v_bfi_b32 v38, s57, v21, v38\n"
    "v_pk_maximum3_f16 v12, v39, v39, s50 op_sel_hi:[1,1,0] neg_lo:[0,1,0] neg_hi:[0,1,0]\n"
    "v_smfmac_f32_16x16x64_f16 v[88:91], v[196:199], v[68:75], v2\n"
    "v_pk_mul_f16 v67, v52, v49 op_sel_hi:[0,1]\n"
    "v_pk_mad_u16 v12, v12, 8, s51 op_sel_hi:[1,0,0]\n"
    "v_mfma_f32_16x16x32_f16 v[76:79], v[200:203], v[60:63], v[76:79]\n"
    "s_and_b32 s40, s45, 15\n"
    "v_pk_mul_f16 v68, v48, v52 op_sel_hi:[0,1]\n"
    "v_pk_mul_f16 v69, v48, v53 op_sel_hi:[0,1]\n"
    "v_bfi_b32 v39, s57, v12, v39\n"
    "v_mfma_f32_16x16x32_f16 v[76:79], v[212:215], v[64:67], v[76:79]\n"
    "s_cbranch_scc1 .Lxd_dr30\n"
    "s_mov_b64 exec, 1\n"
    "global_atomic_add v24, v17, v24, s[52:53] sc0 sc1\n"
    "s_mov_b64 exec, -1\n"
    ".Lxd_dr30r:\n"
    "v_pk_mul_f16 v70, v50, v52 op_sel_hi:[0,1]\n"
    "v_pk_maximum3_f16 v21, v40, v40, s50 op_sel_hi:[1,1,0] neg_lo:[0,1,0] neg_hi:[0,1,0]\n"
    "v_mfma_f32_16x16x32_f16 v[80:83], v[216:219], v[64:67], v[80:83]\n"
    "s_cbranch_vccnz .Lxd_pr29\n"
    "s_setprio 0\n"
    ".Lxd_pr29:\n"
    "v_pk_mul_f16 v71, v50, v53 op_sel_hi:[0,1]\n"
    "v_pk_mad_u16 v21, v21, 8, s51 op_sel_hi:[1,0,0]\n"
    "v_bfi_b32 v40, s57, v21, v40\n"
    "v_mfma_f32_16x16x32_f16 v[80:83], v[204:207], v[60:63], v[80:83]\n"
    "ds_write_b128 v15, v[36:39] offset:288\n"
    "v_pk_mul_f16 v72, v50, v50 op_sel_hi:[0,1]\n"
    "v_cvt_pkrtz_f16_f32 v12, s66, s66\n"
    "v_lshl_or_b32 v21, v12, 16, s47\n"
    "v_mfma_f32_16x16x32_f16 v[84:87], v[208:211], v[60:63], v[84:87]\n"
    "ds_write2_b32 v16, v40, v12 offset1:72\n"
    "v_pk_mul_f16 v73, v50, v49 op_sel_hi:[0,1]\n"
    "v_pk_mul_f16 v74, v48, v50 op_sel_hi:[0,1]\n"
    "v_mfma_f32_16x16x32_f16 v[84:87], v[220:223], v[64:67], v[84:87]\n"
    "ds_or_b32 v25, v21 offset:532\n"
    "v_pk_mul_f16 v75, v48, v51 op_sel_hi:[0,1]\n"
    "v_smfmac_f32_16x16x64_f16 v[88:91], v[224:227], v[60:67], v2\n"
    "ds_read_b64_tr_b16 v[48:49], v3 offset:8192\n"
    "ds_read_b64_tr_b16 v[58:59], v4 offset:11072\n"
    "v_add_u32 v25, 16, v25\n"
    "v_smfmac_f32_16x16x64_f16 v[88:91], v[252:255], v[68:75], v2\n"
    "s_waitcnt lgkmcnt(5)\n"
    "v_pk_mul_f16 v60, v44, s45 op_sel:[0,1]\n"
    "v_pk_mul_f16 v61, v45, s45 op_sel:[0,1]\n"
    "v_mfma_f32_16x16x32_f16 v[84:87], v[236:239], v[68:71], v[84:87]\n"
    "ds_read_b64_tr_b16 v[50:51], v4 offset:8768\n"
    "v_pk_mul_f16 v62, v46, s45 op_sel:[0,1]\n"
    "v_pk_mul_f16 v63, v47, s45 op_sel:[0,1]\n"
    "v_mfma_f32_16x16x32_f16 v[84:87], v[248:251], v[72:75], v[84:87]\n"
    "ds_read_b64_tr_b16 v[52:53], v4 offset:9344\n"
    "v_pk_mul_f16 v64, v44, v46\n"
    "v_pk_mul_f16 v65, v45, v47\n"
    "v_mfma_f32_16x16x32_f16 v[76:79], v[240:243], v[72:75], v[76:79]\n"
    "ds_read_b64_tr_b16 v[54:55], v4 offset:9920\n"
    "v_add_f32 v11, v88, v89\n"
    "v_add_f32 v13, v90, v91\n"
    "v_mfma_f32_16x16x32_f16 v[76:79], v[228:231], v[68:71], v[76:79]\n"
    "ds_read_b64_tr_b16 v[56:57], v4 offset:10496\n"
    "v_mov_b64 v[88:89], 0\n"
    "v_mov_b64 v[90:91], 0\n"
    "v_mfma_f32_16x16x32_f16 v[80:83], v[232:235], v[68:71], v[80:83]\n"
    "s_waitcnt lgkmcnt(4)\n"
    "v_pk_mul_f16 v66, v48, v48 op_sel_hi:[0,1]\n"
    "v_pk_mul_f16 v67, v48, v49 op_sel_hi:[0,1]\n"
    "v_mfma_f32_16x16x32_f16 v[80:83], v[244:247], v[72:75], v[80:83]\n"
    "s_cmp_eq_u32 s36, 0\n"
    "v_mul_f32 v11, v11, v13\n"
    "v_fmac_f32 v11, v84, v85\n"
    "v_fmac_f32 v11, v86, v87\n"
    "s_cbranch_scc1 .Lxd_ph0\n"
    ".Lxd_pl0:\n"
    "v_smfmac_f32_16x16x64_f16 v[88:91], v[112:115], v[60:67], v2\n"
    "s_cbranch_vccnz .Lxd_pr32\n"
    "s_setprio 2\n"
    ".Lxd_pr32:\n"
    "v_pk_mul_f16 v68, v58, v58 op_sel_hi:[0,1]\n"
    "v_pk_mul_f16 v69, v58, v49 op_sel_hi:[0,1]\n"
    "v_mfma_f32_16x16x32_f16 v[84:87], v[108:111], v[64:67], 0\n"
    "v_fmac_f32 v11, v76, v80\n"
    "v_fmac_f32 v11, v77, v81\n"
    "v_mfma_f32_16x16x32_f16 v[84:87], v[96:99], v[60:63], v[84:87]\n"
    "v_fmac_f32 v11, v78, v82\n"
    "v_fma_f32 v10, v79, v83, v11\n"
    "v_mfma_f32_16x16x32_f16 v[80:83], v[92:95], v[60:63], 0\n"
    "s_waitcnt lgkmcnt(0)\n"
    "v_pk_mul_f16 v70, v48, v58 op_sel_hi:[0,1]\n"
    "v_pk_mul_f16 v71, v48, v59 op_sel_hi:[0,1]\n"
    "v_mfma_f32_16x16x32_f16 v[80:83], v[104:107], v[64:67], v[80:83]\n"
    "v_pk_mul_f16 v72, v50, v58 op_sel_hi:[0,1]\n"
    "v_pk_mul_f16 v73, v50, v59 op_sel_hi:[0,1]\n"
    "v_mfma_f32_16x16x32_f16 v[76:79], v[100:103], v[64:67], 0\n"
    "v_pk_mul_f16 v74, v52, v58 op_sel_hi:[0,1]\n"
    "v_pk_mul_f16 v75, v52, v59 op_sel_hi:[0,1]\n"
    "v_mfma_f32_16x16x32_f16 v[76:79], v[116:119], v[68:71], v[76:79]\n"
    "v_pk_mul_f16 v60, v54, v58 op_sel_hi:[0,1]\n"
    "v_pk_mul_f16 v61, v54, v59 op_sel_hi:[0,1]\n"
    "v_mfma_f32_16x16x32_f16 v[76:79], v[128:131], v[72:75], v[76:79]\n"
    "v_pk_mul_f16 v62, v56, v58 op_sel_hi:[0,1]\n"
    "v_permlane32_swap_b32 v7, v8\n"
    "v_mfma_f32_16x16x32_f16 v[80:83], v[132:135], v[72:75], v[80:83]\n"
    "v_pk_mul_f16 v63, v56, v59 op_sel_hi:[0,1]\n"
    "v_permlane32_swap_b32 v9, v10\n"
    "v_mfma_f32_16x16x32_f16 v[80:83], v[120:123], v[68:71], v[80:83]\n"
    "v_pk_mul_f16 v64, v56, v56 op_sel_hi:[0,1]\n"
    "v_mul_f32 v7, v0, v7\n"
    "v_mfma_f32_16x16x32_f16 v[84:87], v[124:127], v[68:71], v[84:87]\n"
    "v_pk_mul_f16 v65, v56, v49 op_sel_hi:[0,1]\n"
    "v_mul_f32 v9, v0, v9\n"
    "v_mfma_f32_16x16x32_f16 v[84:87], v[136:139], v[72:75], v[84:87]\n"
    "v_pk_mul_f16 v66, v48, v56 op_sel_hi:[0,1]\n"
    "v_fmac_f32 v7, v23, v8\n"
    "v_smfmac_f32_16x16x64_f16 v[88:91], v[140:143], v[68:75], v2\n"
    "v_pk_mul_f16 v67, v48, v57 op_sel_hi:[0,1]\n"
    "v_fmac_f32 v9, v23, v10\n"
    "v_mfma_f32_16x16x32_f16 v[76:79], v[144:147], v[60:63], v[76:79]\n"
    "v_pk_mul_f16 v68, v50, v56 op_sel_hi:[0,1]\n"
    "v_pk_mul_f16 v69, v50, v57 op_sel_hi:[0,1]\n"
    "v_mfma_f32_16x16x32_f16 v[76:79], v[156:159], v[64:67], v[76:79]\n"
    "v_pk_mul_f16 v70, v52, v56 op_sel_hi:[0,1]\n"
    "v_permlane16_swap_b32 v7, v9\n"
    "v_mfma_f32_16x16x32_f16 v[80:83], v[160:163], v[64:67], v[80:83]\n"
    "v_pk_mul_f16 v71, v52, v57 op_sel_hi:[0,1]\n"
    "v_add_f32 v7, v7, v9\n"
    "v_mfma_f32_16x16x32_f16 v[80:83], v[148:151], v[60:63], v[80:83]\n"
    "ds_read_b32 v22, v25 offset:516\n"
    "v_pk_mul_f16 v72, v54, v56 op_sel_hi:[0,1]\n"
    "v_add_f32_dpp v7, v7, v7 quad_perm:[1,0,3,2] row_mask:0xf bank_mask:0xf\n"
    "v_mfma_f32_16x16x32_f16 v[84:87], v[152:155], v[60:63], v[84:87]\n"
    "v_pk_mul_f16 v73, v54, v57 op_sel_hi:[0,1]\n"
    "v_add_f32_dpp v7, v7, v7 quad_perm:[2,3,0,1] row_mask:0xf bank_mask:0xf\n"
    "v_mfma_f32_16x16x32_f16 v[84:87], v[164:167], v[64:67], v[84:87]\n"
    "v_pk_mul_f16 v74, v54, v54 op_sel_hi:[0,1]\n"
    "v_add_f32_dpp v7, v7, v7 row_half_mirror row_mask:0xf bank_mask:0xf\n"
    "v_smfmac_f32_16x16x64_f16 v[88:91], v[168:171], v[60:67], v2\n"
    "v_pk_mul_f16 v75, v54, v49 op_sel_hi:[0,1]\n"
    "v_add_f32_dpp v7, v7, v7 row_mirror row_mask:0xf bank_mask:0xf\n"
    "v_mfma_f32_16x16x32_f16 v[76:79], v[172:175], v[68:71], v[76:79]\n"
    "ds_write_b32 v14, v7\n"
    "v_pk_mul_f16 v60, v48, v54 op_sel_hi:[0,1]\n"
    "v_pk_mul_f16 v61, v48, v55 op_sel_hi:[0,1]\n"
    "v_mfma_f32_16x16x32_f16 v[76:79], v[184:187], v[72:75], v[76:79]\n"
    "s_waitcnt lgkmcnt(1)\n"
    "v_pk_mul_f16 v62, v50, v54 op_sel_hi:[0,1]\n"
    "v_readfirstlane_b32 s45, v22\n"
    "v_mfma_f32_16x16x32_f16 v[80:83], v[188:191], v[72:75], v[80:83]\n"
    "s_cmp_lt_i32 s45, 16\n"
    "v_pk_mul_f16 v63, v50, v55 op_sel_hi:[0,1]\n"
    "v_add_u32 v14, 64, v14\n"
    "v_mfma_f32_16x16x32_f16 v[80:83], v[176:179], v[68:71], v[80:83]\n"
    "s_cbranch_scc1 .Lxd_ck34\n"
    ".Lxd_ck34r:\n"
    "v_pk_mul_f16 v64, v52, v54 op_sel_hi:[0,1]\n"
    "v_mfma_f32_16x16x32_f16 v[84:87], v[180:183], v[68:71], v[84:87]\n"
    "ds_read_b64_tr_b16 v[44:45], v5 offset:12288\n"
    "v_pk_mul_f16 v65, v52, v55 op_sel_hi:[0,1]\n"
    "v_mfma_f32_16x16x32_f16 v[84:87], v[192:195], v[72:75], v[84:87]\n"
    "ds_read_b64_tr_b16 v[46:47], v6 offset:12288\n"
    "v_pk_mul_f16 v66, v52, v52 op_sel_hi:[0,1]\n"
    "v_smfmac_f32_16x16x64_f16 v[88:91], v[196:199], v[68:75], v2\n"
    "v_pk_mul_f16 v67, v52, v49 op_sel_hi:[0,1]\n"
    "v_mfma_f32_16x16x32_f16 v[76:79], v[200:203], v[60:63], v[76:79]\n"
    "v_pk_mul_f16 v68, v48, v52 op_sel_hi:[0,1]\n"
    "v_pk_mul_f16 v69, v48, v53 op_sel_hi:[0,1]\n"
    "v_mfma_f32_16x16x32_f16 v[76:79], v[212:215], v[64:67], v[76:79]\n"
    "v_pk_mul_f16 v70, v50, v52 op_sel_hi:[0,1]\n"
    "v_mfma_f32_16x16x32_f16 v[80:83], v[216:219], v[64:67], v[80:83]\n"
    "s_cbranch_vccnz .Lxd_pr33\n"
    "s_setprio 0\n"
    ".Lxd_pr33:\n"
    "v_pk_mul_f16 v71, v50, v53 op_sel_hi:[0,1]\n"
    "v_mfma_f32_16x16x32_f16 v[80:83], v[204:207], v[60:63], v[80:83]\n"
    "v_pk_mul_f16 v72, v50, v50 op_sel_hi:[0,1]\n"
    "v_mfma_f32_16x16x32_f16 v[84:87], v[208:211], v[60:63], v[84:87]\n"
    "v_pk_mul_f16 v73, v50, v49 op_sel_hi:[0,1]\n"
    "v_pk_mul_f16 v74, v48, v50 op_sel_hi:[0,1]\n"
    "v_mfma_f32_16x16x32_f16 v[84:87], v[220:223], v[64:67], v[84:87]\n"
    "v_pk_mul_f16 v75, v48, v51 op_sel_hi:[0,1]\n"
    "v_smfmac_f32_16x16x64_f16 v[88:91], v[224:227], v[60:67], v2\n"
    "ds_read_b64_tr_b16 v[48:49], v3 offset:12288\n"
    "ds_read_b64_tr_b16 v[58:59], v4 offset:15168\n"
    "v_smfmac_f32_16x16x64_f16 v[88:91], v[252:255], v[68:75], v2\n"
    "s_waitcnt lgkmcnt(2)\n"
    "v_pk_mul_f16 v60, v44, s45 op_sel:[0,1]\n"
    "v_pk_mul_f16 v61, v45, s45 op_sel:[0,1]\n"
    "v_mfma_f32_16x16x32_f16 v[84:87], v[236:239], v[68:71], v[84:87]\n"
    "ds_read_b64_tr_b16 v[50:51], v4 offset:12864\n"
    "v_pk_mul_f16 v62, v46, s45 op_sel:[0,1]\n"
    "v_pk_mul_f16 v63, v47, s45 op_sel:[0,1]\n"
    "v_mfma_f32_16x16x32_f16 v[84:87], v[248:251], v[72:75], v[84:87]\n"
    "ds_read_b64_tr_b16 v[52:53], v4 offset:13440\n"
    "v_pk_mul_f16 v64, v44, v46\n"
    "v_pk_mul_f16 v65, v45, v47\n"
    "v_mfma_f32_16x16x32_f16 v[76:79], v[240:243], v[72:75], v[76:79]\n"
    "ds_read_b64_tr_b16 v[54:55], v4 offset:14016\n"
    "v_add_f32 v11, v88, v89\n"
    "v_add_f32 v13, v90, v91\n"
    "v_mfma_f32_16x16x32_f16 v[76:79], v[228:231], v[68:71], v[76:79]\n"
    "ds_read_b64_tr_b16 v[56:57], v4 offset:14592\n"
    "v_mov_b64 v[88:89], 0\n"
    "v_mov_b64 v[90:91], 0\n"
    "v_mfma_f32_16x16x32_f16 v[80:83], v[232:235], v[68:71], v[80:83]\n"
    "s_waitcnt lgkmcnt(4)\n"
    "v_pk_mul_f16 v66, v48, v48 op_sel_hi:[0,1]\n"
    "v_pk_mul_f16 v67, v48, v49 op_sel_hi:[0,1]\n"
    "v_mfma_f32_16x16x32_f16 v[80:83], v[244:247], v[72:75], v[80:83]\n"
    "v_mul_f32 v11, v11, v13\n"
    "v_fmac_f32 v11, v84, v85\n"
    "v_fmac_f32 v11, v86, v87\n"
    ".Lxd_pl1:\n"
    "v_smfmac_f32_16x16x64_f16 v[88:91], v[112:115], v[60:67], v2\n"
    "s_cbranch_vccnz .Lxd_pr35\n"
    "s_setprio 2\n"
    ".Lxd_pr35:\n"
    "v_pk_mul_f16 v68, v58, v58 op_sel_hi:[0,1]\n"
    "v_pk_mul_f16 v69, v58, v49 op_sel_hi:[0,1]\n"
    "v_mfma_f32_16x16x32_f16 v[84:87], v[108:111], v[64:67], 0\n"
    "v_fmac_f32 v11, v76, v80\n"
    "v_fmac_f32 v11, v77, v81\n"
    "v_mfma_f32_16x16x32_f16 v[84:87], v[96:99], v[60:63], v[84:87]\n"
    "v_fmac_f32 v11, v78, v82\n"
    "v_fma_f32 v7, v79, v83, v11\n"
    "v_mfma_f32_16x16x32_f16 v[80:83], v[92:95], v[60:63], 0\n"
    "s_waitcnt lgkmcnt(0)\n"
    "v_pk_mul_f16 v70, v48, v58 op_sel_hi:[0,1]\n"
    "v_pk_mul_f16 v71, v48, v59 op_sel_hi:[0,1]\n"
    "v_mfma_f32_16x16x32_f16 v[80:83], v[104:107], v[64:67], v[80:83]\n"
    "v_pk_mul_f16 v72, v50, v58 op_sel_hi:[0,1]\n"
    "v_pk_mul_f16 v73, v50, v59 op_sel_hi:[0,1]\n"
    "v_mfma_f32_16x16x32_f16 v[76:79], v[100:103], v[64:67], 0\n"
    "s_cmp_lg_u32 s46, 0\n"
    "v_pk_mul_f16 v74, v52, v58 op_sel_hi:[0,1]\n"
    "v_pk_mul_f16 v75, v52, v59 op_sel_hi:[0,1]\n"
    "v_mfma_f32_16x16x32_f16 v[76:79], v[116:119], v[68:71], v[76:79]\n"
    "s_cbranch_scc1 .Lxd_ht37\n"
    ".Lxd_ht37r:\n"
    "v_pk_mul_f16 v60, v54, v58 op_sel_hi:[0,1]\n"
    "v_pk_mul_f16 v61, v54, v59 op_sel_hi:[0,1]\n"
    "v_mfma_f32_16x16x32_f16 v[76:79], v[128:131], v[72:75], v[76:79]\n"
    "v_pk_mul_f16 v62, v56, v58 op_sel_hi:[0,1]\n"
    "v_xor_b32 v3, 0x4000, v3\n"
    "v_mfma_f32_16x16x32_f16 v[80:83], v[132:135], v[72:75], v[80:83]\n"
    "v_pk_mul_f16 v63, v56, v59 op_sel_hi:[0,1]\n"
    "v_xor_b32 v4, 0x4000, v4\n"
    "v_mfma_f32_16x16x32_f16 v[80:83], v[120:123], v[68:71], v[80:83]\n"
    "v_pk_mul_f16 v64, v56, v56 op_sel_hi:[0,1]\n"
    "v_xor_b32 v5, 0x4000, v5\n"
    "v_mfma_f32_16x16x32_f16 v[84:87], v[124:127], v[68:71], v[84:87]\n"
    "v_pk_mul_f16 v65, v56, v49 op_sel_hi:[0,1]\n"
    "v_xor_b32 v6, 0x4000, v6\n"
    "v_mfma_f32_16x16x32_f16 v[84:87], v[136:139], v[72:75], v[84:87]\n"
    "v_pk_mul_f16 v66, v48, v56 op_sel_hi:[0,1]\n"
    "v_smfmac_f32_16x16x64_f16 v[88:91], v[140:143], v[68:75], v2\n"
    "v_pk_mul_f16 v67, v48, v57 op_sel_hi:[0,1]\n"
    "v_mfma_f32_16x16x32_f16 v[76:79], v[144:147], v[60:63], v[76:79]\n"
    "v_pk_mul_f16 v68, v50, v56 op_sel_hi:[0,1]\n"
    "v_pk_mul_f16 v69, v50, v57 op_sel_hi:[0,1]\n"
    "v_mfma_f32_16x16x32_f16 v[76:79], v[156:159], v[64:67], v[76:79]\n"
    "v_pk_mul_f16 v70, v52, v56 op_sel_hi:[0,1]\n"
    "v_mfma_f32_16x16x32_f16 v[80:83], v[160:163], v[64:67], v[80:83]\n"
    "v_pk_mul_f16 v71, v52, v57 op_sel_hi:[0,1]\n"
    "v_mfma_f32_16x16x32_f16 v[80:83], v[148:151], v[60:63], v[80:83]\n"
    "ds_read_b32 v22, v25 offset:520\n"
    "v_pk_mul_f16 v72, v54, v56 op_sel_hi:[0,1]\n"
    "v_mfma_f32_16x16x32_f16 v[84:87], v[152:155], v[60:63], v[84:87]\n"
    "v_pk_mul_f16 v73, v54, v57 op_sel_hi:[0,1]\n"
    "v_mfma_f32_16x16x32_f16 v[84:87], v[164:167], v[64:67], v[84:87]\n"
    "v_pk_mul_f16 v74, v54, v54 op_sel_hi:[0,1]\n"
    "v_smfmac_f32_16x16x64_f16 v[88:91], v[168:171], v[60:67], v2\n"
    "v_pk_mul_f16 v75, v54, v49 op_sel_hi:[0,1]\n"
    "v_mfma_f32_16x16x32_f16 v[76:79], v[172:175], v[68:71], v[76:79]\n"
    "v_pk_mul_f16 v60, v48, v54 op_sel_hi:[0,1]\n"
    "v_pk_mul_f16 v61, v48, v55 op_sel_hi:[0,1]\n"
    "v_mfma_f32_16x16x32_f16 v[76:79], v[184:187], v[72:75], v[76:79]\n"
    "s_waitcnt lgkmcnt(0)\n"
    "v_pk_mul_f16 v62, v50, v54 op_sel_hi:[0,1]\n"
    "v_readfirstlane_b32 s45, v22\n"
    "v_mfma_f32_16x16x32_f16 v[80:83], v[188:191], v[72:75], v[80:83]\n"
    "s_cmp_lt_i32 s45, 16\n"
    "v_pk_mul_f16 v63, v50, v55 op_sel_hi:[0,1]\n"
    "v_mfma_f32_16x16x32_f16 v[80:83], v[176:179], v[68:71], v[80:83]\n"
    "s_cbranch_scc1 .Lxd_ck38\n"
    ".Lxd_ck38r:\n"
    "v_pk_mul_f16 v64, v52, v54 op_sel_hi:[0,1]\n"
    "v_mfma_f32_16x16x32_f16 v[84:87], v[180:183], v[68:71], v[84:87]\n"
    "ds_read_b64_tr_b16 v[44:45], v5 offset:0\n"
    "v_pk_mul_f16 v65, v52, v55 op_sel_hi:[0,1]\n"
    "v_mfma_f32_16x16x32_f16 v[84:87], v[192:195], v[72:75], v[84:87]\n"
    "ds_read_b64_tr_b16 v[46:47], v6 offset:0\n"
    "v_pk_mul_f16 v66, v52, v52 op_sel_hi:[0,1]\n"
    "v_smfmac_f32_16x16x64_f16 v[88:91], v[196:199], v[68:75], v2\n"
    "v_pk_mul_f16 v67, v52, v49 op_sel_hi:[0,1]\n"
    "v_mfma_f32_16x16x32_f16 v[76:79], v[200:203], v[60:63], v[76:79]\n"
    "v_pk_mul_f16 v68, v48, v52 op_sel_hi:[0,1]\n"
    "v_pk_mul_f16 v69, v48, v53 op_sel_hi:[0,1]\n"
    "v_mfma_f32_16x16x32_f16 v[76:79], v[212:215], v[64:67], v[76:79]\n"
    "v_pk_mul_f16 v70, v50, v52 op_sel_hi:[0,1]\n"
    "v_mfma_f32_16x16x32_f16 v[80:83], v[216:219], v[64:67], v[80:83]\n"
    "s_cbranch_vccnz .Lxd_pr36\n"
    "s_setprio 0\n"
    ".Lxd_pr36:\n"
    "v_pk_mul_f16 v71, v50, v53 op_sel_hi:[0,1]\n"
    "v_mfma_f32_16x16x32_f16 v[80:83], v[204:207], v[60:63], v[80:83]\n"
    "v_pk_mul_f16 v72, v50, v50 op_sel_hi:[0,1]\n"
    "v_mfma_f32_16x16x32_f16 v[84:87], v[208:211], v[60:63], v[84:87]\n"
    "v_pk_mul_f16 v73, v50, v49 op_sel_hi:[0,1]\n"
    "v_pk_mul_f16 v74, v48, v50 op_sel_hi:[0,1]\n"
    "v_mfma_f32_16x16x32_f16 v[84:87], v[220:223], v[64:67], v[84:87]\n"
    "v_pk_mul_f16 v75, v48, v51 op_sel_hi:[0,1]\n"
    "v_smfmac_f32_16x16x64_f16 v[88:91], v[224:227], v[60:67], v2\n"
    "ds_read_b64_tr_b16 v[48:49], v3 offset:0\n"
    "ds_read_b64_tr_b16 v[58:59], v4 offset:2880\n"
    "v_smfmac_f32_16x16x64_f16 v[88:91], v[252:255], v[68:75], v2\n"
    "s_waitcnt lgkmcnt(2)\n"
    "v_pk_mul_f16 v60, v44, s45 op_sel:[0,1]\n"
    "v_pk_mul_f16 v61, v45, s45 op_sel:[0,1]\n"
    "v_mfma_f32_16x16x32_f16 v[84:87], v[236:239], v[68:71], v[84:87]\n"
    "ds_read_b64_tr_b16 v[50:51], v4 offset:576\n"
    "v_pk_mul_f16 v62, v46, s45 op_sel:[0,1]\n"
    "v_pk_mul_f16 v63, v47, s45 op_sel:[0,1]\n"
    "v_mfma_f32_16x16x32_f16 v[84:87], v[248:251], v[72:75], v[84:87]\n"
    "ds_read_b64_tr_b16 v[52:53], v4 offset:1152\n"
    "v_pk_mul_f16 v64, v44, v46\n"
    "v_pk_mul_f16 v65, v45, v47\n"
    "v_mfma_f32_16x16x32_f16 v[76:79], v[240:243], v[72:75], v[76:79]\n"
    "ds_read_b64_tr_b16 v[54:55], v4 offset:1728\n"
    "v_add_f32 v11, v88, v89\n"
    "v_add_f32 v13, v90, v91\n"
    "v_mfma_f32_16x16x32_f16 v[76:79], v[228:231], v[68:71], v[76:79]\n"
    "ds_read_b64_tr_b16 v[56:57], v4 offset:2304\n"
    "v_mov_b64 v[88:89], 0\n"
    "v_mov_b64 v[90:91], 0\n"
    "v_mfma_f32_16x16x32_f16 v[80:83], v[232:235], v[68:71], v[80:83]\n"
    "s_waitcnt lgkmcnt(4)\n"
    "v_pk_mul_f16 v66, v48, v48 op_sel_hi:[0,1]\n"
    "v_pk_mul_f16 v67, v48, v49 op_sel_hi:[0,1]\n"
    "v_mfma_f32_16x16x32_f16 v[80:83], v[244:247], v[72:75], v[80:83]\n"
    "v_mul_f32 v11, v11, v13\n"
    "v_fmac_f32 v11, v84, v85\n"
    "v_fmac_f32 v11, v86, v87\n"
    ".Lxd_pl2:\n"
    "v_smfmac_f32_16x16x64_f16 v[88:91], v[112:115], v[60:67], v2\n"
    "s_cbranch_vccnz .Lxd_pr39\n"
    "s_setprio 2\n"
    ".Lxd_pr39:\n"
    "v_pk_mul_f16 v68, v58, v58 op_sel_hi:[0,1]\n"
    "v_pk_mul_f16 v69, v58, v49 op_sel_hi:[0,1]\n"
    "v_mfma_f32_16x16x32_f16 v[84:87], v[108:111], v[64:67], 0\n"
    "v_fmac_f32 v11, v76, v80\n"
    "v_fmac_f32 v11, v77, v81\n"
    "v_mfma_f32_16x16x32_f16 v[84:87], v[96:99], v[60:63], v[84:87]\n"
    "v_fmac_f32 v11, v78, v82\n"
    "v_fma_f32 v8, v79, v83, v11\n"
    "v_mfma_f32_16x16x32_f16 v[80:83], v[92:95], v[60:63], 0\n"
    "s_waitcnt lgkmcnt(0)\n"
    "v_pk_mul_f16 v70, v48, v58 op_sel_hi:[0,1]\n"
    "v_pk_mul_f16 v71, v48, v59 op_sel_hi:[0,1]\n"
    "v_mfma_f32_16x16x32_f16 v[80:83], v[104:107], v[64:67], v[80:83]\n"
    "v_pk_mul_f16 v72, v50, v58 op_sel_hi:[0,1]\n"
    "v_pk_mul_f16 v73, v50, v59 op_sel_hi:[0,1]\n"
    "v_mfma_f32_16x16x32_f16 v[76:79], v[100:103], v[64:67], 0\n"
    "s_cmp_lg_u32 s46, 0\n"
    "v_pk_mul_f16 v74, v52, v58 op_sel_hi:[0,1]\n"
    "v_pk_mul_f16 v75, v52, v59 op_sel_hi:[0,1]\n"
    "v_mfma_f32_16x16x32_f16 v[76:79], v[116:119], v[68:71], v[76:79]\n"
    "s_cbranch_scc1 .Lxd_ht41\n"
    ".Lxd_ht41r:\n"
    "v_pk_mul_f16 v60, v54, v58 op_sel_hi:[0,1]\n"
    "v_pk_mul_f16 v61, v54, v59 op_sel_hi:[0,1]\n"
    "v_mfma_f32_16x16x32_f16 v[76:79], v[128:131], v[72:75], v[76:79]\n"
    "v_pk_mul_f16 v62, v56, v58 op_sel_hi:[0,1]\n"
    "v_mfma_f32_16x16x32_f16 v[80:83], v[132:135], v[72:75], v[80:83]\n"
    "v_pk_mul_f16 v63, v56, v59 op_sel_hi:[0,1]\n"
    "v_mfma_f32_16x16x32_f16 v[80:83], v[120:123], v[68:71], v[80:83]\n"
    "v_pk_mul_f16 v64, v56, v56 op_sel_hi:[0,1]\n"
    "v_mfma_f32_16x16x32_f16 v[84:87], v[124:127], v[68:71], v[84:87]\n"
    "v_pk_mul_f16 v65, v56, v49 op_sel_hi:[0,1]\n"
    "v_mfma_f32_16x16x32_f16 v[84:87], v[136:139], v[72:75], v[84:87]\n"
    "v_pk_mul_f16 v66, v48, v56 op_sel_hi:[0,1]\n"
    "v_smfmac_f32_16x16x64_f16 v[88:91], v[140:143], v[68:75], v2\n"
    "v_pk_mul_f16 v67, v48, v57 op_sel_hi:[0,1]\n"
    "v_mfma_f32_16x16x32_f16 v[76:79], v[144:147], v[60:63], v[76:79]\n"
    "v_pk_mul_f16 v68, v50, v56 op_sel_hi:[0,1]\n"
    "v_pk_mul_f16 v69, v50, v57 op_sel_hi:[0,1]\n"
    "v_mfma_f32_16x16x32_f16 v[76:79], v[156:159], v[64:67], v[76:79]\n"
    "v_pk_mul_f16 v70, v52, v56 op_sel_hi:[0,1]\n"
    "v_mfma_f32_16x16x32_f16 v[80:83], v[160:163], v[64:67], v[80:83]\n"
    "v_pk_mul_f16 v71, v52, v57 op_sel_hi:[0,1]\n"
    "v_mfma_f32_16x16x32_f16 v[80:83], v[148:151], v[60:63], v[80:83]\n"
    "ds_read_b32 v22, v25 offset:524\n"
    "v_pk_mul_f16 v72, v54, v56 op_sel_hi:[0,1]\n"
    "v_mfma_f32_16x16x32_f16 v[84:87], v[152:155], v[60:63], v[84:87]\n"
    "v_pk_mul_f16 v73, v54, v57 op_sel_hi:[0,1]\n"
    "v_mfma_f32_16x16x32_f16 v[84:87], v[164:167], v[64:67], v[84:87]\n"
    "v_pk_mul_f16 v74, v54, v54 op_sel_hi:[0,1]\n"
    "v_smfmac_f32_16x16x64_f16 v[88:91], v[168:171], v[60:67], v2\n"
    "v_pk_mul_f16 v75, v54, v49 op_sel_hi:[0,1]\n"
    "v_mfma_f32_16x16x32_f16 v[76:79], v[172:175], v[68:71], v[76:79]\n"
    "v_pk_mul_f16 v60, v48, v54 op_sel_hi:[0,1]\n"
    "v_pk_mul_f16 v61, v48, v55 op_sel_hi:[0,1]\n"
    "v_mfma_f32_16x16x32_f16 v[76:79], v[184:187], v[72:75], v[76:79]\n"
    "s_waitcnt lgkmcnt(0)\n"
    "v_pk_mul_f16 v62, v50, v54 op_sel_hi:[0,1]\n"
    "v_readfirstlane_b32 s45, v22\n"
    "v_mfma_f32_16x16x32_f16 v[80:83], v[188:191], v[72:75], v[80:83]\n"
    "s_cmp_lt_i32 s45, 16\n"
    "v_pk_mul_f16 v63, v50, v55 op_sel_hi:[0,1]\n"
    "v_mfma_f32_16x16x32_f16 v[80:83], v[176:179], v[68:71], v[80:83]\n"
    "s_cbranch_scc1 .Lxd_ck42\n"
    ".Lxd_ck42r:\n"
    "v_pk_mul_f16 v64, v52, v54 op_sel_hi:[0,1]\n"
    "v_mfma_f32_16x16x32_f16 v[84:87], v[180:183], v[68:71], v[84:87]\n"
    "ds_read_b64_tr_b16 v[44:45], v5 offset:4096\n"
    "v_pk_mul_f16 v65, v52, v55 op_sel_hi:[0,1]\n"
    "v_mfma_f32_16x16x32_f16 v[84:87], v[192:195], v[72:75], v[84:87]\n"
    "ds_read_b64_tr_b16 v[46:47], v6 offset:4096\n"
    "v_pk_mul_f16 v66, v52, v52 op_sel_hi:[0,1]\n"
    "v_smfmac_f32_16x16x64_f16 v[88:91], v[196:199], v[68:75], v2\n"
    "v_pk_mul_f16 v67, v52, v49 op_sel_hi:[0,1]\n"
    "v_mfma_f32_16x16x32_f16 v[76:79], v[200:203], v[60:63], v[76:79]\n"
    "v_pk_mul_f16 v68, v48, v52 op_sel_hi:[0,1]\n"
    "v_pk_mul_f16 v69, v48, v53 op_sel_hi:[0,1]\n"
    "v_mfma_f32_16x16x32_f16 v[76:79], v[212:215], v[64:67], v[76:79]\n"
    "v_pk_mul_f16 v70, v50, v52 op_sel_hi:[0,1]\n"
    "v_mfma_f32_16x16x32_f16 v[80:83], v[216:219], v[64:67], v[80:83]\n"
    "s_cbranch_vccnz .Lxd_pr40\n"
    "s_setprio 0\n"
    ".Lxd_pr40:\n"
    "v_pk_mul_f16 v71, v50, v53 op_sel_hi:[0,1]\n"
    "v_mfma_f32_16x16x32_f16 v[80:83], v[204:207], v[60:63], v[80:83]\n"
    "v_pk_mul_f16 v72, v50, v50 op_sel_hi:[0,1]\n"
    "v_mfma_f32_16x16x32_f16 v[84:87], v[208:211], v[60:63], v[84:87]\n"
    "v_pk_mul_f16 v73, v50, v49 op_sel_hi:[0,1]\n"
    "v_pk_mul_f16 v74, v48, v50 op_sel_hi:[0,1]\n"
    "v_mfma_f32_16x16x32_f16 v[84:87], v[220:223], v[64:67], v[84:87]\n"
    "v_pk_mul_f16 v75, v48, v51 op_sel_hi:[0,1]\n"
    "v_smfmac_f32_16x16x64_f16 v[88:91], v[224:227], v[60:67], v2\n"
    "ds_read_b64_tr_b16 v[48:49], v3 offset:4096\n"
    "ds_read_b64_tr_b16 v[58:59], v4 offset:6976\n"
    "v_smfmac_f32_16x16x64_f16 v[88:91], v[252:255], v[68:75], v2\n"
    "s_waitcnt lgkmcnt(2)\n"
    "v_pk_mul_f16 v60, v44, s45 op_sel:[0,1]\n"
    "v_pk_mul_f16 v61, v45, s45 op_sel:[0,1]\n"
    "v_mfma_f32_16x16x32_f16 v[84:87], v[236:239], v[68:71], v[84:87]\n"
    "ds_read_b64_tr_b16 v[50:51], v4 offset:4672\n"
    "v_pk_mul_f16 v62, v46, s45 op_sel:[0,1]\n"
    "v_pk_mul_f16 v63, v47, s45 op_sel:[0,1]\n"
    "v_mfma_f32_16x16x32_f16 v[84:87], v[248:251], v[72:75], v[84:87]\n"
    "ds_read_b64_tr_b16 v[52:53], v4 offset:5248\n"
    "v_pk_mul_f16 v64, v44, v46\n"
    "v_pk_mul_f16 v65, v45, v47\n"
    "v_mfma_f32_16x16x32_f16 v[76:79], v[240:243], v[72:75], v[76:79]\n"
    "ds_read_b64_tr_b16 v[54:55], v4 offset:5824\n"
    "v_add_f32 v11, v88, v89\n"
    "v_add_f32 v13, v90, v91\n"
    "v_mfma_f32_16x16x32_f16 v[76:79], v[228:231], v[68:71], v[76:79]\n"
    "ds_read_b64_tr_b16 v[56:57], v4 offset:6400\n"
    "v_mov_b64 v[88:89], 0\n"
    "v_mov_b64 v[90:91], 0\n"
    "v_mfma_f32_16x16x32_f16 v[80:83], v[232:235], v[68:71], v[80:83]\n"
    "s_waitcnt lgkmcnt(4)\n"
    "v_pk_mul_f16 v66, v48, v48 op_sel_hi:[0,1]\n"
    "v_pk_mul_f16 v67, v48, v49 op_sel_hi:[0,1]\n"
    "v_mfma_f32_16x16x32_f16 v[80:83], v[244:247], v[72:75], v[80:83]\n"
    "v_mul_f32 v11, v11, v13\n"
    "v_fmac_f32 v11, v84, v85\n"
    "v_fmac_f32 v11, v86, v87\n"
    ".Lxd_pl3:\n"
    "v_smfmac_f32_16x16x64_f16 v[88:91], v[112:115], v[60:67], v2\n"
    "s_cbranch_vccnz .Lxd_pr43\n"
    "s_setprio 2\n"
    ".Lxd_pr43:\n"
    "v_pk_mul_f16 v68, v58, v58 op_sel_hi:[0,1]\n"
    "v_pk_mul_f16 v69, v58, v49 op_sel_hi:[0,1]\n"
    "v_mfma_f32_16x16x32_f16 v[84:87], v[108:111], v[64:67], 0\n"
    "v_fmac_f32 v11, v76, v80\n"
    "v_fmac_f32 v11, v77, v81\n"
    "v_mfma_f32_16x16x32_f16 v[84:87], v[96:99], v[60:63], v[84:87]\n"
    "v_fmac_f32 v11, v78, v82\n"
    "v_fma_f32 v9, v79, v83, v11\n"
    "v_mfma_f32_16x16x32_f16 v[80:83], v[92:95], v[60:63], 0\n"
    "s_waitcnt lgkmcnt(0)\n"
    "v_pk_mul_f16 v70, v48, v58 op_sel_hi:[0,1]\n"
    "v_pk_mul_f16 v71, v48, v59 op_sel_hi:[0,1]\n"
    "v_mfma_f32_16x16x32_f16 v[80:83], v[104:107], v[64:67], v[80:83]\n"
    "v_pk_mul_f16 v72, v50, v58 op_sel_hi:[0,1]\n"
    "v_pk_mul_f16 v73, v50, v59 op_sel_hi:[0,1]\n"
    "v_mfma_f32_16x16x32_f16 v[76:79], v[100:103], v[64:67], 0\n"
    "s_cmp_lt_i32 s46, 0\n"
    "v_pk_mul_f16 v74, v52, v58 op_sel_hi:[0,1]\n"
    "v_pk_mul_f16 v75, v52, v59 op_sel_hi:[0,1]\n"
    "v_mfma_f32_16x16x32_f16 v[76:79], v[116:119], v[68:71], v[76:79]\n"
    "s_cbranch_scc1 .Lxd_ht45\n"
    ".Lxd_ht45r:\n"
    "v_pk_mul_f16 v60, v54, v58 op_sel_hi:[0,1]\n"
    "v_pk_mul_f16 v61, v54, v59 op_sel_hi:[0,1]\n"
    "v_mfma_f32_16x16x32_f16 v[76:79], v[128:131], v[72:75], v[76:79]\n"
    "v_pk_mul_f16 v62, v56, v58 op_sel_hi:[0,1]\n"
    "v_mfma_f32_16x16x32_f16 v[80:83], v[132:135], v[72:75], v[80:83]\n"
    "v_pk_mul_f16 v63, v56, v59 op_sel_hi:[0,1]\n"
    "v_mfma_f32_16x16x32_f16 v[80:83], v[120:123], v[68:71], v[80:83]\n"
    "v_pk_mul_f16 v64, v56, v56 op_sel_hi:[0,1]\n"
    "v_mfma_f32_16x16x32_f16 v[84:87], v[124:127], v[68:71], v[84:87]\n"
    "s_cmp_eq_u32 s36, 0\n"
    "v_pk_mul_f16 v65, v56, v49 op_sel_hi:[0,1]\n"
    "v_mfma_f32_16x16x32_f16 v[84:87], v[136:139], v[72:75], v[84:87]\n"
    "s_cbranch_scc1 .Lxd_ld46\n"
    ".Lxd_ld46r:\n"
    "v_pk_mul_f16 v66, v48, v56 op_sel_hi:[0,1]\n"
    "v_smfmac_f32_16x16x64_f16 v[88:91], v[140:143], v[68:75], v2\n"
    "v_pk_mul_f16 v67, v48, v57 op_sel_hi:[0,1]\n"
    "v_mfma_f32_16x16x32_f16 v[76:79], v[144:147], v[60:63], v[76:79]\n"
    "v_pk_mul_f16 v68, v50, v56 op_sel_hi:[0,1]\n"
    "v_pk_mul_f16 v69, v50, v57 op_sel_hi:[0,1]\n"
    "v_mfma_f32_16x16x32_f16 v[76:79], v[156:159], v[64:67], v[76:79]\n"
    "v_pk_mul_f16 v70, v52, v56 op_sel_hi:[0,1]\n"
    "v_mfma_f32_16x16x32_f16 v[80:83], v[160:163], v[64:67], v[80:83]\n"
    "v_pk_mul_f16 v71, v52, v57 op_sel_hi:[0,1]\n"
    "v_mfma_f32_16x16x32_f16 v[80:83], v[148:151], v[60:63], v[80:83]\n"
    "ds_read_b32 v22, v25 offset:528\n"
    "v_pk_mul_f16 v72, v54, v56 op_sel_hi:[0,1]\n"
    "v_mfma_f32_16x16x32_f16 v[84:87], v[152:155], v[60:63], v[84:87]\n"
    "v_pk_mul_f16 v73, v54, v57 op_sel_hi:[0,1]\n"
    "v_mfma_f32_16x16x32_f16 v[84:87], v[164:167], v[64:67], v[84:87]\n"
    "v_pk_mul_f16 v74, v54, v54 op_sel_hi:[0,1]\n"
    "v_smfmac_f32_16x16x64_f16 v[88:91], v[168:171], v[60:67], v2\n"
    "v_pk_mul_f16 v75, v54, v49 op_sel_hi:[0,1]\n"
    "v_mfma_f32_16x16x32_f16 v[76:79], v[172:175], v[68:71], v[76:79]\n"
    "v_pk_mul_f16 v60, v48, v54 op_sel_hi:[0,1]\n"
    "v_pk_mul_f16 v61, v48, v55 op_sel_hi:[0,1]\n"
    "v_mfma_f32_16x16x32_f16 v[76:79], v[184:187], v[72:75], v[76:79]\n"
    "s_waitcnt lgkmcnt(0)\n"
    "v_pk_mul_f16 v62, v50, v54 op_sel_hi:[0,1]\n"
    "v_readfirstlane_b32 s45, v22\n"
    "v_mfma_f32_16x16x32_f16 v[80:83], v[188:191], v[72:75], v[80:83]\n"
    "s_cmp_lt_i32 s45, 16\n"
    "v_pk_mul_f16 v63, v50, v55 op_sel_hi:[0,1]\n"
    "v_mfma_f32_16x16x32_f16 v[80:83], v[176:179], v[68:71], v[80:83]\n"
    "s_cbranch_scc1 .Lxd_ck48\n"
    ".Lxd_ck48r:\n"
    "v_pk_mul_f16 v64, v52, v54 op_sel_hi:[0,1]\n"
    "v_mfma_f32_16x16x32_f16 v[84:87], v[180:183], v[68:71], v[84:87]\n"
    "ds_read_b64_tr_b16 v[44:45], v5 offset:8192\n"
    "v_pk_mul_f16 v65, v52, v55 op_sel_hi:[0,1]\n"
    "v_mfma_f32_16x16x32_f16 v[84:87], v[192:195], v[72:75], v[84:87]\n"
    "ds_read_b64_tr_b16 v[46:47], v6 offset:8192\n"
    "v_pk_mul_f16 v66, v52, v52 op_sel_hi:[0,1]\n"
    "v_smfmac_f32_16x16x64_f16 v[88:91], v[196:199], v[68:75], v2\n"
    "v_pk_mul_f16 v67, v52, v49 op_sel_hi:[0,1]\n"
    "v_mfma_f32_16x16x32_f16 v[76:79], v[200:203], v[60:63], v[76:79]\n"
    "v_pk_mul_f16 v68, v48, v52 op_sel_hi:[0,1]\n"
    "v_pk_mul_f16 v69, v48, v53 op_sel_hi:[0,1]\n"
    "v_mfma_f32_16x16x32_f16 v[76:79], v[212:215], v[64:67], v[76:79]\n"
    "v_pk_mul_f16 v70, v50, v52 op_sel_hi:[0,1]\n"
    "v_mfma_f32_16x16x32_f16 v[80:83], v[216:219], v[64:67], v[80:83]\n"
    "s_cbranch_vccnz .Lxd_pr44\n"
    "s_setprio 0\n"
    ".Lxd_pr44:\n"
    "v_pk_mul_f16 v71, v50, v53 op_sel_hi:[0,1]\n"
    "v_mfma_f32_16x16x32_f16 v[80:83], v[204:207], v[60:63], v[80:83]\n"
    "v_pk_mul_f16 v72, v50, v50 op_sel_hi:[0,1]\n"
    "v_mfma_f32_16x16x32_f16 v[84:87], v[208:211], v[60:63], v[84:87]\n"
    "v_pk_mul_f16 v73, v50, v49 op_sel_hi:[0,1]\n"
    "v_pk_mul_f16 v74, v48, v50 op_sel_hi:[0,1]\n"
    "v_mfma_f32_16x16x32_f16 v[84:87], v[220:223], v[64:67], v[84:87]\n"
    "v_pk_mul_f16 v75, v48, v51 op_sel_hi:[0,1]\n"
    "v_smfmac_f32_16x16x64_f16 v[88:91], v[224:227], v[60:67], v2\n"
    "ds_read_b64_tr_b16 v[48:49], v3 offset:8192\n"
    "ds_read_b64_tr_b16 v[58:59], v4 offset:11072\n"
    "v_add_u32 v25, 16, v25\n"
    "v_smfmac_f32_16x16x64_f16 v[88:91], v[252:255], v[68:75], v2\n"
    "s_waitcnt lgkmcnt(2)\n"
    "v_pk_mul_f16 v60, v44, s45 op_sel:[0,1]\n"
    "v_pk_mul_f16 v61, v45, s45 op_sel:[0,1]\n"
    "v_mfma_f32_16x16x32_f16 v[84:87], v[236:239], v[68:71], v[84:87]\n"
    "ds_read_b64_tr_b16 v[50:51], v4 offset:8768\n"
    "v_pk_mul_f16 v62, v46, s45 op_sel:[0,1]\n"
    "v_pk_mul_f16 v63, v47, s45 op_sel:[0,1]\n"
    "v_mfma_f32_16x16x32_f16 v[84:87], v[248:251], v[72:75], v[84:87]\n"
    "ds_read_b64_tr_b16 v[52:53], v4 offset:9344\n"
    "v_pk_mul_f16 v64, v44, v46\n"
    "v_pk_mul_f16 v65, v45, v47\n"
    "v_mfma_f32_16x16x32_f16 v[76:79], v[240:243], v[72:75], v[76:79]\n"
    "ds_read_b64_tr_b16 v[54:55], v4 offset:9920\n"
    "v_add_f32 v11, v88, v89\n"
    "v_add_f32 v13, v90, v91\n"
    "v_mfma_f32_16x16x32_f16 v[76:79], v[228:231], v[68:71], v[76:79]\n"
    "ds_read_b64_tr_b16 v[56:57], v4 offset:10496\n"
    "v_mov_b64 v[88:89], 0\n"
    "v_mov_b64 v[90:91], 0\n"
    "v_mfma_f32_16x16x32_f16 v[80:83], v[232:235], v[68:71], v[80:83]\n"
    "s_waitcnt lgkmcnt(4)\n"
    "v_pk_mul_f16 v66, v48, v48 op_sel_hi:[0,1]\n"
    "v_pk_mul_f16 v67, v48, v49 op_sel_hi:[0,1]\n"
    "v_mfma_f32_16x16x32_f16 v[80:83], v[244:247], v[72:75], v[80:83]\n"
    "s_cmp_eq_u32 s36, 0\n"
    "v_mul_f32 v11, v11, v13\n"
    "v_fmac_f32 v11, v84, v85\n"
    "v_fmac_f32 v11, v86, v87\n"
    "s_cbranch_scc1 .Lxd_ph0\n"
    "s_branch .Lxd_pl0\n"
    ".Lxd_last1:\n"
    "s_cbranch_vccnz .Lxd_pr49\n"
    "s_setprio 0\n"
    ".Lxd_pr49:\n"
    "s_waitcnt vmcnt(0)\n"
    "v_pk_mul_f16 v64, v52, v54 op_sel_hi:[0,1]\n"
    "v_mfma_f32_16x16x32_f16 v[84:87], v[180:183], v[68:71], v[84:87]\n"
    "s_lshl4_add_u32 s66, s22, s58\n"
    "v_pk_mul_f16 v65, v52, v55 op_sel_hi:[0,1]\n"
    "v_xor_b32 v46, s28, v42\n"
    "v_mfma_f32_16x16x32_f16 v[84:87], v[192:195], v[72:75], v[84:87]\n"
    "v_pk_mul_f16 v66, v52, v52 op_sel_hi:[0,1]\n"
    "v_xor_b32 v44, s29, v43\n"
    "v_smfmac_f32_16x16x64_f16 v[88:91], v[196:199], v[68:75], v2\n"
    "v_pk_mul_f16 v67, v52, v49 op_sel_hi:[0,1]\n"
    "v_lshl_or_b32 v46, v46, 24, v44\n"
    "v_mfma_f32_16x16x32_f16 v[76:79], v[200:203], v[60:63], v[76:79]\n"
    "v_pk_mul_f16 v68, v48, v52 op_sel_hi:[0,1]\n"
    "v_pk_mul_f16 v69, v48, v53 op_sel_hi:[0,1]\n"
    "v_mfma_f32_16x16x32_f16 v[76:79], v[212:215], v[64:67], v[76:79]\n"
    "v_pk_mul_f16 v70, v50, v52 op_sel_hi:[0,1]\n"
    "v_and_b32 v44, 0xffffff00, v42\n"
    "v_mfma_f32_16x16x32_f16 v[80:83], v[216:219], v[64:67], v[80:83]\n"
    "v_pk_mul_f16 v71, v50, v53 op_sel_hi:[0,1]\n"
    "v_cmp_ne_u32 s[40:41], 0, v46\n"
    "v_xor_b32 v46, s28, v26\n"
    "v_mfma_f32_16x16x32_f16 v[80:83], v[204:207], v[60:63], v[80:83]\n"
    "v_pk_mul_f16 v72, v50, v50 op_sel_hi:[0,1]\n"
    "v_xor_b32 v45, s29, v27\n"
    "v_mfma_f32_16x16x32_f16 v[84:87], v[208:211], v[60:63], v[84:87]\n"
    "v_pk_mul_f16 v73, v50, v49 op_sel_hi:[0,1]\n"
    "v_pk_mul_f16 v74, v48, v50 op_sel_hi:[0,1]\n"
    "v_mfma_f32_16x16x32_f16 v[84:87], v[220:223], v[64:67], v[84:87]\n"
    "v_pk_mul_f16 v75, v48, v51 op_sel_hi:[0,1]\n"
    "v_lshl_or_b32 v46, v46, 24, v45\n"
    "v_smfmac_f32_16x16x64_f16 v[88:91], v[224:227], v[60:67], v2\n"
    "s_nop 0\n"
    "v_and_b32 v45, 0xffffff00, v26\n"
    "v_cmp_ne_u32 s[42:43], 0, v46\n"
    "v_smfmac_f32_16x16x64_f16 v[88:91], v[252:255], v[68:75], v2\n"
    "v_lshrrev_b32 v46, 4, v1\n"
    "v_lshl_add_u32 v46, v46, 2, s66\n"
    "v_mfma_f32_16x16x32_f16 v[84:87], v[236:239], v[68:71], v[84:87]\n"
    "s_and_b64 s[40:41], s[40:41], s[30:31]\n"
    "v_add_f32_dpp v44, v44, v44 quad_perm:[1,0,3,2] row_mask:0xf bank_mask:0xf\n"
    "v_add_f32_dpp v45, v45, v45 quad_perm:[1,0,3,2] row_mask:0xf bank_mask:0xf\n"
    "v_mfma_f32_16x16x32_f16 v[84:87], v[248:251], v[72:75], v[84:87]\n"
    "s_and_b64 s[42:43], s[42:43], s[64:65]\n"
    "v_add_f32_dpp v44, v44, v44 quad_perm:[2,3,0,1] row_mask:0xf bank_mask:0xf\n"
    "v_add_f32_dpp v45, v45, v45 quad_perm:[2,3,0,1] row_mask:0xf bank_mask:0xf\n"
    "v_mfma_f32_16x16x32_f16 v[76:79], v[240:243], v[72:75], v[76:79]\n"
    "s_or_b64 s[40:41], s[40:41], s[42:43]\n"
    "v_add_f32_dpp v44, v44, v44 row_half_mirror row_mask:0xf bank_mask:0xf\n"
    "v_add_f32_dpp v45, v45, v45 row_half_mirror row_mask:0xf bank_mask:0xf\n"
    "v_mfma_f32_16x16x32_f16 v[76:79], v[228:231], v[68:71], v[76:79]\n"
    "s_cselect_b32 s56, 1, 2\n"
    "v_add_f32_dpp v44, v44, v44 row_mirror row_mask:0xf bank_mask:0xf\n"
    "v_add_f32_dpp v45, v45, v45 row_mirror row_mask:0xf bank_mask:0xf\n"
    "v_mfma_f32_16x16x32_f16 v[80:83], v[232:235], v[68:71], v[80:83]\n"
    "ds_write_b32 v46, v44 offset:1024\n"
    "v_add_f32 v11, v88, v89\n"
    "v_add_f32 v13, v90, v91\n"
    "v_mfma_f32_16x16x32_f16 v[80:83], v[244:247], v[72:75], v[80:83]\n"
    "ds_write_b32 v46, v45 offset:1088\n"
    "v_mul_f32 v11, v11, v13\n"
    "v_fmac_f32 v11, v84, v85\n"
    "v_fmac_f32 v11, v86, v87\n"
    "s_nop 2\n"
    "s_branch .Lxd_last2\n"
    ".Lxd_last0:\n"
    "s_cbranch_vccnz .Lxd_pr50\n"
    "s_setprio 0\n"
    ".Lxd_pr50:\n"
    "v_pk_mul_f16 v64, v52, v54 op_sel_hi:[0,1]\n"
    "v_mfma_f32_16x16x32_f16 v[84:87], v[180:183], v[68:71], v[84:87]\n"
    "v_pk_mul_f16 v65, v52, v55 op_sel_hi:[0,1]\n"
    "v_mfma_f32_16x16x32_f16 v[84:87], v[192:195], v[72:75], v[84:87]\n"
    "v_pk_mul_f16 v66, v52, v52 op_sel_hi:[0,1]\n"
    "v_smfmac_f32_16x16x64_f16 v[88:91], v[196:199], v[68:75], v2\n"
    "v_pk_mul_f16 v67, v52, v49 op_sel_hi:[0,1]\n"
    "v_mfma_f32_16x16x32_f16 v[76:79], v[200:203], v[60:63], v[76:79]\n"
    "v_pk_mul_f16 v68, v48, v52 op_sel_hi:[0,1]\n"
    "v_pk_mul_f16 v69, v48, v53 op_sel_hi:[0,1]\n"
    "v_mfma_f32_16x16x32_f16 v[76:79], v[212:215], v[64:67], v[76:79]\n"
    "v_pk_mul_f16 v70, v50, v52 op_sel_hi:[0,1]\n"
    "v_mfma_f32_16x16x32_f16 v[80:83], v[216:219], v[64:67], v[80:83]\n"
    "v_pk_mul_f16 v71, v50, v53 op_sel_hi:[0,1]\n"
    "v_mfma_f32_16x16x32_f16 v[80:83], v[204:207], v[60:63], v[80:83]\n"
    "v_pk_mul_f16 v72, v50, v50 op_sel_hi:[0,1]\n"
    "v_mfma_f32_16x16x32_f16 v[84:87], v[208:211], v[60:63], v[84:87]\n"
    "v_pk_mul_f16 v73, v50, v49 op_sel_hi:[0,1]\n"
    "v_pk_mul_f16 v74, v48, v50 op_sel_hi:[0,1]\n"
    "v_mfma_f32_16x16x32_f16 v[84:87], v[220:223], v[64:67], v[84:87]\n"
    "v_pk_mul_f16 v75, v48, v51 op_sel_hi:[0,1]\n"
    "v_smfmac_f32_16x16x64_f16 v[88:91], v[224:227], v[60:67], v2\n"
    "s_nop 0\n"
    "v_smfmac_f32_16x16x64_f16 v[88:91], v[252:255], v[68:75], v2\n"
    "v_mfma_f32_16x16x32_f16 v[84:87], v[236:239], v[68:71], v[84:87]\n"
    "v_mfma_f32_16x16x32_f16 v[84:87], v[248:251], v[72:75], v[84:87]\n"
    "v_mfma_f32_16x16x32_f16 v[76:79], v[240:243], v[72:75], v[76:79]\n"
    "v_mfma_f32_16x16x32_f16 v[76:79], v[228:231], v[68:71], v[76:79]\n"
    "v_mfma_f32_16x16x32_f16 v[80:83], v[232:235], v[68:71], v[80:83]\n"
    "v_mfma_f32_16x16x32_f16 v[80:83], v[244:247], v[72:75], v[80:83]\n"
    "s_nop 1\n"
    "v_add_f32 v11, v88, v89\n"
    "v_add_f32 v13, v90, v91\n"
    "v_mul_f32 v11, v11, v13\n"
    "v_fmac_f32 v11, v84, v85\n"
    "v_fmac_f32 v11, v86, v87\n"
    "s_nop 0\n"
    ".Lxd_last2:\n"
    "v_fmac_f32 v11, v76, v80\n"
    "v_fmac_f32 v11, v77, v81\n"
    "v_fmac_f32 v11, v78, v82\n"
    "v_fma_f32 v11, v79, v83, v11\n"
    "s_set_gpr_idx_on s37, gpr_idx(DST)\n"
    "s_nop 0\n"
    "v_mov_b32 v7, v11\n"
    "s_set_gpr_idx_off\n"
    ".Lxd_fin:\n"
    "v_lshl_add_u32 v68, v18, 6, v20\n"
    "v_mov_b32 v67, 154144\n"
    "v_permlane32_swap_b32 v7, v8\n"
    "v_permlane32_swap_b32 v9, v10\n"
    "v_mul_f32 v7, v0, v7\n"
    "v_mul_f32 v9, v0, v9\n"
    "v_fmac_f32 v7, v23, v8\n"
    "v_fmac_f32 v9, v23, v10\n"
    "ds_read_b32 v67, v67\n"
    "v_lshlrev_b32 v69, 2, v68\n"
    "v_permlane16_swap_b32 v7, v9\n"
    "v_add_f32 v7, v7, v9\n"
    "v_add_u32 v68, 150528, v68\n"
    "v_add_u32 v69, 152128, v69\n"
    "v_add_f32_dpp v7, v7, v7 quad_perm:[1,0,3,2] row_mask:0xf bank_mask:0xf\n"
    "s_lshl_b32 s26, s26, 16\n"
    "s_nop 0\n"
    "v_add_f32_dpp v7, v7, v7 quad_perm:[2,3,0,1] row_mask:0xf bank_mask:0xf\n"
    "s_nop 1\n"
    "v_add_f32_dpp v7, v7, v7 row_half_mirror row_mask:0xf bank_mask:0xf\n"
    "s_nop 1\n"
    "v_add_f32_dpp v7, v7, v7 row_mirror row_mask:0xf bank_mask:0xf\n"
    "ds_write_b32 v14, v7\n"
    "s_cmp_lg_u32 s56, 2\n"
    "s_cbranch_scc1 .Lxd_f2\n"
    ".Lxd_f3:\n"
    "s_waitcnt vmcnt(0) lgkmcnt(0)\n"
    "s_barrier\n"
    "s_cmp_lg_u32 s20, 0\n"
    "s_cbranch_scc1 .Lxd_done\n"
    "ds_read_b128 v[60:63], v69\n"
    "ds_read_b32 v64, v68\n"
    "ds_read_u16 v65, v68 offset:512\n"
    "ds_read_b32 v66, v68 offset:1024\n"
    "s_waitcnt lgkmcnt(3)\n"
    "v_pk_add_f32 v[60:61], v[60:61], v[62:63]\n"
    "s_waitcnt lgkmcnt(0)\n"
    "v_cmp_lt_i32 vcc, 0, v64\n"
    "v_lshrrev_b32 v65, 4, v65\n"
    "v_add_f32 v60, v60, v61\n"
    "v_lshl_add_u32 v65, v65, 2, v67\n"
    "v_add_f32 v66, s26, v66\n"
    "v_ldexp_f32 v60, v60, v65\n"
    "s_mov_b64 exec, vcc\n"
    "v_add_f32 v60, v60, v66\n"
    "v_cvt_pk_bf16_f32 v60, v60, v60\n"
    "global_store_short v64, v60, s[70:71] offset:-2\n"
    "s_mov_b64 exec, -1\n"
    ".Lxd_done:\n"
    "s_endpgm\n"
    ".Lxd_cin:\n"
    "s_lshr_b32 s20, s43, 6\n"
    "v_and_b32 v1, 63, v0\n"
    "v_lshlrev_b32 v42, 4, v1\n"
    "s_and_b32 s40, s2, 3\n"
    "s_lshl2_add_u32 s23, s40, s20\n"
    "s_lshl1_add_u32 s41, s40, s20\n"
    "s_add_u32 s41, s41, 12\n"
    "s_cmp_ge_u32 s20, 4\n"
    "s_cselect_b32 s23, s41, s23\n"
    "s_add_u32 s41, s40, 24\n"
    "s_cmp_ge_u32 s20, 6\n"
    "s_cselect_b32 s23, s41, s23\n"
    "s_cselect_b32 s24, 1, 0\n"
    "s_min_u32 s34, s23, 25\n"
    "s_lshr_b32 s35, s2, 2\n"
    "v_and_b32 v190, 31, v0\n"
    "v_min_u32 v190, 25, v190\n"
    "v_mul_u32_u24 v190, 52, v190\n"
    "v_bfe_u32 v191, v0, 5, 1\n"
    "v_lshl_add_u32 v190, v191, 4, v190\n"
    "s_mul_i32 s40, s34, 1352\n"
    "s_xor_b32 s66, s20, 7\n"
    "s_or_b32 s66, s66, s2\n"
    "s_waitcnt lgkmcnt(0)\n"
    "s_load_dwordx4 s[68:71], s[0:1], 64\n"
    "s_add_u32 s38, s12, s40\n"
    "s_addc_u32 s39, s13, 0\n"
    "s_cmp_lg_u32 s66, 0\n"
    "s_cbranch_scc1 .Lxd_on51\n"
    "v_and_b32 v188, 31, v0\n"
    "v_lshlrev_b32 v188, 7, v188\n"
    "v_add_u32 v188, 32768, v188\n"
    "v_mov_b32 v176, 114\n"
    "global_store_dword v188, v176, s[16:17] sc1\n"
    "global_load_dwordx4 v[180:183], v42, s[16:17] sc1\n"
    ".Lxd_on51:\n"
    "global_load_dwordx4 v[80:83], v190, s[38:39] offset:0\n"
    "global_load_dwordx4 v[84:87], v190, s[38:39] offset:32\n"
    "s_getpc_b64 s[60:61]\n"
    "s_add_u32 s60, s60, xd_tab@rel32@lo+4\n"
    "s_addc_u32 s61, s61, xd_tab@rel32@hi+12\n"
    "v_lshl_add_u32 v188, s24, 10, v42\n"
    "s_lshl1_add_u32 s40, s24, s35\n"
    "v_lshl_add_u32 v189, s40, 11, v42\n"
    "s_load_dword s25, s[16:17], 0\n"
    "global_load_dwordx4 v[44:47], v188, s[60:61]\n"
    "global_load_dwordx4 v[48:51], v189, s[60:61] offset:2048\n"
    "global_load_dwordx4 v[52:55], v189, s[60:61] offset:3072\n"
    "s_setprio 0\n"
    "v_mov_b64 v[188:189], 0\n"
    "v_lshlrev_b32 v190, 3, v0\n"
    "v_lshrrev_b32 v186, 5, v1\n"
    "ds_write_b64 v190, v[188:189] offset:36864\n"
    "v_bfe_u32 v190, v0, 1, 4\n"
    "v_lshlrev_b32 v72, 2, v186\n"
    "v_sub_u32 v190, v190, v72\n"
    "v_and_b32 v72, 16, v42\n"
    "s_movk_i32 s40, 0x3f80\n"
    "v_lshlrev_b32_e64 v191, v72, s40\n"
    "v_cmp_eq_u32 s[60:61], 0, v190\n"
    "v_cmp_eq_u32 s[62:63], 1, v190\n"
    "v_cmp_eq_u32 s[64:65], 2, v190\n"
    "v_cmp_eq_u32 s[50:51], 3, v190\n"
    "v_cndmask_b32 v72, 0, v191, s[60:61]\n"
    "v_cndmask_b32 v73, 0, v191, s[62:63]\n"
    "v_cndmask_b32 v74, 0, v191, s[64:65]\n"
    "v_cndmask_b32 v75, 0, v191, s[50:51]\n"
    "v_cmp_eq_u32 s[60:61], 8, v190\n"
    "v_cmp_eq_u32 s[62:63], 9, v190\n"
    "v_cmp_eq_u32 s[64:65], 10, v190\n"
    "v_cmp_eq_u32 s[50:51], 11, v190\n"
    "v_cndmask_b32 v76, 0, v191, s[60:61]\n"
    "v_cndmask_b32 v77, 0, v191, s[62:63]\n"
    "v_cndmask_b32 v78, 0, v191, s[64:65]\n"
    "v_cndmask_b32 v79, 0, v191, s[50:51]\n"
    "s_lshl_b32 s30, s20, 12\n"
    "s_add_u32 s30, s30, 65536\n"
    "v_and_b32 v188, 31, v0\n"
    "v_mul_u32_u24 v192, 528, v186\n"
    "v_lshl_add_u32 v192, v188, 2, v192\n"
    "v_add_u32 v192, s30, v192\n"
    "s_lshl_b32 s40, s35, 6\n"
    "s_add_u32 s40, s40, s20\n"
    "s_lshl1_add_u32 s41, s20, s35\n"
    "s_add_u32 s41, s41, 120\n"
    "s_cmp_ge_u32 s20, 4\n"
    "s_cselect_b32 s40, s41, s40\n"
    "s_lshl1_add_u32 s41, s35, 384\n"
    "s_cmp_ge_u32 s20, 6\n"
    "s_cselect_b32 s40, s41, s40\n"
    "s_cselect_b32 s42, 1, 192\n"
    "s_and_b32 s41, s2, 3\n"
    "s_lshl2_add_u32 s40, s41, s40\n"
    "v_lshrrev_b32 v188, 3, v1\n"
    "v_mul_u32_u24 v188, 448, v188\n"
    "v_bfe_u32 v189, v0, 2, 1\n"
    "v_mad_u32_u24 v188, v189, s42, v188\n"
    "v_and_b32 v189, 48, v42\n"
    "v_add3_u32 v188, v188, v189, s40\n"
    "v_lshlrev_b32 v193, 4, v188\n"
    "v_mov_b32 v177, 0\n"
    "s_waitcnt vmcnt(0) lgkmcnt(0)\n"
    "s_cmp_lg_u32 s66, 0\n"
    "s_cbranch_scc1 .Lxd_on52\n"
    "s_mov_b32 s40, 0x9e3779b1\n"
    "v_mul_lo_u32 v144, v180, s40\n"
    "v_xor_b32 v145, 0xa5c30f96, v180\n"
    "v_alignbit_b32 v144, s25, v144, 24\n"
    "v_mul_lo_u32 v146, v181, s40\n"
    "v_xor_b32 v147, 0xa5c30f96, v181\n"
    "v_alignbit_b32 v146, s25, v146, 24\n"
    "v_mul_lo_u32 v148, v182, s40\n"
    "v_xor_b32 v149, 0xa5c30f96, v182\n"
    "v_alignbit_b32 v148, s25, v148, 24\n"
    "v_mul_lo_u32 v150, v183, s40\n"
    "v_xor_b32 v151, 0xa5c30f96, v183\n"
    "v_alignbit_b32 v150, s25, v150, 24\n"
    "v_lshlrev_b32 v180, 1, v42\n"
    "s_add_u32 s40, s16, 4096\n"
    "s_addc_u32 s41, s17, 0\n"
    "global_store_dwordx4 v180, v[144:147], s[40:41] sc1\n"
    "global_store_dwordx4 v180, v[148:151], s[40:41] offset:16 sc1\n"
    ".Lxd_on52:\n"
    "s_barrier\n"
    "s_branch .Lxd_bD\n"
    ".Lxd_s2:\n"
    "s_cmp_lt_u32 s2, 4\n"
    "s_cbranch_scc1 .Lxd_cin\n"
    ".Lxd_bR:\n"
    "s_lshr_b32 s20, s43, 6\n"
    "v_and_b32 v1, 63, v0\n"
    "v_lshlrev_b32 v42, 4, v1\n"
    "s_mul_i32 s40, s20, 4480\n"
    "s_min_u32 s40, s40, 30672\n"
    "s_mov_b32 m0, s40\n"
    "s_add_u32 s40, s40, 70304\n"
    "s_waitcnt lgkmcnt(0)\n"
    "s_load_dwordx4 s[68:71], s[0:1], 64\n"
    "s_add_u32 s38, s12, s40\n"
    "s_addc_u32 s39, s13, 0\n"
    "global_load_lds_dwordx4 v42, s[38:39] offset:0\n"
    "global_load_lds_dwordx4 v42, s[38:39] offset:1024\n"
    "global_load_lds_dwordx4 v42, s[38:39] offset:2048\n"
    "global_load_lds_dwordx4 v42, s[38:39] offset:3072\n"
    "global_load_lds_dwordx4 v42, s[38:39] offset:3456\n"
    "s_and_b32 s40, s2, 3\n"
    "s_lshl2_add_u32 s23, s40, s20\n"
    "s_lshl1_add_u32 s41, s40, s20\n"
    "s_add_u32 s41, s41, 12\n"
    "s_cmp_ge_u32 s20, 4\n"
    "s_cselect_b32 s23, s41, s23\n"
    "s_add_u32 s41, s40, 24\n"
    "s_cmp_ge_u32 s20, 6\n"
    "s_cselect_b32 s23, s41, s23\n"
    "s_cselect_b32 s24, 1, 0\n"
    "s_min_u32 s34, s23, 25\n"
    "s_lshr_b32 s35, s2, 2\n"
    "v_lshrrev_b32 v190, 2, v1\n"
    "v_and_b32 v188, 48, v42\n"
    "s_movk_i32 s42, 1352\n"
    "v_min_u32 v191, 9, v190\n"
    "v_mad_u32_u24 v190, v190, s42, v188\n"
    "v_mad_u32_u24 v191, v191, s42, v188\n"
    "v_add_u32 v191, 20608, v191\n"
    "s_mul_i32 s41, s34, 52\n"
    "s_add_u32 s41, s41, 35152\n"
    "s_cmp_lg_u32 s20, 7\n"
    "s_cbranch_scc1 .Lxd_on53\n"
    "v_min_u32 v188, 38, v1\n"
    "s_mov_b32 m0, 40960\n"
    "v_lshlrev_b32 v188, 2, v188\n"
    "global_load_lds_dword v188, s[14:15]\n"
    ".Lxd_on53:\n"
    "s_lshl_b32 s28, s20, 11\n"
    "s_add_u32 s28, s28, 49152\n"
    "s_mov_b32 m0, s28\n"
    "s_add_u32 s38, s12, s41\n"
    "s_addc_u32 s39, s13, 0\n"
    "global_load_lds_dwordx4 v190, s[38:39] offset:0\n"
    "global_load_lds_dwordx4 v191, s[38:39] offset:1024\n"
    "s_getpc_b64 s[60:61]\n"
    "s_add_u32 s60, s60, xd_tab@rel32@lo+4\n"
    "s_addc_u32 s61, s61, xd_tab@rel32@hi+12\n"
    "v_lshl_add_u32 v188, s24, 10, v42\n"
    "s_lshl1_add_u32 s40, s24, s35\n"
    "v_lshl_add_u32 v189, s40, 11, v42\n"
    "s_load_dword s25, s[16:17], 0\n"
    "global_load_dwordx4 v[44:47], v188, s[60:61]\n"
    "global_load_dwordx4 v[48:51], v189, s[60:61] offset:2048\n"
    "global_load_dwordx4 v[52:55], v189, s[60:61] offset:3072\n"
    "s_setprio 0\n"
    "v_mov_b64 v[188:189], 0\n"
    "v_lshlrev_b32 v190, 3, v0\n"
    "v_lshrrev_b32 v186, 5, v1\n"
    "ds_write_b64 v190, v[188:189] offset:36864\n"
    "v_lshrrev_b32 v184, 4, v1\n"
    "v_and_b32 v185, 15, v0\n"
    "v_lshrrev_b32 v190, 2, v185\n"
    "v_lshl_add_u32 v64, v184, 3, v190\n"
    "v_add_u32 v65, 4, v64\n"
    "s_lshr_b32 s21, s20, 2\n"
    "s_and_b32 s43, s20, 3\n"
    "s_mul_i32 s40, s21, 13\n"
    "s_lshl2_add_u32 s40, s43, s40\n"
    "v_and_b32 v191, 3, v0\n"
    "v_add_lshl_u32 v188, v191, s40, 3\n"
    "v_min_u32 v64, 25, v64\n"
    "v_mad_u32_u24 v64, v64, s42, v188\n"
    "v_min_u32 v65, 25, v65\n"
    "v_mad_u32_u24 v65, v65, s42, v188\n"
    "v_lshl_add_u32 v188, s43, 4, v185\n"
    "v_cmp_lt_u32 vcc, 25, v188\n"
    "v_cmp_lt_u32 s[60:61], 51, v188\n"
    "v_lshl_add_u32 v66, s21, 6, v188\n"
    "v_cndmask_b32_e64 v189, 0, 6, vcc\n"
    "v_add_lshl_u32 v66, v66, v189, 1\n"
    "v_mov_b32 v189, 0x40000\n"
    "v_cmp_gt_u32 vcc, 26, v1\n"
    "v_cndmask_b32 v66, v66, v189, s[60:61]\n"
    "v_lshlrev_b32 v67, 6, v1\n"
    "v_cndmask_b32 v67, v189, v67, vcc\n"
    "v_lshlrev_b32 v68, 4, v184\n"
    "v_add_u32 v68, 40960, v68\n"
    "v_lshlrev_b32 v69, 1, v1\n"
    "v_lshl_add_u32 v190, v186, 3, v190\n"
    "v_and_b32 v188, 1, v184\n"
    "v_lshl_add_u32 v188, v188, 2, v191\n"
    "v_cmp_eq_u32 vcc, 7, v188\n"
    "v_lshlrev_b32 v188, 3, v188\n"
    "v_lshl_add_u32 v70, v190, 6, v188\n"
    "v_add_u32 v71, s28, v70\n"
    "v_mov_b32 v188, 38912\n"
    "v_cndmask_b32 v71, v71, v188, vcc\n"
    "v_cmp_gt_u32 vcc, 3, v184\n"
    "s_waitcnt vmcnt(5) lgkmcnt(0)\n"
    "s_barrier\n"
    "ds_read2_b64 v[176:179], v68 offset0:13 offset1:14\n"
    "ds_read_u16 v68, v69 offset:41012\n"
    "ds_read_u16 v69, v69 offset:40960\n"
    "ds_read_b64_tr_b16 v[112:113], v64 offset:0\n"
    "ds_read_b64_tr_b16 v[114:115], v65 offset:0\n"
    "ds_read_b64_tr_b16 v[116:117], v64 offset:208\n"
    "ds_read_b64_tr_b16 v[118:119], v65 offset:208\n"
    "ds_read_b64_tr_b16 v[120:121], v64 offset:416\n"
    "ds_read_b64_tr_b16 v[122:123], v65 offset:416\n"
    "ds_read_b64_tr_b16 v[124:125], v64 offset:624\n"
    "ds_read_b64_tr_b16 v[126:127], v65 offset:624\n"
    "ds_read_b64_tr_b16 v[128:129], v64 offset:832\n"
    "ds_read_b64_tr_b16 v[130:131], v65 offset:832\n"
    "ds_read_b64_tr_b16 v[132:133], v64 offset:1040\n"
    "ds_read_b64_tr_b16 v[134:135], v65 offset:1040\n"
    "ds_read_b64_tr_b16 v[136:137], v64 offset:1248\n"
    "ds_read_b64_tr_b16 v[138:139], v65 offset:1248\n"
    "s_waitcnt lgkmcnt(14)\n"
    "v_cndmask_b32 v177, 0, v177, vcc\n"
    "v_cndmask_b32 v178, 0, v178, vcc\n"
    "v_cndmask_b32 v179, 0, v179, vcc\n"
    "ds_write_b16 v67, v68 offset:36920\n"
    "s_waitcnt lgkmcnt(13)\n"
    "v_mfma_f32_16x16x32_bf16 v[144:147], v[176:179], v[112:115], 0\n"
    "s_waitcnt lgkmcnt(11)\n"
    "v_mfma_f32_16x16x32_bf16 v[148:151], v[176:179], v[116:119], 0\n"
    "s_waitcnt lgkmcnt(9)\n"
    "v_mfma_f32_16x16x32_bf16 v[152:155], v[176:179], v[120:123], 0\n"
    "s_waitcnt lgkmcnt(7)\n"
    "v_mfma_f32_16x16x32_bf16 v[156:159], v[176:179], v[124:127], 0\n"
    "s_waitcnt lgkmcnt(5)\n"
    "v_mfma_f32_16x16x32_bf16 v[160:163], v[176:179], v[128:131], 0\n"
    "s_waitcnt lgkmcnt(3)\n"
    "v_mfma_f32_16x16x32_bf16 v[164:167], v[176:179], v[132:135], 0\n"
    "s_waitcnt lgkmcnt(1)\n"
    "v_mfma_f32_16x16x32_bf16 v[168:171], v[176:179], v[136:139], 0\n"
    "s_waitcnt vmcnt(3)\n"
    "ds_read_b64_tr_b16 v[80:81], v71 offset:0\n"
    "ds_read_b64_tr_b16 v[82:83], v71 offset:256\n"
    "ds_read_b64_tr_b16 v[84:85], v71 offset:1024\n"
    "ds_read_b64_tr_b16 v[86:87], v71 offset:1280\n"
    "v_cvt_pk_bf16_f32 v144, v144, v144\n"
    "ds_write_b16 v66, v144 offset:36864\n"
    "v_cvt_pk_bf16_f32 v148, v148, v148\n"
    "ds_write_b16 v66, v148 offset:37120\n"
    "v_cvt_pk_bf16_f32 v152, v152, v152\n"
    "ds_write_b16 v66, v152 offset:37376\n"
    "v_cvt_pk_bf16_f32 v156, v156, v156\n"
    "ds_write_b16 v66, v156 offset:37632\n"
    "v_cvt_pk_bf16_f32 v160, v160, v160\n"
    "ds_write_b16 v66, v160 offset:37888\n"
    "v_cvt_pk_bf16_f32 v164, v164, v164\n"
    "ds_write_b16 v66, v164 offset:38144\n"
    "s_cmp_lg_u32 s21, 0\n"
    "s_cbranch_scc1 .Lxd_on54\n"
    "v_cvt_pk_bf16_f32 v168, v168, v168\n"
    "ds_write_b16 v66, v168 offset:38400\n"
    ".Lxd_on54:\n"
    "v_lshlrev_b32 v177, 16, v69\n"
    "s_waitcnt lgkmcnt(0)\n"
    "s_barrier\n"
    "ds_read_b64_tr_b16 v[72:73], v70 offset:36864\n"
    "ds_read_b64_tr_b16 v[74:75], v70 offset:37120\n"
    "ds_read_b64_tr_b16 v[76:77], v70 offset:37888\n"
    "ds_read_b64_tr_b16 v[78:79], v70 offset:38144\n"
    "s_lshl_b32 s30, s20, 12\n"
    "s_add_u32 s30, s30, 65536\n"
    "v_and_b32 v188, 31, v0\n"
    "v_mul_u32_u24 v192, 528, v186\n"
    "v_lshl_add_u32 v192, v188, 2, v192\n"
    "v_add_u32 v192, s30, v192\n"
    "s_lshl_b32 s40, s35, 6\n"
    "s_add_u32 s40, s40, s20\n"
    "s_lshl1_add_u32 s41, s20, s35\n"
    "s_add_u32 s41, s41, 120\n"
    "s_cmp_ge_u32 s20, 4\n"
    "s_cselect_b32 s40, s41, s40\n"
    "s_lshl1_add_u32 s41, s35, 384\n"
    "s_cmp_ge_u32 s20, 6\n"
    "s_cselect_b32 s40, s41, s40\n"
    "s_cselect_b32 s42, 1, 192\n"
    "s_and_b32 s41, s2, 3\n"
    "s_lshl2_add_u32 s40, s41, s40\n"
    "v_lshrrev_b32 v188, 3, v1\n"
    "v_mul_u32_u24 v188, 448, v188\n"
    "v_bfe_u32 v189, v0, 2, 1\n"
    "v_mad_u32_u24 v188, v189, s42, v188\n"
    "v_and_b32 v189, 48, v42\n"
    "v_add3_u32 v188, v188, v189, s40\n"
    "v_lshlrev_b32 v193, 4, v188\n"
    "s_waitcnt vmcnt(0) lgkmcnt(0)\n"
    ".Lxd_bD:\n"
    "v_mfma_f32_32x32x16_bf16 v[96:111], v[72:75], v[80:83], 0\n"
    "v_mfma_f32_32x32x16_bf16 v[96:111], v[80:83], v[72:75], v[96:111]\n"
    "v_mfma_f32_32x32x16_bf16 v[96:111], v[76:79], v[84:87], v[96:111]\n"
    "v_mfma_f32_32x32x16_bf16 v[96:111], v[84:87], v[76:79], v[96:111]\n"
    "v_readlane_b32 s43, v177, s34\n"
    "v_mad_u32_u16 v64, v44, 4, s30 op_sel:[0,0,0,0]\n"
    "v_mad_u32_u16 v65, v44, 4, s30 op_sel:[1,0,0,0]\n"
    "v_mad_u32_u16 v66, v45, 4, s30 op_sel:[0,0,0,0]\n"
    "v_mad_u32_u16 v67, v45, 4, s30 op_sel:[1,0,0,0]\n"
    "v_mad_u32_u16 v68, v46, 4, s30 op_sel:[0,0,0,0]\n"
    "v_mad_u32_u16 v69, v46, 4, s30 op_sel:[1,0,0,0]\n"
    "v_mad_u32_u16 v70, v47, 4, s30 op_sel:[0,0,0,0]\n"
    "v_mad_u32_u16 v71, v47, 4, s30 op_sel:[1,0,0,0]\n"
    "v_mov_b32 v184, 40944\n"
    "s_and_b32 s25, s25, 0xffffff\n"
    "s_mul_i32 s40, s25, 0x9e3779b1\n"
    "s_xor_b32 s41, s25, 0xa5c30f96\n"
    "s_lshr_b32 s40, s40, 24\n"
    "v_writelane_b32 v108, s43, 60\n"
    "ds_write2_b32 v192, v96, v97 offset0:0 offset1:33\n"
    "ds_write2_b32 v192, v98, v99 offset0:66 offset1:99\n"
    "v_add_u32 v192, 1056, v192\n"
    "ds_write2_b32 v192, v100, v101 offset0:0 offset1:33\n"
    "ds_write2_b32 v192, v102, v103 offset0:66 offset1:99\n"
    "v_add_u32 v192, 1056, v192\n"
    "ds_write2_b32 v192, v104, v105 offset0:0 offset1:33\n"
    "ds_write2_b32 v192, v106, v107 offset0:66 offset1:99\n"
    "v_add_u32 v192, 1056, v192\n"
    "ds_write2_b32 v192, v108, v109 offset0:0 offset1:33\n"
    "ds_read_b32 v56, v64\n"
    "ds_read_b32 v57, v65\n"
    "ds_read_b32 v58, v66\n"
    "ds_read_b32 v59, v67\n"
    "ds_read_b32 v60, v68\n"
    "ds_read_b32 v61, v69\n"
    "ds_read_b32 v62, v70\n"
    "ds_read_b32 v63, v71\n"
    "s_waitcnt lgkmcnt(6)\n"
    "v_pk_mul_f32 v[56:57], v[56:57], v[48:49]\n"
    "v_max3_f32 v188, |v56|, |v57|, 0\n"
    "s_waitcnt lgkmcnt(4)\n"
    "v_pk_mul_f32 v[58:59], v[58:59], v[50:51]\n"
    "v_max3_f32 v188, |v58|, |v59|, v188\n"
    "s_waitcnt lgkmcnt(2)\n"
    "v_pk_mul_f32 v[60:61], v[60:61], v[52:53]\n"
    "v_max3_f32 v188, |v60|, |v61|, v188\n"
    "s_waitcnt lgkmcnt(0)\n"
    "v_pk_mul_f32 v[62:63], v[62:63], v[54:55]\n"
    "v_max3_f32 v188, |v62|, |v63|, v188\n"
    "s_nop 1\n"
    "v_max_u32_dpp v188, v188, v188 quad_perm:[1,0,3,2] row_mask:0xf bank_mask:0xf\n"
    "s_nop 1\n"
    "v_max_u32_dpp v188, v188, v188 quad_perm:[2,3,0,1] row_mask:0xf bank_mask:0xf\n"
    "s_nop 1\n"
    "v_max_u32_dpp v188, v188, v188 row_half_mirror row_mask:0xf bank_mask:0xf\n"
    "s_nop 1\n"
    "v_max_u32_dpp v188, v188, v188 row_mirror row_mask:0xf bank_mask:0xf\n"
    "s_mov_b32 exec_lo, 0x10001\n"
    "s_mov_b32 exec_hi, 1\n"
    "ds_max_u32 v184, v188\n"
    "s_mov_b64 exec, -1\n"
    "s_add_u32 s38, s16, 1047552\n"
    "s_addc_u32 s39, s17, 0\n"
    "s_cmp_lt_u32 s23, 26\n"
    "s_cselect_b32 s43, 0xfff8fff8, 0\n"
    "s_waitcnt lgkmcnt(0)\n"
    "s_barrier\n"
    "ds_read_b32 v188, v184\n"
    "v_cmp_le_u32 vcc, 0x400, v193\n"
    "s_cmp_eq_u32 s20, 7\n"
    "s_cselect_b32 s42, 0, 48\n"
    "s_bfm_b64 s[60:61], s42, 0\n"
    "s_and_b64 s[60:61], s[60:61], vcc\n"
    "s_waitcnt lgkmcnt(0)\n"
    "v_lshrrev_b32 v188, 23, v188\n"
    "v_max_u32 v188, 40, v188\n"
    "v_min_u32 v188, 250, v188\n"
    "v_sub_u32 v190, 268, v188\n"
    "v_lshlrev_b32 v190, 23, v190\n"
    "v_pk_mul_f32 v[56:57], v[56:57], v[190:191] op_sel_hi:[1,0]\n"
    "v_cvt_pkrtz_f16_f32 v56, v56, v57\n"
    "v_add_u32 v56, 0x40004, v56\n"
    "v_and_b32 v56, s43, v56\n"
    "v_pk_mul_f32 v[58:59], v[58:59], v[190:191] op_sel_hi:[1,0]\n"
    "v_cvt_pkrtz_f16_f32 v57, v58, v59\n"
    "v_add_u32 v57, 0x40004, v57\n"
    "v_and_b32 v57, s43, v57\n"
    "v_pk_mul_f32 v[60:61], v[60:61], v[190:191] op_sel_hi:[1,0]\n"
    "v_cvt_pkrtz_f16_f32 v58, v60, v61\n"
    "v_add_u32 v58, 0x40004, v58\n"
    "v_and_b32 v58, s43, v58\n"
    "v_pk_mul_f32 v[62:63], v[62:63], v[190:191] op_sel_hi:[1,0]\n"
    "v_cvt_pkrtz_f16_f32 v59, v62, v63\n"
    "v_add_u32 v59, 0x40004, v59\n"
    "v_and_b32 v59, s43, v59\n"
    "v_lshl_or_b32 v188, v188, 8, s40\n"
    "v_mov_b32 v189, s41\n"
    "s_lshl3_add_u32 s40, s2, 40960\n"
    "v_mov_b32 v190, s40\n"
    "s_cmp_eq_u32 s20, 7\n"
    "s_cbranch_scc1 .Lxd_b3\n"
    "s_mov_b64 exec, s[60:61]\n"
    "global_store_dwordx4 v193, v[56:59], s[38:39] sc1\n"
    "s_mov_b64 exec, -1\n"
    ".Lxd_b3:\n"
    "s_waitcnt vmcnt(0)\n"
    "s_barrier\n"
    "s_cmp_lg_u32 s20, 0\n"
    "s_cbranch_scc1 .Lxd_on55\n"
    "s_mov_b64 exec, 1\n"
    "global_store_dwordx2 v190, v[188:189], s[16:17] sc1\n"
    "s_mov_b64 exec, -1\n"
    ".Lxd_on55:\n"
    "s_branch .Lxd_h1\n"
    ".Lxd_help:\n"
    "s_lshr_b32 s20, s43, 6\n"
    "v_and_b32 v1, 63, v0\n"
    "v_lshlrev_b32 v42, 4, v1\n"
    "s_lshr_b32 s41, s2, 3\n"
    "s_sub_u32 s41, s41, 1\n"
    "s_cmp_lg_u32 s20, 7\n"
    "s_cbranch_scc1 .Lxd_on57\n"
    "s_mul_i32 s40, s41, 64\n"
    "v_min_u32 v7, 63, v1\n"
    "v_add_lshl_u32 v7, v7, s40, 7\n"
    "s_getpc_b64 s[60:61]\n"
    ".Lxd_pc56:\n"
    "s_sub_u32 s60, s60, .Lxd_pc56-.Lxd_top\n"
    "s_subb_u32 s61, s61, 0\n"
    "v_min_u32 v7, .Lxd_end-.Lxd_top-4, v7\n"
    "global_load_dword v7, v7, s[60:61]\n"
    ".Lxd_on57:\n"
    "v_mul_u32_u24 v68, 52, v1\n"
    "v_lshrrev_b32 v68, 8, v68\n"
    "v_mad_i32_i24 v69, v68, -5, v1\n"
    "s_and_b32 s40, s2, 0xf8\n"
    "s_add_u32 s40, s40, s20\n"
    "s_mul_i32 s40, s40, 13\n"
    "s_sub_u32 s40, s40, 104\n"
    "v_add_u32 v68, s40, v68\n"
    "v_min_u32 v68, 0x1ff, v68\n"
    "v_lshrrev_b32 v70, 8, v68\n"
    "s_movk_i32 s40, 1792\n"
    "v_mad_u32_u24 v68, v70, s40, v68\n"
    "s_and_b32 s40, s2, 7\n"
    "v_lshl_add_u32 v68, s40, 8, v68\n"
    "v_mul_u32_u24 v68, 104, v68\n"
    "s_mul_i32 s40, s40, 3\n"
    "v_add3_u32 v70, v69, s40, 2\n"
    "v_subrev_u32 v71, 24, v70\n"
    "v_min_u32 v70, v70, v71\n"
    "v_lshl_add_u32 v70, v70, 2, v68\n"
    "s_waitcnt lgkmcnt(0)\n"
    "s_load_dwordx4 s[68:71], s[0:1], 64\n"
    "global_load_dword v60, v70, s[4:5]\n"
    "v_add3_u32 v70, v69, s40, 7\n"
    "v_subrev_u32 v71, 24, v70\n"
    "v_min_u32 v70, v70, v71\n"
    "v_lshl_add_u32 v70, v70, 2, v68\n"
    "global_load_dword v61, v70, s[4:5]\n"
    "v_add3_u32 v70, v69, s40, 12\n"
    "v_subrev_u32 v71, 24, v70\n"
    "v_min_u32 v70, v70, v71\n"
    "v_lshl_add_u32 v70, v70, 2, v68\n"
    "global_load_dword v62, v70, s[4:5]\n"
    "v_add3_u32 v70, v69, s40, 17\n"
    "v_subrev_u32 v71, 24, v70\n"
    "v_min_u32 v70, v70, v71\n"
    "v_lshl_add_u32 v70, v70, 2, v68\n"
    "global_load_dword v63, v70, s[4:5]\n"
    "v_add3_u32 v70, v69, s40, 22\n"
    "v_subrev_u32 v71, 24, v70\n"
    "v_min_u32 v70, v70, v71\n"
    "v_add_u32 v71, 22, v69\n"
    "v_cmp_lt_u32 vcc, 23, v71\n"
    "v_min_u32 v71, 25, v71\n"
    "s_nop 0\n"
    "v_cndmask_b32 v70, v70, v71, vcc\n"
    "v_lshl_add_u32 v70, v70, 2, v68\n"
    "global_load_dword v64, v70, s[4:5]\n"
    "s_cmp_lg_u32 s20, 6\n"
    "s_cbranch_scc1 .Lxd_on58\n"
    "v_min_u32 v12, 25, v1\n"
    "s_mov_b32 m0, 159744\n"
    "v_lshlrev_b32 v12, 7, v12\n"
    "global_load_lds_dword v12, s[10:11]\n"
    "global_load_lds_dword v12, s[18:19]\n"
    ".Lxd_on58:\n"
    ".Lxd_h1:\n"
    "s_lshr_b32 s3, s2, 4\n"
    "s_mul_i32 s40, s3, 3\n"
    "s_add_u32 s47, s2, s40\n"
    "s_and_b32 s47, s47, 15\n"
    "s_lshr_b32 s21, s20, 2\n"
    "s_lshl1_add_u32 s22, s21, s20\n"
    "s_and_b32 s22, s22, 3\n"
    "s_lshl_b32 s58, s21, 8\n"
    "s_add_u32 s58, s58, 150528\n"
    "s_mov_b32 s40, s47\n"
    "s_mul_i32 s54, s40, 512\n"
    "s_add_u32 s54, s54, 2\n"
    "s_lshl_b32 s40, s40, 7\n"
    "s_add_u32 s40, s40, 32768\n"
    "s_add_u32 s52, s16, s40\n"
    "s_addc_u32 s53, s17, 0\n"
    "s_mov_b32 s57, 0x7fff7fff\n"
    "s_mov_b32 s56, 0\n"
    "s_mov_b32 s27, 0\n"
    "s_mov_b32 s55, 9\n"
    "s_mov_b32 s44, 0\n"
    "s_mov_b32 s46, 0\n"
    "v_lshrrev_b32 v17, 3, v1\n"
    "v_mul_u32_u24 v17, 12, v17\n"
    "v_and_b32 v19, 7, v1\n"
    "v_lshlrev_b32 v19, 4, v19\n"
    "v_lshrrev_b32 v18, 5, v1\n"
    "v_lshlrev_b32 v18, 2, v18\n"
    "v_and_b32 v20, 31, v1\n"
    "v_lshlrev_b32 v20, 2, v20\n"
    "s_lshl2_add_u32 s40, s22, s58\n"
    "s_sub_u32 s40, s40, 20\n"
    "v_lshl_add_u32 v25, v1, 18, s40\n"
    "s_lshl1_add_u32 s23, s3, s21\n"
    "s_mul_i32 s23, s23, 6\n"
    "s_add_u32 s23, s23, s22\n"
    "s_add_u32 s23, s23, 78\n"
    "v_mov_b32 v24, s23\n"
    "v_lshlrev_b32 v13, 2, v0\n"
    "v_readfirstlane_b32 s40, v24\n"
    "s_or_b32 s40, s40, s44\n"
    "s_lshl1_add_u32 s41, s40, s54\n"
    "s_cmpk_lt_u32 s40, 256\n"
    "s_cselect_b32 s41, s41, 2\n"
    "v_mov_b32 v12, s41\n"
    "s_mul_i32 s41, s41, 52\n"
    "s_add_u32 s38, s4, s41\n"
    "s_addc_u32 s39, s5, 0\n"
    "global_load_dwordx3 v[36:38], v17, s[38:39] offset:-104\n"
    "global_load_dword v41, v18, s[38:39] offset:-8\n"
    "s_setprio 0\n"
    "v_mov_b32 v21, 0\n"
    "v_add_u32 v13, 149504, v13\n"
    "ds_write_b32 v13, v21\n"
    "s_cmp_lt_u32 s2, 8\n"
    "s_cbranch_scc1 .Lxd_hp59\n"
    "s_waitcnt vmcnt(2)\n"
    "s_mov_b32 m0, 159744\n"
    "v_lshlrev_b32 v60, 7, v60\n"
    "global_load_lds_dword v60, s[8:9]\n"
    "s_mov_b32 m0, 159744\n"
    "v_lshlrev_b32 v61, 7, v61\n"
    "global_load_lds_dword v61, s[8:9]\n"
    "s_waitcnt vmcnt(2)\n"
    "s_branch .Lxd_h3\n"
    ".Lxd_hp59:\n"
    "s_waitcnt vmcnt(0)\n"
    ".Lxd_h3:\n"
    "v_lshl_or_b32 v36, v36, 7, v19\n"
    "v_lshl_or_b32 v37, v37, 7, v19\n"
    "v_lshl_or_b32 v38, v38, 7, v19\n"
    "v_lshl_or_b32 v41, v41, 7, v20\n"
    "global_load_dwordx4 v[28:31], v36, s[8:9]\n"
    "global_load_dwordx4 v[32:35], v37, s[8:9]\n"
    "global_load_dword v40, v41, s[8:9]\n"
    "global_load_dwordx4 v[36:39], v38, s[8:9]\n"
    "s_waitcnt lgkmcnt(0)\n"
    "s_barrier\n"
    "ds_write_b32 v25, v12 offset:20\n"
    "s_load_dword s26, s[68:69], 0\n"
    "s_lshl_b32 s43, s21, 16\n"
    "v_lshrrev_b32 v12, 4, v1\n"
    "v_bfe_u32 v13, v1, 2, 2\n"
    "v_and_b32 v21, 3, v1\n"
    "s_mul_i32 s40, s22, 0x2020\n"
    "s_and_b32 s40, s40, 0x4060\n"
    "s_add_u32 s40, s40, s43\n"
    "v_lshl_add_u32 v21, v21, 3, s40\n"
    "s_movk_i32 s41, 144\n"
    "v_add_u32 v22, v12, v13\n"
    "v_and_b32 v22, 3, v22\n"
    "v_mad_u32_u24 v4, v22, s41, v21\n"
    "v_cmp_gt_u32 vcc, 2, v13\n"
    "v_add_u32 v24, 22, v13\n"
    "v_lshlrev_b32 v13, 3, v13\n"
    "v_cndmask_b32 v22, v24, v22, vcc\n"
    "v_mad_u32_u24 v3, v22, s41, v21\n"
    "v_cmp_eq_u32 vcc, 3, v12\n"
    "v_xor_b32 v22, 0, v12\n"
    "v_and_b32 v22, 1, v22\n"
    "v_lshlrev_b32 v22, 1, v22\n"
    "v_lshl_add_u32 v22, v12, 3, v22\n"
    "s_mov_b32 s40, 0x05040100\n"
    "v_bfe_u32 v24, s40, v13, 8\n"
    "v_add_u32 v22, v22, v24\n"
    "s_mov_b32 s40, 0x1a191818\n"
    "v_bfe_u32 v24, s40, v13, 8\n"
    "v_cndmask_b32 v22, v22, v24, vcc\n"
    "v_mad_u32_u24 v5, v22, s41, v21\n"
    "v_xor_b32 v22, 1, v12\n"
    "v_and_b32 v22, 1, v22\n"
    "v_lshlrev_b32 v22, 1, v22\n"
    "v_lshl_add_u32 v22, v12, 3, v22\n"
    "s_mov_b32 s40, 0x05040100\n"
    "v_bfe_u32 v24, s40, v13, 8\n"
    "v_add_u32 v22, v22, v24\n"
    "s_mov_b32 s40, 0x1a191918\n"
    "v_bfe_u32 v24, s40, v13, 8\n"
    "v_cndmask_b32 v22, v22, v24, vcc\n"
    "v_mad_u32_u24 v6, v22, s41, v21\n"
    "v_bfrev_b32 v14, v12\n"
    "v_lshrrev_b32 v14, 26, v14\n"
    "s_add_u32 s40, s22, 3\n"
    "s_and_b32 s40, s40, 3\n"
    "s_lshl2_add_u32 s40, s40, s22\n"
    "s_add_u32 s40, s40, s58\n"
    "s_lshl_b32 s40, s40, 2\n"
    "s_add_u32 s40, s40, -450048\n"
    "v_add_u32 v14, s40, v14\n"
    "v_and_b32 v2, 1, v1\n"
    "v_mul_u32_u24 v2, 0xaaaa, v2\n"
    "v_add_u32 v2, 0x4444, v2\n"
    "v_mad_u32_u24 v15, v17, 36, v19\n"
    "v_mad_u32_u24 v16, v18, 36, v20\n"
    "s_add_u32 s41, s22, 2\n"
    "v_lshrrev_b32 v12, 5, v19\n"
    "v_sub_u32 v12, 1, v12\n"
    "v_and_b32 v12, 3, v12\n"
    "v_add_lshl_u32 v12, v12, s41, 12\n"
    "v_and_b32 v12, 0x7000, v12\n"
    "v_add3_u32 v15, v12, v15, s43\n"
    "v_lshrrev_b32 v12, 5, v20\n"
    "v_sub_u32 v12, 1, v12\n"
    "v_and_b32 v12, 3, v12\n"
    "v_add_lshl_u32 v12, v12, s41, 12\n"
    "v_and_b32 v12, 0x7000, v12\n"
    "v_add3_u32 v16, v12, v16, s43\n"
    "v_add_u32 v16, 3456, v16\n"
    "s_lshl3_add_u32 s40, s2, 4096\n"
    "s_lshl_b32 s41, s2, 2\n"
    "v_lshlrev_b32 v11, 3, v1\n"
    "v_add_u32 v11, 40960, v11\n"
    "v_writelane_b32 v11, s40, 8\n"
    "v_writelane_b32 v11, s41, 9\n"
    "s_getpc_b64 s[10:11]\n"
    ".Lxd_en60:\n"
    "s_mov_b32 s40, .Lxd_ph1-.Lxd_en60\n"
    "s_cmp_eq_u32 s22, 1\n"
    "s_cselect_b32 s40, .Lxd_ph0-.Lxd_en60, s40\n"
    "s_cmp_eq_u32 s22, 2\n"
    "s_cselect_b32 s40, .Lxd_pl3-.Lxd_en60, s40\n"
    "s_cmp_eq_u32 s22, 3\n"
    "s_cselect_b32 s40, .Lxd_pl2-.Lxd_en60, s40\n"
    "s_ashr_i32 s41, s40, 31\n"
    "s_add_u32 s10, s10, s40\n"
    "s_addc_u32 s11, s11, s41\n"
    "v_mov_b32 v68, v42\n"
    "v_add_u32 v69, 4096, v42\n"
    "v_add_u32 v42, 98304, v42\n"
    "v_bfe_u32 v70, v1, 4, 1\n"
    "v_lshlrev_b32 v70, 2, v70\n"
    "v_add_u32 v70, 154112, v70\n"
    "s_mov_b32 s36, 0\n"
    "v_mov_b64 v[88:89], 0\n"
    "v_mov_b64 v[90:91], 0\n"
    "s_waitcnt vmcnt(0)\n"
    "s_movk_i32 s43, 15744\n"
    "v_pk_maximum3_f16 v21, v28, v28, s43 op_sel_hi:[1,1,0] neg_lo:[0,1,0] neg_hi:[0,1,0]\n"
    "v_pk_maximum3_f16 v21, v29, v29, v21 neg_lo:[0,1,0] neg_hi:[0,1,0]\n"
    "v_pk_maximum3_f16 v21, v30, v30, v21 neg_lo:[0,1,0] neg_hi:[0,1,0]\n"
    "v_pk_maximum3_f16 v21, v31, v31, v21 neg_lo:[0,1,0] neg_hi:[0,1,0]\n"
    "v_pk_maximum3_f16 v21, v32, v32, v21 neg_lo:[0,1,0] neg_hi:[0,1,0]\n"
    "v_pk_maximum3_f16 v21, v33, v33, v21 neg_lo:[0,1,0] neg_hi:[0,1,0]\n"
    "v_pk_maximum3_f16 v21, v34, v34, v21 neg_lo:[0,1,0] neg_hi:[0,1,0]\n"
    "v_pk_maximum3_f16 v21, v35, v35, v21 neg_lo:[0,1,0] neg_hi:[0,1,0]\n"
    "v_pk_maximum3_f16 v21, v36, v36, v21 neg_lo:[0,1,0] neg_hi:[0,1,0]\n"
    "v_pk_maximum3_f16 v21, v37, v37, v21 neg_lo:[0,1,0] neg_hi:[0,1,0]\n"
    "v_pk_maximum3_f16 v21, v38, v38, v21 neg_lo:[0,1,0] neg_hi:[0,1,0]\n"
    "v_pk_maximum3_f16 v21, v39, v39, v21 neg_lo:[0,1,0] neg_hi:[0,1,0]\n"
    "v_pk_maximum3_f16 v21, v40, v40, v21 neg_lo:[0,1,0] neg_hi:[0,1,0]\n"
    "v_pk_max_u16 v21, v21, v21 op_sel:[0,1] op_sel_hi:[1,0]\n"
    "s_nop 1\n"
    "v_max_u32_dpp v21, v21, v21 quad_perm:[1,0,3,2] row_mask:0xf bank_mask:0xf\n"
    "s_nop 1\n"
    "v_max_u32_dpp v21, v21, v21 quad_perm:[2,3,0,1] row_mask:0xf bank_mask:0xf\n"
    "s_nop 1\n"
    "v_max_u32_dpp v21, v21, v21 row_half_mirror row_mask:0xf bank_mask:0xf\n"
    "s_nop 1\n"
    "v_max_u32_dpp v21, v21, v21 row_mirror row_mask:0xf bank_mask:0xf\n"
    "s_nop 1\n"
    "v_max_u32_dpp v21, v21, v21 row_bcast:15 row_mask:0xa bank_mask:0xf\n"
    "s_nop 1\n"
    "v_max_u32_dpp v21, v21, v21 row_bcast:31 row_mask:0xc bank_mask:0xf\n"
    "s_nop 1\n"
    "v_readlane_b32 s47, v21, 63\n"
    "s_and_b32 s47, s47, 0x7f80\n"
    "s_sub_u32 s50, s47, 2816\n"
    "s_mul_i32 s51, s50, -8\n"
    "s_lshr_b32 s47, s47, 3\n"
    "v_pk_maximum3_f16 v21, v28, v28, s50 op_sel_hi:[1,1,0] neg_lo:[0,1,0] neg_hi:[0,1,0]\n"
    "v_pk_mad_u16 v21, v21, 8, s51 op_sel_hi:[1,0,0]\n"
    "v_bfi_b32 v28, s57, v21, v28\n"
    "v_pk_maximum3_f16 v12, v29, v29, s50 op_sel_hi:[1,1,0] neg_lo:[0,1,0] neg_hi:[0,1,0]\n"
    "v_pk_mad_u16 v12, v12, 8, s51 op_sel_hi:[1,0,0]\n"
    "v_bfi_b32 v29, s57, v12, v29\n"
    "v_pk_maximum3_f16 v21, v30, v30, s50 op_sel_hi:[1,1,0] neg_lo:[0,1,0] neg_hi:[0,1,0]\n"
    "v_pk_mad_u16 v21, v21, 8, s51 op_sel_hi:[1,0,0]\n"
    "v_bfi_b32 v30, s57, v21, v30\n"
    "v_pk_maximum3_f16 v12, v31, v31, s50 op_sel_hi:[1,1,0] neg_lo:[0,1,0] neg_hi:[0,1,0]\n"
    "v_pk_mad_u16 v12, v12, 8, s51 op_sel_hi:[1,0,0]\n"
    "v_bfi_b32 v31, s57, v12, v31\n"
    "v_pk_maximum3_f16 v21, v32, v32, s50 op_sel_hi:[1,1,0] neg_lo:[0,1,0] neg_hi:[0,1,0]\n"
    "v_pk_mad_u16 v21, v21, 8, s51 op_sel_hi:[1,0,0]\n"
    "v_bfi_b32 v32, s57, v21, v32\n"
    "v_pk_maximum3_f16 v12, v33, v33, s50 op_sel_hi:[1,1,0] neg_lo:[0,1,0] neg_hi:[0,1,0]\n"
    "v_pk_mad_u16 v12, v12, 8, s51 op_sel_hi:[1,0,0]\n"
    "v_bfi_b32 v33, s57, v12, v33\n"
    "s_cmp_lg_u32 s20, 7\n"
    "s_cbranch_scc1 .Lxd_on61\n"
    "s_mov_b64 exec, 0x3ff\n"
    "global_load_dwordx2 v[26:27], v11, s[16:17] sc1\n"
    "s_mov_b64 exec, -1\n"
    ".Lxd_on61:\n"
    "v_pk_maximum3_f16 v21, v34, v34, s50 op_sel_hi:[1,1,0] neg_lo:[0,1,0] neg_hi:[0,1,0]\n"
    "v_pk_mad_u16 v21, v21, 8, s51 op_sel_hi:[1,0,0]\n"
    "v_bfi_b32 v34, s57, v21, v34\n"
    "v_pk_maximum3_f16 v12, v35, v35, s50 op_sel_hi:[1,1,0] neg_lo:[0,1,0] neg_hi:[0,1,0]\n"
    "v_pk_mad_u16 v12, v12, 8, s51 op_sel_hi:[1,0,0]\n"
    "v_bfi_b32 v35, s57, v12, v35\n"
    "v_pk_maximum3_f16 v21, v36, v36, s50 op_sel_hi:[1,1,0] neg_lo:[0,1,0] neg_hi:[0,1,0]\n"
    "v_pk_mad_u16 v21, v21, 8, s51 op_sel_hi:[1,0,0]\n"
    "v_bfi_b32 v36, s57, v21, v36\n"
    "v_pk_maximum3_f16 v12, v37, v37, s50 op_sel_hi:[1,1,0] neg_lo:[0,1,0] neg_hi:[0,1,0]\n"
    "v_pk_mad_u16 v12, v12, 8, s51 op_sel_hi:[1,0,0]\n"
    "v_bfi_b32 v37, s57, v12, v37\n"
    "v_pk_maximum3_f16 v21, v38, v38, s50 op_sel_hi:[1,1,0] neg_lo:[0,1,0] neg_hi:[0,1,0]\n"
    "v_pk_mad_u16 v21, v21, 8, s51 op_sel_hi:[1,0,0]\n"
    "v_bfi_b32 v38, s57, v21, v38\n"
    "v_pk_maximum3_f16 v12, v39, v39, s50 op_sel_hi:[1,1,0] neg_lo:[0,1,0] neg_hi:[0,1,0]\n"
    "v_pk_mad_u16 v12, v12, 8, s51 op_sel_hi:[1,0,0]\n"
    "v_bfi_b32 v39, s57, v12, v39\n"
    "v_pk_maximum3_f16 v21, v40, v40, s50 op_sel_hi:[1,1,0] neg_lo:[0,1,0] neg_hi:[0,1,0]\n"
    "v_pk_mad_u16 v21, v21, 8, s51 op_sel_hi:[1,0,0]\n"
    "v_bfi_b32 v40, s57, v21, v40\n"
    "s_sub_u32 s66, 4112, s47\n"
    "s_lshl_b32 s66, s66, 19\n"
    "v_cvt_pkrtz_f16_f32 v12, s66, s66\n"
    "v_lshl_or_b32 v21, v12, 16, s47\n"
    "ds_write_b128 v15, v[28:31] offset:0\n"
    "ds_write_b128 v15, v[32:35] offset:144\n"
    "ds_write_b128 v15, v[36:39] offset:288\n"
    "ds_write2_b32 v16, v40, v12 offset1:72\n"
    "ds_or_b32 v25, v21 offset:532\n"
    "s_cmp_lg_u32 s20, 7\n"
    "s_cbranch_scc1 .Lxd_on63\n"
    "s_waitcnt vmcnt(0)\n"
    "v_readlane_b32 s25, v26, 9\n"
    "v_readlane_b32 s40, v26, 8\n"
    "v_readlane_b32 s41, v27, 8\n"
    "s_mul_i32 s42, s25, 0x9e3779b1\n"
    "s_lshr_b32 s42, s42, 24\n"
    "s_xor_b32 s42, s42, s40\n"
    "s_and_b32 s42, s42, 0xff\n"
    "s_xor_b32 s41, s41, s25\n"
    "s_xor_b32 s41, s41, 0xa5c30f96\n"
    "s_or_b32 s42, s41, s42\n"
    "s_lshr_b32 s41, s40, 8\n"
    "s_mul_i32 s40, s41, 0x9e3779b1\n"
    "s_xor_b32 s41, s41, 0xa5c30f96\n"
    "s_lshr_b32 s40, s40, 24\n"
    "v_xor_b32 v13, s40, v26\n"
    "v_xor_b32 v22, s41, v27\n"
    "v_lshl_or_b32 v13, v13, 24, v22\n"
    "v_cmp_ne_u32 s[66:67], 0, v13\n"
    "s_and_b32 s66, s66, 0xff\n"
    "s_or_b32 s42, s42, s66\n"
    "s_cmp_eq_u32 s20, 7\n"
    "s_cselect_b32 s42, s42, 0\n"
    "s_cmp_lg_u32 s42, 0\n"
    "s_cbranch_scc1 .Lxd_pv62\n"
    ".Lxd_pv62r:\n"
    "s_mov_b64 s[64:65], s[40:41]\n"
    ".Lxd_on63:\n"
    "s_waitcnt lgkmcnt(0)\n"
    "s_barrier\n"
    "s_mul_i32 s42, s20, 5376\n"
    "s_add_u32 s40, s42, 1048576\n"
    "s_add_u32 s38, s16, s40\n"
    "s_addc_u32 s39, s17, 0\n"
    "s_add_u32 m0, s42, 98304\n"
    "s_nop 0\n"
    "global_load_lds_dwordx4 v68, s[38:39] offset:0 sc1\n"
    "global_load_lds_dwordx4 v68, s[38:39] offset:1024 sc1\n"
    "global_load_lds_dwordx4 v68, s[38:39] offset:2048 sc1\n"
    "global_load_lds_dwordx4 v68, s[38:39] offset:3072 sc1\n"
    "s_add_u32 m0, s42, 102400\n"
    "s_nop 0\n"
    "global_load_lds_dwordx4 v69, s[38:39] offset:0 sc1\n"
    "global_load_lds_dwordx4 v69, s[38:39] offset:256 sc1\n"
    "s_cmp_lg_u32 s20, 7\n"
    "s_cbranch_scc1 .Lxd_on64\n"
    "s_mov_b64 s[40:41], s[64:65]\n"
    "v_lshrrev_b32 v22, 8, v26\n"
    "v_mov_b32 v26, s40\n"
    "v_mov_b32 v27, s41\n"
    "v_add_u32_dpp v22, v22, v22 row_shl:4 row_mask:0xf bank_mask:0xf\n"
    "v_add_u32_e64 v43, s25, 1\n"
    "v_lshlrev_b32 v8, 2, v1\n"
    "v_max_u32_dpp v13, v22, v22 quad_perm:[1,0,3,2] row_mask:0xf bank_mask:0xf\n"
    "v_add_u32 v8, 154112, v8\n"
    "s_nop 0\n"
    "v_max_u32_dpp v13, v13, v13 quad_perm:[2,3,0,1] row_mask:0xf bank_mask:0xf\n"
    "v_sub_u32 v22, v22, v13\n"
    "v_subrev_u32 v13, 818, v13\n"
    "v_ldexp_f32 v22, 1.0, v22\n"
    "s_mov_b64 exec, 1\n"
    "ds_write_b64 v8, v[26:27]\n"
    "s_mov_b64 exec, 15\n"
    "ds_write2_b32 v8, v22, v13 offset0:4 offset1:8\n"
    "s_mov_b64 exec, -1\n"
    ".Lxd_on64:\n"
    "s_cmp_lt_u32 s22, 2\n"
    "s_cselect_b32 s40, 16, 0\n"
    "v_add_u32 v25, s40, v25\n"
    "s_add_u32 s23, s23, 4\n"
    "v_mov_b32 v24, s23\n"
    "v_mov_b32 v13, s58\n"
    "s_sub_u32 s40, 3, s22\n"
    "s_and_b32 s40, s40, 3\n"
    "v_lshl_add_u32 v7, s40, 12, v3\n"
    "v_lshl_add_u32 v8, s40, 12, v4\n"
    "v_lshl_add_u32 v9, s40, 12, v5\n"
    "v_lshl_add_u32 v10, s40, 12, v6\n"
    "ds_read_b32 v22, v13 offset:512\n"
    "ds_read_b64_tr_b16 v[44:45], v9 offset:0\n"
    "ds_read_b64_tr_b16 v[46:47], v10 offset:0\n"
    "ds_read_b64_tr_b16 v[48:49], v7 offset:0\n"
    "ds_read_b64_tr_b16 v[50:51], v8 offset:576\n"
    "ds_read_b64_tr_b16 v[52:53], v8 offset:1152\n"
    "ds_read_b64_tr_b16 v[54:55], v8 offset:1728\n"
    "ds_read_b64_tr_b16 v[56:57], v8 offset:2304\n"
    "ds_read_b64_tr_b16 v[58:59], v8 offset:2880\n"
    "s_cmp_lg_u32 s22, 0\n"
    "s_cbranch_scc1 .Lxd_on65\n"
    "v_readfirstlane_b32 s40, v24\n"
    "s_or_b32 s40, s40, s44\n"
    "s_lshl1_add_u32 s41, s40, s54\n"
    "s_cmpk_lt_u32 s40, 256\n"
    "s_cselect_b32 s41, s41, 2\n"
    "v_mov_b32 v12, s41\n"
    "s_mul_i32 s41, s41, 52\n"
    "s_add_u32 s38, s4, s41\n"
    "s_addc_u32 s39, s5, 0\n"
    "ds_write_b32 v25, v12 offset:20\n"
    "global_load_dwordx3 v[36:38], v17, s[38:39] offset:-104\n"
    "global_load_dword v41, v18, s[38:39] offset:-8\n"
    ".Lxd_on65:\n"
    "s_waitcnt vmcnt(0) lgkmcnt(0)\n"
    "s_barrier\n"
    "ds_read2_b32 v[26:27], v70 offset1:1\n"
    "ds_read_b32 v0, v70 offset:16\n"
    "ds_read_b32 v23, v70 offset:24\n"
    "s_cmp_lg_u32 s20, 7\n"
    "s_cbranch_scc1 .Lxd_on66\n"
    "s_mov_b64 exec, 0x200\n"
    "global_store_dword v11, v43, s[16:17] sc1\n"
    "s_mov_b64 exec, -1\n"
    ".Lxd_on66:\n"
    "ds_read_b128 v[92:95], v42 offset:0\n"
    "ds_read_b128 v[96:99], v42 offset:1024\n"
    "ds_read_b128 v[100:103], v42 offset:2048\n"
    "ds_read_b128 v[104:107], v42 offset:3072\n"
    "ds_read_b128 v[108:111], v42 offset:4096\n"
    "ds_read_b128 v[112:115], v42 offset:5120\n"
    "ds_read_b128 v[116:119], v42 offset:6144\n"
    "ds_read_b128 v[120:123], v42 offset:7168\n"
    "ds_read_b128 v[124:127], v42 offset:8192\n"
    "ds_read_b128 v[128:131], v42 offset:9216\n"
    "ds_read_b128 v[132:135], v42 offset:10240\n"
    "ds_read_b128 v[136:139], v42 offset:11264\n"
    "ds_read_b128 v[140:143], v42 offset:12288\n"
    "ds_read_b128 v[144:147], v42 offset:13312\n"
    "s_cmp_ge_u32 s2, 8\n"
    "s_cbranch_scc1 .Lxd_h4\n"
    "s_lshl3_add_u32 s40, s2, s20\n"
    "s_mul_i32 s40, s40, 52\n"
    "s_add_u32 s40, s40, 39936\n"
    "v_min_u32 v68, 51, v1\n"
    "s_mov_b32 m0, 159744\n"
    "v_add_lshl_u32 v68, v68, s40, 7\n"
    "global_load_lds_dword v68, s[6:7]\n"
    ".Lxd_h4:\n"
    "ds_read_b128 v[148:151], v42 offset:14336\n"
    "ds_read_b128 v[152:155], v42 offset:15360\n"
    "ds_read_b128 v[156:159], v42 offset:16384\n"
    "ds_read_b128 v[160:163], v42 offset:17408\n"
    "ds_read_b128 v[164:167], v42 offset:18432\n"
    "ds_read_b128 v[168:171], v42 offset:19456\n"
    "ds_read_b128 v[172:175], v42 offset:20480\n"
    "ds_read_b128 v[176:179], v42 offset:21504\n"
    "ds_read_b128 v[180:183], v42 offset:22528\n"
    "ds_read_b128 v[184:187], v42 offset:23552\n"
    "ds_read_b128 v[188:191], v42 offset:24576\n"
    "ds_read_b128 v[192:195], v42 offset:25600\n"
    "ds_read_b128 v[196:199], v42 offset:26624\n"
    "ds_read_b128 v[200:203], v42 offset:27648\n"
    "ds_read_b128 v[204:207], v42 offset:28672\n"
    "ds_read_b128 v[208:211], v42 offset:29696\n"
    "ds_read_b128 v[212:215], v42 offset:30720\n"
    "ds_read_b128 v[216:219], v42 offset:31744\n"
    "ds_read_b128 v[220:223], v42 offset:32768\n"
    "ds_read_b128 v[224:227], v42 offset:33792\n"
    "ds_read_b128 v[228:231], v42 offset:34816\n"
    "ds_read_b128 v[232:235], v42 offset:35840\n"
    "ds_read_b128 v[236:239], v42 offset:36864\n"
    "ds_read_b128 v[240:243], v42 offset:37888\n"
    "ds_read_b128 v[244:247], v42 offset:38912\n"
    "ds_read_b128 v[248:251], v42 offset:39936\n"
    "ds_read_b128 v[252:255], v42 offset:40960\n"
    "s_waitcnt lgkmcnt(15)\n"
    "v_readfirstlane_b32 s45, v22\n"
    "v_readfirstlane_b32 s28, v26\n"
    "v_readfirstlane_b32 s29, v27\n"
    "s_cmp_lt_u32 s2, 8\n"
    "s_cbranch_scc1 .Lxd_hp68\n"
    "s_mov_b32 m0, 159744\n"
    "s_mov_b32 s46, 3\n"
    "v_lshlrev_b32 v26, 7, v62\n"
    "v_lshlrev_b32 v27, 7, v63\n"
    "v_lshlrev_b32 v42, 7, v64\n"
    ".Lxd_hp68:\n"
    "v_pk_mul_f16 v64, v44, v46\n"
    "v_pk_mul_f16 v65, v45, v47\n"
    "v_pk_mul_f16 v66, v48, v48 op_sel_hi:[0,1]\n"
    "v_pk_mul_f16 v67, v48, v49 op_sel_hi:[0,1]\n"
    "v_pk_mul_f16 v60, v44, s45 op_sel:[0,1]\n"
    "v_pk_mul_f16 v61, v45, s45 op_sel:[0,1]\n"
    "v_pk_mul_f16 v62, v46, s45 op_sel:[0,1]\n"
    "v_pk_mul_f16 v63, v47, s45 op_sel:[0,1]\n"
    "s_waitcnt lgkmcnt(0)\n"
    "s_branch .Lxd_enter\n"
    ".Lxd_slow:\n"
    "s_cmp_eq_u32 s45, -1\n"
    "s_cbranch_scc1 .Lxd_sl7\n"
    "s_mov_b32 s59, 0x100000\n"
    "s_and_b32 s40, s45, 0xff0\n"
    "s_cbranch_scc0 .Lxd_sl1\n"
    ".Lxd_sl2:\n"
    "s_cmp_ge_i32 s45, -16\n"
    "s_cbranch_scc1 .Lxd_sl3\n"
    "s_bitset0_b32 s45, 31\n"
    ".Lxd_sl6:\n"
    "s_cmp_lg_u32 s56, 0\n"
    "s_cbranch_scc1 .Lxd_sl9\n"
    "v_readfirstlane_b32 s40, v13\n"
    "s_lshl4_add_u32 s41, s22, s58\n"
    "s_mov_b32 s56, 2\n"
    "s_cmp_lt_u32 s40, s41\n"
    "s_cbranch_scc0 .Lxd_gran\n"
    ".Lxd_sl9:\n"
    "s_setpc_b64 s[60:61]\n"
    ".Lxd_sl1:\n"
    "s_sleep 1\n"
    "ds_read_b32 v22, v13 offset:512\n"
    "s_waitcnt lgkmcnt(0)\n"
    "v_readfirstlane_b32 s45, v22\n"
    "s_sub_u32 s59, s59, 1\n"
    "s_cbranch_scc1 .Lxd_sl5\n"
    "s_and_b32 s40, s45, 0xff0\n"
    "s_cbranch_scc0 .Lxd_sl1\n"
    "s_cmp_gt_i32 s45, 0\n"
    "s_cbranch_scc0 .Lxd_sl2\n"
    "s_setpc_b64 s[60:61]\n"
    ".Lxd_sl3:\n"
    "v_and_b32 v13, 0x3ffff, v13\n"
    "v_lshl_add_u32 v13, v1, 2, v13\n"
    "s_mov_b64 exec, 7\n"
    ".Lxd_sl4:\n"
    "ds_read_b32 v22, v13 offset:4\n"
    "s_waitcnt lgkmcnt(0)\n"
    "v_cmp_eq_u32 s[40:41], 0, v22\n"
    "v_cmp_ne_u32 s[42:43], -1, v22\n"
    "s_sub_u32 s59, s59, 1\n"
    "s_cbranch_scc1 .Lxd_sl5\n"
    "s_cmp_lg_u32 s40, 0\n"
    "s_cbranch_scc1 .Lxd_sl4\n"
    "s_cmp_eq_u32 s42, 0\n"
    "s_cbranch_scc1 .Lxd_sl5\n"
    "s_mov_b64 exec, -1\n"
    "s_mov_b32 s45, 1\n"
    "s_mov_b32 s56, 0\n"
    "s_setpc_b64 s[60:61]\n"
    ".Lxd_sl5:\n"
    "s_mov_b64 exec, -1\n"
    "s_mov_b32 s45, -1\n"
    ".Lxd_sl7:\n"
    "s_cmp_lg_u32 s56, 0\n"
    "s_cbranch_scc1 .Lxd_sl9\n"
    "s_mov_b64 s[62:63], s[60:61]\n"
    "s_call_b64 s[60:61], .Lxd_sl6\n"
    "s_or_b32 s56, s56, 2\n"
    "s_setpc_b64 s[62:63]\n"
    ".Lxd_gran:\n"
    "s_mov_b32 s46, 0\n"
    "s_lshl4_add_u32 s40, s22, s58\n"
    "v_lshrrev_b32 v13, 4, v1\n"
    "v_lshl_add_u32 v13, v13, 2, s40\n"
    "s_waitcnt vmcnt(0)\n"
    "ds_read2_b32 v[26:27], v13 offset1:16\n"
    "v_and_b32 v13, 15, v1\n"
    "v_lshlrev_b32 v13, 15, v13\n"
    "s_mov_b32 s42, 0x1fff1fff\n"
    "s_mov_b32 s43, s42\n"
    "s_add_u32 s40, s16, 65528\n"
    "s_addc_u32 s41, s17, 0\n"
    "v_mov_b32 v42, 0\n"
    "s_mov_b32 s56, 2\n"
    "s_waitcnt lgkmcnt(0)\n"
    "v_cmp_lt_i32 s[30:31], 0, v26\n"
    "v_cmp_lt_i32 s[64:65], 0, v27\n"
    "v_lshl_add_u32 v43, v26, 2, v13\n"
    "v_lshl_add_u32 v27, v27, 2, v13\n"
    "v_mov_b32 v26, 0\n"
    "s_and_b64 s[30:31], s[30:31], s[42:43]\n"
    "s_cbranch_scc0 .Lxd_gr30\n"
    "s_mov_b64 exec, s[30:31]\n"
    "global_load_dwordx2 v[42:43], v43, s[40:41] sc1\n"
    "s_mov_b64 exec, -1\n"
    "s_mov_b32 s56, 1\n"
    ".Lxd_gr30:\n"
    "s_and_b64 s[64:65], s[64:65], s[42:43]\n"
    "s_cbranch_scc0 .Lxd_gr64\n"
    "s_mov_b64 exec, s[64:65]\n"
    "global_load_dwordx2 v[26:27], v27, s[40:41] sc1\n"
    "s_mov_b64 exec, -1\n"
    "s_mov_b32 s56, 1\n"
    ".Lxd_gr64:\n"
    "s_setpc_b64 s[60:61]\n"
    ".Lxd_dsum:\n"
    "s_lshl4_add_u32 s42, s22, s58\n"
    "s_waitcnt vmcnt(0)\n"
    "s_cmp_eq_u64 s[30:31], 0\n"
    "s_cbranch_scc1 .Lxd_ds30\n"
    "v_xor_b32 v13, s28, v42\n"
    "v_xor_b32 v22, s29, v43\n"
    "v_lshl_or_b32 v13, v13, 24, v22\n"
    "v_and_b32 v22, 0xffffff00, v42\n"
    "v_cmp_ne_u32 s[40:41], 0, v13\n"
    "v_lshrrev_b32 v13, 4, v1\n"
    "v_add_f32_dpp v22, v22, v22 quad_perm:[1,0,3,2] row_mask:0xf bank_mask:0xf\n"
    "s_and_b64 s[40:41], s[40:41], s[30:31]\n"
    "s_cbranch_scc1 .Lxd_ds9\n"
    "v_add_f32_dpp v22, v22, v22 quad_perm:[2,3,0,1] row_mask:0xf bank_mask:0xf\n"
    "v_lshl_add_u32 v13, v13, 2, s42\n"
    "s_nop 0\n"
    "v_add_f32_dpp v22, v22, v22 row_half_mirror row_mask:0xf bank_mask:0xf\n"
    "s_nop 1\n"
    "v_add_f32_dpp v22, v22, v22 row_mirror row_mask:0xf bank_mask:0xf\n"
    "ds_write_b32 v13, v22 offset:1024\n"
    ".Lxd_ds30:\n"
    "s_cmp_eq_u64 s[64:65], 0\n"
    "s_cbranch_scc1 .Lxd_ds64\n"
    "v_xor_b32 v13, s28, v26\n"
    "v_xor_b32 v22, s29, v27\n"
    "v_lshl_or_b32 v13, v13, 24, v22\n"
    "v_and_b32 v22, 0xffffff00, v26\n"
    "v_cmp_ne_u32 s[40:41], 0, v13\n"
    "v_lshrrev_b32 v13, 4, v1\n"
    "v_add_f32_dpp v22, v22, v22 quad_perm:[1,0,3,2] row_mask:0xf bank_mask:0xf\n"
    "s_and_b64 s[40:41], s[40:41], s[64:65]\n"
    "s_cbranch_scc1 .Lxd_ds9\n"
    "v_add_f32_dpp v22, v22, v22 quad_perm:[2,3,0,1] row_mask:0xf bank_mask:0xf\n"
    "v_lshl_add_u32 v13, v13, 2, s42\n"
    "s_nop 0\n"
    "v_add_f32_dpp v22, v22, v22 row_half_mirror row_mask:0xf bank_mask:0xf\n"
    "s_nop 1\n"
    "v_add_f32_dpp v22, v22, v22 row_mirror row_mask:0xf bank_mask:0xf\n"
    "ds_write_b32 v13, v22 offset:1088\n"
    ".Lxd_ds64:\n"
    "s_mov_b32 s56, 2\n"
    ".Lxd_ds9:\n"
    "s_setpc_b64 s[60:61]\n"
    ".Lxd_tf17:\n"
    "v_mov_b32 v12, -1\n"
    "v_and_b32 v13, 0x3ffff, v25\n"
    "s_add_u32 s40, s58, 248\n"
    "v_lshl_add_u32 v13, v1, 2, v13\n"
    "v_mov_b32 v24, -1\n"
    "v_cmp_gt_u32 s[42:43], s40, v13\n"
    "s_mov_b32 s62, 0x88888888\n"
    "s_mov_b32 s63, s62\n"
    "s_and_b64 exec, s[42:43], s[62:63]\n"
    "ds_write_b32 v13, v24 offset:8\n"
    "s_mov_b64 exec, s[42:43]\n"
    "v_cndmask_b32 v24, 0, -16, s[62:63]\n"
    "s_brev_b32 s40, 1\n"
    "v_writelane_b32 v24, s40, 2\n"
    "s_lshl_b32 s40, 1, s22\n"
    "v_or_b32 v24, s40, v24\n"
    "s_mov_b32 s36, 1\n"
    "ds_or_b32 v13, v24 offset:520\n"
    "s_mov_b64 exec, -1\n"
    "s_branch .Lxd_tf17r\n"
    ".Lxd_ht18:\n"
    "v_cmp_ne_u32 s[40:41], -1, v27\n"
    "s_mov_b32 s46, 0\n"
    "s_cmp_eq_u64 s[40:41], 0\n"
    "s_cbranch_scc1 .Lxd_ht18r\n"
    "v_add_f32 v26, v26, v27\n"
    "v_lshlrev_b32 v42, 3, v1\n"
    "v_add_u32 v26, 0x80, v26\n"
    "v_mov_b32 v27, s29\n"
    "v_and_b32 v26, 0xffffff00, v26\n"
    "s_mov_b64 exec, s[40:41]\n"
    "v_or_b32 v26, s28, v26\n"
    "global_store_dwordx2 v42, v[26:27], s[48:49] sc1\n"
    "s_mov_b64 exec, -1\n"
    "s_branch .Lxd_ht18r\n"
    ".Lxd_ck19:\n"
    "s_mov_b32 s37, 0\n"
    "s_cmp_eq_u32 s45, -1\n"
    "s_cselect_b32 s40, s56, 0\n"
    "s_cmp_eq_u32 s40, 2\n"
    "s_cbranch_scc1 .Lxd_last0\n"
    "s_cmp_eq_u32 s40, 1\n"
    "s_cbranch_scc1 .Lxd_last1\n"
    "v_readfirstlane_b32 s41, v25\n"
    "s_and_b32 s40, s45, 0xff0\n"
    "s_cselect_b32 s40, s45, 0\n"
    "s_cmp_lt_i32 s40, -16\n"
    "s_cselect_b32 s40, s56, 1\n"
    "s_cmp_lg_u32 s40, 0\n"
    "s_cbranch_scc1 .Lxd_ck19g\n"
    "s_bitset0_b32 s45, 31\n"
    "s_lshl4_add_u32 s40, s22, s58\n"
    "s_add_u32 s41, s41, 4\n"
    "s_mov_b32 s56, 2\n"
    "s_cmp_lt_u32 s41, s40\n"
    "s_cbranch_scc1 .Lxd_ck19r\n"
    "s_call_b64 s[60:61], .Lxd_gran\n"
    "s_branch .Lxd_ck19r\n"
    ".Lxd_ck19g:\n"
    "v_add_u32 v13, 4, v25\n"
    "s_call_b64 s[60:61], .Lxd_slow\n"
    "s_cmp_eq_u32 s45, -1\n"
    "s_cbranch_scc0 .Lxd_ck19r\n"
    "s_cmp_eq_u32 s56, 1\n"
    "s_cbranch_scc1 .Lxd_last1\n"
    "s_branch .Lxd_last0\n"
    ".Lxd_ht22:\n"
    "s_cmp_lt_i32 s46, 0\n"
    "s_cbranch_scc1 .Lxd_ht22s\n"
    "global_load_lds_dword v26, s[8:9]\n"
    "v_mov_b32 v26, v27\n"
    "v_mov_b32 v27, v42\n"
    "s_sub_u32 s46, s46, 1\n"
    "s_branch .Lxd_ht22r\n"
    ".Lxd_ht22s:\n"
    "v_cmp_ne_u32 s[40:41], -1, v27\n"
    "s_mov_b32 s46, 0\n"
    "s_cmp_eq_u64 s[40:41], 0\n"
    "s_cbranch_scc1 .Lxd_ht22r\n"
    "v_add_f32 v26, v26, v27\n"
    "v_lshlrev_b32 v42, 3, v1\n"
    "v_add_u32 v26, 0x80, v26\n"
    "v_mov_b32 v27, s29\n"
    "v_and_b32 v26, 0xffffff00, v26\n"
    "s_mov_b64 exec, s[40:41]\n"
    "v_or_b32 v26, s28, v26\n"
    "global_store_dwordx2 v42, v[26:27], s[48:49] sc1\n"
    "s_mov_b64 exec, -1\n"
    "s_branch .Lxd_ht22r\n"
    ".Lxd_ck23:\n"
    "s_mov_b32 s37, 1\n"
    "s_cmp_eq_u32 s45, -1\n"
    "s_cselect_b32 s40, s56, 0\n"
    "s_cmp_eq_u32 s40, 2\n"
    "s_cbranch_scc1 .Lxd_last0\n"
    "s_cmp_eq_u32 s40, 1\n"
    "s_cbranch_scc1 .Lxd_last1\n"
    "v_readfirstlane_b32 s41, v25\n"
    "s_and_b32 s40, s45, 0xff0\n"
    "s_cselect_b32 s40, s45, 0\n"
    "s_cmp_lt_i32 s40, -16\n"
    "s_cselect_b32 s40, s56, 1\n"
    "s_cmp_lg_u32 s40, 0\n"
    "s_cbranch_scc1 .Lxd_ck23g\n"
    "s_bitset0_b32 s45, 31\n"
    "s_lshl4_add_u32 s40, s22, s58\n"
    "s_add_u32 s41, s41, 8\n"
    "s_mov_b32 s56, 2\n"
    "s_cmp_lt_u32 s41, s40\n"
    "s_cbranch_scc1 .Lxd_ck23r\n"
    "s_call_b64 s[60:61], .Lxd_gran\n"
    "s_branch .Lxd_ck23r\n"
    ".Lxd_ck23g:\n"
    "v_add_u32 v13, 8, v25\n"
    "s_call_b64 s[60:61], .Lxd_slow\n"
    "s_cmp_eq_u32 s45, -1\n"
    "s_cbranch_scc0 .Lxd_ck23r\n"
    "s_cmp_eq_u32 s56, 1\n"
    "s_cbranch_scc1 .Lxd_last1\n"
    "s_branch .Lxd_last0\n"
    ".Lxd_ht26:\n"
    "s_cmp_lt_i32 s46, 0\n"
    "s_cbranch_scc1 .Lxd_ht26s\n"
    "global_load_lds_dword v26, s[8:9]\n"
    "v_mov_b32 v26, v27\n"
    "v_mov_b32 v27, v42\n"
    "s_sub_u32 s46, s46, 1\n"
    "s_branch .Lxd_ht26r\n"
    ".Lxd_ht26s:\n"
    "v_cmp_ne_u32 s[40:41], -1, v27\n"
    "s_mov_b32 s46, 0\n"
    "s_cmp_eq_u64 s[40:41], 0\n"
    "s_cbranch_scc1 .Lxd_ht26r\n"
    "v_add_f32 v26, v26, v27\n"
    "v_lshlrev_b32 v42, 3, v1\n"
    "v_add_u32 v26, 0x80, v26\n"
    "v_mov_b32 v27, s29\n"
    "v_and_b32 v26, 0xffffff00, v26\n"
    "s_mov_b64 exec, s[40:41]\n"
    "v_or_b32 v26, s28, v26\n"
    "global_store_dwordx2 v42, v[26:27], s[48:49] sc1\n"
    "s_mov_b64 exec, -1\n"
    "s_branch .Lxd_ht26r\n"
    ".Lxd_ck27:\n"
    "s_mov_b32 s37, 2\n"
    "s_cmp_eq_u32 s45, -1\n"
    "s_cselect_b32 s40, s56, 0\n"
    "s_cmp_eq_u32 s40, 2\n"
    "s_cbranch_scc1 .Lxd_last0\n"
    "s_cmp_eq_u32 s40, 1\n"
    "s_cbranch_scc1 .Lxd_last1\n"
    "v_readfirstlane_b32 s41, v25\n"
    "s_and_b32 s40, s45, 0xff0\n"
    "s_cselect_b32 s40, s45, 0\n"
    "s_cmp_lt_i32 s40, -16\n"
    "s_cselect_b32 s40, s56, 1\n"
    "s_cmp_lg_u32 s40, 0\n"
    "s_cbranch_scc1 .Lxd_ck27g\n"
    "s_bitset0_b32 s45, 31\n"
    "s_lshl4_add_u32 s40, s22, s58\n"
    "s_add_u32 s41, s41, 12\n"
    "s_mov_b32 s56, 2\n"
    "s_cmp_lt_u32 s41, s40\n"
    "s_cbranch_scc1 .Lxd_ck27r\n"
    "s_call_b64 s[60:61], .Lxd_gran\n"
    "s_branch .Lxd_ck27r\n"
    ".Lxd_ck27g:\n"
    "v_add_u32 v13, 12, v25\n"
    "s_call_b64 s[60:61], .Lxd_slow\n"
    "s_cmp_eq_u32 s45, -1\n"
    "s_cbranch_scc0 .Lxd_ck27r\n"
    "s_cmp_eq_u32 s56, 1\n"
    "s_cbranch_scc1 .Lxd_last1\n"
    "s_branch .Lxd_last0\n"
    ".Lxd_dr30:\n"
    "v_and_b32 v13, 0x3ffff, v25\n"
    "s_add_u32 s40, s58, 232\n"
    "v_lshl_add_u32 v13, v1, 2, v13\n"
    "v_mov_b32 v24, -1\n"
    "v_cmp_gt_u32 s[42:43], s40, v13\n"
    "s_mov_b32 s62, 0x88888888\n"
    "s_mov_b32 s63, s62\n"
    "s_and_b64 exec, s[42:43], s[62:63]\n"
    "ds_write_b32 v13, v24 offset:24\n"
    "s_mov_b64 exec, s[42:43]\n"
    "v_cndmask_b32 v24, 0, -16, s[62:63]\n"
    "s_brev_b32 s40, 1\n"
    "v_writelane_b32 v24, s40, 2\n"
    "s_lshl_b32 s40, 1, s22\n"
    "v_or_b32 v24, s40, v24\n"
    "s_mov_b32 s36, 1\n"
    "ds_or_b32 v13, v24 offset:536\n"
    "s_mov_b64 exec, -1\n"
    "s_branch .Lxd_dr30r\n"
    ".Lxd_ck31:\n"
    "s_mov_b32 s37, 3\n"
    "s_cmp_eq_u32 s45, -1\n"
    "s_cselect_b32 s40, s56, 0\n"
    "s_cmp_eq_u32 s40, 2\n"
    "s_cbranch_scc1 .Lxd_last0\n"
    "s_cmp_eq_u32 s40, 1\n"
    "s_cbranch_scc1 .Lxd_last1\n"
    "v_readfirstlane_b32 s41, v25\n"
    "s_and_b32 s40, s45, 0xff0\n"
    "s_cselect_b32 s40, s45, 0\n"
    "s_cmp_lt_i32 s40, -16\n"
    "s_cselect_b32 s40, s56, 1\n"
    "s_cmp_lg_u32 s40, 0\n"
    "s_cbranch_scc1 .Lxd_ck31g\n"
    "s_bitset0_b32 s45, 31\n"
    "s_lshl4_add_u32 s40, s22, s58\n"
    "s_add_u32 s41, s41, 16\n"
    "s_mov_b32 s56, 2\n"
    "s_cmp_lt_u32 s41, s40\n"
    "s_cbranch_scc1 .Lxd_ck31r\n"
    "s_call_b64 s[60:61], .Lxd_gran\n"
    "s_branch .Lxd_ck31r\n"
    ".Lxd_ck31g:\n"
    "v_add_u32 v13, 16, v25\n"
    "s_call_b64 s[60:61], .Lxd_slow\n"
    "s_cmp_eq_u32 s45, -1\n"
    "s_cbranch_scc0 .Lxd_ck31r\n"
    "s_cmp_eq_u32 s56, 1\n"
    "s_cbranch_scc1 .Lxd_last1\n"
    "s_branch .Lxd_last0\n"
    ".Lxd_ck34:\n"
    "s_mov_b32 s37, 0\n"
    "s_cmp_eq_u32 s45, -1\n"
    "s_cselect_b32 s40, s56, 0\n"
    "s_cmp_eq_u32 s40, 2\n"
    "s_cbranch_scc1 .Lxd_last0\n"
    "s_cmp_eq_u32 s40, 1\n"
    "s_cbranch_scc1 .Lxd_last1\n"
    "v_readfirstlane_b32 s41, v25\n"
    "s_and_b32 s40, s45, 0xff0\n"
    "s_cselect_b32 s40, s45, 0\n"
    "s_cmp_lt_i32 s40, -16\n"
    "s_cselect_b32 s40, s56, 1\n"
    "s_cmp_lg_u32 s40, 0\n"
    "s_cbranch_scc1 .Lxd_ck34g\n"
    "s_bitset0_b32 s45, 31\n"
    "s_lshl4_add_u32 s40, s22, s58\n"
    "s_add_u32 s41, s41, 4\n"
    "s_mov_b32 s56, 2\n"
    "s_cmp_lt_u32 s41, s40\n"
    "s_cbranch_scc1 .Lxd_ck34r\n"
    "s_call_b64 s[60:61], .Lxd_gran\n"
    "s_branch .Lxd_ck34r\n"
    ".Lxd_ck34g:\n"
    "v_add_u32 v13, 4, v25\n"
    "s_call_b64 s[60:61], .Lxd_slow\n"
    "s_cmp_eq_u32 s45, -1\n"
    "s_cbranch_scc0 .Lxd_ck34r\n"
    "s_cmp_eq_u32 s56, 1\n"
    "s_cbranch_scc1 .Lxd_last1\n"
    "s_branch .Lxd_last0\n"
    ".Lxd_ht37:\n"
    "s_cmp_lt_i32 s46, 0\n"
    "s_cbranch_scc1 .Lxd_ht37s\n"
    "global_load_lds_dword v26, s[8:9]\n"
    "v_mov_b32 v26, v27\n"
    "v_mov_b32 v27, v42\n"
    "s_sub_u32 s46, s46, 1\n"
    "s_branch .Lxd_ht37r\n"
    ".Lxd_ht37s:\n"
    "v_cmp_ne_u32 s[40:41], -1, v27\n"
    "s_mov_b32 s46, 0\n"
    "s_cmp_eq_u64 s[40:41], 0\n"
    "s_cbranch_scc1 .Lxd_ht37r\n"
    "v_add_f32 v26, v26, v27\n"
    "v_lshlrev_b32 v42, 3, v1\n"
    "v_add_u32 v26, 0x80, v26\n"
    "v_mov_b32 v27, s29\n"
    "v_and_b32 v26, 0xffffff00, v26\n"
    "s_mov_b64 exec, s[40:41]\n"
    "v_or_b32 v26, s28, v26\n"
    "global_store_dwordx2 v42, v[26:27], s[48:49] sc1\n"
    "s_mov_b64 exec, -1\n"
    "s_branch .Lxd_ht37r\n"
    ".Lxd_ck38:\n"
    "s_mov_b32 s37, 1\n"
    "s_cmp_eq_u32 s45, -1\n"
    "s_cselect_b32 s40, s56, 0\n"
    "s_cmp_eq_u32 s40, 2\n"
    "s_cbranch_scc1 .Lxd_last0\n"
    "s_cmp_eq_u32 s40, 1\n"
    "s_cbranch_scc1 .Lxd_last1\n"
    "v_readfirstlane_b32 s41, v25\n"
    "s_and_b32 s40, s45, 0xff0\n"
    "s_cselect_b32 s40, s45, 0\n"
    "s_cmp_lt_i32 s40, -16\n"
    "s_cselect_b32 s40, s56, 1\n"
    "s_cmp_lg_u32 s40, 0\n"
    "s_cbranch_scc1 .Lxd_ck38g\n"
    "s_bitset0_b32 s45, 31\n"
    "s_lshl4_add_u32 s40, s22, s58\n"
    "s_add_u32 s41, s41, 8\n"
    "s_mov_b32 s56, 2\n"
    "s_cmp_lt_u32 s41, s40\n"
    "s_cbranch_scc1 .Lxd_ck38r\n"
    "s_call_b64 s[60:61], .Lxd_gran\n"
    "s_branch .Lxd_ck38r\n"
    ".Lxd_ck38g:\n"
    "v_add_u32 v13, 8, v25\n"
    "s_call_b64 s[60:61], .Lxd_slow\n"
    "s_cmp_eq_u32 s45, -1\n"
    "s_cbranch_scc0 .Lxd_ck38r\n"
    "s_cmp_eq_u32 s56, 1\n"
    "s_cbranch_scc1 .Lxd_last1\n"
    "s_branch .Lxd_last0\n"
    ".Lxd_ht41:\n"
    "s_cmp_lt_i32 s46, 0\n"
    "s_cbranch_scc1 .Lxd_ht41s\n"
    "global_load_lds_dword v26, s[8:9]\n"
    "v_mov_b32 v26, v27\n"
    "v_mov_b32 v27, v42\n"
    "s_sub_u32 s46, s46, 1\n"
    "s_branch .Lxd_ht41r\n"
    ".Lxd_ht41s:\n"
    "v_cmp_ne_u32 s[40:41], -1, v27\n"
    "s_mov_b32 s46, 0\n"
    "s_cmp_eq_u64 s[40:41], 0\n"
    "s_cbranch_scc1 .Lxd_ht41r\n"
    "v_add_f32 v26, v26, v27\n"
    "v_lshlrev_b32 v42, 3, v1\n"
    "v_add_u32 v26, 0x80, v26\n"
    "v_mov_b32 v27, s29\n"
    "v_and_b32 v26, 0xffffff00, v26\n"
    "s_mov_b64 exec, s[40:41]\n"
    "v_or_b32 v26, s28, v26\n"
    "global_store_dwordx2 v42, v[26:27], s[48:49] sc1\n"
    "s_mov_b64 exec, -1\n"
    "s_branch .Lxd_ht41r\n"
    ".Lxd_ck42:\n"
    "s_mov_b32 s37, 2\n"
    "s_cmp_eq_u32 s45, -1\n"
    "s_cselect_b32 s40, s56, 0\n"
    "s_cmp_eq_u32 s40, 2\n"
    "s_cbranch_scc1 .Lxd_last0\n"
    "s_cmp_eq_u32 s40, 1\n"
    "s_cbranch_scc1 .Lxd_last1\n"
    "v_readfirstlane_b32 s41, v25\n"
    "s_and_b32 s40, s45, 0xff0\n"
    "s_cselect_b32 s40, s45, 0\n"
    "s_cmp_lt_i32 s40, -16\n"
    "s_cselect_b32 s40, s56, 1\n"
    "s_cmp_lg_u32 s40, 0\n"
    "s_cbranch_scc1 .Lxd_ck42g\n"
    "s_bitset0_b32 s45, 31\n"
    "s_lshl4_add_u32 s40, s22, s58\n"
    "s_add_u32 s41, s41, 12\n"
    "s_mov_b32 s56, 2\n"
    "s_cmp_lt_u32 s41, s40\n"
    "s_cbranch_scc1 .Lxd_ck42r\n"
    "s_call_b64 s[60:61], .Lxd_gran\n"
    "s_branch .Lxd_ck42r\n"
    ".Lxd_ck42g:\n"
    "v_add_u32 v13, 12, v25\n"
    "s_call_b64 s[60:61], .Lxd_slow\n"
    "s_cmp_eq_u32 s45, -1\n"
    "s_cbranch_scc0 .Lxd_ck42r\n"
    "s_cmp_eq_u32 s56, 1\n"
    "s_cbranch_scc1 .Lxd_last1\n"
    "s_branch .Lxd_last0\n"
    ".Lxd_ht45:\n"
    "v_cmp_ne_u32 s[40:41], -1, v27\n"
    "s_mov_b32 s46, 0\n"
    "s_cmp_eq_u64 s[40:41], 0\n"
    "s_cbranch_scc1 .Lxd_ht45r\n"
    "v_add_f32 v26, v26, v27\n"
    "v_lshlrev_b32 v42, 3, v1\n"
    "v_add_u32 v26, 0x80, v26\n"
    "v_mov_b32 v27, s29\n"
    "v_and_b32 v26, 0xffffff00, v26\n"
    "s_mov_b64 exec, s[40:41]\n"
    "v_or_b32 v26, s28, v26\n"
    "global_store_dwordx2 v42, v[26:27], s[48:49] sc1\n"
    "s_mov_b64 exec, -1\n"
    "s_branch .Lxd_ht45r\n"
    ".Lxd_dr47:\n"
    "v_and_b32 v13, 0x3ffff, v25\n"
    "s_add_u32 s40, s58, 232\n"
    "v_lshl_add_u32 v13, v1, 2, v13\n"
    "v_mov_b32 v24, -1\n"
    "v_cmp_gt_u32 s[42:43], s40, v13\n"
    "s_mov_b32 s62, 0x88888888\n"
    "s_mov_b32 s63, s62\n"
    "s_and_b64 exec, s[42:43], s[62:63]\n"
    "ds_write_b32 v13, v24 offset:24\n"
    "s_mov_b64 exec, s[42:43]\n"
    "v_cndmask_b32 v24, 0, -16, s[62:63]\n"
    "s_brev_b32 s40, 1\n"
    "v_writelane_b32 v24, s40, 2\n"
    "s_lshl_b32 s40, 1, s22\n"
    "v_or_b32 v24, s40, v24\n"
    "s_mov_b32 s36, 1\n"
    "ds_or_b32 v13, v24 offset:536\n"
    "s_mov_b64 exec, -1\n"
    "s_branch .Lxd_ld46r\n"
    ".Lxd_ld46:\n"
    "v_mov_b32 v24, 1\n"
    "s_and_b32 s40, s45, 15\n"
    "s_cbranch_scc1 .Lxd_dr47\n"
    "s_mov_b64 exec, 1\n"
    "global_atomic_add v24, v17, v24, s[52:53] sc0 sc1\n"
    "s_mov_b64 exec, -1\n"
    ".Lxd_dr47r:\n"
    "s_branch .Lxd_ld46r\n"
    ".Lxd_ck48:\n"
    "s_mov_b32 s37, 3\n"
    "s_cmp_eq_u32 s45, -1\n"
    "s_cselect_b32 s40, s56, 0\n"
    "s_cmp_eq_u32 s40, 2\n"
    "s_cbranch_scc1 .Lxd_last0\n"
    "s_cmp_eq_u32 s40, 1\n"
    "s_cbranch_scc1 .Lxd_last1\n"
    "v_readfirstlane_b32 s41, v25\n"
    "s_and_b32 s40, s45, 0xff0\n"
    "s_cselect_b32 s40, s45, 0\n"
    "s_cmp_lt_i32 s40, -16\n"
    "s_cselect_b32 s40, s56, 1\n"
    "s_cmp_lg_u32 s40, 0\n"
    "s_cbranch_scc1 .Lxd_ck48g\n"
    "s_bitset0_b32 s45, 31\n"
    "s_lshl4_add_u32 s40, s22, s58\n"
    "s_add_u32 s41, s41, 16\n"
    "s_mov_b32 s56, 2\n"
    "s_cmp_lt_u32 s41, s40\n"
    "s_cbranch_scc1 .Lxd_ck48r\n"
    "s_call_b64 s[60:61], .Lxd_gran\n"
    "s_branch .Lxd_ck48r\n"
    ".Lxd_ck48g:\n"
    "v_add_u32 v13, 16, v25\n"
    "s_call_b64 s[60:61], .Lxd_slow\n"
    "s_cmp_eq_u32 s45, -1\n"
    "s_cbranch_scc0 .Lxd_ck48r\n"
    "s_cmp_eq_u32 s56, 1\n"
    "s_cbranch_scc1 .Lxd_last1\n"
    "s_branch .Lxd_last0\n"
    ".Lxd_f2:\n"
    "s_mov_b32 s59, 0x20000\n"
    ".Lxd_f4:\n"
    "s_call_b64 s[60:61], .Lxd_dsum\n"
    "s_cmp_eq_u32 s56, 2\n"
    "s_cbranch_scc1 .Lxd_f3\n"
    "s_sleep 2\n"
    "s_call_b64 s[60:61], .Lxd_gran\n"
    "s_sub_u32 s59, s59, 1\n"
    "s_cbranch_scc0 .Lxd_f4\n"
    "s_waitcnt vmcnt(0)\n"
    "s_branch .Lxd_f3\n"
    ".Lxd_on1:\n"
    "s_mov_b64 exec, 0x3ff\n"
    "global_load_dwordx2 v[26:27], v11, s[16:17] sc1\n"
    "s_mov_b64 exec, -1\n"
    "s_branch .Lxd_on1r\n"
    ".Lxd_pv3:\n"
    "s_mov_b32 s59, 0x20000\n"
    ".Lxd_pv3l:\n"
    "s_sleep 1\n"
    "s_mov_b64 exec, 0x3ff\n"
    "global_load_dwordx2 v[26:27], v11, s[16:17] sc1\n"
    "s_mov_b64 exec, -1\n"
    "s_waitcnt vmcnt(0)\n"
    "v_readlane_b32 s25, v26, 9\n"
    "v_readlane_b32 s40, v26, 8\n"
    "v_readlane_b32 s41, v27, 8\n"
    "s_mul_i32 s42, s25, 0x9e3779b1\n"
    "s_lshr_b32 s42, s42, 24\n"
    "s_xor_b32 s42, s42, s40\n"
    "s_and_b32 s42, s42, 0xff\n"
    "s_xor_b32 s41, s41, s25\n"
    "s_xor_b32 s41, s41, 0xa5c30f96\n"
    "s_or_b32 s42, s41, s42\n"
    "s_lshr_b32 s41, s40, 8\n"
    "s_mul_i32 s40, s41, 0x9e3779b1\n"
    "s_xor_b32 s41, s41, 0xa5c30f96\n"
    "s_lshr_b32 s40, s40, 24\n"
    "v_xor_b32 v13, s40, v26\n"
    "v_xor_b32 v22, s41, v27\n"
    "v_lshl_or_b32 v13, v13, 24, v22\n"
    "v_cmp_ne_u32 s[66:67], 0, v13\n"
    "s_and_b32 s66, s66, 0xff\n"
    "s_or_b32 s42, s42, s66\n"
    "s_cmp_eq_u32 s42, 0\n"
    "s_cbranch_scc1 .Lxd_pv3r\n"
    "s_sub_u32 s59, s59, 1\n"
    "s_cbranch_scc0 .Lxd_pv3l\n"
    "s_branch .Lxd_pv3r\n"
    ".Lxd_on6:\n"
    "v_mov_b32 v24, 1\n"
    "s_mov_b64 exec, 1\n"
    "global_atomic_add v24, v17, v24, s[52:53] sc0 sc1\n"
    "s_mov_b64 exec, -1\n"
    "s_branch .Lxd_on6r\n"
    ".Lxd_ck7:\n"
    "v_mov_b32 v13, s58\n"
    "s_call_b64 s[60:61], .Lxd_slow\n"
    "s_branch .Lxd_ck7r\n"
    ".Lxd_tf8:\n"
    "v_mov_b32 v12, -1\n"
    "v_and_b32 v13, 0x3ffff, v25\n"
    "s_add_u32 s40, s58, 248\n"
    "v_lshl_add_u32 v13, v1, 2, v13\n"
    "v_mov_b32 v24, -1\n"
    "v_cmp_gt_u32 s[42:43], s40, v13\n"
    "s_mov_b32 s62, 0x88888888\n"
    "s_mov_b32 s63, s62\n"
    "s_and_b64 exec, s[42:43], s[62:63]\n"
    "ds_write_b32 v13, v24 offset:8\n"
    "s_mov_b64 exec, s[42:43]\n"
    "v_cndmask_b32 v24, 0, -16, s[62:63]\n"
    "s_brev_b32 s40, 1\n"
    "v_writelane_b32 v24, s40, 2\n"
    "s_lshl_b32 s40, 1, s22\n"
    "v_or_b32 v24, s40, v24\n"
    "s_mov_b32 s36, 1\n"
    "ds_or_b32 v13, v24 offset:520\n"
    "s_mov_b64 exec, -1\n"
    "s_branch .Lxd_tf8r\n"
    ".Lxd_on9:\n"
    "s_waitcnt vmcnt(0)\n"
    "v_readfirstlane_b32 s40, v24\n"
    "s_or_b32 s40, s40, s44\n"
    "s_lshl1_add_u32 s41, s40, s54\n"
    "s_cmpk_lt_u32 s40, 256\n"
    "s_cselect_b32 s41, s41, 2\n"
    "v_mov_b32 v12, s41\n"
    "s_cbranch_scc0 .Lxd_tf8\n"
    ".Lxd_tf8r:\n"
    "s_mul_i32 s41, s41, 52\n"
    "s_add_u32 s38, s4, s41\n"
    "s_addc_u32 s39, s5, 0\n"
    "ds_write_b32 v25, v12 offset:20\n"
    "global_load_dwordx3 v[36:38], v17, s[38:39] offset:-104\n"
    "global_load_dword v41, v18, s[38:39] offset:-8\n"
    "v_mov_b32 v13, s58\n"
    "s_branch .Lxd_on9r\n"
    ".Lxd_on10:\n"
    "v_mov_b32 v24, 1\n"
    "s_mov_b64 exec, 1\n"
    "global_atomic_add v24, v17, v24, s[52:53] sc0 sc1\n"
    "s_mov_b64 exec, -1\n"
    "s_branch .Lxd_on10r\n"
    ".Lxd_on11:\n"
    "s_waitcnt vmcnt(0)\n"
    "v_lshl_or_b32 v36, v36, 7, v19\n"
    "v_lshl_or_b32 v37, v37, 7, v19\n"
    "v_lshl_or_b32 v38, v38, 7, v19\n"
    "v_lshl_or_b32 v41, v41, 7, v20\n"
    "global_load_dwordx4 v[28:31], v36, s[8:9]\n"
    "global_load_dwordx4 v[32:35], v37, s[8:9]\n"
    "global_load_dword v40, v41, s[8:9]\n"
    "global_load_dwordx4 v[36:39], v38, s[8:9]\n"
    "s_branch .Lxd_on11r\n"
    ".Lxd_tf12:\n"
    "v_mov_b32 v12, -1\n"
    "v_and_b32 v13, 0x3ffff, v25\n"
    "s_add_u32 s40, s58, 248\n"
    "v_lshl_add_u32 v13, v1, 2, v13\n"
    "v_mov_b32 v24, -1\n"
    "v_cmp_gt_u32 s[42:43], s40, v13\n"
    "s_mov_b32 s62, 0x88888888\n"
    "s_mov_b32 s63, s62\n"
    "s_and_b64 exec, s[42:43], s[62:63]\n"
    "ds_write_b32 v13, v24 offset:8\n"
    "s_mov_b64 exec, s[42:43]\n"
    "v_cndmask_b32 v24, 0, -16, s[62:63]\n"
    "s_brev_b32 s40, 1\n"
    "v_writelane_b32 v24, s40, 2\n"
    "s_lshl_b32 s40, 1, s22\n"
    "v_or_b32 v24, s40, v24\n"
    "s_mov_b32 s36, 1\n"
    "ds_or_b32 v13, v24 offset:520\n"
    "s_mov_b64 exec, -1\n"
    "s_branch .Lxd_tf12r\n"
    ".Lxd_on13:\n"
    "s_waitcnt vmcnt(0)\n"
    "v_readfirstlane_b32 s40, v24\n"
    "s_or_b32 s40, s40, s44\n"
    "s_lshl1_add_u32 s41, s40, s54\n"
    "s_cmpk_lt_u32 s40, 256\n"
    "s_cselect_b32 s41, s41, 2\n"
    "v_mov_b32 v12, s41\n"
    "s_cbranch_scc0 .Lxd_tf12\n"
    ".Lxd_tf12r:\n"
    "s_mul_i32 s41, s41, 52\n"
    "s_add_u32 s38, s4, s41\n"
    "s_addc_u32 s39, s5, 0\n"
    "ds_write_b32 v25, v12 offset:20\n"
    "global_load_dwordx3 v[36:38], v17, s[38:39] offset:-104\n"
    "global_load_dword v41, v18, s[38:39] offset:-8\n"
    "s_branch .Lxd_on13r\n"
    ".Lxd_on14:\n"
    "v_mov_b32 v24, 1\n"
    "s_mov_b64 exec, 1\n"
    "global_atomic_add v24, v17, v24, s[52:53] sc0 sc1\n"
    "s_mov_b64 exec, -1\n"
    "s_branch .Lxd_on14r\n"
    ".Lxd_pv62:\n"
    "s_mov_b32 s59, 0x20000\n"
    ".Lxd_pv62l:\n"
    "s_sleep 1\n"
    "s_mov_b64 exec, 0x3ff\n"
    "global_load_dwordx2 v[26:27], v11, s[16:17] sc1\n"
    "s_mov_b64 exec, -1\n"
    "s_waitcnt vmcnt(0)\n"
    "v_readlane_b32 s25, v26, 9\n"
    "v_readlane_b32 s40, v26, 8\n"
    "v_readlane_b32 s41, v27, 8\n"
    "s_mul_i32 s42, s25, 0x9e3779b1\n"
    "s_lshr_b32 s42, s42, 24\n"
    "s_xor_b32 s42, s42, s40\n"
    "s_and_b32 s42, s42, 0xff\n"
    "s_xor_b32 s41, s41, s25\n"
    "s_xor_b32 s41, s41, 0xa5c30f96\n"
    "s_or_b32 s42, s41, s42\n"
    "s_lshr_b32 s41, s40, 8\n"
    "s_mul_i32 s40, s41, 0x9e3779b1\n"
    "s_xor_b32 s41, s41, 0xa5c30f96\n"
    "s_lshr_b32 s40, s40, 24\n"
    "v_xor_b32 v13, s40, v26\n"
    "v_xor_b32 v22, s41, v27\n"
    "v_lshl_or_b32 v13, v13, 24, v22\n"
    "v_cmp_ne_u32 s[66:67], 0, v13\n"
    "s_and_b32 s66, s66, 0xff\n"
    "s_or_b32 s42, s42, s66\n"
    "s_cmp_eq_u32 s42, 0\n"
    "s_cbranch_scc1 .Lxd_pv62r\n"
    "s_sub_u32 s59, s59, 1\n"
    "s_cbranch_scc0 .Lxd_pv62l\n"
    "s_branch .Lxd_pv62r\n"
    ".Lxd_end:\n"
    : "+{v0}"(tid) : "{s[0:1]}"(__builtin_amdgcn_kernarg_segment_ptr()), "{s2}"(blockIdx.x)
    : "memory", "vcc", "scc", "v1", "v2", "v3", "v4", "v5", "v6", "v7", "v8", "v9", "v10", "v11", "v12", "v13", "v14", "v15", "v16", "v17", "v18", "v19", "v20", "v21", "v22", "v23", "v24", "v25", "v26", "v27", "v28", "v29", "v30", "v31", "v32", "v33", "v34", "v35", "v36", "v37", "v38", "v39", "v40", "v41", "v42", "v43", "v44", "v45", "v46", "v47", "v48", "v49", "v50", "v51", "v52", "v53", "v54", "v55", "v56", "v57", "v58", "v59", "v60", "v61", "v62", "v63", "v64", "v65", "v66", "v67", "v68", "v69", "v70", "v71", "v72", "v73", "v74", "v75", "v76", "v77", "v78", "v79", "v80", "v81", "v82", "v83", "v84", "v85", "v86", "v87", "v88", "v89", "v90", "v91", "v92", "v93", "v94", "v95", "v96", "v97", "v98", "v99", "v100", "v101", "v102", "v103", "v104", "v105", "v106", "v107", "v108", "v109", "v110", "v111", "v112", "v113", "v114", "v115", "v116", "v117", "v118", "v119", "v120", "v121", "v122", "v123", "v124", "v125", "v126", "v127", "v128", "v129", "v130", "v131", "v132", "v133", "v134", "v135", "v136", "v137", "v138", "v139", "v140", "v141", "v142", "v143", "v144", "v145", "v146", "v147", "v148", "v149", "v150", "v151", "v152", "v153", "v154", "v155", "v156", "v157", "v158", "v159", "v160", "v161", "v162", "v163", "v164", "v165", "v166", "v167", "v168", "v169", "v170", "v171", "v172", "v173", "v174", "v175", "v176", "v177", "v178", "v179", "v180", "v181", "v182", "v183", "v184", "v185", "v186", "v187", "v188", "v189", "v190", "v191", "v192", "v193", "v194", "v195", "v196", "v197", "v198", "v199", "v200", "v201", "v202", "v203", "v204", "v205", "v206", "v207", "v208", "v209", "v210", "v211", "v212", "v213", "v214", "v215", "v216", "v217", "v218", "v219", "v220", "v221", "v222", "v223", "v224", "v225", "v226", "v227", "v228", "v229", "v230", "v231", "v232", "v233", "v234", "v235", "v236", "v237", "v238", "v239", "v240", "v241", "v242", "v243", "v244", "v245", "v246", "v247", "v248", "v249", "v250", "v251", "v252", "v253", "v254", "v255", "s3", "s4", "s5", "s6", "s7", "s8", "s9", "s10", "s11", "s12", "s13", "s14", "s15", "s16", "s17", "s18", "s19", "s20", "s21", "s22", "s23", "s24", "s25", "s26", "s27", "s28", "s29", "s30", "s31", "s34", "s35", "s36", "s37", "s38", "s39", "s40", "s41", "s42", "s43", "s44", "s45", "s46", "s47", "s48", "s49", "s50", "s51", "s52", "s53", "s54", "s55", "s56", "s57", "s58", "s59", "s60", "s61", "s62", "s63", "s64", "s65", "s66", "s67", "s68", "s69", "s70", "s71");
  asm volatile("" :: "v"((__attribute__((address_space(3))) char*)shm));
}

extern "C" void kernel_launch(void* const* d_in, const int* in_sizes, int n_in, void* d_out, int out_size,
                              void* d_ws, size_t ws_size, hipStream_t stream) {
  xd_kernel<<<256, 512, 0, stream>>>((const int*)d_in[0], (const u16*)d_in[4], (const u16*)d_in[1], (const u16*)d_in[5], (const u16*)d_in[2], (const u16*)d_in[3], (char*)d_ws,
                                     (const u16*)d_in[6], (const u16*)d_in[7], (u16*)d_out);
}
```

```cpp
#include <hip/hip_runtime.h>
typedef unsigned short u16;

__device__ const __attribute__((aligned(16), used, visibility("hidden"))) u16 xd_tab[5120] asm("xd_tab") = {924, 925, 928, 929, 926, 927, 930, 931, 934, 935, 938, 939, 932, 933, 936, 937, 940, 941, 944, 945, 942, 943, 946, 947, 948, 0, 949, 0, 0, 0, 0, 0, 2, 36, 138, 172, 0, 1, 24, 25, 338, 372, 474, 508, 34, 35, 57, 58, 546, 580, 682, 716, 68, 69, 90, 91, 816, 817, 850, 952, 102, 99, 123, 124, 680, 681, 684, 685, 20, 21, 22, 23, 714, 715, 717, 718, 54, 55, 56, 53, 748, 749, 750, 751, 88, 89, 86, 87, 782, 779, 783, 784, 122, 119, 120, 121, 152, 153, 154, 155, 284, 285, 286, 287, 186, 187, 188, 185, 318, 319, 320, 317, 220, 221, 218, 219, 352, 353, 350, 351, 254, 251, 252, 253, 386, 383, 384, 385, 416, 417, 418, 419, 548, 549, 550, 551, 450, 451, 452, 449, 582, 583, 584, 581, 484, 485, 482, 483, 616, 617, 614, 615, 518, 515, 516, 517, 650, 647, 648, 649, 544, 545, 552, 553, 16, 17, 18, 19, 578, 579, 585, 586, 50, 51, 52, 49, 612, 613, 618, 619, 84, 85, 82, 83, 646, 643, 651, 652, 118, 115, 116, 117, 148, 149, 150, 151, 280, 281, 282, 283, 182, 183, 184, 181, 314, 315, 316, 313, 216, 217, 214, 215, 348, 349, 346, 347, 250, 247, 248, 249, 382, 379, 380, 381, 412, 413, 414, 415, 408, 409, 420, 421, 446, 447, 448, 445, 442, 443, 453, 454, 480, 481, 478, 479, 476, 477, 486, 487, 514, 511, 512, 513, 510, 507, 519, 520, 12, 13, 14, 15, 144, 145, 146, 147, 46, 47, 48, 45, 178, 179, 180, 177, 80, 81, 78, 79, 212, 213, 210, 211, 114, 111, 112, 113, 246, 243, 244, 245, 276, 277, 278, 279, 272, 273, 288, 289, 310, 311, 312, 309, 306, 307, 321, 322, 344, 345, 342, 343, 340, 341, 354, 355, 378, 375, 376, 377, 374, 371, 387, 388, 8, 9, 10, 11, 140, 141, 142, 143, 42, 43, 44, 41, 174, 175, 176, 173, 76, 77, 74, 75, 208, 209, 206, 207, 110, 107, 108, 109, 242, 239, 240, 241, 136, 137, 156, 157, 4, 5, 6, 7, 170, 171, 189, 190, 38, 39, 40, 37, 204, 205, 222, 223, 72, 73, 70, 71, 238, 235, 255, 256, 106, 103, 104, 105, 0, 0, 0, 0, 0, 0, 0, 0, 0, 0, 0, 0, 0, 0, 0, 0, 0, 0, 0, 0, 0, 0, 0, 0, 0, 0, 0, 0, 0, 0, 0, 0, 0, 0, 0, 0, 0, 0, 0, 0, 0, 0, 0, 0, 0, 0, 0, 0, 0, 0, 0, 0, 0, 0, 0, 0, 0, 0, 0, 0, 0, 0, 0, 0, 0, 0, 0, 0, 0, 0, 0, 0, 0, 0, 0, 0, 0, 0, 0, 0, 0, 0, 0, 0, 0, 0, 0, 0, 0, 0, 0, 0, 0, 0, 0, 0, 0, 0, 0, 0, 0, 0, 0, 0, 0, 0, 0, 0, 0, 0, 0, 0, 0, 0, 0, 0, 0, 0, 0, 0, 0, 0, 0, 0, 0, 0, 0, 0, 924, 925, 926, 927, 934, 935, 932, 933, 940, 941, 942, 943, 948, 0, 0, 0, 2, 36, 0, 1, 338, 372, 34, 35, 546, 580, 68, 69, 816, 817, 102, 99, 928, 929, 930, 931, 938, 939, 936, 937, 944, 945, 946, 947, 949, 0, 0, 0, 138, 172, 24, 25, 474, 508, 57, 58, 682, 716, 90, 91, 850, 952, 123, 124, 680, 681, 20, 21, 714, 715, 54, 55, 748, 749, 88, 89, 782, 779, 122, 119, 152, 153, 284, 285, 186, 187, 318, 319, 220, 221, 352, 353, 254, 251, 386, 383, 684, 685, 22, 23, 717, 718, 56, 53, 750, 751, 86, 87, 783, 784, 120, 121, 154, 155, 286, 287, 188, 185, 320, 317, 218, 219, 350, 351, 252, 253, 384, 385, 416, 417, 548, 549, 450, 451, 582, 583, 484, 485, 616, 617, 518, 515, 650, 647, 544, 545, 16, 17, 578, 579, 50, 51, 612, 613, 84, 85, 646, 643, 118, 115, 418, 419, 550, 551, 452, 449, 584, 581, 482, 483, 614, 615, 516, 517, 648, 649, 552, 553, 18, 19, 585, 586, 52, 49, 618, 619, 82, 83, 651, 652, 116, 117, 148, 149, 280, 281, 182, 183, 314, 315, 216, 217, 348, 349, 250, 247, 382, 379, 412, 413, 408, 409, 446, 447, 442, 443, 480, 481, 476, 477, 514, 511, 510, 507, 150, 151, 282, 283, 184, 181, 316, 313, 214, 215, 346, 347, 248, 249, 380, 381, 414, 415, 420, 421, 448, 445, 453, 454, 478, 479, 486, 487, 512, 513, 519, 520, 12, 13, 144, 145, 46, 47, 178, 179, 80, 81, 212, 213, 114, 111, 246, 243, 276, 277, 272, 273, 310, 311, 306, 307, 344, 345, 340, 341, 378, 375, 374, 371, 14, 15, 146, 147, 48, 45, 180, 177, 78, 79, 210, 211, 112, 113, 244, 245, 278, 279, 288, 289, 312, 309, 321, 322, 342, 343, 354, 355, 376, 377, 387, 388, 8, 9, 140, 141, 42, 43, 174, 175, 76, 77, 208, 209, 110, 107, 242, 239, 136, 137, 4, 5, 170, 171, 38, 39, 204, 205, 72, 73, 238, 235, 106, 103, 10, 11, 142, 143, 44, 41, 176, 173, 74, 75, 206, 207, 108, 109, 240, 241, 156, 157, 6, 7, 189, 190, 40, 37, 222, 223, 70, 71, 255, 256, 104, 105, 0, 0, 0, 0, 0, 0, 0, 0, 0, 0, 0, 0, 0, 0, 0, 0, 0, 0, 0, 0, 0, 0, 0, 0, 0, 0, 0, 0, 0, 0, 0, 0, 0, 0, 0, 0, 0, 0, 0, 0, 0, 0, 0, 0, 0, 0, 0, 0, 0, 0, 0, 0, 0, 0, 0, 0, 0, 0, 0, 0, 0, 0, 0, 0, 0, 0, 0, 0, 0, 0, 0, 0, 0, 0, 0, 0, 0, 0, 0, 0, 0, 0, 0, 0, 0, 0, 0, 0, 0, 0, 0, 0, 0, 0, 0, 0, 0, 0, 0, 0, 0, 0, 0, 0, 0, 0, 0, 0, 0, 0, 0, 0, 0, 0, 0, 0, 0, 0, 0, 0, 0, 0, 0, 0, 0, 0, 0, 0, 0, 0, 0, 0, 0, 0, 0, 0, 0, 0, 0, 0, 0, 0, 0, 0, 0, 0, 0, 0, 0, 0, 0, 0, 0, 0, 0, 0, 0, 0, 0, 0, 0, 16256, 0, 16256, 0, 16256, 0, 16256, 0, 16256, 0, 16256, 0, 16256, 0, 16256, 0, 16256, 0, 16256, 0, 16256, 0, 16256, 0, 16128, 0, 16256, 0, 16128, 0, 0, 0, 16128, 0, 16256, 0, 16256, 0, 16256, 0, 16128, 0, 16256, 0, 16256, 0, 16256, 0, 16128, 0, 16256, 0, 16256, 0, 16256, 0, 16128, 0, 16256, 0, 16256, 0, 16256, 0, 16256, 0, 16256, 0, 16256, 0, 16256, 0, 16256, 0, 16256, 0, 16256, 0, 16256, 0, 16256, 0, 16256, 0, 16256, 0, 16256, 0, 16256, 0, 16256, 0, 16256, 0, 16256, 0, 16256, 0, 16256, 0, 16256, 0, 16256, 0, 16256, 0, 16256, 0, 16256, 0, 16256, 0, 16256, 0, 16256, 0, 16256, 0, 16256, 0, 16256, 0, 16256, 0, 16256, 0, 16256, 0, 16128, 0, 16256, 0, 16256, 0, 16256, 0, 16128, 0, 16256, 0, 16256, 0, 16256, 0, 16128, 0, 16256, 0, 16256, 0, 16256, 0, 16128, 0, 16256, 0, 16256, 0, 16256, 0, 16256, 0, 16256, 0, 16256, 0, 16256, 0, 16256, 0, 16256, 0, 16256, 0, 16256, 0, 16256, 0, 16256, 0, 16256, 0, 16256, 0, 16256, 0, 16256, 0, 16256, 0, 16256, 0, 16256, 0, 16256, 0, 16256, 0, 16256, 0, 16256, 0, 16256, 0, 16256, 0, 16256, 0, 16256, 0, 16256, 0, 16256, 0, 16256, 0, 16256, 0, 16256, 0, 16256, 0, 16256, 0, 16256, 0, 16256, 0, 16256, 0, 16256, 0, 16256, 0, 16256, 0, 16256, 0, 16256, 0, 16256, 0, 16256, 0, 16256, 0, 16256, 0, 16256, 0, 16256, 0, 16256, 0, 16256, 0, 16256, 0, 16256, 0, 16256, 0, 16256, 0, 16256, 0, 16256, 0, 16256, 0, 16256, 0, 16256, 0, 16256, 0, 16256, 0, 16256, 0, 16256, 0, 16256, 0, 16256, 0, 16256, 0, 16256, 0, 16256, 0, 16256, 0, 16256, 0, 16256, 0, 16256, 0, 16256, 0, 16256, 0, 16256, 0, 16256, 0, 16256, 0, 16256, 0, 16256, 0, 16256, 0, 16256, 0, 16256, 0, 16128, 0, 16256, 0, 16256, 0, 16256, 0, 16128, 0, 16256, 0, 16256, 0, 16256, 0, 16128, 0, 16256, 0, 16256, 0, 16256, 0, 16128, 0, 16256, 0, 16256, 0, 16256, 0, 0, 0, 0, 0, 0, 0, 0, 0, 0, 0, 0, 0, 0, 0, 0, 0, 0, 0, 0, 0, 0, 0, 0, 0, 0, 0, 0, 0, 0, 0, 0, 0, 0, 0, 0, 0, 0, 0, 0, 0, 0, 0, 0, 0, 0, 0, 0, 0, 0, 0, 0, 0, 0, 0, 0, 0, 0, 0, 0, 0, 0, 0, 0, 0, 0, 0, 0, 0, 0, 0, 0, 0, 0, 0, 0, 0, 0, 0, 0, 0, 0, 0, 0, 0, 0, 0, 0, 0, 0, 0, 0, 0, 0, 0, 0, 0, 0, 0, 0, 0, 0, 0, 0, 0, 0, 0, 0, 0, 0, 0, 0, 0, 0, 0, 0, 0, 0, 0, 0, 0, 0, 0, 0, 0, 0, 0, 0, 0, 0, 0, 0, 0, 0, 0, 0, 0, 0, 0, 0, 0, 0, 0, 0, 0, 0, 0, 0, 0, 0, 0, 0, 0, 0, 0, 0, 0, 0, 0, 0, 0, 16128, 0, 16256, 0, 16256, 0, 16256, 0, 16128, 0, 16256, 0, 16256, 0, 16256, 0, 16128, 0, 16256, 0, 16256, 0, 16256, 0, 16128, 0, 16256, 0, 16256, 0, 16256, 0, 16256, 0, 16256, 0, 16256, 0, 16256, 0, 16256, 0, 16256, 0, 16256, 0, 16256, 0, 16256, 0, 16256, 0, 16256, 0, 16256, 0, 16256, 0, 16256, 0, 16256, 0, 16256, 0, 16256, 0, 16256, 0, 16256, 0, 16256, 0, 16256, 0, 16256, 0, 16256, 0, 16256, 0, 16256, 0, 16256, 0, 16256, 0, 16256, 0, 16256, 0, 16256, 0, 16256, 0, 16256, 0, 16256, 0, 16256, 0, 16256, 0, 16256, 0, 16256, 0, 16256, 0, 16256, 0, 16256, 0, 16256, 0, 16256, 0, 16256, 0, 16256, 0, 16256, 0, 16256, 0, 16256, 0, 16256, 0, 16256, 0, 16256, 0, 16256, 0, 16256, 0, 16256, 0, 16256, 0, 16256, 0, 16256, 0, 16256, 0, 16256, 0, 16256, 0, 16256, 0, 16256, 0, 16256, 0, 16256, 0, 16256, 0, 16256, 0, 16256, 0, 16256, 0, 16256, 0, 16256, 0, 16256, 0, 16256, 0, 16256, 0, 16256, 0, 16256, 0, 16256, 0, 16256, 0, 16256, 0, 16256, 0, 16256, 0, 16256, 0, 16128, 0, 16256, 0, 16256, 0, 16256, 0, 16128, 0, 16256, 0, 16256, 0, 16256, 0, 16128, 0, 16256, 0, 16256, 0, 16256, 0, 16128, 0, 16256, 0, 16256, 0, 16256, 0, 16256, 0, 16256, 0, 16256, 0, 16256, 0, 16256, 0, 16256, 0, 16256, 0, 16256, 0, 16256, 0, 16256, 0, 16256, 0, 16256, 0, 16256, 0, 16256, 0, 16256, 0, 16256, 0, 16128, 0, 16256, 0, 16256, 0, 16256, 0, 16128, 0, 16256, 0, 16256, 0, 16256, 0, 16128, 0, 16256, 0, 16256, 0, 16256, 0, 16128, 0, 16256, 0, 16256, 0, 16256, 0, 16256, 0, 16256, 0, 16256, 0, 16256, 0, 16256, 0, 16256, 0, 16256, 0, 16256, 0, 16256, 0, 16256, 0, 16256, 0, 16256, 0, 16256, 0, 16256, 0, 16256, 0, 16256, 0, 16256, 0, 16256, 0, 16256, 0, 16256, 0, 16256, 0, 16256, 0, 16256, 0, 16256, 0, 16256, 0, 16256, 0, 16256, 0, 16256, 0, 16256, 0, 16256, 0, 16256, 0, 16256, 0, 0, 0, 0, 0, 0, 0, 0, 0, 0, 0, 0, 0, 0, 0, 0, 0, 0, 0, 0, 0, 0, 0, 0, 0, 0, 0, 0, 0, 0, 0, 0, 0, 0, 0, 0, 0, 0, 0, 0, 0, 0, 0, 0, 0, 0, 0, 0, 0, 0, 0, 0, 0, 0, 0, 0, 0, 0, 0, 0, 0, 0, 0, 0, 0, 0, 0, 0, 0, 0, 0, 0, 0, 0, 0, 0, 0, 0, 0, 0, 0, 0, 0, 0, 0, 0, 0, 0, 0, 0, 0, 0, 0, 0, 0, 0, 0, 0, 0, 0, 0, 0, 0, 0, 0, 0, 0, 0, 0, 0, 0, 0, 0, 0, 0, 0, 0, 0, 0, 0, 0, 0, 0, 0, 0, 0, 0, 0, 0, 16768, 0, 16768, 0, 16768, 0, 16768, 0, 16768, 0, 16768, 0, 16768, 0, 16768, 0, 16768, 0, 16768, 0, 16768, 0, 16768, 0, 16768, 0, 0, 0, 16768, 0, 0, 0, 16256, 0, 16256, 0, 16256, 0, 16256, 0, 16256, 0, 16256, 0, 16256, 0, 16256, 0, 16256, 0, 16256, 0, 16256, 0, 16256, 0, 16128, 0, 16256, 0, 16128, 0, 17280, 0, 16128, 0, 16256, 0, 16256, 0, 16256, 0, 16128, 0, 16256, 0, 16256, 0, 16256, 0, 16128, 0, 16256, 0, 16256, 0, 16256, 0, 16128, 0, 16256, 0, 16256, 0, 16256, 0, 16256, 0, 16256, 0, 16256, 0, 16256, 0, 16256, 0, 16256, 0, 16256, 0, 16256, 0, 16256, 0, 16256, 0, 16256, 0, 16256, 0, 16256, 0, 16256, 0, 16256, 0, 16256, 0, 16256, 0, 16256, 0, 16256, 0, 16256, 0, 16256, 0, 16256, 0, 16256, 0, 16256, 0, 16256, 0, 16256, 0, 16256, 0, 16256, 0, 16256, 0, 16256, 0, 16256, 0, 16256, 0, 16128, 0, 16256, 0, 16256, 0, 16256, 0, 16128, 0, 16256, 0, 16256, 0, 16256, 0, 16128, 0, 16256, 0, 16256, 0, 16256, 0, 16128, 0, 16256, 0, 16256, 0, 16256, 0, 16256, 0, 16256, 0, 16256, 0, 16256, 0, 16256, 0, 16256, 0, 16256, 0, 16256, 0, 16256, 0, 16256, 0, 16256, 0, 16256, 0, 16256, 0, 16256, 0, 16256, 0, 16256, 0, 16256, 0, 16256, 0, 16256, 0, 16256, 0, 16256, 0, 16256, 0, 16256, 0, 16256, 0, 16256, 0, 16256, 0, 16256, 0, 16256, 0, 16256, 0, 16256, 0, 16256, 0, 16256, 0, 16256, 0, 16256, 0, 16256, 0, 16256, 0, 16256, 0, 16256, 0, 16256, 0, 16256, 0, 16256, 0, 16256, 0, 16256, 0, 16256, 0, 16256, 0, 16256, 0, 16256, 0, 16256, 0, 16256, 0, 16256, 0, 16256, 0, 16256, 0, 16256, 0, 16256, 0, 16256, 0, 16256, 0, 16256, 0, 16256, 0, 16256, 0, 16256, 0, 16256, 0, 16256, 0, 16256, 0, 16256, 0, 16256, 0, 16256, 0, 16256, 0, 16256, 0, 16256, 0, 16256, 0, 16256, 0, 16256, 0, 16256, 0, 16256, 0, 16256, 0, 16256, 0, 16256, 0, 16256, 0, 16256, 0, 16256, 0, 16128, 0, 16256, 0, 16256, 0, 16256, 0, 16128, 0, 16256, 0, 16256, 0, 16256, 0, 16128, 0, 16256, 0, 16256, 0, 16256, 0, 16128, 0, 16256, 0, 16256, 0, 16256, 0, 0, 0, 0, 0, 0, 0, 0, 0, 0, 0, 0, 0, 0, 0, 0, 0, 0, 0, 0, 0, 0, 0, 0, 0, 0, 0, 0, 0, 0, 0, 0, 0, 0, 0, 0, 0, 0, 0, 0, 0, 0, 0, 0, 0, 0, 0, 0, 0, 0, 0, 0, 0, 0, 0, 0, 0, 0, 0, 0, 0, 0, 0, 0, 0, 0, 0, 0, 0, 0, 0, 0, 0, 0, 0, 0, 0, 0, 0, 0, 0, 0, 0, 0, 0, 0, 0, 0, 0, 0, 0, 0, 0, 0, 0, 0, 0, 0, 0, 0, 0, 0, 0, 0, 0, 0, 0, 0, 0, 0, 0, 0, 0, 0, 0, 0, 0, 0, 0, 0, 0, 0, 0, 0, 0, 0, 0, 0, 0, 16768, 0, 16768, 0, 16768, 0, 16768, 0, 16768, 0, 16768, 0, 16768, 0, 16768, 0, 16768, 0, 16768, 0, 16768, 0, 16768, 0, 0, 0, 0, 0, 0, 0, 0, 0, 16128, 0, 16256, 0, 16256, 0, 16256, 0, 16128, 0, 16256, 0, 16256, 0, 16256, 0, 16128, 0, 16256, 0, 16256, 0, 16256, 0, 16128, 0, 16256, 0, 16256, 0, 16256, 0, 16256, 0, 16256, 0, 16256, 0, 16256, 0, 16256, 0, 16256, 0, 16256, 0, 16256, 0, 16256, 0, 16256, 0, 16256, 0, 16256, 0, 16256, 0, 16256, 0, 16256, 0, 16256, 0, 16256, 0, 16256, 0, 16256, 0, 16256, 0, 16256, 0, 16256, 0, 16256, 0, 16256, 0, 16256, 0, 16256, 0, 16256, 0, 16256, 0, 16256, 0, 16256, 0, 16256, 0, 16256, 0, 16256, 0, 16256, 0, 16256, 0, 16256, 0, 16256, 0, 16256, 0, 16256, 0, 16256, 0, 16256, 0, 16256, 0, 16256, 0, 16256, 0, 16256, 0, 16256, 0, 16256, 0, 16256, 0, 16256, 0, 16256, 0, 16256, 0, 16256, 0, 16256, 0, 16256, 0, 16256, 0, 16256, 0, 16256, 0, 16256, 0, 16256, 0, 16256, 0, 16256, 0, 16256, 0, 16256, 0, 16256, 0, 16256, 0, 16256, 0, 16256, 0, 16256, 0, 16256, 0, 16256, 0, 16256, 0, 16256, 0, 16256, 0, 16256, 0, 16256, 0, 16256, 0, 16256, 0, 16256, 0, 16256, 0, 16256, 0, 16128, 0, 16256, 0, 16256, 0, 16256, 0, 16128, 0, 16256, 0, 16256, 0, 16256, 0, 16128, 0, 16256, 0, 16256, 0, 16256, 0, 16128, 0, 16256, 0, 16256, 0, 16256, 0, 16256, 0, 16256, 0, 16256, 0, 16256, 0, 16256, 0, 16256, 0, 16256, 0, 16256, 0, 16256, 0, 16256, 0, 16256, 0, 16256, 0, 16256, 0, 16256, 0, 16256, 0, 16256, 0, 16128, 0, 16256, 0, 16256, 0, 16256, 0, 16128, 0, 16256, 0, 16256, 0, 16256, 0, 16128, 0, 16256, 0, 16256, 0, 16256, 0, 16128, 0, 16256, 0, 16256, 0, 16256, 0, 16256, 0, 16256, 0, 16256, 0, 16256, 0, 16256, 0, 16256, 0, 16256, 0, 16256, 0, 16256, 0, 16256, 0, 16256, 0, 16256, 0, 16256, 0, 16256, 0, 16256, 0, 16256, 0, 16256, 0, 16256, 0, 16256, 0, 16256, 0, 16256, 0, 16256, 0, 16256, 0, 16256, 0, 16256, 0, 16256, 0, 16256, 0, 16256, 0, 16256, 0, 16256, 0, 16256, 0, 16256, 0, 0, 0, 0, 0, 0, 0, 0, 0, 0, 0, 0, 0, 0, 0, 0, 0, 0, 0, 0, 0, 0, 0, 0, 0, 0, 0, 0, 0, 0, 0, 0, 0, 0, 0, 0, 0, 0, 0, 0, 0, 0, 0, 0, 0, 0, 0, 0, 0, 0, 0, 0, 0, 0, 0, 0, 0, 0, 0, 0, 0, 0, 0, 0, 0, 0, 0, 0, 0, 0, 0, 0, 0, 0, 0, 0, 0, 0, 0, 0, 0, 0, 0, 0, 0, 0, 0, 0, 0, 0, 0, 0, 0, 0, 0, 0, 0, 0, 0, 0, 0, 0, 0, 0, 0, 0, 0, 0, 0, 0, 0, 0, 0, 0, 0, 0, 0, 0, 0, 0, 0, 0, 0, 0, 0, 0, 0, 0, 0, 0, 0, 0, 0, 0, 0, 0, 0, 0, 0, 0, 0, 0, 0, 0, 0, 16256, 0, 16256, 0, 16128, 0, 16256, 0, 16256, 0, 16256, 0, 16128, 0, 16256, 0, 0, 0, 0, 0, 0, 0, 0, 0, 0, 0, 0, 0, 0, 0, 0, 0, 16256, 0, 16256, 0, 16256, 0, 16256, 0, 16256, 0, 16256, 0, 16256, 0, 16256, 0, 16128, 0, 16256, 0, 16256, 0, 16256, 0, 16128, 0, 16256, 0, 16256, 0, 16256, 0, 16256, 0, 16256, 0, 16256, 0, 16256, 0, 16256, 0, 16256, 0, 16256, 0, 16256, 0, 16256, 0, 16256, 0, 16256, 0, 16256, 0, 16256, 0, 16256, 0, 16256, 0, 16256, 0, 16256, 0, 16256, 0, 16256, 0, 16256, 0, 16256, 0, 16256, 0, 16256, 0, 16256, 0, 16256, 0, 16256, 0, 16256, 0, 16256, 0, 16256, 0, 16256, 0, 16256, 0, 16256, 0, 16128, 0, 16256, 0, 16256, 0, 16256, 0, 16128, 0, 16256, 0, 16256, 0, 16256, 0, 16256, 0, 16256, 0, 16256, 0, 16256, 0, 16256, 0, 16256, 0, 16256, 0, 16256, 0, 16256, 0, 16256, 0, 16256, 0, 16256, 0, 16256, 0, 16256, 0, 16256, 0, 16256, 0, 16256, 0, 16256, 0, 16256, 0, 16256, 0, 16256, 0, 16256, 0, 16256, 0, 16256, 0, 16256, 0, 16256, 0, 16128, 0, 16256, 0, 16256, 0, 16256, 0, 16128, 0, 16256, 0, 16256, 0, 16256, 0, 16256, 0, 16256, 0, 16256, 0, 16256, 0, 16256, 0, 16256, 0, 16256, 0, 16256, 0, 16256, 0, 16256, 0, 16256, 0, 16256, 0, 16256, 0, 16256, 0, 16256, 0, 16256, 0, 16256, 0, 16256, 0, 16256, 0, 16256, 0, 16256, 0, 16256, 0, 16256, 0, 16256, 0, 16128, 0, 16256, 0, 16256, 0, 16256, 0, 16128, 0, 16256, 0, 16256, 0, 16256, 0, 16256, 0, 16256, 0, 16256, 0, 16256, 0, 16256, 0, 16256, 0, 16256, 0, 16256, 0, 16256, 0, 16256, 0, 16256, 0, 16256, 0, 16256, 0, 16256, 0, 16256, 0, 16256, 0, 16256, 0, 16256, 0, 16256, 0, 16256, 0, 16256, 0, 16256, 0, 16128, 0, 16256, 0, 16256, 0, 16256, 0, 16128, 0, 16256, 0, 16256, 0, 16256, 0, 16256, 0, 16256, 0, 16256, 0, 16256, 0, 16256, 0, 16256, 0, 16256, 0, 16256, 0, 16256, 0, 16256, 0, 16256, 0, 16256, 0, 16256, 0, 16256, 0, 16256, 0, 16256, 0, 0, 0, 0, 0, 0, 0, 0, 0, 0, 0, 0, 0, 0, 0, 0, 0, 0, 0, 0, 0, 0, 0, 0, 0, 0, 0, 0, 0, 0, 0, 0, 0, 0, 0, 0, 0, 0, 0, 0, 0, 0, 0, 0, 0, 0, 0, 0, 0, 0, 0, 0, 0, 0, 0, 0, 0, 0, 0, 0, 0, 0, 0, 0, 0, 0, 0, 0, 0, 0, 0, 0, 0, 0, 0, 0, 0, 0, 0, 0, 0, 0, 0, 0, 0, 0, 0, 0, 0, 0, 0, 0, 0, 0, 0, 0, 0, 0, 0, 0, 0, 0, 0, 0, 0, 0, 0, 0, 0, 0, 0, 0, 0, 0, 0, 0, 0, 0, 0, 0, 0, 0, 0, 0, 0, 0, 0, 0, 0, 0, 0, 0, 0, 0, 0, 0, 0, 0, 0, 0, 0, 0, 0, 0, 0, 16256, 0, 16256, 0, 16128, 0, 16256, 0, 16128, 0, 16256, 0, 16128, 0, 16256, 0, 0, 0, 0, 0, 0, 0, 0, 0, 0, 0, 0, 0, 0, 0, 0, 0, 16256, 0, 16256, 0, 16256, 0, 16256, 0, 16128, 0, 0, 0, 16256, 0, 16256, 0, 16128, 0, 16256, 0, 16256, 0, 16256, 0, 16128, 0, 16256, 0, 16256, 0, 16256, 0, 16256, 0, 16256, 0, 16256, 0, 16256, 0, 16256, 0, 16256, 0, 16256, 0, 16256, 0, 16256, 0, 16256, 0, 16256, 0, 16256, 0, 16256, 0, 16256, 0, 16256, 0, 16256, 0, 16256, 0, 16256, 0, 16256, 0, 16256, 0, 16256, 0, 16256, 0, 16256, 0, 16256, 0, 16256, 0, 16256, 0, 16256, 0, 16256, 0, 16256, 0, 16256, 0, 16256, 0, 16256, 0, 16128, 0, 16256, 0, 16256, 0, 16256, 0, 16128, 0, 16256, 0, 16256, 0, 16256, 0, 16256, 0, 16256, 0, 16256, 0, 16256, 0, 16256, 0, 16256, 0, 16256, 0, 16256, 0, 16256, 0, 16256, 0, 16256, 0, 16256, 0, 16256, 0, 16256, 0, 16256, 0, 16256, 0, 16256, 0, 16256, 0, 16256, 0, 16256, 0, 16256, 0, 16256, 0, 16256, 0, 16256, 0, 16256, 0, 16256, 0, 16128, 0, 16256, 0, 16256, 0, 16256, 0, 16128, 0, 16256, 0, 16256, 0, 16256, 0, 16256, 0, 16256, 0, 16256, 0, 16256, 0, 16256, 0, 16256, 0, 16256, 0, 16256, 0, 16256, 0, 16256, 0, 16256, 0, 16256, 0, 16256, 0, 16256, 0, 16256, 0, 16256, 0, 16256, 0, 16256, 0, 16256, 0, 16256, 0, 16256, 0, 16256, 0, 16256, 0, 16256, 0, 16128, 0, 16256, 0, 16256, 0, 16256, 0, 16128, 0, 16256, 0, 16256, 0, 16256, 0, 16256, 0, 16256, 0, 16256, 0, 16256, 0, 16256, 0, 16256, 0, 16256, 0, 16256, 0, 16256, 0, 16256, 0, 16256, 0, 16256, 0, 16256, 0, 16256, 0, 16256, 0, 16256, 0, 16256, 0, 16256, 0, 16256, 0, 16256, 0, 16256, 0, 16256, 0, 16128, 0, 16256, 0, 16256, 0, 16256, 0, 16128, 0, 16256, 0, 16256, 0, 16256, 0, 16256, 0, 16256, 0, 16256, 0, 16256, 0, 16256, 0, 16256, 0, 16256, 0, 16256, 0, 16256, 0, 16256, 0, 16256, 0, 16256, 0, 16256, 0, 16256, 0, 16256, 0, 16256, 0, 0, 0, 0, 0, 0, 0, 0, 0, 0, 0, 0, 0, 0, 0, 0, 0, 0, 0, 0, 0, 0, 0, 0, 0, 0, 0, 0, 0, 0, 0, 0, 0, 0, 0, 0, 0, 0, 0, 0, 0, 0, 0, 0, 0, 0, 0, 0, 0, 0, 0, 0, 0, 0, 0, 0, 0, 0, 0, 0, 0, 0, 0, 0, 0, 0, 0, 0, 0, 0, 0, 0, 0, 0, 0, 0, 0, 0, 0, 0, 0, 0, 0, 0, 0, 0, 0, 0, 0, 0, 0, 0, 0, 0, 0, 0, 0, 0, 0, 0, 0, 0, 0, 0, 0, 0, 0, 0, 0, 0, 0, 0, 0, 0, 0, 0, 0, 0, 0, 0, 0, 0, 0, 0, 0, 0, 0, 0, 0, 16768, 0, 16768, 0, 16768, 0, 16768, 0, 16768, 0, 16768, 0, 16768, 0, 16768, 0, 16256, 0, 16256, 0, 16128, 0, 16256, 0, 16256, 0, 16256, 0, 16128, 0, 16256, 0, 16768, 0, 16768, 0, 16768, 0, 16768, 0, 16768, 0, 16768, 0, 16768, 0, 16768, 0, 16256, 0, 16256, 0, 16256, 0, 16256, 0, 16256, 0, 16256, 0, 16256, 0, 16256, 0, 16128, 0, 16256, 0, 16256, 0, 16256, 0, 16128, 0, 16256, 0, 16256, 0, 16256, 0, 16256, 0, 16256, 0, 16256, 0, 16256, 0, 16256, 0, 16256, 0, 16256, 0, 16256, 0, 16256, 0, 16256, 0, 16256, 0, 16256, 0, 16256, 0, 16256, 0, 16256, 0, 16256, 0, 16256, 0, 16256, 0, 16256, 0, 16256, 0, 16256, 0, 16256, 0, 16256, 0, 16256, 0, 16256, 0, 16256, 0, 16256, 0, 16256, 0, 16256, 0, 16256, 0, 16256, 0, 16256, 0, 16128, 0, 16256, 0, 16256, 0, 16256, 0, 16128, 0, 16256, 0, 16256, 0, 16256, 0, 16256, 0, 16256, 0, 16256, 0, 16256, 0, 16256, 0, 16256, 0, 16256, 0, 16256, 0, 16256, 0, 16256, 0, 16256, 0, 16256, 0, 16256, 0, 16256, 0, 16256, 0, 16256, 0, 16256, 0, 16256, 0, 16256, 0, 16256, 0, 16256, 0, 16256, 0, 16256, 0, 16256, 0, 16256, 0, 16256, 0, 16128, 0, 16256, 0, 16256, 0, 16256, 0, 16128, 0, 16256, 0, 16256, 0, 16256, 0, 16256, 0, 16256, 0, 16256, 0, 16256, 0, 16256, 0, 16256, 0, 16256, 0, 16256, 0, 16256, 0, 16256, 0, 16256, 0, 16256, 0, 16256, 0, 16256, 0, 16256, 0, 16256, 0, 16256, 0, 16256, 0, 16256, 0, 16256, 0, 16256, 0, 16256, 0, 16256, 0, 16256, 0, 16128, 0, 16256, 0, 16256, 0, 16256, 0, 16128, 0, 16256, 0, 16256, 0, 16256, 0, 16256, 0, 16256, 0, 16256, 0, 16256, 0, 16256, 0, 16256, 0, 16256, 0, 16256, 0, 16256, 0, 16256, 0, 16256, 0, 16256, 0, 16256, 0, 16256, 0, 16256, 0, 16256, 0, 16256, 0, 16256, 0, 16256, 0, 16256, 0, 16256, 0, 16256, 0, 16128, 0, 16256, 0, 16256, 0, 16256, 0, 16128, 0, 16256, 0, 16256, 0, 16256, 0, 16256, 0, 16256, 0, 16256, 0, 16256, 0, 16256, 0, 16256, 0, 16256, 0, 16256, 0, 16256, 0, 16256, 0, 16256, 0, 16256, 0, 16256, 0, 16256, 0, 16256, 0, 16256, 0, 0, 0, 0, 0, 0, 0, 0, 0, 0, 0, 0, 0, 0, 0, 0, 0, 0, 0, 0, 0, 0, 0, 0, 0, 0, 0, 0, 0, 0, 0, 0, 0, 0, 0, 0, 0, 0, 0, 0, 0, 0, 0, 0, 0, 0, 0, 0, 0, 0, 0, 0, 0, 0, 0, 0, 0, 0, 0, 0, 0, 0, 0, 0, 0, 0, 0, 0, 0, 0, 0, 0, 0, 0, 0, 0, 0, 0, 0, 0, 0, 0, 0, 0, 0, 0, 0, 0, 0, 0, 0, 0, 0, 0, 0, 0, 0, 0, 0, 0, 0, 0, 0, 0, 0, 0, 0, 0, 0, 0, 0, 0, 0, 0, 0, 0, 0, 0, 0, 0, 0, 0, 0, 0, 0, 0, 0, 0, 0, 16768, 0, 16768, 0, 16768, 0, 16768, 0, 16768, 0, 0, 0, 0, 0, 0, 0, 16256, 0, 16256, 0, 16128, 0, 16256, 0, 16128, 0, 16256, 0, 16128, 0, 16256, 0, 16768, 0, 16768, 0, 16768, 0, 16768, 0, 16768, 0, 0, 0, 0, 0, 0, 0, 16256, 0, 16256, 0, 16256, 0, 16256, 0, 16128, 0, 17280, 0, 16256, 0, 16256, 0, 16128, 0, 16256, 0, 16256, 0, 16256, 0, 16128, 0, 16256, 0, 16256, 0, 16256, 0, 16256, 0, 16256, 0, 16256, 0, 16256, 0, 16256, 0, 16256, 0, 16256, 0, 16256, 0, 16256, 0, 16256, 0, 16256, 0, 16256, 0, 16256, 0, 16256, 0, 16256, 0, 16256, 0, 16256, 0, 16256, 0, 16256, 0, 16256, 0, 16256, 0, 16256, 0, 16256, 0, 16256, 0, 16256, 0, 16256, 0, 16256, 0, 16256, 0, 16256, 0, 16256, 0, 16256, 0, 16256, 0, 16128, 0, 16256, 0, 16256, 0, 16256, 0, 16128, 0, 16256, 0, 16256, 0, 16256, 0, 16256, 0, 16256, 0, 16256, 0, 16256, 0, 16256, 0, 16256, 0, 16256, 0, 16256, 0, 16256, 0, 16256, 0, 16256, 0, 16256, 0, 16256, 0, 16256, 0, 16256, 0, 16256, 0, 16256, 0, 16256, 0, 16256, 0, 16256, 0, 16256, 0, 16256, 0, 16256, 0, 16256, 0, 16256, 0, 16256, 0, 16128, 0, 16256, 0, 16256, 0, 16256, 0, 16128, 0, 16256, 0, 16256, 0, 16256, 0, 16256, 0, 16256, 0, 16256, 0, 16256, 0, 16256, 0, 16256, 0, 16256, 0, 16256, 0, 16256, 0, 16256, 0, 16256, 0, 16256, 0, 16256, 0, 16256, 0, 16256, 0, 16256, 0, 16256, 0, 16256, 0, 16256, 0, 16256, 0, 16256, 0, 16256, 0, 16256, 0, 16256, 0, 16128, 0, 16256, 0, 16256, 0, 16256, 0, 16128, 0, 16256, 0, 16256, 0, 16256, 0, 16256, 0, 16256, 0, 16256, 0, 16256, 0, 16256, 0, 16256, 0, 16256, 0, 16256, 0, 16256, 0, 16256, 0, 16256, 0, 16256, 0, 16256, 0, 16256, 0, 16256, 0, 16256, 0, 16256, 0, 16256, 0, 16256, 0, 16256, 0, 16256, 0, 16256, 0, 16128, 0, 16256, 0, 16256, 0, 16256, 0, 16128, 0, 16256, 0, 16256, 0, 16256, 0, 16256, 0, 16256, 0, 16256, 0, 16256, 0, 16256, 0, 16256, 0, 16256, 0, 16256, 0, 16256, 0, 16256, 0, 16256, 0, 16256, 0, 16256, 0, 16256, 0, 16256, 0, 16256, 0, 0, 0, 0, 0, 0, 0, 0, 0, 0, 0, 0, 0, 0, 0, 0, 0, 0, 0, 0, 0, 0, 0, 0, 0, 0, 0, 0, 0, 0, 0, 0, 0, 0, 0, 0, 0, 0, 0, 0, 0, 0, 0, 0, 0, 0, 0, 0, 0, 0, 0, 0, 0, 0, 0, 0, 0, 0, 0, 0, 0, 0, 0, 0, 0, 0, 0, 0, 0, 0, 0, 0, 0, 0, 0, 0, 0, 0, 0, 0, 0, 0, 0, 0, 0, 0, 0, 0, 0, 0, 0, 0, 0, 0, 0, 0, 0, 0, 0, 0, 0, 0, 0, 0, 0, 0, 0, 0, 0, 0, 0, 0, 0, 0, 0, 0, 0, 0, 0, 0, 0, 0, 0, 0, 0, 0, 0, 0};

__global__ __launch_bounds__(512) void xd_kernel(const int* idx, const u16* w1, const u16* table, const u16* b1, const u16* ws_cin, const u16* wout, char* ws, const u16* w2, const u16* b_out, u16* out) {
  __shared__ __attribute__((aligned(1024))) char shm[163840];
  unsigned tid = threadIdx.x;
  asm volatile(
    "s_load_dwordx16 s[4:19], s[0:1], 0\n"
    ".Lxd_top:\n"
    "v_readfirstlane_b32 s43, v0\n"
    "s_setprio 3\n"
    "s_cmp_lt_u32 s2, 8\n"
    "s_cbranch_scc1 .Lxd_s2\n"
    "s_cmp_lt_u32 s2, 48\n"
    "s_cbranch_scc1 .Lxd_help\n"
    "s_and_b32 s24, s2, 15\n"
    "s_mul_i32 s40, s43, 36\n"
    "s_mul_i32 s41, s24, 26624\n"
    "s_add_u32 s38, s40, s41\n"
    "v_lshlrev_b32 v86, 4, v0\n"
    "s_lshl_b32 s34, s43, 6\n"
    "s_add_u32 m0, s34, 98304\n"
    "s_waitcnt lgkmcnt(0)\n"
    "s_add_u32 s38, s4, s38\n"
    "s_addc_u32 s39, s5, 0\n"
    "global_load_lds_dwordx4 v86, s[38:39] offset:0\n"
    "global_load_lds_dwordx4 v86, s[38:39] offset:1024\n"
    "global_load_lds_dwordx4 v86, s[38:39] offset:2048\n"
    "global_load_lds_dwordx4 v86, s[38:39] offset:2304\n"
    "s_sub_u32 s3, s2, 48\n"
    "s_and_b32 s28, s2, 7\n"
    "s_lshr_b32 s23, s3, 4\n"
    "s_mul_i32 s42, s28, 3\n"
    "v_bfe_u32 v85, v0, 4, 2\n"
    "v_lshlrev_b32 v87, 1, v0\n"
    "v_and_b32 v87, 0xe0, v87\n"
    "v_and_b32 v88, 0xf0, v86\n"
    "v_xor_b32 v87, v87, v88\n"
    "s_movk_i32 s41, 3328\n"
    "v_mad_u32_u24 v46, v85, s41, v87\n"
    "v_add_u32 v47, 12288, v46\n"
    "s_mul_i32 s41, s43, 416\n"
    "s_lshl_b32 s40, s23, 8\n"
    "s_add_u32 s41, s41, s40\n"
    "s_mul_i32 s30, s42, 212992\n"
    "s_lshl_b32 s35, s43, 5\n"
    "s_bitset1_b32 s35, 15\n"
    "s_add_u32 s6, s6, s41\n"
    "s_addc_u32 s7, s7, 0\n"
    "s_add_u32 s30, s6, s30\n"
    "s_addc_u32 s31, s7, 0\n"
    "s_add_u32 m0, s35, 0\n"
    "s_nop 0\n"
    "global_load_lds_dwordx4 v46, s[30:31] offset:0\n"
    "global_load_lds_dwordx4 v47, s[30:31] offset:1024\n"
    "s_add_u32 s30, s30, 212992\n"
    "s_addc_u32 s31, s31, 0\n"
    "s_setprio 2\n"
    "v_and_b32 v84, 3, v0\n"
    "v_bfe_u32 v44, v0, 3, 3\n"
    "v_lshl_add_u32 v44, v84, 3, v44\n"
    "s_add_u32 s40, s34, 98304\n"
    "v_mul_u32_u24 v44, 104, v44\n"
    "v_add_u32 v44, s40, v44\n"
    "v_lshl_add_u32 v45, s42, 2, v44\n"
    "v_xor_b32 v51, v0, v86\n"
    "v_and_b32 v86, 0x70, v86\n"
    "v_bfi_b32 v86, 48, v51, v86\n"
    "v_xor_b32 v51, 64, v86\n"
    "v_add_u32 v48, 3072, v86\n"
    "v_add_u32 v49, 2048, v51\n"
    "v_add_u32 v50, 1024, v86\n"
    "s_sub_u32 s36, s8, 3072\n"
    "s_subb_u32 s37, s9, 0\n"
    "s_waitcnt vmcnt(2)\n"
    "ds_read2_b32 v[96:97], v45 offset0:0 offset1:1\n"
    "ds_read_b32 v98, v45 offset:8\n"
    "s_lshr_b32 s20, s43, 6\n"
    "s_lshr_b32 s21, s43, 8\n"
    "v_and_b32 v1, 63, v0\n"
    "s_waitcnt lgkmcnt(0)\n"
    "v_lshlrev_b32 v96, 7, v96\n"
    "v_lshlrev_b32 v97, 7, v97\n"
    "v_lshlrev_b32 v98, 7, v98\n"
    "v_add_u32_dpp v52, v96, v48 quad_perm:[0,0,0,0] row_mask:0xf bank_mask:0xf\n"
    "v_add_u32_dpp v53, v96, v49 quad_perm:[1,1,1,1] row_mask:0xf bank_mask:0xf\n"
    "v_add_u32_dpp v54, v96, v50 quad_perm:[2,2,2,2] row_mask:0xf bank_mask:0xf\n"
    "v_add_u32_dpp v55, v96, v51 quad_perm:[3,3,3,3] row_mask:0xf bank_mask:0xf\n"
    "s_add_u32 m0, s34, 0\n"
    "s_nop 0\n"
    "global_load_lds_dwordx4 v52, s[36:37] offset:0\n"
    "global_load_lds_dwordx4 v53, s[36:37] offset:1024\n"
    "global_load_lds_dwordx4 v54, s[36:37] offset:2048\n"
    "global_load_lds_dwordx4 v55, s[36:37] offset:3072\n"
    "s_setprio 1\n"
    "v_add_u32_dpp v52, v97, v48 quad_perm:[0,0,0,0] row_mask:0xf bank_mask:0xf\n"
    "v_add_u32_dpp v53, v97, v49 quad_perm:[1,1,1,1] row_mask:0xf bank_mask:0xf\n"
    "v_add_u32_dpp v54, v97, v50 quad_perm:[2,2,2,2] row_mask:0xf bank_mask:0xf\n"
    "v_add_u32_dpp v55, v97, v51 quad_perm:[3,3,3,3] row_mask:0xf bank_mask:0xf\n"
    "s_load_dwordx4 s[68:71], s[0:1], 64\n"
    "s_add_u32 m0, s35, 49152\n"
    "s_nop 0\n"
    "global_load_lds_dwordx4 v46, s[30:31] offset:0\n"
    "global_load_lds_dwordx4 v47, s[30:31] offset:1024\n"
    "s_add_u32 s30, s30, 212992\n"
    "s_addc_u32 s31, s31, 0\n"
    "s_add_u32 s40, s28, 1\n"
    "s_and_b32 s40, s40, 7\n"
    "v_mad_u32_u24 v45, s40, 12, v44\n"
    "ds_read2_b32 v[100:101], v45 offset0:1 offset1:2\n"
    "ds_read_b32 v99, v45 offset:0\n"
    "s_add_u32 s40, s28, 2\n"
    "s_and_b32 s40, s40, 7\n"
    "v_mad_u32_u24 v45, s40, 12, v44\n"
    "ds_read2_b32 v[102:103], v45 offset0:0 offset1:1\n"
    "ds_read_b32 v104, v45 offset:8\n"
    "s_add_u32 s40, s28, 3\n"
    "s_and_b32 s40, s40, 7\n"
    "v_mad_u32_u24 v45, s40, 12, v44\n"
    "ds_read2_b32 v[106:107], v45 offset0:1 offset1:2\n"
    "ds_read_b32 v105, v45 offset:0\n"
    "s_add_u32 s40, s28, 4\n"
    "s_and_b32 s40, s40, 7\n"
    "v_mad_u32_u24 v45, s40, 12, v44\n"
    "ds_read2_b32 v[108:109], v45 offset0:0 offset1:1\n"
    "ds_read_b32 v110, v45 offset:8\n"
    "s_add_u32 s40, s28, 5\n"
    "s_and_b32 s40, s40, 7\n"
    "v_mad_u32_u24 v45, s40, 12, v44\n"
    "ds_read2_b32 v[112:113], v45 offset0:1 offset1:2\n"
    "ds_read_b32 v111, v45 offset:0\n"
    "s_add_u32 s40, s28, 6\n"
    "s_and_b32 s40, s40, 7\n"
    "v_mad_u32_u24 v45, s40, 12, v44\n"
    "ds_read2_b32 v[114:115], v45 offset0:0 offset1:1\n"
    "ds_read_b32 v116, v45 offset:8\n"
    "s_add_u32 s40, s28, 7\n"
    "s_and_b32 s40, s40, 7\n"
    "v_mad_u32_u24 v45, s40, 12, v44\n"
    "ds_read2_b32 v[118:119], v45 offset0:1 offset1:2\n"
    "ds_read_b32 v117, v45 offset:0\n"
    "ds_read_b64 v[120:121], v44 offset:96\n"
    "v_lshrrev_b32 v86, 5, v88\n"
    "v_xor_b32 v86, v86, v85\n"
    "v_lshlrev_b32 v86, 4, v86\n"
    "v_lshl_add_u32 v86, v88, 3, v86\n"
    "s_and_b32 s41, s20, 1\n"
    "s_lshl1_add_u32 s40, s21, s41\n"
    "v_lshl_add_u32 v56, s40, 13, v86\n"
    "v_xor_b32 v57, 64, v56\n"
    "v_lshrrev_b32 v86, 6, v88\n"
    "v_lshl_add_u32 v88, v85, 3, v86\n"
    "v_bfe_u32 v86, v0, 2, 3\n"
    "s_and_b32 s40, s20, 2\n"
    "s_lshl1_add_u32 s40, s40, 1024\n"
    "v_xor_b32 v86, s40, v86\n"
    "v_lshlrev_b32 v86, 5, v86\n"
    "v_lshl_add_u32 v86, v88, 8, v86\n"
    "v_lshl_add_u32 v76, v84, 3, v86\n"
    "v_xor_b32 v77, 32, v76\n"
    "v_xor_b32 v78, 64, v76\n"
    "v_xor_b32 v79, 96, v76\n"
    "v_lshlrev_b32 v92, 2, v0\n"
    "v_add_u32_e64 v93, s21, -1\n"
    "v_add_u32 v92, 149504, v92\n"
    "ds_write_b32 v92, v93\n"
    "s_waitcnt lgkmcnt(0)\n"
    "v_lshlrev_b32 v99, 7, v99\n"
    "s_setprio 0\n"
    "s_cmp_eq_u32 s21, 0\n"
    "s_cbranch_scc1 .Lxd_g0\n"
    "s_waitcnt vmcnt(6)\n"
    "s_barrier\n"
    ".Lxd_g0:\n"
    "s_add_u32 m0, s34, 49152\n"
    "s_nop 0\n"
    "global_load_lds_dwordx4 v52, s[36:37] offset:0\n"
    "global_load_lds_dwordx4 v53, s[36:37] offset:1024\n"
    "global_load_lds_dwordx4 v54, s[36:37] offset:2048\n"
    "global_load_lds_dwordx4 v55, s[36:37] offset:3072\n"
    "v_add_u32_dpp v52, v98, v48 quad_perm:[0,0,0,0] row_mask:0xf bank_mask:0xf\n"
    "v_add_u32_dpp v53, v98, v49 quad_perm:[1,1,1,1] row_mask:0xf bank_mask:0xf\n"
    "v_add_u32_dpp v54, v98, v50 quad_perm:[2,2,2,2] row_mask:0xf bank_mask:0xf\n"
    "v_add_u32_dpp v55, v98, v51 quad_perm:[3,3,3,3] row_mask:0xf bank_mask:0xf\n"
    "s_waitcnt vmcnt(6) lgkmcnt(0)\n"
    "s_barrier\n"
    "ds_read_b128 v[128:131], v56 offset:0\n"
    "ds_read_b128 v[132:135], v56 offset:2048\n"
    "ds_read_b128 v[136:139], v56 offset:4096\n"
    "ds_read_b128 v[140:143], v56 offset:6144\n"
    "ds_read_b64_tr_b16 v[144:145], v76 offset:0\n"
    "ds_read_b64_tr_b16 v[146:147], v76 offset:1024\n"
    "ds_read_b64_tr_b16 v[148:149], v77 offset:0\n"
    "ds_read_b64_tr_b16 v[150:151], v77 offset:1024\n"
    "ds_read_b64_tr_b16 v[152:153], v78 offset:0\n"
    "ds_read_b64_tr_b16 v[154:155], v78 offset:1024\n"
    "ds_read_b64_tr_b16 v[156:157], v79 offset:0\n"
    "ds_read_b64_tr_b16 v[158:159], v79 offset:1024\n"
    "ds_read_b128 v[160:163], v57 offset:0\n"
    "ds_read_b128 v[164:167], v57 offset:2048\n"
    "ds_read_b128 v[168:171], v57 offset:4096\n"
    "ds_read_b128 v[172:175], v57 offset:6144\n"
    "ds_read_b64_tr_b16 v[176:177], v76 offset:8192\n"
    "ds_read_b64_tr_b16 v[178:179], v76 offset:9216\n"
    "ds_read_b64_tr_b16 v[180:181], v77 offset:8192\n"
    "ds_read_b64_tr_b16 v[182:183], v77 offset:9216\n"
    "ds_read_b64_tr_b16 v[184:185], v78 offset:8192\n"
    "ds_read_b64_tr_b16 v[186:187], v78 offset:9216\n"
    "ds_read_b64_tr_b16 v[188:189], v79 offset:8192\n"
    "ds_read_b64_tr_b16 v[190:191], v79 offset:9216\n"
    "s_add_u32 m0, s35, 98304\n"
    "s_nop 0\n"
    "global_load_lds_dwordx4 v46, s[30:31] offset:0\n"
    "global_load_lds_dwordx4 v47, s[30:31] offset:1024\n"
    "s_add_u32 s66, s28, 1\n"
    "s_and_b32 s66, s66, 7\n"
    "s_mul_i32 s66, s66, 638976\n"
    "s_add_u32 s30, s6, s66\n"
    "s_addc_u32 s31, s7, 0\n"
    "s_waitcnt vmcnt(6) lgkmcnt(0)\n"
    "s_barrier\n"
    "v_mfma_f32_16x16x32_bf16 v[192:195], v[144:147], v[128:131], 0\n"
    "v_lshlrev_b32 v100, 7, v100\n"
    "v_mfma_f32_16x16x32_bf16 v[192:195], v[176:179], v[160:163], v[192:195]\n"
    "v_add_u32_dpp v28, v99, v48 quad_perm:[0,0,0,0] row_mask:0xf bank_mask:0xf\n"
    "v_mfma_f32_16x16x32_bf16 v[196:199], v[176:179], v[164:167], 0\n"
    "v_mfma_f32_16x16x32_bf16 v[196:199], v[144:147], v[132:135], v[196:199]\n"
    "v_add_u32_dpp v29, v99, v49 quad_perm:[1,1,1,1] row_mask:0xf bank_mask:0xf\n"
    "v_mfma_f32_16x16x32_bf16 v[200:203], v[144:147], v[136:139], 0\n"
    "v_mfma_f32_16x16x32_bf16 v[200:203], v[176:179], v[168:171], v[200:203]\n"
    "v_add_u32_dpp v30, v99, v50 quad_perm:[2,2,2,2] row_mask:0xf bank_mask:0xf\n"
    "v_mfma_f32_16x16x32_bf16 v[204:207], v[176:179], v[172:175], 0\n"
    "v_mfma_f32_16x16x32_bf16 v[204:207], v[144:147], v[140:143], v[204:207]\n"
    "v_add_u32_dpp v31, v99, v51 quad_perm:[3,3,3,3] row_mask:0xf bank_mask:0xf\n"
    "v_mfma_f32_16x16x32_bf16 v[220:223], v[148:151], v[140:143], 0\n"
    "v_mfma_f32_16x16x32_bf16 v[220:223], v[180:183], v[172:175], v[220:223]\n"
    "s_bfe_u32 s40, s34, 0x1000d\n"
    "v_mfma_f32_16x16x32_bf16 v[216:219], v[180:183], v[168:171], 0\n"
    "v_mfma_f32_16x16x32_bf16 v[216:219], v[148:151], v[136:139], v[216:219]\n"
    "s_lshl1_add_u32 s40, s23, s40\n"
    "v_mfma_f32_16x16x32_bf16 v[212:215], v[148:151], v[132:135], 0\n"
    "v_mfma_f32_16x16x32_bf16 v[212:215], v[180:183], v[164:167], v[212:215]\n"
    "s_lshl_b32 s40, s40, 7\n"
    "v_mfma_f32_16x16x32_bf16 v[208:211], v[180:183], v[160:163], 0\n"
    "v_mfma_f32_16x16x32_bf16 v[208:211], v[148:151], v[128:131], v[208:211]\n"
    "v_lshl_add_u32 v42, v85, 3, s40\n"
    "v_mfma_f32_16x16x32_bf16 v[224:227], v[152:155], v[128:131], 0\n"
    "v_mfma_f32_16x16x32_bf16 v[224:227], v[184:187], v[160:163], v[224:227]\n"
    "s_lshl_b32 s24, s24, 8\n"
    "v_mfma_f32_16x16x32_bf16 v[228:231], v[184:187], v[164:167], 0\n"
    "v_mfma_f32_16x16x32_bf16 v[228:231], v[152:155], v[132:135], v[228:231]\n"
    "v_add_u32 v58, 98304, v56\n"
    "v_mfma_f32_16x16x32_bf16 v[232:235], v[152:155], v[136:139], 0\n"
    "v_mfma_f32_16x16x32_bf16 v[232:235], v[184:187], v[168:171], v[232:235]\n"
    "v_add_u32 v59, 98304, v57\n"
    "v_mfma_f32_16x16x32_bf16 v[236:239], v[184:187], v[172:175], 0\n"
    "v_mfma_f32_16x16x32_bf16 v[236:239], v[152:155], v[140:143], v[236:239]\n"
    "v_add_u32 v80, 98304, v76\n"
    "v_mfma_f32_16x16x32_bf16 v[252:255], v[156:159], v[140:143], 0\n"
    "v_mfma_f32_16x16x32_bf16 v[252:255], v[188:191], v[172:175], v[252:255]\n"
    "v_add_u32 v81, 98304, v77\n"
    "v_mfma_f32_16x16x32_bf16 v[248:251], v[188:191], v[168:171], 0\n"
    "v_mfma_f32_16x16x32_bf16 v[248:251], v[156:159], v[136:139], v[248:251]\n"
    "v_add_u32 v82, 98304, v78\n"
    "v_mfma_f32_16x16x32_bf16 v[244:247], v[156:159], v[132:135], 0\n"
    "v_mfma_f32_16x16x32_bf16 v[244:247], v[188:191], v[164:167], v[244:247]\n"
    "v_add_u32 v83, 98304, v79\n"
    "v_mfma_f32_16x16x32_bf16 v[240:243], v[188:191], v[160:163], 0\n"
    "v_mfma_f32_16x16x32_bf16 v[240:243], v[156:159], v[128:131], v[240:243]\n"
    "s_waitcnt vmcnt(2)\n"
    "s_barrier\n"
    "s_add_u32 m0, s34, 98304\n"
    "s_nop 0\n"
    "global_load_lds_dwordx4 v52, s[36:37] offset:0\n"
    "ds_read_b128 v[128:131], v56 offset:49152\n"
    "ds_read_b128 v[132:135], v56 offset:51200\n"
    "ds_read_b128 v[136:139], v56 offset:53248\n"
    "ds_read_b128 v[140:143], v56 offset:55296\n"
    "global_load_lds_dwordx4 v53, s[36:37] offset:1024\n"
    "ds_read_b64_tr_b16 v[144:145], v76 offset:49152\n"
    "ds_read_b64_tr_b16 v[146:147], v76 offset:50176\n"
    "ds_read_b64_tr_b16 v[148:149], v77 offset:49152\n"
    "ds_read_b64_tr_b16 v[150:151], v77 offset:50176\n"
    "global_load_lds_dwordx4 v54, s[36:37] offset:2048\n"
    "ds_read_b64_tr_b16 v[152:153], v78 offset:49152\n"
    "ds_read_b64_tr_b16 v[154:155], v78 offset:50176\n"
    "ds_read_b64_tr_b16 v[156:157], v79 offset:49152\n"
    "ds_read_b64_tr_b16 v[158:159], v79 offset:50176\n"
    "global_load_lds_dwordx4 v55, s[36:37] offset:3072\n"
    "ds_read_b128 v[160:163], v57 offset:49152\n"
    "ds_read_b128 v[164:167], v57 offset:51200\n"
    "ds_read_b128 v[168:171], v57 offset:53248\n"
    "ds_read_b128 v[172:175], v57 offset:55296\n"
    "s_add_u32 m0, s35, 0\n"
    "s_nop 0\n"
    "global_load_lds_dwordx4 v46, s[30:31] offset:0\n"
    "ds_read_b64_tr_b16 v[176:177], v76 offset:57344\n"
    "ds_read_b64_tr_b16 v[178:179], v76 offset:58368\n"
    "ds_read_b64_tr_b16 v[180:181], v77 offset:57344\n"
    "ds_read_b64_tr_b16 v[182:183], v77 offset:58368\n"
    "global_load_lds_dwordx4 v47, s[30:31] offset:1024\n"
    "s_add_u32 s30, s30, 212992\n"
    "s_addc_u32 s31, s31, 0\n"
    "ds_read_b64_tr_b16 v[184:185], v78 offset:57344\n"
    "ds_read_b64_tr_b16 v[186:187], v78 offset:58368\n"
    "ds_read_b64_tr_b16 v[188:189], v79 offset:57344\n"
    "ds_read_b64_tr_b16 v[190:191], v79 offset:58368\n"
    "s_waitcnt vmcnt(6) lgkmcnt(0)\n"
    "s_barrier\n"
    "v_mfma_f32_16x16x32_bf16 v[192:195], v[144:147], v[128:131], v[192:195]\n"
    "v_lshlrev_b32 v101, 7, v101\n"
    "v_mfma_f32_16x16x32_bf16 v[192:195], v[176:179], v[160:163], v[192:195]\n"
    "v_mfma_f32_16x16x32_bf16 v[196:199], v[176:179], v[164:167], v[196:199]\n"
    "v_add_u32_dpp v52, v100, v48 quad_perm:[0,0,0,0] row_mask:0xf bank_mask:0xf\n"
    "v_mfma_f32_16x16x32_bf16 v[196:199], v[144:147], v[132:135], v[196:199]\n"
    "v_mfma_f32_16x16x32_bf16 v[200:203], v[144:147], v[136:139], v[200:203]\n"
    "v_add_u32_dpp v53, v100, v49 quad_perm:[1,1,1,1] row_mask:0xf bank_mask:0xf\n"
    "v_mfma_f32_16x16x32_bf16 v[200:203], v[176:179], v[168:171], v[200:203]\n"
    "v_mfma_f32_16x16x32_bf16 v[204:207], v[176:179], v[172:175], v[204:207]\n"
    "v_add_u32_dpp v54, v100, v50 quad_perm:[2,2,2,2] row_mask:0xf bank_mask:0xf\n"
    "v_mfma_f32_16x16x32_bf16 v[204:207], v[144:147], v[140:143], v[204:207]\n"
    "v_mfma_f32_16x16x32_bf16 v[220:223], v[148:151], v[140:143], v[220:223]\n"
    "v_add_u32_dpp v55, v100, v51 quad_perm:[3,3,3,3] row_mask:0xf bank_mask:0xf\n"
    "v_mfma_f32_16x16x32_bf16 v[220:223], v[180:183], v[172:175], v[220:223]\n"
    "v_mfma_f32_16x16x32_bf16 v[216:219], v[180:183], v[168:171], v[216:219]\n"
    "s_mul_i32 s47, s3, 5042\n"
    "v_mfma_f32_16x16x32_bf16 v[216:219], v[148:151], v[136:139], v[216:219]\n"
    "v_mfma_f32_16x16x32_bf16 v[212:215], v[148:151], v[132:135], v[212:215]\n"
    "s_lshr_b32 s47, s47, 16\n"
    "v_mfma_f32_16x16x32_bf16 v[212:215], v[180:183], v[164:167], v[212:215]\n"
    "v_mfma_f32_16x16x32_bf16 v[208:211], v[180:183], v[160:163], v[208:211]\n"
    "s_lshr_b32 s21, s20, 2\n"
    "v_mfma_f32_16x16x32_bf16 v[208:211], v[148:151], v[128:131], v[208:211]\n"
    "v_mfma_f32_16x16x32_bf16 v[224:227], v[152:155], v[128:131], v[224:227]\n"
    "s_lshl1_add_u32 s22, s21, s20\n"
    "v_mfma_f32_16x16x32_bf16 v[224:227], v[184:187], v[160:163], v[224:227]\n"
    "v_mfma_f32_16x16x32_bf16 v[228:231], v[184:187], v[164:167], v[228:231]\n"
    "s_and_b32 s22, s22, 3\n"
    "v_mfma_f32_16x16x32_bf16 v[228:231], v[152:155], v[132:135], v[228:231]\n"
    "v_mfma_f32_16x16x32_bf16 v[232:235], v[152:155], v[136:139], v[232:235]\n"
    "s_lshl_b32 s58, s21, 8\n"
    "v_mfma_f32_16x16x32_bf16 v[232:235], v[184:187], v[168:171], v[232:235]\n"
    "v_mfma_f32_16x16x32_bf16 v[236:239], v[184:187], v[172:175], v[236:239]\n"
    "s_add_u32 s58, s58, 150528\n"
    "v_mfma_f32_16x16x32_bf16 v[236:239], v[152:155], v[140:143], v[236:239]\n"
    "v_mfma_f32_16x16x32_bf16 v[252:255], v[156:159], v[140:143], v[252:255]\n"
    "s_mov_b32 s40, s47\n"
    "v_mfma_f32_16x16x32_bf16 v[252:255], v[188:191], v[172:175], v[252:255]\n"
    "v_mfma_f32_16x16x32_bf16 v[248:251], v[188:191], v[168:171], v[248:251]\n"
    "s_mul_i32 s54, s40, 512\n"
    "v_mfma_f32_16x16x32_bf16 v[248:251], v[156:159], v[136:139], v[248:251]\n"
    "v_mfma_f32_16x16x32_bf16 v[244:247], v[156:159], v[132:135], v[244:247]\n"
    "s_add_u32 s54, s54, 2\n"
    "v_mfma_f32_16x16x32_bf16 v[244:247], v[188:191], v[164:167], v[244:247]\n"
    "v_mfma_f32_16x16x32_bf16 v[240:243], v[188:191], v[160:163], v[240:243]\n"
    "v_mfma_f32_16x16x32_bf16 v[240:243], v[156:159], v[128:131], v[240:243]\n"
    "s_waitcnt vmcnt(2)\n"
    "s_barrier\n"
    "s_add_u32 m0, s34, 0\n"
    "s_nop 0\n"
    "global_load_lds_dwordx4 v28, s[36:37] offset:0\n"
    "ds_read_b128 v[128:131], v58 offset:0\n"
    "ds_read_b128 v[132:135], v58 offset:2048\n"
    "ds_read_b128 v[136:139], v58 offset:4096\n"
    "global_load_lds_dwordx4 v29, s[36:37] offset:1024\n"
    "ds_read_b128 v[140:143], v58 offset:6144\n"
    "ds_read_b64_tr_b16 v[144:145], v80 offset:0\n"
    "ds_read_b64_tr_b16 v[146:147], v80 offset:1024\n"
    "global_load_lds_dwordx4 v30, s[36:37] offset:2048\n"
    "ds_read_b64_tr_b16 v[148:149], v81 offset:0\n"
    "ds_read_b64_tr_b16 v[150:151], v81 offset:1024\n"
    "ds_read_b64_tr_b16 v[152:153], v82 offset:0\n"
    "global_load_lds_dwordx4 v31, s[36:37] offset:3072\n"
    "ds_read_b64_tr_b16 v[154:155], v82 offset:1024\n"
    "ds_read_b64_tr_b16 v[156:157], v83 offset:0\n"
    "ds_read_b64_tr_b16 v[158:159], v83 offset:1024\n"
    "s_add_u32 m0, s35, 49152\n"
    "s_nop 0\n"
    "global_load_lds_dwordx4 v46, s[30:31] offset:0\n"
    "ds_read_b128 v[160:163], v59 offset:0\n"
    "ds_read_b128 v[164:167], v59 offset:2048\n"
    "ds_read_b128 v[168:171], v59 offset:4096\n"
    "global_load_lds_dwordx4 v47, s[30:31] offset:1024\n"
    "s_add_u32 s30, s30, 212992\n"
    "s_addc_u32 s31, s31, 0\n"
    "ds_read_b128 v[172:175], v59 offset:6144\n"
    "ds_read_b64_tr_b16 v[176:177], v80 offset:8192\n"
    "ds_read_b64_tr_b16 v[178:179], v80 offset:9216\n"
    "global_load_dwordx2 v[84:85], v42, s[10:11] offset:0\n"
    "ds_read_b64_tr_b16 v[180:181], v81 offset:8192\n"
    "ds_read_b64_tr_b16 v[182:183], v81 offset:9216\n"
    "ds_read_b64_tr_b16 v[184:185], v82 offset:8192\n"
    "ds_read_b64_tr_b16 v[186:187], v82 offset:9216\n"
    "ds_read_b64_tr_b16 v[188:189], v83 offset:8192\n"
    "ds_read_b64_tr_b16 v[190:191], v83 offset:9216\n"
    "s_waitcnt vmcnt(7) lgkmcnt(0)\n"
    "s_barrier\n"
    "v_mfma_f32_16x16x32_bf16 v[192:195], v[144:147], v[128:131], v[192:195]\n"
    "v_lshlrev_b32 v102, 7, v102\n"
    "v_mfma_f32_16x16x32_bf16 v[192:195], v[176:179], v[160:163], v[192:195]\n"
    "v_mfma_f32_16x16x32_bf16 v[196:199], v[176:179], v[164:167], v[196:199]\n"
    "v_add_u32_dpp v28, v101, v48 quad_perm:[0,0,0,0] row_mask:0xf bank_mask:0xf\n"
    "v_mfma_f32_16x16x32_bf16 v[196:199], v[144:147], v[132:135], v[196:199]\n"
    "v_mfma_f32_16x16x32_bf16 v[200:203], v[144:147], v[136:139], v[200:203]\n"
    "v_add_u32_dpp v29, v101, v49 quad_perm:[1,1,1,1] row_mask:0xf bank_mask:0xf\n"
    "v_mfma_f32_16x16x32_bf16 v[200:203], v[176:179], v[168:171], v[200:203]\n"
    "v_mfma_f32_16x16x32_bf16 v[204:207], v[176:179], v[172:175], v[204:207]\n"
    "v_add_u32_dpp v30, v101, v50 quad_perm:[2,2,2,2] row_mask:0xf bank_mask:0xf\n"
    "v_mfma_f32_16x16x32_bf16 v[204:207], v[144:147], v[140:143], v[204:207]\n"
    "v_mfma_f32_16x16x32_bf16 v[220:223], v[148:151], v[140:143], v[220:223]\n"
    "v_add_u32_dpp v31, v101, v51 quad_perm:[3,3,3,3] row_mask:0xf bank_mask:0xf\n"
    "v_mfma_f32_16x16x32_bf16 v[220:223], v[180:183], v[172:175], v[220:223]\n"
    "v_mfma_f32_16x16x32_bf16 v[216:219], v[180:183], v[168:171], v[216:219]\n"
    "s_lshl_b32 s40, s40, 7\n"
    "v_mfma_f32_16x16x32_bf16 v[216:219], v[148:151], v[136:139], v[216:219]\n"
    "v_mfma_f32_16x16x32_bf16 v[212:215], v[148:151], v[132:135], v[212:215]\n"
    "s_add_u32 s40, s40, 32768\n"
    "v_mfma_f32_16x16x32_bf16 v[212:215], v[180:183], v[164:167], v[212:215]\n"
    "v_mfma_f32_16x16x32_bf16 v[208:211], v[180:183], v[160:163], v[208:211]\n"
    "s_add_u32 s52, s16, s40\n"
    "s_addc_u32 s53, s17, 0\n"
    "v_mfma_f32_16x16x32_bf16 v[208:211], v[148:151], v[128:131], v[208:211]\n"
    "v_mfma_f32_16x16x32_bf16 v[224:227], v[152:155], v[128:131], v[224:227]\n"
    "s_mov_b32 s57, 0x7fff7fff\n"
    "v_mfma_f32_16x16x32_bf16 v[224:227], v[184:187], v[160:163], v[224:227]\n"
    "v_mfma_f32_16x16x32_bf16 v[228:231], v[184:187], v[164:167], v[228:231]\n"
    "s_mov_b32 s56, 0\n"
    "v_mfma_f32_16x16x32_bf16 v[228:231], v[152:155], v[132:135], v[228:231]\n"
    "v_mfma_f32_16x16x32_bf16 v[232:235], v[152:155], v[136:139], v[232:235]\n"
    "s_mov_b32 s27, 0\n"
    "v_mfma_f32_16x16x32_bf16 v[232:235], v[184:187], v[168:171], v[232:235]\n"
    "v_mfma_f32_16x16x32_bf16 v[236:239], v[184:187], v[172:175], v[236:239]\n"
    "s_mov_b32 s55, 9\n"
    "v_mfma_f32_16x16x32_bf16 v[236:239], v[152:155], v[140:143], v[236:239]\n"
    "v_mfma_f32_16x16x32_bf16 v[252:255], v[156:159], v[140:143], v[252:255]\n"
    "s_mov_b32 s44, 0\n"
    "v_mfma_f32_16x16x32_bf16 v[252:255], v[188:191], v[172:175], v[252:255]\n"
    "v_mfma_f32_16x16x32_bf16 v[248:251], v[188:191], v[168:171], v[248:251]\n"
    "s_mov_b32 s46, 0\n"
    "v_mfma_f32_16x16x32_bf16 v[248:251], v[156:159], v[136:139], v[248:251]\n"
    "v_mfma_f32_16x16x32_bf16 v[244:247], v[156:159], v[132:135], v[244:247]\n"
    "v_lshrrev_b32 v17, 3, v1\n"
    "v_mfma_f32_16x16x32_bf16 v[244:247], v[188:191], v[164:167], v[244:247]\n"
    "v_mfma_f32_16x16x32_bf16 v[240:243], v[188:191], v[160:163], v[240:243]\n"
    "v_mfma_f32_16x16x32_bf16 v[240:243], v[156:159], v[128:131], v[240:243]\n"
    "s_waitcnt vmcnt(3)\n"
    "s_barrier\n"
    "s_add_u32 m0, s34, 49152\n"
    "s_nop 0\n"
    "global_load_lds_dwordx4 v52, s[36:37] offset:0\n"
    "ds_read_b128 v[128:131], v56 offset:0\n"
    "ds_read_b128 v[132:135], v56 offset:2048\n"
    "ds_read_b128 v[136:139], v56 offset:4096\n"
    "global_load_lds_dwordx4 v53, s[36:37] offset:1024\n"
    "ds_read_b128 v[140:143], v56 offset:6144\n"
    "ds_read_b64_tr_b16 v[144:145], v76 offset:0\n"
    "ds_read_b64_tr_b16 v[146:147], v76 offset:1024\n"
    "global_load_lds_dwordx4 v54, s[36:37] offset:2048\n"
    "ds_read_b64_tr_b16 v[148:149], v77 offset:0\n"
    "ds_read_b64_tr_b16 v[150:151], v77 offset:1024\n"
    "ds_read_b64_tr_b16 v[152:153], v78 offset:0\n"
    "global_load_lds_dwordx4 v55, s[36:37] offset:3072\n"
    "ds_read_b64_tr_b16 v[154:155], v78 offset:1024\n"
    "ds_read_b64_tr_b16 v[156:157], v79 offset:0\n"
    "ds_read_b64_tr_b16 v[158:159], v79 offset:1024\n"
    "s_add_u32 m0, s35, 98304\n"
    "s_nop 0\n"
    "global_load_lds_dwordx4 v46, s[30:31] offset:0\n"
    "ds_read_b128 v[160:163], v57 offset:0\n"
    "ds_read_b128 v[164:167], v57 offset:2048\n"
    "ds_read_b128 v[168:171], v57 offset:4096\n"
    "global_load_lds_dwordx4 v47, s[30:31] offset:1024\n"
    "s_add_u32 s66, s28, 2\n"
    "s_and_b32 s66, s66, 7\n"
    "s_mul_i32 s66, s66, 638976\n"
    "s_add_u32 s30, s6, s66\n"
    "s_addc_u32 s31, s7, 0\n"
    "ds_read_b128 v[172:175], v57 offset:6144\n"
    "ds_read_b64_tr_b16 v[176:177], v76 offset:8192\n"
    "ds_read_b64_tr_b16 v[178:179], v76 offset:9216\n"
    "global_load_dwordx2 v[86:87], v42, s[10:11] offset:32\n"
    "ds_read_b64_tr_b16 v[180:181], v77 offset:8192\n"
    "ds_read_b64_tr_b16 v[182:183], v77 offset:9216\n"
    "ds_read_b64_tr_b16 v[184:185], v78 offset:8192\n"
    "ds_read_b64_tr_b16 v[186:187], v78 offset:9216\n"
    "ds_read_b64_tr_b16 v[188:189], v79 offset:8192\n"
    "ds_read_b64_tr_b16 v[190:191], v79 offset:9216\n"
    "s_waitcnt vmcnt(8) lgkmcnt(0)\n"
    "s_barrier\n"
    "v_mfma_f32_16x16x32_bf16 v[192:195], v[144:147], v[128:131], v[192:195]\n"
    "v_lshlrev_b32 v103, 7, v103\n"
    "v_mfma_f32_16x16x32_bf16 v[192:195], v[176:179], v[160:163], v[192:195]\n"
    "v_add_u32_dpp v52, v102, v48 quad_perm:[0,0,0,0] row_mask:0xf bank_mask:0xf\n"
    "v_mfma_f32_16x16x32_bf16 v[196:199], v[176:179], v[164:167], v[196:199]\n"
    "v_mfma_f32_16x16x32_bf16 v[196:199], v[144:147], v[132:135], v[196:199]\n"
    "v_add_u32_dpp v53, v102, v49 quad_perm:[1,1,1,1] row_mask:0xf bank_mask:0xf\n"
    "v_mfma_f32_16x16x32_bf16 v[200:203], v[144:147], v[136:139], v[200:203]\n"
    "v_mfma_f32_16x16x32_bf16 v[200:203], v[176:179], v[168:171], v[200:203]\n"
    "v_add_u32_dpp v54, v102, v50 quad_perm:[2,2,2,2] row_mask:0xf bank_mask:0xf\n"
    "v_mfma_f32_16x16x32_bf16 v[204:207], v[176:179], v[172:175], v[204:207]\n"
    "v_mfma_f32_16x16x32_bf16 v[204:207], v[144:147], v[140:143], v[204:207]\n"
    "v_add_u32_dpp v55, v102, v51 quad_perm:[3,3,3,3] row_mask:0xf bank_mask:0xf\n"
    "v_mfma_f32_16x16x32_bf16 v[220:223], v[148:151], v[140:143], v[220:223]\n"
    "v_mfma_f32_16x16x32_bf16 v[220:223], v[180:183], v[172:175], v[220:223]\n"
    "v_mul_u32_u24 v17, 12, v17\n"
    "v_mfma_f32_16x16x32_bf16 v[216:219], v[180:183], v[168:171], v[216:219]\n"
    "v_mfma_f32_16x16x32_bf16 v[216:219], v[148:151], v[136:139], v[216:219]\n"
    "v_and_b32 v19, 7, v1\n"
    "v_mfma_f32_16x16x32_bf16 v[212:215], v[148:151], v[132:135], v[212:215]\n"
    "v_mfma_f32_16x16x32_bf16 v[212:215], v[180:183], v[164:167], v[212:215]\n"
    "v_lshlrev_b32 v19, 4, v19\n"
    "v_mfma_f32_16x16x32_bf16 v[208:211], v[180:183], v[160:163], v[208:211]\n"
    "v_mfma_f32_16x16x32_bf16 v[208:211], v[148:151], v[128:131], v[208:211]\n"
    "v_lshrrev_b32 v18, 5, v1\n"
    "v_mfma_f32_16x16x32_bf16 v[224:227], v[152:155], v[128:131], v[224:227]\n"
    "v_mfma_f32_16x16x32_bf16 v[224:227], v[184:187], v[160:163], v[224:227]\n"
    "v_lshlrev_b32 v18, 2, v18\n"
    "v_mfma_f32_16x16x32_bf16 v[228:231], v[184:187], v[164:167], v[228:231]\n"
    "v_mfma_f32_16x16x32_bf16 v[228:231], v[152:155], v[132:135], v[228:231]\n"
    "v_and_b32 v20, 31, v1\n"
    "v_mfma_f32_16x16x32_bf16 v[232:235], v[152:155], v[136:139], v[232:235]\n"
    "v_mfma_f32_16x16x32_bf16 v[232:235], v[184:187], v[168:171], v[232:235]\n"
    "v_lshlrev_b32 v20, 2, v20\n"
    "v_mfma_f32_16x16x32_bf16 v[236:239], v[184:187], v[172:175], v[236:239]\n"
    "v_mfma_f32_16x16x32_bf16 v[236:239], v[152:155], v[140:143], v[236:239]\n"
    "s_lshl2_add_u32 s40, s22, s58\n"
    "v_mfma_f32_16x16x32_bf16 v[252:255], v[156:159], v[140:143], v[252:255]\n"
    "v_mfma_f32_16x16x32_bf16 v[252:255], v[188:191], v[172:175], v[252:255]\n"
    "s_sub_u32 s40, s40, 20\n"
    "v_mfma_f32_16x16x32_bf16 v[248:251], v[188:191], v[168:171], v[248:251]\n"
    "v_mfma_f32_16x16x32_bf16 v[248:251], v[156:159], v[136:139], v[248:251]\n"
    "v_lshl_add_u32 v25, v1, 18, s40\n"
    "v_mfma_f32_16x16x32_bf16 v[244:247], v[156:159], v[132:135], v[244:247]\n"
    "v_mfma_f32_16x16x32_bf16 v[244:247], v[188:191], v[164:167], v[244:247]\n"
    "s_lshl_b32 s43, s21, 16\n"
    "v_mfma_f32_16x16x32_bf16 v[240:243], v[188:191], v[160:163], v[240:243]\n"
    "v_mfma_f32_16x16x32_bf16 v[240:243], v[156:159], v[128:131], v[240:243]\n"
    "s_waitcnt vmcnt(3)\n"
    "s_barrier\n"
    "s_add_u32 m0, s34, 98304\n"
    "s_nop 0\n"
    "global_load_lds_dwordx4 v28, s[36:37] offset:0\n"
    "ds_read_b128 v[128:131], v56 offset:49152\n"
    "ds_read_b128 v[132:135], v56 offset:51200\n"
    "ds_read_b128 v[136:139], v56 offset:53248\n"
    "global_load_lds_dwordx4 v29, s[36:37] offset:1024\n"
    "ds_read_b128 v[140:143], v56 offset:55296\n"
    "ds_read_b64_tr_b16 v[144:145], v76 offset:49152\n"
    "ds_read_b64_tr_b16 v[146:147], v76 offset:50176\n"
    "global_load_lds_dwordx4 v30, s[36:37] offset:2048\n"
    "ds_read_b64_tr_b16 v[148:149], v77 offset:49152\n"
    "ds_read_b64_tr_b16 v[150:151], v77 offset:50176\n"
    "ds_read_b64_tr_b16 v[152:153], v78 offset:49152\n"
    "global_load_lds_dwordx4 v31, s[36:37] offset:3072\n"
    "ds_read_b64_tr_b16 v[154:155], v78 offset:50176\n"
    "ds_read_b64_tr_b16 v[156:157], v79 offset:49152\n"
    "ds_read_b64_tr_b16 v[158:159], v79 offset:50176\n"
    "s_add_u32 m0, s35, 0\n"
    "s_nop 0\n"
    "global_load_lds_dwordx4 v46, s[30:31] offset:0\n"
    "ds_read_b128 v[160:163], v57 offset:49152\n"
    "ds_read_b128 v[164:167], v57 offset:51200\n"
    "ds_read_b128 v[168:171], v57 offset:53248\n"
    "global_load_lds_dwordx4 v47, s[30:31] offset:1024\n"
    "s_add_u32 s30, s30, 212992\n"
    "s_addc_u32 s31, s31, 0\n"
    "ds_read_b128 v[172:175], v57 offset:55296\n"
    "ds_read_b64_tr_b16 v[176:177], v76 offset:57344\n"
    "ds_read_b64_tr_b16 v[178:179], v76 offset:58368\n"
    "global_load_dwordx2 v[88:89], v42, s[10:11] offset:64\n"
    "ds_read_b64_tr_b16 v[180:181], v77 offset:57344\n"
    "ds_read_b64_tr_b16 v[182:183], v77 offset:58368\n"
    "ds_read_b64_tr_b16 v[184:185], v78 offset:57344\n"
    "ds_read_b64_tr_b16 v[186:187], v78 offset:58368\n"
    "ds_read_b64_tr_b16 v[188:189], v79 offset:57344\n"
    "ds_read_b64_tr_b16 v[190:191], v79 offset:58368\n"
    "s_waitcnt vmcnt(8) lgkmcnt(0)\n"
    "s_barrier\n"
    "v_mfma_f32_16x16x32_bf16 v[192:195], v[144:147], v[128:131], v[192:195]\n"
    "v_lshlrev_b32 v104, 7, v104\n"
    "v_mfma_f32_16x16x32_bf16 v[192:195], v[176:179], v[160:163], v[192:195]\n"
    "v_mfma_f32_16x16x32_bf16 v[196:199], v[176:179], v[164:167], v[196:199]\n"
    "v_add_u32_dpp v28, v103, v48 quad_perm:[0,0,0,0] row_mask:0xf bank_mask:0xf\n"
    "v_mfma_f32_16x16x32_bf16 v[196:199], v[144:147], v[132:135], v[196:199]\n"
    "v_mfma_f32_16x16x32_bf16 v[200:203], v[144:147], v[136:139], v[200:203]\n"
    "v_add_u32_dpp v29, v103, v49 quad_perm:[1,1,1,1] row_mask:0xf bank_mask:0xf\n"
    "v_mfma_f32_16x16x32_bf16 v[200:203], v[176:179], v[168:171], v[200:203]\n"
    "v_mfma_f32_16x16x32_bf16 v[204:207], v[176:179], v[172:175], v[204:207]\n"
    "v_add_u32_dpp v30, v103, v50 quad_perm:[2,2,2,2] row_mask:0xf bank_mask:0xf\n"
    "v_mfma_f32_16x16x32_bf16 v[204:207], v[144:147], v[140:143], v[204:207]\n"
    "v_mfma_f32_16x16x32_bf16 v[220:223], v[148:151], v[140:143], v[220:223]\n"
    "v_add_u32_dpp v31, v103, v51 quad_perm:[3,3,3,3] row_mask:0xf bank_mask:0xf\n"
    "v_mfma_f32_16x16x32_bf16 v[220:223], v[180:183], v[172:175], v[220:223]\n"
    "v_mfma_f32_16x16x32_bf16 v[216:219], v[180:183], v[168:171], v[216:219]\n"
    "v_lshrrev_b32 v12, 4, v1\n"
    "v_mfma_f32_16x16x32_bf16 v[216:219], v[148:151], v[136:139], v[216:219]\n"
    "v_mfma_f32_16x16x32_bf16 v[212:215], v[148:151], v[132:135], v[212:215]\n"
    "v_bfe_u32 v13, v1, 2, 2\n"
    "v_mfma_f32_16x16x32_bf16 v[212:215], v[180:183], v[164:167], v[212:215]\n"
    "v_mfma_f32_16x16x32_bf16 v[208:211], v[180:183], v[160:163], v[208:211]\n"
    "v_and_b32 v21, 3, v1\n"
    "v_mfma_f32_16x16x32_bf16 v[208:211], v[148:151], v[128:131], v[208:211]\n"
    "v_mfma_f32_16x16x32_bf16 v[224:227], v[152:155], v[128:131], v[224:227]\n"
    "s_mul_i32 s40, s22, 0x2020\n"
    "v_mfma_f32_16x16x32_bf16 v[224:227], v[184:187], v[160:163], v[224:227]\n"
    "v_mfma_f32_16x16x32_bf16 v[228:231], v[184:187], v[164:167], v[228:231]\n"
    "s_and_b32 s40, s40, 0x4060\n"
    "v_mfma_f32_16x16x32_bf16 v[228:231], v[152:155], v[132:135], v[228:231]\n"
    "v_mfma_f32_16x16x32_bf16 v[232:235], v[152:155], v[136:139], v[232:235]\n"
    "s_add_u32 s40, s40, s43\n"
    "v_mfma_f32_16x16x32_bf16 v[232:235], v[184:187], v[168:171], v[232:235]\n"
    "v_mfma_f32_16x16x32_bf16 v[236:239], v[184:187], v[172:175], v[236:239]\n"
    "v_lshl_add_u32 v21, v21, 3, s40\n"
    "v_mfma_f32_16x16x32_bf16 v[236:239], v[152:155], v[140:143], v[236:239]\n"
    "v_mfma_f32_16x16x32_bf16 v[252:255], v[156:159], v[140:143], v[252:255]\n"
    "s_movk_i32 s41, 144\n"
    "v_mfma_f32_16x16x32_bf16 v[252:255], v[188:191], v[172:175], v[252:255]\n"
    "v_mfma_f32_16x16x32_bf16 v[248:251], v[188:191], v[168:171], v[248:251]\n"
    "v_add_u32 v22, v12, v13\n"
    "v_mfma_f32_16x16x32_bf16 v[248:251], v[156:159], v[136:139], v[248:251]\n"
    "v_mfma_f32_16x16x32_bf16 v[244:247], v[156:159], v[132:135], v[244:247]\n"
    "v_and_b32 v22, 3, v22\n"
    "v_mfma_f32_16x16x32_bf16 v[244:247], v[188:191], v[164:167], v[244:247]\n"
    "v_mfma_f32_16x16x32_bf16 v[240:243], v[188:191], v[160:163], v[240:243]\n"
    "v_mfma_f32_16x16x32_bf16 v[240:243], v[156:159], v[128:131], v[240:243]\n"
    "s_waitcnt vmcnt(3)\n"
    "s_barrier\n"
    "s_add_u32 m0, s34, 0\n"
    "s_nop 0\n"
    "global_load_lds_dwordx4 v52, s[36:37] offset:0\n"
    "ds_read_b128 v[128:131], v58 offset:0\n"
    "ds_read_b128 v[132:135], v58 offset:2048\n"
    "ds_read_b128 v[136:139], v58 offset:4096\n"
    "global_load_lds_dwordx4 v53, s[36:37] offset:1024\n"
    "ds_read_b128 v[140:143], v58 offset:6144\n"
    "ds_read_b64_tr_b16 v[144:145], v80 offset:0\n"
    "ds_read_b64_tr_b16 v[146:147], v80 offset:1024\n"
    "global_load_lds_dwordx4 v54, s[36:37] offset:2048\n"
    "ds_read_b64_tr_b16 v[148:149], v81 offset:0\n"
    "ds_read_b64_tr_b16 v[150:151], v81 offset:1024\n"
    "ds_read_b64_tr_b16 v[152:153], v82 offset:0\n"
    "global_load_lds_dwordx4 v55, s[36:37] offset:3072\n"
    "ds_read_b64_tr_b16 v[154:155], v82 offset:1024\n"
    "ds_read_b64_tr_b16 v[156:157], v83 offset:0\n"
    "ds_read_b64_tr_b16 v[158:159], v83 offset:1024\n"
    "s_add_u32 m0, s35, 49152\n"
    "s_nop 0\n"
    "global_load_lds_dwordx4 v46, s[30:31] offset:0\n"
    "ds_read_b128 v[160:163], v59 offset:0\n"
    "ds_read_b128 v[164:167], v59 offset:2048\n"
    "ds_read_b128 v[168:171], v59 offset:4096\n"
    "global_load_lds_dwordx4 v47, s[30:31] offset:1024\n"
    "s_add_u32 s30, s30, 212992\n"
    "s_addc_u32 s31, s31, 0\n"
    "ds_read_b128 v[172:175], v59 offset:6144\n"
    "ds_read_b64_tr_b16 v[176:177], v80 offset:8192\n"
    "ds_read_b64_tr_b16 v[178:179], v80 offset:9216\n"
    "s_add_u32 m0, s34, 49152\n"
    "s_nop 0\n"
    "global_load_lds_dwordx4 v28, s[36:37] offset:0\n"
    "ds_read_b64_tr_b16 v[180:181], v81 offset:8192\n"
    "ds_read_b64_tr_b16 v[182:183], v81 offset:9216\n"
    "ds_read_b64_tr_b16 v[184:185], v82 offset:8192\n"
    "ds_read_b64_tr_b16 v[186:187], v82 offset:9216\n"
    "ds_read_b64_tr_b16 v[188:189], v83 offset:8192\n"
    "ds_read_b64_tr_b16 v[190:191], v83 offset:9216\n"
    "s_waitcnt vmcnt(8) lgkmcnt(0)\n"
    "s_barrier\n"
    "v_mfma_f32_16x16x32_bf16 v[192:195], v[144:147], v[128:131], v[192:195]\n"
    "v_lshlrev_b32 v105, 7, v105\n"
    "v_mfma_f32_16x16x32_bf16 v[192:195], v[176:179], v[160:163], v[192:195]\n"
    "v_add_u32_dpp v52, v104, v48 quad_perm:[0,0,0,0] row_mask:0xf bank_mask:0xf\n"
    "v_mfma_f32_16x16x32_bf16 v[196:199], v[176:179], v[164:167], v[196:199]\n"
    "v_mfma_f32_16x16x32_bf16 v[196:199], v[144:147], v[132:135], v[196:199]\n"
    "v_add_u32_dpp v53, v104, v49 quad_perm:[1,1,1,1] row_mask:0xf bank_mask:0xf\n"
    "v_mfma_f32_16x16x32_bf16 v[200:203], v[144:147], v[136:139], v[200:203]\n"
    "v_mfma_f32_16x16x32_bf16 v[200:203], v[176:179], v[168:171], v[200:203]\n"
    "v_add_u32_dpp v54, v104, v50 quad_perm:[2,2,2,2] row_mask:0xf bank_mask:0xf\n"
    "v_mfma_f32_16x16x32_bf16 v[204:207], v[176:179], v[172:175], v[204:207]\n"
    "v_mfma_f32_16x16x32_bf16 v[204:207], v[144:147], v[140:143], v[204:207]\n"
    "v_add_u32_dpp v55, v104, v51 quad_perm:[3,3,3,3] row_mask:0xf bank_mask:0xf\n"
    "v_mfma_f32_16x16x32_bf16 v[220:223], v[148:151], v[140:143], v[220:223]\n"
    "v_mfma_f32_16x16x32_bf16 v[220:223], v[180:183], v[172:175], v[220:223]\n"
    "v_mad_u32_u24 v4, v22, s41, v21\n"
    "v_mfma_f32_16x16x32_bf16 v[216:219], v[180:183], v[168:171], v[216:219]\n"
    "v_mfma_f32_16x16x32_bf16 v[216:219], v[148:151], v[136:139], v[216:219]\n"
    "v_cmp_gt_u32 vcc, 2, v13\n"
    "v_mfma_f32_16x16x32_bf16 v[212:215], v[148:151], v[132:135], v[212:215]\n"
    "v_mfma_f32_16x16x32_bf16 v[212:215], v[180:183], v[164:167], v[212:215]\n"
    "v_add_u32 v24, 22, v13\n"
    "v_mfma_f32_16x16x32_bf16 v[208:211], v[180:183], v[160:163], v[208:211]\n"
    "v_mfma_f32_16x16x32_bf16 v[208:211], v[148:151], v[128:131], v[208:211]\n"
    "v_lshlrev_b32 v13, 3, v13\n"
    "v_mfma_f32_16x16x32_bf16 v[224:227], v[152:155], v[128:131], v[224:227]\n"
    "v_mfma_f32_16x16x32_bf16 v[224:227], v[184:187], v[160:163], v[224:227]\n"
    "v_cndmask_b32 v22, v24, v22, vcc\n"
    "v_mfma_f32_16x16x32_bf16 v[228:231], v[184:187], v[164:167], v[228:231]\n"
    "v_mfma_f32_16x16x32_bf16 v[228:231], v[152:155], v[132:135], v[228:231]\n"
    "v_mad_u32_u24 v3, v22, s41, v21\n"
    "v_mfma_f32_16x16x32_bf16 v[232:235], v[152:155], v[136:139], v[232:235]\n"
    "v_mfma_f32_16x16x32_bf16 v[232:235], v[184:187], v[168:171], v[232:235]\n"
    "v_cmp_eq_u32 vcc, 3, v12\n"
    "v_mfma_f32_16x16x32_bf16 v[236:239], v[184:187], v[172:175], v[236:239]\n"
    "v_mfma_f32_16x16x32_bf16 v[236:239], v[152:155], v[140:143], v[236:239]\n"
    "v_xor_b32 v22, 0, v12\n"
    "v_mfma_f32_16x16x32_bf16 v[252:255], v[156:159], v[140:143], v[252:255]\n"
    "v_mfma_f32_16x16x32_bf16 v[252:255], v[188:191], v[172:175], v[252:255]\n"
    "v_and_b32 v22, 1, v22\n"
    "v_mfma_f32_16x16x32_bf16 v[248:251], v[188:191], v[168:171], v[248:251]\n"
    "v_mfma_f32_16x16x32_bf16 v[248:251], v[156:159], v[136:139], v[248:251]\n"
    "v_lshlrev_b32 v22, 1, v22\n"
    "v_mfma_f32_16x16x32_bf16 v[244:247], v[156:159], v[132:135], v[244:247]\n"
    "v_mfma_f32_16x16x32_bf16 v[244:247], v[188:191], v[164:167], v[244:247]\n"
    "v_lshl_add_u32 v22, v12, 3, v22\n"
    "v_mfma_f32_16x16x32_bf16 v[240:243], v[188:191], v[160:163], v[240:243]\n"
    "v_mfma_f32_16x16x32_bf16 v[240:243], v[156:159], v[128:131], v[240:243]\n"
    "s_waitcnt vmcnt(3)\n"
    "s_barrier\n"
    "s_add_u32 m0, s34, 49152\n"
    "s_nop 0\n"
    "global_load_lds_dwordx4 v29, s[36:37] offset:1024\n"
    "ds_read_b128 v[128:131], v56 offset:0\n"
    "ds_read_b128 v[132:135], v56 offset:2048\n"
    "ds_read_b128 v[136:139], v56 offset:4096\n"
    "global_load_lds_dwordx4 v30, s[36:37] offset:2048\n"
    "ds_read_b128 v[140:143], v56 offset:6144\n"
    "ds_read_b64_tr_b16 v[144:145], v76 offset:0\n"
    "ds_read_b64_tr_b16 v[146:147], v76 offset:1024\n"
    "global_load_lds_dwordx4 v31, s[36:37] offset:3072\n"
    "ds_read_b64_tr_b16 v[148:149], v77 offset:0\n"
    "ds_read_b64_tr_b16 v[150:151], v77 offset:1024\n"
    "ds_read_b64_tr_b16 v[152:153], v78 offset:0\n"
    "s_add_u32 m0, s35, 98304\n"
    "s_nop 0\n"
    "global_load_lds_dwordx4 v46, s[30:31] offset:0\n"
    "ds_read_b64_tr_b16 v[154:155], v78 offset:1024\n"
    "ds_read_b64_tr_b16 v[156:157], v79 offset:0\n"
    "ds_read_b64_tr_b16 v[158:159], v79 offset:1024\n"
    "global_load_lds_dwordx4 v47, s[30:31] offset:1024\n"
    "s_add_u32 s66, s28, 3\n"
    "s_and_b32 s66, s66, 7\n"
    "s_mul_i32 s66, s66, 638976\n"
    "s_add_u32 s30, s6, s66\n"
    "s_addc_u32 s31, s7, 0\n"
    "ds_read_b128 v[160:163], v57 offset:0\n"
    "ds_read_b128 v[164:167], v57 offset:2048\n"
    "ds_read_b128 v[168:171], v57 offset:4096\n"
    "s_add_u32 m0, s34, 98304\n"
    "s_nop 0\n"
    "global_load_lds_dwordx4 v52, s[36:37] offset:0\n"
    "ds_read_b128 v[172:175], v57 offset:6144\n"
    "ds_read_b64_tr_b16 v[176:177], v76 offset:8192\n"
    "ds_read_b64_tr_b16 v[178:179], v76 offset:9216\n"
    "global_load_lds_dwordx4 v53, s[36:37] offset:1024\n"
    "ds_read_b64_tr_b16 v[180:181], v77 offset:8192\n"
    "ds_read_b64_tr_b16 v[182:183], v77 offset:9216\n"
    "ds_read_b64_tr_b16 v[184:185], v78 offset:8192\n"
    "ds_read_b64_tr_b16 v[186:187], v78 offset:9216\n"
    "ds_read_b64_tr_b16 v[188:189], v79 offset:8192\n"
    "ds_read_b64_tr_b16 v[190:191], v79 offset:9216\n"
    "s_waitcnt vmcnt(8) lgkmcnt(0)\n"
    "s_barrier\n"
    "v_mfma_f32_16x16x32_bf16 v[192:195], v[144:147], v[128:131], v[192:195]\n"
    "v_lshlrev_b32 v106, 7, v106\n"
    "v_mfma_f32_16x16x32_bf16 v[192:195], v[176:179], v[160:163], v[192:195]\n"
    "v_mfma_f32_16x16x32_bf16 v[196:199], v[176:179], v[164:167], v[196:199]\n"
    "v_add_u32_dpp v28, v105, v48 quad_perm:[0,0,0,0] row_mask:0xf bank_mask:0xf\n"
    "v_mfma_f32_16x16x32_bf16 v[196:199], v[144:147], v[132:135], v[196:199]\n"
    "v_mfma_f32_16x16x32_bf16 v[200:203], v[144:147], v[136:139], v[200:203]\n"
    "v_add_u32_dpp v29, v105, v49 quad_perm:[1,1,1,1] row_mask:0xf bank_mask:0xf\n"
    "v_mfma_f32_16x16x32_bf16 v[200:203], v[176:179], v[168:171], v[200:203]\n"
    "v_mfma_f32_16x16x32_bf16 v[204:207], v[176:179], v[172:175], v[204:207]\n"
    "v_add_u32_dpp v30, v105, v50 quad_perm:[2,2,2,2] row_mask:0xf bank_mask:0xf\n"
    "v_mfma_f32_16x16x32_bf16 v[204:207], v[144:147], v[140:143], v[204:207]\n"
    "v_mfma_f32_16x16x32_bf16 v[220:223], v[148:151], v[140:143], v[220:223]\n"
    "v_add_u32_dpp v31, v105, v51 quad_perm:[3,3,3,3] row_mask:0xf bank_mask:0xf\n"
    "v_mfma_f32_16x16x32_bf16 v[220:223], v[180:183], v[172:175], v[220:223]\n"
    "v_mfma_f32_16x16x32_bf16 v[216:219], v[180:183], v[168:171], v[216:219]\n"
    "s_mov_b32 s40, 0x05040100\n"
    "v_mfma_f32_16x16x32_bf16 v[216:219], v[148:151], v[136:139], v[216:219]\n"
    "v_mfma_f32_16x16x32_bf16 v[212:215], v[148:151], v[132:135], v[212:215]\n"
    "v_bfe_u32 v24, s40, v13, 8\n"
    "v_mfma_f32_16x16x32_bf16 v[212:215], v[180:183], v[164:167], v[212:215]\n"
    "v_mfma_f32_16x16x32_bf16 v[208:211], v[180:183], v[160:163], v[208:211]\n"
    "v_add_u32 v22, v22, v24\n"
    "v_mfma_f32_16x16x32_bf16 v[208:211], v[148:151], v[128:131], v[208:211]\n"
    "v_mfma_f32_16x16x32_bf16 v[224:227], v[152:155], v[128:131], v[224:227]\n"
    "s_mov_b32 s40, 0x1a191818\n"
    "v_mfma_f32_16x16x32_bf16 v[224:227], v[184:187], v[160:163], v[224:227]\n"
    "v_mfma_f32_16x16x32_bf16 v[228:231], v[184:187], v[164:167], v[228:231]\n"
    "v_bfe_u32 v24, s40, v13, 8\n"
    "v_mfma_f32_16x16x32_bf16 v[228:231], v[152:155], v[132:135], v[228:231]\n"
    "v_mfma_f32_16x16x32_bf16 v[232:235], v[152:155], v[136:139], v[232:235]\n"
    "v_cndmask_b32 v22, v22, v24, vcc\n"
    "v_mfma_f32_16x16x32_bf16 v[232:235], v[184:187], v[168:171], v[232:235]\n"
    "v_mfma_f32_16x16x32_bf16 v[236:239], v[184:187], v[172:175], v[236:239]\n"
    "v_mad_u32_u24 v5, v22, s41, v21\n"
    "v_mfma_f32_16x16x32_bf16 v[236:239], v[152:155], v[140:143], v[236:239]\n"
    "v_mfma_f32_16x16x32_bf16 v[252:255], v[156:159], v[140:143], v[252:255]\n"
    "v_xor_b32 v22, 1, v12\n"
    "v_mfma_f32_16x16x32_bf16 v[252:255], v[188:191], v[172:175], v[252:255]\n"
    "v_mfma_f32_16x16x32_bf16 v[248:251], v[188:191], v[168:171], v[248:251]\n"
    "v_and_b32 v22, 1, v22\n"
    "v_mfma_f32_16x16x32_bf16 v[248:251], v[156:159], v[136:139], v[248:251]\n"
    "v_mfma_f32_16x16x32_bf16 v[244:247], v[156:159], v[132:135], v[244:247]\n"
    "v_lshlrev_b32 v22, 1, v22\n"
    "v_mfma_f32_16x16x32_bf16 v[244:247], v[188:191], v[164:167], v[244:247]\n"
    "v_mfma_f32_16x16x32_bf16 v[240:243], v[188:191], v[160:163], v[240:243]\n"
    "v_mfma_f32_16x16x32_bf16 v[240:243], v[156:159], v[128:131], v[240:243]\n"
    "s_waitcnt vmcnt(4)\n"
    "s_barrier\n"
    "s_add_u32 m0, s34, 98304\n"
    "s_nop 0\n"
    "global_load_lds_dwordx4 v54, s[36:37] offset:2048\n"
    "ds_read_b128 v[128:131], v56 offset:49152\n"
    "ds_read_b128 v[132:135], v56 offset:51200\n"
    "ds_read_b128 v[136:139], v56 offset:53248\n"
    "global_load_lds_dwordx4 v55, s[36:37] offset:3072\n"
    "ds_read_b128 v[140:143], v56 offset:55296\n"
    "ds_read_b64_tr_b16 v[144:145], v76 offset:49152\n"
    "ds_read_b64_tr_b16 v[146:147], v76 offset:50176\n"
    "s_add_u32 m0, s35, 0\n"
    "s_nop 0\n"
    "global_load_lds_dwordx4 v46, s[30:31] offset:0\n"
    "ds_read_b64_tr_b16 v[148:149], v77 offset:49152\n"
    "ds_read_b64_tr_b16 v[150:151], v77 offset:50176\n"
    "ds_read_b64_tr_b16 v[152:153], v78 offset:49152\n"
    "global_load_lds_dwordx4 v47, s[30:31] offset:1024\n"
    "s_add_u32 s30, s30, 212992\n"
    "s_addc_u32 s31, s31, 0\n"
    "ds_read_b64_tr_b16 v[154:155], v78 offset:50176\n"
    "ds_read_b64_tr_b16 v[156:157], v79 offset:49152\n"
    "ds_read_b64_tr_b16 v[158:159], v79 offset:50176\n"
    "s_add_u32 m0, s34, 0\n"
    "s_nop 0\n"
    "global_load_lds_dwordx4 v28, s[36:37] offset:0\n"
    "ds_read_b128 v[160:163], v57 offset:49152\n"
    "ds_read_b128 v[164:167], v57 offset:51200\n"
    "ds_read_b128 v[168:171], v57 offset:53248\n"
    "global_load_lds_dwordx4 v29, s[36:37] offset:1024\n"
    "ds_read_b128 v[172:175], v57 offset:55296\n"
    "ds_read_b64_tr_b16 v[176:177], v76 offset:57344\n"
    "ds_read_b64_tr_b16 v[178:179], v76 offset:58368\n"
    "global_load_lds_dwordx4 v30, s[36:37] offset:2048\n"
    "ds_read_b64_tr_b16 v[180:181], v77 offset:57344\n"
    "ds_read_b64_tr_b16 v[182:183], v77 offset:58368\n"
    "ds_read_b64_tr_b16 v[184:185], v78 offset:57344\n"
    "ds_read_b64_tr_b16 v[186:187], v78 offset:58368\n"
    "ds_read_b64_tr_b16 v[188:189], v79 offset:57344\n"
    "ds_read_b64_tr_b16 v[190:191], v79 offset:58368\n"
    "s_waitcnt vmcnt(9) lgkmcnt(0)\n"
    "s_barrier\n"
    "v_mfma_f32_16x16x32_bf16 v[192:195], v[144:147], v[128:131], v[192:195]\n"
    "v_lshlrev_b32 v107, 7, v107\n"
    "v_mfma_f32_16x16x32_bf16 v[192:195], v[176:179], v[160:163], v[192:195]\n"
    "v_mfma_f32_16x16x32_bf16 v[196:199], v[176:179], v[164:167], v[196:199]\n"
    "v_add_u32_dpp v52, v106, v48 quad_perm:[0,0,0,0] row_mask:0xf bank_mask:0xf\n"
    "v_mfma_f32_16x16x32_bf16 v[196:199], v[144:147], v[132:135], v[196:199]\n"
    "v_mfma_f32_16x16x32_bf16 v[200:203], v[144:147], v[136:139], v[200:203]\n"
    "v_add_u32_dpp v53, v106, v49 quad_perm:[1,1,1,1] row_mask:0xf bank_mask:0xf\n"
    "v_mfma_f32_16x16x32_bf16 v[200:203], v[176:179], v[168:171], v[200:203]\n"
    "v_mfma_f32_16x16x32_bf16 v[204:207], v[176:179], v[172:175], v[204:207]\n"
    "v_add_u32_dpp v54, v106, v50 quad_perm:[2,2,2,2] row_mask:0xf bank_mask:0xf\n"
    "v_mfma_f32_16x16x32_bf16 v[204:207], v[144:147], v[140:143], v[204:207]\n"
    "v_mfma_f32_16x16x32_bf16 v[220:223], v[148:151], v[140:143], v[220:223]\n"
    "v_add_u32_dpp v55, v106, v51 quad_perm:[3,3,3,3] row_mask:0xf bank_mask:0xf\n"
    "v_mfma_f32_16x16x32_bf16 v[220:223], v[180:183], v[172:175], v[220:223]\n"
    "v_mfma_f32_16x16x32_bf16 v[216:219], v[180:183], v[168:171], v[216:219]\n"
    "v_lshl_add_u32 v22, v12, 3, v22\n"
    "v_mfma_f32_16x16x32_bf16 v[216:219], v[148:151], v[136:139], v[216:219]\n"
    "v_mfma_f32_16x16x32_bf16 v[212:215], v[148:151], v[132:135], v[212:215]\n"
    "s_mov_b32 s40, 0x05040100\n"
    "v_mfma_f32_16x16x32_bf16 v[212:215], v[180:183], v[164:167], v[212:215]\n"
    "v_mfma_f32_16x16x32_bf16 v[208:211], v[180:183], v[160:163], v[208:211]\n"
    "v_bfe_u32 v24, s40, v13, 8\n"
    "v_mfma_f32_16x16x32_bf16 v[208:211], v[148:151], v[128:131], v[208:211]\n"
    "v_mfma_f32_16x16x32_bf16 v[224:227], v[152:155], v[128:131], v[224:227]\n"
    "v_add_u32 v22, v22, v24\n"
    "v_mfma_f32_16x16x32_bf16 v[224:227], v[184:187], v[160:163], v[224:227]\n"
    "v_mfma_f32_16x16x32_bf16 v[228:231], v[184:187], v[164:167], v[228:231]\n"
    "s_mov_b32 s40, 0x1a191918\n"
    "v_mfma_f32_16x16x32_bf16 v[228:231], v[152:155], v[132:135], v[228:231]\n"
    "v_mfma_f32_16x16x32_bf16 v[232:235], v[152:155], v[136:139], v[232:235]\n"
    "v_bfe_u32 v24, s40, v13, 8\n"
    "v_mfma_f32_16x16x32_bf16 v[232:235], v[184:187], v[168:171], v[232:235]\n"
    "v_mfma_f32_16x16x32_bf16 v[236:239], v[184:187], v[172:175], v[236:239]\n"
    "v_cndmask_b32 v22, v22, v24, vcc\n"
    "v_mfma_f32_16x16x32_bf16 v[236:239], v[152:155], v[140:143], v[236:239]\n"
    "v_mfma_f32_16x16x32_bf16 v[252:255], v[156:159], v[140:143], v[252:255]\n"
    "v_mad_u32_u24 v6, v22, s41, v21\n"
    "v_mfma_f32_16x16x32_bf16 v[252:255], v[188:191], v[172:175], v[252:255]\n"
    "v_mfma_f32_16x16x32_bf16 v[248:251], v[188:191], v[168:171], v[248:251]\n"
    "v_bfrev_b32 v14, v12\n"
    "v_mfma_f32_16x16x32_bf16 v[248:251], v[156:159], v[136:139], v[248:251]\n"
    "v_mfma_f32_16x16x32_bf16 v[244:247], v[156:159], v[132:135], v[244:247]\n"
    "v_lshrrev_b32 v14, 26, v14\n"
    "v_mfma_f32_16x16x32_bf16 v[244:247], v[188:191], v[164:167], v[244:247]\n"
    "v_mfma_f32_16x16x32_bf16 v[240:243], v[188:191], v[160:163], v[240:243]\n"
    "v_mfma_f32_16x16x32_bf16 v[240:243], v[156:159], v[128:131], v[240:243]\n"
    "s_waitcnt vmcnt(5)\n"
    "s_barrier\n"
    "s_add_u32 m0, s34, 0\n"
    "s_nop 0\n"
    "global_load_lds_dwordx4 v31, s[36:37] offset:3072\n"
    "ds_read_b128 v[128:131], v58 offset:0\n"
    "ds_read_b128 v[132:135], v58 offset:2048\n"
    "ds_read_b128 v[136:139], v58 offset:4096\n"
    "s_add_u32 m0, s35, 49152\n"
    "s_nop 0\n"
    "global_load_lds_dwordx4 v46, s[30:31] offset:0\n"
    "ds_read_b128 v[140:143], v58 offset:6144\n"
    "ds_read_b64_tr_b16 v[144:145], v80 offset:0\n"
    "ds_read_b64_tr_b16 v[146:147], v80 offset:1024\n"
    "global_load_lds_dwordx4 v47, s[30:31] offset:1024\n"
    "s_add_u32 s30, s30, 212992\n"
    "s_addc_u32 s31, s31, 0\n"
    "ds_read_b64_tr_b16 v[148:149], v81 offset:0\n"
    "ds_read_b64_tr_b16 v[150:151], v81 offset:1024\n"
    "ds_read_b64_tr_b16 v[152:153], v82 offset:0\n"
    "s_add_u32 m0, s34, 49152\n"
    "s_nop 0\n"
    "global_load_lds_dwordx4 v52, s[36:37] offset:0\n"
    "ds_read_b64_tr_b16 v[154:155], v82 offset:1024\n"
    "ds_read_b64_tr_b16 v[156:157], v83 offset:0\n"
    "ds_read_b64_tr_b16 v[158:159], v83 offset:1024\n"
    "global_load_lds_dwordx4 v53, s[36:37] offset:1024\n"
    "ds_read_b128 v[160:163], v59 offset:0\n"
    "ds_read_b128 v[164:167], v59 offset:2048\n"
    "ds_read_b128 v[168:171], v59 offset:4096\n"
    "global_load_lds_dwordx4 v54, s[36:37] offset:2048\n"
    "ds_read_b128 v[172:175], v59 offset:6144\n"
    "ds_read_b64_tr_b16 v[176:177], v80 offset:8192\n"
    "ds_read_b64_tr_b16 v[178:179], v80 offset:9216\n"
    "global_load_lds_dwordx4 v55, s[36:37] offset:3072\n"
    "ds_read_b64_tr_b16 v[180:181], v81 offset:8192\n"
    "ds_read_b64_tr_b16 v[182:183], v81 offset:9216\n"
    "ds_read_b64_tr_b16 v[184:185], v82 offset:8192\n"
    "ds_read_b64_tr_b16 v[186:187], v82 offset:9216\n"
    "ds_read_b64_tr_b16 v[188:189], v83 offset:8192\n"
    "ds_read_b64_tr_b16 v[190:191], v83 offset:9216\n"
    "s_waitcnt vmcnt(10) lgkmcnt(0)\n"
    "s_barrier\n"
    "v_mfma_f32_16x16x32_bf16 v[192:195], v[144:147], v[128:131], v[192:195]\n"
    "v_lshlrev_b32 v108, 7, v108\n"
    "v_mfma_f32_16x16x32_bf16 v[192:195], v[176:179], v[160:163], v[192:195]\n"
    "v_add_u32_dpp v52, v107, v48 quad_perm:[0,0,0,0] row_mask:0xf bank_mask:0xf\n"
    "v_mfma_f32_16x16x32_bf16 v[196:199], v[176:179], v[164:167], v[196:199]\n"
    "v_mfma_f32_16x16x32_bf16 v[196:199], v[144:147], v[132:135], v[196:199]\n"
    "v_add_u32_dpp v53, v107, v49 quad_perm:[1,1,1,1] row_mask:0xf bank_mask:0xf\n"
    "v_mfma_f32_16x16x32_bf16 v[200:203], v[144:147], v[136:139], v[200:203]\n"
    "v_mfma_f32_16x16x32_bf16 v[200:203], v[176:179], v[168:171], v[200:203]\n"
    "v_add_u32_dpp v54, v107, v50 quad_perm:[2,2,2,2] row_mask:0xf bank_mask:0xf\n"
    "v_mfma_f32_16x16x32_bf16 v[204:207], v[176:179], v[172:175], v[204:207]\n"
    "v_mfma_f32_16x16x32_bf16 v[204:207], v[144:147], v[140:143], v[204:207]\n"
    "v_add_u32_dpp v55, v107, v51 quad_perm:[3,3,3,3] row_mask:0xf bank_mask:0xf\n"
    "v_mfma_f32_16x16x32_bf16 v[220:223], v[148:151], v[140:143], v[220:223]\n"
    "v_mfma_f32_16x16x32_bf16 v[220:223], v[180:183], v[172:175], v[220:223]\n"
    "s_add_u32 s40, s22, 3\n"
    "v_mfma_f32_16x16x32_bf16 v[216:219], v[180:183], v[168:171], v[216:219]\n"
    "v_mfma_f32_16x16x32_bf16 v[216:219], v[148:151], v[136:139], v[216:219]\n"
    "s_and_b32 s40, s40, 3\n"
    "v_mfma_f32_16x16x32_bf16 v[212:215], v[148:151], v[132:135], v[212:215]\n"
    "v_mfma_f32_16x16x32_bf16 v[212:215], v[180:183], v[164:167], v[212:215]\n"
    "s_lshl2_add_u32 s40, s40, s22\n"
    "v_mfma_f32_16x16x32_bf16 v[208:211], v[180:183], v[160:163], v[208:211]\n"
    "v_mfma_f32_16x16x32_bf16 v[208:211], v[148:151], v[128:131], v[208:211]\n"
    "s_add_u32 s40, s40, s58\n"
    "v_mfma_f32_16x16x32_bf16 v[224:227], v[152:155], v[128:131], v[224:227]\n"
    "v_mfma_f32_16x16x32_bf16 v[224:227], v[184:187], v[160:163], v[224:227]\n"
    "s_lshl_b32 s40, s40, 2\n"
    "v_mfma_f32_16x16x32_bf16 v[228:231], v[184:187], v[164:167], v[228:231]\n"
    "v_mfma_f32_16x16x32_bf16 v[228:231], v[152:155], v[132:135], v[228:231]\n"
    "s_add_u32 s40, s40, -450048\n"
    "v_mfma_f32_16x16x32_bf16 v[232:235], v[152:155], v[136:139], v[232:235]\n"
    "v_mfma_f32_16x16x32_bf16 v[232:235], v[184:187], v[168:171], v[232:235]\n"
    "v_add_u32 v14, s40, v14\n"
    "v_mfma_f32_16x16x32_bf16 v[236:239], v[184:187], v[172:175], v[236:239]\n"
    "v_mfma_f32_16x16x32_bf16 v[236:239], v[152:155], v[140:143], v[236:239]\n"
    "v_and_b32 v2, 1, v1\n"
    "v_mfma_f32_16x16x32_bf16 v[252:255], v[156:159], v[140:143], v[252:255]\n"
    "v_mfma_f32_16x16x32_bf16 v[252:255], v[188:191], v[172:175], v[252:255]\n"
    "v_mul_u32_u24 v2, 0xaaaa, v2\n"
    "v_mfma_f32_16x16x32_bf16 v[248:251], v[188:191], v[168:171], v[248:251]\n"
    "v_mfma_f32_16x16x32_bf16 v[248:251], v[156:159], v[136:139], v[248:251]\n"
    "v_add_u32 v2, 0x4444, v2\n"
    "v_mfma_f32_16x16x32_bf16 v[244:247], v[156:159], v[132:135], v[244:247]\n"
    "v_mfma_f32_16x16x32_bf16 v[244:247], v[188:191], v[164:167], v[244:247]\n"
    "v_mad_u32_u24 v15, v17, 36, v19\n"
    "v_mfma_f32_16x16x32_bf16 v[240:243], v[188:191], v[160:163], v[240:243]\n"
    "v_mfma_f32_16x16x32_bf16 v[240:243], v[156:159], v[128:131], v[240:243]\n"
    "s_waitcnt vmcnt(6)\n"
    "s_barrier\n"
    "s_add_u32 m0, s35, 98304\n"
    "s_nop 0\n"
    "global_load_lds_dwordx4 v46, s[30:31] offset:0\n"
    "ds_read_b128 v[128:131], v56 offset:0\n"
    "ds_read_b128 v[132:135], v56 offset:2048\n"
    "ds_read_b128 v[136:139], v56 offset:4096\n"
    "global_load_lds_dwordx4 v47, s[30:31] offset:1024\n"
    "s_add_u32 s66, s28, 4\n"
    "s_and_b32 s66, s66, 7\n"
    "s_mul_i32 s66, s66, 638976\n"
    "s_add_u32 s30, s6, s66\n"
    "s_addc_u32 s31, s7, 0\n"
    "ds_read_b128 v[140:143], v56 offset:6144\n"
    "ds_read_b64_tr_b16 v[144:145], v76 offset:0\n"
    "ds_read_b64_tr_b16 v[146:147], v76 offset:1024\n"
    "s_add_u32 m0, s34, 98304\n"
    "s_nop 0\n"
    "global_load_lds_dwordx4 v52, s[36:37] offset:0\n"
    "ds_read_b64_tr_b16 v[148:149], v77 offset:0\n"
    "ds_read_b64_tr_b16 v[150:151], v77 offset:1024\n"
    "ds_read_b64_tr_b16 v[152:153], v78 offset:0\n"
    "global_load_lds_dwordx4 v53, s[36:37] offset:1024\n"
    "ds_read_b64_tr_b16 v[154:155], v78 offset:1024\n"
    "ds_read_b64_tr_b16 v[156:157], v79 offset:0\n"
    "ds_read_b64_tr_b16 v[158:159], v79 offset:1024\n"
    "global_load_lds_dwordx4 v54, s[36:37] offset:2048\n"
    "ds_read_b128 v[160:163], v57 offset:0\n"
    "ds_read_b128 v[164:167], v57 offset:2048\n"
    "ds_read_b128 v[168:171], v57 offset:4096\n"
    "global_load_lds_dwordx4 v55, s[36:37] offset:3072\n"
    "ds_read_b128 v[172:175], v57 offset:6144\n"
    "ds_read_b64_tr_b16 v[176:177], v76 offset:8192\n"
    "ds_read_b64_tr_b16 v[178:179], v76 offset:9216\n"
    "global_load_dwordx2 v[90:91], v42, s[10:11] offset:96\n"
    "ds_read_b64_tr_b16 v[180:181], v77 offset:8192\n"
    "ds_read_b64_tr_b16 v[182:183], v77 offset:9216\n"
    "ds_read_b64_tr_b16 v[184:185], v78 offset:8192\n"
    "ds_read_b64_tr_b16 v[186:187], v78 offset:9216\n"
    "ds_read_b64_tr_b16 v[188:189], v79 offset:8192\n"
    "ds_read_b64_tr_b16 v[190:191], v79 offset:9216\n"
    "s_waitcnt vmcnt(11) lgkmcnt(0)\n"
    "s_barrier\n"
    "v_mfma_f32_16x16x32_bf16 v[192:195], v[144:147], v[128:131], v[192:195]\n"
    "v_lshlrev_b32 v109, 7, v109\n"
    "v_mfma_f32_16x16x32_bf16 v[192:195], v[176:179], v[160:163], v[192:195]\n"
    "v_mfma_f32_16x16x32_bf16 v[196:199], v[176:179], v[164:167], v[196:199]\n"
    "v_add_u32_dpp v52, v108, v48 quad_perm:[0,0,0,0] row_mask:0xf bank_mask:0xf\n"
    "v_mfma_f32_16x16x32_bf16 v[196:199], v[144:147], v[132:135], v[196:199]\n"
    "v_mfma_f32_16x16x32_bf16 v[200:203], v[144:147], v[136:139], v[200:203]\n"
    "v_add_u32_dpp v53, v108, v49 quad_perm:[1,1,1,1] row_mask:0xf bank_mask:0xf\n"
    "v_mfma_f32_16x16x32_bf16 v[200:203], v[176:179], v[168:171], v[200:203]\n"
    "v_mfma_f32_16x16x32_bf16 v[204:207], v[176:179], v[172:175], v[204:207]\n"
    "v_add_u32_dpp v54, v108, v50 quad_perm:[2,2,2,2] row_mask:0xf bank_mask:0xf\n"
    "v_mfma_f32_16x16x32_bf16 v[204:207], v[144:147], v[140:143], v[204:207]\n"
    "v_mfma_f32_16x16x32_bf16 v[220:223], v[148:151], v[140:143], v[220:223]\n"
    "v_add_u32_dpp v55, v108, v51 quad_perm:[3,3,3,3] row_mask:0xf bank_mask:0xf\n"
    "v_mfma_f32_16x16x32_bf16 v[220:223], v[180:183], v[172:175], v[220:223]\n"
    "v_mfma_f32_16x16x32_bf16 v[216:219], v[180:183], v[168:171], v[216:219]\n"
    "v_mad_u32_u24 v16, v18, 36, v20\n"
    "v_mfma_f32_16x16x32_bf16 v[216:219], v[148:151], v[136:139], v[216:219]\n"
    "v_mfma_f32_16x16x32_bf16 v[212:215], v[148:151], v[132:135], v[212:215]\n"
    "s_add_u32 s41, s22, 2\n"
    "v_mfma_f32_16x16x32_bf16 v[212:215], v[180:183], v[164:167], v[212:215]\n"
    "v_mfma_f32_16x16x32_bf16 v[208:211], v[180:183], v[160:163], v[208:211]\n"
    "v_lshrrev_b32 v12, 5, v19\n"
    "v_mfma_f32_16x16x32_bf16 v[208:211], v[148:151], v[128:131], v[208:211]\n"
    "v_mfma_f32_16x16x32_bf16 v[224:227], v[152:155], v[128:131], v[224:227]\n"
    "v_sub_u32 v12, 1, v12\n"
    "v_mfma_f32_16x16x32_bf16 v[224:227], v[184:187], v[160:163], v[224:227]\n"
    "v_mfma_f32_16x16x32_bf16 v[228:231], v[184:187], v[164:167], v[228:231]\n"
    "v_and_b32 v12, 3, v12\n"
    "v_mfma_f32_16x16x32_bf16 v[228:231], v[152:155], v[132:135], v[228:231]\n"
    "v_mfma_f32_16x16x32_bf16 v[232:235], v[152:155], v[136:139], v[232:235]\n"
    "v_add_lshl_u32 v12, v12, s41, 12\n"
    "v_mfma_f32_16x16x32_bf16 v[232:235], v[184:187], v[168:171], v[232:235]\n"
    "v_mfma_f32_16x16x32_bf16 v[236:239], v[184:187], v[172:175], v[236:239]\n"
    "v_and_b32 v12, 0x7000, v12\n"
    "v_mfma_f32_16x16x32_bf16 v[236:239], v[152:155], v[140:143], v[236:239]\n"
    "v_mfma_f32_16x16x32_bf16 v[252:255], v[156:159], v[140:143], v[252:255]\n"
    "v_add3_u32 v15, v12, v15, s43\n"
    "v_mfma_f32_16x16x32_bf16 v[252:255], v[188:191], v[172:175], v[252:255]\n"
    "v_mfma_f32_16x16x32_bf16 v[248:251], v[188:191], v[168:171], v[248:251]\n"
    "v_lshrrev_b32 v12, 5, v20\n"
    "v_mfma_f32_16x16x32_bf16 v[248:251], v[156:159], v[136:139], v[248:251]\n"
    "v_mfma_f32_16x16x32_bf16 v[244:247], v[156:159], v[132:135], v[244:247]\n"
    "v_sub_u32 v12, 1, v12\n"
    "v_mfma_f32_16x16x32_bf16 v[244:247], v[188:191], v[164:167], v[244:247]\n"
    "v_mfma_f32_16x16x32_bf16 v[240:243], v[188:191], v[160:163], v[240:243]\n"
    "v_mfma_f32_16x16x32_bf16 v[240:243], v[156:159], v[128:131], v[240:243]\n"
    "s_waitcnt vmcnt(7)\n"
    "s_barrier\n"
    "s_add_u32 m0, s35, 0\n"
    "s_nop 0\n"
    "global_load_lds_dwordx4 v46, s[30:31] offset:0\n"
    "ds_read_b128 v[128:131], v56 offset:49152\n"
    "ds_read_b128 v[132:135], v56 offset:51200\n"
    "ds_read_b128 v[136:139], v56 offset:53248\n"
    "global_load_lds_dwordx4 v47, s[30:31] offset:1024\n"
    "s_add_u32 s30, s30, 212992\n"
    "s_addc_u32 s31, s31, 0\n"
    "ds_read_b128 v[140:143], v56 offset:55296\n"
    "ds_read_b64_tr_b16 v[144:145], v76 offset:49152\n"
    "ds_read_b64_tr_b16 v[146:147], v76 offset:50176\n"
    "s_add_u32 m0, s34, 0\n"
    "s_nop 0\n"
    "global_load_lds_dwordx4 v52, s[36:37] offset:0\n"
    "ds_read_b64_tr_b16 v[148:149], v77 offset:49152\n"
    "ds_read_b64_tr_b16 v[150:151], v77 offset:50176\n"
    "ds_read_b64_tr_b16 v[152:153], v78 offset:49152\n"
    "global_load_lds_dwordx4 v53, s[36:37] offset:1024\n"
    "ds_read_b64_tr_b16 v[154:155], v78 offset:50176\n"
    "ds_read_b64_tr_b16 v[156:157], v79 offset:49152\n"
    "ds_read_b64_tr_b16 v[158:159], v79 offset:50176\n"
    "global_load_lds_dwordx4 v54, s[36:37] offset:2048\n"
    "ds_read_b128 v[160:163], v57 offset:49152\n"
    "ds_read_b128 v[164:167], v57 offset:51200\n"
    "ds_read_b128 v[168:171], v57 offset:53248\n"
    "global_load_lds_dwordx4 v55, s[36:37] offset:3072\n"
    "ds_read_b128 v[172:175], v57 offset:55296\n"
    "ds_read_b64_tr_b16 v[176:177], v76 offset:57344\n"
    "ds_read_b64_tr_b16 v[178:179], v76 offset:58368\n"
    "global_load_dwordx2 v[92:93], v42, s[18:19] offset:0\n"
    "ds_read_b64_tr_b16 v[180:181], v77 offset:57344\n"
    "ds_read_b64_tr_b16 v[182:183], v77 offset:58368\n"
    "ds_read_b64_tr_b16 v[184:185], v78 offset:57344\n"
    "ds_read_b64_tr_b16 v[186:187], v78 offset:58368\n"
    "ds_read_b64_tr_b16 v[188:189], v79 offset:57344\n"
    "ds_read_b64_tr_b16 v[190:191], v79 offset:58368\n"
    "s_waitcnt vmcnt(12) lgkmcnt(0)\n"
    "s_barrier\n"
    "v_mfma_f32_16x16x32_bf16 v[192:195], v[144:147], v[128:131], v[192:195]\n"
    "v_lshlrev_b32 v110, 7, v110\n"
    "v_mfma_f32_16x16x32_bf16 v[192:195], v[176:179], v[160:163], v[192:195]\n"
    "v_add_u32_dpp v52, v109, v48 quad_perm:[0,0,0,0] row_mask:0xf bank_mask:0xf\n"
    "v_mfma_f32_16x16x32_bf16 v[196:199], v[176:179], v[164:167], v[196:199]\n"
    "v_mfma_f32_16x16x32_bf16 v[196:199], v[144:147], v[132:135], v[196:199]\n"
    "v_add_u32_dpp v53, v109, v49 quad_perm:[1,1,1,1] row_mask:0xf bank_mask:0xf\n"
    "v_mfma_f32_16x16x32_bf16 v[200:203], v[144:147], v[136:139], v[200:203]\n"
    "v_mfma_f32_16x16x32_bf16 v[200:203], v[176:179], v[168:171], v[200:203]\n"
    "v_add_u32_dpp v54, v109, v50 quad_perm:[2,2,2,2] row_mask:0xf bank_mask:0xf\n"
    "v_mfma_f32_16x16x32_bf16 v[204:207], v[176:179], v[172:175], v[204:207]\n"
    "v_mfma_f32_16x16x32_bf16 v[204:207], v[144:147], v[140:143], v[204:207]\n"
    "v_add_u32_dpp v55, v109, v51 quad_perm:[3,3,3,3] row_mask:0xf bank_mask:0xf\n"
    "v_mfma_f32_16x16x32_bf16 v[220:223], v[148:151], v[140:143], v[220:223]\n"
    "v_mfma_f32_16x16x32_bf16 v[220:223], v[180:183], v[172:175], v[220:223]\n"
    "v_and_b32 v12, 3, v12\n"
    "v_mfma_f32_16x16x32_bf16 v[216:219], v[180:183], v[168:171], v[216:219]\n"
    "v_mfma_f32_16x16x32_bf16 v[216:219], v[148:151], v[136:139], v[216:219]\n"
    "v_add_lshl_u32 v12, v12, s41, 12\n"
    "v_mfma_f32_16x16x32_bf16 v[212:215], v[148:151], v[132:135], v[212:215]\n"
    "v_mfma_f32_16x16x32_bf16 v[212:215], v[180:183], v[164:167], v[212:215]\n"
    "v_and_b32 v12, 0x7000, v12\n"
    "v_mfma_f32_16x16x32_bf16 v[208:211], v[180:183], v[160:163], v[208:211]\n"
    "v_mfma_f32_16x16x32_bf16 v[208:211], v[148:151], v[128:131], v[208:211]\n"
    "v_add3_u32 v16, v12, v16, s43\n"
    "v_mfma_f32_16x16x32_bf16 v[224:227], v[152:155], v[128:131], v[224:227]\n"
    "v_mfma_f32_16x16x32_bf16 v[224:227], v[184:187], v[160:163], v[224:227]\n"
    "v_add_u32 v16, 3456, v16\n"
    "v_mfma_f32_16x16x32_bf16 v[228:231], v[184:187], v[164:167], v[228:231]\n"
    "v_mfma_f32_16x16x32_bf16 v[228:231], v[152:155], v[132:135], v[228:231]\n"
    "s_mul_i32 s41, s47, 13\n"
    "v_mfma_f32_16x16x32_bf16 v[232:235], v[152:155], v[136:139], v[232:235]\n"
    "v_mfma_f32_16x16x32_bf16 v[232:235], v[184:187], v[168:171], v[232:235]\n"
    "s_sub_u32 s41, s3, s41\n"
    "v_mfma_f32_16x16x32_bf16 v[236:239], v[184:187], v[172:175], v[236:239]\n"
    "v_mfma_f32_16x16x32_bf16 v[236:239], v[152:155], v[140:143], v[236:239]\n"
    "s_lshl1_add_u32 s41, s41, s21\n"
    "v_mfma_f32_16x16x32_bf16 v[252:255], v[156:159], v[140:143], v[252:255]\n"
    "v_mfma_f32_16x16x32_bf16 v[252:255], v[188:191], v[172:175], v[252:255]\n"
    "s_mul_i32 s41, s41, 3\n"
    "v_mfma_f32_16x16x32_bf16 v[248:251], v[188:191], v[168:171], v[248:251]\n"
    "v_mfma_f32_16x16x32_bf16 v[248:251], v[156:159], v[136:139], v[248:251]\n"
    "s_add_u32 s41, s41, s22\n"
    "v_mfma_f32_16x16x32_bf16 v[244:247], v[156:159], v[132:135], v[244:247]\n"
    "v_mfma_f32_16x16x32_bf16 v[244:247], v[188:191], v[164:167], v[244:247]\n"
    "v_mov_b32 v24, s41\n"
    "v_mfma_f32_16x16x32_bf16 v[240:243], v[188:191], v[160:163], v[240:243]\n"
    "v_mfma_f32_16x16x32_bf16 v[240:243], v[156:159], v[128:131], v[240:243]\n"
    "s_waitcnt vmcnt(8)\n"
    "s_barrier\n"
    "s_add_u32 m0, s35, 49152\n"
    "s_nop 0\n"
    "global_load_lds_dwordx4 v46, s[30:31] offset:0\n"
    "ds_read_b128 v[128:131], v58 offset:0\n"
    "ds_read_b128 v[132:135], v58 offset:2048\n"
    "ds_read_b128 v[136:139], v58 offset:4096\n"
    "global_load_lds_dwordx4 v47, s[30:31] offset:1024\n"
    "s_add_u32 s30, s30, 212992\n"
    "s_addc_u32 s31, s31, 0\n"
    "ds_read_b128 v[140:143], v58 offset:6144\n"
    "ds_read_b64_tr_b16 v[144:145], v80 offset:0\n"
    "ds_read_b64_tr_b16 v[146:147], v80 offset:1024\n"
    "s_add_u32 m0, s34, 49152\n"
    "s_nop 0\n"
    "global_load_lds_dwordx4 v52, s[36:37] offset:0\n"
    "ds_read_b64_tr_b16 v[148:149], v81 offset:0\n"
    "ds_read_b64_tr_b16 v[150:151], v81 offset:1024\n"
    "ds_read_b64_tr_b16 v[152:153], v82 offset:0\n"
    "global_load_lds_dwordx4 v53, s[36:37] offset:1024\n"
    "ds_read_b64_tr_b16 v[154:155], v82 offset:1024\n"
    "ds_read_b64_tr_b16 v[156:157], v83 offset:0\n"
    "ds_read_b64_tr_b16 v[158:159], v83 offset:1024\n"
    "global_load_lds_dwordx4 v54, s[36:37] offset:2048\n"
    "ds_read_b128 v[160:163], v59 offset:0\n"
    "ds_read_b128 v[164:167], v59 offset:2048\n"
    "ds_read_b128 v[168:171], v59 offset:4096\n"
    "global_load_lds_dwordx4 v55, s[36:37] offset:3072\n"
    "ds_read_b128 v[172:175], v59 offset:6144\n"
    "ds_read_b64_tr_b16 v[176:177], v80 offset:8192\n"
    "ds_read_b64_tr_b16 v[178:179], v80 offset:9216\n"
    "global_load_dwordx2 v[94:95], v42, s[18:19] offset:32\n"
    "ds_read_b64_tr_b16 v[180:181], v81 offset:8192\n"
    "ds_read_b64_tr_b16 v[182:183], v81 offset:9216\n"
    "ds_read_b64_tr_b16 v[184:185], v82 offset:8192\n"
    "ds_read_b64_tr_b16 v[186:187], v82 offset:9216\n"
    "ds_read_b64_tr_b16 v[188:189], v83 offset:8192\n"
    "ds_read_b64_tr_b16 v[190:191], v83 offset:9216\n"
    "s_waitcnt vmcnt(12) lgkmcnt(0)\n"
    "s_barrier\n"
    "v_mfma_f32_16x16x32_bf16 v[192:195], v[144:147], v[128:131], v[192:195]\n"
    "v_lshlrev_b32 v111, 7, v111\n"
    "v_mfma_f32_16x16x32_bf16 v[192:195], v[176:179], v[160:163], v[192:195]\n"
    "v_mfma_f32_16x16x32_bf16 v[196:199], v[176:179], v[164:167], v[196:199]\n"
    "v_add_u32_dpp v52, v110, v48 quad_perm:[0,0,0,0] row_mask:0xf bank_mask:0xf\n"
    "v_mfma_f32_16x16x32_bf16 v[196:199], v[144:147], v[132:135], v[196:199]\n"
    "v_mfma_f32_16x16x32_bf16 v[200:203], v[144:147], v[136:139], v[200:203]\n"
    "v_add_u32_dpp v53, v110, v49 quad_perm:[1,1,1,1] row_mask:0xf bank_mask:0xf\n"
    "v_mfma_f32_16x16x32_bf16 v[200:203], v[176:179], v[168:171], v[200:203]\n"
    "v_mfma_f32_16x16x32_bf16 v[204:207], v[176:179], v[172:175], v[204:207]\n"
    "v_add_u32_dpp v54, v110, v50 quad_perm:[2,2,2,2] row_mask:0xf bank_mask:0xf\n"
    "v_mfma_f32_16x16x32_bf16 v[204:207], v[144:147], v[140:143], v[204:207]\n"
    "v_mfma_f32_16x16x32_bf16 v[220:223], v[148:151], v[140:143], v[220:223]\n"
    "v_add_u32_dpp v55, v110, v51 quad_perm:[3,3,3,3] row_mask:0xf bank_mask:0xf\n"
    "v_mfma_f32_16x16x32_bf16 v[220:223], v[180:183], v[172:175], v[220:223]\n"
    "v_mfma_f32_16x16x32_bf16 v[216:219], v[180:183], v[168:171], v[216:219]\n"
    "v_mfma_f32_16x16x32_bf16 v[216:219], v[148:151], v[136:139], v[216:219]\n"
    "v_readfirstlane_b32 s40, v24\n"
    "v_mfma_f32_16x16x32_bf16 v[212:215], v[148:151], v[132:135], v[212:215]\n"
    "v_mfma_f32_16x16x32_bf16 v[212:215], v[180:183], v[164:167], v[212:215]\n"
    "s_or_b32 s40, s40, s44\n"
    "v_mfma_f32_16x16x32_bf16 v[208:211], v[180:183], v[160:163], v[208:211]\n"
    "v_mfma_f32_16x16x32_bf16 v[208:211], v[148:151], v[128:131], v[208:211]\n"
    "s_lshl1_add_u32 s41, s40, s54\n"
    "v_mfma_f32_16x16x32_bf16 v[224:227], v[152:155], v[128:131], v[224:227]\n"
    "v_mfma_f32_16x16x32_bf16 v[224:227], v[184:187], v[160:163], v[224:227]\n"
    "s_cmpk_lt_u32 s40, 256\n"
    "v_mfma_f32_16x16x32_bf16 v[228:231], v[184:187], v[164:167], v[228:231]\n"
    "v_mfma_f32_16x16x32_bf16 v[228:231], v[152:155], v[132:135], v[228:231]\n"
    "s_cselect_b32 s41, s41, 2\n"
    "v_mfma_f32_16x16x32_bf16 v[232:235], v[152:155], v[136:139], v[232:235]\n"
    "v_mfma_f32_16x16x32_bf16 v[232:235], v[184:187], v[168:171], v[232:235]\n"
    "v_mfma_f32_16x16x32_bf16 v[236:239], v[184:187], v[172:175], v[236:239]\n"
    "v_mov_b32 v24, s41\n"
    "v_mfma_f32_16x16x32_bf16 v[236:239], v[152:155], v[140:143], v[236:239]\n"
    "v_mfma_f32_16x16x32_bf16 v[252:255], v[156:159], v[140:143], v[252:255]\n"
    "s_mul_i32 s41, s41, 52\n"
    "v_mfma_f32_16x16x32_bf16 v[252:255], v[188:191], v[172:175], v[252:255]\n"
    "v_mfma_f32_16x16x32_bf16 v[248:251], v[188:191], v[168:171], v[248:251]\n"
    "s_add_u32 s38, s4, s41\n"
    "v_mfma_f32_16x16x32_bf16 v[248:251], v[156:159], v[136:139], v[248:251]\n"
    "v_mfma_f32_16x16x32_bf16 v[244:247], v[156:159], v[132:135], v[244:247]\n"
    "s_addc_u32 s39, s5, 0\n"
    "v_mfma_f32_16x16x32_bf16 v[244:247], v[188:191], v[164:167], v[244:247]\n"
    "v_mfma_f32_16x16x32_bf16 v[240:243], v[188:191], v[160:163], v[240:243]\n"
    "v_mfma_f32_16x16x32_bf16 v[240:243], v[156:159], v[128:131], v[240:243]\n"
    "s_waitcnt vmcnt(8)\n"
    "s_barrier\n"
    "s_add_u32 m0, s35, 98304\n"
    "s_nop 0\n"
    "global_load_lds_dwordx4 v46, s[30:31] offset:0\n"
    "ds_read_b128 v[128:131], v56 offset:0\n"
    "ds_read_b128 v[132:135], v56 offset:2048\n"
    "ds_read_b128 v[136:139], v56 offset:4096\n"
    "global_load_lds_dwordx4 v47, s[30:31] offset:1024\n"
    "s_add_u32 s66, s28, 5\n"
    "s_and_b32 s66, s66, 7\n"
    "s_mul_i32 s66, s66, 638976\n"
    "s_add_u32 s30, s6, s66\n"
    "s_addc_u32 s31, s7, 0\n"
    "ds_read_b128 v[140:143], v56 offset:6144\n"
    "ds_read_b64_tr_b16 v[144:145], v76 offset:0\n"
    "ds_read_b64_tr_b16 v[146:147], v76 offset:1024\n"
    "s_add_u32 m0, s34, 98304\n"
    "s_nop 0\n"
    "global_load_lds_dwordx4 v52, s[36:37] offset:0\n"
    "ds_read_b64_tr_b16 v[148:149], v77 offset:0\n"
    "ds_read_b64_tr_b16 v[150:151], v77 offset:1024\n"
    "ds_read_b64_tr_b16 v[152:153], v78 offset:0\n"
    "global_load_lds_dwordx4 v53, s[36:37] offset:1024\n"
    "ds_read_b64_tr_b16 v[154:155], v78 offset:1024\n"
    "ds_read_b64_tr_b16 v[156:157], v79 offset:0\n"
    "ds_read_b64_tr_b16 v[158:159], v79 offset:1024\n"
    "global_load_lds_dwordx4 v54, s[36:37] offset:2048\n"
    "ds_read_b128 v[160:163], v57 offset:0\n"
    "ds_read_b128 v[164:167], v57 offset:2048\n"
    "ds_read_b128 v[168:171], v57 offset:4096\n"
    "global_load_lds_dwordx4 v55, s[36:37] offset:3072\n"
    "ds_read_b128 v[172:175], v57 offset:6144\n"
    "ds_read_b64_tr_b16 v[176:177], v76 offset:8192\n"
    "ds_read_b64_tr_b16 v[178:179], v76 offset:9216\n"
    "global_load_dwordx3 v[36:38], v17, s[38:39] offset:-104\n"
    "ds_read_b64_tr_b16 v[180:181], v77 offset:8192\n"
    "ds_read_b64_tr_b16 v[182:183], v77 offset:9216\n"
    "ds_read_b64_tr_b16 v[184:185], v78 offset:8192\n"
    "ds_read_b64_tr_b16 v[186:187], v78 offset:9216\n"
    "ds_read_b64_tr_b16 v[188:189], v79 offset:8192\n"
    "ds_read_b64_tr_b16 v[190:191], v79 offset:9216\n"
    "s_waitcnt vmcnt(12) lgkmcnt(0)\n"
    "s_barrier\n"
    "v_mfma_f32_16x16x32_bf16 v[192:195], v[144:147], v[128:131], v[192:195]\n"
    "v_lshlrev_b32 v112, 7, v112\n"
    "v_mfma_f32_16x16x32_bf16 v[192:195], v[176:179], v[160:163], v[192:195]\n"
    "v_add_u32_dpp v52, v111, v48 quad_perm:[0,0,0,0] row_mask:0xf bank_mask:0xf\n"
    "v_mfma_f32_16x16x32_bf16 v[196:199], v[176:179], v[164:167], v[196:199]\n"
    "v_add_u32_dpp v53, v111, v49 quad_perm:[1,1,1,1] row_mask:0xf bank_mask:0xf\n"
    "v_mfma_f32_16x16x32_bf16 v[196:199], v[144:147], v[132:135], v[196:199]\n"
    "v_add_u32_dpp v54, v111, v50 quad_perm:[2,2,2,2] row_mask:0xf bank_mask:0xf\n"
    "v_mfma_f32_16x16x32_bf16 v[200:203], v[144:147], v[136:139], v[200:203]\n"
    "v_add_u32_dpp v55, v111, v51 quad_perm:[3,3,3,3] row_mask:0xf bank_mask:0xf\n"
    "v_mfma_f32_16x16x32_bf16 v[200:203], v[176:179], v[168:171], v[200:203]\n"
    "v_lshlrev_b32 v60, 16, v84\n"
    "v_mfma_f32_16x16x32_bf16 v[204:207], v[176:179], v[172:175], v[204:207]\n"
    "v_and_b32 v61, 0xffff0000, v84\n"
    "v_mfma_f32_16x16x32_bf16 v[204:207], v[144:147], v[140:143], v[204:207]\n"
    "v_mfma_f32_16x16x32_bf16 v[220:223], v[148:151], v[140:143], v[220:223]\n"
    "v_lshlrev_b32 v62, 16, v85\n"
    "v_mfma_f32_16x16x32_bf16 v[220:223], v[180:183], v[172:175], v[220:223]\n"
    "v_and_b32 v63, 0xffff0000, v85\n"
    "v_mfma_f32_16x16x32_bf16 v[216:219], v[180:183], v[168:171], v[216:219]\n"
    "v_lshlrev_b32 v64, 16, v86\n"
    "v_mfma_f32_16x16x32_bf16 v[216:219], v[148:151], v[136:139], v[216:219]\n"
    "v_and_b32 v65, 0xffff0000, v86\n"
    "v_mfma_f32_16x16x32_bf16 v[212:215], v[148:151], v[132:135], v[212:215]\n"
    "v_lshlrev_b32 v66, 16, v87\n"
    "v_mfma_f32_16x16x32_bf16 v[212:215], v[180:183], v[164:167], v[212:215]\n"
    "v_and_b32 v67, 0xffff0000, v87\n"
    "v_mfma_f32_16x16x32_bf16 v[208:211], v[180:183], v[160:163], v[208:211]\n"
    "v_lshlrev_b32 v68, 16, v88\n"
    "v_mfma_f32_16x16x32_bf16 v[208:211], v[148:151], v[128:131], v[208:211]\n"
    "v_mfma_f32_16x16x32_bf16 v[224:227], v[152:155], v[128:131], v[224:227]\n"
    "v_and_b32 v69, 0xffff0000, v88\n"
    "v_mfma_f32_16x16x32_bf16 v[224:227], v[184:187], v[160:163], v[224:227]\n"
    "v_lshlrev_b32 v70, 16, v89\n"
    "v_mfma_f32_16x16x32_bf16 v[228:231], v[184:187], v[164:167], v[228:231]\n"
    "v_and_b32 v71, 0xffff0000, v89\n"
    "v_mfma_f32_16x16x32_bf16 v[228:231], v[152:155], v[132:135], v[228:231]\n"
    "v_lshlrev_b32 v72, 16, v90\n"
    "v_mfma_f32_16x16x32_bf16 v[232:235], v[152:155], v[136:139], v[232:235]\n"
    "v_and_b32 v73, 0xffff0000, v90\n"
    "v_mfma_f32_16x16x32_bf16 v[232:235], v[184:187], v[168:171], v[232:235]\n"
    "v_lshlrev_b32 v74, 16, v91\n"
    "v_mfma_f32_16x16x32_bf16 v[236:239], v[184:187], v[172:175], v[236:239]\n"
    "v_and_b32 v75, 0xffff0000, v91\n"
    "v_mfma_f32_16x16x32_bf16 v[236:239], v[152:155], v[140:143], v[236:239]\n"
    "v_mfma_f32_16x16x32_bf16 v[252:255], v[156:159], v[140:143], v[252:255]\n"
    "s_lshl3_add_u32 s40, s2, 4096\n"
    "v_mfma_f32_16x16x32_bf16 v[252:255], v[188:191], v[172:175], v[252:255]\n"
    "s_lshl_b32 s41, s2, 2\n"
    "v_mfma_f32_16x16x32_bf16 v[248:251], v[188:191], v[168:171], v[248:251]\n"
    "v_lshlrev_b32 v11, 3, v1\n"
    "v_mfma_f32_16x16x32_bf16 v[248:251], v[156:159], v[136:139], v[248:251]\n"
    "v_add_u32 v11, 40960, v11\n"
    "v_mfma_f32_16x16x32_bf16 v[244:247], v[156:159], v[132:135], v[244:247]\n"
    "v_writelane_b32 v11, s40, 8\n"
    "v_mfma_f32_16x16x32_bf16 v[244:247], v[188:191], v[164:167], v[244:247]\n"
    "v_writelane_b32 v11, s41, 9\n"
    "v_mfma_f32_16x16x32_bf16 v[240:243], v[188:191], v[160:163], v[240:243]\n"
    "v_mfma_f32_16x16x32_bf16 v[240:243], v[156:159], v[128:131], v[240:243]\n"
    "s_waitcnt vmcnt(8)\n"
    "s_barrier\n"
    "s_add_u32 m0, s35, 0\n"
    "s_nop 0\n"
    "global_load_lds_dwordx4 v46, s[30:31] offset:0\n"
    "ds_read_b128 v[128:131], v56 offset:49152\n"
    "ds_read_b128 v[132:135], v56 offset:51200\n"
    "ds_read_b128 v[136:139], v56 offset:53248\n"
    "global_load_lds_dwordx4 v47, s[30:31] offset:1024\n"
    "s_add_u32 s30, s30, 212992\n"
    "s_addc_u32 s31, s31, 0\n"
    "ds_read_b128 v[140:143], v56 offset:55296\n"
    "ds_read_b64_tr_b16 v[144:145], v76 offset:49152\n"
    "ds_read_b64_tr_b16 v[146:147], v76 offset:50176\n"
    "s_add_u32 m0, s34, 0\n"
    "s_nop 0\n"
    "global_load_lds_dwordx4 v52, s[36:37] offset:0\n"
    "ds_read_b64_tr_b16 v[148:149], v77 offset:49152\n"
    "ds_read_b64_tr_b16 v[150:151], v77 offset:50176\n"
    "ds_read_b64_tr_b16 v[152:153], v78 offset:49152\n"
    "global_load_lds_dwordx4 v53, s[36:37] offset:1024\n"
    "ds_read_b64_tr_b16 v[154:155], v78 offset:50176\n"
    "ds_read_b64_tr_b16 v[156:157], v79 offset:49152\n"
    "ds_read_b64_tr_b16 v[158:159], v79 offset:50176\n"
    "global_load_lds_dwordx4 v54, s[36:37] offset:2048\n"
    "ds_read_b128 v[160:163], v57 offset:49152\n"
    "ds_read_b128 v[164:167], v57 offset:51200\n"
    "ds_read_b128 v[168:171], v57 offset:53248\n"
    "global_load_lds_dwordx4 v55, s[36:37] offset:3072\n"
    "ds_read_b128 v[172:175], v57 offset:55296\n"
    "ds_read_b64_tr_b16 v[176:177], v76 offset:57344\n"
    "ds_read_b64_tr_b16 v[178:179], v76 offset:58368\n"
    "global_load_dword v41, v18, s[38:39] offset:-8\n"
    "ds_read_b64_tr_b16 v[180:181], v77 offset:57344\n"
    "ds_read_b64_tr_b16 v[182:183], v77 offset:58368\n"
    "ds_read_b64_tr_b16 v[184:185], v78 offset:57344\n"
    "ds_read_b64_tr_b16 v[186:187], v78 offset:58368\n"
    "ds_read_b64_tr_b16 v[188:189], v79 offset:57344\n"
    "ds_read_b64_tr_b16 v[190:191], v79 offset:58368\n"
    "s_waitcnt vmcnt(12) lgkmcnt(0)\n"
    "s_barrier\n"
    "v_mfma_f32_16x16x32_bf16 v[192:195], v[144:147], v[128:131], v[192:195]\n"
    "v_mfma_f32_16x16x32_bf16 v[192:195], v[176:179], v[160:163], v[192:195]\n"
    "v_lshlrev_b32 v113, 7, v113\n"
    "v_mfma_f32_16x16x32_bf16 v[196:199], v[176:179], v[164:167], v[196:199]\n"
    "v_mfma_f32_16x16x32_bf16 v[196:199], v[144:147], v[132:135], v[196:199]\n"
    "v_add_u32_dpp v52, v112, v48 quad_perm:[0,0,0,0] row_mask:0xf bank_mask:0xf\n"
    "v_mfma_f32_16x16x32_bf16 v[200:203], v[144:147], v[136:139], v[200:203]\n"
    "v_mfma_f32_16x16x32_bf16 v[200:203], v[176:179], v[168:171], v[200:203]\n"
    "v_add_u32_dpp v53, v112, v49 quad_perm:[1,1,1,1] row_mask:0xf bank_mask:0xf\n"
    "v_mfma_f32_16x16x32_bf16 v[204:207], v[176:179], v[172:175], v[204:207]\n"
    "v_mfma_f32_16x16x32_bf16 v[204:207], v[144:147], v[140:143], v[204:207]\n"
    "v_add_u32_dpp v54, v112, v50 quad_perm:[2,2,2,2] row_mask:0xf bank_mask:0xf\n"
    "v_mfma_f32_16x16x32_bf16 v[220:223], v[148:151], v[140:143], v[220:223]\n"
    "v_mfma_f32_16x16x32_bf16 v[220:223], v[180:183], v[172:175], v[220:223]\n"
    "v_add_u32_dpp v55, v112, v51 quad_perm:[3,3,3,3] row_mask:0xf bank_mask:0xf\n"
    "v_mfma_f32_16x16x32_bf16 v[216:219], v[180:183], v[168:171], v[216:219]\n"
    "v_mfma_f32_16x16x32_bf16 v[216:219], v[148:151], v[136:139], v[216:219]\n"
    "v_mfma_f32_16x16x32_bf16 v[212:215], v[148:151], v[132:135], v[212:215]\n"
    "v_mfma_f32_16x16x32_bf16 v[212:215], v[180:183], v[164:167], v[212:215]\n"
    "v_mfma_f32_16x16x32_bf16 v[208:211], v[180:183], v[160:163], v[208:211]\n"
    "v_add_f32 v192, v192, v60\n"
    "v_mfma_f32_16x16x32_bf16 v[208:211], v[148:151], v[128:131], v[208:211]\n"
    "v_add_f32 v193, v193, v61\n"
    "v_mfma_f32_16x16x32_bf16 v[224:227], v[152:155], v[128:131], v[224:227]\n"
    "v_add_f32 v194, v194, v62\n"
    "v_mfma_f32_16x16x32_bf16 v[224:227], v[184:187], v[160:163], v[224:227]\n"
    "v_add_f32 v195, v195, v63\n"
    "v_mfma_f32_16x16x32_bf16 v[228:231], v[184:187], v[164:167], v[228:231]\n"
    "v_add_f32 v196, v196, v60\n"
    "v_mfma_f32_16x16x32_bf16 v[228:231], v[152:155], v[132:135], v[228:231]\n"
    "v_add_f32 v197, v197, v61\n"
    "v_mfma_f32_16x16x32_bf16 v[232:235], v[152:155], v[136:139], v[232:235]\n"
    "v_add_f32 v198, v198, v62\n"
    "v_mfma_f32_16x16x32_bf16 v[232:235], v[184:187], v[168:171], v[232:235]\n"
    "v_add_f32 v199, v199, v63\n"
    "v_mfma_f32_16x16x32_bf16 v[236:239], v[184:187], v[172:175], v[236:239]\n"
    "v_add_f32 v200, v200, v60\n"
    "v_mfma_f32_16x16x32_bf16 v[236:239], v[152:155], v[140:143], v[236:239]\n"
    "v_add_f32 v201, v201, v61\n"
    "v_mfma_f32_16x16x32_bf16 v[252:255], v[156:159], v[140:143], v[252:255]\n"
    "v_add_f32 v202, v202, v62\n"
    "v_mfma_f32_16x16x32_bf16 v[252:255], v[188:191], v[172:175], v[252:255]\n"
    "v_add_f32 v203, v203, v63\n"
    "v_mfma_f32_16x16x32_bf16 v[248:251], v[188:191], v[168:171], v[248:251]\n"
    "v_add_f32 v204, v204, v60\n"
    "v_mfma_f32_16x16x32_bf16 v[248:251], v[156:159], v[136:139], v[248:251]\n"
    "v_add_f32 v205, v205, v61\n"
    "v_mfma_f32_16x16x32_bf16 v[244:247], v[156:159], v[132:135], v[244:247]\n"
    "v_add_f32 v206, v206, v62\n"
    "v_mfma_f32_16x16x32_bf16 v[244:247], v[188:191], v[164:167], v[244:247]\n"
    "v_add_f32 v207, v207, v63\n"
    "v_mfma_f32_16x16x32_bf16 v[240:243], v[188:191], v[160:163], v[240:243]\n"
    "v_mfma_f32_16x16x32_bf16 v[240:243], v[156:159], v[128:131], v[240:243]\n"
    "s_waitcnt vmcnt(8)\n"
    "s_barrier\n"
    "s_cmp_eq_u32 s20, 3\n"
    "s_cbranch_scc1 .Lxd_on1\n"
    ".Lxd_on1r:\n"
    "s_add_u32 m0, s35, 49152\n"
    "s_nop 0\n"
    "global_load_lds_dwordx4 v46, s[30:31] offset:0\n"
    "ds_read_b128 v[128:131], v58 offset:0\n"
    "ds_read_b128 v[132:135], v58 offset:2048\n"
    "ds_read_b128 v[136:139], v58 offset:4096\n"
    "global_load_lds_dwordx4 v47, s[30:31] offset:1024\n"
    "s_add_u32 s30, s30, 212992\n"
    "s_addc_u32 s31, s31, 0\n"
    "ds_read_b128 v[140:143], v58 offset:6144\n"
    "ds_read_b64_tr_b16 v[144:145], v80 offset:0\n"
    "ds_read_b64_tr_b16 v[146:147], v80 offset:1024\n"
    "s_add_u32 m0, s34, 49152\n"
    "s_nop 0\n"
    "global_load_lds_dwordx4 v52, s[36:37] offset:0\n"
    "ds_read_b64_tr_b16 v[148:149], v81 offset:0\n"
    "ds_read_b64_tr_b16 v[150:151], v81 offset:1024\n"
    "ds_read_b64_tr_b16 v[152:153], v82 offset:0\n"
    "global_load_lds_dwordx4 v53, s[36:37] offset:1024\n"
    "ds_read_b64_tr_b16 v[154:155], v82 offset:1024\n"
    "ds_read_b64_tr_b16 v[156:157], v83 offset:0\n"
    "ds_read_b64_tr_b16 v[158:159], v83 offset:1024\n"
    "global_load_lds_dwordx4 v54, s[36:37] offset:2048\n"
    "ds_read_b128 v[160:163], v59 offset:0\n"
    "ds_read_b128 v[164:167], v59 offset:2048\n"
    "ds_read_b128 v[168:171], v59 offset:4096\n"
    "global_load_lds_dwordx4 v55, s[36:37] offset:3072\n"
    "ds_read_b128 v[172:175], v59 offset:6144\n"
    "ds_read_b64_tr_b16 v[176:177], v80 offset:8192\n"
    "ds_read_b64_tr_b16 v[178:179], v80 offset:9216\n"
    "global_load_dwordx2 v[122:123], v42, s[18:19] offset:64\n"
    "ds_read_b64_tr_b16 v[180:181], v81 offset:8192\n"
    "ds_read_b64_tr_b16 v[182:183], v81 offset:9216\n"
    "ds_read_b64_tr_b16 v[184:185], v82 offset:8192\n"
    "ds_read_b64_tr_b16 v[186:187], v82 offset:9216\n"
    "ds_read_b64_tr_b16 v[188:189], v83 offset:8192\n"
    "ds_read_b64_tr_b16 v[190:191], v83 offset:9216\n"
    "s_waitcnt vmcnt(12) lgkmcnt(0)\n"
    "s_barrier\n"
    "v_mfma_f32_16x16x32_bf16 v[192:195], v[144:147], v[128:131], v[192:195]\n"
    "v_mfma_f32_16x16x32_bf16 v[192:195], v[176:179], v[160:163], v[192:195]\n"
    "v_lshlrev_b32 v114, 7, v114\n"
    "v_mfma_f32_16x16x32_bf16 v[196:199], v[176:179], v[164:167], v[196:199]\n"
    "v_mfma_f32_16x16x32_bf16 v[196:199], v[144:147], v[132:135], v[196:199]\n"
    "v_add_u32_dpp v52, v113, v48 quad_perm:[0,0,0,0] row_mask:0xf bank_mask:0xf\n"
    "v_mfma_f32_16x16x32_bf16 v[200:203], v[144:147], v[136:139], v[200:203]\n"
    "v_mfma_f32_16x16x32_bf16 v[200:203], v[176:179], v[168:171], v[200:203]\n"
    "v_add_u32_dpp v53, v113, v49 quad_perm:[1,1,1,1] row_mask:0xf bank_mask:0xf\n"
    "v_mfma_f32_16x16x32_bf16 v[204:207], v[176:179], v[172:175], v[204:207]\n"
    "v_mfma_f32_16x16x32_bf16 v[204:207], v[144:147], v[140:143], v[204:207]\n"
    "v_add_u32_dpp v54, v113, v50 quad_perm:[2,2,2,2] row_mask:0xf bank_mask:0xf\n"
    "v_mfma_f32_16x16x32_bf16 v[220:223], v[148:151], v[140:143], v[220:223]\n"
    "v_mfma_f32_16x16x32_bf16 v[220:223], v[180:183], v[172:175], v[220:223]\n"
    "v_add_u32_dpp v55, v113, v51 quad_perm:[3,3,3,3] row_mask:0xf bank_mask:0xf\n"
    "v_mfma_f32_16x16x32_bf16 v[216:219], v[180:183], v[168:171], v[216:219]\n"
    "v_mfma_f32_16x16x32_bf16 v[216:219], v[148:151], v[136:139], v[216:219]\n"
    "v_mfma_f32_16x16x32_bf16 v[212:215], v[148:151], v[132:135], v[212:215]\n"
    "v_mfma_f32_16x16x32_bf16 v[212:215], v[180:183], v[164:167], v[212:215]\n"
    "v_mfma_f32_16x16x32_bf16 v[208:211], v[180:183], v[160:163], v[208:211]\n"
    "v_mfma_f32_16x16x32_bf16 v[208:211], v[148:151], v[128:131], v[208:211]\n"
    "v_mfma_f32_16x16x32_bf16 v[224:227], v[152:155], v[128:131], v[224:227]\n"
    "v_mfma_f32_16x16x32_bf16 v[224:227], v[184:187], v[160:163], v[224:227]\n"
    "v_mfma_f32_16x16x32_bf16 v[228:231], v[184:187], v[164:167], v[228:231]\n"
    "v_add_f32 v222, v222, v66\n"
    "v_add_f32 v223, v223, v67\n"
    "v_mfma_f32_16x16x32_bf16 v[228:231], v[152:155], v[132:135], v[228:231]\n"
    "v_add_f32 v221, v221, v65\n"
    "v_mfma_f32_16x16x32_bf16 v[232:235], v[152:155], v[136:139], v[232:235]\n"
    "v_add_f32 v220, v220, v64\n"
    "v_mfma_f32_16x16x32_bf16 v[232:235], v[184:187], v[168:171], v[232:235]\n"
    "v_add_f32 v219, v219, v67\n"
    "v_mfma_f32_16x16x32_bf16 v[236:239], v[184:187], v[172:175], v[236:239]\n"
    "v_add_f32 v217, v217, v65\n"
    "v_add_f32 v218, v218, v66\n"
    "v_mfma_f32_16x16x32_bf16 v[236:239], v[152:155], v[140:143], v[236:239]\n"
    "v_add_f32 v216, v216, v64\n"
    "v_mfma_f32_16x16x32_bf16 v[252:255], v[156:159], v[140:143], v[252:255]\n"
    "v_add_f32 v215, v215, v67\n"
    "v_mfma_f32_16x16x32_bf16 v[252:255], v[188:191], v[172:175], v[252:255]\n"
    "v_add_f32 v214, v214, v66\n"
    "v_mfma_f32_16x16x32_bf16 v[248:251], v[188:191], v[168:171], v[248:251]\n"
    "v_add_f32 v212, v212, v64\n"
    "v_add_f32 v213, v213, v65\n"
    "v_mfma_f32_16x16x32_bf16 v[248:251], v[156:159], v[136:139], v[248:251]\n"
    "v_add_f32 v211, v211, v67\n"
    "v_mfma_f32_16x16x32_bf16 v[244:247], v[156:159], v[132:135], v[244:247]\n"
    "v_add_f32 v210, v210, v66\n"
    "v_mfma_f32_16x16x32_bf16 v[244:247], v[188:191], v[164:167], v[244:247]\n"
    "v_add_f32 v209, v209, v65\n"
    "v_mfma_f32_16x16x32_bf16 v[240:243], v[188:191], v[160:163], v[240:243]\n"
    "v_add_f32 v208, v208, v64\n"
    "v_mfma_f32_16x16x32_bf16 v[240:243], v[156:159], v[128:131], v[240:243]\n"
    "s_waitcnt vmcnt(8)\n"
    "s_barrier\n"
    "s_add_u32 m0, s35, 98304\n"
    "s_nop 0\n"
    "global_load_lds_dwordx4 v46, s[30:31] offset:0\n"
    "ds_read_b128 v[128:131], v56 offset:0\n"
    "ds_read_b128 v[132:135], v56 offset:2048\n"
    "ds_read_b128 v[136:139], v56 offset:4096\n"
    "global_load_lds_dwordx4 v47, s[30:31] offset:1024\n"
    "s_add_u32 s66, s28, 6\n"
    "s_and_b32 s66, s66, 7\n"
    "s_mul_i32 s66, s66, 638976\n"
    "s_add_u32 s30, s6, s66\n"
    "s_addc_u32 s31, s7, 0\n"
    "ds_read_b128 v[140:143], v56 offset:6144\n"
    "ds_read_b64_tr_b16 v[144:145], v76 offset:0\n"
    "ds_read_b64_tr_b16 v[146:147], v76 offset:1024\n"
    "s_add_u32 m0, s34, 98304\n"
    "s_nop 0\n"
    "global_load_lds_dwordx4 v52, s[36:37] offset:0\n"
    "ds_read_b64_tr_b16 v[148:149], v77 offset:0\n"
    "ds_read_b64_tr_b16 v[150:151], v77 offset:1024\n"
    "ds_read_b64_tr_b16 v[152:153], v78 offset:0\n"
    "global_load_lds_dwordx4 v53, s[36:37] offset:1024\n"
    "ds_read_b64_tr_b16 v[154:155], v78 offset:1024\n"
    "ds_read_b64_tr_b16 v[156:157], v79 offset:0\n"
    "ds_read_b64_tr_b16 v[158:159], v79 offset:1024\n"
    "global_load_lds_dwordx4 v54, s[36:37] offset:2048\n"
    "ds_read_b128 v[160:163], v57 offset:0\n"
    "ds_read_b128 v[164:167], v57 offset:2048\n"
    "ds_read_b128 v[168:171], v57 offset:4096\n"
    "global_load_lds_dwordx4 v55, s[36:37] offset:3072\n"
    "ds_read_b128 v[172:175], v57 offset:6144\n"
    "ds_read_b64_tr_b16 v[176:177], v76 offset:8192\n"
    "ds_read_b64_tr_b16 v[178:179], v76 offset:9216\n"
    "global_load_dwordx2 v[124:125], v42, s[18:19] offset:96\n"
    "ds_read_b64_tr_b16 v[180:181], v77 offset:8192\n"
    "ds_read_b64_tr_b16 v[182:183], v77 offset:9216\n"
    "ds_read_b64_tr_b16 v[184:185], v78 offset:8192\n"
    "ds_read_b64_tr_b16 v[186:187], v78 offset:9216\n"
    "ds_read_b64_tr_b16 v[188:189], v79 offset:8192\n"
    "ds_read_b64_tr_b16 v[190:191], v79 offset:9216\n"
    "s_waitcnt vmcnt(12) lgkmcnt(0)\n"
    "s_barrier\n"
    "v_mfma_f32_16x16x32_bf16 v[192:195], v[144:147], v[128:131], v[192:195]\n"
    "v_lshlrev_b32 v115, 7, v115\n"
    "v_mfma_f32_16x16x32_bf16 v[192:195], v[176:179], v[160:163], v[192:195]\n"
    "v_add_u32_dpp v52, v114, v48 quad_perm:[0,0,0,0] row_mask:0xf bank_mask:0xf\n"
    "v_mfma_f32_16x16x32_bf16 v[196:199], v[176:179], v[164:167], v[196:199]\n"
    "v_add_u32_dpp v53, v114, v49 quad_perm:[1,1,1,1] row_mask:0xf bank_mask:0xf\n"
    "v_mfma_f32_16x16x32_bf16 v[196:199], v[144:147], v[132:135], v[196:199]\n"
    "v_add_u32_dpp v54, v114, v50 quad_perm:[2,2,2,2] row_mask:0xf bank_mask:0xf\n"
    "v_mfma_f32_16x16x32_bf16 v[200:203], v[144:147], v[136:139], v[200:203]\n"
    "v_add_u32_dpp v55, v114, v51 quad_perm:[3,3,3,3] row_mask:0xf bank_mask:0xf\n"
    "v_mfma_f32_16x16x32_bf16 v[200:203], v[176:179], v[168:171], v[200:203]\n"
    "v_lshl_or_b32 v36, v36, 7, v19\n"
    "v_mfma_f32_16x16x32_bf16 v[204:207], v[176:179], v[172:175], v[204:207]\n"
    "v_mfma_f32_16x16x32_bf16 v[204:207], v[144:147], v[140:143], v[204:207]\n"
    "v_lshl_or_b32 v37, v37, 7, v19\n"
    "v_mfma_f32_16x16x32_bf16 v[220:223], v[148:151], v[140:143], v[220:223]\n"
    "v_lshl_or_b32 v38, v38, 7, v19\n"
    "v_mfma_f32_16x16x32_bf16 v[220:223], v[180:183], v[172:175], v[220:223]\n"
    "v_lshl_or_b32 v41, v41, 7, v20\n"
    "v_mfma_f32_16x16x32_bf16 v[216:219], v[180:183], v[168:171], v[216:219]\n"
    "s_mov_b32 s64, 0xbdd2d3e8\n"
    "v_mfma_f32_16x16x32_bf16 v[216:219], v[148:151], v[136:139], v[216:219]\n"
    "s_mov_b32 s65, 0xc0135761\n"
    "v_mfma_f32_16x16x32_bf16 v[212:215], v[148:151], v[132:135], v[212:215]\n"
    "v_mfma_f32_16x16x32_bf16 v[212:215], v[180:183], v[164:167], v[212:215]\n"
    "s_and_b32 s41, s20, 1\n"
    "v_mfma_f32_16x16x32_bf16 v[208:211], v[180:183], v[160:163], v[208:211]\n"
    "s_lshl1_add_u32 s40, s21, s41\n"
    "v_mfma_f32_16x16x32_bf16 v[208:211], v[148:151], v[128:131], v[208:211]\n"
    "s_lshl_b32 s41, s40, 8\n"
    "v_mfma_f32_16x16x32_bf16 v[224:227], v[152:155], v[128:131], v[224:227]\n"
    "s_add_u32 s3, s41, 149504\n"
    "v_mfma_f32_16x16x32_bf16 v[224:227], v[184:187], v[160:163], v[224:227]\n"
    "s_lshl_b32 s40, s40, 6\n"
    "v_mfma_f32_16x16x32_bf16 v[228:231], v[184:187], v[164:167], v[228:231]\n"
    "v_mfma_f32_16x16x32_bf16 v[228:231], v[152:155], v[132:135], v[228:231]\n"
    "s_add_u32 s40, s40, s24\n"
    "v_mfma_f32_16x16x32_bf16 v[232:235], v[152:155], v[136:139], v[232:235]\n"
    "s_lshl_b32 s41, s23, 12\n"
    "v_mfma_f32_16x16x32_bf16 v[232:235], v[184:187], v[168:171], v[232:235]\n"
    "s_add_u32 s40, s40, s41\n"
    "v_mfma_f32_16x16x32_bf16 v[236:239], v[184:187], v[172:175], v[236:239]\n"
    "s_lshl3_add_u32 s40, s40, 65536\n"
    "v_mfma_f32_16x16x32_bf16 v[236:239], v[152:155], v[140:143], v[236:239]\n"
    "s_add_u32 s48, s40, s16\n"
    "v_mfma_f32_16x16x32_bf16 v[252:255], v[156:159], v[140:143], v[252:255]\n"
    "v_mfma_f32_16x16x32_bf16 v[252:255], v[188:191], v[172:175], v[252:255]\n"
    "s_addc_u32 s49, s17, 0\n"
    "v_mfma_f32_16x16x32_bf16 v[248:251], v[188:191], v[168:171], v[248:251]\n"
    "s_cmp_lt_u32 s22, 2\n"
    "v_mfma_f32_16x16x32_bf16 v[248:251], v[156:159], v[136:139], v[248:251]\n"
    "s_cselect_b32 s24, 16, 0\n"
    "v_mfma_f32_16x16x32_bf16 v[244:247], v[156:159], v[132:135], v[244:247]\n"
    "s_sub_u32 s23, 3, s22\n"
    "v_mfma_f32_16x16x32_bf16 v[244:247], v[188:191], v[164:167], v[244:247]\n"
    "s_and_b32 s23, s23, 3\n"
    "v_mfma_f32_16x16x32_bf16 v[240:243], v[188:191], v[160:163], v[240:243]\n"
    "v_mfma_f32_16x16x32_bf16 v[240:243], v[156:159], v[128:131], v[240:243]\n"
    "s_waitcnt vmcnt(8)\n"
    "s_barrier\n"
    "s_add_u32 m0, s35, 0\n"
    "s_nop 0\n"
    "global_load_lds_dwordx4 v46, s[30:31] offset:0\n"
    "ds_read_b128 v[128:131], v56 offset:49152\n"
    "ds_read_b128 v[132:135], v56 offset:51200\n"
    "ds_read_b128 v[136:139], v56 offset:53248\n"
    "global_load_lds_dwordx4 v47, s[30:31] offset:1024\n"
    "s_add_u32 s30, s30, 212992\n"
    "s_addc_u32 s31, s31, 0\n"
    "ds_read_b128 v[140:143], v56 offset:55296\n"
    "ds_read_b64_tr_b16 v[144:145], v76 offset:49152\n"
    "ds_read_b64_tr_b16 v[146:147], v76 offset:50176\n"
    "s_add_u32 m0, s34, 0\n"
    "s_nop 0\n"
    "global_load_lds_dwordx4 v52, s[36:37] offset:0\n"
    "ds_read_b64_tr_b16 v[148:149], v77 offset:49152\n"
    "ds_read_b64_tr_b16 v[150:151], v77 offset:50176\n"
    "ds_read_b64_tr_b16 v[152:153], v78 offset:49152\n"
    "global_load_lds_dwordx4 v53, s[36:37] offset:1024\n"
    "ds_read_b64_tr_b16 v[154:155], v78 offset:50176\n"
    "ds_read_b64_tr_b16 v[156:157], v79 offset:49152\n"
    "ds_read_b64_tr_b16 v[158:159], v79 offset:50176\n"
    "global_load_lds_dwordx4 v54, s[36:37] offset:2048\n"
    "ds_read_b128 v[160:163], v57 offset:49152\n"
    "ds_read_b128 v[164:167], v57 offset:51200\n"
    "ds_read_b128 v[168:171], v57 offset:53248\n"
    "global_load_lds_dwordx4 v55, s[36:37] offset:3072\n"
    "ds_read_b128 v[172:175], v57 offset:55296\n"
    "ds_read_b64_tr_b16 v[176:177], v76 offset:57344\n"
    "ds_read_b64_tr_b16 v[178:179], v76 offset:58368\n"
    "global_load_dwordx4 v[28:31], v36, s[8:9]\n"
    "ds_read_b64_tr_b16 v[180:181], v77 offset:57344\n"
    "ds_read_b64_tr_b16 v[182:183], v77 offset:58368\n"
    "ds_read_b64_tr_b16 v[184:185], v78 offset:57344\n"
    "ds_read_b64_tr_b16 v[186:187], v78 offset:58368\n"
    "ds_read_b64_tr_b16 v[188:189], v79 offset:57344\n"
    "ds_read_b64_tr_b16 v[190:191], v79 offset:58368\n"
    "s_waitcnt vmcnt(12) lgkmcnt(0)\n"
    "s_barrier\n"
    "v_mfma_f32_16x16x32_bf16 v[192:195], v[144:147], v[128:131], v[192:195]\n"
    "v_mfma_f32_16x16x32_bf16 v[192:195], v[176:179], v[160:163], v[192:195]\n"
    "v_add_f32 v224, v224, v68\n"
    "v_add_f32 v225, v225, v69\n"
    "v_mfma_f32_16x16x32_bf16 v[196:199], v[176:179], v[164:167], v[196:199]\n"
    "v_add_f32 v226, v226, v70\n"
    "v_mfma_f32_16x16x32_bf16 v[196:199], v[144:147], v[132:135], v[196:199]\n"
    "v_add_f32 v227, v227, v71\n"
    "v_mfma_f32_16x16x32_bf16 v[200:203], v[144:147], v[136:139], v[200:203]\n"
    "v_add_f32 v228, v228, v68\n"
    "v_mfma_f32_16x16x32_bf16 v[200:203], v[176:179], v[168:171], v[200:203]\n"
    "v_add_f32 v229, v229, v69\n"
    "v_add_f32 v230, v230, v70\n"
    "v_mfma_f32_16x16x32_bf16 v[204:207], v[176:179], v[172:175], v[204:207]\n"
    "v_add_f32 v231, v231, v71\n"
    "v_mfma_f32_16x16x32_bf16 v[204:207], v[144:147], v[140:143], v[204:207]\n"
    "v_add_f32 v232, v232, v68\n"
    "v_mfma_f32_16x16x32_bf16 v[220:223], v[148:151], v[140:143], v[220:223]\n"
    "v_add_f32 v233, v233, v69\n"
    "v_mfma_f32_16x16x32_bf16 v[220:223], v[180:183], v[172:175], v[220:223]\n"
    "v_add_f32 v234, v234, v70\n"
    "v_add_f32 v235, v235, v71\n"
    "v_mfma_f32_16x16x32_bf16 v[216:219], v[180:183], v[168:171], v[216:219]\n"
    "v_add_f32 v236, v236, v68\n"
    "v_mfma_f32_16x16x32_bf16 v[216:219], v[148:151], v[136:139], v[216:219]\n"
    "v_add_f32 v237, v237, v69\n"
    "v_mfma_f32_16x16x32_bf16 v[212:215], v[148:151], v[132:135], v[212:215]\n"
    "v_add_f32 v238, v238, v70\n"
    "v_mfma_f32_16x16x32_bf16 v[212:215], v[180:183], v[164:167], v[212:215]\n"
    "v_add_f32 v239, v239, v71\n"
    "v_mfma_f32_16x16x32_bf16 v[208:211], v[180:183], v[160:163], v[208:211]\n"
    "v_mfma_f32_16x16x32_bf16 v[208:211], v[148:151], v[128:131], v[208:211]\n"
    "v_mfma_f32_16x16x32_bf16 v[224:227], v[152:155], v[128:131], v[224:227]\n"
    "v_mfma_f32_16x16x32_bf16 v[224:227], v[184:187], v[160:163], v[224:227]\n"
    "v_mfma_f32_16x16x32_bf16 v[228:231], v[184:187], v[164:167], v[228:231]\n"
    "v_lshlrev_b32 v116, 7, v116\n"
    "v_mfma_f32_16x16x32_bf16 v[228:231], v[152:155], v[132:135], v[228:231]\n"
    "v_add_u32_dpp v52, v115, v48 quad_perm:[0,0,0,0] row_mask:0xf bank_mask:0xf\n"
    "v_mfma_f32_16x16x32_bf16 v[232:235], v[152:155], v[136:139], v[232:235]\n"
    "v_add_u32_dpp v53, v115, v49 quad_perm:[1,1,1,1] row_mask:0xf bank_mask:0xf\n"
    "v_mfma_f32_16x16x32_bf16 v[232:235], v[184:187], v[168:171], v[232:235]\n"
    "v_add_u32_dpp v54, v115, v50 quad_perm:[2,2,2,2] row_mask:0xf bank_mask:0xf\n"
    "v_mfma_f32_16x16x32_bf16 v[236:239], v[184:187], v[172:175], v[236:239]\n"
    "v_add_u32_dpp v55, v115, v51 quad_perm:[3,3,3,3] row_mask:0xf bank_mask:0xf\n"
    "v_mfma_f32_16x16x32_bf16 v[236:239], v[152:155], v[140:143], v[236:239]\n"
    "s_getpc_b64 s[10:11]\n"
    ".Lxd_en2:\n"
    "v_mfma_f32_16x16x32_bf16 v[252:255], v[156:159], v[140:143], v[252:255]\n"
    "s_mov_b32 s40, .Lxd_ph1-.Lxd_en2\n"
    "v_mfma_f32_16x16x32_bf16 v[252:255], v[188:191], v[172:175], v[252:255]\n"
    "s_cmp_eq_u32 s22, 1\n"
    "s_cselect_b32 s40, .Lxd_ph0-.Lxd_en2, s40\n"
    "v_mfma_f32_16x16x32_bf16 v[248:251], v[188:191], v[168:171], v[248:251]\n"
    "s_cmp_eq_u32 s22, 2\n"
    "s_cselect_b32 s40, .Lxd_pl3-.Lxd_en2, s40\n"
    "v_mfma_f32_16x16x32_bf16 v[248:251], v[156:159], v[136:139], v[248:251]\n"
    "s_cmp_eq_u32 s22, 3\n"
    "s_cselect_b32 s40, .Lxd_ph2-.Lxd_en2, s40\n"
    "v_mfma_f32_16x16x32_bf16 v[244:247], v[156:159], v[132:135], v[244:247]\n"
    "s_ashr_i32 s41, s40, 31\n"
    "v_mfma_f32_16x16x32_bf16 v[244:247], v[188:191], v[164:167], v[244:247]\n"
    "s_add_u32 s10, s10, s40\n"
    "s_addc_u32 s11, s11, s41\n"
    "v_mfma_f32_16x16x32_bf16 v[240:243], v[188:191], v[160:163], v[240:243]\n"
    "v_mfma_f32_16x16x32_bf16 v[240:243], v[156:159], v[128:131], v[240:243]\n"
    "s_waitcnt vmcnt(8)\n"
    "s_barrier\n"
    "s_add_u32 m0, s35, 49152\n"
    "s_nop 0\n"
    "global_load_lds_dwordx4 v46, s[30:31] offset:0\n"
    "ds_read_b128 v[128:131], v58 offset:0\n"
    "ds_read_b128 v[132:135], v58 offset:2048\n"
    "ds_read_b128 v[136:139], v58 offset:4096\n"
    "global_load_lds_dwordx4 v47, s[30:31] offset:1024\n"
    "s_add_u32 s30, s30, 212992\n"
    "s_addc_u32 s31, s31, 0\n"
    "ds_read_b128 v[140:143], v58 offset:6144\n"
    "ds_read_b64_tr_b16 v[144:145], v80 offset:0\n"
    "ds_read_b64_tr_b16 v[146:147], v80 offset:1024\n"
    "s_add_u32 m0, s34, 49152\n"
    "s_nop 0\n"
    "global_load_lds_dwordx4 v52, s[36:37] offset:0\n"
    "ds_read_b64_tr_b16 v[148:149], v81 offset:0\n"
    "ds_read_b64_tr_b16 v[150:151], v81 offset:1024\n"
    "ds_read_b64_tr_b16 v[152:153], v82 offset:0\n"
    "global_load_lds_dwordx4 v53, s[36:37] offset:1024\n"
    "ds_read_b64_tr_b16 v[154:155], v82 offset:1024\n"
    "ds_read_b64_tr_b16 v[156:157], v83 offset:0\n"
    "ds_read_b64_tr_b16 v[158:159], v83 offset:1024\n"
    "global_load_lds_dwordx4 v54, s[36:37] offset:2048\n"
    "ds_read_b128 v[160:163], v59 offset:0\n"
    "ds_read_b128 v[164:167], v59 offset:2048\n"
    "ds_read_b128 v[168:171], v59 offset:4096\n"
    "global_load_lds_dwordx4 v55, s[36:37] offset:3072\n"
    "ds_read_b128 v[172:175], v59 offset:6144\n"
    "ds_read_b64_tr_b16 v[176:177], v80 offset:8192\n"
    "ds_read_b64_tr_b16 v[178:179], v80 offset:9216\n"
    "global_load_dwordx4 v[32:35], v37, s[8:9]\n"
    "ds_read_b64_tr_b16 v[180:181], v81 offset:8192\n"
    "ds_read_b64_tr_b16 v[182:183], v81 offset:9216\n"
    "ds_read_b64_tr_b16 v[184:185], v82 offset:8192\n"
    "ds_read_b64_tr_b16 v[186:187], v82 offset:9216\n"
    "ds_read_b64_tr_b16 v[188:189], v83 offset:8192\n"
    "ds_read_b64_tr_b16 v[190:191], v83 offset:9216\n"
    "s_waitcnt vmcnt(12) lgkmcnt(0)\n"
    "s_barrier\n"
    "v_mfma_f32_16x16x32_bf16 v[192:195], v[144:147], v[128:131], v[192:195]\n"
    "v_mfma_f32_16x16x32_bf16 v[192:195], v[176:179], v[160:163], v[192:195]\n"
    "v_mfma_f32_16x16x32_bf16 v[196:199], v[176:179], v[164:167], v[196:199]\n"
    "v_mfma_f32_16x16x32_bf16 v[196:199], v[144:147], v[132:135], v[196:199]\n"
    "v_mfma_f32_16x16x32_bf16 v[200:203], v[144:147], v[136:139], v[200:203]\n"
    "v_add_f32 v240, v240, v72\n"
    "v_mfma_f32_16x16x32_bf16 v[200:203], v[176:179], v[168:171], v[200:203]\n"
    "v_add_f32 v241, v241, v73\n"
    "v_mfma_f32_16x16x32_bf16 v[204:207], v[176:179], v[172:175], v[204:207]\n"
    "v_add_f32 v242, v242, v74\n"
    "v_mfma_f32_16x16x32_bf16 v[204:207], v[144:147], v[140:143], v[204:207]\n"
    "v_add_f32 v243, v243, v75\n"
    "v_mfma_f32_16x16x32_bf16 v[220:223], v[148:151], v[140:143], v[220:223]\n"
    "v_add_f32 v244, v244, v72\n"
    "v_mfma_f32_16x16x32_bf16 v[220:223], v[180:183], v[172:175], v[220:223]\n"
    "v_add_f32 v245, v245, v73\n"
    "v_mfma_f32_16x16x32_bf16 v[216:219], v[180:183], v[168:171], v[216:219]\n"
    "v_add_f32 v246, v246, v74\n"
    "v_mfma_f32_16x16x32_bf16 v[216:219], v[148:151], v[136:139], v[216:219]\n"
    "v_add_f32 v247, v247, v75\n"
    "v_mfma_f32_16x16x32_bf16 v[212:215], v[148:151], v[132:135], v[212:215]\n"
    "v_add_f32 v248, v248, v72\n"
    "v_mfma_f32_16x16x32_bf16 v[212:215], v[180:183], v[164:167], v[212:215]\n"
    "v_add_f32 v249, v249, v73\n"
    "v_mfma_f32_16x16x32_bf16 v[208:211], v[180:183], v[160:163], v[208:211]\n"
    "v_add_f32 v250, v250, v74\n"
    "v_mfma_f32_16x16x32_bf16 v[208:211], v[148:151], v[128:131], v[208:211]\n"
    "v_add_f32 v251, v251, v75\n"
    "v_mfma_f32_16x16x32_bf16 v[224:227], v[152:155], v[128:131], v[224:227]\n"
    "v_add_f32 v252, v252, v72\n"
    "v_mfma_f32_16x16x32_bf16 v[224:227], v[184:187], v[160:163], v[224:227]\n"
    "v_add_f32 v253, v253, v73\n"
    "v_mfma_f32_16x16x32_bf16 v[228:231], v[184:187], v[164:167], v[228:231]\n"
    "v_lshlrev_b32 v117, 7, v117\n"
    "v_add_f32 v254, v254, v74\n"
    "v_mfma_f32_16x16x32_bf16 v[228:231], v[152:155], v[132:135], v[228:231]\n"
    "v_add_f32 v255, v255, v75\n"
    "v_mfma_f32_16x16x32_bf16 v[232:235], v[152:155], v[136:139], v[232:235]\n"
    "v_add_u32_dpp v52, v116, v48 quad_perm:[0,0,0,0] row_mask:0xf bank_mask:0xf\n"
    "v_mfma_f32_16x16x32_bf16 v[232:235], v[184:187], v[168:171], v[232:235]\n"
    "v_mfma_f32_16x16x32_bf16 v[236:239], v[184:187], v[172:175], v[236:239]\n"
    "v_mfma_f32_16x16x32_bf16 v[236:239], v[152:155], v[140:143], v[236:239]\n"
    "v_add_u32_dpp v53, v116, v49 quad_perm:[1,1,1,1] row_mask:0xf bank_mask:0xf\n"
    "v_mfma_f32_16x16x32_bf16 v[252:255], v[156:159], v[140:143], v[252:255]\n"
    "v_mfma_f32_16x16x32_bf16 v[252:255], v[188:191], v[172:175], v[252:255]\n"
    "v_add_u32_dpp v54, v116, v50 quad_perm:[2,2,2,2] row_mask:0xf bank_mask:0xf\n"
    "v_mfma_f32_16x16x32_bf16 v[248:251], v[188:191], v[168:171], v[248:251]\n"
    "v_mfma_f32_16x16x32_bf16 v[248:251], v[156:159], v[136:139], v[248:251]\n"
    "v_mfma_f32_16x16x32_bf16 v[244:247], v[156:159], v[132:135], v[244:247]\n"
    "v_add_u32_dpp v55, v116, v51 quad_perm:[3,3,3,3] row_mask:0xf bank_mask:0xf\n"
    "v_mfma_f32_16x16x32_bf16 v[244:247], v[188:191], v[164:167], v[244:247]\n"
    "v_mfma_f32_16x16x32_bf16 v[240:243], v[188:191], v[160:163], v[240:243]\n"
    "v_mfma_f32_16x16x32_bf16 v[240:243], v[156:159], v[128:131], v[240:243]\n"
    "s_waitcnt vmcnt(8)\n"
    "s_barrier\n"
    "s_add_u32 m0, s35, 98304\n"
    "s_nop 0\n"
    "global_load_lds_dwordx4 v46, s[30:31] offset:0\n"
    "ds_read_b128 v[128:131], v56 offset:0\n"
    "ds_read_b128 v[132:135], v56 offset:2048\n"
    "ds_read_b128 v[136:139], v56 offset:4096\n"
    "global_load_lds_dwordx4 v47, s[30:31] offset:1024\n"
    "s_add_u32 s66, s28, 7\n"
    "s_and_b32 s66, s66, 7\n"
    "s_mul_i32 s66, s66, 638976\n"
    "s_add_u32 s30, s6, s66\n"
    "s_addc_u32 s31, s7, 0\n"
    "ds_read_b128 v[140:143], v56 offset:6144\n"
    "ds_read_b64_tr_b16 v[144:145], v76 offset:0\n"
    "ds_read_b64_tr_b16 v[146:147], v76 offset:1024\n"
    "s_add_u32 m0, s34, 98304\n"
    "s_nop 0\n"
    "global_load_lds_dwordx4 v52, s[36:37] offset:0\n"
    "ds_read_b64_tr_b16 v[148:149], v77 offset:0\n"
    "ds_read_b64_tr_b16 v[150:151], v77 offset:1024\n"
    "ds_read_b64_tr_b16 v[152:153], v78 offset:0\n"
    "global_load_lds_dwordx4 v53, s[36:37] offset:1024\n"
    "ds_read_b64_tr_b16 v[154:155], v78 offset:1024\n"
    "ds_read_b64_tr_b16 v[156:157], v79 offset:0\n"
    "ds_read_b64_tr_b16 v[158:159], v79 offset:1024\n"
    "global_load_lds_dwordx4 v54, s[36:37] offset:2048\n"
    "ds_read_b128 v[160:163], v57 offset:0\n"
    "ds_read_b128 v[164:167], v57 offset:2048\n"
    "ds_read_b128 v[168:171], v57 offset:4096\n"
    "global_load_lds_dwordx4 v55, s[36:37] offset:3072\n"
    "ds_read_b128 v[172:175], v57 offset:6144\n"
    "ds_read_b64_tr_b16 v[176:177], v76 offset:8192\n"
    "ds_read_b64_tr_b16 v[178:179], v76 offset:9216\n"
    "global_load_dword v40, v41, s[8:9]\n"
    "ds_read_b64_tr_b16 v[180:181], v77 offset:8192\n"
    "ds_read_b64_tr_b16 v[182:183], v77 offset:9216\n"
    "ds_read_b64_tr_b16 v[184:185], v78 offset:8192\n"
    "ds_read_b64_tr_b16 v[186:187], v78 offset:9216\n"
    "ds_read_b64_tr_b16 v[188:189], v79 offset:8192\n"
    "ds_read_b64_tr_b16 v[190:191], v79 offset:9216\n"
    "s_waitcnt vmcnt(12) lgkmcnt(0)\n"
    "s_barrier\n"
    "v_mfma_f32_16x16x32_bf16 v[192:195], v[144:147], v[128:131], v[192:195]\n"
    "v_lshlrev_b32 v118, 7, v118\n"
    "v_mfma_f32_16x16x32_bf16 v[192:195], v[176:179], v[160:163], v[192:195]\n"
    "v_add_u32_dpp v52, v117, v48 quad_perm:[0,0,0,0] row_mask:0xf bank_mask:0xf\n"
    "v_mfma_f32_16x16x32_bf16 v[196:199], v[176:179], v[164:167], v[196:199]\n"
    "v_add_u32_dpp v53, v117, v49 quad_perm:[1,1,1,1] row_mask:0xf bank_mask:0xf\n"
    "v_mfma_f32_16x16x32_bf16 v[196:199], v[144:147], v[132:135], v[196:199]\n"
    "v_add_u32_dpp v54, v117, v50 quad_perm:[2,2,2,2] row_mask:0xf bank_mask:0xf\n"
    "v_mfma_f32_16x16x32_bf16 v[200:203], v[144:147], v[136:139], v[200:203]\n"
    "v_add_u32_dpp v55, v117, v51 quad_perm:[3,3,3,3] row_mask:0xf bank_mask:0xf\n"
    "v_mfma_f32_16x16x32_bf16 v[200:203], v[176:179], v[168:171], v[200:203]\n"
    "v_readlane_b32 s25, v26, 9\n"
    "v_mfma_f32_16x16x32_bf16 v[204:207], v[176:179], v[172:175], v[204:207]\n"
    "v_readlane_b32 s40, v26, 8\n"
    "v_mfma_f32_16x16x32_bf16 v[204:207], v[144:147], v[140:143], v[204:207]\n"
    "v_readlane_b32 s41, v27, 8\n"
    "v_mfma_f32_16x16x32_bf16 v[220:223], v[148:151], v[140:143], v[220:223]\n"
    "s_mul_i32 s42, s25, 0x9e3779b1\n"
    "v_mfma_f32_16x16x32_bf16 v[220:223], v[180:183], v[172:175], v[220:223]\n"
    "s_lshr_b32 s42, s42, 24\n"
    "v_mfma_f32_16x16x32_bf16 v[216:219], v[180:183], v[168:171], v[216:219]\n"
    "s_xor_b32 s42, s42, s40\n"
    "v_mfma_f32_16x16x32_bf16 v[216:219], v[148:151], v[136:139], v[216:219]\n"
    "s_and_b32 s42, s42, 0xff\n"
    "v_mfma_f32_16x16x32_bf16 v[212:215], v[148:151], v[132:135], v[212:215]\n"
    "s_xor_b32 s41, s41, s25\n"
    "v_mfma_f32_16x16x32_bf16 v[212:215], v[180:183], v[164:167], v[212:215]\n"
    "s_xor_b32 s41, s41, 0xa5c30f96\n"
    "v_mfma_f32_16x16x32_bf16 v[208:211], v[180:183], v[160:163], v[208:211]\n"
    "s_or_b32 s42, s41, s42\n"
    "v_mfma_f32_16x16x32_bf16 v[208:211], v[148:151], v[128:131], v[208:211]\n"
    "v_mfma_f32_16x16x32_bf16 v[224:227], v[152:155], v[128:131], v[224:227]\n"
    "s_lshr_b32 s41, s40, 8\n"
    "v_mfma_f32_16x16x32_bf16 v[224:227], v[184:187], v[160:163], v[224:227]\n"
    "s_mul_i32 s40, s41, 0x9e3779b1\n"
    "v_mfma_f32_16x16x32_bf16 v[228:231], v[184:187], v[164:167], v[228:231]\n"
    "s_xor_b32 s41, s41, 0xa5c30f96\n"
    "v_mfma_f32_16x16x32_bf16 v[228:231], v[152:155], v[132:135], v[228:231]\n"
    "s_lshr_b32 s40, s40, 24\n"
    "v_mfma_f32_16x16x32_bf16 v[232:235], v[152:155], v[136:139], v[232:235]\n"
    "v_xor_b32 v13, s40, v26\n"
    "v_mfma_f32_16x16x32_bf16 v[232:235], v[184:187], v[168:171], v[232:235]\n"
    "v_xor_b32 v22, s41, v27\n"
    "v_mfma_f32_16x16x32_bf16 v[236:239], v[184:187], v[172:175], v[236:239]\n"
    "v_lshl_or_b32 v13, v13, 24, v22\n"
    "v_mfma_f32_16x16x32_bf16 v[236:239], v[152:155], v[140:143], v[236:239]\n"
    "v_cmp_ne_u32 s[66:67], 0, v13\n"
    "v_mfma_f32_16x16x32_bf16 v[252:255], v[156:159], v[140:143], v[252:255]\n"
    "s_and_b32 s66, s66, 0xff\n"
    "v_mfma_f32_16x16x32_bf16 v[252:255], v[188:191], v[172:175], v[252:255]\n"
    "s_or_b32 s42, s42, s66\n"
    "v_mfma_f32_16x16x32_bf16 v[248:251], v[188:191], v[168:171], v[248:251]\n"
    "s_cmp_eq_u32 s20, 3\n"
    "v_mfma_f32_16x16x32_bf16 v[248:251], v[156:159], v[136:139], v[248:251]\n"
    "s_cselect_b32 s42, s42, 0\n"
    "v_mfma_f32_16x16x32_bf16 v[244:247], v[156:159], v[132:135], v[244:247]\n"
    "s_cmp_lg_u32 s42, 0\n"
    "v_mfma_f32_16x16x32_bf16 v[244:247], v[188:191], v[164:167], v[244:247]\n"
    "s_cbranch_scc1 .Lxd_pv3\n"
    ".Lxd_pv3r:\n"
    "v_mfma_f32_16x16x32_bf16 v[240:243], v[188:191], v[160:163], v[240:243]\n"
    "v_mfma_f32_16x16x32_bf16 v[240:243], v[156:159], v[128:131], v[240:243]\n"
    "s_waitcnt vmcnt(8)\n"
    "s_barrier\n"
    "s_add_u32 m0, s35, 0\n"
    "s_nop 0\n"
    "global_load_lds_dwordx4 v46, s[30:31] offset:0\n"
    "ds_read_b128 v[128:131], v56 offset:49152\n"
    "ds_read_b128 v[132:135], v56 offset:51200\n"
    "ds_read_b128 v[136:139], v56 offset:53248\n"
    "global_load_lds_dwordx4 v47, s[30:31] offset:1024\n"
    "s_add_u32 s30, s30, 212992\n"
    "s_addc_u32 s31, s31, 0\n"
    "ds_read_b128 v[140:143], v56 offset:55296\n"
    "ds_read_b64_tr_b16 v[144:145], v76 offset:49152\n"
    "ds_read_b64_tr_b16 v[146:147], v76 offset:50176\n"
    "s_add_u32 m0, s34, 0\n"
    "s_nop 0\n"
    "global_load_lds_dwordx4 v52, s[36:37] offset:0\n"
    "ds_read_b64_tr_b16 v[148:149], v77 offset:49152\n"
    "ds_read_b64_tr_b16 v[150:151], v77 offset:50176\n"
    "ds_read_b64_tr_b16 v[152:153], v78 offset:49152\n"
    "global_load_lds_dwordx4 v53, s[36:37] offset:1024\n"
    "ds_read_b64_tr_b16 v[154:155], v78 offset:50176\n"
    "ds_read_b64_tr_b16 v[156:157], v79 offset:49152\n"
    "ds_read_b64_tr_b16 v[158:159], v79 offset:50176\n"
    "global_load_lds_dwordx4 v54, s[36:37] offset:2048\n"
    "ds_read_b128 v[160:163], v57 offset:49152\n"
    "ds_read_b128 v[164:167], v57 offset:51200\n"
    "ds_read_b128 v[168:171], v57 offset:53248\n"
    "global_load_lds_dwordx4 v55, s[36:37] offset:3072\n"
    "ds_read_b128 v[172:175], v57 offset:55296\n"
    "ds_read_b64_tr_b16 v[176:177], v76 offset:57344\n"
    "ds_read_b64_tr_b16 v[178:179], v76 offset:58368\n"
    "global_load_dwordx4 v[36:39], v38, s[8:9]\n"
    "ds_read_b64_tr_b16 v[180:181], v77 offset:57344\n"
    "ds_read_b64_tr_b16 v[182:183], v77 offset:58368\n"
    "ds_read_b64_tr_b16 v[184:185], v78 offset:57344\n"
    "ds_read_b64_tr_b16 v[186:187], v78 offset:58368\n"
    "ds_read_b64_tr_b16 v[188:189], v79 offset:57344\n"
    "ds_read_b64_tr_b16 v[190:191], v79 offset:58368\n"
    "s_waitcnt vmcnt(12) lgkmcnt(0)\n"
    "s_barrier\n"
    "v_mfma_f32_16x16x32_bf16 v[192:195], v[144:147], v[128:131], v[192:195]\n"
    "v_lshlrev_b32 v119, 7, v119\n"
    "v_mfma_f32_16x16x32_bf16 v[192:195], v[176:179], v[160:163], v[192:195]\n"
    "v_add_u32_dpp v52, v118, v48 quad_perm:[0,0,0,0] row_mask:0xf bank_mask:0xf\n"
    "v_mfma_f32_16x16x32_bf16 v[196:199], v[176:179], v[164:167], v[196:199]\n"
    "v_add_u32_dpp v53, v118, v49 quad_perm:[1,1,1,1] row_mask:0xf bank_mask:0xf\n"
    "v_mfma_f32_16x16x32_bf16 v[196:199], v[144:147], v[132:135], v[196:199]\n"
    "v_mfma_f32_16x16x32_bf16 v[200:203], v[144:147], v[136:139], v[200:203]\n"
    "v_add_u32_dpp v54, v118, v50 quad_perm:[2,2,2,2] row_mask:0xf bank_mask:0xf\n"
    "v_mfma_f32_16x16x32_bf16 v[200:203], v[176:179], v[168:171], v[200:203]\n"
    "v_add_u32_dpp v55, v118, v51 quad_perm:[3,3,3,3] row_mask:0xf bank_mask:0xf\n"
    "v_mfma_f32_16x16x32_bf16 v[204:207], v[176:179], v[172:175], v[204:207]\n"
    "v_mfma_f32_16x16x32_bf16 v[204:207], v[144:147], v[140:143], v[204:207]\n"
    "v_lshlrev_b32 v60, 16, v92\n"
    "v_mfma_f32_16x16x32_bf16 v[220:223], v[148:151], v[140:143], v[220:223]\n"
    "v_and_b32 v61, 0xffff0000, v92\n"
    "v_mfma_f32_16x16x32_bf16 v[220:223], v[180:183], v[172:175], v[220:223]\n"
    "v_mfma_f32_16x16x32_bf16 v[216:219], v[180:183], v[168:171], v[216:219]\n"
    "v_lshlrev_b32 v62, 16, v93\n"
    "v_mfma_f32_16x16x32_bf16 v[216:219], v[148:151], v[136:139], v[216:219]\n"
    "v_and_b32 v63, 0xffff0000, v93\n"
    "v_mfma_f32_16x16x32_bf16 v[212:215], v[148:151], v[132:135], v[212:215]\n"
    "v_mfma_f32_16x16x32_bf16 v[212:215], v[180:183], v[164:167], v[212:215]\n"
    "v_lshlrev_b32 v64, 16, v94\n"
    "v_mfma_f32_16x16x32_bf16 v[208:211], v[180:183], v[160:163], v[208:211]\n"
    "v_and_b32 v65, 0xffff0000, v94\n"
    "v_mfma_f32_16x16x32_bf16 v[208:211], v[148:151], v[128:131], v[208:211]\n"
    "v_mfma_f32_16x16x32_bf16 v[224:227], v[152:155], v[128:131], v[224:227]\n"
    "v_lshlrev_b32 v66, 16, v95\n"
    "v_mfma_f32_16x16x32_bf16 v[224:227], v[184:187], v[160:163], v[224:227]\n"
    "v_and_b32 v67, 0xffff0000, v95\n"
    "v_mfma_f32_16x16x32_bf16 v[228:231], v[184:187], v[164:167], v[228:231]\n"
    "v_mfma_f32_16x16x32_bf16 v[228:231], v[152:155], v[132:135], v[228:231]\n"
    "v_lshlrev_b32 v68, 16, v122\n"
    "v_mfma_f32_16x16x32_bf16 v[232:235], v[152:155], v[136:139], v[232:235]\n"
    "v_and_b32 v69, 0xffff0000, v122\n"
    "v_mfma_f32_16x16x32_bf16 v[232:235], v[184:187], v[168:171], v[232:235]\n"
    "v_mfma_f32_16x16x32_bf16 v[236:239], v[184:187], v[172:175], v[236:239]\n"
    "v_lshlrev_b32 v70, 16, v123\n"
    "v_mfma_f32_16x16x32_bf16 v[236:239], v[152:155], v[140:143], v[236:239]\n"
    "v_and_b32 v71, 0xffff0000, v123\n"
    "v_mfma_f32_16x16x32_bf16 v[252:255], v[156:159], v[140:143], v[252:255]\n"
    "v_mfma_f32_16x16x32_bf16 v[252:255], v[188:191], v[172:175], v[252:255]\n"
    "v_lshlrev_b32 v72, 16, v124\n"
    "v_mfma_f32_16x16x32_bf16 v[248:251], v[188:191], v[168:171], v[248:251]\n"
    "v_and_b32 v73, 0xffff0000, v124\n"
    "v_mfma_f32_16x16x32_bf16 v[248:251], v[156:159], v[136:139], v[248:251]\n"
    "v_mfma_f32_16x16x32_bf16 v[244:247], v[156:159], v[132:135], v[244:247]\n"
    "v_lshlrev_b32 v74, 16, v125\n"
    "v_mfma_f32_16x16x32_bf16 v[244:247], v[188:191], v[164:167], v[244:247]\n"
    "v_and_b32 v75, 0xffff0000, v125\n"
    "v_mfma_f32_16x16x32_bf16 v[240:243], v[188:191], v[160:163], v[240:243]\n"
    "v_mfma_f32_16x16x32_bf16 v[240:243], v[156:159], v[128:131], v[240:243]\n"
    "s_waitcnt vmcnt(8)\n"
    "s_barrier\n"
    "s_add_u32 m0, s35, 49152\n"
    "s_nop 0\n"
    "global_load_lds_dwordx4 v46, s[30:31] offset:0\n"
    "ds_read_b128 v[128:131], v58 offset:0\n"
    "ds_read_b128 v[132:135], v58 offset:2048\n"
    "ds_read_b128 v[136:139], v58 offset:4096\n"
    "ds_read_b128 v[140:143], v58 offset:6144\n"
    "global_load_lds_dwordx4 v47, s[30:31] offset:1024\n"
    "s_add_u32 s30, s30, 212992\n"
    "s_addc_u32 s31, s31, 0\n"
    "ds_read_b64_tr_b16 v[144:145], v80 offset:0\n"
    "ds_read_b64_tr_b16 v[146:147], v80 offset:1024\n"
    "ds_read_b64_tr_b16 v[148:149], v81 offset:0\n"
    "ds_read_b64_tr_b16 v[150:151], v81 offset:1024\n"
    "s_add_u32 m0, s34, 49152\n"
    "s_nop 0\n"
    "global_load_lds_dwordx4 v52, s[36:37] offset:0\n"
    "ds_read_b64_tr_b16 v[152:153], v82 offset:0\n"
    "ds_read_b64_tr_b16 v[154:155], v82 offset:1024\n"
    "ds_read_b64_tr_b16 v[156:157], v83 offset:0\n"
    "ds_read_b64_tr_b16 v[158:159], v83 offset:1024\n"
    "global_load_lds_dwordx4 v53, s[36:37] offset:1024\n"
    "ds_read_b128 v[160:163], v59 offset:0\n"
    "ds_read_b128 v[164:167], v59 offset:2048\n"
    "ds_read_b128 v[168:171], v59 offset:4096\n"
    "ds_read_b128 v[172:175], v59 offset:6144\n"
    "global_load_lds_dwordx4 v54, s[36:37] offset:2048\n"
    "ds_read_b64_tr_b16 v[176:177], v80 offset:8192\n"
    "ds_read_b64_tr_b16 v[178:179], v80 offset:9216\n"
    "ds_read_b64_tr_b16 v[180:181], v81 offset:8192\n"
    "ds_read_b64_tr_b16 v[182:183], v81 offset:9216\n"
    "global_load_lds_dwordx4 v55, s[36:37] offset:3072\n"
    "ds_read_b64_tr_b16 v[184:185], v82 offset:8192\n"
    "ds_read_b64_tr_b16 v[186:187], v82 offset:9216\n"
    "ds_read_b64_tr_b16 v[188:189], v83 offset:8192\n"
    "ds_read_b64_tr_b16 v[190:191], v83 offset:9216\n"
    "s_waitcnt vmcnt(11) lgkmcnt(0)\n"
    "s_barrier\n"
    "v_mfma_f32_16x16x32_bf16 v[192:195], v[144:147], v[128:131], v[192:195]\n"
    "v_lshlrev_b32 v120, 7, v120\n"
    "v_mfma_f32_16x16x32_bf16 v[192:195], v[176:179], v[160:163], v[192:195]\n"
    "v_add_u32_dpp v52, v119, v48 quad_perm:[0,0,0,0] row_mask:0xf bank_mask:0xf\n"
    "v_mfma_f32_16x16x32_bf16 v[196:199], v[176:179], v[164:167], v[196:199]\n"
    "v_add_u32_dpp v53, v119, v49 quad_perm:[1,1,1,1] row_mask:0xf bank_mask:0xf\n"
    "v_mfma_f32_16x16x32_bf16 v[196:199], v[144:147], v[132:135], v[196:199]\n"
    "v_add_u32_dpp v54, v119, v50 quad_perm:[2,2,2,2] row_mask:0xf bank_mask:0xf\n"
    "v_mfma_f32_16x16x32_bf16 v[200:203], v[144:147], v[136:139], v[200:203]\n"
    "v_add_u32_dpp v55, v119, v51 quad_perm:[3,3,3,3] row_mask:0xf bank_mask:0xf\n"
    "v_mfma_f32_16x16x32_bf16 v[200:203], v[176:179], v[168:171], v[200:203]\n"
    "v_lshrrev_b32 v22, 8, v26\n"
    "v_mfma_f32_16x16x32_bf16 v[204:207], v[176:179], v[172:175], v[204:207]\n"
    "v_mfma_f32_16x16x32_bf16 v[204:207], v[144:147], v[140:143], v[204:207]\n"
    "v_mov_b32 v26, s40\n"
    "v_mfma_f32_16x16x32_bf16 v[220:223], v[148:151], v[140:143], v[220:223]\n"
    "v_mov_b32 v27, s41\n"
    "v_mfma_f32_16x16x32_bf16 v[220:223], v[180:183], v[172:175], v[220:223]\n"
    "v_add_u32_dpp v22, v22, v22 row_shl:4 row_mask:0xf bank_mask:0xf\n"
    "v_mfma_f32_16x16x32_bf16 v[216:219], v[180:183], v[168:171], v[216:219]\n"
    "v_add_u32_e64 v43, s25, 1\n"
    "v_mfma_f32_16x16x32_bf16 v[216:219], v[148:151], v[136:139], v[216:219]\n"
    "v_lshlrev_b32 v8, 2, v1\n"
    "v_mfma_f32_16x16x32_bf16 v[212:215], v[148:151], v[132:135], v[212:215]\n"
    "v_mfma_f32_16x16x32_bf16 v[212:215], v[180:183], v[164:167], v[212:215]\n"
    "v_max_u32_dpp v13, v22, v22 quad_perm:[1,0,3,2] row_mask:0xf bank_mask:0xf\n"
    "v_mfma_f32_16x16x32_bf16 v[208:211], v[180:183], v[160:163], v[208:211]\n"
    "v_add_u32 v8, 154112, v8\n"
    "v_mfma_f32_16x16x32_bf16 v[208:211], v[148:151], v[128:131], v[208:211]\n"
    "s_nop 0\n"
    "v_mfma_f32_16x16x32_bf16 v[224:227], v[152:155], v[128:131], v[224:227]\n"
    "v_max_u32_dpp v13, v13, v13 quad_perm:[2,3,0,1] row_mask:0xf bank_mask:0xf\n"
    "v_mfma_f32_16x16x32_bf16 v[224:227], v[184:187], v[160:163], v[224:227]\n"
    "v_sub_u32 v22, v22, v13\n"
    "v_mfma_f32_16x16x32_bf16 v[228:231], v[184:187], v[164:167], v[228:231]\n"
    "v_mfma_f32_16x16x32_bf16 v[228:231], v[152:155], v[132:135], v[228:231]\n"
    "v_subrev_u32 v13, 818, v13\n"
    "v_mfma_f32_16x16x32_bf16 v[232:235], v[152:155], v[136:139], v[232:235]\n"
    "v_ldexp_f32 v22, 1.0, v22\n"
    "v_mfma_f32_16x16x32_bf16 v[232:235], v[184:187], v[168:171], v[232:235]\n"
    "v_rcp_f32 v60, v60\n"
    "v_mfma_f32_16x16x32_bf16 v[236:239], v[184:187], v[172:175], v[236:239]\n"
    "v_rcp_f32 v61, v61\n"
    "v_mfma_f32_16x16x32_bf16 v[236:239], v[152:155], v[140:143], v[236:239]\n"
    "v_rcp_f32 v62, v62\n"
    "v_mfma_f32_16x16x32_bf16 v[252:255], v[156:159], v[140:143], v[252:255]\n"
    "v_mfma_f32_16x16x32_bf16 v[252:255], v[188:191], v[172:175], v[252:255]\n"
    "v_rcp_f32 v63, v63\n"
    "v_mfma_f32_16x16x32_bf16 v[248:251], v[188:191], v[168:171], v[248:251]\n"
    "v_rcp_f32 v64, v64\n"
    "v_mfma_f32_16x16x32_bf16 v[248:251], v[156:159], v[136:139], v[248:251]\n"
    "v_rcp_f32 v65, v65\n"
    "v_mfma_f32_16x16x32_bf16 v[244:247], v[156:159], v[132:135], v[244:247]\n"
    "v_rcp_f32 v66, v66\n"
    "v_mfma_f32_16x16x32_bf16 v[244:247], v[188:191], v[164:167], v[244:247]\n"
    "v_rcp_f32 v67, v67\n"
    "v_mfma_f32_16x16x32_bf16 v[240:243], v[188:191], v[160:163], v[240:243]\n"
    "v_mfma_f32_16x16x32_bf16 v[240:243], v[156:159], v[128:131], v[240:243]\n"
    "s_waitcnt vmcnt(7)\n"
    "s_barrier\n"
    "s_add_u32 m0, s35, 98304\n"
    "s_nop 0\n"
    "global_load_lds_dwordx4 v46, s[30:31] offset:0\n"
    "ds_read_b128 v[128:131], v56 offset:0\n"
    "ds_read_b128 v[132:135], v56 offset:2048\n"
    "ds_read_b128 v[136:139], v56 offset:4096\n"
    "ds_read_b128 v[140:143], v56 offset:6144\n"
    "global_load_lds_dwordx4 v47, s[30:31] offset:1024\n"
    "s_add_u32 s30, s6, 5111808\n"
    "s_addc_u32 s31, s7, 0\n"
    "ds_read_b64_tr_b16 v[144:145], v76 offset:0\n"
    "ds_read_b64_tr_b16 v[146:147], v76 offset:1024\n"
    "ds_read_b64_tr_b16 v[148:149], v77 offset:0\n"
    "ds_read_b64_tr_b16 v[150:151], v77 offset:1024\n"
    "s_add_u32 m0, s34, 98304\n"
    "s_nop 0\n"
    "global_load_lds_dwordx4 v52, s[36:37] offset:0\n"
    "ds_read_b64_tr_b16 v[152:153], v78 offset:0\n"
    "ds_read_b64_tr_b16 v[154:155], v78 offset:1024\n"
    "ds_read_b64_tr_b16 v[156:157], v79 offset:0\n"
    "ds_read_b64_tr_b16 v[158:159], v79 offset:1024\n"
    "global_load_lds_dwordx4 v53, s[36:37] offset:1024\n"
    "ds_read_b128 v[160:163], v57 offset:0\n"
    "ds_read_b128 v[164:167], v57 offset:2048\n"
    "ds_read_b128 v[168:171], v57 offset:4096\n"
    "ds_read_b128 v[172:175], v57 offset:6144\n"
    "global_load_lds_dwordx4 v54, s[36:37] offset:2048\n"
    "ds_read_b64_tr_b16 v[176:177], v76 offset:8192\n"
    "ds_read_b64_tr_b16 v[178:179], v76 offset:9216\n"
    "ds_read_b64_tr_b16 v[180:181], v77 offset:8192\n"
    "ds_read_b64_tr_b16 v[182:183], v77 offset:9216\n"
    "global_load_lds_dwordx4 v55, s[36:37] offset:3072\n"
    "ds_read_b64_tr_b16 v[184:185], v78 offset:8192\n"
    "ds_read_b64_tr_b16 v[186:187], v78 offset:9216\n"
    "ds_read_b64_tr_b16 v[188:189], v79 offset:8192\n"
    "ds_read_b64_tr_b16 v[190:191], v79 offset:9216\n"
    "s_waitcnt vmcnt(10) lgkmcnt(0)\n"
    "s_barrier\n"
    "v_mfma_f32_16x16x32_bf16 v[192:195], v[144:147], v[128:131], v[192:195]\n"
    "v_lshlrev_b32 v121, 7, v121\n"
    "v_mfma_f32_16x16x32_bf16 v[192:195], v[176:179], v[160:163], v[192:195]\n"
    "v_add_u32_dpp v52, v120, v48 quad_perm:[0,0,0,0] row_mask:0xf bank_mask:0xf\n"
    "v_mfma_f32_16x16x32_bf16 v[196:199], v[176:179], v[164:167], v[196:199]\n"
    "v_add_u32_dpp v53, v120, v49 quad_perm:[1,1,1,1] row_mask:0xf bank_mask:0xf\n"
    "v_mfma_f32_16x16x32_bf16 v[196:199], v[144:147], v[132:135], v[196:199]\n"
    "v_add_u32_dpp v54, v120, v50 quad_perm:[2,2,2,2] row_mask:0xf bank_mask:0xf\n"
    "v_mfma_f32_16x16x32_bf16 v[200:203], v[144:147], v[136:139], v[200:203]\n"
    "v_add_u32_dpp v55, v120, v51 quad_perm:[3,3,3,3] row_mask:0xf bank_mask:0xf\n"
    "v_mfma_f32_16x16x32_bf16 v[200:203], v[176:179], v[168:171], v[200:203]\n"
    "s_movk_i32 s43, 15744\n"
    "v_pk_maximum3_f16 v21, v28, v28, s43 op_sel_hi:[1,1,0] neg_lo:[0,1,0] neg_hi:[0,1,0]\n"
    "v_mfma_f32_16x16x32_bf16 v[204:207], v[176:179], v[172:175], v[204:207]\n"
    "v_pk_maximum3_f16 v21, v29, v29, v21 neg_lo:[0,1,0] neg_hi:[0,1,0]\n"
    "v_mfma_f32_16x16x32_bf16 v[204:207], v[144:147], v[140:143], v[204:207]\n"
    "v_mfma_f32_16x16x32_bf16 v[220:223], v[148:151], v[140:143], v[220:223]\n"
    "v_pk_maximum3_f16 v21, v30, v30, v21 neg_lo:[0,1,0] neg_hi:[0,1,0]\n"
    "v_mfma_f32_16x16x32_bf16 v[220:223], v[180:183], v[172:175], v[220:223]\n"
    "v_pk_maximum3_f16 v21, v31, v31, v21 neg_lo:[0,1,0] neg_hi:[0,1,0]\n"
    "v_mfma_f32_16x16x32_bf16 v[216:219], v[180:183], v[168:171], v[216:219]\n"
    "v_pk_maximum3_f16 v21, v32, v32, v21 neg_lo:[0,1,0] neg_hi:[0,1,0]\n"
    "v_mfma_f32_16x16x32_bf16 v[216:219], v[148:151], v[136:139], v[216:219]\n"
    "v_pk_maximum3_f16 v21, v33, v33, v21 neg_lo:[0,1,0] neg_hi:[0,1,0]\n"
    "v_mfma_f32_16x16x32_bf16 v[212:215], v[148:151], v[132:135], v[212:215]\n"
    "v_pk_maximum3_f16 v21, v34, v34, v21 neg_lo:[0,1,0] neg_hi:[0,1,0]\n"
    "v_mfma_f32_16x16x32_bf16 v[212:215], v[180:183], v[164:167], v[212:215]\n"
    "v_pk_maximum3_f16 v21, v35, v35, v21 neg_lo:[0,1,0] neg_hi:[0,1,0]\n"
    "v_mfma_f32_16x16x32_bf16 v[208:211], v[180:183], v[160:163], v[208:211]\n"
    "v_pk_maximum3_f16 v21, v36, v36, v21 neg_lo:[0,1,0] neg_hi:[0,1,0]\n"
    "v_mfma_f32_16x16x32_bf16 v[208:211], v[148:151], v[128:131], v[208:211]\n"
    "v_mfma_f32_16x16x32_bf16 v[224:227], v[152:155], v[128:131], v[224:227]\n"
    "v_pk_maximum3_f16 v21, v37, v37, v21 neg_lo:[0,1,0] neg_hi:[0,1,0]\n"
    "v_mfma_f32_16x16x32_bf16 v[224:227], v[184:187], v[160:163], v[224:227]\n"
    "v_pk_maximum3_f16 v21, v38, v38, v21 neg_lo:[0,1,0] neg_hi:[0,1,0]\n"
    "v_mfma_f32_16x16x32_bf16 v[228:231], v[184:187], v[164:167], v[228:231]\n"
    "v_pk_maximum3_f16 v21, v39, v39, v21 neg_lo:[0,1,0] neg_hi:[0,1,0]\n"
    "v_mfma_f32_16x16x32_bf16 v[228:231], v[152:155], v[132:135], v[228:231]\n"
    "v_pk_maximum3_f16 v21, v40, v40, v21 neg_lo:[0,1,0] neg_hi:[0,1,0]\n"
    "v_mfma_f32_16x16x32_bf16 v[232:235], v[152:155], v[136:139], v[232:235]\n"
    "v_pk_max_u16 v21, v21, v21 op_sel:[0,1] op_sel_hi:[1,0]\n"
    "v_mfma_f32_16x16x32_bf16 v[232:235], v[184:187], v[168:171], v[232:235]\n"
    "v_rcp_f32 v68, v68\n"
    "v_mfma_f32_16x16x32_bf16 v[236:239], v[184:187], v[172:175], v[236:239]\n"
    "v_rcp_f32 v69, v69\n"
    "v_mfma_f32_16x16x32_bf16 v[236:239], v[152:155], v[140:143], v[236:239]\n"
    "v_mfma_f32_16x16x32_bf16 v[252:255], v[156:159], v[140:143], v[252:255]\n"
    "v_rcp_f32 v70, v70\n"
    "v_mfma_f32_16x16x32_bf16 v[252:255], v[188:191], v[172:175], v[252:255]\n"
    "v_rcp_f32 v71, v71\n"
    "v_mfma_f32_16x16x32_bf16 v[248:251], v[188:191], v[168:171], v[248:251]\n"
    "v_rcp_f32 v72, v72\n"
    "v_mfma_f32_16x16x32_bf16 v[248:251], v[156:159], v[136:139], v[248:251]\n"
    "v_rcp_f32 v73, v73\n"
    "v_mfma_f32_16x16x32_bf16 v[244:247], v[156:159], v[132:135], v[244:247]\n"
    "v_rcp_f32 v74, v74\n"
    "v_mfma_f32_16x16x32_bf16 v[244:247], v[188:191], v[164:167], v[244:247]\n"
    "v_rcp_f32 v75, v75\n"
    "v_mfma_f32_16x16x32_bf16 v[240:243], v[188:191], v[160:163], v[240:243]\n"
    "v_mfma_f32_16x16x32_bf16 v[240:243], v[156:159], v[128:131], v[240:243]\n"
    "s_waitcnt vmcnt(6)\n"
    "s_barrier\n"
    "s_add_u32 m0, s35, 0\n"
    "s_nop 0\n"
    "global_load_lds_dwordx4 v46, s[30:31] offset:0\n"
    "ds_read_b128 v[128:131], v56 offset:49152\n"
    "ds_read_b128 v[132:135], v56 offset:51200\n"
    "ds_read_b128 v[136:139], v56 offset:53248\n"
    "ds_read_b128 v[140:143], v56 offset:55296\n"
    "global_load_lds_dwordx4 v47, s[30:31] offset:1024\n"
    "s_add_u32 s30, s30, 212992\n"
    "s_addc_u32 s31, s31, 0\n"
    "ds_read_b64_tr_b16 v[144:145], v76 offset:49152\n"
    "ds_read_b64_tr_b16 v[146:147], v76 offset:50176\n"
    "ds_read_b64_tr_b16 v[148:149], v77 offset:49152\n"
    "ds_read_b64_tr_b16 v[150:151], v77 offset:50176\n"
    "s_add_u32 m0, s34, 0\n"
    "s_nop 0\n"
    "global_load_lds_dwordx4 v52, s[36:37] offset:0\n"
    "ds_read_b64_tr_b16 v[152:153], v78 offset:49152\n"
    "ds_read_b64_tr_b16 v[154:155], v78 offset:50176\n"
    "ds_read_b64_tr_b16 v[156:157], v79 offset:49152\n"
    "ds_read_b64_tr_b16 v[158:159], v79 offset:50176\n"
    "global_load_lds_dwordx4 v53, s[36:37] offset:1024\n"
    "ds_read_b128 v[160:163], v57 offset:49152\n"
    "ds_read_b128 v[164:167], v57 offset:51200\n"
    "ds_read_b128 v[168:171], v57 offset:53248\n"
    "ds_read_b128 v[172:175], v57 offset:55296\n"
    "global_load_lds_dwordx4 v54, s[36:37] offset:2048\n"
    "ds_read_b64_tr_b16 v[176:177], v76 offset:57344\n"
    "ds_read_b64_tr_b16 v[178:179], v76 offset:58368\n"
    "ds_read_b64_tr_b16 v[180:181], v77 offset:57344\n"
    "ds_read_b64_tr_b16 v[182:183], v77 offset:58368\n"
    "global_load_lds_dwordx4 v55, s[36:37] offset:3072\n"
    "ds_read_b64_tr_b16 v[184:185], v78 offset:57344\n"
    "ds_read_b64_tr_b16 v[186:187], v78 offset:58368\n"
    "ds_read_b64_tr_b16 v[188:189], v79 offset:57344\n"
    "ds_read_b64_tr_b16 v[190:191], v79 offset:58368\n"
    "s_waitcnt vmcnt(10) lgkmcnt(0)\n"
    "s_barrier\n"
    "v_mfma_f32_16x16x32_bf16 v[192:195], v[144:147], v[128:131], v[192:195]\n"
    "v_add_u32_dpp v52, v121, v48 quad_perm:[0,0,0,0] row_mask:0xf bank_mask:0xf\n"
    "s_mov_b32 s42, 0x71800000\n"
    "v_mfma_f32_16x16x32_bf16 v[192:195], v[176:179], v[160:163], v[192:195]\n"
    "v_add_u32_dpp v53, v121, v49 quad_perm:[1,1,1,1] row_mask:0xf bank_mask:0xf\n"
    "v_mfma_f32_16x16x32_bf16 v[196:199], v[176:179], v[164:167], v[196:199]\n"
    "v_add_u32_dpp v54, v121, v50 quad_perm:[2,2,2,2] row_mask:0xf bank_mask:0xf\n"
    "v_mfma_f32_16x16x32_bf16 v[196:199], v[144:147], v[132:135], v[196:199]\n"
    "v_add_u32_dpp v55, v121, v51 quad_perm:[3,3,3,3] row_mask:0xf bank_mask:0xf\n"
    "v_mfma_f32_16x16x32_bf16 v[200:203], v[144:147], v[136:139], v[200:203]\n"
    "v_bfe_u32 v41, v1, 4, 1\n"
    "v_mfma_f32_16x16x32_bf16 v[200:203], v[176:179], v[168:171], v[200:203]\n"
    "v_lshlrev_b32 v41, 2, v41\n"
    "v_mfma_f32_16x16x32_bf16 v[204:207], v[176:179], v[172:175], v[204:207]\n"
    "v_add_u32 v41, 154112, v41\n"
    "v_mfma_f32_16x16x32_bf16 v[204:207], v[144:147], v[140:143], v[204:207]\n"
    "v_med3_f32 v60, v60, s42, -s42\n"
    "v_mfma_f32_16x16x32_bf16 v[220:223], v[148:151], v[140:143], v[220:223]\n"
    "v_max_u32_dpp v21, v21, v21 quad_perm:[1,0,3,2] row_mask:0xf bank_mask:0xf\n"
    "v_mfma_f32_16x16x32_bf16 v[220:223], v[180:183], v[172:175], v[220:223]\n"
    "v_med3_f32 v61, v61, s42, -s42\n"
    "v_mfma_f32_16x16x32_bf16 v[216:219], v[180:183], v[168:171], v[216:219]\n"
    "v_max_u32_dpp v21, v21, v21 quad_perm:[2,3,0,1] row_mask:0xf bank_mask:0xf\n"
    "v_mfma_f32_16x16x32_bf16 v[216:219], v[148:151], v[136:139], v[216:219]\n"
    "v_med3_f32 v62, v62, s42, -s42\n"
    "v_mfma_f32_16x16x32_bf16 v[212:215], v[148:151], v[132:135], v[212:215]\n"
    "v_max_u32_dpp v21, v21, v21 row_half_mirror row_mask:0xf bank_mask:0xf\n"
    "v_mfma_f32_16x16x32_bf16 v[212:215], v[180:183], v[164:167], v[212:215]\n"
    "v_med3_f32 v63, v63, s42, -s42\n"
    "v_mfma_f32_16x16x32_bf16 v[208:211], v[180:183], v[160:163], v[208:211]\n"
    "v_max_u32_dpp v21, v21, v21 row_mirror row_mask:0xf bank_mask:0xf\n"
    "v_mfma_f32_16x16x32_bf16 v[208:211], v[148:151], v[128:131], v[208:211]\n"
    "v_med3_f32 v64, v64, s42, -s42\n"
    "v_mfma_f32_16x16x32_bf16 v[224:227], v[152:155], v[128:131], v[224:227]\n"
    "v_max_u32_dpp v21, v21, v21 row_bcast:15 row_mask:0xa bank_mask:0xf\n"
    "v_mfma_f32_16x16x32_bf16 v[224:227], v[184:187], v[160:163], v[224:227]\n"
    "v_med3_f32 v65, v65, s42, -s42\n"
    "v_mfma_f32_16x16x32_bf16 v[228:231], v[184:187], v[164:167], v[228:231]\n"
    "v_max_u32_dpp v21, v21, v21 row_bcast:31 row_mask:0xc bank_mask:0xf\n"
    "v_mfma_f32_16x16x32_bf16 v[228:231], v[152:155], v[132:135], v[228:231]\n"
    "v_med3_f32 v66, v66, s42, -s42\n"
    "v_mfma_f32_16x16x32_bf16 v[232:235], v[152:155], v[136:139], v[232:235]\n"
    "v_readlane_b32 s47, v21, 63\n"
    "v_mfma_f32_16x16x32_bf16 v[232:235], v[184:187], v[168:171], v[232:235]\n"
    "v_med3_f32 v67, v67, s42, -s42\n"
    "v_mfma_f32_16x16x32_bf16 v[236:239], v[184:187], v[172:175], v[236:239]\n"
    "s_and_b32 s47, s47, 0x7f80\n"
    "v_mfma_f32_16x16x32_bf16 v[236:239], v[152:155], v[140:143], v[236:239]\n"
    "s_sub_u32 s50, s47, 2816\n"
    "v_mfma_f32_16x16x32_bf16 v[252:255], v[156:159], v[140:143], v[252:255]\n"
    "s_mul_i32 s51, s50, -8\n"
    "v_med3_f32 v68, v68, s42, -s42\n"
    "v_mfma_f32_16x16x32_bf16 v[252:255], v[188:191], v[172:175], v[252:255]\n"
    "s_lshr_b32 s47, s47, 3\n"
    "v_med3_f32 v69, v69, s42, -s42\n"
    "v_mfma_f32_16x16x32_bf16 v[248:251], v[188:191], v[168:171], v[248:251]\n"
    "v_med3_f32 v70, v70, s42, -s42\n"
    "v_mfma_f32_16x16x32_bf16 v[248:251], v[156:159], v[136:139], v[248:251]\n"
    "v_med3_f32 v71, v71, s42, -s42\n"
    "v_mfma_f32_16x16x32_bf16 v[244:247], v[156:159], v[132:135], v[244:247]\n"
    "v_med3_f32 v72, v72, s42, -s42\n"
    "v_mfma_f32_16x16x32_bf16 v[244:247], v[188:191], v[164:167], v[244:247]\n"
    "v_med3_f32 v73, v73, s42, -s42\n"
    "v_mfma_f32_16x16x32_bf16 v[240:243], v[188:191], v[160:163], v[240:243]\n"
    "v_med3_f32 v74, v74, s42, -s42\n"
    "v_mfma_f32_16x16x32_bf16 v[240:243], v[156:159], v[128:131], v[240:243]\n"
    "v_med3_f32 v75, v75, s42, -s42\n"
    "s_waitcnt vmcnt(6)\n"
    "s_barrier\n"
    "s_add_u32 m0, s35, 49152\n"
    "s_nop 0\n"
    "global_load_lds_dwordx4 v46, s[30:31] offset:0\n"
    "ds_read_b128 v[128:131], v58 offset:0\n"
    "ds_read_b128 v[132:135], v58 offset:2048\n"
    "ds_read_b128 v[136:139], v58 offset:4096\n"
    "ds_read_b128 v[140:143], v58 offset:6144\n"
    "global_load_lds_dwordx4 v47, s[30:31] offset:1024\n"
    "ds_read_b64_tr_b16 v[144:145], v80 offset:0\n"
    "ds_read_b64_tr_b16 v[146:147], v80 offset:1024\n"
    "ds_read_b64_tr_b16 v[148:149], v81 offset:0\n"
    "ds_read_b64_tr_b16 v[150:151], v81 offset:1024\n"
    "s_add_u32 m0, s34, 49152\n"
    "s_nop 0\n"
    "global_load_lds_dwordx4 v52, s[36:37] offset:0\n"
    "ds_read_b64_tr_b16 v[152:153], v82 offset:0\n"
    "ds_read_b64_tr_b16 v[154:155], v82 offset:1024\n"
    "ds_read_b64_tr_b16 v[156:157], v83 offset:0\n"
    "ds_read_b64_tr_b16 v[158:159], v83 offset:1024\n"
    "global_load_lds_dwordx4 v53, s[36:37] offset:1024\n"
    "ds_read_b128 v[160:163], v59 offset:0\n"
    "ds_read_b128 v[164:167], v59 offset:2048\n"
    "ds_read_b128 v[168:171], v59 offset:4096\n"
    "ds_read_b128 v[172:175], v59 offset:6144\n"
    "global_load_lds_dwordx4 v54, s[36:37] offset:2048\n"
    "ds_read_b64_tr_b16 v[176:177], v80 offset:8192\n"
    "ds_read_b64_tr_b16 v[178:179], v80 offset:9216\n"
    "ds_read_b64_tr_b16 v[180:181], v81 offset:8192\n"
    "ds_read_b64_tr_b16 v[182:183], v81 offset:9216\n"
    "global_load_lds_dwordx4 v55, s[36:37] offset:3072\n"
    "ds_read_b64_tr_b16 v[184:185], v82 offset:8192\n"
    "ds_read_b64_tr_b16 v[186:187], v82 offset:9216\n"
    "ds_read_b64_tr_b16 v[188:189], v83 offset:8192\n"
    "ds_read_b64_tr_b16 v[190:191], v83 offset:9216\n"
    "s_waitcnt vmcnt(10) lgkmcnt(0)\n"
    "s_barrier\n"
    "v_mfma_f32_16x16x32_bf16 v[192:195], v[144:147], v[128:131], v[192:195]\n"
    "v_pk_maximum3_f16 v21, v28, v28, s50 op_sel_hi:[1,1,0] neg_lo:[0,1,0] neg_hi:[0,1,0]\n"
    "v_mfma_f32_16x16x32_bf16 v[192:195], v[176:179], v[160:163], v[192:195]\n"
    "v_pk_mad_u16 v21, v21, 8, s51 op_sel_hi:[1,0,0]\n"
    "v_mfma_f32_16x16x32_bf16 v[196:199], v[176:179], v[164:167], v[196:199]\n"
    "v_bfi_b32 v28, s57, v21, v28\n"
    "v_mfma_f32_16x16x32_bf16 v[196:199], v[144:147], v[132:135], v[196:199]\n"
    "v_mfma_f32_16x16x32_bf16 v[200:203], v[144:147], v[136:139], v[200:203]\n"
    "v_pk_maximum3_f16 v12, v29, v29, s50 op_sel_hi:[1,1,0] neg_lo:[0,1,0] neg_hi:[0,1,0]\n"
    "v_mfma_f32_16x16x32_bf16 v[200:203], v[176:179], v[168:171], v[200:203]\n"
    "v_pk_mad_u16 v12, v12, 8, s51 op_sel_hi:[1,0,0]\n"
    "v_mfma_f32_16x16x32_bf16 v[204:207], v[176:179], v[172:175], v[204:207]\n"
    "v_bfi_b32 v29, s57, v12, v29\n"
    "v_mfma_f32_16x16x32_bf16 v[204:207], v[144:147], v[140:143], v[204:207]\n"
    "v_mfma_f32_16x16x32_bf16 v[220:223], v[148:151], v[140:143], v[220:223]\n"
    "v_pk_maximum3_f16 v21, v30, v30, s50 op_sel_hi:[1,1,0] neg_lo:[0,1,0] neg_hi:[0,1,0]\n"
    "v_mfma_f32_16x16x32_bf16 v[220:223], v[180:183], v[172:175], v[220:223]\n"
    "v_pk_mad_u16 v21, v21, 8, s51 op_sel_hi:[1,0,0]\n"
    "v_mfma_f32_16x16x32_bf16 v[216:219], v[180:183], v[168:171], v[216:219]\n"
    "v_bfi_b32 v30, s57, v21, v30\n"
    "v_mfma_f32_16x16x32_bf16 v[216:219], v[148:151], v[136:139], v[216:219]\n"
    "v_mfma_f32_16x16x32_bf16 v[212:215], v[148:151], v[132:135], v[212:215]\n"
    "v_pk_maximum3_f16 v12, v31, v31, s50 op_sel_hi:[1,1,0] neg_lo:[0,1,0] neg_hi:[0,1,0]\n"
    "v_mfma_f32_16x16x32_bf16 v[212:215], v[180:183], v[164:167], v[212:215]\n"
    "v_pk_mad_u16 v12, v12, 8, s51 op_sel_hi:[1,0,0]\n"
    "v_mfma_f32_16x16x32_bf16 v[208:211], v[180:183], v[160:163], v[208:211]\n"
    "v_bfi_b32 v31, s57, v12, v31\n"
    "v_mfma_f32_16x16x32_bf16 v[208:211], v[148:151], v[128:131], v[208:211]\n"
    "v_mfma_f32_16x16x32_bf16 v[224:227], v[152:155], v[128:131], v[224:227]\n"
    "v_pk_maximum3_f16 v21, v32, v32, s50 op_sel_hi:[1,1,0] neg_lo:[0,1,0] neg_hi:[0,1,0]\n"
    "v_mfma_f32_16x16x32_bf16 v[224:227], v[184:187], v[160:163], v[224:227]\n"
    "v_pk_mad_u16 v21, v21, 8, s51 op_sel_hi:[1,0,0]\n"
    "v_mfma_f32_16x16x32_bf16 v[228:231], v[184:187], v[164:167], v[228:231]\n"
    "v_bfi_b32 v32, s57, v21, v32\n"
    "v_mfma_f32_16x16x32_bf16 v[228:231], v[152:155], v[132:135], v[228:231]\n"
    "v_mfma_f32_16x16x32_bf16 v[232:235], v[152:155], v[136:139], v[232:235]\n"
    "v_pk_maximum3_f16 v12, v33, v33, s50 op_sel_hi:[1,1,0] neg_lo:[0,1,0] neg_hi:[0,1,0]\n"
    "v_mfma_f32_16x16x32_bf16 v[232:235], v[184:187], v[168:171], v[232:235]\n"
    "v_pk_mad_u16 v12, v12, 8, s51 op_sel_hi:[1,0,0]\n"
    "v_mfma_f32_16x16x32_bf16 v[236:239], v[184:187], v[172:175], v[236:239]\n"
    "v_bfi_b32 v33, s57, v12, v33\n"
    "v_mfma_f32_16x16x32_bf16 v[236:239], v[152:155], v[140:143], v[236:239]\n"
    "v_mfma_f32_16x16x32_bf16 v[252:255], v[156:159], v[140:143], v[252:255]\n"
    "v_pk_maximum3_f16 v21, v34, v34, s50 op_sel_hi:[1,1,0] neg_lo:[0,1,0] neg_hi:[0,1,0]\n"
    "v_mfma_f32_16x16x32_bf16 v[252:255], v[188:191], v[172:175], v[252:255]\n"
    "v_pk_mad_u16 v21, v21, 8, s51 op_sel_hi:[1,0,0]\n"
    "v_mfma_f32_16x16x32_bf16 v[248:251], v[188:191], v[168:171], v[248:251]\n"
    "v_bfi_b32 v34, s57, v21, v34\n"
    "v_mfma_f32_16x16x32_bf16 v[248:251], v[156:159], v[136:139], v[248:251]\n"
    "v_mfma_f32_16x16x32_bf16 v[244:247], v[156:159], v[132:135], v[244:247]\n"
    "v_lshlrev_b32 v52, 4, v1\n"
    "v_mfma_f32_16x16x32_bf16 v[244:247], v[188:191], v[164:167], v[244:247]\n"
    "v_add_u32 v53, 4096, v52\n"
    "v_mfma_f32_16x16x32_bf16 v[240:243], v[188:191], v[160:163], v[240:243]\n"
    "v_mfma_f32_16x16x32_bf16 v[240:243], v[156:159], v[128:131], v[240:243]\n"
    "s_waitcnt vmcnt(6)\n"
    "s_barrier\n"
    "s_mul_i32 s42, s20, 5376\n"
    "s_add_u32 s40, s42, 1048576\n"
    "s_add_u32 s38, s16, s40\n"
    "s_addc_u32 s39, s17, 0\n"
    "s_add_u32 m0, s42, 98304\n"
    "s_nop 0\n"
    "global_load_lds_dwordx4 v52, s[38:39] offset:0 sc1\n"
    "ds_read_b128 v[128:131], v56 offset:0\n"
    "ds_read_b128 v[132:135], v56 offset:2048\n"
    "ds_read_b128 v[136:139], v56 offset:4096\n"
    "ds_read_b128 v[140:143], v56 offset:6144\n"
    "global_load_lds_dwordx4 v52, s[38:39] offset:1024 sc1\n"
    "ds_read_b64_tr_b16 v[144:145], v76 offset:0\n"
    "ds_read_b64_tr_b16 v[146:147], v76 offset:1024\n"
    "ds_read_b64_tr_b16 v[148:149], v77 offset:0\n"
    "ds_read_b64_tr_b16 v[150:151], v77 offset:1024\n"
    "global_load_lds_dwordx4 v52, s[38:39] offset:2048 sc1\n"
    "ds_read_b64_tr_b16 v[152:153], v78 offset:0\n"
    "ds_read_b64_tr_b16 v[154:155], v78 offset:1024\n"
    "ds_read_b64_tr_b16 v[156:157], v79 offset:0\n"
    "ds_read_b64_tr_b16 v[158:159], v79 offset:1024\n"
    "global_load_lds_dwordx4 v52, s[38:39] offset:3072 sc1\n"
    "ds_read_b128 v[160:163], v57 offset:0\n"
    "ds_read_b128 v[164:167], v57 offset:2048\n"
    "ds_read_b128 v[168:171], v57 offset:4096\n"
    "ds_read_b128 v[172:175], v57 offset:6144\n"
    "s_add_u32 m0, s42, 102400\n"
    "s_nop 0\n"
    "global_load_lds_dwordx4 v53, s[38:39] offset:0 sc1\n"
    "ds_read_b64_tr_b16 v[176:177], v76 offset:8192\n"
    "ds_read_b64_tr_b16 v[178:179], v76 offset:9216\n"
    "ds_read_b64_tr_b16 v[180:181], v77 offset:8192\n"
    "ds_read_b64_tr_b16 v[182:183], v77 offset:9216\n"
    "global_load_lds_dwordx4 v53, s[38:39] offset:256 sc1\n"
    "ds_read_b64_tr_b16 v[184:185], v78 offset:8192\n"
    "ds_read_b64_tr_b16 v[186:187], v78 offset:9216\n"
    "ds_read_b64_tr_b16 v[188:189], v79 offset:8192\n"
    "ds_read_b64_tr_b16 v[190:191], v79 offset:9216\n"
    "s_waitcnt vmcnt(10) lgkmcnt(0)\n"
    "s_barrier\n"
    "v_mfma_f32_16x16x32_bf16 v[192:195], v[144:147], v[128:131], v[192:195]\n"
    "v_add_u32 v42, 98304, v52\n"
    "v_mfma_f32_16x16x32_bf16 v[192:195], v[176:179], v[160:163], v[192:195]\n"
    "s_mov_b32 s36, 0\n"
    "v_mfma_f32_16x16x32_bf16 v[196:199], v[176:179], v[164:167], v[196:199]\n"
    "v_mov_b32 v84, s65\n"
    "v_mfma_f32_16x16x32_bf16 v[196:199], v[144:147], v[132:135], v[196:199]\n"
    "v_mfma_f32_16x16x32_bf16 v[200:203], v[144:147], v[136:139], v[200:203]\n"
    "v_pk_maximum3_f16 v12, v35, v35, s50 op_sel_hi:[1,1,0] neg_lo:[0,1,0] neg_hi:[0,1,0]\n"
    "v_mfma_f32_16x16x32_bf16 v[200:203], v[176:179], v[168:171], v[200:203]\n"
    "v_pk_mad_u16 v12, v12, 8, s51 op_sel_hi:[1,0,0]\n"
    "v_mfma_f32_16x16x32_bf16 v[204:207], v[176:179], v[172:175], v[204:207]\n"
    "v_bfi_b32 v35, s57, v12, v35\n"
    "v_mfma_f32_16x16x32_bf16 v[204:207], v[144:147], v[140:143], v[204:207]\n"
    "v_mfma_f32_16x16x32_bf16 v[220:223], v[148:151], v[140:143], v[220:223]\n"
    "v_pk_maximum3_f16 v21, v36, v36, s50 op_sel_hi:[1,1,0] neg_lo:[0,1,0] neg_hi:[0,1,0]\n"
    "v_mfma_f32_16x16x32_bf16 v[220:223], v[180:183], v[172:175], v[220:223]\n"
    "v_pk_mad_u16 v21, v21, 8, s51 op_sel_hi:[1,0,0]\n"
    "v_mfma_f32_16x16x32_bf16 v[216:219], v[180:183], v[168:171], v[216:219]\n"
    "v_bfi_b32 v36, s57, v21, v36\n"
    "v_mfma_f32_16x16x32_bf16 v[216:219], v[148:151], v[136:139], v[216:219]\n"
    "v_mfma_f32_16x16x32_bf16 v[212:215], v[148:151], v[132:135], v[212:215]\n"
    "v_pk_maximum3_f16 v12, v37, v37, s50 op_sel_hi:[1,1,0] neg_lo:[0,1,0] neg_hi:[0,1,0]\n"
    "v_mfma_f32_16x16x32_bf16 v[212:215], v[180:183], v[164:167], v[212:215]\n"
    "v_pk_mad_u16 v12, v12, 8, s51 op_sel_hi:[1,0,0]\n"
    "v_mfma_f32_16x16x32_bf16 v[208:211], v[180:183], v[160:163], v[208:211]\n"
    "v_bfi_b32 v37, s57, v12, v37\n"
    "v_mfma_f32_16x16x32_bf16 v[208:211], v[148:151], v[128:131], v[208:211]\n"
    "v_mfma_f32_16x16x32_bf16 v[224:227], v[152:155], v[128:131], v[224:227]\n"
    "v_pk_maximum3_f16 v21, v38, v38, s50 op_sel_hi:[1,1,0] neg_lo:[0,1,0] neg_hi:[0,1,0]\n"
    "v_mfma_f32_16x16x32_bf16 v[224:227], v[184:187], v[160:163], v[224:227]\n"
    "v_pk_mad_u16 v21, v21, 8, s51 op_sel_hi:[1,0,0]\n"
    "v_mfma_f32_16x16x32_bf16 v[228:231], v[184:187], v[164:167], v[228:231]\n"
    "v_bfi_b32 v38, s57, v21, v38\n"
    "v_mfma_f32_16x16x32_bf16 v[228:231], v[152:155], v[132:135], v[228:231]\n"
    "v_mfma_f32_16x16x32_bf16 v[232:235], v[152:155], v[136:139], v[232:235]\n"
    "v_pk_maximum3_f16 v12, v39, v39, s50 op_sel_hi:[1,1,0] neg_lo:[0,1,0] neg_hi:[0,1,0]\n"
    "v_mfma_f32_16x16x32_bf16 v[232:235], v[184:187], v[168:171], v[232:235]\n"
    "v_pk_mad_u16 v12, v12, 8, s51 op_sel_hi:[1,0,0]\n"
    "v_mfma_f32_16x16x32_bf16 v[236:239], v[184:187], v[172:175], v[236:239]\n"
    "v_bfi_b32 v39, s57, v12, v39\n"
    "v_mfma_f32_16x16x32_bf16 v[236:239], v[152:155], v[140:143], v[236:239]\n"
    "v_mfma_f32_16x16x32_bf16 v[252:255], v[156:159], v[140:143], v[252:255]\n"
    "v_pk_maximum3_f16 v21, v40, v40, s50 op_sel_hi:[1,1,0] neg_lo:[0,1,0] neg_hi:[0,1,0]\n"
    "v_mfma_f32_16x16x32_bf16 v[252:255], v[188:191], v[172:175], v[252:255]\n"
    "v_pk_mad_u16 v21, v21, 8, s51 op_sel_hi:[1,0,0]\n"
    "v_mfma_f32_16x16x32_bf16 v[248:251], v[188:191], v[168:171], v[248:251]\n"
    "v_bfi_b32 v40, s57, v21, v40\n"
    "v_mfma_f32_16x16x32_bf16 v[248:251], v[156:159], v[136:139], v[248:251]\n"
    "v_mfma_f32_16x16x32_bf16 v[244:247], v[156:159], v[132:135], v[244:247]\n"
    "s_sub_u32 s66, 4112, s47\n"
    "s_lshl_b32 s66, s66, 19\n"
    "v_cvt_pkrtz_f16_f32 v12, s66, s66\n"
    "v_mfma_f32_16x16x32_bf16 v[244:247], v[188:191], v[164:167], v[244:247]\n"
    "v_lshl_or_b32 v21, v12, 16, s47\n"
    "v_mfma_f32_16x16x32_bf16 v[240:243], v[188:191], v[160:163], v[240:243]\n"
    "v_mfma_f32_16x16x32_bf16 v[240:243], v[156:159], v[128:131], v[240:243]\n"
    "s_waitcnt vmcnt(6)\n"
    "s_barrier\n"
    "ds_read_b128 v[128:131], v56 offset:49152\n"
    "ds_read_b128 v[132:135], v56 offset:51200\n"
    "ds_read_b128 v[136:139], v56 offset:53248\n"
    "ds_read_b128 v[140:143], v56 offset:55296\n"
    "ds_read_b64_tr_b16 v[144:145], v76 offset:49152\n"
    "ds_read_b64_tr_b16 v[146:147], v76 offset:50176\n"
    "ds_read_b64_tr_b16 v[148:149], v77 offset:49152\n"
    "ds_read_b64_tr_b16 v[150:151], v77 offset:50176\n"
    "ds_read_b64_tr_b16 v[152:153], v78 offset:49152\n"
    "ds_read_b64_tr_b16 v[154:155], v78 offset:50176\n"
    "ds_read_b64_tr_b16 v[156:157], v79 offset:49152\n"
    "ds_read_b64_tr_b16 v[158:159], v79 offset:50176\n"
    "ds_read_b128 v[160:163], v57 offset:49152\n"
    "ds_read_b128 v[164:167], v57 offset:51200\n"
    "ds_read_b128 v[168:171], v57 offset:53248\n"
    "ds_read_b128 v[172:175], v57 offset:55296\n"
    "ds_read_b64_tr_b16 v[176:177], v76 offset:57344\n"
    "ds_read_b64_tr_b16 v[178:179], v76 offset:58368\n"
    "ds_read_b64_tr_b16 v[180:181], v77 offset:57344\n"
    "ds_read_b64_tr_b16 v[182:183], v77 offset:58368\n"
    "ds_read_b64_tr_b16 v[184:185], v78 offset:57344\n"
    "ds_read_b64_tr_b16 v[186:187], v78 offset:58368\n"
    "ds_read_b64_tr_b16 v[188:189], v79 offset:57344\n"
    "ds_read_b64_tr_b16 v[190:191], v79 offset:58368\n"
    "s_cmp_lg_u32 s20, 3\n"
    "s_cbranch_scc1 .Lxd_on4\n"
    "s_mov_b64 exec, 1\n"
    "ds_write_b64 v8, v[26:27]\n"
    "s_mov_b64 exec, 15\n"
    "ds_write2_b32 v8, v22, v13 offset0:4 offset1:8\n"
    "s_mov_b64 exec, -1\n"
    ".Lxd_on4:\n"
    "s_waitcnt vmcnt(0) lgkmcnt(0)\n"
    "s_cmp_lg_u32 s20, 3\n"
    "s_cbranch_scc1 .Lxd_on5\n"
    "s_mov_b64 exec, 0x200\n"
    "global_store_dword v11, v43, s[16:17] sc1\n"
    "s_mov_b64 exec, -1\n"
    ".Lxd_on5:\n"
    "s_barrier\n"
    "s_setprio 3\n"
    "v_mfma_f32_16x16x32_bf16 v[192:195], v[144:147], v[128:131], v[192:195]\n"
    "v_lshl_add_u32 v11, v1, 2, s3\n"
    "v_mfma_f32_16x16x32_bf16 v[192:195], v[176:179], v[160:163], v[192:195]\n"
    "v_mov_b32 v13, s58\n"
    "v_mfma_f32_16x16x32_bf16 v[196:199], v[176:179], v[164:167], v[196:199]\n"
    "v_mfma_f32_16x16x32_bf16 v[196:199], v[144:147], v[132:135], v[196:199]\n"
    "v_lshl_add_u32 v7, s23, 12, v3\n"
    "v_mfma_f32_16x16x32_bf16 v[200:203], v[144:147], v[136:139], v[200:203]\n"
    "v_mfma_f32_16x16x32_bf16 v[200:203], v[176:179], v[168:171], v[200:203]\n"
    "v_lshl_add_u32 v8, s23, 12, v4\n"
    "v_mfma_f32_16x16x32_bf16 v[204:207], v[176:179], v[172:175], v[204:207]\n"
    "v_lshl_add_u32 v9, s23, 12, v5\n"
    "v_mfma_f32_16x16x32_bf16 v[204:207], v[144:147], v[140:143], v[204:207]\n"
    "v_mfma_f32_16x16x32_bf16 v[220:223], v[148:151], v[140:143], v[220:223]\n"
    "v_lshl_add_u32 v10, s23, 12, v6\n"
    "v_mfma_f32_16x16x32_bf16 v[220:223], v[180:183], v[172:175], v[220:223]\n"
    "v_mfma_f32_16x16x32_bf16 v[216:219], v[180:183], v[168:171], v[216:219]\n"
    "v_mul_f32 v76, v192, v192\n"
    "v_mul_f32 v77, v193, v193\n"
    "v_mfma_f32_16x16x32_bf16 v[216:219], v[148:151], v[136:139], v[216:219]\n"
    "v_mul_f32 v78, v196, v196\n"
    "v_mul_f32 v79, v197, v197\n"
    "v_mfma_f32_16x16x32_bf16 v[212:215], v[148:151], v[132:135], v[212:215]\n"
    "v_mul_f32 v80, v200, v200\n"
    "v_mul_f32 v81, v201, v201\n"
    "v_mfma_f32_16x16x32_bf16 v[212:215], v[180:183], v[164:167], v[212:215]\n"
    "v_mul_f32 v82, v204, v204\n"
    "v_mul_f32 v83, v205, v205\n"
    "v_mfma_f32_16x16x32_bf16 v[208:211], v[180:183], v[160:163], v[208:211]\n"
    "v_fma_f32 v76, v76, s64, v84\n"
    "v_fma_f32 v77, v77, s64, v84\n"
    "v_mfma_f32_16x16x32_bf16 v[208:211], v[148:151], v[128:131], v[208:211]\n"
    "v_fma_f32 v78, v78, s64, v84\n"
    "v_fma_f32 v79, v79, s64, v84\n"
    "v_mfma_f32_16x16x32_bf16 v[224:227], v[152:155], v[128:131], v[224:227]\n"
    "v_fma_f32 v80, v80, s64, v84\n"
    "v_fma_f32 v81, v81, s64, v84\n"
    "v_mfma_f32_16x16x32_bf16 v[224:227], v[184:187], v[160:163], v[224:227]\n"
    "v_fma_f32 v82, v82, s64, v84\n"
    "v_fma_f32 v83, v83, s64, v84\n"
    "v_mfma_f32_16x16x32_bf16 v[228:231], v[184:187], v[164:167], v[228:231]\n"
    "v_mul_f32 v76, v76, v192\n"
    "v_mul_f32 v77, v77, v193\n"
    "v_mfma_f32_16x16x32_bf16 v[228:231], v[152:155], v[132:135], v[228:231]\n"
    "v_mul_f32 v78, v78, v196\n"
    "v_mul_f32 v79, v79, v197\n"
    "v_mfma_f32_16x16x32_bf16 v[232:235], v[152:155], v[136:139], v[232:235]\n"
    "v_mul_f32 v80, v80, v200\n"
    "v_mul_f32 v81, v81, v201\n"
    "v_mfma_f32_16x16x32_bf16 v[232:235], v[184:187], v[168:171], v[232:235]\n"
    "v_mul_f32 v82, v82, v204\n"
    "v_mul_f32 v83, v83, v205\n"
    "v_mfma_f32_16x16x32_bf16 v[236:239], v[184:187], v[172:175], v[236:239]\n"
    "v_exp_f32 v76, v76\n"
    "v_mfma_f32_16x16x32_bf16 v[236:239], v[152:155], v[140:143], v[236:239]\n"
    "v_exp_f32 v77, v77\n"
    "v_mfma_f32_16x16x32_bf16 v[252:255], v[156:159], v[140:143], v[252:255]\n"
    "v_exp_f32 v78, v78\n"
    "v_mfma_f32_16x16x32_bf16 v[252:255], v[188:191], v[172:175], v[252:255]\n"
    "v_exp_f32 v79, v79\n"
    "v_mfma_f32_16x16x32_bf16 v[248:251], v[188:191], v[168:171], v[248:251]\n"
    "v_exp_f32 v80, v80\n"
    "v_mfma_f32_16x16x32_bf16 v[248:251], v[156:159], v[136:139], v[248:251]\n"
    "v_exp_f32 v81, v81\n"
    "v_mfma_f32_16x16x32_bf16 v[244:247], v[156:159], v[132:135], v[244:247]\n"
    "v_exp_f32 v82, v82\n"
    "v_mfma_f32_16x16x32_bf16 v[244:247], v[188:191], v[164:167], v[244:247]\n"
    "v_exp_f32 v83, v83\n"
    "v_mfma_f32_16x16x32_bf16 v[240:243], v[188:191], v[160:163], v[240:243]\n"
    "v_mfma_f32_16x16x32_bf16 v[240:243], v[156:159], v[128:131], v[240:243]\n"
    "s_cmp_lg_u32 s21, 0\n"
    "s_cbranch_scc1 .Lxd_g1\n"
    "s_barrier\n"
    ".Lxd_g1:\n"
    "s_setprio 2\n"
    "s_cmp_eq_u32 s22, 3\n"
    "s_cbranch_scc1 .Lxd_np\n"
    "ds_write_b32 v25, v24 offset:20\n"
    "ds_write_b128 v15, v[28:31] offset:0\n"
    "ds_write_b128 v15, v[32:35] offset:144\n"
    "ds_write_b128 v15, v[36:39] offset:288\n"
    "ds_write2_b32 v16, v40, v12 offset1:72\n"
    "ds_or_b32 v25, v21 offset:532\n"
    ".Lxd_np:\n"
    "ds_read2_b32 v[26:27], v41 offset1:1\n"
    "ds_read_b32 v0, v41 offset:16\n"
    "ds_read_b32 v23, v41 offset:24\n"
    "s_load_dword s26, s[68:69], 0\n"
    "v_add_u32 v25, s24, v25\n"
    "ds_read_b128 v[92:95], v42 offset:0\n"
    "ds_read_b128 v[96:99], v42 offset:1024\n"
    "ds_read_b128 v[100:103], v42 offset:2048\n"
    "ds_read_b128 v[104:107], v42 offset:3072\n"
    "ds_read_b128 v[108:111], v42 offset:4096\n"
    "ds_read_b128 v[112:115], v42 offset:5120\n"
    "ds_read_b128 v[116:119], v42 offset:6144\n"
    "ds_read_b128 v[120:123], v42 offset:7168\n"
    "ds_read_b128 v[124:127], v42 offset:8192\n"
    "s_cmp_eq_u32 s22, 3\n"
    "s_cbranch_scc1 .Lxd_on6\n"
    ".Lxd_on6r:\n"
    "v_pk_fma_f32 v[76:77], v[76:77], v[60:61], v[60:61]\n"
    "v_pk_fma_f32 v[78:79], v[78:79], v[60:61], v[60:61]\n"
    "v_pk_fma_f32 v[80:81], v[80:81], v[60:61], v[60:61]\n"
    "v_pk_fma_f32 v[82:83], v[82:83], v[60:61], v[60:61]\n"
    "v_rcp_f32 v76, v76\n"
    "v_rcp_f32 v77, v77\n"
    "v_rcp_f32 v78, v78\n"
    "v_rcp_f32 v79, v79\n"
    "v_rcp_f32 v80, v80\n"
    "v_rcp_f32 v81, v81\n"
    "v_rcp_f32 v82, v82\n"
    "v_rcp_f32 v83, v83\n"
    "v_pk_mul_f32 v[84:85], v[192:193], v[76:77]\n"
    "v_pk_mul_f32 v[86:87], v[196:197], v[78:79]\n"
    "v_pk_mul_f32 v[88:89], v[200:201], v[80:81]\n"
    "v_pk_mul_f32 v[90:91], v[204:205], v[82:83]\n"
    "s_setprio 3\n"
    "v_pk_mul_f32 v[76:77], v[194:195], v[194:195]\n"
    "v_pk_mul_f32 v[78:79], v[198:199], v[198:199]\n"
    "v_pk_mul_f32 v[80:81], v[202:203], v[202:203]\n"
    "v_pk_mul_f32 v[82:83], v[206:207], v[206:207]\n"
    "v_pk_fma_f32 v[76:77], v[76:77], s[64:65], s[64:65] op_sel:[0,0,1] op_sel_hi:[1,0,1]\n"
    "v_pk_fma_f32 v[78:79], v[78:79], s[64:65], s[64:65] op_sel:[0,0,1] op_sel_hi:[1,0,1]\n"
    "v_pk_fma_f32 v[80:81], v[80:81], s[64:65], s[64:65] op_sel:[0,0,1] op_sel_hi:[1,0,1]\n"
    "v_pk_fma_f32 v[82:83], v[82:83], s[64:65], s[64:65] op_sel:[0,0,1] op_sel_hi:[1,0,1]\n"
    "v_pk_mul_f32 v[76:77], v[76:77], v[194:195]\n"
    "v_pk_mul_f32 v[78:79], v[78:79], v[198:199]\n"
    "v_pk_mul_f32 v[80:81], v[80:81], v[202:203]\n"
    "v_pk_mul_f32 v[82:83], v[82:83], v[206:207]\n"
    "v_exp_f32 v76, v76\n"
    "v_exp_f32 v77, v77\n"
    "v_exp_f32 v78, v78\n"
    "v_exp_f32 v79, v79\n"
    "v_exp_f32 v80, v80\n"
    "v_exp_f32 v81, v81\n"
    "v_exp_f32 v82, v82\n"
    "v_exp_f32 v83, v83\n"
    "v_pk_fma_f32 v[76:77], v[76:77], v[62:63], v[62:63]\n"
    "v_pk_fma_f32 v[78:79], v[78:79], v[62:63], v[62:63]\n"
    "v_pk_fma_f32 v[80:81], v[80:81], v[62:63], v[62:63]\n"
    "v_pk_fma_f32 v[82:83], v[82:83], v[62:63], v[62:63]\n"
    "v_rcp_f32 v76, v76\n"
    "v_rcp_f32 v77, v77\n"
    "v_rcp_f32 v78, v78\n"
    "v_rcp_f32 v79, v79\n"
    "v_rcp_f32 v80, v80\n"
    "v_rcp_f32 v81, v81\n"
    "v_rcp_f32 v82, v82\n"
    "v_rcp_f32 v83, v83\n"
    "v_pk_fma_f32 v[84:85], v[194:195], v[76:77], v[84:85]\n"
    "v_pk_fma_f32 v[86:87], v[198:199], v[78:79], v[86:87]\n"
    "v_pk_fma_f32 v[88:89], v[202:203], v[80:81], v[88:89]\n"
    "v_pk_fma_f32 v[90:91], v[206:207], v[82:83], v[90:91]\n"
    "ds_read_b32 v22, v13 offset:512\n"
    "v_pk_mul_f32 v[76:77], v[208:209], v[208:209]\n"
    "v_pk_mul_f32 v[78:79], v[212:213], v[212:213]\n"
    "v_pk_mul_f32 v[80:81], v[216:217], v[216:217]\n"
    "v_pk_mul_f32 v[82:83], v[220:221], v[220:221]\n"
    "v_pk_fma_f32 v[76:77], v[76:77], s[64:65], s[64:65] op_sel:[0,0,1] op_sel_hi:[1,0,1]\n"
    "v_pk_fma_f32 v[78:79], v[78:79], s[64:65], s[64:65] op_sel:[0,0,1] op_sel_hi:[1,0,1]\n"
    "v_pk_fma_f32 v[80:81], v[80:81], s[64:65], s[64:65] op_sel:[0,0,1] op_sel_hi:[1,0,1]\n"
    "v_pk_fma_f32 v[82:83], v[82:83], s[64:65], s[64:65] op_sel:[0,0,1] op_sel_hi:[1,0,1]\n"
    "v_pk_mul_f32 v[76:77], v[76:77], v[208:209]\n"
    "v_pk_mul_f32 v[78:79], v[78:79], v[212:213]\n"
    "v_pk_mul_f32 v[80:81], v[80:81], v[216:217]\n"
    "v_pk_mul_f32 v[82:83], v[82:83], v[220:221]\n"
    "v_exp_f32 v76, v76\n"
    "v_exp_f32 v77, v77\n"
    "ds_read_b128 v[128:131], v42 offset:9216\n"
    "v_exp_f32 v78, v78\n"
    "v_exp_f32 v79, v79\n"
    "v_exp_f32 v80, v80\n"
    "ds_read_b128 v[132:135], v42 offset:10240\n"
    "v_exp_f32 v81, v81\n"
    "v_exp_f32 v82, v82\n"
    "v_exp_f32 v83, v83\n"
    "ds_read_b128 v[136:139], v42 offset:11264\n"
    "v_pk_fma_f32 v[76:77], v[76:77], v[64:65], v[64:65]\n"
    "v_pk_fma_f32 v[78:79], v[78:79], v[64:65], v[64:65]\n"
    "v_pk_fma_f32 v[80:81], v[80:81], v[64:65], v[64:65]\n"
    "v_pk_fma_f32 v[82:83], v[82:83], v[64:65], v[64:65]\n"
    "v_rcp_f32 v76, v76\n"
    "v_rcp_f32 v77, v77\n"
    "ds_read_b128 v[140:143], v42 offset:12288\n"
    "v_rcp_f32 v78, v78\n"
    "v_rcp_f32 v79, v79\n"
    "v_rcp_f32 v80, v80\n"
    "ds_read_b128 v[144:147], v42 offset:13312\n"
    "v_rcp_f32 v81, v81\n"
    "v_rcp_f32 v82, v82\n"
    "v_rcp_f32 v83, v83\n"
    "ds_read_b128 v[148:151], v42 offset:14336\n"
    "v_pk_fma_f32 v[84:85], v[208:209], v[76:77], v[84:85]\n"
    "v_pk_fma_f32 v[86:87], v[212:213], v[78:79], v[86:87]\n"
    "v_pk_fma_f32 v[88:89], v[216:217], v[80:81], v[88:89]\n"
    "v_pk_fma_f32 v[90:91], v[220:221], v[82:83], v[90:91]\n"
    "s_waitcnt lgkmcnt(6)\n"
    "v_readfirstlane_b32 s45, v22\n"
    "s_cmp_lt_i32 s45, 16\n"
    "s_cbranch_scc1 .Lxd_ck7\n"
    ".Lxd_ck7r:\n"
    "ds_read_b64_tr_b16 v[44:45], v9 offset:0\n"
    "ds_read_b64_tr_b16 v[46:47], v10 offset:0\n"
    "ds_read_b64_tr_b16 v[48:49], v7 offset:0\n"
    "ds_read_b64_tr_b16 v[50:51], v8 offset:576\n"
    "ds_read_b64_tr_b16 v[52:53], v8 offset:1152\n"
    "ds_read_b64_tr_b16 v[54:55], v8 offset:1728\n"
    "ds_read_b64_tr_b16 v[56:57], v8 offset:2304\n"
    "ds_read_b64_tr_b16 v[58:59], v8 offset:2880\n"
    "v_readfirstlane_b32 s28, v26\n"
    "v_readfirstlane_b32 s29, v27\n"
    "s_cmp_eq_u32 s22, 3\n"
    "s_cbranch_scc1 .Lxd_on9\n"
    ".Lxd_on9r:\n"
    "s_cmp_eq_u32 s22, 0\n"
    "s_cbranch_scc1 .Lxd_on10\n"
    ".Lxd_on10r:\n"
    "v_pk_mul_f32 v[76:77], v[210:211], v[210:211]\n"
    "v_pk_mul_f32 v[78:79], v[214:215], v[214:215]\n"
    "v_pk_mul_f32 v[80:81], v[218:219], v[218:219]\n"
    "v_pk_mul_f32 v[82:83], v[222:223], v[222:223]\n"
    "v_pk_fma_f32 v[76:77], v[76:77], s[64:65], s[64:65] op_sel:[0,0,1] op_sel_hi:[1,0,1]\n"
    "v_pk_fma_f32 v[78:79], v[78:79], s[64:65], s[64:65] op_sel:[0,0,1] op_sel_hi:[1,0,1]\n"
    "v_pk_fma_f32 v[80:81], v[80:81], s[64:65], s[64:65] op_sel:[0,0,1] op_sel_hi:[1,0,1]\n"
    "v_pk_fma_f32 v[82:83], v[82:83], s[64:65], s[64:65] op_sel:[0,0,1] op_sel_hi:[1,0,1]\n"
    "v_pk_mul_f32 v[76:77], v[76:77], v[210:211]\n"
    "v_pk_mul_f32 v[78:79], v[78:79], v[214:215]\n"
    "v_pk_mul_f32 v[80:81], v[80:81], v[218:219]\n"
    "v_pk_mul_f32 v[82:83], v[82:83], v[222:223]\n"
    "v_exp_f32 v76, v76\n"
    "v_exp_f32 v77, v77\n"
    "ds_read_b128 v[152:155], v42 offset:15360\n"
    "v_exp_f32 v78, v78\n"
    "v_exp_f32 v79, v79\n"
    "v_exp_f32 v80, v80\n"
    "ds_read_b128 v[156:159], v42 offset:16384\n"
    "v_exp_f32 v81, v81\n"
    "v_exp_f32 v82, v82\n"
    "v_exp_f32 v83, v83\n"
    "ds_read_b128 v[160:163], v42 offset:17408\n"
    "v_pk_fma_f32 v[76:77], v[76:77], v[66:67], v[66:67]\n"
    "v_pk_fma_f32 v[78:79], v[78:79], v[66:67], v[66:67]\n"
    "v_pk_fma_f32 v[80:81], v[80:81], v[66:67], v[66:67]\n"
    "v_pk_fma_f32 v[82:83], v[82:83], v[66:67], v[66:67]\n"
    "v_rcp_f32 v76, v76\n"
    "v_rcp_f32 v77, v77\n"
    "ds_read_b128 v[164:167], v42 offset:18432\n"
    "v_rcp_f32 v78, v78\n"
    "v_rcp_f32 v79, v79\n"
    "v_rcp_f32 v80, v80\n"
    "ds_read_b128 v[168:171], v42 offset:19456\n"
    "v_rcp_f32 v81, v81\n"
    "v_rcp_f32 v82, v82\n"
    "v_rcp_f32 v83, v83\n"
    "ds_read_b128 v[172:175], v42 offset:20480\n"
    "v_pk_fma_f32 v[84:85], v[210:211], v[76:77], v[84:85]\n"
    "v_pk_fma_f32 v[86:87], v[214:215], v[78:79], v[86:87]\n"
    "v_pk_fma_f32 v[88:89], v[218:219], v[80:81], v[88:89]\n"
    "v_pk_fma_f32 v[90:91], v[222:223], v[82:83], v[90:91]\n"
    "s_setprio 2\n"
    "v_pk_mul_f32 v[76:77], v[224:225], v[224:225]\n"
    "v_pk_mul_f32 v[78:79], v[228:229], v[228:229]\n"
    "v_pk_mul_f32 v[80:81], v[232:233], v[232:233]\n"
    "v_pk_mul_f32 v[82:83], v[236:237], v[236:237]\n"
    "v_pk_fma_f32 v[76:77], v[76:77], s[64:65], s[64:65] op_sel:[0,0,1] op_sel_hi:[1,0,1]\n"
    "v_pk_fma_f32 v[78:79], v[78:79], s[64:65], s[64:65] op_sel:[0,0,1] op_sel_hi:[1,0,1]\n"
    "v_pk_fma_f32 v[80:81], v[80:81], s[64:65], s[64:65] op_sel:[0,0,1] op_sel_hi:[1,0,1]\n"
    "v_pk_fma_f32 v[82:83], v[82:83], s[64:65], s[64:65] op_sel:[0,0,1] op_sel_hi:[1,0,1]\n"
    "v_pk_mul_f32 v[76:77], v[76:77], v[224:225]\n"
    "v_pk_mul_f32 v[78:79], v[78:79], v[228:229]\n"
    "v_pk_mul_f32 v[80:81], v[80:81], v[232:233]\n"
    "v_pk_mul_f32 v[82:83], v[82:83], v[236:237]\n"
    "v_exp_f32 v76, v76\n"
    "v_exp_f32 v77, v77\n"
    "ds_read_b128 v[176:179], v42 offset:21504\n"
    "v_exp_f32 v78, v78\n"
    "v_exp_f32 v79, v79\n"
    "v_exp_f32 v80, v80\n"
    "ds_read_b128 v[180:183], v42 offset:22528\n"
    "v_exp_f32 v81, v81\n"
    "v_exp_f32 v82, v82\n"
    "v_exp_f32 v83, v83\n"
    "ds_read_b128 v[184:187], v42 offset:23552\n"
    "v_pk_fma_f32 v[76:77], v[76:77], v[68:69], v[68:69]\n"
    "v_pk_fma_f32 v[78:79], v[78:79], v[68:69], v[68:69]\n"
    "v_pk_fma_f32 v[80:81], v[80:81], v[68:69], v[68:69]\n"
    "v_pk_fma_f32 v[82:83], v[82:83], v[68:69], v[68:69]\n"
    "v_rcp_f32 v76, v76\n"
    "v_rcp_f32 v77, v77\n"
    "ds_read_b128 v[188:191], v42 offset:24576\n"
    "v_rcp_f32 v78, v78\n"
    "v_rcp_f32 v79, v79\n"
    "v_rcp_f32 v80, v80\n"
    "ds_read_b128 v[192:195], v42 offset:25600\n"
    "v_rcp_f32 v81, v81\n"
    "v_rcp_f32 v82, v82\n"
    "v_rcp_f32 v83, v83\n"
    "ds_read_b128 v[196:199], v42 offset:26624\n"
    "v_pk_fma_f32 v[84:85], v[224:225], v[76:77], v[84:85]\n"
    "v_pk_fma_f32 v[86:87], v[228:229], v[78:79], v[86:87]\n"
    "v_pk_fma_f32 v[88:89], v[232:233], v[80:81], v[88:89]\n"
    "v_pk_fma_f32 v[90:91], v[236:237], v[82:83], v[90:91]\n"
    "v_pk_mul_f32 v[76:77], v[226:227], v[226:227]\n"
    "v_pk_mul_f32 v[78:79], v[230:231], v[230:231]\n"
    "v_pk_mul_f32 v[80:81], v[234:235], v[234:235]\n"
    "v_pk_mul_f32 v[82:83], v[238:239], v[238:239]\n"
    "v_pk_fma_f32 v[76:77], v[76:77], s[64:65], s[64:65] op_sel:[0,0,1] op_sel_hi:[1,0,1]\n"
    "v_pk_fma_f32 v[78:79], v[78:79], s[64:65], s[64:65] op_sel:[0,0,1] op_sel_hi:[1,0,1]\n"
    "v_pk_fma_f32 v[80:81], v[80:81], s[64:65], s[64:65] op_sel:[0,0,1] op_sel_hi:[1,0,1]\n"
    "v_pk_fma_f32 v[82:83], v[82:83], s[64:65], s[64:65] op_sel:[0,0,1] op_sel_hi:[1,0,1]\n"
    "v_pk_mul_f32 v[76:77], v[76:77], v[226:227]\n"
    "v_pk_mul_f32 v[78:79], v[78:79], v[230:231]\n"
    "v_pk_mul_f32 v[80:81], v[80:81], v[234:235]\n"
    "v_pk_mul_f32 v[82:83], v[82:83], v[238:239]\n"
    "v_exp_f32 v76, v76\n"
    "v_exp_f32 v77, v77\n"
    "ds_read_b128 v[200:203], v42 offset:27648\n"
    "v_exp_f32 v78, v78\n"
    "v_exp_f32 v79, v79\n"
    "v_exp_f32 v80, v80\n"
    "ds_read_b128 v[204:207], v42 offset:28672\n"
    "v_exp_f32 v81, v81\n"
    "v_exp_f32 v82, v82\n"
    "v_exp_f32 v83, v83\n"
    "ds_read_b128 v[208:211], v42 offset:29696\n"
    "v_pk_fma_f32 v[76:77], v[76:77], v[70:71], v[70:71]\n"
    "v_pk_fma_f32 v[78:79], v[78:79], v[70:71], v[70:71]\n"
    "v_pk_fma_f32 v[80:81], v[80:81], v[70:71], v[70:71]\n"
    "v_pk_fma_f32 v[82:83], v[82:83], v[70:71], v[70:71]\n"
    "v_rcp_f32 v76, v76\n"
    "v_rcp_f32 v77, v77\n"
    "ds_read_b128 v[212:215], v42 offset:30720\n"
    "v_rcp_f32 v78, v78\n"
    "v_rcp_f32 v79, v79\n"
    "v_rcp_f32 v80, v80\n"
    "ds_read_b128 v[216:219], v42 offset:31744\n"
    "v_rcp_f32 v81, v81\n"
    "v_rcp_f32 v82, v82\n"
    "v_rcp_f32 v83, v83\n"
    "ds_read_b128 v[220:223], v42 offset:32768\n"
    "v_pk_fma_f32 v[84:85], v[226:227], v[76:77], v[84:85]\n"
    "v_pk_fma_f32 v[86:87], v[230:231], v[78:79], v[86:87]\n"
    "v_pk_fma_f32 v[88:89], v[234:235], v[80:81], v[88:89]\n"
    "v_pk_fma_f32 v[90:91], v[238:239], v[82:83], v[90:91]\n"
    "s_cmp_eq_u32 s22, 3\n"
    "s_cbranch_scc1 .Lxd_on11\n"
    ".Lxd_on11r:\n"
    "s_cmp_eq_u32 s22, 0\n"
    "s_cbranch_scc1 .Lxd_on13\n"
    ".Lxd_on13r:\n"
    "s_cmp_eq_u32 s22, 1\n"
    "s_cbranch_scc1 .Lxd_on14\n"
    ".Lxd_on14r:\n"
    "s_setprio 1\n"
    "v_pk_mul_f32 v[76:77], v[240:241], v[240:241]\n"
    "v_pk_mul_f32 v[78:79], v[244:245], v[244:245]\n"
    "v_pk_mul_f32 v[80:81], v[248:249], v[248:249]\n"
    "v_pk_mul_f32 v[82:83], v[252:253], v[252:253]\n"
    "v_pk_fma_f32 v[76:77], v[76:77], s[64:65], s[64:65] op_sel:[0,0,1] op_sel_hi:[1,0,1]\n"
    "v_pk_fma_f32 v[78:79], v[78:79], s[64:65], s[64:65] op_sel:[0,0,1] op_sel_hi:[1,0,1]\n"
    "v_pk_fma_f32 v[80:81], v[80:81], s[64:65], s[64:65] op_sel:[0,0,1] op_sel_hi:[1,0,1]\n"
    "v_pk_fma_f32 v[82:83], v[82:83], s[64:65], s[64:65] op_sel:[0,0,1] op_sel_hi:[1,0,1]\n"
    "v_pk_mul_f32 v[76:77], v[76:77], v[240:241]\n"
    "v_pk_mul_f32 v[78:79], v[78:79], v[244:245]\n"
    "v_pk_mul_f32 v[80:81], v[80:81], v[248:249]\n"
    "v_pk_mul_f32 v[82:83], v[82:83], v[252:253]\n"
    "v_exp_f32 v76, v76\n"
    "v_exp_f32 v77, v77\n"
    "v_exp_f32 v78, v78\n"
    "v_exp_f32 v79, v79\n"
    "v_exp_f32 v80, v80\n"
    "v_exp_f32 v81, v81\n"
    "v_exp_f32 v82, v82\n"
    "v_exp_f32 v83, v83\n"
    "v_pk_fma_f32 v[76:77], v[76:77], v[72:73], v[72:73]\n"
    "v_pk_fma_f32 v[78:79], v[78:79], v[72:73], v[72:73]\n"
    "v_pk_fma_f32 v[80:81], v[80:81], v[72:73], v[72:73]\n"
    "v_pk_fma_f32 v[82:83], v[82:83], v[72:73], v[72:73]\n"
    "v_rcp_f32 v76, v76\n"
    "v_rcp_f32 v77, v77\n"
    "v_rcp_f32 v78, v78\n"
    "v_rcp_f32 v79, v79\n"
    "v_rcp_f32 v80, v80\n"
    "v_rcp_f32 v81, v81\n"
    "v_rcp_f32 v82, v82\n"
    "v_rcp_f32 v83, v83\n"
    "v_pk_fma_f32 v[84:85], v[240:241], v[76:77], v[84:85]\n"
    "v_pk_fma_f32 v[86:87], v[244:245], v[78:79], v[86:87]\n"
    "v_pk_fma_f32 v[88:89], v[248:249], v[80:81], v[88:89]\n"
    "v_pk_fma_f32 v[90:91], v[252:253], v[82:83], v[90:91]\n"
    "s_setprio 0\n"
    "v_pk_mul_f32 v[76:77], v[242:243], v[242:243]\n"
    "v_pk_mul_f32 v[78:79], v[246:247], v[246:247]\n"
    "v_pk_mul_f32 v[80:81], v[250:251], v[250:251]\n"
    "v_pk_mul_f32 v[82:83], v[254:255], v[254:255]\n"
    "v_pk_fma_f32 v[76:77], v[76:77], s[64:65], s[64:65] op_sel:[0,0,1] op_sel_hi:[1,0,1]\n"
    "v_pk_fma_f32 v[78:79], v[78:79], s[64:65], s[64:65] op_sel:[0,0,1] op_sel_hi:[1,0,1]\n"
    "v_pk_fma_f32 v[80:81], v[80:81], s[64:65], s[64:65] op_sel:[0,0,1] op_sel_hi:[1,0,1]\n"
    "v_pk_fma_f32 v[82:83], v[82:83], s[64:65], s[64:65] op_sel:[0,0,1] op_sel_hi:[1,0,1]\n"
    "v_pk_mul_f32 v[76:77], v[76:77], v[242:243]\n"
    "v_pk_mul_f32 v[78:79], v[78:79], v[246:247]\n"
    "v_pk_mul_f32 v[80:81], v[80:81], v[250:251]\n"
    "v_pk_mul_f32 v[82:83], v[82:83], v[254:255]\n"
    "v_exp_f32 v76, v76\n"
    "v_exp_f32 v77, v77\n"
    "v_exp_f32 v78, v78\n"
    "v_exp_f32 v79, v79\n"
    "v_exp_f32 v80, v80\n"
    "v_exp_f32 v81, v81\n"
    "v_exp_f32 v82, v82\n"
    "v_exp_f32 v83, v83\n"
    "v_pk_fma_f32 v[76:77], v[76:77], v[74:75], v[74:75]\n"
    "v_pk_fma_f32 v[78:79], v[78:79], v[74:75], v[74:75]\n"
    "v_pk_fma_f32 v[80:81], v[80:81], v[74:75], v[74:75]\n"
    "v_pk_fma_f32 v[82:83], v[82:83], v[74:75], v[74:75]\n"
    "v_rcp_f32 v76, v76\n"
    "v_rcp_f32 v77, v77\n"
    "v_rcp_f32 v78, v78\n"
    "v_rcp_f32 v79, v79\n"
    "v_rcp_f32 v80, v80\n"
    "v_rcp_f32 v81, v81\n"
    "v_rcp_f32 v82, v82\n"
    "v_rcp_f32 v83, v83\n"
    "v_pk_fma_f32 v[84:85], v[242:243], v[76:77], v[84:85]\n"
    "v_pk_fma_f32 v[86:87], v[246:247], v[78:79], v[86:87]\n"
    "v_pk_fma_f32 v[88:89], v[250:251], v[80:81], v[88:89]\n"
    "v_pk_fma_f32 v[90:91], v[254:255], v[82:83], v[90:91]\n"
    "v_add_f32 v84, v84, v85\n"
    "v_add_f32 v86, v86, v87\n"
    "v_add_f32 v88, v88, v89\n"
    "v_add_f32 v90, v90, v91\n"
    "s_waitcnt lgkmcnt(15)\n"
    "v_pk_mul_f16 v60, v44, s45 op_sel:[0,1]\n"
    "v_pk_mul_f16 v61, v45, s45 op_sel:[0,1]\n"
    "v_permlane32_swap_b32 v84, v88\n"
    "v_permlane32_swap_b32 v86, v90\n"
    "v_add_f32 v84, v84, v88\n"
    "v_add_f32 v86, v86, v90\n"
    "v_pk_mul_f16 v62, v46, s45 op_sel:[0,1]\n"
    "v_pk_mul_f16 v63, v47, s45 op_sel:[0,1]\n"
    "v_permlane16_swap_b32 v84, v86\n"
    "v_add_f32 v84, v84, v86\n"
    "v_and_b32 v26, -2, v84\n"
    "s_waitcnt lgkmcnt(0)\n"
    "ds_wrxchg_rtn_b32 v27, v11, v26\n"
    "ds_read_b128 v[224:227], v42 offset:33792\n"
    "ds_read_b128 v[228:231], v42 offset:34816\n"
    "ds_read_b128 v[232:235], v42 offset:35840\n"
    "ds_read_b128 v[236:239], v42 offset:36864\n"
    "ds_read_b128 v[240:243], v42 offset:37888\n"
    "ds_read_b128 v[244:247], v42 offset:38912\n"
    "ds_read_b128 v[248:251], v42 offset:39936\n"
    "ds_read_b128 v[252:255], v42 offset:40960\n"
    "v_pk_mul_f16 v64, v44, v46\n"
    "v_pk_mul_f16 v65, v45, v47\n"
    "v_pk_mul_f16 v66, v48, v48 op_sel_hi:[0,1]\n"
    "v_pk_mul_f16 v67, v48, v49 op_sel_hi:[0,1]\n"
    "v_mov_b64 v[88:89], 0\n"
    "v_mov_b64 v[90:91], 0\n"
    "s_mov_b32 s46, -1\n"
    ".Lxd_enter:\n"
    "s_cmp_eq_u32 s21, 0\n"
    "s_cselect_b64 vcc, 0, -1\n"
    "s_cbranch_scc1 .Lxd_pr\n"
    "s_setprio 1\n"
    ".Lxd_pr:\n"
    "s_cmp_lg_u32 s36, 0\n"
    "s_cbranch_scc1 .Lxd_el\n"
    "s_setpc_b64 s[10:11]\n"
    ".Lxd_el:\n"
    "s_cmp_eq_u32 s22, 0\n"
    "s_cbranch_scc1 .Lxd_pl1\n"
    "s_branch .Lxd_pl2\n"
    ".Lxd_ph0:\n"
    "v_smfmac_f32_16x16x64_f16 v[88:91], v[112:115], v[60:67], v2\n"
    "s_cbranch_vccnz .Lxd_pr15\n"
    "s_setprio 2\n"
    ".Lxd_pr15:\n"
    "v_pk_mul_f16 v68, v58, v58 op_sel_hi:[0,1]\n"
    "v_pk_mul_f16 v69, v58, v49 op_sel_hi:[0,1]\n"
    "v_mfma_f32_16x16x32_f16 v[84:87], v[108:111], v[64:67], 0\n"
    "v_fmac_f32 v11, v76, v80\n"
    "v_fmac_f32 v11, v77, v81\n"
    "v_mfma_f32_16x16x32_f16 v[84:87], v[96:99], v[60:63], v[84:87]\n"
    "v_fmac_f32 v11, v78, v82\n"
    "v_fma_f32 v10, v79, v83, v11\n"
    "v_mfma_f32_16x16x32_f16 v[80:83], v[92:95], v[60:63], 0\n"
    "s_waitcnt lgkmcnt(0)\n"
    "v_pk_mul_f16 v70, v48, v58 op_sel_hi:[0,1]\n"
    "v_pk_mul_f16 v71, v48, v59 op_sel_hi:[0,1]\n"
    "v_mfma_f32_16x16x32_f16 v[80:83], v[104:107], v[64:67], v[80:83]\n"
    "s_waitcnt vmcnt(0)\n"
    "v_pk_mul_f16 v72, v50, v58 op_sel_hi:[0,1]\n"
    "v_pk_mul_f16 v73, v50, v59 op_sel_hi:[0,1]\n"
    "v_mfma_f32_16x16x32_f16 v[76:79], v[100:103], v[64:67], 0\n"
    "s_cmp_lt_i32 s46, 0\n"
    "v_pk_mul_f16 v74, v52, v58 op_sel_hi:[0,1]\n"
    "v_pk_mul_f16 v75, v52, v59 op_sel_hi:[0,1]\n"
    "v_mfma_f32_16x16x32_f16 v[76:79], v[116:119], v[68:71], v[76:79]\n"
    "s_cbranch_scc1 .Lxd_ht18\n"
    ".Lxd_ht18r:\n"
    "v_pk_mul_f16 v60, v54, v58 op_sel_hi:[0,1]\n"
    "v_pk_mul_f16 v61, v54, v59 op_sel_hi:[0,1]\n"
    "v_mfma_f32_16x16x32_f16 v[76:79], v[128:131], v[72:75], v[76:79]\n"
    "v_pk_mul_f16 v62, v56, v58 op_sel_hi:[0,1]\n"
    "v_readfirstlane_b32 s40, v24\n"
    "v_mfma_f32_16x16x32_f16 v[80:83], v[132:135], v[72:75], v[80:83]\n"
    "s_or_b32 s40, s40, s44\n"
    "v_pk_mul_f16 v63, v56, v59 op_sel_hi:[0,1]\n"
    "v_permlane32_swap_b32 v7, v8\n"
    "v_mfma_f32_16x16x32_f16 v[80:83], v[120:123], v[68:71], v[80:83]\n"
    "s_lshl1_add_u32 s41, s40, s54\n"
    "v_pk_mul_f16 v64, v56, v56 op_sel_hi:[0,1]\n"
    "v_permlane32_swap_b32 v9, v10\n"
    "v_mfma_f32_16x16x32_f16 v[84:87], v[124:127], v[68:71], v[84:87]\n"
    "s_cmpk_lt_u32 s40, 256\n"
    "v_pk_mul_f16 v65, v56, v49 op_sel_hi:[0,1]\n"
    "v_mul_f32 v7, v0, v7\n"
    "v_mfma_f32_16x16x32_f16 v[84:87], v[136:139], v[72:75], v[84:87]\n"
    "s_cselect_b32 s41, s41, 2\n"
    "v_pk_mul_f16 v66, v48, v56 op_sel_hi:[0,1]\n"
    "v_mov_b32 v12, s41\n"
    "v_smfmac_f32_16x16x64_f16 v[88:91], v[140:143], v[68:75], v2\n"
    "s_cbranch_scc0 .Lxd_tf17\n"
    ".Lxd_tf17r:\n"
    "v_pk_mul_f16 v67, v48, v57 op_sel_hi:[0,1]\n"
    "v_mul_f32 v9, v0, v9\n"
    "v_mfma_f32_16x16x32_f16 v[76:79], v[144:147], v[60:63], v[76:79]\n"
    "s_mul_i32 s41, s41, 52\n"
    "v_pk_mul_f16 v68, v50, v56 op_sel_hi:[0,1]\n"
    "v_pk_mul_f16 v69, v50, v57 op_sel_hi:[0,1]\n"
    "v_mfma_f32_16x16x32_f16 v[76:79], v[156:159], v[64:67], v[76:79]\n"
    "s_add_u32 s38, s4, s41\n"
    "v_pk_mul_f16 v70, v52, v56 op_sel_hi:[0,1]\n"
    "v_fmac_f32 v7, v23, v8\n"
    "v_mfma_f32_16x16x32_f16 v[80:83], v[160:163], v[64:67], v[80:83]\n"
    "s_addc_u32 s39, s5, 0\n"
    "v_pk_mul_f16 v71, v52, v57 op_sel_hi:[0,1]\n"
    "v_fmac_f32 v9, v23, v10\n"
    "v_mfma_f32_16x16x32_f16 v[80:83], v[148:151], v[60:63], v[80:83]\n"
    "ds_read_b32 v22, v25 offset:516\n"
    "v_pk_mul_f16 v72, v54, v56 op_sel_hi:[0,1]\n"
    "v_permlane16_swap_b32 v7, v9\n"
    "v_mfma_f32_16x16x32_f16 v[84:87], v[152:155], v[60:63], v[84:87]\n"
    "global_load_dwordx3 v[36:38], v17, s[38:39] offset:-104\n"
    "v_pk_mul_f16 v73, v54, v57 op_sel_hi:[0,1]\n"
    "v_add_f32 v7, v7, v9\n"
    "v_mfma_f32_16x16x32_f16 v[84:87], v[164:167], v[64:67], v[84:87]\n"
    "global_load_dword v41, v18, s[38:39] offset:-8\n"
    "v_pk_mul_f16 v74, v54, v54 op_sel_hi:[0,1]\n"
    "v_add_f32_dpp v7, v7, v7 quad_perm:[1,0,3,2] row_mask:0xf bank_mask:0xf\n"
    "v_smfmac_f32_16x16x64_f16 v[88:91], v[168:171], v[60:67], v2\n"
    "ds_write_b32 v25, v12 offset:20\n"
    "v_pk_mul_f16 v75, v54, v49 op_sel_hi:[0,1]\n"
    "v_add_f32_dpp v7, v7, v7 quad_perm:[2,3,0,1] row_mask:0xf bank_mask:0xf\n"
    "v_mfma_f32_16x16x32_f16 v[76:79], v[172:175], v[68:71], v[76:79]\n"
    "v_pk_mul_f16 v60, v48, v54 op_sel_hi:[0,1]\n"
    "v_pk_mul_f16 v61, v48, v55 op_sel_hi:[0,1]\n"
    "v_mfma_f32_16x16x32_f16 v[76:79], v[184:187], v[72:75], v[76:79]\n"
    "s_waitcnt lgkmcnt(1)\n"
    "v_pk_mul_f16 v62, v50, v54 op_sel_hi:[0,1]\n"
    "v_readfirstlane_b32 s45, v22\n"
    "v_mfma_f32_16x16x32_f16 v[80:83], v[188:191], v[72:75], v[80:83]\n"
    "s_cmp_lt_i32 s45, 16\n"
    "v_pk_mul_f16 v63, v50, v55 op_sel_hi:[0,1]\n"
    "v_add_f32_dpp v7, v7, v7 row_half_mirror row_mask:0xf bank_mask:0xf\n"
    "v_mfma_f32_16x16x32_f16 v[80:83], v[176:179], v[68:71], v[80:83]\n"
    "s_cbranch_scc1 .Lxd_ck19\n"
    ".Lxd_ck19r:\n"
    "v_pk_mul_f16 v64, v52, v54 op_sel_hi:[0,1]\n"
    "v_add_f32_dpp v7, v7, v7 row_mirror row_mask:0xf bank_mask:0xf\n"
    "v_mfma_f32_16x16x32_f16 v[84:87], v[180:183], v[68:71], v[84:87]\n"
    "ds_read_b64_tr_b16 v[44:45], v5 offset:12288\n"
    "v_pk_mul_f16 v65, v52, v55 op_sel_hi:[0,1]\n"
    "v_mfma_f32_16x16x32_f16 v[84:87], v[192:195], v[72:75], v[84:87]\n"
    "ds_read_b64_tr_b16 v[46:47], v6 offset:12288\n"
    "v_pk_mul_f16 v66, v52, v52 op_sel_hi:[0,1]\n"
    "v_smfmac_f32_16x16x64_f16 v[88:91], v[196:199], v[68:75], v2\n"
    "ds_write_b32 v14, v7\n"
    "v_pk_mul_f16 v67, v52, v49 op_sel_hi:[0,1]\n"
    "v_add_u32 v14, 64, v14\n"
    "v_mfma_f32_16x16x32_f16 v[76:79], v[200:203], v[60:63], v[76:79]\n"
    "v_pk_mul_f16 v68, v48, v52 op_sel_hi:[0,1]\n"
    "v_pk_mul_f16 v69, v48, v53 op_sel_hi:[0,1]\n"
    "v_mfma_f32_16x16x32_f16 v[76:79], v[212:215], v[64:67], v[76:79]\n"
    "v_pk_mul_f16 v70, v50, v52 op_sel_hi:[0,1]\n"
    "v_mfma_f32_16x16x32_f16 v[80:83], v[216:219], v[64:67], v[80:83]\n"
    "s_cbranch_vccnz .Lxd_pr16\n"
    "s_setprio 0\n"
    ".Lxd_pr16:\n"
    "v_pk_mul_f16 v71, v50, v53 op_sel_hi:[0,1]\n"
    "v_mfma_f32_16x16x32_f16 v[80:83], v[204:207], v[60:63], v[80:83]\n"
    "v_pk_mul_f16 v72, v50, v50 op_sel_hi:[0,1]\n"
    "v_mfma_f32_16x16x32_f16 v[84:87], v[208:211], v[60:63], v[84:87]\n"
    "v_pk_mul_f16 v73, v50, v49 op_sel_hi:[0,1]\n"
    "v_pk_mul_f16 v74, v48, v50 op_sel_hi:[0,1]\n"
    "v_mfma_f32_16x16x32_f16 v[84:87], v[220:223], v[64:67], v[84:87]\n"
    "v_pk_mul_f16 v75, v48, v51 op_sel_hi:[0,1]\n"
    "v_smfmac_f32_16x16x64_f16 v[88:91], v[224:227], v[60:67], v2\n"
    "ds_read_b64_tr_b16 v[48:49], v3 offset:12288\n"
    "ds_read_b64_tr_b16 v[58:59], v4 offset:15168\n"
    "v_smfmac_f32_16x16x64_f16 v[88:91], v[252:255], v[68:75], v2\n"
    "s_waitcnt lgkmcnt(3)\n"
    "v_pk_mul_f16 v60, v44, s45 op_sel:[0,1]\n"
    "v_pk_mul_f16 v61, v45, s45 op_sel:[0,1]\n"
    "v_mfma_f32_16x16x32_f16 v[84:87], v[236:239], v[68:71], v[84:87]\n"
    "ds_read_b64_tr_b16 v[50:51], v4 offset:12864\n"
    "v_pk_mul_f16 v62, v46, s45 op_sel:[0,1]\n"
    "v_pk_mul_f16 v63, v47, s45 op_sel:[0,1]\n"
    "v_mfma_f32_16x16x32_f16 v[84:87], v[248:251], v[72:75], v[84:87]\n"
    "ds_read_b64_tr_b16 v[52:53], v4 offset:13440\n"
    "v_pk_mul_f16 v64, v44, v46\n"
    "v_pk_mul_f16 v65, v45, v47\n"
    "v_mfma_f32_16x16x32_f16 v[76:79], v[240:243], v[72:75], v[76:79]\n"
    "ds_read_b64_tr_b16 v[54:55], v4 offset:14016\n"
    "v_add_f32 v11, v88, v89\n"
    "v_add_f32 v13, v90, v91\n"
    "v_mfma_f32_16x16x32_f16 v[76:79], v[228:231], v[68:71], v[76:79]\n"
    "ds_read_b64_tr_b16 v[56:57], v4 offset:14592\n"
    "v_mov_b64 v[88:89], 0\n"
    "v_mov_b64 v[90:91], 0\n"
    "v_mfma_f32_16x16x32_f16 v[80:83], v[232:235], v[68:71], v[80:83]\n"
    "s_waitcnt lgkmcnt(4)\n"
    "v_pk_mul_f16 v66, v48, v48 op_sel_hi:[0,1]\n"
    "v_pk_mul_f16 v67, v48, v49 op_sel_hi:[0,1]\n"
    "v_mfma_f32_16x16x32_f16 v[80:83], v[244:247], v[72:75], v[80:83]\n"
    "s_cmp_lg_u32 s36, 0\n"
    "v_mul_f32 v11, v11, v13\n"
    "v_fmac_f32 v11, v84, v85\n"
    "v_fmac_f32 v11, v86, v87\n"
    "s_cbranch_scc1 .Lxd_pl1\n"
    ".Lxd_ph1:\n"
    "v_smfmac_f32_16x16x64_f16 v[88:91], v[112:115], v[60:67], v2\n"
    "s_cbranch_vccnz .Lxd_pr20\n"
    "s_setprio 2\n"
    ".Lxd_pr20:\n"
    "v_pk_mul_f16 v68, v58, v58 op_sel_hi:[0,1]\n"
    "v_pk_mul_f16 v69, v58, v49 op_sel_hi:[0,1]\n"
    "v_mfma_f32_16x16x32_f16 v[84:87], v[108:111], v[64:67], 0\n"
    "v_fmac_f32 v11, v76, v80\n"
    "v_fmac_f32 v11, v77, v81\n"
    "v_mfma_f32_16x16x32_f16 v[84:87], v[96:99], v[60:63], v[84:87]\n"
    "v_fmac_f32 v11, v78, v82\n"
    "v_fma_f32 v7, v79, v83, v11\n"
    "v_mfma_f32_16x16x32_f16 v[80:83], v[92:95], v[60:63], 0\n"
    "s_waitcnt lgkmcnt(0)\n"
    "v_pk_mul_f16 v70, v48, v58 op_sel_hi:[0,1]\n"
    "v_pk_mul_f16 v71, v48, v59 op_sel_hi:[0,1]\n"
    "v_mfma_f32_16x16x32_f16 v[80:83], v[104:107], v[64:67], v[80:83]\n"
    "s_waitcnt vmcnt(0)\n"
    "v_pk_mul_f16 v72, v50, v58 op_sel_hi:[0,1]\n"
    "v_pk_mul_f16 v73, v50, v59 op_sel_hi:[0,1]\n"
    "v_mfma_f32_16x16x32_f16 v[76:79], v[100:103], v[64:67], 0\n"
    "s_cmp_lg_u32 s46, 0\n"
    "v_pk_mul_f16 v74, v52, v58 op_sel_hi:[0,1]\n"
    "v_pk_mul_f16 v75, v52, v59 op_sel_hi:[0,1]\n"
    "v_mfma_f32_16x16x32_f16 v[76:79], v[116:119], v[68:71], v[76:79]\n"
    "s_cbranch_scc1 .Lxd_ht22\n"
    ".Lxd_ht22r:\n"
    "v_pk_mul_f16 v60, v54, v58 op_sel_hi:[0,1]\n"
    "v_pk_mul_f16 v61, v54, v59 op_sel_hi:[0,1]\n"
    "v_mfma_f32_16x16x32_f16 v[76:79], v[128:131], v[72:75], v[76:79]\n"
    "v_pk_mul_f16 v62, v56, v58 op_sel_hi:[0,1]\n"
    "v_lshl_or_b32 v36, v36, 7, v19\n"
    "v_mfma_f32_16x16x32_f16 v[80:83], v[132:135], v[72:75], v[80:83]\n"
    "global_load_dwordx4 v[28:31], v36, s[8:9]\n"
    "v_pk_mul_f16 v63, v56, v59 op_sel_hi:[0,1]\n"
    "v_lshl_or_b32 v37, v37, 7, v19\n"
    "v_mfma_f32_16x16x32_f16 v[80:83], v[120:123], v[68:71], v[80:83]\n"
    "global_load_dwordx4 v[32:35], v37, s[8:9]\n"
    "v_pk_mul_f16 v64, v56, v56 op_sel_hi:[0,1]\n"
    "v_lshl_or_b32 v41, v41, 7, v20\n"
    "v_mfma_f32_16x16x32_f16 v[84:87], v[124:127], v[68:71], v[84:87]\n"
    "global_load_dword v40, v41, s[8:9]\n"
    "v_pk_mul_f16 v65, v56, v49 op_sel_hi:[0,1]\n"
    "v_lshl_or_b32 v38, v38, 7, v19\n"
    "v_mfma_f32_16x16x32_f16 v[84:87], v[136:139], v[72:75], v[84:87]\n"
    "global_load_dwordx4 v[36:39], v38, s[8:9]\n"
    "v_pk_mul_f16 v66, v48, v56 op_sel_hi:[0,1]\n"
    "v_xor_b32 v15, 0x4000, v15\n"
    "v_smfmac_f32_16x16x64_f16 v[88:91], v[140:143], v[68:75], v2\n"
    "v_pk_mul_f16 v67, v48, v57 op_sel_hi:[0,1]\n"
    "v_xor_b32 v16, 0x4000, v16\n"
    "v_mfma_f32_16x16x32_f16 v[76:79], v[144:147], v[60:63], v[76:79]\n"
    "v_pk_mul_f16 v68, v50, v56 op_sel_hi:[0,1]\n"
    "v_pk_mul_f16 v69, v50, v57 op_sel_hi:[0,1]\n"
    "v_mfma_f32_16x16x32_f16 v[76:79], v[156:159], v[64:67], v[76:79]\n"
    "v_pk_mul_f16 v70, v52, v56 op_sel_hi:[0,1]\n"
    "v_xor_b32 v3, 0x4000, v3\n"
    "v_mfma_f32_16x16x32_f16 v[80:83], v[160:163], v[64:67], v[80:83]\n"
    "v_pk_mul_f16 v71, v52, v57 op_sel_hi:[0,1]\n"
    "v_xor_b32 v4, 0x4000, v4\n"
    "v_mfma_f32_16x16x32_f16 v[80:83], v[148:151], v[60:63], v[80:83]\n"
    "ds_read_b32 v22, v25 offset:520\n"
    "v_pk_mul_f16 v72, v54, v56 op_sel_hi:[0,1]\n"
    "v_xor_b32 v5, 0x4000, v5\n"
    "v_mfma_f32_16x16x32_f16 v[84:87], v[152:155], v[60:63], v[84:87]\n"
    "v_pk_mul_f16 v73, v54, v57 op_sel_hi:[0,1]\n"
    "v_xor_b32 v6, 0x4000, v6\n"
    "v_mfma_f32_16x16x32_f16 v[84:87], v[164:167], v[64:67], v[84:87]\n"
    "v_pk_mul_f16 v74, v54, v54 op_sel_hi:[0,1]\n"
    "v_smfmac_f32_16x16x64_f16 v[88:91], v[168:171], v[60:67], v2\n"
    "v_pk_mul_f16 v75, v54, v49 op_sel_hi:[0,1]\n"
    "v_mfma_f32_16x16x32_f16 v[76:79], v[172:175], v[68:71], v[76:79]\n"
    "v_pk_mul_f16 v60, v48, v54 op_sel_hi:[0,1]\n"
    "v_pk_mul_f16 v61, v48, v55 op_sel_hi:[0,1]\n"
    "v_mfma_f32_16x16x32_f16 v[76:79], v[184:187], v[72:75], v[76:79]\n"
    "s_waitcnt lgkmcnt(0)\n"
    "v_pk_mul_f16 v62, v50, v54 op_sel_hi:[0,1]\n"
    "v_readfirstlane_b32 s45, v22\n"
    "v_mfma_f32_16x16x32_f16 v[80:83], v[188:191], v[72:75], v[80:83]\n"
    "s_cmp_lt_i32 s45, 16\n"
    "v_pk_mul_f16 v63, v50, v55 op_sel_hi:[0,1]\n"
    "v_mfma_f32_16x16x32_f16 v[80:83], v[176:179], v[68:71], v[80:83]\n"
    "s_cbranch_scc1 .Lxd_ck23\n"
    ".Lxd_ck23r:\n"
    "v_pk_mul_f16 v64, v52, v54 op_sel_hi:[0,1]\n"
    "v_mfma_f32_16x16x32_f16 v[84:87], v[180:183], v[68:71], v[84:87]\n"
    "ds_read_b64_tr_b16 v[44:45], v5 offset:0\n"
    "v_pk_mul_f16 v65, v52, v55 op_sel_hi:[0,1]\n"
    "v_mfma_f32_16x16x32_f16 v[84:87], v[192:195], v[72:75], v[84:87]\n"
    "ds_read_b64_tr_b16 v[46:47], v6 offset:0\n"
    "v_pk_mul_f16 v66, v52, v52 op_sel_hi:[0,1]\n"
    "v_smfmac_f32_16x16x64_f16 v[88:91], v[196:199], v[68:75], v2\n"
    "v_pk_mul_f16 v67, v52, v49 op_sel_hi:[0,1]\n"
    "v_mfma_f32_16x16x32_f16 v[76:79], v[200:203], v[60:63], v[76:79]\n"
    "v_pk_mul_f16 v68, v48, v52 op_sel_hi:[0,1]\n"
    "v_pk_mul_f16 v69, v48, v53 op_sel_hi:[0,1]\n"
    "v_mfma_f32_16x16x32_f16 v[76:79], v[212:215], v[64:67], v[76:79]\n"
    "v_pk_mul_f16 v70, v50, v52 op_sel_hi:[0,1]\n"
    "v_mfma_f32_16x16x32_f16 v[80:83], v[216:219], v[64:67], v[80:83]\n"
    "s_cbranch_vccnz .Lxd_pr21\n"
    "s_setprio 0\n"
    ".Lxd_pr21:\n"
    "v_pk_mul_f16 v71, v50, v53 op_sel_hi:[0,1]\n"
    "v_mfma_f32_16x16x32_f16 v[80:83], v[204:207], v[60:63], v[80:83]\n"
    "v_pk_mul_f16 v72, v50, v50 op_sel_hi:[0,1]\n"
    "v_mfma_f32_16x16x32_f16 v[84:87], v[208:211], v[60:63], v[84:87]\n"
    "v_pk_mul_f16 v73, v50, v49 op_sel_hi:[0,1]\n"
    "v_pk_mul_f16 v74, v48, v50 op_sel_hi:[0,1]\n"
    "v_mfma_f32_16x16x32_f16 v[84:87], v[220:223], v[64:67], v[84:87]\n"
    "v_pk_mul_f16 v75, v48, v51 op_sel_hi:[0,1]\n"
    "v_smfmac_f32_16x16x64_f16 v[88:91], v[224:227], v[60:67], v2\n"
    "ds_read_b64_tr_b16 v[48:49], v3 offset:0\n"
    "ds_read_b64_tr_b16 v[58:59], v4 offset:2880\n"
    "v_smfmac_f32_16x16x64_f16 v[88:91], v[252:255], v[68:75], v2\n"
    "s_waitcnt lgkmcnt(2)\n"
    "v_pk_mul_f16 v60, v44, s45 op_sel:[0,1]\n"
    "v_pk_mul_f16 v61, v45, s45 op_sel:[0,1]\n"
    "v_mfma_f32_16x16x32_f16 v[84:87], v[236:239], v[68:71], v[84:87]\n"
    "ds_read_b64_tr_b16 v[50:51], v4 offset:576\n"
    "v_pk_mul_f16 v62, v46, s45 op_sel:[0,1]\n"
    "v_pk_mul_f16 v63, v47, s45 op_sel:[0,1]\n"
    "v_mfma_f32_16x16x32_f16 v[84:87], v[248:251], v[72:75], v[84:87]\n"
    "ds_read_b64_tr_b16 v[52:53], v4 offset:1152\n"
    "v_pk_mul_f16 v64, v44, v46\n"
    "v_pk_mul_f16 v65, v45, v47\n"
    "v_mfma_f32_16x16x32_f16 v[76:79], v[240:243], v[72:75], v[76:79]\n"
    "ds_read_b64_tr_b16 v[54:55], v4 offset:1728\n"
    "v_add_f32 v11, v88, v89\n"
    "v_add_f32 v13, v90, v91\n"
    "v_mfma_f32_16x16x32_f16 v[76:79], v[228:231], v[68:71], v[76:79]\n"
    "ds_read_b64_tr_b16 v[56:57], v4 offset:2304\n"
    "v_mov_b64 v[88:89], 0\n"
    "v_mov_b64 v[90:91], 0\n"
    "v_mfma_f32_16x16x32_f16 v[80:83], v[232:235], v[68:71], v[80:83]\n"
    "s_waitcnt lgkmcnt(4)\n"
    "v_pk_mul_f16 v66, v48, v48 op_sel_hi:[0,1]\n"
    "v_pk_mul_f16 v67, v48, v49 op_sel_hi:[0,1]\n"
    "v_mfma_f32_16x16x32_f16 v[80:83], v[244:247], v[72:75], v[80:83]\n"
    "v_mul_f32 v11, v11, v13\n"
    "v_fmac_f32 v11, v84, v85\n"
    "v_fmac_f32 v11, v86, v87\n"
    ".Lxd_ph2:\n"
    "v_smfmac_f32_16x16x64_f16 v[88:91], v[112:115], v[60:67], v2\n"
    "s_cbranch_vccnz .Lxd_pr24\n"
    "s_setprio 2\n"
    ".Lxd_pr24:\n"
    "v_pk_mul_f16 v68, v58, v58 op_sel_hi:[0,1]\n"
    "v_pk_mul_f16 v69, v58, v49 op_sel_hi:[0,1]\n"
    "v_mfma_f32_16x16x32_f16 v[84:87], v[108:111], v[64:67], 0\n"
    "v_fmac_f32 v11, v76, v80\n"
    "v_fmac_f32 v11, v77, v81\n"
    "v_mfma_f32_16x16x32_f16 v[84:87], v[96:99], v[60:63], v[84:87]\n"
    "s_movk_i32 s43, 15744\n"
    "v_fmac_f32 v11, v78, v82\n"
    "v_fma_f32 v8, v79, v83, v11\n"
    "v_mfma_f32_16x16x32_f16 v[80:83], v[92:95], v[60:63], 0\n"
    "s_waitcnt lgkmcnt(0)\n"
    "v_pk_mul_f16 v70, v48, v58 op_sel_hi:[0,1]\n"
    "v_pk_mul_f16 v71, v48, v59 op_sel_hi:[0,1]\n"
    "v_mfma_f32_16x16x32_f16 v[80:83], v[104:107], v[64:67], v[80:83]\n"
    "s_waitcnt vmcnt(0)\n"
    "v_pk_mul_f16 v72, v50, v58 op_sel_hi:[0,1]\n"
    "v_pk_mul_f16 v73, v50, v59 op_sel_hi:[0,1]\n"
    "v_mfma_f32_16x16x32_f16 v[76:79], v[100:103], v[64:67], 0\n"
    "s_cmp_lg_u32 s46, 0\n"
    "v_pk_mul_f16 v74, v52, v58 op_sel_hi:[0,1]\n"
    "v_pk_mul_f16 v75, v52, v59 op_sel_hi:[0,1]\n"
    "v_mfma_f32_16x16x32_f16 v[76:79], v[116:119], v[68:71], v[76:79]\n"
    "s_cbranch_scc1 .Lxd_ht26\n"
    ".Lxd_ht26r:\n"
    "v_pk_mul_f16 v60, v54, v58 op_sel_hi:[0,1]\n"
    "v_pk_mul_f16 v61, v54, v59 op_sel_hi:[0,1]\n"
    "v_mfma_f32_16x16x32_f16 v[76:79], v[128:131], v[72:75], v[76:79]\n"
    "v_pk_mul_f16 v62, v56, v58 op_sel_hi:[0,1]\n"
    "v_pk_maximum3_f16 v21, v28, v28, s43 op_sel_hi:[1,1,0] neg_lo:[0,1,0] neg_hi:[0,1,0]\n"
    "v_pk_maximum3_f16 v21, v29, v29, v21 neg_lo:[0,1,0] neg_hi:[0,1,0]\n"
    "v_mfma_f32_16x16x32_f16 v[80:83], v[132:135], v[72:75], v[80:83]\n"
    "v_pk_mul_f16 v63, v56, v59 op_sel_hi:[0,1]\n"
    "v_pk_maximum3_f16 v21, v30, v30, v21 neg_lo:[0,1,0] neg_hi:[0,1,0]\n"
    "v_mfma_f32_16x16x32_f16 v[80:83], v[120:123], v[68:71], v[80:83]\n"
    "v_pk_mul_f16 v64, v56, v56 op_sel_hi:[0,1]\n"
    "v_pk_maximum3_f16 v21, v31, v31, v21 neg_lo:[0,1,0] neg_hi:[0,1,0]\n"
    "v_mfma_f32_16x16x32_f16 v[84:87], v[124:127], v[68:71], v[84:87]\n"
    "v_pk_mul_f16 v65, v56, v49 op_sel_hi:[0,1]\n"
    "v_pk_maximum3_f16 v21, v32, v32, v21 neg_lo:[0,1,0] neg_hi:[0,1,0]\n"
    "v_mfma_f32_16x16x32_f16 v[84:87], v[136:139], v[72:75], v[84:87]\n"
    "v_pk_mul_f16 v66, v48, v56 op_sel_hi:[0,1]\n"
    "v_pk_maximum3_f16 v21, v33, v33, v21 neg_lo:[0,1,0] neg_hi:[0,1,0]\n"
    "v_smfmac_f32_16x16x64_f16 v[88:91], v[140:143], v[68:75], v2\n"
    "v_pk_mul_f16 v67, v48, v57 op_sel_hi:[0,1]\n"
    "v_pk_maximum3_f16 v21, v34, v34, v21 neg_lo:[0,1,0] neg_hi:[0,1,0]\n"
    "v_mfma_f32_16x16x32_f16 v[76:79], v[144:147], v[60:63], v[76:79]\n"
    "v_pk_mul_f16 v68, v50, v56 op_sel_hi:[0,1]\n"
    "v_pk_mul_f16 v69, v50, v57 op_sel_hi:[0,1]\n"
    "v_pk_maximum3_f16 v21, v35, v35, v21 neg_lo:[0,1,0] neg_hi:[0,1,0]\n"
    "v_mfma_f32_16x16x32_f16 v[76:79], v[156:159], v[64:67], v[76:79]\n"
    "v_pk_mul_f16 v70, v52, v56 op_sel_hi:[0,1]\n"
    "v_pk_maximum3_f16 v21, v36, v36, v21 neg_lo:[0,1,0] neg_hi:[0,1,0]\n"
    "v_mfma_f32_16x16x32_f16 v[80:83], v[160:163], v[64:67], v[80:83]\n"
    "v_pk_mul_f16 v71, v52, v57 op_sel_hi:[0,1]\n"
    "v_pk_maximum3_f16 v21, v37, v37, v21 neg_lo:[0,1,0] neg_hi:[0,1,0]\n"
    "v_mfma_f32_16x16x32_f16 v[80:83], v[148:151], v[60:63], v[80:83]\n"
    "ds_read_b32 v22, v25 offset:524\n"
    "v_pk_mul_f16 v72, v54, v56 op_sel_hi:[0,1]\n"
    "v_pk_maximum3_f16 v21, v38, v38, v21 neg_lo:[0,1,0] neg_hi:[0,1,0]\n"
    "v_mfma_f32_16x16x32_f16 v[84:87], v[152:155], v[60:63], v[84:87]\n"
    "v_pk_mul_f16 v73, v54, v57 op_sel_hi:[0,1]\n"
    "v_pk_maximum3_f16 v21, v39, v39, v21 neg_lo:[0,1,0] neg_hi:[0,1,0]\n"
    "v_mfma_f32_16x16x32_f16 v[84:87], v[164:167], v[64:67], v[84:87]\n"
    "v_pk_mul_f16 v74, v54, v54 op_sel_hi:[0,1]\n"
    "v_pk_maximum3_f16 v21, v40, v40, v21 neg_lo:[0,1,0] neg_hi:[0,1,0]\n"
    "v_smfmac_f32_16x16x64_f16 v[88:91], v[168:171], v[60:67], v2\n"
    "v_pk_mul_f16 v75, v54, v49 op_sel_hi:[0,1]\n"
    "v_pk_max_u16 v21, v21, v21 op_sel:[0,1] op_sel_hi:[1,0]\n"
    "v_mfma_f32_16x16x32_f16 v[76:79], v[172:175], v[68:71], v[76:79]\n"
    "v_pk_mul_f16 v60, v48, v54 op_sel_hi:[0,1]\n"
    "v_pk_mul_f16 v61, v48, v55 op_sel_hi:[0,1]\n"
    "v_mfma_f32_16x16x32_f16 v[76:79], v[184:187], v[72:75], v[76:79]\n"
    "s_waitcnt lgkmcnt(0)\n"
    "v_pk_mul_f16 v62, v50, v54 op_sel_hi:[0,1]\n"
    "v_readfirstlane_b32 s45, v22\n"
    "v_mfma_f32_16x16x32_f16 v[80:83], v[188:191], v[72:75], v[80:83]\n"
    "s_cmp_lt_i32 s45, 16\n"
    "v_pk_mul_f16 v63, v50, v55 op_sel_hi:[0,1]\n"
    "v_max_u32_dpp v21, v21, v21 quad_perm:[1,0,3,2] row_mask:0xf bank_mask:0xf\n"
    "v_mfma_f32_16x16x32_f16 v[80:83], v[176:179], v[68:71], v[80:83]\n"
    "s_cbranch_scc1 .Lxd_ck27\n"
    ".Lxd_ck27r:\n"
    "v_pk_mul_f16 v64, v52, v54 op_sel_hi:[0,1]\n"
    "v_max_u32_dpp v21, v21, v21 quad_perm:[2,3,0,1] row_mask:0xf bank_mask:0xf\n"
    "v_mfma_f32_16x16x32_f16 v[84:87], v[180:183], v[68:71], v[84:87]\n"
    "ds_read_b64_tr_b16 v[44:45], v5 offset:4096\n"
    "v_pk_mul_f16 v65, v52, v55 op_sel_hi:[0,1]\n"
    "v_max_u32_dpp v21, v21, v21 row_half_mirror row_mask:0xf bank_mask:0xf\n"
    "v_mfma_f32_16x16x32_f16 v[84:87], v[192:195], v[72:75], v[84:87]\n"
    "ds_read_b64_tr_b16 v[46:47], v6 offset:4096\n"
    "v_pk_mul_f16 v66, v52, v52 op_sel_hi:[0,1]\n"
    "v_max_u32_dpp v21, v21, v21 row_mirror row_mask:0xf bank_mask:0xf\n"
    "v_smfmac_f32_16x16x64_f16 v[88:91], v[196:199], v[68:75], v2\n"
    "v_pk_mul_f16 v67, v52, v49 op_sel_hi:[0,1]\n"
    "v_max_u32_dpp v21, v21, v21 row_bcast:15 row_mask:0xa bank_mask:0xf\n"
    "v_mfma_f32_16x16x32_f16 v[76:79], v[200:203], v[60:63], v[76:79]\n"
    "v_pk_mul_f16 v68, v48, v52 op_sel_hi:[0,1]\n"
    "v_pk_mul_f16 v69, v48, v53 op_sel_hi:[0,1]\n"
    "v_mfma_f32_16x16x32_f16 v[76:79], v[212:215], v[64:67], v[76:79]\n"
    "v_pk_mul_f16 v70, v50, v52 op_sel_hi:[0,1]\n"
    "v_max_u32_dpp v21, v21, v21 row_bcast:31 row_mask:0xc bank_mask:0xf\n"
    "v_mfma_f32_16x16x32_f16 v[80:83], v[216:219], v[64:67], v[80:83]\n"
    "s_cbranch_vccnz .Lxd_pr25\n"
    "s_setprio 0\n"
    ".Lxd_pr25:\n"
    "v_pk_mul_f16 v71, v50, v53 op_sel_hi:[0,1]\n"
    "v_readlane_b32 s47, v21, 63\n"
    "v_mfma_f32_16x16x32_f16 v[80:83], v[204:207], v[60:63], v[80:83]\n"
    "s_and_b32 s47, s47, 0x7f80\n"
    "v_pk_mul_f16 v72, v50, v50 op_sel_hi:[0,1]\n"
    "v_mfma_f32_16x16x32_f16 v[84:87], v[208:211], v[60:63], v[84:87]\n"
    "s_sub_u32 s50, s47, 2816\n"
    "v_pk_mul_f16 v73, v50, v49 op_sel_hi:[0,1]\n"
    "v_pk_mul_f16 v74, v48, v50 op_sel_hi:[0,1]\n"
    "v_pk_maximum3_f16 v21, v28, v28, s50 op_sel_hi:[1,1,0] neg_lo:[0,1,0] neg_hi:[0,1,0]\n"
    "v_mfma_f32_16x16x32_f16 v[84:87], v[220:223], v[64:67], v[84:87]\n"
    "s_mul_i32 s51, s50, -8\n"
    "v_pk_mul_f16 v75, v48, v51 op_sel_hi:[0,1]\n"
    "v_pk_mad_u16 v21, v21, 8, s51 op_sel_hi:[1,0,0]\n"
    "v_smfmac_f32_16x16x64_f16 v[88:91], v[224:227], v[60:67], v2\n"
    "ds_read_b64_tr_b16 v[48:49], v3 offset:4096\n"
    "ds_read_b64_tr_b16 v[58:59], v4 offset:6976\n"
    "v_bfi_b32 v28, s57, v21, v28\n"
    "v_smfmac_f32_16x16x64_f16 v[88:91], v[252:255], v[68:75], v2\n"
    "s_waitcnt lgkmcnt(2)\n"
    "v_pk_mul_f16 v60, v44, s45 op_sel:[0,1]\n"
    "v_pk_mul_f16 v61, v45, s45 op_sel:[0,1]\n"
    "v_mfma_f32_16x16x32_f16 v[84:87], v[236:239], v[68:71], v[84:87]\n"
    "ds_read_b64_tr_b16 v[50:51], v4 offset:4672\n"
    "v_pk_mul_f16 v62, v46, s45 op_sel:[0,1]\n"
    "v_pk_mul_f16 v63, v47, s45 op_sel:[0,1]\n"
    "v_mfma_f32_16x16x32_f16 v[84:87], v[248:251], v[72:75], v[84:87]\n"
    "ds_read_b64_tr_b16 v[52:53], v4 offset:5248\n"
    "v_pk_mul_f16 v64, v44, v46\n"
    "v_pk_mul_f16 v65, v45, v47\n"
    "v_mfma_f32_16x16x32_f16 v[76:79], v[240:243], v[72:75], v[76:79]\n"
    "ds_read_b64_tr_b16 v[54:55], v4 offset:5824\n"
    "v_add_f32 v11, v88, v89\n"
    "v_add_f32 v13, v90, v91\n"
    "v_mfma_f32_16x16x32_f16 v[76:79], v[228:231], v[68:71], v[76:79]\n"
    "ds_read_b64_tr_b16 v[56:57], v4 offset:6400\n"
    "v_mov_b64 v[88:89], 0\n"
    "v_mov_b64 v[90:91], 0\n"
    "v_mfma_f32_16x16x32_f16 v[80:83], v[232:235], v[68:71], v[80:83]\n"
    "s_waitcnt lgkmcnt(4)\n"
    "v_pk_mul_f16 v66, v48, v48 op_sel_hi:[0,1]\n"
    "v_pk_mul_f16 v67, v48, v49 op_sel_hi:[0,1]\n"
    "v_mfma_f32_16x16x32_f16 v[80:83], v[244:247], v[72:75], v[80:83]\n"
    "s_lshr_b32 s47, s47, 3\n"
    "v_mul_f32 v11, v11, v13\n"
    "v_fmac_f32 v11, v84, v85\n"
    "v_fmac_f32 v11, v86, v87\n"
    ".Lxd_ph3:\n"
    "v_smfmac_f32_16x16x64_f16 v[88:91], v[112:115], v[60:67], v2\n"
    "s_cbranch_vccnz .Lxd_pr28\n"
    "s_setprio 2\n"
    ".Lxd_pr28:\n"
    "v_pk_mul_f16 v68, v58, v58 op_sel_hi:[0,1]\n"
    "v_pk_mul_f16 v69, v58, v49 op_sel_hi:[0,1]\n"
    "v_mfma_f32_16x16x32_f16 v[84:87], v[108:111], v[64:67], 0\n"
    "s_cmp_lt_u32 s55, 32\n"
    "v_fmac_f32 v11, v76, v80\n"
    "v_fmac_f32 v11, v77, v81\n"
    "v_pk_maximum3_f16 v12, v29, v29, s50 op_sel_hi:[1,1,0] neg_lo:[0,1,0] neg_hi:[0,1,0]\n"
    "v_mfma_f32_16x16x32_f16 v[84:87], v[96:99], v[60:63], v[84:87]\n"
    "s_cselect_b32 s44, 0, -1\n"
    "v_fmac_f32 v11, v78, v82\n"
    "v_fma_f32 v9, v79, v83, v11\n"
    "v_mfma_f32_16x16x32_f16 v[80:83], v[92:95], v[60:63], 0\n"
    "s_waitcnt lgkmcnt(0)\n"
    "v_pk_mul_f16 v70, v48, v58 op_sel_hi:[0,1]\n"
    "v_pk_mul_f16 v71, v48, v59 op_sel_hi:[0,1]\n"
    "v_pk_mad_u16 v12, v12, 8, s51 op_sel_hi:[1,0,0]\n"
    "v_mfma_f32_16x16x32_f16 v[80:83], v[104:107], v[64:67], v[80:83]\n"
    "s_add_u32 s55, s55, 4\n"
    "v_pk_mul_f16 v72, v50, v58 op_sel_hi:[0,1]\n"
    "v_pk_mul_f16 v73, v50, v59 op_sel_hi:[0,1]\n"
    "v_mfma_f32_16x16x32_f16 v[76:79], v[100:103], v[64:67], 0\n"
    "v_pk_mul_f16 v74, v52, v58 op_sel_hi:[0,1]\n"
    "v_pk_mul_f16 v75, v52, v59 op_sel_hi:[0,1]\n"
    "v_bfi_b32 v29, s57, v12, v29\n"
    "v_mfma_f32_16x16x32_f16 v[76:79], v[116:119], v[68:71], v[76:79]\n"
    "v_pk_mul_f16 v60, v54, v58 op_sel_hi:[0,1]\n"
    "v_pk_mul_f16 v61, v54, v59 op_sel_hi:[0,1]\n"
    "v_pk_maximum3_f16 v21, v30, v30, s50 op_sel_hi:[1,1,0] neg_lo:[0,1,0] neg_hi:[0,1,0]\n"
    "v_mfma_f32_16x16x32_f16 v[76:79], v[128:131], v[72:75], v[76:79]\n"
    "v_pk_mul_f16 v62, v56, v58 op_sel_hi:[0,1]\n"
    "v_add_u32_e64 v24, s44, 1\n"
    "v_mfma_f32_16x16x32_f16 v[80:83], v[132:135], v[72:75], v[80:83]\n"
    "s_sub_u32 s66, 4112, s47\n"
    "v_pk_mul_f16 v63, v56, v59 op_sel_hi:[0,1]\n"
    "v_pk_mad_u16 v21, v21, 8, s51 op_sel_hi:[1,0,0]\n"
    "v_bfi_b32 v30, s57, v21, v30\n"
    "v_mfma_f32_16x16x32_f16 v[80:83], v[120:123], v[68:71], v[80:83]\n"
    "s_lshl_b32 s66, s66, 19\n"
    "v_pk_mul_f16 v64, v56, v56 op_sel_hi:[0,1]\n"
    "v_pk_maximum3_f16 v12, v31, v31, s50 op_sel_hi:[1,1,0] neg_lo:[0,1,0] neg_hi:[0,1,0]\n"
    "v_pk_mad_u16 v12, v12, 8, s51 op_sel_hi:[1,0,0]\n"
    "v_mfma_f32_16x16x32_f16 v[84:87], v[124:127], v[68:71], v[84:87]\n"
    "v_pk_mul_f16 v65, v56, v49 op_sel_hi:[0,1]\n"
    "v_bfi_b32 v31, s57, v12, v31\n"
    "v_mfma_f32_16x16x32_f16 v[84:87], v[136:139], v[72:75], v[84:87]\n"
    "ds_write_b128 v15, v[28:31] offset:0\n"
    "v_pk_mul_f16 v66, v48, v56 op_sel_hi:[0,1]\n"
    "v_pk_maximum3_f16 v21, v32, v32, s50 op_sel_hi:[1,1,0] neg_lo:[0,1,0] neg_hi:[0,1,0]\n"
    "v_pk_mad_u16 v21, v21, 8, s51 op_sel_hi:[1,0,0]\n"
    "v_smfmac_f32_16x16x64_f16 v[88:91], v[140:143], v[68:75], v2\n"
    "v_pk_mul_f16 v67, v48, v57 op_sel_hi:[0,1]\n"
    "v_bfi_b32 v32, s57, v21, v32\n"
    "v_mfma_f32_16x16x32_f16 v[76:79], v[144:147], v[60:63], v[76:79]\n"
    "v_pk_mul_f16 v68, v50, v56 op_sel_hi:[0,1]\n"
    "v_pk_mul_f16 v69, v50, v57 op_sel_hi:[0,1]\n"
    "v_pk_maximum3_f16 v12, v33, v33, s50 op_sel_hi:[1,1,0] neg_lo:[0,1,0] neg_hi:[0,1,0]\n"
    "v_mfma_f32_16x16x32_f16 v[76:79], v[156:159], v[64:67], v[76:79]\n"
    "v_pk_mul_f16 v70, v52, v56 op_sel_hi:[0,1]\n"
    "v_pk_mad_u16 v12, v12, 8, s51 op_sel_hi:[1,0,0]\n"
    "v_bfi_b32 v33, s57, v12, v33\n"
    "v_mfma_f32_16x16x32_f16 v[80:83], v[160:163], v[64:67], v[80:83]\n"
    "v_pk_mul_f16 v71, v52, v57 op_sel_hi:[0,1]\n"
    "v_pk_maximum3_f16 v21, v34, v34, s50 op_sel_hi:[1,1,0] neg_lo:[0,1,0] neg_hi:[0,1,0]\n"
    "v_mfma_f32_16x16x32_f16 v[80:83], v[148:151], v[60:63], v[80:83]\n"
    "ds_read_b32 v22, v25 offset:528\n"
    "v_pk_mul_f16 v72, v54, v56 op_sel_hi:[0,1]\n"
    "v_pk_mad_u16 v21, v21, 8, s51 op_sel_hi:[1,0,0]\n"
    "v_bfi_b32 v34, s57, v21, v34\n"
    "v_mfma_f32_16x16x32_f16 v[84:87], v[152:155], v[60:63], v[84:87]\n"
    "v_pk_mul_f16 v73, v54, v57 op_sel_hi:[0,1]\n"
    "v_pk_maximum3_f16 v12, v35, v35, s50 op_sel_hi:[1,1,0] neg_lo:[0,1,0] neg_hi:[0,1,0]\n"
    "v_pk_mad_u16 v12, v12, 8, s51 op_sel_hi:[1,0,0]\n"
    "v_mfma_f32_16x16x32_f16 v[84:87], v[164:167], v[64:67], v[84:87]\n"
    "v_pk_mul_f16 v74, v54, v54 op_sel_hi:[0,1]\n"
    "v_bfi_b32 v35, s57, v12, v35\n"
    "v_smfmac_f32_16x16x64_f16 v[88:91], v[168:171], v[60:67], v2\n"
    "ds_write_b128 v15, v[32:35] offset:144\n"
    "v_pk_mul_f16 v75, v54, v49 op_sel_hi:[0,1]\n"
    "v_pk_maximum3_f16 v21, v36, v36, s50 op_sel_hi:[1,1,0] neg_lo:[0,1,0] neg_hi:[0,1,0]\n"
    "v_pk_mad_u16 v21, v21, 8, s51 op_sel_hi:[1,0,0]\n"
    "v_mfma_f32_16x16x32_f16 v[76:79], v[172:175], v[68:71], v[76:79]\n"
    "v_pk_mul_f16 v60, v48, v54 op_sel_hi:[0,1]\n"
    "v_pk_mul_f16 v61, v48, v55 op_sel_hi:[0,1]\n"
    "v_mfma_f32_16x16x32_f16 v[76:79], v[184:187], v[72:75], v[76:79]\n"
    "s_waitcnt lgkmcnt(1)\n"
    "v_pk_mul_f16 v62, v50, v54 op_sel_hi:[0,1]\n"
    "v_readfirstlane_b32 s45, v22\n"
    "v_bfi_b32 v36, s57, v21, v36\n"
    "v_mfma_f32_16x16x32_f16 v[80:83], v[188:191], v[72:75], v[80:83]\n"
    "s_cmp_lt_i32 s45, 16\n"
    "v_pk_mul_f16 v63, v50, v55 op_sel_hi:[0,1]\n"
    "v_pk_maximum3_f16 v12, v37, v37, s50 op_sel_hi:[1,1,0] neg_lo:[0,1,0] neg_hi:[0,1,0]\n"
    "v_pk_mad_u16 v12, v12, 8, s51 op_sel_hi:[1,0,0]\n"
    "v_mfma_f32_16x16x32_f16 v[80:83], v[176:179], v[68:71], v[80:83]\n"
    "s_cbranch_scc1 .Lxd_ck31\n"
    ".Lxd_ck31r:\n"
    "v_pk_mul_f16 v64, v52, v54 op_sel_hi:[0,1]\n"
    "v_bfi_b32 v37, s57, v12, v37\n"
    "v_mfma_f32_16x16x32_f16 v[84:87], v[180:183], v[68:71], v[84:87]\n"
    "ds_read_b64_tr_b16 v[44:45], v5 offset:8192\n"
    "v_pk_mul_f16 v65, v52, v55 op_sel_hi:[0,1]\n"
    "v_pk_maximum3_f16 v21, v38, v38, s50 op_sel_hi:[1,1,0] neg_lo:[0,1,0] neg_hi:[0,1,0]\n"
    "v_pk_mad_u16 v21, v21, 8, s51 op_sel_hi:[1,0,0]\n"
    "v_mfma_f32_16x16x32_f16 v[84:87], v[192:195], v[72:75], v[84:87]\n"
    "ds_read_b64_tr_b16 v[46:47], v6 offset:8192\n"
    "v_pk_mul_f16 v66, v52, v52 op_sel_hi:[0,1]\n"
    "v_bfi_b32 v38, s57, v21, v38\n"
    "v_pk_maximum3_f16 v12, v39, v39, s50 op_sel_hi:[1,1,0] neg_lo:[0,1,0] neg_hi:[0,1,0]\n"
    "v_smfmac_f32_16x16x64_f16 v[88:91], v[196:199], v[68:75], v2\n"
    "v_pk_mul_f16 v67, v52, v49 op_sel_hi:[0,1]\n"
    "v_pk_mad_u16 v12, v12, 8, s51 op_sel_hi:[1,0,0]\n"
    "v_mfma_f32_16x16x32_f16 v[76:79], v[200:203], v[60:63], v[76:79]\n"
    "s_and_b32 s40, s45, 15\n"
    "v_pk_mul_f16 v68, v48, v52 op_sel_hi:[0,1]\n"
    "v_pk_mul_f16 v69, v48, v53 op_sel_hi:[0,1]\n"
    "v_bfi_b32 v39, s57, v12, v39\n"
    "v_mfma_f32_16x16x32_f16 v[76:79], v[212:215], v[64:67], v[76:79]\n"
    "s_cbranch_scc1 .Lxd_dr30\n"
    "s_mov_b64 exec, 1\n"
    "global_atomic_add v24, v17, v24, s[52:53] sc0 sc1\n"
    "s_mov_b64 exec, -1\n"
    ".Lxd_dr30r:\n"
    "v_pk_mul_f16 v70, v50, v52 op_sel_hi:[0,1]\n"
    "v_pk_maximum3_f16 v21, v40, v40, s50 op_sel_hi:[1,1,0] neg_lo:[0,1,0] neg_hi:[0,1,0]\n"
    "v_mfma_f32_16x16x32_f16 v[80:83], v[216:219], v[64:67], v[80:83]\n"
    "s_cbranch_vccnz .Lxd_pr29\n"
    "s_setprio 0\n"
    ".Lxd_pr29:\n"
    "v_pk_mul_f16 v71, v50, v53 op_sel_hi:[0,1]\n"
    "v_pk_mad_u16 v21, v21, 8, s51 op_sel_hi:[1,0,0]\n"
    "v_bfi_b32 v40, s57, v21, v40\n"
    "v_mfma_f32_16x16x32_f16 v[80:83], v[204:207], v[60:63], v[80:83]\n"
    "ds_write_b128 v15, v[36:39] offset:288\n"
    "v_pk_mul_f16 v72, v50, v50 op_sel_hi:[0,1]\n"
    "v_cvt_pkrtz_f16_f32 v12, s66, s66\n"
    "v_lshl_or_b32 v21, v12, 16, s47\n"
    "v_mfma_f32_16x16x32_f16 v[84:87], v[208:211], v[60:63], v[84:87]\n"
    "ds_write2_b32 v16, v40, v12 offset1:72\n"
    "v_pk_mul_f16 v73, v50, v49 op_sel_hi:[0,1]\n"
    "v_pk_mul_f16 v74, v48, v50 op_sel_hi:[0,1]\n"
    "v_mfma_f32_16x16x32_f16 v[84:87], v[220:223], v[64:67], v[84:87]\n"
    "ds_or_b32 v25, v21 offset:532\n"
    "v_pk_mul_f16 v75, v48, v51 op_sel_hi:[0,1]\n"
    "v_smfmac_f32_16x16x64_f16 v[88:91], v[224:227], v[60:67], v2\n"
    "ds_read_b64_tr_b16 v[48:49], v3 offset:8192\n"
    "ds_read_b64_tr_b16 v[58:59], v4 offset:11072\n"
    "v_add_u32 v25, 16, v25\n"
    "v_smfmac_f32_16x16x64_f16 v[88:91], v[252:255], v[68:75], v2\n"
    "s_waitcnt lgkmcnt(5)\n"
    "v_pk_mul_f16 v60, v44, s45 op_sel:[0,1]\n"
    "v_pk_mul_f16 v61, v45, s45 op_sel:[0,1]\n"
    "v_mfma_f32_16x16x32_f16 v[84:87], v[236:239], v[68:71], v[84:87]\n"
    "ds_read_b64_tr_b16 v[50:51], v4 offset:8768\n"
    "v_pk_mul_f16 v62, v46, s45 op_sel:[0,1]\n"
    "v_pk_mul_f16 v63, v47, s45 op_sel:[0,1]\n"
    "v_mfma_f32_16x16x32_f16 v[84:87], v[248:251], v[72:75], v[84:87]\n"
    "ds_read_b64_tr_b16 v[52:53], v4 offset:9344\n"
    "v_pk_mul_f16 v64, v44, v46\n"
    "v_pk_mul_f16 v65, v45, v47\n"
    "v_mfma_f32_16x16x32_f16 v[76:79], v[240:243], v[72:75], v[76:79]\n"
    "ds_read_b64_tr_b16 v[54:55], v4 offset:9920\n"
    "v_add_f32 v11, v88, v89\n"
    "v_add_f32 v13, v90, v91\n"
    "v_mfma_f32_16x16x32_f16 v[76:79], v[228:231], v[68:71], v[76:79]\n"
    "ds_read_b64_tr_b16 v[56:57], v4 offset:10496\n"
    "v_mov_b64 v[88:89], 0\n"
    "v_mov_b64 v[90:91], 0\n"
    "v_mfma_f32_16x16x32_f16 v[80:83], v[232:235], v[68:71], v[80:83]\n"
    "s_waitcnt lgkmcnt(4)\n"
    "v_pk_mul_f16 v66, v48, v48 op_sel_hi:[0,1]\n"
    "v_pk_mul_f16 v67, v48, v49 op_sel_hi:[0,1]\n"
    "v_mfma_f32_16x16x32_f16 v[80:83], v[244:247], v[72:75], v[80:83]\n"
    "s_cmp_eq_u32 s36, 0\n"
    "v_mul_f32 v11, v11, v13\n"
    "v_fmac_f32 v11, v84, v85\n"
    "v_fmac_f32 v11, v86, v87\n"
    "s_cbranch_scc1 .Lxd_ph0\n"
    ".Lxd_pl0:\n"
    "v_smfmac_f32_16x16x64_f16 v[88:91], v[112:115], v[60:67], v2\n"
    "s_cbranch_vccnz .Lxd_pr32\n"
    "s_setprio 2\n"
    ".Lxd_pr32:\n"
    "v_pk_mul_f16 v68, v58, v58 op_sel_hi:[0,1]\n"
    "v_pk_mul_f16 v69, v58, v49 op_sel_hi:[0,1]\n"
    "v_mfma_f32_16x16x32_f16 v[84:87], v[108:111], v[64:67], 0\n"
    "v_fmac_f32 v11, v76, v80\n"
    "v_fmac_f32 v11, v77, v81\n"
    "v_mfma_f32_16x16x32_f16 v[84:87], v[96:99], v[60:63], v[84:87]\n"
    "v_fmac_f32 v11, v78, v82\n"
    "v_fma_f32 v10, v79, v83, v11\n"
    "v_mfma_f32_16x16x32_f16 v[80:83], v[92:95], v[60:63], 0\n"
    "s_waitcnt lgkmcnt(0)\n"
    "v_pk_mul_f16 v70, v48, v58 op_sel_hi:[0,1]\n"
    "v_pk_mul_f16 v71, v48, v59 op_sel_hi:[0,1]\n"
    "v_mfma_f32_16x16x32_f16 v[80:83], v[104:107], v[64:67], v[80:83]\n"
    "v_pk_mul_f16 v72, v50, v58 op_sel_hi:[0,1]\n"
    "v_pk_mul_f16 v73, v50, v59 op_sel_hi:[0,1]\n"
    "v_mfma_f32_16x16x32_f16 v[76:79], v[100:103], v[64:67], 0\n"
    "v_pk_mul_f16 v74, v52, v58 op_sel_hi:[0,1]\n"
    "v_pk_mul_f16 v75, v52, v59 op_sel_hi:[0,1]\n"
    "v_mfma_f32_16x16x32_f16 v[76:79], v[116:119], v[68:71], v[76:79]\n"
    "v_pk_mul_f16 v60, v54, v58 op_sel_hi:[0,1]\n"
    "v_pk_mul_f16 v61, v54, v59 op_sel_hi:[0,1]\n"
    "v_mfma_f32_16x16x32_f16 v[76:79], v[128:131], v[72:75], v[76:79]\n"
    "v_pk_mul_f16 v62, v56, v58 op_sel_hi:[0,1]\n"
    "v_permlane32_swap_b32 v7, v8\n"
    "v_mfma_f32_16x16x32_f16 v[80:83], v[132:135], v[72:75], v[80:83]\n"
    "v_pk_mul_f16 v63, v56, v59 op_sel_hi:[0,1]\n"
    "v_permlane32_swap_b32 v9, v10\n"
    "v_mfma_f32_16x16x32_f16 v[80:83], v[120:123], v[68:71], v[80:83]\n"
    "v_pk_mul_f16 v64, v56, v56 op_sel_hi:[0,1]\n"
    "v_mul_f32 v7, v0, v7\n"
    "v_mfma_f32_16x16x32_f16 v[84:87], v[124:127], v[68:71], v[84:87]\n"
    "v_pk_mul_f16 v65, v56, v49 op_sel_hi:[0,1]\n"
    "v_mul_f32 v9, v0, v9\n"
    "v_mfma_f32_16x16x32_f16 v[84:87], v[136:139], v[72:75], v[84:87]\n"
    "v_pk_mul_f16 v66, v48, v56 op_sel_hi:[0,1]\n"
    "v_fmac_f32 v7, v23, v8\n"
    "v_smfmac_f32_16x16x64_f16 v[88:91], v[140:143], v[68:75], v2\n"
    "v_pk_mul_f16 v67, v48, v57 op_sel_hi:[0,1]\n"
    "v_fmac_f32 v9, v23, v10\n"
    "v_mfma_f32_16x16x32_f16 v[76:79], v[144:147], v[60:63], v[76:79]\n"
    "v_pk_mul_f16 v68, v50, v56 op_sel_hi:[0,1]\n"
    "v_pk_mul_f16 v69, v50, v57 op_sel_hi:[0,1]\n"
    "v_mfma_f32_16x16x32_f16 v[76:79], v[156:159], v[64:67], v[76:79]\n"
    "v_pk_mul_f16 v70, v52, v56 op_sel_hi:[0,1]\n"
    "v_permlane16_swap_b32 v7, v9\n"
    "v_mfma_f32_16x16x32_f16 v[80:83], v[160:163], v[64:67], v[80:83]\n"
    "v_pk_mul_f16 v71, v52, v57 op_sel_hi:[0,1]\n"
    "v_add_f32 v7, v7, v9\n"
    "v_mfma_f32_16x16x32_f16 v[80:83], v[148:151], v[60:63], v[80:83]\n"
    "ds_read_b32 v22, v25 offset:516\n"
    "v_pk_mul_f16 v72, v54, v56 op_sel_hi:[0,1]\n"
    "v_add_f32_dpp v7, v7, v7 quad_perm:[1,0,3,2] row_mask:0xf bank_mask:0xf\n"
    "v_mfma_f32_16x16x32_f16 v[84:87], v[152:155], v[60:63], v[84:87]\n"
    "v_pk_mul_f16 v73, v54, v57 op_sel_hi:[0,1]\n"
    "v_add_f32_dpp v7, v7, v7 quad_perm:[2,3,0,1] row_mask:0xf bank_mask:0xf\n"
    "v_mfma_f32_16x16x32_f16 v[84:87], v[164:167], v[64:67], v[84:87]\n"
    "v_pk_mul_f16 v74, v54, v54 op_sel_hi:[0,1]\n"
    "v_add_f32_dpp v7, v7, v7 row_half_mirror row_mask:0xf bank_mask:0xf\n"
    "v_smfmac_f32_16x16x64_f16 v[88:91], v[168:171], v[60:67], v2\n"
    "v_pk_mul_f16 v75, v54, v49 op_sel_hi:[0,1]\n"
    "v_add_f32_dpp v7, v7, v7 row_mirror row_mask:0xf bank_mask:0xf\n"
    "v_mfma_f32_16x16x32_f16 v[76:79], v[172:175], v[68:71], v[76:79]\n"
    "ds_write_b32 v14, v7\n"
    "v_pk_mul_f16 v60, v48, v54 op_sel_hi:[0,1]\n"
    "v_pk_mul_f16 v61, v48, v55 op_sel_hi:[0,1]\n"
    "v_mfma_f32_16x16x32_f16 v[76:79], v[184:187], v[72:75], v[76:79]\n"
    "s_waitcnt lgkmcnt(1)\n"
    "v_pk_mul_f16 v62, v50, v54 op_sel_hi:[0,1]\n"
    "v_readfirstlane_b32 s45, v22\n"
    "v_mfma_f32_16x16x32_f16 v[80:83], v[188:191], v[72:75], v[80:83]\n"
    "s_cmp_lt_i32 s45, 16\n"
    "v_pk_mul_f16 v63, v50, v55 op_sel_hi:[0,1]\n"
    "v_add_u32 v14, 64, v14\n"
    "v_mfma_f32_16x16x32_f16 v[80:83], v[176:179], v[68:71], v[80:83]\n"
    "s_cbranch_scc1 .Lxd_ck34\n"
    ".Lxd_ck34r:\n"
    "v_pk_mul_f16 v64, v52, v54 op_sel_hi:[0,1]\n"
    "v_mfma_f32_16x16x32_f16 v[84:87], v[180:183], v[68:71], v[84:87]\n"
    "ds_read_b64_tr_b16 v[44:45], v5 offset:12288\n"
    "v_pk_mul_f16 v65, v52, v55 op_sel_hi:[0,1]\n"
    "v_mfma_f32_16x16x32_f16 v[84:87], v[192:195], v[72:75], v[84:87]\n"
    "ds_read_b64_tr_b16 v[46:47], v6 offset:12288\n"
    "v_pk_mul_f16 v66, v52, v52 op_sel_hi:[0,1]\n"
    "v_smfmac_f32_16x16x64_f16 v[88:91], v[196:199], v[68:75], v2\n"
    "v_pk_mul_f16 v67, v52, v49 op_sel_hi:[0,1]\n"
    "v_mfma_f32_16x16x32_f16 v[76:79], v[200:203], v[60:63], v[76:79]\n"
    "v_pk_mul_f16 v68, v48, v52 op_sel_hi:[0,1]\n"
    "v_pk_mul_f16 v69, v48, v53 op_sel_hi:[0,1]\n"
    "v_mfma_f32_16x16x32_f16 v[76:79], v[212:215], v[64:67], v[76:79]\n"
    "v_pk_mul_f16 v70, v50, v52 op_sel_hi:[0,1]\n"
    "v_mfma_f32_16x16x32_f16 v[80:83], v[216:219], v[64:67], v[80:83]\n"
    "s_cbranch_vccnz .Lxd_pr33\n"
    "s_setprio 0\n"
    ".Lxd_pr33:\n"
    "v_pk_mul_f16 v71, v50, v53 op_sel_hi:[0,1]\n"
    "v_mfma_f32_16x16x32_f16 v[80:83], v[204:207], v[60:63], v[80:83]\n"
    "v_pk_mul_f16 v72, v50, v50 op_sel_hi:[0,1]\n"
    "v_mfma_f32_16x16x32_f16 v[84:87], v[208:211], v[60:63], v[84:87]\n"
    "v_pk_mul_f16 v73, v50, v49 op_sel_hi:[0,1]\n"
    "v_pk_mul_f16 v74, v48, v50 op_sel_hi:[0,1]\n"
    "v_mfma_f32_16x16x32_f16 v[84:87], v[220:223], v[64:67], v[84:87]\n"
    "v_pk_mul_f16 v75, v48, v51 op_sel_hi:[0,1]\n"
    "v_smfmac_f32_16x16x64_f16 v[88:91], v[224:227], v[60:67], v2\n"
    "ds_read_b64_tr_b16 v[48:49], v3 offset:12288\n"
    "ds_read_b64_tr_b16 v[58:59], v4 offset:15168\n"
    "v_smfmac_f32_16x16x64_f16 v[88:91], v[252:255], v[68:75], v2\n"
    "s_waitcnt lgkmcnt(2)\n"
    "v_pk_mul_f16 v60, v44, s45 op_sel:[0,1]\n"
    "v_pk_mul_f16 v61, v45, s45 op_sel:[0,1]\n"
    "v_mfma_f32_16x16x32_f16 v[84:87], v[236:239], v[68:71], v[84:87]\n"
    "ds_read_b64_tr_b16 v[50:51], v4 offset:12864\n"
    "v_pk_mul_f16 v62, v46, s45 op_sel:[0,1]\n"
    "v_pk_mul_f16 v63, v47, s45 op_sel:[0,1]\n"
    "v_mfma_f32_16x16x32_f16 v[84:87], v[248:251], v[72:75], v[84:87]\n"
    "ds_read_b64_tr_b16 v[52:53], v4 offset:13440\n"
    "v_pk_mul_f16 v64, v44, v46\n"
    "v_pk_mul_f16 v65, v45, v47\n"
    "v_mfma_f32_16x16x32_f16 v[76:79], v[240:243], v[72:75], v[76:79]\n"
    "ds_read_b64_tr_b16 v[54:55], v4 offset:14016\n"
    "v_add_f32 v11, v88, v89\n"
    "v_add_f32 v13, v90, v91\n"
    "v_mfma_f32_16x16x32_f16 v[76:79], v[228:231], v[68:71], v[76:79]\n"
    "ds_read_b64_tr_b16 v[56:57], v4 offset:14592\n"
    "v_mov_b64 v[88:89], 0\n"
    "v_mov_b64 v[90:91], 0\n"
    "v_mfma_f32_16x16x32_f16 v[80:83], v[232:235], v[68:71], v[80:83]\n"
    "s_waitcnt lgkmcnt(4)\n"
    "v_pk_mul_f16 v66, v48, v48 op_sel_hi:[0,1]\n"
    "v_pk_mul_f16 v67, v48, v49 op_sel_hi:[0,1]\n"
    "v_mfma_f32_16x16x32_f16 v[80:83], v[244:247], v[72:75], v[80:83]\n"
    "v_mul_f32 v11, v11, v13\n"
    "v_fmac_f32 v11, v84, v85\n"
    "v_fmac_f32 v11, v86, v87\n"
    ".Lxd_pl1:\n"
    "v_smfmac_f32_16x16x64_f16 v[88:91], v[112:115], v[60:67], v2\n"
    "s_cbranch_vccnz .Lxd_pr35\n"
    "s_setprio 2\n"
    ".Lxd_pr35:\n"
    "v_pk_mul_f16 v68, v58, v58 op_sel_hi:[0,1]\n"
    "v_pk_mul_f16 v69, v58, v49 op_sel_hi:[0,1]\n"
    "v_mfma_f32_16x16x32_f16 v[84:87], v[108:111], v[64:67], 0\n"
    "v_fmac_f32 v11, v76, v80\n"
    "v_fmac_f32 v11, v77, v81\n"
    "v_mfma_f32_16x16x32_f16 v[84:87], v[96:99], v[60:63], v[84:87]\n"
    "v_fmac_f32 v11, v78, v82\n"
    "v_fma_f32 v7, v79, v83, v11\n"
    "v_mfma_f32_16x16x32_f16 v[80:83], v[92:95], v[60:63], 0\n"
    "s_waitcnt lgkmcnt(0)\n"
    "v_pk_mul_f16 v70, v48, v58 op_sel_hi:[0,1]\n"
    "v_pk_mul_f16 v71, v48, v59 op_sel_hi:[0,1]\n"
    "v_mfma_f32_16x16x32_f16 v[80:83], v[104:107], v[64:67], v[80:83]\n"
    "v_pk_mul_f16 v72, v50, v58 op_sel_hi:[0,1]\n"
    "v_pk_mul_f16 v73, v50, v59 op_sel_hi:[0,1]\n"
    "v_mfma_f32_16x16x32_f16 v[76:79], v[100:103], v[64:67], 0\n"
    "s_cmp_lg_u32 s46, 0\n"
    "v_pk_mul_f16 v74, v52, v58 op_sel_hi:[0,1]\n"
    "v_pk_mul_f16 v75, v52, v59 op_sel_hi:[0,1]\n"
    "v_mfma_f32_16x16x32_f16 v[76:79], v[116:119], v[68:71], v[76:79]\n"
    "s_cbranch_scc1 .Lxd_ht37\n"
    ".Lxd_ht37r:\n"
    "v_pk_mul_f16 v60, v54, v58 op_sel_hi:[0,1]\n"
    "v_pk_mul_f16 v61, v54, v59 op_sel_hi:[0,1]\n"
    "v_mfma_f32_16x16x32_f16 v[76:79], v[128:131], v[72:75], v[76:79]\n"
    "v_pk_mul_f16 v62, v56, v58 op_sel_hi:[0,1]\n"
    "v_xor_b32 v3, 0x4000, v3\n"
    "v_mfma_f32_16x16x32_f16 v[80:83], v[132:135], v[72:75], v[80:83]\n"
    "v_pk_mul_f16 v63, v56, v59 op_sel_hi:[0,1]\n"
    "v_xor_b32 v4, 0x4000, v4\n"
    "v_mfma_f32_16x16x32_f16 v[80:83], v[120:123], v[68:71], v[80:83]\n"
    "v_pk_mul_f16 v64, v56, v56 op_sel_hi:[0,1]\n"
    "v_xor_b32 v5, 0x4000, v5\n"
    "v_mfma_f32_16x16x32_f16 v[84:87], v[124:127], v[68:71], v[84:87]\n"
    "v_pk_mul_f16 v65, v56, v49 op_sel_hi:[0,1]\n"
    "v_xor_b32 v6, 0x4000, v6\n"
    "v_mfma_f32_16x16x32_f16 v[84:87], v[136:139], v[72:75], v[84:87]\n"
    "v_pk_mul_f16 v66, v48, v56 op_sel_hi:[0,1]\n"
    "v_smfmac_f32_16x16x64_f16 v[88:91], v[140:143], v[68:75], v2\n"
    "v_pk_mul_f16 v67, v48, v57 op_sel_hi:[0,1]\n"
    "v_mfma_f32_16x16x32_f16 v[76:79], v[144:147], v[60:63], v[76:79]\n"
    "v_pk_mul_f16 v68, v50, v56 op_sel_hi:[0,1]\n"
    "v_pk_mul_f16 v69, v50, v57 op_sel_hi:[0,1]\n"
    "v_mfma_f32_16x16x32_f16 v[76:79], v[156:159], v[64:67], v[76:79]\n"
    "v_pk_mul_f16 v70, v52, v56 op_sel_hi:[0,1]\n"
    "v_mfma_f32_16x16x32_f16 v[80:83], v[160:163], v[64:67], v[80:83]\n"
    "v_pk_mul_f16 v71, v52, v57 op_sel_hi:[0,1]\n"
    "v_mfma_f32_16x16x32_f16 v[80:83], v[148:151], v[60:63], v[80:83]\n"
    "ds_read_b32 v22, v25 offset:520\n"
    "v_pk_mul_f16 v72, v54, v56 op_sel_hi:[0,1]\n"
    "v_mfma_f32_16x16x32_f16 v[84:87], v[152:155], v[60:63], v[84:87]\n"
    "v_pk_mul_f16 v73, v54, v57 op_sel_hi:[0,1]\n"
    "v_mfma_f32_16x16x32_f16 v[84:87], v[164:167], v[64:67], v[84:87]\n"
    "v_pk_mul_f16 v74, v54, v54 op_sel_hi:[0,1]\n"
    "v_smfmac_f32_16x16x64_f16 v[88:91], v[168:171], v[60:67], v2\n"
    "v_pk_mul_f16 v75, v54, v49 op_sel_hi:[0,1]\n"
    "v_mfma_f32_16x16x32_f16 v[76:79], v[172:175], v[68:71], v[76:79]\n"
    "v_pk_mul_f16 v60, v48, v54 op_sel_hi:[0,1]\n"
    "v_pk_mul_f16 v61, v48, v55 op_sel_hi:[0,1]\n"
    "v_mfma_f32_16x16x32_f16 v[76:79], v[184:187], v[72:75], v[76:79]\n"
    "s_waitcnt lgkmcnt(0)\n"
    "v_pk_mul_f16 v62, v50, v54 op_sel_hi:[0,1]\n"
    "v_readfirstlane_b32 s45, v22\n"
    "v_mfma_f32_16x16x32_f16 v[80:83], v[188:191], v[72:75], v[80:83]\n"
    "s_cmp_lt_i32 s45, 16\n"
    "v_pk_mul_f16 v63, v50, v55 op_sel_hi:[0,1]\n"
    "v_mfma_f32_16x16x32_f16 v[80:83], v[176:179], v[68:71], v[80:83]\n"
    "s_cbranch_scc1 .Lxd_ck38\n"
    ".Lxd_ck38r:\n"
    "v_pk_mul_f16 v64, v52, v54 op_sel_hi:[0,1]\n"
    "v_mfma_f32_16x16x32_f16 v[84:87], v[180:183], v[68:71], v[84:87]\n"
    "ds_read_b64_tr_b16 v[44:45], v5 offset:0\n"
    "v_pk_mul_f16 v65, v52, v55 op_sel_hi:[0,1]\n"
    "v_mfma_f32_16x16x32_f16 v[84:87], v[192:195], v[72:75], v[84:87]\n"
    "ds_read_b64_tr_b16 v[46:47], v6 offset:0\n"
    "v_pk_mul_f16 v66, v52, v52 op_sel_hi:[0,1]\n"
    "v_smfmac_f32_16x16x64_f16 v[88:91], v[196:199], v[68:75], v2\n"
    "v_pk_mul_f16 v67, v52, v49 op_sel_hi:[0,1]\n"
    "v_mfma_f32_16x16x32_f16 v[76:79], v[200:203], v[60:63], v[76:79]\n"
    "v_pk_mul_f16 v68, v48, v52 op_sel_hi:[0,1]\n"
    "v_pk_mul_f16 v69, v48, v53 op_sel_hi:[0,1]\n"
    "v_mfma_f32_16x16x32_f16 v[76:79], v[212:215], v[64:67], v[76:79]\n"
    "v_pk_mul_f16 v70, v50, v52 op_sel_hi:[0,1]\n"
    "v_mfma_f32_16x16x32_f16 v[80:83], v[216:219], v[64:67], v[80:83]\n"
    "s_cbranch_vccnz .Lxd_pr36\n"
    "s_setprio 0\n"
    ".Lxd_pr36:\n"
    "v_pk_mul_f16 v71, v50, v53 op_sel_hi:[0,1]\n"
    "v_mfma_f32_16x16x32_f16 v[80:83], v[204:207], v[60:63], v[80:83]\n"
    "v_pk_mul_f16 v72, v50, v50 op_sel_hi:[0,1]\n"
    "v_mfma_f32_16x16x32_f16 v[84:87], v[208:211], v[60:63], v[84:87]\n"
    "v_pk_mul_f16 v73, v50, v49 op_sel_hi:[0,1]\n"
    "v_pk_mul_f16 v74, v48, v50 op_sel_hi:[0,1]\n"
    "v_mfma_f32_16x16x32_f16 v[84:87], v[220:223], v[64:67], v[84:87]\n"
    "v_pk_mul_f16 v75, v48, v51 op_sel_hi:[0,1]\n"
    "v_smfmac_f32_16x16x64_f16 v[88:91], v[224:227], v[60:67], v2\n"
    "ds_read_b64_tr_b16 v[48:49], v3 offset:0\n"
    "ds_read_b64_tr_b16 v[58:59], v4 offset:2880\n"
    "v_smfmac_f32_16x16x64_f16 v[88:91], v[252:255], v[68:75], v2\n"
    "s_waitcnt lgkmcnt(2)\n"
    "v_pk_mul_f16 v60, v44, s45 op_sel:[0,1]\n"
    "v_pk_mul_f16 v61, v45, s45 op_sel:[0,1]\n"
    "v_mfma_f32_16x16x32_f16 v[84:87], v[236:239], v[68:71], v[84:87]\n"
    "ds_read_b64_tr_b16 v[50:51], v4 offset:576\n"
    "v_pk_mul_f16 v62, v46, s45 op_sel:[0,1]\n"
    "v_pk_mul_f16 v63, v47, s45 op_sel:[0,1]\n"
    "v_mfma_f32_16x16x32_f16 v[84:87], v[248:251], v[72:75], v[84:87]\n"
    "ds_read_b64_tr_b16 v[52:53], v4 offset:1152\n"
    "v_pk_mul_f16 v64, v44, v46\n"
    "v_pk_mul_f16 v65, v45, v47\n"
    "v_mfma_f32_16x16x32_f16 v[76:79], v[240:243], v[72:75], v[76:79]\n"
    "ds_read_b64_tr_b16 v[54:55], v4 offset:1728\n"
    "v_add_f32 v11, v88, v89\n"
    "v_add_f32 v13, v90, v91\n"
    "v_mfma_f32_16x16x32_f16 v[76:79], v[228:231], v[68:71], v[76:79]\n"
    "ds_read_b64_tr_b16 v[56:57], v4 offset:2304\n"
    "v_mov_b64 v[88:89], 0\n"
    "v_mov_b64 v[90:91], 0\n"
    "v_mfma_f32_16x16x32_f16 v[80:83], v[232:235], v[68:71], v[80:83]\n"
    "s_waitcnt lgkmcnt(4)\n"
    "v_pk_mul_f16 v66, v48, v48 op_sel_hi:[0,1]\n"
    "v_pk_mul_f16 v67, v48, v49 op_sel_hi:[0,1]\n"
    "v_mfma_f32_16x16x32_f16 v[80:83], v[244:247], v[72:75], v[80:83]\n"
    "v_mul_f32 v11, v11, v13\n"
    "v_fmac_f32 v11, v84, v85\n"
    "v_fmac_f32 v11, v86, v87\n"
    ".Lxd_pl2:\n"
    "v_smfmac_f32_16x16x64_f16 v[88:91], v[112:115], v[60:67], v2\n"
    "s_cbranch_vccnz .Lxd_pr39\n"
    "s_setprio 2\n"
    ".Lxd_pr39:\n"
    "v_pk_mul_f16 v68, v58, v58 op_sel_hi:[0,1]\n"
    "v_pk_mul_f16 v69, v58, v49 op_sel_hi:[0,1]\n"
    "v_mfma_f32_16x16x32_f16 v[84:87], v[108:111], v[64:67], 0\n"
    "v_fmac_f32 v11, v76, v80\n"
    "v_fmac_f32 v11, v77, v81\n"
    "v_mfma_f32_16x16x32_f16 v[84:87], v[96:99], v[60:63], v[84:87]\n"
    "v_fmac_f32 v11, v78, v82\n"
    "v_fma_f32 v8, v79, v83, v11\n"
    "v_mfma_f32_16x16x32_f16 v[80:83], v[92:95], v[60:63], 0\n"
    "s_waitcnt lgkmcnt(0)\n"
    "v_pk_mul_f16 v70, v48, v58 op_sel_hi:[0,1]\n"
    "v_pk_mul_f16 v71, v48, v59 op_sel_hi:[0,1]\n"
    "v_mfma_f32_16x16x32_f16 v[80:83], v[104:107], v[64:67], v[80:83]\n"
    "v_pk_mul_f16 v72, v50, v58 op_sel_hi:[0,1]\n"
    "v_pk_mul_f16 v73, v50, v59 op_sel_hi:[0,1]\n"
    "v_mfma_f32_16x16x32_f16 v[76:79], v[100:103], v[64:67], 0\n"
    "s_cmp_lg_u32 s46, 0\n"
    "v_pk_mul_f16 v74, v52, v58 op_sel_hi:[0,1]\n"
    "v_pk_mul_f16 v75, v52, v59 op_sel_hi:[0,1]\n"
    "v_mfma_f32_16x16x32_f16 v[76:79], v[116:119], v[68:71], v[76:79]\n"
    "s_cbranch_scc1 .Lxd_ht41\n"
    ".Lxd_ht41r:\n"
    "v_pk_mul_f16 v60, v54, v58 op_sel_hi:[0,1]\n"
    "v_pk_mul_f16 v61, v54, v59 op_sel_hi:[0,1]\n"
    "v_mfma_f32_16x16x32_f16 v[76:79], v[128:131], v[72:75], v[76:79]\n"
    "v_pk_mul_f16 v62, v56, v58 op_sel_hi:[0,1]\n"
    "v_mfma_f32_16x16x32_f16 v[80:83], v[132:135], v[72:75], v[80:83]\n"
    "v_pk_mul_f16 v63, v56, v59 op_sel_hi:[0,1]\n"
    "v_mfma_f32_16x16x32_f16 v[80:83], v[120:123], v[68:71], v[80:83]\n"
    "v_pk_mul_f16 v64, v56, v56 op_sel_hi:[0,1]\n"
    "v_mfma_f32_16x16x32_f16 v[84:87], v[124:127], v[68:71], v[84:87]\n"
    "v_pk_mul_f16 v65, v56, v49 op_sel_hi:[0,1]\n"
    "v_mfma_f32_16x16x32_f16 v[84:87], v[136:139], v[72:75], v[84:87]\n"
    "v_pk_mul_f16 v66, v48, v56 op_sel_hi:[0,1]\n"
    "v_smfmac_f32_16x16x64_f16 v[88:91], v[140:143], v[68:75], v2\n"
    "v_pk_mul_f16 v67, v48, v57 op_sel_hi:[0,1]\n"
    "v_mfma_f32_16x16x32_f16 v[76:79], v[144:147], v[60:63], v[76:79]\n"
    "v_pk_mul_f16 v68, v50, v56 op_sel_hi:[0,1]\n"
    "v_pk_mul_f16 v69, v50, v57 op_sel_hi:[0,1]\n"
    "v_mfma_f32_16x16x32_f16 v[76:79], v[156:159], v[64:67], v[76:79]\n"
    "v_pk_mul_f16 v70, v52, v56 op_sel_hi:[0,1]\n"
    "v_mfma_f32_16x16x32_f16 v[80:83], v[160:163], v[64:67], v[80:83]\n"
    "v_pk_mul_f16 v71, v52, v57 op_sel_hi:[0,1]\n"
    "v_mfma_f32_16x16x32_f16 v[80:83], v[148:151], v[60:63], v[80:83]\n"
    "ds_read_b32 v22, v25 offset:524\n"
    "v_pk_mul_f16 v72, v54, v56 op_sel_hi:[0,1]\n"
    "v_mfma_f32_16x16x32_f16 v[84:87], v[152:155], v[60:63], v[84:87]\n"
    "v_pk_mul_f16 v73, v54, v57 op_sel_hi:[0,1]\n"
    "v_mfma_f32_16x16x32_f16 v[84:87], v[164:167], v[64:67], v[84:87]\n"
    "v_pk_mul_f16 v74, v54, v54 op_sel_hi:[0,1]\n"
    "v_smfmac_f32_16x16x64_f16 v[88:91], v[168:171], v[60:67], v2\n"
    "v_pk_mul_f16 v75, v54, v49 op_sel_hi:[0,1]\n"
    "v_mfma_f32_16x16x32_f16 v[76:79], v[172:175], v[68:71], v[76:79]\n"
    "v_pk_mul_f16 v60, v48, v54 op_sel_hi:[0,1]\n"
    "v_pk_mul_f16 v61, v48, v55 op_sel_hi:[0,1]\n"
    "v_mfma_f32_16x16x32_f16 v[76:79], v[184:187], v[72:75], v[76:79]\n"
    "s_waitcnt lgkmcnt(0)\n"
    "v_pk_mul_f16 v62, v50, v54 op_sel_hi:[0,1]\n"
    "v_readfirstlane_b32 s45, v22\n"
    "v_mfma_f32_16x16x32_f16 v[80:83], v[188:191], v[72:75], v[80:83]\n"
    "s_cmp_lt_i32 s45, 16\n"
    "v_pk_mul_f16 v63, v50, v55 op_sel_hi:[0,1]\n"
    "v_mfma_f32_16x16x32_f16 v[80:83], v[176:179], v[68:71], v[80:83]\n"
    "s_cbranch_scc1 .Lxd_ck42\n"
    ".Lxd_ck42r:\n"
    "v_pk_mul_f16 v64, v52, v54 op_sel_hi:[0,1]\n"
    "v_mfma_f32_16x16x32_f16 v[84:87], v[180:183], v[68:71], v[84:87]\n"
    "ds_read_b64_tr_b16 v[44:45], v5 offset:4096\n"
    "v_pk_mul_f16 v65, v52, v55 op_sel_hi:[0,1]\n"
    "v_mfma_f32_16x16x32_f16 v[84:87], v[192:195], v[72:75], v[84:87]\n"
    "ds_read_b64_tr_b16 v[46:47], v6 offset:4096\n"
    "v_pk_mul_f16 v66, v52, v52 op_sel_hi:[0,1]\n"
    "v_smfmac_f32_16x16x64_f16 v[88:91], v[196:199], v[68:75], v2\n"
    "v_pk_mul_f16 v67, v52, v49 op_sel_hi:[0,1]\n"
    "v_mfma_f32_16x16x32_f16 v[76:79], v[200:203], v[60:63], v[76:79]\n"
    "v_pk_mul_f16 v68, v48, v52 op_sel_hi:[0,1]\n"
    "v_pk_mul_f16 v69, v48, v53 op_sel_hi:[0,1]\n"
    "v_mfma_f32_16x16x32_f16 v[76:79], v[212:215], v[64:67], v[76:79]\n"
    "v_pk_mul_f16 v70, v50, v52 op_sel_hi:[0,1]\n"
    "v_mfma_f32_16x16x32_f16 v[80:83], v[216:219], v[64:67], v[80:83]\n"
    "s_cbranch_vccnz .Lxd_pr40\n"
    "s_setprio 0\n"
    ".Lxd_pr40:\n"
    "v_pk_mul_f16 v71, v50, v53 op_sel_hi:[0,1]\n"
    "v_mfma_f32_16x16x32_f16 v[80:83], v[204:207], v[60:63], v[80:83]\n"
    "v_pk_mul_f16 v72, v50, v50 op_sel_hi:[0,1]\n"
    "v_mfma_f32_16x16x32_f16 v[84:87], v[208:211], v[60:63], v[84:87]\n"
    "v_pk_mul_f16 v73, v50, v49 op_sel_hi:[0,1]\n"
    "v_pk_mul_f16 v74, v48, v50 op_sel_hi:[0,1]\n"
    "v_mfma_f32_16x16x32_f16 v[84:87], v[220:223], v[64:67], v[84:87]\n"
    "v_pk_mul_f16 v75, v48, v51 op_sel_hi:[0,1]\n"
    "v_smfmac_f32_16x16x64_f16 v[88:91], v[224:227], v[60:67], v2\n"
    "ds_read_b64_tr_b16 v[48:49], v3 offset:4096\n"
    "ds_read_b64_tr_b16 v[58:59], v4 offset:6976\n"
    "v_smfmac_f32_16x16x64_f16 v[88:91], v[252:255], v[68:75], v2\n"
    "s_waitcnt lgkmcnt(2)\n"
    "v_pk_mul_f16 v60, v44, s45 op_sel:[0,1]\n"
    "v_pk_mul_f16 v61, v45, s45 op_sel:[0,1]\n"
    "v_mfma_f32_16x16x32_f16 v[84:87], v[236:239], v[68:71], v[84:87]\n"
    "ds_read_b64_tr_b16 v[50:51], v4 offset:4672\n"
    "v_pk_mul_f16 v62, v46, s45 op_sel:[0,1]\n"
    "v_pk_mul_f16 v63, v47, s45 op_sel:[0,1]\n"
    "v_mfma_f32_16x16x32_f16 v[84:87], v[248:251], v[72:75], v[84:87]\n"
    "ds_read_b64_tr_b16 v[52:53], v4 offset:5248\n"
    "v_pk_mul_f16 v64, v44, v46\n"
    "v_pk_mul_f16 v65, v45, v47\n"
    "v_mfma_f32_16x16x32_f16 v[76:79], v[240:243], v[72:75], v[76:79]\n"
    "ds_read_b64_tr_b16 v[54:55], v4 offset:5824\n"
    "v_add_f32 v11, v88, v89\n"
    "v_add_f32 v13, v90, v91\n"
    "v_mfma_f32_16x16x32_f16 v[76:79], v[228:231], v[68:71], v[76:79]\n"
    "ds_read_b64_tr_b16 v[56:57], v4 offset:6400\n"
    "v_mov_b64 v[88:89], 0\n"
    "v_mov_b64 v[90:91], 0\n"
    "v_mfma_f32_16x16x32_f16 v[80:83], v[232:235], v[68:71], v[80:83]\n"
    "s_waitcnt lgkmcnt(4)\n"
    "v_pk_mul_f16 v66, v48, v48 op_sel_hi:[0,1]\n"
    "v_pk_mul_f16 v67, v48, v49 op_sel_hi:[0,1]\n"
    "v_mfma_f32_16x16x32_f16 v[80:83], v[244:247], v[72:75], v[80:83]\n"
    "v_mul_f32 v11, v11, v13\n"
    "v_fmac_f32 v11, v84, v85\n"
    "v_fmac_f32 v11, v86, v87\n"
    ".Lxd_pl3:\n"
    "v_smfmac_f32_16x16x64_f16 v[88:91], v[112:115], v[60:67], v2\n"
    "s_cbranch_vccnz .Lxd_pr43\n"
    "s_setprio 2\n"
    ".Lxd_pr43:\n"
    "v_pk_mul_f16 v68, v58, v58 op_sel_hi:[0,1]\n"
    "v_pk_mul_f16 v69, v58, v49 op_sel_hi:[0,1]\n"
    "v_mfma_f32_16x16x32_f16 v[84:87], v[108:111], v[64:67], 0\n"
    "v_fmac_f32 v11, v76, v80\n"
    "v_fmac_f32 v11, v77, v81\n"
    "v_mfma_f32_16x16x32_f16 v[84:87], v[96:99], v[60:63], v[84:87]\n"
    "v_fmac_f32 v11, v78, v82\n"
    "v_fma_f32 v9, v79, v83, v11\n"
    "v_mfma_f32_16x16x32_f16 v[80:83], v[92:95], v[60:63], 0\n"
    "s_waitcnt lgkmcnt(0)\n"
    "v_pk_mul_f16 v70, v48, v58 op_sel_hi:[0,1]\n"
    "v_pk_mul_f16 v71, v48, v59 op_sel_hi:[0,1]\n"
    "v_mfma_f32_16x16x32_f16 v[80:83], v[104:107], v[64:67], v[80:83]\n"
    "v_pk_mul_f16 v72, v50, v58 op_sel_hi:[0,1]\n"
    "v_pk_mul_f16 v73, v50, v59 op_sel_hi:[0,1]\n"
    "v_mfma_f32_16x16x32_f16 v[76:79], v[100:103], v[64:67], 0\n"
    "s_cmp_lt_i32 s46, 0\n"
    "v_pk_mul_f16 v74, v52, v58 op_sel_hi:[0,1]\n"
    "v_pk_mul_f16 v75, v52, v59 op_sel_hi:[0,1]\n"
    "v_mfma_f32_16x16x32_f16 v[76:79], v[116:119], v[68:71], v[76:79]\n"
    "s_cbranch_scc1 .Lxd_ht45\n"
    ".Lxd_ht45r:\n"
    "v_pk_mul_f16 v60, v54, v58 op_sel_hi:[0,1]\n"
    "v_pk_mul_f16 v61, v54, v59 op_sel_hi:[0,1]\n"
    "v_mfma_f32_16x16x32_f16 v[76:79], v[128:131], v[72:75], v[76:79]\n"
    "v_pk_mul_f16 v62, v56, v58 op_sel_hi:[0,1]\n"
    "v_mfma_f32_16x16x32_f16 v[80:83], v[132:135], v[72:75], v[80:83]\n"
    "v_pk_mul_f16 v63, v56, v59 op_sel_hi:[0,1]\n"
    "v_mfma_f32_16x16x32_f16 v[80:83], v[120:123], v[68:71], v[80:83]\n"
    "v_pk_mul_f16 v64, v56, v56 op_sel_hi:[0,1]\n"
    "v_mfma_f32_16x16x32_f16 v[84:87], v[124:127], v[68:71], v[84:87]\n"
    "s_cmp_eq_u32 s36, 0\n"
    "v_pk_mul_f16 v65, v56, v49 op_sel_hi:[0,1]\n"
    "v_mfma_f32_16x16x32_f16 v[84:87], v[136:139], v[72:75], v[84:87]\n"
    "s_cbranch_scc1 .Lxd_ld46\n"
    ".Lxd_ld46r:\n"
    "v_pk_mul_f16 v66, v48, v56 op_sel_hi:[0,1]\n"
    "v_smfmac_f32_16x16x64_f16 v[88:91], v[140:143], v[68:75], v2\n"
    "v_pk_mul_f16 v67, v48, v57 op_sel_hi:[0,1]\n"
    "v_mfma_f32_16x16x32_f16 v[76:79], v[144:147], v[60:63], v[76:79]\n"
    "v_pk_mul_f16 v68, v50, v56 op_sel_hi:[0,1]\n"
    "v_pk_mul_f16 v69, v50, v57 op_sel_hi:[0,1]\n"
    "v_mfma_f32_16x16x32_f16 v[76:79], v[156:159], v[64:67], v[76:79]\n"
    "v_pk_mul_f16 v70, v52, v56 op_sel_hi:[0,1]\n"
    "v_mfma_f32_16x16x32_f16 v[80:83], v[160:163], v[64:67], v[80:83]\n"
    "v_pk_mul_f16 v71, v52, v57 op_sel_hi:[0,1]\n"
    "v_mfma_f32_16x16x32_f16 v[80:83], v[148:151], v[60:63], v[80:83]\n"
    "ds_read_b32 v22, v25 offset:528\n"
    "v_pk_mul_f16 v72, v54, v56 op_sel_hi:[0,1]\n"
    "v_mfma_f32_16x16x32_f16 v[84:87], v[152:155], v[60:63], v[84:87]\n"
    "v_pk_mul_f16 v73, v54, v57 op_sel_hi:[0,1]\n"
    "v_mfma_f32_16x16x32_f16 v[84:87], v[164:167], v[64:67], v[84:87]\n"
    "v_pk_mul_f16 v74, v54, v54 op_sel_hi:[0,1]\n"
    "v_smfmac_f32_16x16x64_f16 v[88:91], v[168:171], v[60:67], v2\n"
    "v_pk_mul_f16 v75, v54, v49 op_sel_hi:[0,1]\n"
    "v_mfma_f32_16x16x32_f16 v[76:79], v[172:175], v[68:71], v[76:79]\n"
    "v_pk_mul_f16 v60, v48, v54 op_sel_hi:[0,1]\n"
    "v_pk_mul_f16 v61, v48, v55 op_sel_hi:[0,1]\n"
    "v_mfma_f32_16x16x32_f16 v[76:79], v[184:187], v[72:75], v[76:79]\n"
    "s_waitcnt lgkmcnt(0)\n"
    "v_pk_mul_f16 v62, v50, v54 op_sel_hi:[0,1]\n"
    "v_readfirstlane_b32 s45, v22\n"
    "v_mfma_f32_16x16x32_f16 v[80:83], v[188:191], v[72:75], v[80:83]\n"
    "s_cmp_lt_i32 s45, 16\n"
    "v_pk_mul_f16 v63, v50, v55 op_sel_hi:[0,1]\n"
    "v_mfma_f32_16x16x32_f16 v[80:83], v[176:179], v[68:71], v[80:83]\n"
    "s_cbranch_scc1 .Lxd_ck48\n"
    ".Lxd_ck48r:\n"
    "v_pk_mul_f16 v64, v52, v54 op_sel_hi:[0,1]\n"
    "v_mfma_f32_16x16x32_f16 v[84:87], v[180:183], v[68:71], v[84:87]\n"
    "ds_read_b64_tr_b16 v[44:45], v5 offset:8192\n"
    "v_pk_mul_f16 v65, v52, v55 op_sel_hi:[0,1]\n"
    "v_mfma_f32_16x16x32_f16 v[84:87], v[192:195], v[72:75], v[84:87]\n"
    "ds_read_b64_tr_b16 v[46:47], v6 offset:8192\n"
    "v_pk_mul_f16 v66, v52, v52 op_sel_hi:[0,1]\n"
    "v_smfmac_f32_16x16x64_f16 v[88:91], v[196:199], v[68:75], v2\n"
    "v_pk_mul_f16 v67, v52, v49 op_sel_hi:[0,1]\n"
    "v_mfma_f32_16x16x32_f16 v[76:79], v[200:203], v[60:63], v[76:79]\n"
    "v_pk_mul_f16 v68, v48, v52 op_sel_hi:[0,1]\n"
    "v_pk_mul_f16 v69, v48, v53 op_sel_hi:[0,1]\n"
    "v_mfma_f32_16x16x32_f16 v[76:79], v[212:215], v[64:67], v[76:79]\n"
    "v_pk_mul_f16 v70, v50, v52 op_sel_hi:[0,1]\n"
    "v_mfma_f32_16x16x32_f16 v[80:83], v[216:219], v[64:67], v[80:83]\n"
    "s_cbranch_vccnz .Lxd_pr44\n"
    "s_setprio 0\n"
    ".Lxd_pr44:\n"
    "v_pk_mul_f16 v71, v50, v53 op_sel_hi:[0,1]\n"
    "v_mfma_f32_16x16x32_f16 v[80:83], v[204:207], v[60:63], v[80:83]\n"
    "v_pk_mul_f16 v72, v50, v50 op_sel_hi:[0,1]\n"
    "v_mfma_f32_16x16x32_f16 v[84:87], v[208:211], v[60:63], v[84:87]\n"
    "v_pk_mul_f16 v73, v50, v49 op_sel_hi:[0,1]\n"
    "v_pk_mul_f16 v74, v48, v50 op_sel_hi:[0,1]\n"
    "v_mfma_f32_16x16x32_f16 v[84:87], v[220:223], v[64:67], v[84:87]\n"
    "v_pk_mul_f16 v75, v48, v51 op_sel_hi:[0,1]\n"
    "v_smfmac_f32_16x16x64_f16 v[88:91], v[224:227], v[60:67], v2\n"
    "ds_read_b64_tr_b16 v[48:49], v3 offset:8192\n"
    "ds_read_b64_tr_b16 v[58:59], v4 offset:11072\n"
    "v_add_u32 v25, 16, v25\n"
    "v_smfmac_f32_16x16x64_f16 v[88:91], v[252:255], v[68:75], v2\n"
    "s_waitcnt lgkmcnt(2)\n"
    "v_pk_mul_f16 v60, v44, s45 op_sel:[0,1]\n"
    "v_pk_mul_f16 v61, v45, s45 op_sel:[0,1]\n"
    "v_mfma_f32_16x16x32_f16 v[84:87], v[236:239], v[68:71], v[84:87]\n"
    "ds_read_b64_tr_b16 v[50:51], v4 offset:8768\n"
    "v_pk_mul_f16 v62, v46, s45 op_sel:[0,1]\n"
    "v_pk_mul_f16 v63, v47, s45 op_sel:[0,1]\n"
    "v_mfma_f32_16x16x32_f16 v[84:87], v[248:251], v[72:75], v[84:87]\n"
    "ds_read_b64_tr_b16 v[52:53], v4 offset:9344\n"
    "v_pk_mul_f16 v64, v44, v46\n"
    "v_pk_mul_f16 v65, v45, v47\n"
    "v_mfma_f32_16x16x32_f16 v[76:79], v[240:243], v[72:75], v[76:79]\n"
    "ds_read_b64_tr_b16 v[54:55], v4 offset:9920\n"
    "v_add_f32 v11, v88, v89\n"
    "v_add_f32 v13, v90, v91\n"
    "v_mfma_f32_16x16x32_f16 v[76:79], v[228:231], v[68:71], v[76:79]\n"
    "ds_read_b64_tr_b16 v[56:57], v4 offset:10496\n"
    "v_mov_b64 v[88:89], 0\n"
    "v_mov_b64 v[90:91], 0\n"
    "v_mfma_f32_16x16x32_f16 v[80:83], v[232:235], v[68:71], v[80:83]\n"
    "s_waitcnt lgkmcnt(4)\n"
    "v_pk_mul_f16 v66, v48, v48 op_sel_hi:[0,1]\n"
    "v_pk_mul_f16 v67, v48, v49 op_sel_hi:[0,1]\n"
    "v_mfma_f32_16x16x32_f16 v[80:83], v[244:247], v[72:75], v[80:83]\n"
    "s_cmp_eq_u32 s36, 0\n"
    "v_mul_f32 v11, v11, v13\n"
    "v_fmac_f32 v11, v84, v85\n"
    "v_fmac_f32 v11, v86, v87\n"
    "s_cbranch_scc1 .Lxd_ph0\n"
    "s_branch .Lxd_pl0\n"
    ".Lxd_last1:\n"
    "s_cbranch_vccnz .Lxd_pr49\n"
    "s_setprio 0\n"
    ".Lxd_pr49:\n"
    "s_waitcnt vmcnt(0)\n"
    "v_pk_mul_f16 v64, v52, v54 op_sel_hi:[0,1]\n"
    "v_mfma_f32_16x16x32_f16 v[84:87], v[180:183], v[68:71], v[84:87]\n"
    "s_lshl4_add_u32 s66, s22, s58\n"
    "v_pk_mul_f16 v65, v52, v55 op_sel_hi:[0,1]\n"
    "v_xor_b32 v46, s28, v42\n"
    "v_mfma_f32_16x16x32_f16 v[84:87], v[192:195], v[72:75], v[84:87]\n"
    "v_pk_mul_f16 v66, v52, v52 op_sel_hi:[0,1]\n"
    "v_xor_b32 v44, s29, v43\n"
    "v_smfmac_f32_16x16x64_f16 v[88:91], v[196:199], v[68:75], v2\n"
    "v_pk_mul_f16 v67, v52, v49 op_sel_hi:[0,1]\n"
    "v_lshl_or_b32 v46, v46, 24, v44\n"
    "v_mfma_f32_16x16x32_f16 v[76:79], v[200:203], v[60:63], v[76:79]\n"
    "v_pk_mul_f16 v68, v48, v52 op_sel_hi:[0,1]\n"
    "v_pk_mul_f16 v69, v48, v53 op_sel_hi:[0,1]\n"
    "v_mfma_f32_16x16x32_f16 v[76:79], v[212:215], v[64:67], v[76:79]\n"
    "v_pk_mul_f16 v70, v50, v52 op_sel_hi:[0,1]\n"
    "v_and_b32 v44, 0xffffff00, v42\n"
    "v_mfma_f32_16x16x32_f16 v[80:83], v[216:219], v[64:67], v[80:83]\n"
    "v_pk_mul_f16 v71, v50, v53 op_sel_hi:[0,1]\n"
    "v_cmp_ne_u32 s[40:41], 0, v46\n"
    "v_xor_b32 v46, s28, v26\n"
    "v_mfma_f32_16x16x32_f16 v[80:83], v[204:207], v[60:63], v[80:83]\n"
    "v_pk_mul_f16 v72, v50, v50 op_sel_hi:[0,1]\n"
    "v_xor_b32 v45, s29, v27\n"
    "v_mfma_f32_16x16x32_f16 v[84:87], v[208:211], v[60:63], v[84:87]\n"
    "v_pk_mul_f16 v73, v50, v49 op_sel_hi:[0,1]\n"
    "v_pk_mul_f16 v74, v48, v50 op_sel_hi:[0,1]\n"
    "v_mfma_f32_16x16x32_f16 v[84:87], v[220:223], v[64:67], v[84:87]\n"
    "v_pk_mul_f16 v75, v48, v51 op_sel_hi:[0,1]\n"
    "v_lshl_or_b32 v46, v46, 24, v45\n"
    "v_smfmac_f32_16x16x64_f16 v[88:91], v[224:227], v[60:67], v2\n"
    "s_nop 0\n"
    "v_and_b32 v45, 0xffffff00, v26\n"
    "v_cmp_ne_u32 s[42:43], 0, v46\n"
    "v_smfmac_f32_16x16x64_f16 v[88:91], v[252:255], v[68:75], v2\n"
    "v_lshrrev_b32 v46, 4, v1\n"
    "v_lshl_add_u32 v46, v46, 2, s66\n"
    "v_mfma_f32_16x16x32_f16 v[84:87], v[236:239], v[68:71], v[84:87]\n"
    "s_and_b64 s[40:41], s[40:41], s[30:31]\n"
    "v_add_f32_dpp v44, v44, v44 quad_perm:[1,0,3,2] row_mask:0xf bank_mask:0xf\n"
    "v_add_f32_dpp v45, v45, v45 quad_perm:[1,0,3,2] row_mask:0xf bank_mask:0xf\n"
    "v_mfma_f32_16x16x32_f16 v[84:87], v[248:251], v[72:75], v[84:87]\n"
    "s_and_b64 s[42:43], s[42:43], s[64:65]\n"
    "v_add_f32_dpp v44, v44, v44 quad_perm:[2,3,0,1] row_mask:0xf bank_mask:0xf\n"
    "v_add_f32_dpp v45, v45, v45 quad_perm:[2,3,0,1] row_mask:0xf bank_mask:0xf\n"
    "v_mfma_f32_16x16x32_f16 v[76:79], v[240:243], v[72:75], v[76:79]\n"
    "s_or_b64 s[40:41], s[40:41], s[42:43]\n"
    "v_add_f32_dpp v44, v44, v44 row_half_mirror row_mask:0xf bank_mask:0xf\n"
    "v_add_f32_dpp v45, v45, v45 row_half_mirror row_mask:0xf bank_mask:0xf\n"
    "v_mfma_f32_16x16x32_f16 v[76:79], v[228:231], v[68:71], v[76:79]\n"
    "s_cselect_b32 s56, 1, 2\n"
    "v_add_f32_dpp v44, v44, v44 row_mirror row_mask:0xf bank_mask:0xf\n"
    "v_add_f32_dpp v45, v45, v45 row_mirror row_mask:0xf bank_mask:0xf\n"
    "v_mfma_f32_16x16x32_f16 v[80:83], v[232:235], v[68:71], v[80:83]\n"
    "ds_write_b32 v46, v44 offset:1024\n"
    "v_add_f32 v11, v88, v89\n"
    "v_add_f32 v13, v90, v91\n"
    "v_mfma_f32_16x16x32_f16 v[80:83], v[244:247], v[72:75], v[80:83]\n"
    "ds_write_b32 v46, v45 offset:1088\n"
    "v_mul_f32 v11, v11, v13\n"
    "v_fmac_f32 v11, v84, v85\n"
    "v_fmac_f32 v11, v86, v87\n"
    "s_nop 2\n"
    "s_branch .Lxd_last2\n"
    ".Lxd_last0:\n"
    "s_cbranch_vccnz .Lxd_pr50\n"
    "s_setprio 0\n"
    ".Lxd_pr50:\n"
    "v_pk_mul_f16 v64, v52, v54 op_sel_hi:[0,1]\n"
    "v_mfma_f32_16x16x32_f16 v[84:87], v[180:183], v[68:71], v[84:87]\n"
    "v_pk_mul_f16 v65, v52, v55 op_sel_hi:[0,1]\n"
    "v_mfma_f32_16x16x32_f16 v[84:87], v[192:195], v[72:75], v[84:87]\n"
    "v_pk_mul_f16 v66, v52, v52 op_sel_hi:[0,1]\n"
    "v_smfmac_f32_16x16x64_f16 v[88:91], v[196:199], v[68:75], v2\n"
    "v_pk_mul_f16 v67, v52, v49 op_sel_hi:[0,1]\n"
    "v_mfma_f32_16x16x32_f16 v[76:79], v[200:203], v[60:63], v[76:79]\n"
    "v_pk_mul_f16 v68, v48, v52 op_sel_hi:[0,1]\n"
    "v_pk_mul_f16 v69, v48, v53 op_sel_hi:[0,1]\n"
    "v_mfma_f32_16x16x32_f16 v[76:79], v[212:215], v[64:67], v[76:79]\n"
    "v_pk_mul_f16 v70, v50, v52 op_sel_hi:[0,1]\n"
    "v_mfma_f32_16x16x32_f16 v[80:83], v[216:219], v[64:67], v[80:83]\n"
    "v_pk_mul_f16 v71, v50, v53 op_sel_hi:[0,1]\n"
    "v_mfma_f32_16x16x32_f16 v[80:83], v[204:207], v[60:63], v[80:83]\n"
    "v_pk_mul_f16 v72, v50, v50 op_sel_hi:[0,1]\n"
    "v_mfma_f32_16x16x32_f16 v[84:87], v[208:211], v[60:63], v[84:87]\n"
    "v_pk_mul_f16 v73, v50, v49 op_sel_hi:[0,1]\n"
    "v_pk_mul_f16 v74, v48, v50 op_sel_hi:[0,1]\n"
    "v_mfma_f32_16x16x32_f16 v[84:87], v[220:223], v[64:67], v[84:87]\n"
    "v_pk_mul_f16 v75, v48, v51 op_sel_hi:[0,1]\n"
    "v_smfmac_f32_16x16x64_f16 v[88:91], v[224:227], v[60:67], v2\n"
    "s_nop 0\n"
    "v_smfmac_f32_16x16x64_f16 v[88:91], v[252:255], v[68:75], v2\n"
    "v_mfma_f32_16x16x32_f16 v[84:87], v[236:239], v[68:71], v[84:87]\n"
    "v_mfma_f32_16x16x32_f16 v[84:87], v[248:251], v[72:75], v[84:87]\n"
    "v_mfma_f32_16x16x32_f16 v[76:79], v[240:243], v[72:75], v[76:79]\n"
    "v_mfma_f32_16x16x32_f16 v[76:79], v[228:231], v[68:71], v[76:79]\n"
    "v_mfma_f32_16x16x32_f16 v[80:83], v[232:235], v[68:71], v[80:83]\n"
    "v_mfma_f32_16x16x32_f16 v[80:83], v[244:247], v[72:75], v[80:83]\n"
    "s_nop 1\n"
    "v_add_f32 v11, v88, v89\n"
    "v_add_f32 v13, v90, v91\n"
    "v_mul_f32 v11, v11, v13\n"
    "v_fmac_f32 v11, v84, v85\n"
    "v_fmac_f32 v11, v86, v87\n"
    "s_nop 0\n"
    ".Lxd_last2:\n"
    "v_fmac_f32 v11, v76, v80\n"
    "v_fmac_f32 v11, v77, v81\n"
    "v_fmac_f32 v11, v78, v82\n"
    "v_fma_f32 v11, v79, v83, v11\n"
    "s_set_gpr_idx_on s37, gpr_idx(DST)\n"
    "s_nop 0\n"
    "v_mov_b32 v7, v11\n"
    "s_set_gpr_idx_off\n"
    ".Lxd_fin:\n"
    "v_lshl_add_u32 v68, v18, 6, v20\n"
    "v_mov_b32 v67, 154144\n"
    "v_permlane32_swap_b32 v7, v8\n"
    "v_permlane32_swap_b32 v9, v10\n"
    "v_mul_f32 v7, v0, v7\n"
    "v_mul_f32 v9, v0, v9\n"
    "v_fmac_f32 v7, v23, v8\n"
    "v_fmac_f32 v9, v23, v10\n"
    "ds_read_b32 v67, v67\n"
    "v_lshlrev_b32 v69, 2, v68\n"
    "v_permlane16_swap_b32 v7, v9\n"
    "v_add_f32 v7, v7, v9\n"
    "v_add_u32 v68, 150528, v68\n"
    "v_add_u32 v69, 152128, v69\n"
    "v_add_f32_dpp v7, v7, v7 quad_perm:[1,0,3,2] row_mask:0xf bank_mask:0xf\n"
    "s_lshl_b32 s26, s26, 16\n"
    "s_nop 0\n"
    "v_add_f32_dpp v7, v7, v7 quad_perm:[2,3,0,1] row_mask:0xf bank_mask:0xf\n"
    "s_nop 1\n"
    "v_add_f32_dpp v7, v7, v7 row_half_mirror row_mask:0xf bank_mask:0xf\n"
    "s_nop 1\n"
    "v_add_f32_dpp v7, v7, v7 row_mirror row_mask:0xf bank_mask:0xf\n"
    "ds_write_b32 v14, v7\n"
    "s_cmp_lg_u32 s56, 2\n"
    "s_cbranch_scc1 .Lxd_f2\n"
    ".Lxd_f3:\n"
    "s_waitcnt vmcnt(0) lgkmcnt(0)\n"
    "s_barrier\n"
    "s_cmp_lg_u32 s20, 0\n"
    "s_cbranch_scc1 .Lxd_done\n"
    "ds_read_b128 v[60:63], v69\n"
    "ds_read_b32 v64, v68\n"
    "ds_read_u16 v65, v68 offset:512\n"
    "ds_read_b32 v66, v68 offset:1024\n"
    "s_waitcnt lgkmcnt(3)\n"
    "v_pk_add_f32 v[60:61], v[60:61], v[62:63]\n"
    "s_waitcnt lgkmcnt(0)\n"
    "v_cmp_lt_i32 vcc, 0, v64\n"
    "v_lshrrev_b32 v65, 4, v65\n"
    "v_add_f32 v60, v60, v61\n"
    "v_lshl_add_u32 v65, v65, 2, v67\n"
    "v_add_f32 v66, s26, v66\n"
    "v_ldexp_f32 v60, v60, v65\n"
    "s_mov_b64 exec, vcc\n"
    "v_add_f32 v60, v60, v66\n"
    "v_cvt_pk_bf16_f32 v60, v60, v60\n"
    "global_store_short v64, v60, s[70:71] offset:-2\n"
    "s_mov_b64 exec, -1\n"
    ".Lxd_done:\n"
    "s_endpgm\n"
    ".Lxd_cin:\n"
    "s_lshr_b32 s20, s43, 6\n"
    "v_and_b32 v1, 63, v0\n"
    "v_lshlrev_b32 v42, 4, v1\n"
    "s_and_b32 s40, s2, 3\n"
    "s_lshl2_add_u32 s23, s40, s20\n"
    "s_lshl1_add_u32 s41, s40, s20\n"
    "s_add_u32 s41, s41, 12\n"
    "s_cmp_ge_u32 s20, 4\n"
    "s_cselect_b32 s23, s41, s23\n"
    "s_add_u32 s41, s40, 24\n"
    "s_cmp_ge_u32 s20, 6\n"
    "s_cselect_b32 s23, s41, s23\n"
    "s_cselect_b32 s24, 1, 0\n"
    "s_min_u32 s34, s23, 25\n"
    "s_lshr_b32 s35, s2, 2\n"
    "v_and_b32 v190, 31, v0\n"
    "v_min_u32 v190, 25, v190\n"
    "v_mul_u32_u24 v190, 52, v190\n"
    "v_bfe_u32 v191, v0, 5, 1\n"
    "v_lshl_add_u32 v190, v191, 4, v190\n"
    "s_mul_i32 s40, s34, 1352\n"
    "s_xor_b32 s66, s20, 7\n"
    "s_or_b32 s66, s66, s2\n"
    "s_waitcnt lgkmcnt(0)\n"
    "s_load_dwordx4 s[68:71], s[0:1], 64\n"
    "s_add_u32 s38, s12, s40\n"
    "s_addc_u32 s39, s13, 0\n"
    "s_cmp_lg_u32 s66, 0\n"
    "s_cbranch_scc1 .Lxd_on51\n"
    "v_and_b32 v188, 31, v0\n"
    "v_lshlrev_b32 v188, 7, v188\n"
    "v_add_u32 v188, 32768, v188\n"
    "v_mov_b32 v176, 114\n"
    "global_store_dword v188, v176, s[16:17] sc1\n"
    "global_load_dwordx4 v[180:183], v42, s[16:17] sc1\n"
    ".Lxd_on51:\n"
    "global_load_dwordx4 v[80:83], v190, s[38:39] offset:0\n"
    "global_load_dwordx4 v[84:87], v190, s[38:39] offset:32\n"
    "s_getpc_b64 s[60:61]\n"
    "s_add_u32 s60, s60, xd_tab@rel32@lo+4\n"
    "s_addc_u32 s61, s61, xd_tab@rel32@hi+12\n"
    "v_lshl_add_u32 v188, s24, 10, v42\n"
    "s_lshl1_add_u32 s40, s24, s35\n"
    "v_lshl_add_u32 v189, s40, 11, v42\n"
    "s_load_dword s25, s[16:17], 0\n"
    "global_load_dwordx4 v[44:47], v188, s[60:61]\n"
    "global_load_dwordx4 v[48:51], v189, s[60:61] offset:2048\n"
    "global_load_dwordx4 v[52:55], v189, s[60:61] offset:3072\n"
    "s_setprio 0\n"
    "v_mov_b64 v[188:189], 0\n"
    "v_lshlrev_b32 v190, 3, v0\n"
    "v_lshrrev_b32 v186, 5, v1\n"
    "ds_write_b64 v190, v[188:189] offset:36864\n"
    "v_bfe_u32 v190, v0, 1, 4\n"
    "v_lshlrev_b32 v72, 2, v186\n"
    "v_sub_u32 v190, v190, v72\n"
    "v_and_b32 v72, 16, v42\n"
    "s_movk_i32 s40, 0x3f80\n"
    "v_lshlrev_b32_e64 v191, v72, s40\n"
    "v_cmp_eq_u32 s[60:61], 0, v190\n"
    "v_cmp_eq_u32 s[62:63], 1, v190\n"
    "v_cmp_eq_u32 s[64:65], 2, v190\n"
    "v_cmp_eq_u32 s[50:51], 3, v190\n"
    "v_cndmask_b32 v72, 0, v191, s[60:61]\n"
    "v_cndmask_b32 v73, 0, v191, s[62:63]\n"
    "v_cndmask_b32 v74, 0, v191, s[64:65]\n"
    "v_cndmask_b32 v75, 0, v191, s[50:51]\n"
    "v_cmp_eq_u32 s[60:61], 8, v190\n"
    "v_cmp_eq_u32 s[62:63], 9, v190\n"
    "v_cmp_eq_u32 s[64:65], 10, v190\n"
    "v_cmp_eq_u32 s[50:51], 11, v190\n"
    "v_cndmask_b32 v76, 0, v191, s[60:61]\n"
    "v_cndmask_b32 v77, 0, v191, s[62:63]\n"
    "v_cndmask_b32 v78, 0, v191, s[64:65]\n"
    "v_cndmask_b32 v79, 0, v191, s[50:51]\n"
    "s_lshl_b32 s30, s20, 12\n"
    "s_add_u32 s30, s30, 65536\n"
    "v_and_b32 v188, 31, v0\n"
    "v_mul_u32_u24 v192, 528, v186\n"
    "v_lshl_add_u32 v192, v188, 2, v192\n"
    "v_add_u32 v192, s30, v192\n"
    "s_lshl_b32 s40, s35, 6\n"
    "s_add_u32 s40, s40, s20\n"
    "s_lshl1_add_u32 s41, s20, s35\n"
    "s_add_u32 s41, s41, 120\n"
    "s_cmp_ge_u32 s20, 4\n"
    "s_cselect_b32 s40, s41, s40\n"
    "s_lshl1_add_u32 s41, s35, 384\n"
    "s_cmp_ge_u32 s20, 6\n"
    "s_cselect_b32 s40, s41, s40\n"
    "s_cselect_b32 s42, 1, 192\n"
    "s_and_b32 s41, s2, 3\n"
    "s_lshl2_add_u32 s40, s41, s40\n"
    "v_lshrrev_b32 v188, 3, v1\n"
    "v_mul_u32_u24 v188, 448, v188\n"
    "v_bfe_u32 v189, v0, 2, 1\n"
    "v_mad_u32_u24 v188, v189, s42, v188\n"
    "v_and_b32 v189, 48, v42\n"
    "v_add3_u32 v188, v188, v189, s40\n"
    "v_lshlrev_b32 v193, 4, v188\n"
    "v_mov_b32 v177, 0\n"
    "s_waitcnt vmcnt(0) lgkmcnt(0)\n"
    "s_cmp_lg_u32 s66, 0\n"
    "s_cbranch_scc1 .Lxd_on52\n"
    "s_mov_b32 s40, 0x9e3779b1\n"
    "v_mul_lo_u32 v144, v180, s40\n"
    "v_xor_b32 v145, 0xa5c30f96, v180\n"
    "v_alignbit_b32 v144, s25, v144, 24\n"
    "v_mul_lo_u32 v146, v181, s40\n"
    "v_xor_b32 v147, 0xa5c30f96, v181\n"
    "v_alignbit_b32 v146, s25, v146, 24\n"
    "v_mul_lo_u32 v148, v182, s40\n"
    "v_xor_b32 v149, 0xa5c30f96, v182\n"
    "v_alignbit_b32 v148, s25, v148, 24\n"
    "v_mul_lo_u32 v150, v183, s40\n"
    "v_xor_b32 v151, 0xa5c30f96, v183\n"
    "v_alignbit_b32 v150, s25, v150, 24\n"
    "v_lshlrev_b32 v180, 1, v42\n"
    "s_add_u32 s40, s16, 4096\n"
    "s_addc_u32 s41, s17, 0\n"
    "global_store_dwordx4 v180, v[144:147], s[40:41] sc1\n"
    "global_store_dwordx4 v180, v[148:151], s[40:41] offset:16 sc1\n"
    ".Lxd_on52:\n"
    "s_barrier\n"
    "s_branch .Lxd_bD\n"
    ".Lxd_s2:\n"
    "s_cmp_lt_u32 s2, 4\n"
    "s_cbranch_scc1 .Lxd_cin\n"
    ".Lxd_bR:\n"
    "s_lshr_b32 s20, s43, 6\n"
    "v_and_b32 v1, 63, v0\n"
    "v_lshlrev_b32 v42, 4, v1\n"
    "s_mul_i32 s40, s20, 4480\n"
    "s_min_u32 s40, s40, 30672\n"
    "s_mov_b32 m0, s40\n"
    "s_add_u32 s40, s40, 70304\n"
    "s_waitcnt lgkmcnt(0)\n"
    "s_load_dwordx4 s[68:71], s[0:1], 64\n"
    "s_add_u32 s38, s12, s40\n"
    "s_addc_u32 s39, s13, 0\n"
    "global_load_lds_dwordx4 v42, s[38:39] offset:0\n"
    "global_load_lds_dwordx4 v42, s[38:39] offset:1024\n"
    "global_load_lds_dwordx4 v42, s[38:39] offset:2048\n"
    "global_load_lds_dwordx4 v42, s[38:39] offset:3072\n"
    "global_load_lds_dwordx4 v42, s[38:39] offset:3456\n"
    "s_and_b32 s40, s2, 3\n"
    "s_lshl2_add_u32 s23, s40, s20\n"
    "s_lshl1_add_u32 s41, s40, s20\n"
    "s_add_u32 s41, s41, 12\n"
    "s_cmp_ge_u32 s20, 4\n"
    "s_cselect_b32 s23, s41, s23\n"
    "s_add_u32 s41, s40, 24\n"
    "s_cmp_ge_u32 s20, 6\n"
    "s_cselect_b32 s23, s41, s23\n"
    "s_cselect_b32 s24, 1, 0\n"
    "s_min_u32 s34, s23, 25\n"
    "s_lshr_b32 s35, s2, 2\n"
    "v_lshrrev_b32 v190, 2, v1\n"
    "v_and_b32 v188, 48, v42\n"
    "s_movk_i32 s42, 1352\n"
    "v_min_u32 v191, 9, v190\n"
    "v_mad_u32_u24 v190, v190, s42, v188\n"
    "v_mad_u32_u24 v191, v191, s42, v188\n"
    "v_add_u32 v191, 20608, v191\n"
    "s_mul_i32 s41, s34, 52\n"
    "s_add_u32 s41, s41, 35152\n"
    "s_cmp_lg_u32 s20, 7\n"
    "s_cbranch_scc1 .Lxd_on53\n"
    "v_min_u32 v188, 38, v1\n"
    "s_mov_b32 m0, 40960\n"
    "v_lshlrev_b32 v188, 2, v188\n"
    "global_load_lds_dword v188, s[14:15]\n"
    ".Lxd_on53:\n"
    "s_lshl_b32 s28, s20, 11\n"
    "s_add_u32 s28, s28, 49152\n"
    "s_mov_b32 m0, s28\n"
    "s_add_u32 s38, s12, s41\n"
    "s_addc_u32 s39, s13, 0\n"
    "global_load_lds_dwordx4 v190, s[38:39] offset:0\n"
    "global_load_lds_dwordx4 v191, s[38:39] offset:1024\n"
    "s_getpc_b64 s[60:61]\n"
    "s_add_u32 s60, s60, xd_tab@rel32@lo+4\n"
    "s_addc_u32 s61, s61, xd_tab@rel32@hi+12\n"
    "v_lshl_add_u32 v188, s24, 10, v42\n"
    "s_lshl1_add_u32 s40, s24, s35\n"
    "v_lshl_add_u32 v189, s40, 11, v42\n"
    "s_load_dword s25, s[16:17], 0\n"
    "global_load_dwordx4 v[44:47], v188, s[60:61]\n"
    "global_load_dwordx4 v[48:51], v189, s[60:61] offset:2048\n"
    "global_load_dwordx4 v[52:55], v189, s[60:61] offset:3072\n"
    "s_setprio 0\n"
    "v_mov_b64 v[188:189], 0\n"
    "v_lshlrev_b32 v190, 3, v0\n"
    "v_lshrrev_b32 v186, 5, v1\n"
    "ds_write_b64 v190, v[188:189] offset:36864\n"
    "v_lshrrev_b32 v184, 4, v1\n"
    "v_and_b32 v185, 15, v0\n"
    "v_lshrrev_b32 v190, 2, v185\n"
    "v_lshl_add_u32 v64, v184, 3, v190\n"
    "v_add_u32 v65, 4, v64\n"
    "s_lshr_b32 s21, s20, 2\n"
    "s_and_b32 s43, s20, 3\n"
    "s_mul_i32 s40, s21, 13\n"
    "s_lshl2_add_u32 s40, s43, s40\n"
    "v_and_b32 v191, 3, v0\n"
    "v_add_lshl_u32 v188, v191, s40, 3\n"
    "v_min_u32 v64, 25, v64\n"
    "v_mad_u32_u24 v64, v64, s42, v188\n"
    "v_min_u32 v65, 25, v65\n"
    "v_mad_u32_u24 v65, v65, s42, v188\n"
    "v_lshl_add_u32 v188, s43, 4, v185\n"
    "v_cmp_lt_u32 vcc, 25, v188\n"
    "v_cmp_lt_u32 s[60:61], 51, v188\n"
    "v_lshl_add_u32 v66, s21, 6, v188\n"
    "v_cndmask_b32_e64 v189, 0, 6, vcc\n"
    "v_add_lshl_u32 v66, v66, v189, 1\n"
    "v_mov_b32 v189, 0x40000\n"
    "v_cmp_gt_u32 vcc, 26, v1\n"
    "v_cndmask_b32 v66, v66, v189, s[60:61]\n"
    "v_lshlrev_b32 v67, 6, v1\n"
    "v_cndmask_b32 v67, v189, v67, vcc\n"
    "v_lshlrev_b32 v68, 4, v184\n"
    "v_add_u32 v68, 40960, v68\n"
    "v_lshlrev_b32 v69, 1, v1\n"
    "v_lshl_add_u32 v190, v186, 3, v190\n"
    "v_and_b32 v188, 1, v184\n"
    "v_lshl_add_u32 v188, v188, 2, v191\n"
    "v_cmp_eq_u32 vcc, 7, v188\n"
    "v_lshlrev_b32 v188, 3, v188\n"
    "v_lshl_add_u32 v70, v190, 6, v188\n"
    "v_add_u32 v71, s28, v70\n"
    "v_mov_b32 v188, 38912\n"
    "v_cndmask_b32 v71, v71, v188, vcc\n"
    "v_cmp_gt_u32 vcc, 3, v184\n"
    "s_waitcnt vmcnt(5) lgkmcnt(0)\n"
    "s_barrier\n"
    "ds_read2_b64 v[176:179], v68 offset0:13 offset1:14\n"
    "ds_read_u16 v68, v69 offset:41012\n"
    "ds_read_u16 v69, v69 offset:40960\n"
    "ds_read_b64_tr_b16 v[112:113], v64 offset:0\n"
    "ds_read_b64_tr_b16 v[114:115], v65 offset:0\n"
    "ds_read_b64_tr_b16 v[116:117], v64 offset:208\n"
    "ds_read_b64_tr_b16 v[118:119], v65 offset:208\n"
    "ds_read_b64_tr_b16 v[120:121], v64 offset:416\n"
    "ds_read_b64_tr_b16 v[122:123], v65 offset:416\n"
    "ds_read_b64_tr_b16 v[124:125], v64 offset:624\n"
    "ds_read_b64_tr_b16 v[126:127], v65 offset:624\n"
    "ds_read_b64_tr_b16 v[128:129], v64 offset:832\n"
    "ds_read_b64_tr_b16 v[130:131], v65 offset:832\n"
    "ds_read_b64_tr_b16 v[132:133], v64 offset:1040\n"
    "ds_read_b64_tr_b16 v[134:135], v65 offset:1040\n"
    "ds_read_b64_tr_b16 v[136:137], v64 offset:1248\n"
    "ds_read_b64_tr_b16 v[138:139], v65 offset:1248\n"
    "s_waitcnt lgkmcnt(14)\n"
    "v_cndmask_b32 v177, 0, v177, vcc\n"
    "v_cndmask_b32 v178, 0, v178, vcc\n"
    "v_cndmask_b32 v179, 0, v179, vcc\n"
    "ds_write_b16 v67, v68 offset:36920\n"
    "s_waitcnt lgkmcnt(13)\n"
    "v_mfma_f32_16x16x32_bf16 v[144:147], v[176:179], v[112:115], 0\n"
    "s_waitcnt lgkmcnt(11)\n"
    "v_mfma_f32_16x16x32_bf16 v[148:151], v[176:179], v[116:119], 0\n"
    "s_waitcnt lgkmcnt(9)\n"
    "v_mfma_f32_16x16x32_bf16 v[152:155], v[176:179], v[120:123], 0\n"
    "s_waitcnt lgkmcnt(7)\n"
    "v_mfma_f32_16x16x32_bf16 v[156:159], v[176:179], v[124:127], 0\n"
    "s_waitcnt lgkmcnt(5)\n"
    "v_mfma_f32_16x16x32_bf16 v[160:163], v[176:179], v[128:131], 0\n"
    "s_waitcnt lgkmcnt(3)\n"
    "v_mfma_f32_16x16x32_bf16 v[164:167], v[176:179], v[132:135], 0\n"
    "s_waitcnt lgkmcnt(1)\n"
    "v_mfma_f32_16x16x32_bf16 v[168:171], v[176:179], v[136:139], 0\n"
    "s_waitcnt vmcnt(3)\n"
    "ds_read_b64_tr_b16 v[80:81], v71 offset:0\n"
    "ds_read_b64_tr_b16 v[82:83], v71 offset:256\n"
    "ds_read_b64_tr_b16 v[84:85], v71 offset:1024\n"
    "ds_read_b64_tr_b16 v[86:87], v71 offset:1280\n"
    "v_cvt_pk_bf16_f32 v144, v144, v144\n"
    "ds_write_b16 v66, v144 offset:36864\n"
    "v_cvt_pk_bf16_f32 v148, v148, v148\n"
    "ds_write_b16 v66, v148 offset:37120\n"
    "v_cvt_pk_bf16_f32 v152, v152, v152\n"
    "ds_write_b16 v66, v152 offset:37376\n"
    "v_cvt_pk_bf16_f32 v156, v156, v156\n"
    "ds_write_b16 v66, v156 offset:37632\n"
    "v_cvt_pk_bf16_f32 v160, v160, v160\n"
    "ds_write_b16 v66, v160 offset:37888\n"
    "v_cvt_pk_bf16_f32 v164, v164, v164\n"
    "ds_write_b16 v66, v164 offset:38144\n"
    "s_cmp_lg_u32 s21, 0\n"
    "s_cbranch_scc1 .Lxd_on54\n"
    "v_cvt_pk_bf16_f32 v168, v168, v168\n"
    "ds_write_b16 v66, v168 offset:38400\n"
    ".Lxd_on54:\n"
    "v_lshlrev_b32 v177, 16, v69\n"
    "s_waitcnt lgkmcnt(0)\n"
    "s_barrier\n"
    "ds_read_b64_tr_b16 v[72:73], v70 offset:36864\n"
    "ds_read_b64_tr_b16 v[74:75], v70 offset:37120\n"
    "ds_read_b64_tr_b16 v[76:77], v70 offset:37888\n"
    "ds_read_b64_tr_b16 v[78:79], v70 offset:38144\n"
    "s_lshl_b32 s30, s20, 12\n"
    "s_add_u32 s30, s30, 65536\n"
    "v_and_b32 v188, 31, v0\n"
    "v_mul_u32_u24 v192, 528, v186\n"
    "v_lshl_add_u32 v192, v188, 2, v192\n"
    "v_add_u32 v192, s30, v192\n"
    "s_lshl_b32 s40, s35, 6\n"
    "s_add_u32 s40, s40, s20\n"
    "s_lshl1_add_u32 s41, s20, s35\n"
    "s_add_u32 s41, s41, 120\n"
    "s_cmp_ge_u32 s20, 4\n"
    "s_cselect_b32 s40, s41, s40\n"
    "s_lshl1_add_u32 s41, s35, 384\n"
    "s_cmp_ge_u32 s20, 6\n"
    "s_cselect_b32 s40, s41, s40\n"
    "s_cselect_b32 s42, 1, 192\n"
    "s_and_b32 s41, s2, 3\n"
    "s_lshl2_add_u32 s40, s41, s40\n"
    "v_lshrrev_b32 v188, 3, v1\n"
    "v_mul_u32_u24 v188, 448, v188\n"
    "v_bfe_u32 v189, v0, 2, 1\n"
    "v_mad_u32_u24 v188, v189, s42, v188\n"
    "v_and_b32 v189, 48, v42\n"
    "v_add3_u32 v188, v188, v189, s40\n"
    "v_lshlrev_b32 v193, 4, v188\n"
    "s_waitcnt vmcnt(0) lgkmcnt(0)\n"
    ".Lxd_bD:\n"
    "v_mfma_f32_32x32x16_bf16 v[96:111], v[72:75], v[80:83], 0\n"
    "v_mfma_f32_32x32x16_bf16 v[96:111], v[80:83], v[72:75], v[96:111]\n"
    "v_mfma_f32_32x32x16_bf16 v[96:111], v[76:79], v[84:87], v[96:111]\n"
    "v_mfma_f32_32x32x16_bf16 v[96:111], v[84:87], v[76:79], v[96:111]\n"
    "v_readlane_b32 s43, v177, s34\n"
    "v_mad_u32_u16 v64, v44, 4, s30 op_sel:[0,0,0,0]\n"
    "v_mad_u32_u16 v65, v44, 4, s30 op_sel:[1,0,0,0]\n"
    "v_mad_u32_u16 v66, v45, 4, s30 op_sel:[0,0,0,0]\n"
    "v_mad_u32_u16 v67, v45, 4, s30 op_sel:[1,0,0,0]\n"
    "v_mad_u32_u16 v68, v46, 4, s30 op_sel:[0,0,0,0]\n"
    "v_mad_u32_u16 v69, v46, 4, s30 op_sel:[1,0,0,0]\n"
    "v_mad_u32_u16 v70, v47, 4, s30 op_sel:[0,0,0,0]\n"
    "v_mad_u32_u16 v71, v47, 4, s30 op_sel:[1,0,0,0]\n"
    "v_mov_b32 v184, 40944\n"
    "s_and_b32 s25, s25, 0xffffff\n"
    "s_mul_i32 s40, s25, 0x9e3779b1\n"
    "s_xor_b32 s41, s25, 0xa5c30f96\n"
    "s_lshr_b32 s40, s40, 24\n"
    "v_writelane_b32 v108, s43, 60\n"
    "ds_write2_b32 v192, v96, v97 offset0:0 offset1:33\n"
    "ds_write2_b32 v192, v98, v99 offset0:66 offset1:99\n"
    "v_add_u32 v192, 1056, v192\n"
    "ds_write2_b32 v192, v100, v101 offset0:0 offset1:33\n"
    "ds_write2_b32 v192, v102, v103 offset0:66 offset1:99\n"
    "v_add_u32 v192, 1056, v192\n"
    "ds_write2_b32 v192, v104, v105 offset0:0 offset1:33\n"
    "ds_write2_b32 v192, v106, v107 offset0:66 offset1:99\n"
    "v_add_u32 v192, 1056, v192\n"
    "ds_write2_b32 v192, v108, v109 offset0:0 offset1:33\n"
    "ds_read_b32 v56, v64\n"
    "ds_read_b32 v57, v65\n"
    "ds_read_b32 v58, v66\n"
    "ds_read_b32 v59, v67\n"
    "ds_read_b32 v60, v68\n"
    "ds_read_b32 v61, v69\n"
    "ds_read_b32 v62, v70\n"
    "ds_read_b32 v63, v71\n"
    "s_waitcnt lgkmcnt(6)\n"
    "v_pk_mul_f32 v[56:57], v[56:57], v[48:49]\n"
    "v_max3_f32 v188, |v56|, |v57|, 0\n"
    "s_waitcnt lgkmcnt(4)\n"
    "v_pk_mul_f32 v[58:59], v[58:59], v[50:51]\n"
    "v_max3_f32 v188, |v58|, |v59|, v188\n"
    "s_waitcnt lgkmcnt(2)\n"
    "v_pk_mul_f32 v[60:61], v[60:61], v[52:53]\n"
    "v_max3_f32 v188, |v60|, |v61|, v188\n"
    "s_waitcnt lgkmcnt(0)\n"
    "v_pk_mul_f32 v[62:63], v[62:63], v[54:55]\n"
    "v_max3_f32 v188, |v62|, |v63|, v188\n"
    "s_nop 1\n"
    "v_max_u32_dpp v188, v188, v188 quad_perm:[1,0,3,2] row_mask:0xf bank_mask:0xf\n"
    "s_nop 1\n"
    "v_max_u32_dpp v188, v188, v188 quad_perm:[2,3,0,1] row_mask:0xf bank_mask:0xf\n"
    "s_nop 1\n"
    "v_max_u32_dpp v188, v188, v188 row_half_mirror row_mask:0xf bank_mask:0xf\n"
    "s_nop 1\n"
    "v_max_u32_dpp v188, v188, v188 row_mirror row_mask:0xf bank_mask:0xf\n"
    "s_mov_b32 exec_lo, 0x10001\n"
    "s_mov_b32 exec_hi, 1\n"
    "ds_max_u32 v184, v188\n"
    "s_mov_b64 exec, -1\n"
    "s_add_u32 s38, s16, 1047552\n"
    "s_addc_u32 s39, s17, 0\n"
    "s_cmp_lt_u32 s23, 26\n"
    "s_cselect_b32 s43, 0xfff8fff8, 0\n"
    "s_waitcnt lgkmcnt(0)\n"
    "s_barrier\n"
    "ds_read_b32 v188, v184\n"
    "v_cmp_le_u32 vcc, 0x400, v193\n"
    "s_cmp_eq_u32 s20, 7\n"
    "s_cselect_b32 s42, 0, 48\n"
    "s_bfm_b64 s[60:61], s42, 0\n"
    "s_and_b64 s[60:61], s[60:61], vcc\n"
    "s_waitcnt lgkmcnt(0)\n"
    "v_lshrrev_b32 v188, 23, v188\n"
    "v_max_u32 v188, 40, v188\n"
    "v_min_u32 v188, 250, v188\n"
    "v_sub_u32 v190, 268, v188\n"
    "v_lshlrev_b32 v190, 23, v190\n"
    "v_pk_mul_f32 v[56:57], v[56:57], v[190:191] op_sel_hi:[1,0]\n"
    "v_cvt_pkrtz_f16_f32 v56, v56, v57\n"
    "v_add_u32 v56, 0x40004, v56\n"
    "v_and_b32 v56, s43, v56\n"
    "v_pk_mul_f32 v[58:59], v[58:59], v[190:191] op_sel_hi:[1,0]\n"
    "v_cvt_pkrtz_f16_f32 v57, v58, v59\n"
    "v_add_u32 v57, 0x40004, v57\n"
    "v_and_b32 v57, s43, v57\n"
    "v_pk_mul_f32 v[60:61], v[60:61], v[190:191] op_sel_hi:[1,0]\n"
    "v_cvt_pkrtz_f16_f32 v58, v60, v61\n"
    "v_add_u32 v58, 0x40004, v58\n"
    "v_and_b32 v58, s43, v58\n"
    "v_pk_mul_f32 v[62:63], v[62:63], v[190:191] op_sel_hi:[1,0]\n"
    "v_cvt_pkrtz_f16_f32 v59, v62, v63\n"
    "v_add_u32 v59, 0x40004, v59\n"
    "v_and_b32 v59, s43, v59\n"
    "v_lshl_or_b32 v188, v188, 8, s40\n"
    "v_mov_b32 v189, s41\n"
    "s_lshl3_add_u32 s40, s2, 40960\n"
    "v_mov_b32 v190, s40\n"
    "s_cmp_eq_u32 s20, 7\n"
    "s_cbranch_scc1 .Lxd_b3\n"
    "s_mov_b64 exec, s[60:61]\n"
    "global_store_dwordx4 v193, v[56:59], s[38:39] sc1\n"
    "s_mov_b64 exec, -1\n"
    ".Lxd_b3:\n"
    "s_waitcnt vmcnt(0)\n"
    "s_barrier\n"
    "s_cmp_lg_u32 s20, 0\n"
    "s_cbranch_scc1 .Lxd_on55\n"
    "s_mov_b64 exec, 1\n"
    "global_store_dwordx2 v190, v[188:189], s[16:17] sc1\n"
    "s_mov_b64 exec, -1\n"
    ".Lxd_on55:\n"
    "s_branch .Lxd_h1\n"
    ".Lxd_help:\n"
    "s_lshr_b32 s20, s43, 6\n"
    "v_and_b32 v1, 63, v0\n"
    "v_lshlrev_b32 v42, 4, v1\n"
    "s_lshr_b32 s41, s2, 3\n"
    "s_sub_u32 s41, s41, 1\n"
    "s_cmp_lg_u32 s20, 7\n"
    "s_cbranch_scc1 .Lxd_on57\n"
    "s_mul_i32 s40, s41, 64\n"
    "v_min_u32 v7, 63, v1\n"
    "v_add_lshl_u32 v7, v7, s40, 7\n"
    "s_getpc_b64 s[60:61]\n"
    ".Lxd_pc56:\n"
    "s_sub_u32 s60, s60, .Lxd_pc56-.Lxd_top\n"
    "s_subb_u32 s61, s61, 0\n"
    "v_min_u32 v7, .Lxd_end-.Lxd_top-4, v7\n"
    "global_load_dword v7, v7, s[60:61]\n"
    ".Lxd_on57:\n"
    "v_mul_u32_u24 v68, 52, v1\n"
    "v_lshrrev_b32 v68, 8, v68\n"
    "v_mad_i32_i24 v69, v68, -5, v1\n"
    "s_and_b32 s40, s2, 0xf8\n"
    "s_add_u32 s40, s40, s20\n"
    "s_mul_i32 s40, s40, 13\n"
    "s_sub_u32 s40, s40, 104\n"
    "v_add_u32 v68, s40, v68\n"
    "v_min_u32 v68, 0x1ff, v68\n"
    "v_lshrrev_b32 v70, 8, v68\n"
    "s_movk_i32 s40, 1792\n"
    "v_mad_u32_u24 v68, v70, s40, v68\n"
    "s_and_b32 s40, s2, 7\n"
    "v_lshl_add_u32 v68, s40, 8, v68\n"
    "v_mul_u32_u24 v68, 104, v68\n"
    "s_mul_i32 s40, s40, 3\n"
    "v_add3_u32 v70, v69, s40, 2\n"
    "v_subrev_u32 v71, 24, v70\n"
    "v_min_u32 v70, v70, v71\n"
    "v_lshl_add_u32 v70, v70, 2, v68\n"
    "s_waitcnt lgkmcnt(0)\n"
    "s_load_dwordx4 s[68:71], s[0:1], 64\n"
    "global_load_dword v60, v70, s[4:5]\n"
    "v_add3_u32 v70, v69, s40, 7\n"
    "v_subrev_u32 v71, 24, v70\n"
    "v_min_u32 v70, v70, v71\n"
    "v_lshl_add_u32 v70, v70, 2, v68\n"
    "global_load_dword v61, v70, s[4:5]\n"
    "v_add3_u32 v70, v69, s40, 12\n"
    "v_subrev_u32 v71, 24, v70\n"
    "v_min_u32 v70, v70, v71\n"
    "v_lshl_add_u32 v70, v70, 2, v68\n"
    "global_load_dword v62, v70, s[4:5]\n"
    "v_add3_u32 v70, v69, s40, 17\n"
    "v_subrev_u32 v71, 24, v70\n"
    "v_min_u32 v70, v70, v71\n"
    "v_lshl_add_u32 v70, v70, 2, v68\n"
    "global_load_dword v63, v70, s[4:5]\n"
    "v_add3_u32 v70, v69, s40, 22\n"
    "v_subrev_u32 v71, 24, v70\n"
    "v_min_u32 v70, v70, v71\n"
    "v_add_u32 v71, 22, v69\n"
    "v_cmp_lt_u32 vcc, 23, v71\n"
    "v_min_u32 v71, 25, v71\n"
    "s_nop 0\n"
    "v_cndmask_b32 v70, v70, v71, vcc\n"
    "v_lshl_add_u32 v70, v70, 2, v68\n"
    "global_load_dword v64, v70, s[4:5]\n"
    "s_cmp_lg_u32 s20, 6\n"
    "s_cbranch_scc1 .Lxd_on58\n"
    "v_min_u32 v12, 25, v1\n"
    "s_mov_b32 m0, 159744\n"
    "v_lshlrev_b32 v12, 7, v12\n"
    "global_load_lds_dword v12, s[10:11]\n"
    "global_load_lds_dword v12, s[18:19]\n"
    ".Lxd_on58:\n"
    ".Lxd_h1:\n"
    "s_lshr_b32 s3, s2, 4\n"
    "s_mul_i32 s40, s3, 3\n"
    "s_add_u32 s47, s2, s40\n"
    "s_and_b32 s47, s47, 15\n"
    "s_lshr_b32 s21, s20, 2\n"
    "s_lshl1_add_u32 s22, s21, s20\n"
    "s_and_b32 s22, s22, 3\n"
    "s_lshl_b32 s58, s21, 8\n"
    "s_add_u32 s58, s58, 150528\n"
    "s_mov_b32 s40, s47\n"
    "s_mul_i32 s54, s40, 512\n"
    "s_add_u32 s54, s54, 2\n"
    "s_lshl_b32 s40, s40, 7\n"
    "s_add_u32 s40, s40, 32768\n"
    "s_add_u32 s52, s16, s40\n"
    "s_addc_u32 s53, s17, 0\n"
    "s_mov_b32 s57, 0x7fff7fff\n"
    "s_mov_b32 s56, 0\n"
    "s_mov_b32 s27, 0\n"
    "s_mov_b32 s55, 9\n"
    "s_mov_b32 s44, 0\n"
    "s_mov_b32 s46, 0\n"
    "v_lshrrev_b32 v17, 3, v1\n"
    "v_mul_u32_u24 v17, 12, v17\n"
    "v_and_b32 v19, 7, v1\n"
    "v_lshlrev_b32 v19, 4, v19\n"
    "v_lshrrev_b32 v18, 5, v1\n"
    "v_lshlrev_b32 v18, 2, v18\n"
    "v_and_b32 v20, 31, v1\n"
    "v_lshlrev_b32 v20, 2, v20\n"
    "s_lshl2_add_u32 s40, s22, s58\n"
    "s_sub_u32 s40, s40, 20\n"
    "v_lshl_add_u32 v25, v1, 18, s40\n"
    "s_lshl1_add_u32 s23, s3, s21\n"
    "s_mul_i32 s23, s23, 6\n"
    "s_add_u32 s23, s23, s22\n"
    "s_add_u32 s23, s23, 78\n"
    "v_mov_b32 v24, s23\n"
    "v_lshlrev_b32 v13, 2, v0\n"
    "v_readfirstlane_b32 s40, v24\n"
    "s_or_b32 s40, s40, s44\n"
    "s_lshl1_add_u32 s41, s40, s54\n"
    "s_cmpk_lt_u32 s40, 256\n"
    "s_cselect_b32 s41, s41, 2\n"
    "v_mov_b32 v12, s41\n"
    "s_mul_i32 s41, s41, 52\n"
    "s_add_u32 s38, s4, s41\n"
    "s_addc_u32 s39, s5, 0\n"
    "global_load_dwordx3 v[36:38], v17, s[38:39] offset:-104\n"
    "global_load_dword v41, v18, s[38:39] offset:-8\n"
    "s_setprio 0\n"
    "v_mov_b32 v21, 0\n"
    "v_add_u32 v13, 149504, v13\n"
    "ds_write_b32 v13, v21\n"
    "s_cmp_lt_u32 s2, 8\n"
    "s_cbranch_scc1 .Lxd_hp59\n"
    "s_waitcnt vmcnt(2)\n"
    "s_mov_b32 m0, 159744\n"
    "v_lshlrev_b32 v60, 7, v60\n"
    "global_load_lds_dword v60, s[8:9]\n"
    "s_mov_b32 m0, 159744\n"
    "v_lshlrev_b32 v61, 7, v61\n"
    "global_load_lds_dword v61, s[8:9]\n"
    "s_waitcnt vmcnt(2)\n"
    "s_branch .Lxd_h3\n"
    ".Lxd_hp59:\n"
    "s_waitcnt vmcnt(0)\n"
    ".Lxd_h3:\n"
    "v_lshl_or_b32 v36, v36, 7, v19\n"
    "v_lshl_or_b32 v37, v37, 7, v19\n"
    "v_lshl_or_b32 v38, v38, 7, v19\n"
    "v_lshl_or_b32 v41, v41, 7, v20\n"
    "global_load_dwordx4 v[28:31], v36, s[8:9]\n"
    "global_load_dwordx4 v[32:35], v37, s[8:9]\n"
    "global_load_dword v40, v41, s[8:9]\n"
    "global_load_dwordx4 v[36:39], v38, s[8:9]\n"
    "s_waitcnt lgkmcnt(0)\n"
    "s_barrier\n"
    "ds_write_b32 v25, v12 offset:20\n"
    "s_load_dword s26, s[68:69], 0\n"
    "s_lshl_b32 s43, s21, 16\n"
    "v_lshrrev_b32 v12, 4, v1\n"
    "v_bfe_u32 v13, v1, 2, 2\n"
    "v_and_b32 v21, 3, v1\n"
    "s_mul_i32 s40, s22, 0x2020\n"
    "s_and_b32 s40, s40, 0x4060\n"
    "s_add_u32 s40, s40, s43\n"
    "v_lshl_add_u32 v21, v21, 3, s40\n"
    "s_movk_i32 s41, 144\n"
    "v_add_u32 v22, v12, v13\n"
    "v_and_b32 v22, 3, v22\n"
    "v_mad_u32_u24 v4, v22, s41, v21\n"
    "v_cmp_gt_u32 vcc, 2, v13\n"
    "v_add_u32 v24, 22, v13\n"
    "v_lshlrev_b32 v13, 3, v13\n"
    "v_cndmask_b32 v22, v24, v22, vcc\n"
    "v_mad_u32_u24 v3, v22, s41, v21\n"
    "v_cmp_eq_u32 vcc, 3, v12\n"
    "v_xor_b32 v22, 0, v12\n"
    "v_and_b32 v22, 1, v22\n"
    "v_lshlrev_b32 v22, 1, v22\n"
    "v_lshl_add_u32 v22, v12, 3, v22\n"
    "s_mov_b32 s40, 0x05040100\n"
    "v_bfe_u32 v24, s40, v13, 8\n"
    "v_add_u32 v22, v22, v24\n"
    "s_mov_b32 s40, 0x1a191818\n"
    "v_bfe_u32 v24, s40, v13, 8\n"
    "v_cndmask_b32 v22, v22, v24, vcc\n"
    "v_mad_u32_u24 v5, v22, s41, v21\n"
    "v_xor_b32 v22, 1, v12\n"
    "v_and_b32 v22, 1, v22\n"
    "v_lshlrev_b32 v22, 1, v22\n"
    "v_lshl_add_u32 v22, v12, 3, v22\n"
    "s_mov_b32 s40, 0x05040100\n"
    "v_bfe_u32 v24, s40, v13, 8\n"
    "v_add_u32 v22, v22, v24\n"
    "s_mov_b32 s40, 0x1a191918\n"
    "v_bfe_u32 v24, s40, v13, 8\n"
    "v_cndmask_b32 v22, v22, v24, vcc\n"
    "v_mad_u32_u24 v6, v22, s41, v21\n"
    "v_bfrev_b32 v14, v12\n"
    "v_lshrrev_b32 v14, 26, v14\n"
    "s_add_u32 s40, s22, 3\n"
    "s_and_b32 s40, s40, 3\n"
    "s_lshl2_add_u32 s40, s40, s22\n"
    "s_add_u32 s40, s40, s58\n"
    "s_lshl_b32 s40, s40, 2\n"
    "s_add_u32 s40, s40, -450048\n"
    "v_add_u32 v14, s40, v14\n"
    "v_and_b32 v2, 1, v1\n"
    "v_mul_u32_u24 v2, 0xaaaa, v2\n"
    "v_add_u32 v2, 0x4444, v2\n"
    "v_mad_u32_u24 v15, v17, 36, v19\n"
    "v_mad_u32_u24 v16, v18, 36, v20\n"
    "s_add_u32 s41, s22, 2\n"
    "v_lshrrev_b32 v12, 5, v19\n"
    "v_sub_u32 v12, 1, v12\n"
    "v_and_b32 v12, 3, v12\n"
    "v_add_lshl_u32 v12, v12, s41, 12\n"
    "v_and_b32 v12, 0x7000, v12\n"
    "v_add3_u32 v15, v12, v15, s43\n"
    "v_lshrrev_b32 v12, 5, v20\n"
    "v_sub_u32 v12, 1, v12\n"
    "v_and_b32 v12, 3, v12\n"
    "v_add_lshl_u32 v12, v12, s41, 12\n"
    "v_and_b32 v12, 0x7000, v12\n"
    "v_add3_u32 v16, v12, v16, s43\n"
    "v_add_u32 v16, 3456, v16\n"
    "s_lshl3_add_u32 s40, s2, 4096\n"
    "s_lshl_b32 s41, s2, 2\n"
    "v_lshlrev_b32 v11, 3, v1\n"
    "v_add_u32 v11, 40960, v11\n"
    "v_writelane_b32 v11, s40, 8\n"
    "v_writelane_b32 v11, s41, 9\n"
    "s_getpc_b64 s[10:11]\n"
    ".Lxd_en60:\n"
    "s_mov_b32 s40, .Lxd_ph1-.Lxd_en60\n"
    "s_cmp_eq_u32 s22, 1\n"
    "s_cselect_b32 s40, .Lxd_ph0-.Lxd_en60, s40\n"
    "s_cmp_eq_u32 s22, 2\n"
    "s_cselect_b32 s40, .Lxd_pl3-.Lxd_en60, s40\n"
    "s_cmp_eq_u32 s22, 3\n"
    "s_cselect_b32 s40, .Lxd_pl2-.Lxd_en60, s40\n"
    "s_ashr_i32 s41, s40, 31\n"
    "s_add_u32 s10, s10, s40\n"
    "s_addc_u32 s11, s11, s41\n"
    "v_mov_b32 v68, v42\n"
    "v_add_u32 v69, 4096, v42\n"
    "v_add_u32 v42, 98304, v42\n"
    "v_bfe_u32 v70, v1, 4, 1\n"
    "v_lshlrev_b32 v70, 2, v70\n"
    "v_add_u32 v70, 154112, v70\n"
    "s_mov_b32 s36, 0\n"
    "v_mov_b64 v[88:89], 0\n"
    "v_mov_b64 v[90:91], 0\n"
    "s_waitcnt vmcnt(0)\n"
    "s_movk_i32 s43, 15744\n"
    "v_pk_maximum3_f16 v21, v28, v28, s43 op_sel_hi:[1,1,0] neg_lo:[0,1,0] neg_hi:[0,1,0]\n"
    "v_pk_maximum3_f16 v21, v29, v29, v21 neg_lo:[0,1,0] neg_hi:[0,1,0]\n"
    "v_pk_maximum3_f16 v21, v30, v30, v21 neg_lo:[0,1,0] neg_hi:[0,1,0]\n"
    "v_pk_maximum3_f16 v21, v31, v31, v21 neg_lo:[0,1,0] neg_hi:[0,1,0]\n"
    "v_pk_maximum3_f16 v21, v32, v32, v21 neg_lo:[0,1,0] neg_hi:[0,1,0]\n"
    "v_pk_maximum3_f16 v21, v33, v33, v21 neg_lo:[0,1,0] neg_hi:[0,1,0]\n"
    "v_pk_maximum3_f16 v21, v34, v34, v21 neg_lo:[0,1,0] neg_hi:[0,1,0]\n"
    "v_pk_maximum3_f16 v21, v35, v35, v21 neg_lo:[0,1,0] neg_hi:[0,1,0]\n"
    "v_pk_maximum3_f16 v21, v36, v36, v21 neg_lo:[0,1,0] neg_hi:[0,1,0]\n"
    "v_pk_maximum3_f16 v21, v37, v37, v21 neg_lo:[0,1,0] neg_hi:[0,1,0]\n"
    "v_pk_maximum3_f16 v21, v38, v38, v21 neg_lo:[0,1,0] neg_hi:[0,1,0]\n"
    "v_pk_maximum3_f16 v21, v39, v39, v21 neg_lo:[0,1,0] neg_hi:[0,1,0]\n"
    "v_pk_maximum3_f16 v21, v40, v40, v21 neg_lo:[0,1,0] neg_hi:[0,1,0]\n"
    "v_pk_max_u16 v21, v21, v21 op_sel:[0,1] op_sel_hi:[1,0]\n"
    "s_nop 1\n"
    "v_max_u32_dpp v21, v21, v21 quad_perm:[1,0,3,2] row_mask:0xf bank_mask:0xf\n"
    "s_nop 1\n"
    "v_max_u32_dpp v21, v21, v21 quad_perm:[2,3,0,1] row_mask:0xf bank_mask:0xf\n"
    "s_nop 1\n"
    "v_max_u32_dpp v21, v21, v21 row_half_mirror row_mask:0xf bank_mask:0xf\n"
    "s_nop 1\n"
    "v_max_u32_dpp v21, v21, v21 row_mirror row_mask:0xf bank_mask:0xf\n"
    "s_nop 1\n"
    "v_max_u32_dpp v21, v21, v21 row_bcast:15 row_mask:0xa bank_mask:0xf\n"
    "s_nop 1\n"
    "v_max_u32_dpp v21, v21, v21 row_bcast:31 row_mask:0xc bank_mask:0xf\n"
    "s_nop 1\n"
    "v_readlane_b32 s47, v21, 63\n"
    "s_and_b32 s47, s47, 0x7f80\n"
    "s_sub_u32 s50, s47, 2816\n"
    "s_mul_i32 s51, s50, -8\n"
    "s_lshr_b32 s47, s47, 3\n"
    "v_pk_maximum3_f16 v21, v28, v28, s50 op_sel_hi:[1,1,0] neg_lo:[0,1,0] neg_hi:[0,1,0]\n"
    "v_pk_mad_u16 v21, v21, 8, s51 op_sel_hi:[1,0,0]\n"
    "v_bfi_b32 v28, s57, v21, v28\n"
    "v_pk_maximum3_f16 v12, v29, v29, s50 op_sel_hi:[1,1,0] neg_lo:[0,1,0] neg_hi:[0,1,0]\n"
    "v_pk_mad_u16 v12, v12, 8, s51 op_sel_hi:[1,0,0]\n"
    "v_bfi_b32 v29, s57, v12, v29\n"
    "v_pk_maximum3_f16 v21, v30, v30, s50 op_sel_hi:[1,1,0] neg_lo:[0,1,0] neg_hi:[0,1,0]\n"
    "v_pk_mad_u16 v21, v21, 8, s51 op_sel_hi:[1,0,0]\n"
    "v_bfi_b32 v30, s57, v21, v30\n"
    "v_pk_maximum3_f16 v12, v31, v31, s50 op_sel_hi:[1,1,0] neg_lo:[0,1,0] neg_hi:[0,1,0]\n"
    "v_pk_mad_u16 v12, v12, 8, s51 op_sel_hi:[1,0,0]\n"
    "v_bfi_b32 v31, s57, v12, v31\n"
    "v_pk_maximum3_f16 v21, v32, v32, s50 op_sel_hi:[1,1,0] neg_lo:[0,1,0] neg_hi:[0,1,0]\n"
    "v_pk_mad_u16 v21, v21, 8, s51 op_sel_hi:[1,0,0]\n"
    "v_bfi_b32 v32, s57, v21, v32\n"
    "v_pk_maximum3_f16 v12, v33, v33, s50 op_sel_hi:[1,1,0] neg_lo:[0,1,0] neg_hi:[0,1,0]\n"
    "v_pk_mad_u16 v12, v12, 8, s51 op_sel_hi:[1,0,0]\n"
    "v_bfi_b32 v33, s57, v12, v33\n"
    "s_cmp_lg_u32 s20, 7\n"
    "s_cbranch_scc1 .Lxd_on61\n"
    "s_mov_b64 exec, 0x3ff\n"
    "global_load_dwordx2 v[26:27], v11, s[16:17] sc1\n"
    "s_mov_b64 exec, -1\n"
    ".Lxd_on61:\n"
    "v_pk_maximum3_f16 v21, v34, v34, s50 op_sel_hi:[1,1,0] neg_lo:[0,1,0] neg_hi:[0,1,0]\n"
    "v_pk_mad_u16 v21, v21, 8, s51 op_sel_hi:[1,0,0]\n"
    "v_bfi_b32 v34, s57, v21, v34\n"
    "v_pk_maximum3_f16 v12, v35, v35, s50 op_sel_hi:[1,1,0] neg_lo:[0,1,0] neg_hi:[0,1,0]\n"
    "v_pk_mad_u16 v12, v12, 8, s51 op_sel_hi:[1,0,0]\n"
    "v_bfi_b32 v35, s57, v12, v35\n"
    "v_pk_maximum3_f16 v21, v36, v36, s50 op_sel_hi:[1,1,0] neg_lo:[0,1,0] neg_hi:[0,1,0]\n"
    "v_pk_mad_u16 v21, v21, 8, s51 op_sel_hi:[1,0,0]\n"
    "v_bfi_b32 v36, s57, v21, v36\n"
    "v_pk_maximum3_f16 v12, v37, v37, s50 op_sel_hi:[1,1,0] neg_lo:[0,1,0] neg_hi:[0,1,0]\n"
    "v_pk_mad_u16 v12, v12, 8, s51 op_sel_hi:[1,0,0]\n"
    "v_bfi_b32 v37, s57, v12, v37\n"
    "v_pk_maximum3_f16 v21, v38, v38, s50 op_sel_hi:[1,1,0] neg_lo:[0,1,0] neg_hi:[0,1,0]\n"
    "v_pk_mad_u16 v21, v21, 8, s51 op_sel_hi:[1,0,0]\n"
    "v_bfi_b32 v38, s57, v21, v38\n"
    "v_pk_maximum3_f16 v12, v39, v39, s50 op_sel_hi:[1,1,0] neg_lo:[0,1,0] neg_hi:[0,1,0]\n"
    "v_pk_mad_u16 v12, v12, 8, s51 op_sel_hi:[1,0,0]\n"
    "v_bfi_b32 v39, s57, v12, v39\n"
    "v_pk_maximum3_f16 v21, v40, v40, s50 op_sel_hi:[1,1,0] neg_lo:[0,1,0] neg_hi:[0,1,0]\n"
    "v_pk_mad_u16 v21, v21, 8, s51 op_sel_hi:[1,0,0]\n"
    "v_bfi_b32 v40, s57, v21, v40\n"
    "s_sub_u32 s66, 4112, s47\n"
    "s_lshl_b32 s66, s66, 19\n"
    "v_cvt_pkrtz_f16_f32 v12, s66, s66\n"
    "v_lshl_or_b32 v21, v12, 16, s47\n"
    "ds_write_b128 v15, v[28:31] offset:0\n"
    "ds_write_b128 v15, v[32:35] offset:144\n"
    "ds_write_b128 v15, v[36:39] offset:288\n"
    "ds_write2_b32 v16, v40, v12 offset1:72\n"
    "ds_or_b32 v25, v21 offset:532\n"
    "s_cmp_lg_u32 s20, 7\n"
    "s_cbranch_scc1 .Lxd_on63\n"
    "s_waitcnt vmcnt(0)\n"
    "v_readlane_b32 s25, v26, 9\n"
    "v_readlane_b32 s40, v26, 8\n"
    "v_readlane_b32 s41, v27, 8\n"
    "s_mul_i32 s42, s25, 0x9e3779b1\n"
    "s_lshr_b32 s42, s42, 24\n"
    "s_xor_b32 s42, s42, s40\n"
    "s_and_b32 s42, s42, 0xff\n"
    "s_xor_b32 s41, s41, s25\n"
    "s_xor_b32 s41, s41, 0xa5c30f96\n"
    "s_or_b32 s42, s41, s42\n"
    "s_lshr_b32 s41, s40, 8\n"
    "s_mul_i32 s40, s41, 0x9e3779b1\n"
    "s_xor_b32 s41, s41, 0xa5c30f96\n"
    "s_lshr_b32 s40, s40, 24\n"
    "v_xor_b32 v13, s40, v26\n"
    "v_xor_b32 v22, s41, v27\n"
    "v_lshl_or_b32 v13, v13, 24, v22\n"
    "v_cmp_ne_u32 s[66:67], 0, v13\n"
    "s_and_b32 s66, s66, 0xff\n"
    "s_or_b32 s42, s42, s66\n"
    "s_cmp_eq_u32 s20, 7\n"
    "s_cselect_b32 s42, s42, 0\n"
    "s_cmp_lg_u32 s42, 0\n"
    "s_cbranch_scc1 .Lxd_pv62\n"
    ".Lxd_pv62r:\n"
    "s_mov_b64 s[64:65], s[40:41]\n"
    ".Lxd_on63:\n"
    "s_waitcnt lgkmcnt(0)\n"
    "s_barrier\n"
    "s_mul_i32 s42, s20, 5376\n"
    "s_add_u32 s40, s42, 1048576\n"
    "s_add_u32 s38, s16, s40\n"
    "s_addc_u32 s39, s17, 0\n"
    "s_add_u32 m0, s42, 98304\n"
    "s_nop 0\n"
    "global_load_lds_dwordx4 v68, s[38:39] offset:0 sc1\n"
    "global_load_lds_dwordx4 v68, s[38:39] offset:1024 sc1\n"
    "global_load_lds_dwordx4 v68, s[38:39] offset:2048 sc1\n"
    "global_load_lds_dwordx4 v68, s[38:39] offset:3072 sc1\n"
    "s_add_u32 m0, s42, 102400\n"
    "s_nop 0\n"
    "global_load_lds_dwordx4 v69, s[38:39] offset:0 sc1\n"
    "global_load_lds_dwordx4 v69, s[38:39] offset:256 sc1\n"
    "s_cmp_lg_u32 s20, 7\n"
    "s_cbranch_scc1 .Lxd_on64\n"
    "s_mov_b64 s[40:41], s[64:65]\n"
    "v_lshrrev_b32 v22, 8, v26\n"
    "v_mov_b32 v26, s40\n"
    "v_mov_b32 v27, s41\n"
    "v_add_u32_dpp v22, v22, v22 row_shl:4 row_mask:0xf bank_mask:0xf\n"
    "v_add_u32_e64 v43, s25, 1\n"
    "v_lshlrev_b32 v8, 2, v1\n"
    "v_max_u32_dpp v13, v22, v22 quad_perm:[1,0,3,2] row_mask:0xf bank_mask:0xf\n"
    "v_add_u32 v8, 154112, v8\n"
    "s_nop 0\n"
    "v_max_u32_dpp v13, v13, v13 quad_perm:[2,3,0,1] row_mask:0xf bank_mask:0xf\n"
    "v_sub_u32 v22, v22, v13\n"
    "v_subrev_u32 v13, 818, v13\n"
    "v_ldexp_f32 v22, 1.0, v22\n"
    "s_mov_b64 exec, 1\n"
    "ds_write_b64 v8, v[26:27]\n"
    "s_mov_b64 exec, 15\n"
    "ds_write2_b32 v8, v22, v13 offset0:4 offset1:8\n"
    "s_mov_b64 exec, -1\n"
    ".Lxd_on64:\n"
    "s_cmp_lt_u32 s22, 2\n"
    "s_cselect_b32 s40, 16, 0\n"
    "v_add_u32 v25, s40, v25\n"
    "s_add_u32 s23, s23, 4\n"
    "v_mov_b32 v24, s23\n"
    "v_mov_b32 v13, s58\n"
    "s_sub_u32 s40, 3, s22\n"
    "s_and_b32 s40, s40, 3\n"
    "v_lshl_add_u32 v7, s40, 12, v3\n"
    "v_lshl_add_u32 v8, s40, 12, v4\n"
    "v_lshl_add_u32 v9, s40, 12, v5\n"
    "v_lshl_add_u32 v10, s40, 12, v6\n"
    "ds_read_b32 v22, v13 offset:512\n"
    "ds_read_b64_tr_b16 v[44:45], v9 offset:0\n"
    "ds_read_b64_tr_b16 v[46:47], v10 offset:0\n"
    "ds_read_b64_tr_b16 v[48:49], v7 offset:0\n"
    "ds_read_b64_tr_b16 v[50:51], v8 offset:576\n"
    "ds_read_b64_tr_b16 v[52:53], v8 offset:1152\n"
    "ds_read_b64_tr_b16 v[54:55], v8 offset:1728\n"
    "ds_read_b64_tr_b16 v[56:57], v8 offset:2304\n"
    "ds_read_b64_tr_b16 v[58:59], v8 offset:2880\n"
    "s_cmp_lg_u32 s22, 0\n"
    "s_cbranch_scc1 .Lxd_on65\n"
    "v_readfirstlane_b32 s40, v24\n"
    "s_or_b32 s40, s40, s44\n"
    "s_lshl1_add_u32 s41, s40, s54\n"
    "s_cmpk_lt_u32 s40, 256\n"
    "s_cselect_b32 s41, s41, 2\n"
    "v_mov_b32 v12, s41\n"
    "s_mul_i32 s41, s41, 52\n"
    "s_add_u32 s38, s4, s41\n"
    "s_addc_u32 s39, s5, 0\n"
    "ds_write_b32 v25, v12 offset:20\n"
    "global_load_dwordx3 v[36:38], v17, s[38:39] offset:-104\n"
    "global_load_dword v41, v18, s[38:39] offset:-8\n"
    ".Lxd_on65:\n"
    "s_waitcnt vmcnt(0) lgkmcnt(0)\n"
    "s_barrier\n"
    "ds_read2_b32 v[26:27], v70 offset1:1\n"
    "ds_read_b32 v0, v70 offset:16\n"
    "ds_read_b32 v23, v70 offset:24\n"
    "s_cmp_lg_u32 s20, 7\n"
    "s_cbranch_scc1 .Lxd_on66\n"
    "s_mov_b64 exec, 0x200\n"
    "global_store_dword v11, v43, s[16:17] sc1\n"
    "s_mov_b64 exec, -1\n"
    ".Lxd_on66:\n"
    "ds_read_b128 v[92:95], v42 offset:0\n"
    "ds_read_b128 v[96:99], v42 offset:1024\n"
    "ds_read_b128 v[100:103], v42 offset:2048\n"
    "ds_read_b128 v[104:107], v42 offset:3072\n"
    "ds_read_b128 v[108:111], v42 offset:4096\n"
    "ds_read_b128 v[112:115], v42 offset:5120\n"
    "ds_read_b128 v[116:119], v42 offset:6144\n"
    "ds_read_b128 v[120:123], v42 offset:7168\n"
    "ds_read_b128 v[124:127], v42 offset:8192\n"
    "ds_read_b128 v[128:131], v42 offset:9216\n"
    "ds_read_b128 v[132:135], v42 offset:10240\n"
    "ds_read_b128 v[136:139], v42 offset:11264\n"
    "ds_read_b128 v[140:143], v42 offset:12288\n"
    "ds_read_b128 v[144:147], v42 offset:13312\n"
    "s_cmp_ge_u32 s2, 8\n"
    "s_cbranch_scc1 .Lxd_h4\n"
    "s_lshl3_add_u32 s40, s2, s20\n"
    "s_mul_i32 s40, s40, 52\n"
    "s_add_u32 s40, s40, 39936\n"
    "v_min_u32 v68, 51, v1\n"
    "s_mov_b32 m0, 159744\n"
    "v_add_lshl_u32 v68, v68, s40, 7\n"
    "global_load_lds_dword v68, s[6:7]\n"
    ".Lxd_h4:\n"
    "ds_read_b128 v[148:151], v42 offset:14336\n"
    "ds_read_b128 v[152:155], v42 offset:15360\n"
    "ds_read_b128 v[156:159], v42 offset:16384\n"
    "ds_read_b128 v[160:163], v42 offset:17408\n"
    "ds_read_b128 v[164:167], v42 offset:18432\n"
    "ds_read_b128 v[168:171], v42 offset:19456\n"
    "ds_read_b128 v[172:175], v42 offset:20480\n"
    "ds_read_b128 v[176:179], v42 offset:21504\n"
    "ds_read_b128 v[180:183], v42 offset:22528\n"
    "ds_read_b128 v[184:187], v42 offset:23552\n"
    "ds_read_b128 v[188:191], v42 offset:24576\n"
    "ds_read_b128 v[192:195], v42 offset:25600\n"
    "ds_read_b128 v[196:199], v42 offset:26624\n"
    "ds_read_b128 v[200:203], v42 offset:27648\n"
    "ds_read_b128 v[204:207], v42 offset:28672\n"
    "ds_read_b128 v[208:211], v42 offset:29696\n"
    "ds_read_b128 v[212:215], v42 offset:30720\n"
    "ds_read_b128 v[216:219], v42 offset:31744\n"
    "ds_read_b128 v[220:223], v42 offset:32768\n"
    "ds_read_b128 v[224:227], v42 offset:33792\n"
    "ds_read_b128 v[228:231], v42 offset:34816\n"
    "ds_read_b128 v[232:235], v42 offset:35840\n"
    "ds_read_b128 v[236:239], v42 offset:36864\n"
    "ds_read_b128 v[240:243], v42 offset:37888\n"
    "ds_read_b128 v[244:247], v42 offset:38912\n"
    "ds_read_b128 v[248:251], v42 offset:39936\n"
    "ds_read_b128 v[252:255], v42 offset:40960\n"
    "s_waitcnt lgkmcnt(15)\n"
    "v_readfirstlane_b32 s45, v22\n"
    "v_readfirstlane_b32 s28, v26\n"
    "v_readfirstlane_b32 s29, v27\n"
    "s_cmp_lt_u32 s2, 8\n"
    "s_cbranch_scc1 .Lxd_hp68\n"
    "s_mov_b32 m0, 159744\n"
    "s_mov_b32 s46, 3\n"
    "v_lshlrev_b32 v26, 7, v62\n"
    "v_lshlrev_b32 v27, 7, v63\n"
    "v_lshlrev_b32 v42, 7, v64\n"
    ".Lxd_hp68:\n"
    "v_pk_mul_f16 v64, v44, v46\n"
    "v_pk_mul_f16 v65, v45, v47\n"
    "v_pk_mul_f16 v66, v48, v48 op_sel_hi:[0,1]\n"
    "v_pk_mul_f16 v67, v48, v49 op_sel_hi:[0,1]\n"
    "v_pk_mul_f16 v60, v44, s45 op_sel:[0,1]\n"
    "v_pk_mul_f16 v61, v45, s45 op_sel:[0,1]\n"
    "v_pk_mul_f16 v62, v46, s45 op_sel:[0,1]\n"
    "v_pk_mul_f16 v63, v47, s45 op_sel:[0,1]\n"
    "s_waitcnt lgkmcnt(0)\n"
    "s_branch .Lxd_enter\n"
    ".Lxd_slow:\n"
    "s_cmp_eq_u32 s45, -1\n"
    "s_cbranch_scc1 .Lxd_sl7\n"
    "s_mov_b32 s59, 0x100000\n"
    "s_and_b32 s40, s45, 0xff0\n"
    "s_cbranch_scc0 .Lxd_sl1\n"
    ".Lxd_sl2:\n"
    "s_cmp_ge_i32 s45, -16\n"
    "s_cbranch_scc1 .Lxd_sl3\n"
    "s_bitset0_b32 s45, 31\n"
    ".Lxd_sl6:\n"
    "s_cmp_lg_u32 s56, 0\n"
    "s_cbranch_scc1 .Lxd_sl9\n"
    "v_readfirstlane_b32 s40, v13\n"
    "s_lshl4_add_u32 s41, s22, s58\n"
    "s_mov_b32 s56, 2\n"
    "s_cmp_lt_u32 s40, s41\n"
    "s_cbranch_scc0 .Lxd_gran\n"
    ".Lxd_sl9:\n"
    "s_setpc_b64 s[60:61]\n"
    ".Lxd_sl1:\n"
    "s_sleep 1\n"
    "ds_read_b32 v22, v13 offset:512\n"
    "s_waitcnt lgkmcnt(0)\n"
    "v_readfirstlane_b32 s45, v22\n"
    "s_sub_u32 s59, s59, 1\n"
    "s_cbranch_scc1 .Lxd_sl5\n"
    "s_and_b32 s40, s45, 0xff0\n"
    "s_cbranch_scc0 .Lxd_sl1\n"
    "s_cmp_gt_i32 s45, 0\n"
    "s_cbranch_scc0 .Lxd_sl2\n"
    "s_setpc_b64 s[60:61]\n"
    ".Lxd_sl3:\n"
    "v_and_b32 v13, 0x3ffff, v13\n"
    "v_lshl_add_u32 v13, v1, 2, v13\n"
    "s_mov_b64 exec, 7\n"
    ".Lxd_sl4:\n"
    "ds_read_b32 v22, v13 offset:4\n"
    "s_waitcnt lgkmcnt(0)\n"
    "v_cmp_eq_u32 s[40:41], 0, v22\n"
    "v_cmp_ne_u32 s[42:43], -1, v22\n"
    "s_sub_u32 s59, s59, 1\n"
    "s_cbranch_scc1 .Lxd_sl5\n"
    "s_cmp_lg_u32 s40, 0\n"
    "s_cbranch_scc1 .Lxd_sl4\n"
    "s_cmp_eq_u32 s42, 0\n"
    "s_cbranch_scc1 .Lxd_sl5\n"
    "s_mov_b64 exec, -1\n"
    "s_mov_b32 s45, 1\n"
    "s_mov_b32 s56, 0\n"
    "s_setpc_b64 s[60:61]\n"
    ".Lxd_sl5:\n"
    "s_mov_b64 exec, -1\n"
    "s_mov_b32 s45, -1\n"
    ".Lxd_sl7:\n"
    "s_cmp_lg_u32 s56, 0\n"
    "s_cbranch_scc1 .Lxd_sl9\n"
    "s_mov_b64 s[62:63], s[60:61]\n"
    "s_call_b64 s[60:61], .Lxd_sl6\n"
    "s_or_b32 s56, s56, 2\n"
    "s_setpc_b64 s[62:63]\n"
    ".Lxd_gran:\n"
    "s_mov_b32 s46, 0\n"
    "s_lshl4_add_u32 s40, s22, s58\n"
    "v_lshrrev_b32 v13, 4, v1\n"
    "v_lshl_add_u32 v13, v13, 2, s40\n"
    "s_waitcnt vmcnt(0)\n"
    "ds_read2_b32 v[26:27], v13 offset1:16\n"
    "v_and_b32 v13, 15, v1\n"
    "v_lshlrev_b32 v13, 15, v13\n"
    "s_mov_b32 s42, 0x1fff1fff\n"
    "s_mov_b32 s43, s42\n"
    "s_add_u32 s40, s16, 65528\n"
    "s_addc_u32 s41, s17, 0\n"
    "v_mov_b32 v42, 0\n"
    "s_mov_b32 s56, 2\n"
    "s_waitcnt lgkmcnt(0)\n"
    "v_cmp_lt_i32 s[30:31], 0, v26\n"
    "v_cmp_lt_i32 s[64:65], 0, v27\n"
    "v_lshl_add_u32 v43, v26, 2, v13\n"
    "v_lshl_add_u32 v27, v27, 2, v13\n"
    "v_mov_b32 v26, 0\n"
    "s_and_b64 s[30:31], s[30:31], s[42:43]\n"
    "s_cbranch_scc0 .Lxd_gr30\n"
    "s_mov_b64 exec, s[30:31]\n"
    "global_load_dwordx2 v[42:43], v43, s[40:41] sc1\n"
    "s_mov_b64 exec, -1\n"
    "s_mov_b32 s56, 1\n"
    ".Lxd_gr30:\n"
    "s_and_b64 s[64:65], s[64:65], s[42:43]\n"
    "s_cbranch_scc0 .Lxd_gr64\n"
    "s_mov_b64 exec, s[64:65]\n"
    "global_load_dwordx2 v[26:27], v27, s[40:41] sc1\n"
    "s_mov_b64 exec, -1\n"
    "s_mov_b32 s56, 1\n"
    ".Lxd_gr64:\n"
    "s_setpc_b64 s[60:61]\n"
    ".Lxd_dsum:\n"
    "s_lshl4_add_u32 s42, s22, s58\n"
    "s_waitcnt vmcnt(0)\n"
    "s_cmp_eq_u64 s[30:31], 0\n"
    "s_cbranch_scc1 .Lxd_ds30\n"
    "v_xor_b32 v13, s28, v42\n"
    "v_xor_b32 v22, s29, v43\n"
    "v_lshl_or_b32 v13, v13, 24, v22\n"
    "v_and_b32 v22, 0xffffff00, v42\n"
    "v_cmp_ne_u32 s[40:41], 0, v13\n"
    "v_lshrrev_b32 v13, 4, v1\n"
    "v_add_f32_dpp v22, v22, v22 quad_perm:[1,0,3,2] row_mask:0xf bank_mask:0xf\n"
    "s_and_b64 s[40:41], s[40:41], s[30:31]\n"
    "s_cbranch_scc1 .Lxd_ds9\n"
    "v_add_f32_dpp v22, v22, v22 quad_perm:[2,3,0,1] row_mask:0xf bank_mask:0xf\n"
    "v_lshl_add_u32 v13, v13, 2, s42\n"
    "s_nop 0\n"
    "v_add_f32_dpp v22, v22, v22 row_half_mirror row_mask:0xf bank_mask:0xf\n"
    "s_nop 1\n"
    "v_add_f32_dpp v22, v22, v22 row_mirror row_mask:0xf bank_mask:0xf\n"
    "ds_write_b32 v13, v22 offset:1024\n"
    ".Lxd_ds30:\n"
    "s_cmp_eq_u64 s[64:65], 0\n"
    "s_cbranch_scc1 .Lxd_ds64\n"
    "v_xor_b32 v13, s28, v26\n"
    "v_xor_b32 v22, s29, v27\n"
    "v_lshl_or_b32 v13, v13, 24, v22\n"
    "v_and_b32 v22, 0xffffff00, v26\n"
    "v_cmp_ne_u32 s[40:41], 0, v13\n"
    "v_lshrrev_b32 v13, 4, v1\n"
    "v_add_f32_dpp v22, v22, v22 quad_perm:[1,0,3,2] row_mask:0xf bank_mask:0xf\n"
    "s_and_b64 s[40:41], s[40:41], s[64:65]\n"
    "s_cbranch_scc1 .Lxd_ds9\n"
    "v_add_f32_dpp v22, v22, v22 quad_perm:[2,3,0,1] row_mask:0xf bank_mask:0xf\n"
    "v_lshl_add_u32 v13, v13, 2, s42\n"
    "s_nop 0\n"
    "v_add_f32_dpp v22, v22, v22 row_half_mirror row_mask:0xf bank_mask:0xf\n"
    "s_nop 1\n"
    "v_add_f32_dpp v22, v22, v22 row_mirror row_mask:0xf bank_mask:0xf\n"
    "ds_write_b32 v13, v22 offset:1088\n"
    ".Lxd_ds64:\n"
    "s_mov_b32 s56, 2\n"
    ".Lxd_ds9:\n"
    "s_setpc_b64 s[60:61]\n"
    ".Lxd_tf17:\n"
    "v_mov_b32 v12, -1\n"
    "v_and_b32 v13, 0x3ffff, v25\n"
    "s_add_u32 s40, s58, 248\n"
    "v_lshl_add_u32 v13, v1, 2, v13\n"
    "v_mov_b32 v24, -1\n"
    "v_cmp_gt_u32 s[42:43], s40, v13\n"
    "s_mov_b32 s62, 0x88888888\n"
    "s_mov_b32 s63, s62\n"
    "s_and_b64 exec, s[42:43], s[62:63]\n"
    "ds_write_b32 v13, v24 offset:8\n"
    "s_mov_b64 exec, s[42:43]\n"
    "v_cndmask_b32 v24, 0, -16, s[62:63]\n"
    "s_brev_b32 s40, 1\n"
    "v_writelane_b32 v24, s40, 2\n"
    "s_lshl_b32 s40, 1, s22\n"
    "v_or_b32 v24, s40, v24\n"
    "s_mov_b32 s36, 1\n"
    "ds_or_b32 v13, v24 offset:520\n"
    "s_mov_b64 exec, -1\n"
    "s_branch .Lxd_tf17r\n"
    ".Lxd_ht18:\n"
    "v_cmp_ne_u32 s[40:41], -1, v27\n"
    "s_mov_b32 s46, 0\n"
    "s_cmp_eq_u64 s[40:41], 0\n"
    "s_cbranch_scc1 .Lxd_ht18r\n"
    "v_add_f32 v26, v26, v27\n"
    "v_lshlrev_b32 v42, 3, v1\n"
    "v_add_u32 v26, 0x80, v26\n"
    "v_mov_b32 v27, s29\n"
    "v_and_b32 v26, 0xffffff00, v26\n"
    "s_mov_b64 exec, s[40:41]\n"
    "v_or_b32 v26, s28, v26\n"
    "global_store_dwordx2 v42, v[26:27], s[48:49] sc1\n"
    "s_mov_b64 exec, -1\n"
    "s_branch .Lxd_ht18r\n"
    ".Lxd_ck19:\n"
    "s_mov_b32 s37, 0\n"
    "s_cmp_eq_u32 s45, -1\n"
    "s_cselect_b32 s40, s56, 0\n"
    "s_cmp_eq_u32 s40, 2\n"
    "s_cbranch_scc1 .Lxd_last0\n"
    "s_cmp_eq_u32 s40, 1\n"
    "s_cbranch_scc1 .Lxd_last1\n"
    "v_readfirstlane_b32 s41, v25\n"
    "s_and_b32 s40, s45, 0xff0\n"
    "s_cselect_b32 s40, s45, 0\n"
    "s_cmp_lt_i32 s40, -16\n"
    "s_cselect_b32 s40, s56, 1\n"
    "s_cmp_lg_u32 s40, 0\n"
    "s_cbranch_scc1 .Lxd_ck19g\n"
    "s_bitset0_b32 s45, 31\n"
    "s_lshl4_add_u32 s40, s22, s58\n"
    "s_add_u32 s41, s41, 4\n"
    "s_mov_b32 s56, 2\n"
    "s_cmp_lt_u32 s41, s40\n"
    "s_cbranch_scc1 .Lxd_ck19r\n"
    "s_call_b64 s[60:61], .Lxd_gran\n"
    "s_branch .Lxd_ck19r\n"
    ".Lxd_ck19g:\n"
    "v_add_u32 v13, 4, v25\n"
    "s_call_b64 s[60:61], .Lxd_slow\n"
    "s_cmp_eq_u32 s45, -1\n"
    "s_cbranch_scc0 .Lxd_ck19r\n"
    "s_cmp_eq_u32 s56, 1\n"
    "s_cbranch_scc1 .Lxd_last1\n"
    "s_branch .Lxd_last0\n"
    ".Lxd_ht22:\n"
    "s_cmp_lt_i32 s46, 0\n"
    "s_cbranch_scc1 .Lxd_ht22s\n"
    "global_load_lds_dword v26, s[8:9]\n"
    "v_mov_b32 v26, v27\n"
    "v_mov_b32 v27, v42\n"
    "s_sub_u32 s46, s46, 1\n"
    "s_branch .Lxd_ht22r\n"
    ".Lxd_ht22s:\n"
    "v_cmp_ne_u32 s[40:41], -1, v27\n"
    "s_mov_b32 s46, 0\n"
    "s_cmp_eq_u64 s[40:41], 0\n"
    "s_cbranch_scc1 .Lxd_ht22r\n"
    "v_add_f32 v26, v26, v27\n"
    "v_lshlrev_b32 v42, 3, v1\n"
    "v_add_u32 v26, 0x80, v26\n"
    "v_mov_b32 v27, s29\n"
    "v_and_b32 v26, 0xffffff00, v26\n"
    "s_mov_b64 exec, s[40:41]\n"
    "v_or_b32 v26, s28, v26\n"
    "global_store_dwordx2 v42, v[26:27], s[48:49] sc1\n"
    "s_mov_b64 exec, -1\n"
    "s_branch .Lxd_ht22r\n"
    ".Lxd_ck23:\n"
    "s_mov_b32 s37, 1\n"
    "s_cmp_eq_u32 s45, -1\n"
    "s_cselect_b32 s40, s56, 0\n"
    "s_cmp_eq_u32 s40, 2\n"
    "s_cbranch_scc1 .Lxd_last0\n"
    "s_cmp_eq_u32 s40, 1\n"
    "s_cbranch_scc1 .Lxd_last1\n"
    "v_readfirstlane_b32 s41, v25\n"
    "s_and_b32 s40, s45, 0xff0\n"
    "s_cselect_b32 s40, s45, 0\n"
    "s_cmp_lt_i32 s40, -16\n"
    "s_cselect_b32 s40, s56, 1\n"
    "s_cmp_lg_u32 s40, 0\n"
    "s_cbranch_scc1 .Lxd_ck23g\n"
    "s_bitset0_b32 s45, 31\n"
    "s_lshl4_add_u32 s40, s22, s58\n"
    "s_add_u32 s41, s41, 8\n"
    "s_mov_b32 s56, 2\n"
    "s_cmp_lt_u32 s41, s40\n"
    "s_cbranch_scc1 .Lxd_ck23r\n"
    "s_call_b64 s[60:61], .Lxd_gran\n"
    "s_branch .Lxd_ck23r\n"
    ".Lxd_ck23g:\n"
    "v_add_u32 v13, 8, v25\n"
    "s_call_b64 s[60:61], .Lxd_slow\n"
    "s_cmp_eq_u32 s45, -1\n"
    "s_cbranch_scc0 .Lxd_ck23r\n"
    "s_cmp_eq_u32 s56, 1\n"
    "s_cbranch_scc1 .Lxd_last1\n"
    "s_branch .Lxd_last0\n"
    ".Lxd_ht26:\n"
    "s_cmp_lt_i32 s46, 0\n"
    "s_cbranch_scc1 .Lxd_ht26s\n"
    "global_load_lds_dword v26, s[8:9]\n"
    "v_mov_b32 v26, v27\n"
    "v_mov_b32 v27, v42\n"
    "s_sub_u32 s46, s46, 1\n"
    "s_branch .Lxd_ht26r\n"
    ".Lxd_ht26s:\n"
    "v_cmp_ne_u32 s[40:41], -1, v27\n"
    "s_mov_b32 s46, 0\n"
    "s_cmp_eq_u64 s[40:41], 0\n"
    "s_cbranch_scc1 .Lxd_ht26r\n"
    "v_add_f32 v26, v26, v27\n"
    "v_lshlrev_b32 v42, 3, v1\n"
    "v_add_u32 v26, 0x80, v26\n"
    "v_mov_b32 v27, s29\n"
    "v_and_b32 v26, 0xffffff00, v26\n"
    "s_mov_b64 exec, s[40:41]\n"
    "v_or_b32 v26, s28, v26\n"
    "global_store_dwordx2 v42, v[26:27], s[48:49] sc1\n"
    "s_mov_b64 exec, -1\n"
    "s_branch .Lxd_ht26r\n"
    ".Lxd_ck27:\n"
    "s_mov_b32 s37, 2\n"
    "s_cmp_eq_u32 s45, -1\n"
    "s_cselect_b32 s40, s56, 0\n"
    "s_cmp_eq_u32 s40, 2\n"
    "s_cbranch_scc1 .Lxd_last0\n"
    "s_cmp_eq_u32 s40, 1\n"
    "s_cbranch_scc1 .Lxd_last1\n"
    "v_readfirstlane_b32 s41, v25\n"
    "s_and_b32 s40, s45, 0xff0\n"
    "s_cselect_b32 s40, s45, 0\n"
    "s_cmp_lt_i32 s40, -16\n"
    "s_cselect_b32 s40, s56, 1\n"
    "s_cmp_lg_u32 s40, 0\n"
    "s_cbranch_scc1 .Lxd_ck27g\n"
    "s_bitset0_b32 s45, 31\n"
    "s_lshl4_add_u32 s40, s22, s58\n"
    "s_add_u32 s41, s41, 12\n"
    "s_mov_b32 s56, 2\n"
    "s_cmp_lt_u32 s41, s40\n"
    "s_cbranch_scc1 .Lxd_ck27r\n"
    "s_call_b64 s[60:61], .Lxd_gran\n"
    "s_branch .Lxd_ck27r\n"
    ".Lxd_ck27g:\n"
    "v_add_u32 v13, 12, v25\n"
    "s_call_b64 s[60:61], .Lxd_slow\n"
    "s_cmp_eq_u32 s45, -1\n"
    "s_cbranch_scc0 .Lxd_ck27r\n"
    "s_cmp_eq_u32 s56, 1\n"
    "s_cbranch_scc1 .Lxd_last1\n"
    "s_branch .Lxd_last0\n"
    ".Lxd_dr30:\n"
    "v_and_b32 v13, 0x3ffff, v25\n"
    "s_add_u32 s40, s58, 232\n"
    "v_lshl_add_u32 v13, v1, 2, v13\n"
    "v_mov_b32 v24, -1\n"
    "v_cmp_gt_u32 s[42:43], s40, v13\n"
    "s_mov_b32 s62, 0x88888888\n"
    "s_mov_b32 s63, s62\n"
    "s_and_b64 exec, s[42:43], s[62:63]\n"
    "ds_write_b32 v13, v24 offset:24\n"
    "s_mov_b64 exec, s[42:43]\n"
    "v_cndmask_b32 v24, 0, -16, s[62:63]\n"
    "s_brev_b32 s40, 1\n"
    "v_writelane_b32 v24, s40, 2\n"
    "s_lshl_b32 s40, 1, s22\n"
    "v_or_b32 v24, s40, v24\n"
    "s_mov_b32 s36, 1\n"
    "ds_or_b32 v13, v24 offset:536\n"
    "s_mov_b64 exec, -1\n"
    "s_branch .Lxd_dr30r\n"
    ".Lxd_ck31:\n"
    "s_mov_b32 s37, 3\n"
    "s_cmp_eq_u32 s45, -1\n"
    "s_cselect_b32 s40, s56, 0\n"
    "s_cmp_eq_u32 s40, 2\n"
    "s_cbranch_scc1 .Lxd_last0\n"
    "s_cmp_eq_u32 s40, 1\n"
    "s_cbranch_scc1 .Lxd_last1\n"
    "v_readfirstlane_b32 s41, v25\n"
    "s_and_b32 s40, s45, 0xff0\n"
    "s_cselect_b32 s40, s45, 0\n"
    "s_cmp_lt_i32 s40, -16\n"
    "s_cselect_b32 s40, s56, 1\n"
    "s_cmp_lg_u32 s40, 0\n"
    "s_cbranch_scc1 .Lxd_ck31g\n"
    "s_bitset0_b32 s45, 31\n"
    "s_lshl4_add_u32 s40, s22, s58\n"
    "s_add_u32 s41, s41, 16\n"
    "s_mov_b32 s56, 2\n"
    "s_cmp_lt_u32 s41, s40\n"
    "s_cbranch_scc1 .Lxd_ck31r\n"
    "s_call_b64 s[60:61], .Lxd_gran\n"
    "s_branch .Lxd_ck31r\n"
    ".Lxd_ck31g:\n"
    "v_add_u32 v13, 16, v25\n"
    "s_call_b64 s[60:61], .Lxd_slow\n"
    "s_cmp_eq_u32 s45, -1\n"
    "s_cbranch_scc0 .Lxd_ck31r\n"
    "s_cmp_eq_u32 s56, 1\n"
    "s_cbranch_scc1 .Lxd_last1\n"
    "s_branch .Lxd_last0\n"
    ".Lxd_ck34:\n"
    "s_mov_b32 s37, 0\n"
    "s_cmp_eq_u32 s45, -1\n"
    "s_cselect_b32 s40, s56, 0\n"
    "s_cmp_eq_u32 s40, 2\n"
    "s_cbranch_scc1 .Lxd_last0\n"
    "s_cmp_eq_u32 s40, 1\n"
    "s_cbranch_scc1 .Lxd_last1\n"
    "v_readfirstlane_b32 s41, v25\n"
    "s_and_b32 s40, s45, 0xff0\n"
    "s_cselect_b32 s40, s45, 0\n"
    "s_cmp_lt_i32 s40, -16\n"
    "s_cselect_b32 s40, s56, 1\n"
    "s_cmp_lg_u32 s40, 0\n"
    "s_cbranch_scc1 .Lxd_ck34g\n"
    "s_bitset0_b32 s45, 31\n"
    "s_lshl4_add_u32 s40, s22, s58\n"
    "s_add_u32 s41, s41, 4\n"
    "s_mov_b32 s56, 2\n"
    "s_cmp_lt_u32 s41, s40\n"
    "s_cbranch_scc1 .Lxd_ck34r\n"
    "s_call_b64 s[60:61], .Lxd_gran\n"
    "s_branch .Lxd_ck34r\n"
    ".Lxd_ck34g:\n"
    "v_add_u32 v13, 4, v25\n"
    "s_call_b64 s[60:61], .Lxd_slow\n"
    "s_cmp_eq_u32 s45, -1\n"
    "s_cbranch_scc0 .Lxd_ck34r\n"
    "s_cmp_eq_u32 s56, 1\n"
    "s_cbranch_scc1 .Lxd_last1\n"
    "s_branch .Lxd_last0\n"
    ".Lxd_ht37:\n"
    "s_cmp_lt_i32 s46, 0\n"
    "s_cbranch_scc1 .Lxd_ht37s\n"
    "global_load_lds_dword v26, s[8:9]\n"
    "v_mov_b32 v26, v27\n"
    "v_mov_b32 v27, v42\n"
    "s_sub_u32 s46, s46, 1\n"
    "s_branch .Lxd_ht37r\n"
    ".Lxd_ht37s:\n"
    "v_cmp_ne_u32 s[40:41], -1, v27\n"
    "s_mov_b32 s46, 0\n"
    "s_cmp_eq_u64 s[40:41], 0\n"
    "s_cbranch_scc1 .Lxd_ht37r\n"
    "v_add_f32 v26, v26, v27\n"
    "v_lshlrev_b32 v42, 3, v1\n"
    "v_add_u32 v26, 0x80, v26\n"
    "v_mov_b32 v27, s29\n"
    "v_and_b32 v26, 0xffffff00, v26\n"
    "s_mov_b64 exec, s[40:41]\n"
    "v_or_b32 v26, s28, v26\n"
    "global_store_dwordx2 v42, v[26:27], s[48:49] sc1\n"
    "s_mov_b64 exec, -1\n"
    "s_branch .Lxd_ht37r\n"
    ".Lxd_ck38:\n"
    "s_mov_b32 s37, 1\n"
    "s_cmp_eq_u32 s45, -1\n"
    "s_cselect_b32 s40, s56, 0\n"
    "s_cmp_eq_u32 s40, 2\n"
    "s_cbranch_scc1 .Lxd_last0\n"
    "s_cmp_eq_u32 s40, 1\n"
    "s_cbranch_scc1 .Lxd_last1\n"
    "v_readfirstlane_b32 s41, v25\n"
    "s_and_b32 s40, s45, 0xff0\n"
    "s_cselect_b32 s40, s45, 0\n"
    "s_cmp_lt_i32 s40, -16\n"
    "s_cselect_b32 s40, s56, 1\n"
    "s_cmp_lg_u32 s40, 0\n"
    "s_cbranch_scc1 .Lxd_ck38g\n"
    "s_bitset0_b32 s45, 31\n"
    "s_lshl4_add_u32 s40, s22, s58\n"
    "s_add_u32 s41, s41, 8\n"
    "s_mov_b32 s56, 2\n"
    "s_cmp_lt_u32 s41, s40\n"
    "s_cbranch_scc1 .Lxd_ck38r\n"
    "s_call_b64 s[60:61], .Lxd_gran\n"
    "s_branch .Lxd_ck38r\n"
    ".Lxd_ck38g:\n"
    "v_add_u32 v13, 8, v25\n"
    "s_call_b64 s[60:61], .Lxd_slow\n"
    "s_cmp_eq_u32 s45, -1\n"
    "s_cbranch_scc0 .Lxd_ck38r\n"
    "s_cmp_eq_u32 s56, 1\n"
    "s_cbranch_scc1 .Lxd_last1\n"
    "s_branch .Lxd_last0\n"
    ".Lxd_ht41:\n"
    "s_cmp_lt_i32 s46, 0\n"
    "s_cbranch_scc1 .Lxd_ht41s\n"
    "global_load_lds_dword v26, s[8:9]\n"
    "v_mov_b32 v26, v27\n"
    "v_mov_b32 v27, v42\n"
    "s_sub_u32 s46, s46, 1\n"
    "s_branch .Lxd_ht41r\n"
    ".Lxd_ht41s:\n"
    "v_cmp_ne_u32 s[40:41], -1, v27\n"
    "s_mov_b32 s46, 0\n"
    "s_cmp_eq_u64 s[40:41], 0\n"
    "s_cbranch_scc1 .Lxd_ht41r\n"
    "v_add_f32 v26, v26, v27\n"
    "v_lshlrev_b32 v42, 3, v1\n"
    "v_add_u32 v26, 0x80, v26\n"
    "v_mov_b32 v27, s29\n"
    "v_and_b32 v26, 0xffffff00, v26\n"
    "s_mov_b64 exec, s[40:41]\n"
    "v_or_b32 v26, s28, v26\n"
    "global_store_dwordx2 v42, v[26:27], s[48:49] sc1\n"
    "s_mov_b64 exec, -1\n"
    "s_branch .Lxd_ht41r\n"
    ".Lxd_ck42:\n"
    "s_mov_b32 s37, 2\n"
    "s_cmp_eq_u32 s45, -1\n"
    "s_cselect_b32 s40, s56, 0\n"
    "s_cmp_eq_u32 s40, 2\n"
    "s_cbranch_scc1 .Lxd_last0\n"
    "s_cmp_eq_u32 s40, 1\n"
    "s_cbranch_scc1 .Lxd_last1\n"
    "v_readfirstlane_b32 s41, v25\n"
    "s_and_b32 s40, s45, 0xff0\n"
    "s_cselect_b32 s40, s45, 0\n"
    "s_cmp_lt_i32 s40, -16\n"
    "s_cselect_b32 s40, s56, 1\n"
    "s_cmp_lg_u32 s40, 0\n"
    "s_cbranch_scc1 .Lxd_ck42g\n"
    "s_bitset0_b32 s45, 31\n"
    "s_lshl4_add_u32 s40, s22, s58\n"
    "s_add_u32 s41, s41, 12\n"
    "s_mov_b32 s56, 2\n"
    "s_cmp_lt_u32 s41, s40\n"
    "s_cbranch_scc1 .Lxd_ck42r\n"
    "s_call_b64 s[60:61], .Lxd_gran\n"
    "s_branch .Lxd_ck42r\n"
    ".Lxd_ck42g:\n"
    "v_add_u32 v13, 12, v25\n"
    "s_call_b64 s[60:61], .Lxd_slow\n"
    "s_cmp_eq_u32 s45, -1\n"
    "s_cbranch_scc0 .Lxd_ck42r\n"
    "s_cmp_eq_u32 s56, 1\n"
    "s_cbranch_scc1 .Lxd_last1\n"
    "s_branch .Lxd_last0\n"
    ".Lxd_ht45:\n"
    "v_cmp_ne_u32 s[40:41], -1, v27\n"
    "s_mov_b32 s46, 0\n"
    "s_cmp_eq_u64 s[40:41], 0\n"
    "s_cbranch_scc1 .Lxd_ht45r\n"
    "v_add_f32 v26, v26, v27\n"
    "v_lshlrev_b32 v42, 3, v1\n"
    "v_add_u32 v26, 0x80, v26\n"
    "v_mov_b32 v27, s29\n"
    "v_and_b32 v26, 0xffffff00, v26\n"
    "s_mov_b64 exec, s[40:41]\n"
    "v_or_b32 v26, s28, v26\n"
    "global_store_dwordx2 v42, v[26:27], s[48:49] sc1\n"
    "s_mov_b64 exec, -1\n"
    "s_branch .Lxd_ht45r\n"
    ".Lxd_dr47:\n"
    "v_and_b32 v13, 0x3ffff, v25\n"
    "s_add_u32 s40, s58, 232\n"
    "v_lshl_add_u32 v13, v1, 2, v13\n"
    "v_mov_b32 v24, -1\n"
    "v_cmp_gt_u32 s[42:43], s40, v13\n"
    "s_mov_b32 s62, 0x88888888\n"
    "s_mov_b32 s63, s62\n"
    "s_and_b64 exec, s[42:43], s[62:63]\n"
    "ds_write_b32 v13, v24 offset:24\n"
    "s_mov_b64 exec, s[42:43]\n"
    "v_cndmask_b32 v24, 0, -16, s[62:63]\n"
    "s_brev_b32 s40, 1\n"
    "v_writelane_b32 v24, s40, 2\n"
    "s_lshl_b32 s40, 1, s22\n"
    "v_or_b32 v24, s40, v24\n"
    "s_mov_b32 s36, 1\n"
    "ds_or_b32 v13, v24 offset:536\n"
    "s_mov_b64 exec, -1\n"
    "s_branch .Lxd_ld46r\n"
    ".Lxd_ld46:\n"
    "v_mov_b32 v24, 1\n"
    "s_and_b32 s40, s45, 15\n"
    "s_cbranch_scc1 .Lxd_dr47\n"
    "s_mov_b64 exec, 1\n"
    "global_atomic_add v24, v17, v24, s[52:53] sc0 sc1\n"
    "s_mov_b64 exec, -1\n"
    ".Lxd_dr47r:\n"
    "s_branch .Lxd_ld46r\n"
    ".Lxd_ck48:\n"
    "s_mov_b32 s37, 3\n"
    "s_cmp_eq_u32 s45, -1\n"
    "s_cselect_b32 s40, s56, 0\n"
    "s_cmp_eq_u32 s40, 2\n"
    "s_cbranch_scc1 .Lxd_last0\n"
    "s_cmp_eq_u32 s40, 1\n"
    "s_cbranch_scc1 .Lxd_last1\n"
    "v_readfirstlane_b32 s41, v25\n"
    "s_and_b32 s40, s45, 0xff0\n"
    "s_cselect_b32 s40, s45, 0\n"
    "s_cmp_lt_i32 s40, -16\n"
    "s_cselect_b32 s40, s56, 1\n"
    "s_cmp_lg_u32 s40, 0\n"
    "s_cbranch_scc1 .Lxd_ck48g\n"
    "s_bitset0_b32 s45, 31\n"
    "s_lshl4_add_u32 s40, s22, s58\n"
    "s_add_u32 s41, s41, 16\n"
    "s_mov_b32 s56, 2\n"
    "s_cmp_lt_u32 s41, s40\n"
    "s_cbranch_scc1 .Lxd_ck48r\n"
    "s_call_b64 s[60:61], .Lxd_gran\n"
    "s_branch .Lxd_ck48r\n"
    ".Lxd_ck48g:\n"
    "v_add_u32 v13, 16, v25\n"
    "s_call_b64 s[60:61], .Lxd_slow\n"
    "s_cmp_eq_u32 s45, -1\n"
    "s_cbranch_scc0 .Lxd_ck48r\n"
    "s_cmp_eq_u32 s56, 1\n"
    "s_cbranch_scc1 .Lxd_last1\n"
    "s_branch .Lxd_last0\n"
    ".Lxd_f2:\n"
    "s_mov_b32 s59, 0x20000\n"
    ".Lxd_f4:\n"
    "s_call_b64 s[60:61], .Lxd_dsum\n"
    "s_cmp_eq_u32 s56, 2\n"
    "s_cbranch_scc1 .Lxd_f3\n"
    "s_sleep 2\n"
    "s_call_b64 s[60:61], .Lxd_gran\n"
    "s_sub_u32 s59, s59, 1\n"
    "s_cbranch_scc0 .Lxd_f4\n"
    "s_waitcnt vmcnt(0)\n"
    "s_branch .Lxd_f3\n"
    ".Lxd_on1:\n"
    "s_mov_b64 exec, 0x3ff\n"
    "global_load_dwordx2 v[26:27], v11, s[16:17] sc1\n"
    "s_mov_b64 exec, -1\n"
    "s_branch .Lxd_on1r\n"
    ".Lxd_pv3:\n"
    "s_mov_b32 s59, 0x20000\n"
    ".Lxd_pv3l:\n"
    "s_sleep 1\n"
    "s_mov_b64 exec, 0x3ff\n"
    "global_load_dwordx2 v[26:27], v11, s[16:17] sc1\n"
    "s_mov_b64 exec, -1\n"
    "s_waitcnt vmcnt(0)\n"
    "v_readlane_b32 s25, v26, 9\n"
    "v_readlane_b32 s40, v26, 8\n"
    "v_readlane_b32 s41, v27, 8\n"
    "s_mul_i32 s42, s25, 0x9e3779b1\n"
    "s_lshr_b32 s42, s42, 24\n"
    "s_xor_b32 s42, s42, s40\n"
    "s_and_b32 s42, s42, 0xff\n"
    "s_xor_b32 s41, s41, s25\n"
    "s_xor_b32 s41, s41, 0xa5c30f96\n"
    "s_or_b32 s42, s41, s42\n"
    "s_lshr_b32 s41, s40, 8\n"
    "s_mul_i32 s40, s41, 0x9e3779b1\n"
    "s_xor_b32 s41, s41, 0xa5c30f96\n"
    "s_lshr_b32 s40, s40, 24\n"
    "v_xor_b32 v13, s40, v26\n"
    "v_xor_b32 v22, s41, v27\n"
    "v_lshl_or_b32 v13, v13, 24, v22\n"
    "v_cmp_ne_u32 s[66:67], 0, v13\n"
    "s_and_b32 s66, s66, 0xff\n"
    "s_or_b32 s42, s42, s66\n"
    "s_cmp_eq_u32 s42, 0\n"
    "s_cbranch_scc1 .Lxd_pv3r\n"
    "s_sub_u32 s59, s59, 1\n"
    "s_cbranch_scc0 .Lxd_pv3l\n"
    "s_branch .Lxd_pv3r\n"
    ".Lxd_on6:\n"
    "v_mov_b32 v24, 1\n"
    "s_mov_b64 exec, 1\n"
    "global_atomic_add v24, v17, v24, s[52:53] sc0 sc1\n"
    "s_mov_b64 exec, -1\n"
    "s_branch .Lxd_on6r\n"
    ".Lxd_ck7:\n"
    "v_mov_b32 v13, s58\n"
    "s_call_b64 s[60:61], .Lxd_slow\n"
    "s_branch .Lxd_ck7r\n"
    ".Lxd_tf8:\n"
    "v_mov_b32 v12, -1\n"
    "v_and_b32 v13, 0x3ffff, v25\n"
    "s_add_u32 s40, s58, 248\n"
    "v_lshl_add_u32 v13, v1, 2, v13\n"
    "v_mov_b32 v24, -1\n"
    "v_cmp_gt_u32 s[42:43], s40, v13\n"
    "s_mov_b32 s62, 0x88888888\n"
    "s_mov_b32 s63, s62\n"
    "s_and_b64 exec, s[42:43], s[62:63]\n"
    "ds_write_b32 v13, v24 offset:8\n"
    "s_mov_b64 exec, s[42:43]\n"
    "v_cndmask_b32 v24, 0, -16, s[62:63]\n"
    "s_brev_b32 s40, 1\n"
    "v_writelane_b32 v24, s40, 2\n"
    "s_lshl_b32 s40, 1, s22\n"
    "v_or_b32 v24, s40, v24\n"
    "s_mov_b32 s36, 1\n"
    "ds_or_b32 v13, v24 offset:520\n"
    "s_mov_b64 exec, -1\n"
    "s_branch .Lxd_tf8r\n"
    ".Lxd_on9:\n"
    "s_waitcnt vmcnt(0)\n"
    "v_readfirstlane_b32 s40, v24\n"
    "s_or_b32 s40, s40, s44\n"
    "s_lshl1_add_u32 s41, s40, s54\n"
    "s_cmpk_lt_u32 s40, 256\n"
    "s_cselect_b32 s41, s41, 2\n"
    "v_mov_b32 v12, s41\n"
    "s_cbranch_scc0 .Lxd_tf8\n"
    ".Lxd_tf8r:\n"
    "s_mul_i32 s41, s41, 52\n"
    "s_add_u32 s38, s4, s41\n"
    "s_addc_u32 s39, s5, 0\n"
    "ds_write_b32 v25, v12 offset:20\n"
    "global_load_dwordx3 v[36:38], v17, s[38:39] offset:-104\n"
    "global_load_dword v41, v18, s[38:39] offset:-8\n"
    "v_mov_b32 v13, s58\n"
    "s_branch .Lxd_on9r\n"
    ".Lxd_on10:\n"
    "v_mov_b32 v24, 1\n"
    "s_mov_b64 exec, 1\n"
    "global_atomic_add v24, v17, v24, s[52:53] sc0 sc1\n"
    "s_mov_b64 exec, -1\n"
    "s_branch .Lxd_on10r\n"
    ".Lxd_on11:\n"
    "s_waitcnt vmcnt(0)\n"
    "v_lshl_or_b32 v36, v36, 7, v19\n"
    "v_lshl_or_b32 v37, v37, 7, v19\n"
    "v_lshl_or_b32 v38, v38, 7, v19\n"
    "v_lshl_or_b32 v41, v41, 7, v20\n"
    "global_load_dwordx4 v[28:31], v36, s[8:9]\n"
    "global_load_dwordx4 v[32:35], v37, s[8:9]\n"
    "global_load_dword v40, v41, s[8:9]\n"
    "global_load_dwordx4 v[36:39], v38, s[8:9]\n"
    "s_branch .Lxd_on11r\n"
    ".Lxd_tf12:\n"
    "v_mov_b32 v12, -1\n"
    "v_and_b32 v13, 0x3ffff, v25\n"
    "s_add_u32 s40, s58, 248\n"
    "v_lshl_add_u32 v13, v1, 2, v13\n"
    "v_mov_b32 v24, -1\n"
    "v_cmp_gt_u32 s[42:43], s40, v13\n"
    "s_mov_b32 s62, 0x88888888\n"
    "s_mov_b32 s63, s62\n"
    "s_and_b64 exec, s[42:43], s[62:63]\n"
    "ds_write_b32 v13, v24 offset:8\n"
    "s_mov_b64 exec, s[42:43]\n"
    "v_cndmask_b32 v24, 0, -16, s[62:63]\n"
    "s_brev_b32 s40, 1\n"
    "v_writelane_b32 v24, s40, 2\n"
    "s_lshl_b32 s40, 1, s22\n"
    "v_or_b32 v24, s40, v24\n"
    "s_mov_b32 s36, 1\n"
    "ds_or_b32 v13, v24 offset:520\n"
    "s_mov_b64 exec, -1\n"
    "s_branch .Lxd_tf12r\n"
    ".Lxd_on13:\n"
    "s_waitcnt vmcnt(0)\n"
    "v_readfirstlane_b32 s40, v24\n"
    "s_or_b32 s40, s40, s44\n"
    "s_lshl1_add_u32 s41, s40, s54\n"
    "s_cmpk_lt_u32 s40, 256\n"
    "s_cselect_b32 s41, s41, 2\n"
    "v_mov_b32 v12, s41\n"
    "s_cbranch_scc0 .Lxd_tf12\n"
    ".Lxd_tf12r:\n"
    "s_mul_i32 s41, s41, 52\n"
    "s_add_u32 s38, s4, s41\n"
    "s_addc_u32 s39, s5, 0\n"
    "ds_write_b32 v25, v12 offset:20\n"
    "global_load_dwordx3 v[36:38], v17, s[38:39] offset:-104\n"
    "global_load_dword v41, v18, s[38:39] offset:-8\n"
    "s_branch .Lxd_on13r\n"
    ".Lxd_on14:\n"
    "v_mov_b32 v24, 1\n"
    "s_mov_b64 exec, 1\n"
    "global_atomic_add v24, v17, v24, s[52:53] sc0 sc1\n"
    "s_mov_b64 exec, -1\n"
    "s_branch .Lxd_on14r\n"
    ".Lxd_pv62:\n"
    "s_mov_b32 s59, 0x20000\n"
    ".Lxd_pv62l:\n"
    "s_sleep 1\n"
    "s_mov_b64 exec, 0x3ff\n"
    "global_load_dwordx2 v[26:27], v11, s[16:17] sc1\n"
    "s_mov_b64 exec, -1\n"
    "s_waitcnt vmcnt(0)\n"
    "v_readlane_b32 s25, v26, 9\n"
    "v_readlane_b32 s40, v26, 8\n"
    "v_readlane_b32 s41, v27, 8\n"
    "s_mul_i32 s42, s25, 0x9e3779b1\n"
    "s_lshr_b32 s42, s42, 24\n"
    "s_xor_b32 s42, s42, s40\n"
    "s_and_b32 s42, s42, 0xff\n"
    "s_xor_b32 s41, s41, s25\n"
    "s_xor_b32 s41, s41, 0xa5c30f96\n"
    "s_or_b32 s42, s41, s42\n"
    "s_lshr_b32 s41, s40, 8\n"
    "s_mul_i32 s40, s41, 0x9e3779b1\n"
    "s_xor_b32 s41, s41, 0xa5c30f96\n"
    "s_lshr_b32 s40, s40, 24\n"
    "v_xor_b32 v13, s40, v26\n"
    "v_xor_b32 v22, s41, v27\n"
    "v_lshl_or_b32 v13, v13, 24, v22\n"
    "v_cmp_ne_u32 s[66:67], 0, v13\n"
    "s_and_b32 s66, s66, 0xff\n"
    "s_or_b32 s42, s42, s66\n"
    "s_cmp_eq_u32 s42, 0\n"
    "s_cbranch_scc1 .Lxd_pv62r\n"
    "s_sub_u32 s59, s59, 1\n"
    "s_cbranch_scc0 .Lxd_pv62l\n"
    "s_branch .Lxd_pv62r\n"
    ".Lxd_end:\n"
    : "+{v0}"(tid) : "{s[0:1]}"(__builtin_amdgcn_kernarg_segment_ptr()), "{s2}"(blockIdx.x)
    : "memory", "vcc", "scc", "v1", "v2", "v3", "v4", "v5", "v6", "v7", "v8", "v9", "v10", "v11", "v12", "v13", "v14", "v15", "v16", "v17", "v18", "v19", "v20", "v21", "v22", "v23", "v24", "v25", "v26", "v27", "v28", "v29", "v30", "v31", "v32", "v33", "v34", "v35", "v36", "v37", "v38", "v39", "v40", "v41", "v42", "v43", "v44", "v45", "v46", "v47", "v48", "v49", "v50", "v51", "v52", "v53", "v54", "v55", "v56", "v57", "v58", "v59", "v60", "v61", "v62", "v63", "v64", "v65", "v66", "v67", "v68", "v69", "v70", "v71", "v72", "v73", "v74", "v75", "v76", "v77", "v78", "v79", "v80", "v81", "v82", "v83", "v84", "v85", "v86", "v87", "v88", "v89", "v90", "v91", "v92", "v93", "v94", "v95", "v96", "v97", "v98", "v99", "v100", "v101", "v102", "v103", "v104", "v105", "v106", "v107", "v108", "v109", "v110", "v111", "v112", "v113", "v114", "v115", "v116", "v117", "v118", "v119", "v120", "v121", "v122", "v123", "v124", "v125", "v126", "v127", "v128", "v129", "v130", "v131", "v132", "v133", "v134", "v135", "v136", "v137", "v138", "v139", "v140", "v141", "v142", "v143", "v144", "v145", "v146", "v147", "v148", "v149", "v150", "v151", "v152", "v153", "v154", "v155", "v156", "v157", "v158", "v159", "v160", "v161", "v162", "v163", "v164", "v165", "v166", "v167", "v168", "v169", "v170", "v171", "v172", "v173", "v174", "v175", "v176", "v177", "v178", "v179", "v180", "v181", "v182", "v183", "v184", "v185", "v186", "v187", "v188", "v189", "v190", "v191", "v192", "v193", "v194", "v195", "v196", "v197", "v198", "v199", "v200", "v201", "v202", "v203", "v204", "v205", "v206", "v207", "v208", "v209", "v210", "v211", "v212", "v213", "v214", "v215", "v216", "v217", "v218", "v219", "v220", "v221", "v222", "v223", "v224", "v225", "v226", "v227", "v228", "v229", "v230", "v231", "v232", "v233", "v234", "v235", "v236", "v237", "v238", "v239", "v240", "v241", "v242", "v243", "v244", "v245", "v246", "v247", "v248", "v249", "v250", "v251", "v252", "v253", "v254", "v255", "s3", "s4", "s5", "s6", "s7", "s8", "s9", "s10", "s11", "s12", "s13", "s14", "s15", "s16", "s17", "s18", "s19", "s20", "s21", "s22", "s23", "s24", "s25", "s26", "s27", "s28", "s29", "s30", "s31", "s34", "s35", "s36", "s37", "s38", "s39", "s40", "s41", "s42", "s43", "s44", "s45", "s46", "s47", "s48", "s49", "s50", "s51", "s52", "s53", "s54", "s55", "s56", "s57", "s58", "s59", "s60", "s61", "s62", "s63", "s64", "s65", "s66", "s67", "s68", "s69", "s70", "s71");
  asm volatile("" :: "v"((__attribute__((address_space(3))) char*)shm));
}

extern "C" void kernel_launch(void* const* d_in, const int* in_sizes, int n_in, void* d_out, int out_size,
                              void* d_ws, size_t ws_size, hipStream_t stream) {
  xd_kernel<<<256, 512, 0, stream>>>((const int*)d_in[0], (const u16*)d_in[4], (const u16*)d_in[1], (const u16*)d_in[5], (const u16*)d_in[2], (const u16*)d_in[3], (char*)d_ws,
                                     (const u16*)d_in[6], (const u16*)d_in[7], (u16*)d_out);
}
```
